```python
import torch

def _softmax(x, axis=-1):
    return torch.softmax(x, dim=axis)

def _gelu(x):
    return torch.nn.functional.gelu(x, approximate='tanh')

def _silu(x):
    return torch.nn.functional.silu(x)

def _sigmoid(x):
    return torch.sigmoid(x)

def _layernorm(x, axis=-1, eps=1e-05):
    m = x.mean(dim=axis, keepdim=True)
    v = x.var(dim=axis, keepdim=True, unbiased=False)
    return (x - m) / torch.sqrt(v + eps)

def _scatter_add(tgt, idx, src, axis=0):
    return tgt.clone().index_add_(axis, idx, src)

def _segment_sum(src, seg, n):
    out = torch.zeros((n,) + src.shape[1:], dtype=src.dtype)
    return out.index_add_(0, seg, src)

def _pad(x, pw):
    flat = tuple((v for pair in reversed(pw) for v in pair))
    return torch.nn.functional.pad(x, flat)
kernel_name = 'adaLN_cond__sliding_local_dilated__glu_gated_glu__prompt'

def setup_inputs():

    _w = lambda a: (lambda t: t.to(torch.bfloat16) if t.is_floating_point() else t)(torch.as_tensor(a))

    def _A_setup():
        B, S, D = (1, 2048, 1024)
        return {'x': _w((lambda *s: torch.randn(*s, dtype=torch.bfloat16) * 0.05)(B, S, D)), 'wqkv': _w((lambda *s: torch.randn(*s, dtype=torch.bfloat16) * 0.05)(D, 3 * D)), 'wo': _w((lambda *s: torch.randn(*s, dtype=torch.bfloat16) * 0.05)(D, D))}
    _A_in = _A_setup()

    def _B_setup():
        B, S, D, F = (1, 2048, 1024, 3584)
        return {'x': _w((lambda *s: torch.randn(*s, dtype=torch.bfloat16) * 0.05)(B, S, D)), 'w_gate': _w((lambda *s: torch.randn(*s, dtype=torch.bfloat16) * 0.05)(D, F)), 'w_up': _w((lambda *s: torch.randn(*s, dtype=torch.bfloat16) * 0.05)(D, F)), 'w_down': _w((lambda *s: torch.randn(*s, dtype=torch.bfloat16) * 0.05)(F, D))}
    _B_in = _B_setup()
    return {'A_x': _A_in['x'], 'A_wqkv': _A_in['wqkv'], 'A_wo': _A_in['wo'], 'B_x': _B_in['x'], 'B_w_gate': _B_in['w_gate'], 'B_w_up': _B_in['w_up'], 'B_w_down': _B_in['w_down'], 'c': _w((lambda *s: torch.randn(*s, dtype=torch.bfloat16) * 0.1)(1, 1024) * 8.0), 'w_mod': _w((lambda *s: torch.randn(*s, dtype=torch.bfloat16) * 0.1)(1024, 6 * 1024) * 1024 ** (-0.5) * 8.0)}

def reference(A_x, A_wqkv, A_wo, B_x, B_w_gate, B_w_up, B_w_down, c, w_mod):

    def _A_ref(x, wqkv, wo):
        B, S, D = x.shape
        H = 32
        Dh = D // H
        W, dil = (8, 2)
        qkv = (lambda x, *d: x.permute(*d))((x @ wqkv).reshape(B, S, 3, H, Dh), 2, 0, 3, 1, 4)
        q, k, v = (qkv[0], qkv[1], qkv[2])
        scale = Dh ** (-0.5)
        pos = torch.arange(S)
        off = torch.arange(W) * dil
        idx = pos[:, None] - off[None, :]
        mask = idx >= 0
        idx_c = torch.where(mask, idx, 0).reshape(-1)
        kg = k[:, :, idx_c, :].reshape(B, H, S, W, Dh)
        vg = v[:, :, idx_c, :].reshape(B, H, S, W, Dh)
        logits = torch.einsum('bhsd,bhswd->bhsw', q, kg) * scale
        logits = torch.where(mask[None, None, :, :], logits, -1000000000.0)
        attn = _softmax(logits, axis=-1)
        o = torch.einsum('bhsw,bhswd->bhsd', attn, vg)
        return (lambda x, *d: x.permute(*d))(o, 0, 2, 1, 3).reshape(B, S, D) @ wo

    def _B_ref(x, w_gate, w_up, w_down):
        g = torch.einsum('bsd,df->bsf', x, w_gate)
        u = torch.einsum('bsd,df->bsf', x, w_up)
        h = _sigmoid(g) * u
        return torch.einsum('bsf,fd->bsd', h, w_down)
    x = A_x
    mod = (_silu(c) @ w_mod).reshape(c.shape[0], 6, 1, -1)
    sa, ca, ga, sb, cb, gb = (mod[:, 0].clone(), mod[:, 1].clone(), mod[:, 2].clone(), mod[:, 3].clone(), mod[:, 4].clone(), mod[:, 5].clone())
    h = _layernorm(x, axis=-1) * (1 + ca) + sa
    x = x + ga * _A_ref(h, A_wqkv, A_wo)
    h = _layernorm(x, axis=-1) * (1 + cb) + sb
    x = x + gb * _B_ref(h, B_w_gate, B_w_up, B_w_down)
    return x
```

```cpp
#include <hip/hip_runtime.h>
typedef unsigned short u16;
typedef float f32x4 __attribute__((ext_vector_type(4)));
typedef float f32x2 __attribute__((ext_vector_type(2)));
typedef unsigned u32x4 __attribute__((ext_vector_type(4)));
typedef unsigned u32x2 __attribute__((ext_vector_type(2)));
typedef __bf16 bf2_t __attribute__((ext_vector_type(2)));
#define LAS __attribute__((address_space(3)))
#define DI __device__ __forceinline__

constexpr int S_ = 2048, D_ = 1024, F_ = 3584;

constexpr unsigned W_GB = 0, W_EF = 1 << 16, W_TAGS = W_EF + (1 << 15), W_PART = W_TAGS + (1 << 16), W_MODP = W_PART + (1 << 15), W_FLAGS = W_MODP + (1 << 16), W_UVP = W_FLAGS + (1 << 19),
                 W_STATS = W_UVP + (1 << 19), W_X1 = W_STATS + 16 * S_ * 8, W_O = W_X1 + S_ * D_ * 2, W_Y = W_O + S_ * D_ * 2, W_HH = W_Y + S_ * D_ * 2,
                 W_XCH = W_HH + S_ * F_ * 2;
DI float bf_lo(unsigned v) { return __uint_as_float(v << 16); }
DI float bf_hi(unsigned v) { return __uint_as_float(v & 0xffff0000u); }
DI unsigned pk_bf16(float lo, float hi) { f32x2 v = {lo, hi}; bf2_t r = __builtin_convertvector(v, bf2_t); return __builtin_bit_cast(unsigned, r); }

DI void swap16(unsigned& a, unsigned& b) { auto r = __builtin_amdgcn_permlane16_swap(a, b, false, false); a = r[0]; b = r[1]; }
DI u32x4 widen(u32x2 a, u32x2 b) { unsigned a0 = a[0], a1 = a[1], b0 = b[0], b1 = b[1]; swap16(a0, b0); swap16(a1, b1); return (u32x4){a0, a1, b0, b1}; }
DI unsigned f_vb(int wid, int lane) { const int r = wid * 8 + (lane >> 3), c = lane & 7; return (unsigned)(r * 2048 + (((((c >> 1) ^ ((r >> 1) & 3)) << 1) | (c & 1)) << 4)); }
DI unsigned f_vr(int lane, int j) { const int row = lane >> 2; return (unsigned)(row * 128 + ((j ^ ((row >> 1) & 3)) << 5) + (lane & 3) * 8); }

constexpr int OF = 0, YF = 2048, HF = 4096, EP = 6144;
extern "C" __global__ __launch_bounds__(256, 1) void k_main(const u16* __restrict__ c, const u16* __restrict__ wmod, const u16* __restrict__ x, const u16* __restrict__ w, const u16* __restrict__ wo,
                                                            const u16* __restrict__ wg, const u16* __restrict__ wu, const u16* __restrict__ pfw, u16* __restrict__ out, char* ws) {
  const char* code; int len;
  asm volatile("s_getpc_b64 %0\n.Lpc%=:\n\ts_mov_b32 %1, k_down_end-.Lpc%=\n\ts_mov_b64 exec, 3\n\tglobal_load_dword a255, %2, %0\n\ts_mov_b64 exec, -1"
               : "=&s"(code), "=s"(len) : "v"(((blockIdx.x & ~7u) + (threadIdx.x >> 5) + (threadIdx.x & 1)) * 128) : "a255");
  asm volatile("" :: "s"(c), "s"(wmod), "s"(x), "s"(w), "s"(wo), "s"(wg), "s"(wu), "s"(pfw), "s"(out), "s"(ws));
  constexpr int SCT = 16384, PH = 18944, RING = 20480, HALO = 131072;
  __shared__ __attribute__((aligned(1024))) char smem[163840];
  LAS char* lds = (LAS char*)smem;
  const unsigned ldsb = (unsigned)(size_t)lds;
  int wid = __builtin_amdgcn_readfirstlane(threadIdx.x >> 6);
  int bx = blockIdx.x;
#define IDX() const int xcd = bx & 7, loc = bx >> 3, gk = loc >> 3, gc = xcd * 8 + (loc & 7), rb = (xcd >> 1) * 4 + (loc & 3), cb = (xcd & 1) * 8 + (loc >> 2); const unsigned sl1 = ldsb + wid * 1024; const int co = (loc >> 2) * 2 + (xcd & 1); const char* so = (const char*)(wo + co * 64)
  unsigned long long tag; unsigned cv; float mu[2]; unsigned vbo; int tid = threadIdx.x;
  { IDX();
  { const int lane = threadIdx.x & 63;
    const unsigned vc = threadIdx.x * 2, vs = (lane >> 2) * 12288 + (((lane & 3) ^ ((lane >> 5) << 1)) << 4), sl4 = ldsb + wid * 4096;
    const u16* sc = c + gk * 256; const u16* sw = wmod + (size_t)(gk * 256 + wid * 64) * 6144 + gc * 32; const char* st = ws + W_TAGS + EP * 8;
    asm volatile( "global_load_ushort v204, %[vc], %[sc]\n\t" "s_mov_b64 s[40:41], %[sw]\n\t" "s_add_u32 m0, %[sl4], 0\n\t" "s_nop 0\n\t" "global_load_lds_dwordx4 %[vs], s[40:41] nt\n\t"
    "s_add_u32 s40, s40, 196608\n\t" "s_addc_u32 s41, s41, 0\n\t" "s_add_u32 m0, %[sl4], 1024\n\t" "s_nop 0\n\t" "global_load_lds_dwordx4 %[vs], s[40:41] nt\n\t" "s_add_u32 s40, s40, 196608\n\t"
    "s_addc_u32 s41, s41, 0\n\t" "s_add_u32 m0, %[sl4], 2048\n\t" "s_nop 0\n\t" "global_load_lds_dwordx4 %[vs], s[40:41] nt\n\t" "s_add_u32 s40, s40, 196608\n\t" "s_addc_u32 s41, s41, 0\n\t"
    "s_add_u32 m0, %[sl4], 3072\n\t" "s_nop 0\n\t" "global_load_lds_dwordx4 %[vs], s[40:41] nt\n\t" "s_load_dwordx2 s[62:63], %[st], 0x0\n\t"
    : "=&{v204}"(cv), "=&{s[62:63]}"(tag)
    : [vc] "v"(vc), [vs] "v"(vs), [sc] "s"(sc), [sw] "s"(sw), [sl4] "s"(sl4), [st] "s"(st)
    : "s40", "s41", "scc", "memory");
  }
  { asm volatile("" : "+v"(tid));
    int lane = tid & 63;
    const int rot = loc >> 2;
    const unsigned sl = ldsb + (wid & 1) * 1024 + (wid >> 1) * 6144;
    const unsigned dx = (lane >> 3) * 2048 + (((lane & 7) ^ (lane >> 3)) << 4); unsigned d0 = dx + ((rb ? 16 : 0) + wid * 32) * 2048, d1 = d0 + 16384, d2 = d0 + 32768, d3 = d0 + 49152;
    const char* xb0 = (const char*)x + (size_t)(rb ? rb * 128 - 16 : 0) * 2048;
    const unsigned xl = (unsigned)(size_t)xb0, xh_ = (unsigned)((size_t)xb0 >> 32), k0 = rot * 256;
    asm volatile("s_add_u32 s45, %[k0], 0\n\t" "s_and_b32 s45, s45, 0x7ff\n\t" "s_add_u32 s64, %[xl], s45\n\t" "s_addc_u32 s65, %[xh_], 0\n\t" "s_add_u32 m0, %[sl], 20480\n\t" "s_nop 0\n\t"
    "global_load_lds_dwordx4 v196, s[64:65] offset:0 nt\n\t" "s_add_u32 m0, %[sl], 32768\n\t" "s_nop 0\n\t" "global_load_lds_dwordx4 v197, s[64:65] offset:0 nt\n\t" "s_add_u32 m0, %[sl], 22528\n\t"
    "s_nop 0\n\t" "global_load_lds_dwordx4 v198, s[64:65] offset:0 nt\n\t" "s_add_u32 m0, %[sl], 34816\n\t" "s_nop 0\n\t" "global_load_lds_dwordx4 v199, s[64:65] offset:0 nt\n\t"
    :: "{v196}"(d0), "{v197}"(d1), "{v198}"(d2), "{v199}"(d3), [sl] "s"(sl), [xl] "s"(xl), [xh_] "s"(xh_), [k0] "s"(k0)
    : "s45", "s64", "s65", "scc", "memory");
    asm volatile("" : "+v"(tid)); lane = tid & 63;
    const int ww = loc * 4 + wid; unsigned tv;
    { const int g = lane >> 4, l6 = (lane * 43) >> 8;
      const unsigned vsr = ldsb + (8 * g + ((lane >> 2) & 3)) * 64 + (((wid ^ g) & 1) << 5) + (lane & 3) * 8, vsc = ldsb + g * 16, vcw = ldsb + SCT + tid * 2;
      const char* su = ws + W_PART + (gk * 512 + gc * 8 + wid * 4) * 16 - ldsb; const u16* sq = w;
      const unsigned t0 = (l6 ? (rot * 128 + l6 * 32 - 32 + ((loc & 3) * 4 + wid) * 2 + (lane & 1)) * 6144 + cb * 128 : ((ww * 2 + (lane & 1)) * 4 + (xcd >> 1)) * 6144 + (xcd & 1) * 1024 + ((384 - (unsigned)(size_t)w) & 896)) + ((lane >> 1) - 3 * l6) * 2048;
      asm volatile( "s_mov_b32 s54, 0x07060503\n\t" "s_mov_b32 s55, 0x07060502\n\t" "s_mov_b32 s56, 0x07060501\n\t" "s_mov_b32 s60, 0x00010001\n\t" "s_mov_b32 s61, s60\n\t" "s_add_u32 s45, %[k0], 0\n\t"
    "s_and_b32 s45, s45, 0x7ff\n\t" "s_add_u32 s64, %[xl], s45\n\t" "s_addc_u32 s65, %[xh_], 0\n\t" "s_add_u32 m0, %[sl], 24448\n\t" "s_nop 0\n\t"
    "global_load_lds_dwordx4 v196, s[64:65] offset:128 nt\n\t" "s_add_u32 m0, %[sl], 36736\n\t" "s_nop 0\n\t" "global_load_lds_dwordx4 v197, s[64:65] offset:128 nt\n\t" "s_add_u32 m0, %[sl], 44928\n\t"
    "s_nop 0\n\t" "global_load_lds_dwordx4 v198, s[64:65] offset:128 nt\n\t" "s_add_u32 m0, %[sl], 57216\n\t" "s_nop 0\n\t" "global_load_lds_dwordx4 v199, s[64:65] offset:128 nt\n\t"
    "s_mov_b64 exec, 63\n\t" "global_load_dword v205, %[t0], %[sq]\n\t" "s_mov_b64 exec, -1\n\t" "s_waitcnt vmcnt(13)\n\t" "v_lshlrev_b32 v204, 16, v204\n\t"
    "v_mul_f32 v206, 0xbfb8aa3b, v204\n\t" "v_exp_f32 v206, v206\n\t" "s_nop 0\n\t" "v_add_f32 v206, 1.0, v206\n\t" "v_rcp_f32 v206, v206\n\t" "s_nop 0\n\t" "v_mul_f32 v204, v204, v206\n\t"
    "v_cvt_pk_bf16_f32 v204, v204, v204\n\t" "ds_write_b16 v233, v204\n\t" "s_waitcnt vmcnt(9)\n\t" "s_waitcnt lgkmcnt(0)\n\t" "s_barrier\n\t" "s_add_u32 s62, s62, 1\n\t"
    "s_addc_u32 s63, s63, 0\n\t" "s_cmp_gt_u32 %[wid], 1\n\t" "s_cbranch_scc1 .Lns_%=\n\t" "ds_read_b64_tr_b16 v[48:49], v228 offset:0\n\t" "ds_read_b64_tr_b16 v[50:51], v228 offset:256\n\t"
    "ds_read_b128 v[80:83], v229 offset:16384\n\t" "ds_read_b64_tr_b16 v[52:53], v228 offset:2048\n\t" "ds_read_b64_tr_b16 v[54:55], v228 offset:2304\n\t" "ds_read_b128 v[84:87], v229 offset:16448\n\t"
    "ds_read_b64_tr_b16 v[56:57], v228 offset:4096\n\t" "ds_read_b64_tr_b16 v[58:59], v228 offset:4352\n\t" "ds_read_b128 v[88:91], v229 offset:16512\n\t"
    "ds_read_b64_tr_b16 v[60:61], v228 offset:6144\n\t" "ds_read_b64_tr_b16 v[62:63], v228 offset:6400\n\t" "ds_read_b128 v[92:95], v229 offset:16576\n\t" "s_waitcnt lgkmcnt(9)\n\t"
    "v_mfma_f32_16x16x32_bf16 v[112:115], v[48:51], v[80:83], 0\n\t" "ds_read_b64_tr_b16 v[64:65], v228 offset:8192\n\t" "ds_read_b64_tr_b16 v[66:67], v228 offset:8448\n\t"
    "ds_read_b128 v[96:99], v229 offset:16640\n\t" "s_waitcnt lgkmcnt(9)\n\t" "v_mfma_f32_16x16x32_bf16 v[112:115], v[52:55], v[84:87], v[112:115]\n\t" "ds_read_b64_tr_b16 v[68:69], v228 offset:10240\n\t"
    "ds_read_b64_tr_b16 v[70:71], v228 offset:10496\n\t" "ds_read_b128 v[100:103], v229 offset:16704\n\t" "s_waitcnt lgkmcnt(9)\n\t"
    "v_mfma_f32_16x16x32_bf16 v[112:115], v[56:59], v[88:91], v[112:115]\n\t" "ds_read_b64_tr_b16 v[72:73], v228 offset:12288\n\t" "ds_read_b64_tr_b16 v[74:75], v228 offset:12544\n\t"
    "ds_read_b128 v[104:107], v229 offset:16768\n\t" "s_waitcnt lgkmcnt(9)\n\t" "v_mfma_f32_16x16x32_bf16 v[112:115], v[60:63], v[92:95], v[112:115]\n\t"
    "ds_read_b64_tr_b16 v[76:77], v228 offset:14336\n\t" "ds_read_b64_tr_b16 v[78:79], v228 offset:14592\n\t" "ds_read_b128 v[108:111], v229 offset:16832\n\t" "s_waitcnt lgkmcnt(9)\n\t"
    "v_mfma_f32_16x16x32_bf16 v[112:115], v[64:67], v[96:99], v[112:115]\n\t" "s_waitcnt lgkmcnt(6)\n\t" "v_mfma_f32_16x16x32_bf16 v[112:115], v[68:71], v[100:103], v[112:115]\n\t"
    "s_waitcnt lgkmcnt(3)\n\t" "v_mfma_f32_16x16x32_bf16 v[112:115], v[72:75], v[104:107], v[112:115]\n\t" "s_waitcnt lgkmcnt(0)\n\t"
    "v_mfma_f32_16x16x32_bf16 v[112:115], v[76:79], v[108:111], v[112:115]\n\t" "s_nop 7\n\t" "v_perm_b32 v116, v112, v115, s54\n\t" "v_perm_b32 v117, v113, v115, s55\n\t"
    "v_perm_b32 v118, v114, v115, s56\n\t" "v_xad_u32 v119, v116, v117, s62\n\t" "s_mov_b64 exec, s[60:61]\n\t" "v_xad_u32 v119, v118, v117, v119\n\t"
    "global_store_dwordx4 v229, v[116:119], %[su] sc1\n\t" "s_mov_b64 exec, -1\n\t" ".Lns_%=:\n\t"
    : "+{v204}"(cv), "=&{v205}"(tv), "+{s[62:63]}"(tag)
    : "{v196}"(d0), "{v197}"(d1), "{v198}"(d2), "{v199}"(d3), [sl] "s"(sl), [xl] "s"(xl), [xh_] "s"(xh_), [k0] "s"(k0), "{v233}"(vcw), "{v228}"(vsr), "{v229}"(vsc), [t0] "v"(t0), [sq] "s"(sq), [su] "s"(su), [wid] "s"(wid)
    : "v48", "v49", "v50", "v51", "v52", "v53", "v54", "v55", "v56", "v57", "v58", "v59", "v60", "v61", "v62", "v63", "v64", "v65", "v66", "v67", "v68", "v69", "v70", "v71", "v72", "v73", "v74", "v75", "v76", "v77", "v78", "v79", "v80", "v81", "v82", "v83", "v84", "v85", "v86", "v87", "v88", "v89", "v90", "v91", "v92", "v93", "v94", "v95", "v96", "v97", "v98", "v99", "v100", "v101", "v102", "v103", "v104", "v105", "v106", "v107", "v108", "v109", "v110", "v111", "v112", "v113", "v114", "v115", "v116", "v117", "v118", "v119", "v206", "s45", "s54", "s55", "s56", "s60", "s61", "s64", "s65", "scc", "memory");
    }
    asm volatile("" : "+v"(tid)); lane = tid & 63;
    const int fr = lane & 15, g = lane >> 4, q = (lane >> 2) & 3, ls = ((tid >> 3) - 4 * rot) & 31;
    unsigned vb = (unsigned)((rot * 128 + wid * 8 + (lane >> 3)) * 6144 + (((lane & 7) ^ ((((lane >> 4) & 1) | ((wid & 1) << 1)) << 1)) << 4));
    unsigned dh0 = dx, dh1 = dh0 + 16384, xr0 = sl + RING + ((lane >> 3) & 1) * 12288 + (lane & 7) * 128 + ((g ^ (lane & 7)) << 4), xr1 = xr0 ^ 64;
    unsigned vh = ldsb + HALO + (g * 16 + (fr ^ g)) * 16, vdt = ldsb + (lane & 3) * 64 + g * 16, vhd = vdt + wid * 512, vhx = vh + wid * 2048;
    unsigned vg = tid * 16, vtw = ldsb + ls * 256 + (tid & 7) * 8, vhw = ldsb + HALO + ls * 1024 + ((tid >> 1) & 3) * 272 + (tid & 1) * 8, vs8 = ldsb + SCT + (tid >> 3) * 16;
    unsigned vpr = ldsb + PH + fr * 8, vph = vpr + wid * 128, vm = (tid >> 3) * 98304 + (tid & 7) * 16; vbo = f_vb(wid, lane);
    unsigned vr0 = ldsb + RING + (g & 1) * 1024 + (g >> 1) * 6144 + q * 128 + ((wid ^ ((q >> 1) | ((g & 1) << 1))) << 5) + (lane & 3) * 8, vr1 = vr0 ^ 32, vr2 = vr0 ^ 64, vr3 = vr0 ^ 96;
    unsigned vr4 = vr0 + 61440, vr5 = vr1 + 61440, vr6 = vr2 + 61440, vr7 = vr3 + 61440;
    const unsigned l0 = ldsb, lh = ldsb + HALO, lt = lh + 20480 + wid * 128, lr = lh + 22528 + wid * 256, w128 = wid * 128, w4k = wid * 4096, xw = w4k + 4096 + (wid == 1 ? 2048 : 0), xh = (wid >> 1) * 2048 + (wid & 1) * 8, ox = (wid >> 1) * 2048, kh = k0 + w128;
    const int rw = rb * 4 + wid;
    const char* sb = (const char*)(w + 1024 + cb * 64); const char* sg = ws + W_PART + 4096; const u16* sm = wmod + (size_t)gk * 256 * 6144 + 2048 + gc * 64;
    const char* sf = ws + W_TAGS + (OF + (rb * 16 + cb) * 8) * 8; const char* so2 = ws + W_O + (size_t)(rb * 4 + wid) * 65536 + cb * 4096;
    const int l14 = min(lane, 55), r14 = l14 / 14;
    const unsigned fo = lane == 8 ? W_TAGS + (OF + (rb * 16 + cb) * 8) * 8 : lane == 9 ? W_TAGS + (YF + (rb * 16 + cb) * 8) * 8 : lane == 10 ? W_TAGS + (HF + (((xcd & 1) * 4 + (loc & 3)) * 32 + (xcd >> 1) * 8 + (loc >> 2)) * 8) * 8
                                  : W_FLAGS + (bx * 4 + wid) * 128;
    const char* t2 = lane < 8 ? code + min(32768 + (ww * 8 + lane) * 128, len - 4) : lane < 12 ? ws + fo : (const char*)wo + ((ww * 2 + (lane & 1)) * 4 + (xcd >> 1)) * 2048 + (xcd & 1) * 1024 + ((384 - (unsigned)(size_t)wo) & 896);
    unsigned t3 = ((xcd & 1) * 512 + (ww & 63) * 4 + r14) * 7168 + (xcd >> 1) * 1792 + (l14 - r14 * 14) * 128; const u16* pf = (ww & 64) ? wu : wg; const char* ps = (const char*)pf + ((xcd & 1) * 512 + (ww & 63) * 4) * 7168 + (xcd >> 1) * 1792;
    asm volatile( "s_mov_b32 s43, 0xffa30000\n\t" "s_mov_b32 s46, 0x00100001\n\t" "s_mov_b32 s47, 0x10000100\n\t" "s_lshl_b64 s[48:49], s[46:47], 1\n\t" "s_lshl_b64 s[50:51], s[46:47], 2\n\t"
    "s_lshl_b64 s[52:53], s[46:47], 3\n\t" "s_mov_b32 s57, 0x04000c0c\n\t" "s_mov_b32 s58, 0x0706000c\n\t" "s_add_u32 s45, %[k0], 256\n\t" "s_and_b32 s45, s45, 0x7ff\n\t" "s_add_u32 s64, %[xl], s45\n\t"
    "s_addc_u32 s65, %[xh_], 0\n\t" "s_add_u32 m0, %[sl], 47104\n\t" "s_nop 0\n\t" "global_load_lds_dwordx4 v196, s[64:65] offset:0\n\t" "s_add_u32 m0, %[sl], 59392\n\t" "s_nop 0\n\t"
    "global_load_lds_dwordx4 v197, s[64:65] offset:0\n\t" "s_add_u32 m0, %[sl], 49152\n\t" "s_nop 0\n\t" "global_load_lds_dwordx4 v198, s[64:65] offset:0\n\t" "s_add_u32 m0, %[sl], 61440\n\t"
    "s_nop 0\n\t" "global_load_lds_dwordx4 v199, s[64:65] offset:0\n\t" "s_add_u32 m0, %[sl], 69504\n\t" "s_nop 0\n\t" "global_load_lds_dwordx4 v196, s[64:65] offset:128\n\t"
    "s_add_u32 m0, %[sl], 81792\n\t" "s_nop 0\n\t" "global_load_lds_dwordx4 v197, s[64:65] offset:128\n\t" "s_add_u32 m0, %[sl], 71552\n\t" "s_nop 0\n\t"
    "global_load_lds_dwordx4 v198, s[64:65] offset:128\n\t" "s_add_u32 m0, %[sl], 83840\n\t" "s_nop 0\n\t" "global_load_lds_dwordx4 v199, s[64:65] offset:128\n\t" "ds_read_b128 v[252:255], v234\n\t"
    "v_mov_b32 v112, 0x3f803f80\n\t" "v_mov_b32 v113, 0x3f803f80\n\t" "v_mov_b32 v114, 0x3f803f80\n\t" "v_mov_b32 v115, 0x3f803f80\n\t" "s_mov_b32 s60, 0xfffcfffc\n\t" "s_mov_b32 s61, s60\n\t"
    "s_mov_b32 s44, 0x100000\n\t" "s_barrier\n\t" "s_waitcnt vmcnt(13)\n\t" "ds_read_b128 a[0:3], v202 offset:0\n\t" "ds_read_b128 a[4:7], v202 offset:2048\n\t"
    "ds_read_b128 a[8:11], v203 offset:0\n\t" "ds_read_b128 a[12:15], v203 offset:2048\n\t" "s_waitcnt lgkmcnt(0)\n\t" "s_add_u32 s45, %[kh], 0\n\t" "s_and_b32 s45, s45, 0x7ff\n\t"
    "s_add_u32 s40, %[xl], s45\n\t" "s_addc_u32 s41, %[xh_], 0\n\t" "s_add_u32 m0, %[sl], 20480\n\t" "s_nop 0\n\t" "global_load_lds_dwordx4 v200, s[40:41]\n\t" "s_add_u32 m0, %[sl], 32768\n\t"
    "s_nop 0\n\t" "global_load_lds_dwordx4 v201, s[40:41]\n\t" "s_add_u32 s45, %[kh], 512\n\t" "s_and_b32 s45, s45, 0x7ff\n\t" "s_add_u32 s40, %[xl], s45\n\t" "s_addc_u32 s41, %[xh_], 0\n\t"
    "s_add_u32 m0, %[sl], 22528\n\t" "s_nop 0\n\t" "global_load_lds_dwordx4 v200, s[40:41]\n\t" "s_add_u32 m0, %[sl], 34816\n\t" "s_nop 0\n\t" "global_load_lds_dwordx4 v201, s[40:41]\n\t"
    "s_add_u32 m0, %[sl], 96256\n\t" "s_nop 0\n\t" "global_load_lds_dwordx4 v0, %[sb] offset:-2048\n\t" "global_load_lds_dwordx4 v0, %[sb] offset:0\n\t" "global_load_lds_dwordx4 v0, %[sb] offset:2048\n\t"
    "v_add_u32 v0, 0x30000, v0\n\t" "s_add_u32 m0, %[sl], 108544\n\t" "s_nop 0\n\t" "global_load_lds_dwordx4 v0, %[sb] offset:-2048\n\t" "global_load_lds_dwordx4 v0, %[sb] offset:0\n\t"
    "global_load_lds_dwordx4 v0, %[sb] offset:2048\n\t" "v_add_u32 v0, 0x30000, v0\n\t" "s_waitcnt vmcnt(19)\n\t" "v_mfma_f32_16x16x32_bf16 v[116:119], v[112:115], a[0:3], 0\n\t"
    "ds_read_b128 a[16:19], v202 offset:4096\n\t" "ds_read_b128 a[20:23], v202 offset:24576\n\t" "v_mfma_f32_16x16x32_bf16 v[128:131], a[0:3], a[0:3], 0\n\t" "ds_read_b128 a[24:27], v203 offset:4096\n\t"
    "ds_read_b128 a[28:31], v203 offset:24576\n\t" "v_mfma_f32_16x16x32_bf16 v[120:123], v[112:115], a[4:7], 0\n\t" "s_add_u32 s45, %[kh], 1024\n\t" "s_and_b32 s45, s45, 0x7ff\n\t"
    "s_add_u32 s40, %[xl], s45\n\t" "s_addc_u32 s41, %[xh_], 0\n\t" "v_mfma_f32_16x16x32_bf16 v[132:135], a[4:7], a[4:7], 0\n\t" "v_mfma_f32_16x16x32_bf16 v[116:119], v[112:115], a[8:11], v[116:119]\n\t"
    "v_mfma_f32_16x16x32_bf16 v[128:131], a[8:11], a[8:11], v[128:131]\n\t" "s_waitcnt lgkmcnt(0)\n\t" "s_add_u32 m0, %[sl], 24576\n\t"
    "v_mfma_f32_16x16x32_bf16 v[120:123], v[112:115], a[12:15], v[120:123]\n\t" "global_load_lds_dwordx4 v200, s[40:41]\n\t" "s_add_u32 m0, %[sl], 36864\n\t"
    "v_mfma_f32_16x16x32_bf16 v[132:135], a[12:15], a[12:15], v[132:135]\n\t" "global_load_lds_dwordx4 v201, s[40:41]\n\t" "s_add_u32 s45, %[kh], 1536\n\t" "s_and_b32 s45, s45, 0x7ff\n\t"
    "s_add_u32 s40, %[xl], s45\n\t" "s_addc_u32 s41, %[xh_], 0\n\t" "s_add_u32 m0, %[sl], 45056\n\t" "s_nop 0\n\t" "global_load_lds_dwordx4 v200, s[40:41]\n\t" "s_add_u32 m0, %[sl], 57344\n\t"
    "s_nop 0\n\t" "global_load_lds_dwordx4 v201, s[40:41]\n\t" "s_waitcnt vmcnt(18)\n\t" "v_mfma_f32_16x16x32_bf16 v[116:119], v[112:115], a[16:19], v[116:119]\n\t"
    "ds_read_b128 a[32:35], v202 offset:26624\n\t" "ds_read_b128 a[36:39], v202 offset:28672\n\t" "v_mfma_f32_16x16x32_bf16 v[128:131], a[16:19], a[16:19], v[128:131]\n\t"
    "ds_read_b128 a[40:43], v203 offset:26624\n\t" "ds_read_b128 a[44:47], v203 offset:28672\n\t" "v_mfma_f32_16x16x32_bf16 v[120:123], v[112:115], a[20:23], v[120:123]\n\t"
    "s_add_u32 s45, %[k0], 512\n\t" "s_and_b32 s45, s45, 0x7ff\n\t" "s_add_u32 s64, %[xl], s45\n\t" "s_addc_u32 s65, %[xh_], 0\n\t"
    "v_mfma_f32_16x16x32_bf16 v[132:135], a[20:23], a[20:23], v[132:135]\n\t" "v_mfma_f32_16x16x32_bf16 v[116:119], v[112:115], a[24:27], v[116:119]\n\t"
    "v_mfma_f32_16x16x32_bf16 v[128:131], a[24:27], a[24:27], v[128:131]\n\t" "s_waitcnt lgkmcnt(0)\n\t" "s_add_u32 m0, %[sl], 47104\n\t"
    "v_mfma_f32_16x16x32_bf16 v[120:123], v[112:115], a[28:31], v[120:123]\n\t" "global_load_lds_dwordx4 v196, s[64:65] offset:0\n\t" "s_add_u32 m0, %[sl], 59392\n\t"
    "v_mfma_f32_16x16x32_bf16 v[132:135], a[28:31], a[28:31], v[132:135]\n\t" "global_load_lds_dwordx4 v197, s[64:65] offset:0\n\t" "s_add_u32 m0, %[sl], 49152\n\t" "s_nop 0\n\t"
    "global_load_lds_dwordx4 v198, s[64:65] offset:0\n\t" "s_add_u32 m0, %[sl], 61440\n\t" "s_nop 0\n\t" "global_load_lds_dwordx4 v199, s[64:65] offset:0\n\t" "s_waitcnt vmcnt(18)\n\t"
    "v_mfma_f32_16x16x32_bf16 v[116:119], v[112:115], a[32:35], v[116:119]\n\t" "ds_read_b128 a[48:51], v202 offset:49152\n\t" "ds_read_b128 a[52:55], v202 offset:51200\n\t"
    "v_mfma_f32_16x16x32_bf16 v[128:131], a[32:35], a[32:35], v[128:131]\n\t" "ds_read_b128 a[56:59], v203 offset:49152\n\t" "ds_read_b128 a[60:63], v203 offset:51200\n\t"
    "v_mfma_f32_16x16x32_bf16 v[120:123], v[112:115], a[36:39], v[120:123]\n\t" "v_mfma_f32_16x16x32_bf16 v[132:135], a[36:39], a[36:39], v[132:135]\n\t"
    "v_mfma_f32_16x16x32_bf16 v[116:119], v[112:115], a[40:43], v[116:119]\n\t" "v_mfma_f32_16x16x32_bf16 v[128:131], a[40:43], a[40:43], v[128:131]\n\t" "s_waitcnt lgkmcnt(0)\n\t"
    "s_add_u32 m0, %[sl], 69504\n\t" "v_mfma_f32_16x16x32_bf16 v[120:123], v[112:115], a[44:47], v[120:123]\n\t" "global_load_lds_dwordx4 v196, s[64:65] offset:128\n\t" "s_add_u32 m0, %[sl], 81792\n\t"
    "v_mfma_f32_16x16x32_bf16 v[132:135], a[44:47], a[44:47], v[132:135]\n\t" "global_load_lds_dwordx4 v197, s[64:65] offset:128\n\t" "s_add_u32 m0, %[sl], 71552\n\t" "s_nop 0\n\t"
    "global_load_lds_dwordx4 v198, s[64:65] offset:128\n\t" "s_add_u32 m0, %[sl], 83840\n\t" "s_nop 0\n\t" "global_load_lds_dwordx4 v199, s[64:65] offset:128\n\t" "s_waitcnt vmcnt(18)\n\t"
    "v_mfma_f32_16x16x32_bf16 v[116:119], v[112:115], a[48:51], v[116:119]\n\t" "ds_read_b128 v[48:51], v202 offset:0\n\t" "ds_read_b128 v[20:23], v202 offset:2048\n\t"
    "v_mfma_f32_16x16x32_bf16 v[128:131], a[48:51], a[48:51], v[128:131]\n\t" "ds_read_b128 v[52:55], v203 offset:0\n\t" "ds_read_b128 v[24:27], v203 offset:2048\n\t"
    "v_mfma_f32_16x16x32_bf16 v[120:123], v[112:115], a[52:55], v[120:123]\n\t" "s_add_u32 s45, %[k0], 768\n\t" "s_and_b32 s45, s45, 0x7ff\n\t" "s_add_u32 s64, %[xl], s45\n\t"
    "s_addc_u32 s65, %[xh_], 0\n\t" "v_mfma_f32_16x16x32_bf16 v[132:135], a[52:55], a[52:55], v[132:135]\n\t" "v_mfma_f32_16x16x32_bf16 v[116:119], v[112:115], a[56:59], v[116:119]\n\t"
    "v_mfma_f32_16x16x32_bf16 v[128:131], a[56:59], a[56:59], v[128:131]\n\t" "s_waitcnt lgkmcnt(0)\n\t" "s_add_u32 m0, %[sl], 20480\n\t"
    "v_mfma_f32_16x16x32_bf16 v[120:123], v[112:115], a[60:63], v[120:123]\n\t" "global_load_lds_dwordx4 v196, s[64:65] offset:0\n\t" "s_add_u32 m0, %[sl], 32768\n\t"
    "v_mfma_f32_16x16x32_bf16 v[132:135], a[60:63], a[60:63], v[132:135]\n\t" "global_load_lds_dwordx4 v197, s[64:65] offset:0\n\t" "s_add_u32 m0, %[sl], 22528\n\t" "s_nop 0\n\t"
    "global_load_lds_dwordx4 v198, s[64:65] offset:0\n\t" "s_add_u32 m0, %[sl], 34816\n\t" "s_nop 0\n\t" "global_load_lds_dwordx4 v199, s[64:65] offset:0\n\t" "s_waitcnt vmcnt(12)\n\t"
    "v_mfma_f32_16x16x32_bf16 v[124:127], v[112:115], v[48:51], 0\n\t" "ds_read_b128 v[28:31], v202 offset:4096\n\t" "ds_read_b128 v[36:39], v202 offset:24576\n\t"
    "v_mfma_f32_16x16x32_bf16 v[136:139], v[48:51], v[48:51], 0\n\t" "ds_read_b128 v[32:35], v203 offset:4096\n\t" "ds_read_b128 v[40:43], v203 offset:24576\n\t"
    "v_mfma_f32_16x16x32_bf16 v[124:127], v[112:115], v[20:23], v[124:127]\n\t" "v_mfma_f32_16x16x32_bf16 v[136:139], v[20:23], v[20:23], v[136:139]\n\t"
    "v_mfma_f32_16x16x32_bf16 v[124:127], v[112:115], v[52:55], v[124:127]\n\t" "v_mfma_f32_16x16x32_bf16 v[136:139], v[52:55], v[52:55], v[136:139]\n\t" "s_waitcnt lgkmcnt(0)\n\t"
    "s_add_u32 m0, %[sl], 24448\n\t" "v_mfma_f32_16x16x32_bf16 v[124:127], v[112:115], v[24:27], v[124:127]\n\t" "global_load_lds_dwordx4 v196, s[64:65] offset:128\n\t" "s_add_u32 m0, %[sl], 36736\n\t"
    "v_mfma_f32_16x16x32_bf16 v[136:139], v[24:27], v[24:27], v[136:139]\n\t" "global_load_lds_dwordx4 v197, s[64:65] offset:128\n\t" "s_add_u32 m0, %[sl], 44928\n\t" "s_nop 0\n\t"
    "global_load_lds_dwordx4 v198, s[64:65] offset:128\n\t" "s_add_u32 m0, %[sl], 57216\n\t" "s_nop 0\n\t" "global_load_lds_dwordx4 v199, s[64:65] offset:128\n\t" "s_mov_b64 s[40:41], %[sg]\n\t"
    "global_load_dwordx4 v[80:83], v230, s[40:41] offset:-4096 sc1\n\t" "global_load_dwordx4 v[96:99], v230, s[40:41] offset:0 sc1\n\t" "s_add_u32 s40, s40, 8192\n\t" "s_addc_u32 s41, s41, 0\n\t"
    "global_load_dwordx4 v[84:87], v230, s[40:41] offset:-4096 sc1\n\t" "global_load_dwordx4 v[100:103], v230, s[40:41] offset:0 sc1\n\t" "s_add_u32 s40, s40, 8192\n\t" "s_addc_u32 s41, s41, 0\n\t"
    "global_load_dwordx4 v[88:91], v230, s[40:41] offset:-4096 sc1\n\t" "global_load_dwordx4 v[104:107], v230, s[40:41] offset:0 sc1\n\t" "s_add_u32 s40, s40, 8192\n\t" "s_addc_u32 s41, s41, 0\n\t"
    "global_load_dwordx4 v[92:95], v230, s[40:41] offset:-4096 sc1\n\t" "global_load_dwordx4 v[108:111], v230, s[40:41] offset:0 sc1\n\t" "s_waitcnt vmcnt(20)\n\t"
    "v_mfma_f32_16x16x32_bf16 v[124:127], v[112:115], v[28:31], v[124:127]\n\t" "ds_read_b128 a[64:67], v202 offset:26624\n\t" "ds_read_b128 a[68:71], v202 offset:28672\n\t"
    "v_mfma_f32_16x16x32_bf16 v[136:139], v[28:31], v[28:31], v[136:139]\n\t" "ds_read_b128 a[72:75], v203 offset:26624\n\t" "ds_read_b128 a[76:79], v203 offset:28672\n\t"
    "v_mfma_f32_16x16x32_bf16 v[124:127], v[112:115], v[36:39], v[124:127]\n\t" "s_add_u32 s45, %[k0], 1024\n\t" "s_and_b32 s45, s45, 0x7ff\n\t" "s_add_u32 s64, %[xl], s45\n\t"
    "s_addc_u32 s65, %[xh_], 0\n\t" "v_mfma_f32_16x16x32_bf16 v[136:139], v[36:39], v[36:39], v[136:139]\n\t" "v_mfma_f32_16x16x32_bf16 v[124:127], v[112:115], v[32:35], v[124:127]\n\t"
    "v_mfma_f32_16x16x32_bf16 v[136:139], v[32:35], v[32:35], v[136:139]\n\t" "s_waitcnt lgkmcnt(0)\n\t" "s_add_u32 m0, %[sl], 47104\n\t"
    "v_mfma_f32_16x16x32_bf16 v[124:127], v[112:115], v[40:43], v[124:127]\n\t" "global_load_lds_dwordx4 v196, s[64:65] offset:0\n\t" "s_add_u32 m0, %[sl], 59392\n\t"
    "v_mfma_f32_16x16x32_bf16 v[136:139], v[40:43], v[40:43], v[136:139]\n\t" "global_load_lds_dwordx4 v197, s[64:65] offset:0\n\t" "s_add_u32 m0, %[sl], 49152\n\t" "s_nop 0\n\t"
    "global_load_lds_dwordx4 v198, s[64:65] offset:0\n\t" "s_add_u32 m0, %[sl], 61440\n\t" "s_nop 0\n\t" "global_load_lds_dwordx4 v199, s[64:65] offset:0\n\t" "s_waitcnt vmcnt(20)\n\t"
    "v_mfma_f32_16x16x32_bf16 v[116:119], v[112:115], a[64:67], v[116:119]\n\t" "ds_read_b128 a[80:83], v202 offset:49152\n\t" "ds_read_b128 a[84:87], v202 offset:51200\n\t"
    "v_mfma_f32_16x16x32_bf16 v[128:131], a[64:67], a[64:67], v[128:131]\n\t" "ds_read_b128 a[88:91], v203 offset:49152\n\t" "ds_read_b128 a[92:95], v203 offset:51200\n\t"
    "v_mfma_f32_16x16x32_bf16 v[120:123], v[112:115], a[68:71], v[120:123]\n\t" "v_cndmask_b32 v142, 0, v136, s[46:47]\n\t" "v_cndmask_b32 v142, v142, v137, s[48:49]\n\t"
    "v_cndmask_b32 v142, v142, v138, s[50:51]\n\t" "v_cndmask_b32 v142, v142, v139, s[52:53]\n\t" "v_mfma_f32_16x16x32_bf16 v[132:135], a[68:71], a[68:71], v[132:135]\n\t" "v_mov_b32 v143, v142\n\t"
    "s_nop 1\n\t" "v_permlane16_swap_b32 v142, v143\n\t" "v_add_f32 v142, v142, v143\n\t" "v_mfma_f32_16x16x32_bf16 v[116:119], v[112:115], a[72:75], v[116:119]\n\t" "v_mov_b32 v143, v142\n\t"
    "s_nop 1\n\t" "v_permlane32_swap_b32 v142, v143\n\t" "v_add_f32 v142, v142, v143\n\t" "v_mfma_f32_16x16x32_bf16 v[128:131], a[72:75], a[72:75], v[128:131]\n\t" "s_waitcnt lgkmcnt(0)\n\t"
    "s_add_u32 m0, %[sl], 69504\n\t" "v_mfma_f32_16x16x32_bf16 v[120:123], v[112:115], a[76:79], v[120:123]\n\t" "global_load_lds_dwordx4 v196, s[64:65] offset:128\n\t" "s_add_u32 m0, %[sl], 81792\n\t"
    "v_mfma_f32_16x16x32_bf16 v[132:135], a[76:79], a[76:79], v[132:135]\n\t" "global_load_lds_dwordx4 v197, s[64:65] offset:128\n\t" "s_add_u32 m0, %[sl], 71552\n\t" "s_nop 0\n\t"
    "global_load_lds_dwordx4 v198, s[64:65] offset:128\n\t" "s_add_u32 m0, %[sl], 83840\n\t" "s_nop 0\n\t" "global_load_lds_dwordx4 v199, s[64:65] offset:128\n\t" "s_waitcnt vmcnt(20)\n\t"
    "v_mfma_f32_16x16x32_bf16 v[116:119], v[112:115], a[80:83], v[116:119]\n\t" "ds_read_b128 a[96:99], v202 offset:0\n\t" "ds_read_b128 a[100:103], v202 offset:2048\n\t"
    "v_mfma_f32_16x16x32_bf16 v[128:131], a[80:83], a[80:83], v[128:131]\n\t" "ds_read_b128 a[104:107], v203 offset:0\n\t" "ds_read_b128 a[108:111], v203 offset:2048\n\t"
    "v_mfma_f32_16x16x32_bf16 v[120:123], v[112:115], a[84:87], v[120:123]\n\t" "s_add_u32 s45, %[k0], 1280\n\t" "s_and_b32 s45, s45, 0x7ff\n\t" "s_add_u32 s64, %[xl], s45\n\t"
    "s_addc_u32 s65, %[xh_], 0\n\t" "v_mfma_f32_16x16x32_bf16 v[132:135], a[84:87], a[84:87], v[132:135]\n\t" "v_mfma_f32_16x16x32_bf16 v[116:119], v[112:115], a[88:91], v[116:119]\n\t"
    "v_mfma_f32_16x16x32_bf16 v[128:131], a[88:91], a[88:91], v[128:131]\n\t" "s_waitcnt lgkmcnt(0)\n\t" "s_add_u32 m0, %[sl], 20480\n\t"
    "v_mfma_f32_16x16x32_bf16 v[120:123], v[112:115], a[92:95], v[120:123]\n\t" "global_load_lds_dwordx4 v196, s[64:65] offset:0\n\t" "s_add_u32 m0, %[sl], 32768\n\t"
    "v_mfma_f32_16x16x32_bf16 v[132:135], a[92:95], a[92:95], v[132:135]\n\t" "global_load_lds_dwordx4 v197, s[64:65] offset:0\n\t" "s_add_u32 m0, %[sl], 22528\n\t" "s_nop 0\n\t"
    "global_load_lds_dwordx4 v198, s[64:65] offset:0\n\t" "s_add_u32 m0, %[sl], 34816\n\t" "s_nop 0\n\t" "global_load_lds_dwordx4 v199, s[64:65] offset:0\n\t" "s_waitcnt vmcnt(20)\n\t"
    "v_mfma_f32_16x16x32_bf16 v[116:119], v[112:115], a[96:99], v[116:119]\n\t" "ds_read_b128 a[112:115], v202 offset:4096\n\t" "ds_read_b128 a[116:119], v202 offset:24576\n\t"
    "v_mfma_f32_16x16x32_bf16 v[128:131], a[96:99], a[96:99], v[128:131]\n\t" "ds_read_b128 a[120:123], v203 offset:4096\n\t" "ds_read_b128 a[124:127], v203 offset:24576\n\t"
    "v_mfma_f32_16x16x32_bf16 v[120:123], v[112:115], a[100:103], v[120:123]\n\t" "v_mfma_f32_16x16x32_bf16 v[132:135], a[100:103], a[100:103], v[132:135]\n\t"
    "v_mfma_f32_16x16x32_bf16 v[116:119], v[112:115], a[104:107], v[116:119]\n\t" "v_mfma_f32_16x16x32_bf16 v[128:131], a[104:107], a[104:107], v[128:131]\n\t" "s_waitcnt lgkmcnt(0)\n\t"
    "s_add_u32 m0, %[sl], 24448\n\t" "v_mfma_f32_16x16x32_bf16 v[120:123], v[112:115], a[108:111], v[120:123]\n\t" "global_load_lds_dwordx4 v196, s[64:65] offset:128\n\t" "s_add_u32 m0, %[sl], 36736\n\t"
    "v_mfma_f32_16x16x32_bf16 v[132:135], a[108:111], a[108:111], v[132:135]\n\t" "global_load_lds_dwordx4 v197, s[64:65] offset:128\n\t" "s_add_u32 m0, %[sl], 44928\n\t" "s_nop 0\n\t"
    "global_load_lds_dwordx4 v198, s[64:65] offset:128\n\t" "s_add_u32 m0, %[sl], 57216\n\t" "s_nop 0\n\t" "global_load_lds_dwordx4 v199, s[64:65] offset:128\n\t" "s_waitcnt vmcnt(12)\n\t"
    "v_mfma_f32_16x16x32_bf16 v[116:119], v[112:115], a[112:115], v[116:119]\n\t" "ds_read_b128 a[128:131], v202 offset:26624\n\t" "ds_read_b128 a[132:135], v202 offset:28672\n\t"
    "v_mfma_f32_16x16x32_bf16 v[128:131], a[112:115], a[112:115], v[128:131]\n\t" "ds_read_b128 a[136:139], v203 offset:26624\n\t" "ds_read_b128 a[140:143], v203 offset:28672\n\t"
    "v_mfma_f32_16x16x32_bf16 v[120:123], v[112:115], a[116:119], v[120:123]\n\t" "s_add_u32 s45, %[k0], 1536\n\t" "s_and_b32 s45, s45, 0x7ff\n\t" "s_add_u32 s64, %[xl], s45\n\t"
    "s_addc_u32 s65, %[xh_], 0\n\t" "v_xad_u32 v144, v80, v81, s62\n\t" "v_xad_u32 v145, v84, v85, s62\n\t" "v_xad_u32 v146, v88, v89, s62\n\t"
    "v_mfma_f32_16x16x32_bf16 v[132:135], a[116:119], a[116:119], v[132:135]\n\t" "v_xad_u32 v147, v92, v93, s62\n\t" "v_xad_u32 v148, v96, v97, s62\n\t" "v_xad_u32 v149, v100, v101, s62\n\t"
    "v_xad_u32 v150, v104, v105, s62\n\t" "v_mfma_f32_16x16x32_bf16 v[116:119], v[112:115], a[120:123], v[116:119]\n\t" "v_xad_u32 v151, v108, v109, s62\n\t" "v_xad_u32 v144, v82, v81, v144\n\t"
    "v_xad_u32 v145, v86, v85, v145\n\t" "v_xad_u32 v146, v90, v89, v146\n\t" "v_mfma_f32_16x16x32_bf16 v[128:131], a[120:123], a[120:123], v[128:131]\n\t" "v_xad_u32 v147, v94, v93, v147\n\t"
    "v_xad_u32 v148, v98, v97, v148\n\t" "v_xad_u32 v149, v102, v101, v149\n\t" "v_xad_u32 v150, v106, v105, v150\n\t" "s_waitcnt lgkmcnt(0)\n\t" "s_add_u32 m0, %[sl], 47104\n\t"
    "v_mfma_f32_16x16x32_bf16 v[120:123], v[112:115], a[124:127], v[120:123]\n\t" "global_load_lds_dwordx4 v196, s[64:65] offset:0\n\t" "s_add_u32 m0, %[sl], 59392\n\t"
    "v_mfma_f32_16x16x32_bf16 v[132:135], a[124:127], a[124:127], v[132:135]\n\t" "global_load_lds_dwordx4 v197, s[64:65] offset:0\n\t" "s_add_u32 m0, %[sl], 49152\n\t" "s_nop 0\n\t"
    "global_load_lds_dwordx4 v198, s[64:65] offset:0\n\t" "s_add_u32 m0, %[sl], 61440\n\t" "s_nop 0\n\t" "global_load_lds_dwordx4 v199, s[64:65] offset:0\n\t" "s_waitcnt vmcnt(12)\n\t"
    "v_mfma_f32_16x16x32_bf16 v[116:119], v[112:115], a[128:131], v[116:119]\n\t" "ds_read_b128 a[144:147], v202 offset:49152\n\t" "ds_read_b128 a[148:151], v202 offset:51200\n\t"
    "v_mfma_f32_16x16x32_bf16 v[128:131], a[128:131], a[128:131], v[128:131]\n\t" "ds_read_b128 a[152:155], v203 offset:49152\n\t" "ds_read_b128 a[156:159], v203 offset:51200\n\t"
    "v_mfma_f32_16x16x32_bf16 v[120:123], v[112:115], a[132:135], v[120:123]\n\t" "v_xad_u32 v151, v110, v109, v151\n\t" "v_xor_b32 v140, v144, v83\n\t"
    "v_bitop3_b32 v140, v140, v145, v87 bitop3:0xf6\n\t" "v_bitop3_b32 v140, v140, v146, v91 bitop3:0xf6\n\t" "v_mfma_f32_16x16x32_bf16 v[132:135], a[132:135], a[132:135], v[132:135]\n\t"
    "v_bitop3_b32 v140, v140, v147, v95 bitop3:0xf6\n\t" "v_bitop3_b32 v140, v140, v148, v99 bitop3:0xf6\n\t" "v_bitop3_b32 v140, v140, v149, v103 bitop3:0xf6\n\t"
    "v_bitop3_b32 v140, v140, v150, v107 bitop3:0xf6\n\t" "v_mfma_f32_16x16x32_bf16 v[116:119], v[112:115], a[136:139], v[116:119]\n\t" "v_bitop3_b32 v140, v140, v151, v111 bitop3:0xf6\n\t"
    "v_cmp_ne_u32 vcc, 0, v140\n\t" "v_perm_b32 v83, v80, v81, s57\n\t" "v_perm_b32 v87, v84, v85, s57\n\t" "v_mfma_f32_16x16x32_bf16 v[128:131], a[136:139], a[136:139], v[128:131]\n\t"
    "v_perm_b32 v91, v88, v89, s57\n\t" "v_perm_b32 v95, v92, v93, s57\n\t" "v_perm_b32 v99, v96, v97, s57\n\t" "v_perm_b32 v103, v100, v101, s57\n\t" "s_waitcnt lgkmcnt(0)\n\t"
    "s_add_u32 m0, %[sl], 69504\n\t" "v_mfma_f32_16x16x32_bf16 v[120:123], v[112:115], a[140:143], v[120:123]\n\t" "global_load_lds_dwordx4 v196, s[64:65] offset:128\n\t" "s_add_u32 m0, %[sl], 81792\n\t"
    "v_mfma_f32_16x16x32_bf16 v[132:135], a[140:143], a[140:143], v[132:135]\n\t" "global_load_lds_dwordx4 v197, s[64:65] offset:128\n\t" "s_add_u32 m0, %[sl], 71552\n\t" "s_nop 0\n\t"
    "global_load_lds_dwordx4 v198, s[64:65] offset:128\n\t" "s_add_u32 m0, %[sl], 83840\n\t" "s_nop 0\n\t" "global_load_lds_dwordx4 v199, s[64:65] offset:128\n\t" "s_waitcnt vmcnt(12)\n\t"
    "v_mfma_f32_16x16x32_bf16 v[116:119], v[112:115], a[144:147], v[116:119]\n\t" "ds_read_b128 a[160:163], v202 offset:0\n\t" "ds_read_b128 a[164:167], v202 offset:2048\n\t"
    "v_mfma_f32_16x16x32_bf16 v[128:131], a[144:147], a[144:147], v[128:131]\n\t" "ds_read_b128 a[168:171], v203 offset:0\n\t" "ds_read_b128 a[172:175], v203 offset:2048\n\t"
    "v_mfma_f32_16x16x32_bf16 v[120:123], v[112:115], a[148:151], v[120:123]\n\t" "s_add_u32 s45, %[k0], 1792\n\t" "s_and_b32 s45, s45, 0x7ff\n\t" "s_add_u32 s64, %[xl], s45\n\t"
    "s_addc_u32 s65, %[xh_], 0\n\t" "v_perm_b32 v107, v104, v105, s57\n\t" "v_perm_b32 v111, v108, v109, s57\n\t" "s_cbranch_vccnz .Lslow_%=\n\t" ".Lgo_%=:\n\t" "v_perm_b32 v83, v83, v82, s58\n\t"
    "v_mfma_f32_16x16x32_bf16 v[132:135], a[148:151], a[148:151], v[132:135]\n\t" "v_perm_b32 v87, v87, v86, s58\n\t" "v_perm_b32 v91, v91, v90, s58\n\t" "v_perm_b32 v95, v95, v94, s58\n\t"
    "v_perm_b32 v99, v99, v98, s58\n\t" "v_mfma_f32_16x16x32_bf16 v[116:119], v[112:115], a[152:155], v[116:119]\n\t" "v_perm_b32 v103, v103, v102, s58\n\t" "v_perm_b32 v107, v107, v106, s58\n\t"
    "v_perm_b32 v111, v111, v110, s58\n\t" "v_add_f32 v96, v96, v100\n\t" "v_mfma_f32_16x16x32_bf16 v[128:131], a[152:155], a[152:155], v[128:131]\n\t" "v_add_f32 v97, v97, v101\n\t"
    "v_add_f32 v98, v98, v102\n\t" "v_add_f32 v99, v99, v103\n\t" "v_add_f32 v80, v80, v84\n\t" "s_waitcnt lgkmcnt(0)\n\t" "s_add_u32 m0, %[sl], 20480\n\t"
    "v_mfma_f32_16x16x32_bf16 v[120:123], v[112:115], a[156:159], v[120:123]\n\t" "global_load_lds_dwordx4 v196, s[64:65] offset:0\n\t" "s_add_u32 m0, %[sl], 32768\n\t"
    "v_mfma_f32_16x16x32_bf16 v[132:135], a[156:159], a[156:159], v[132:135]\n\t" "global_load_lds_dwordx4 v197, s[64:65] offset:0\n\t" "s_add_u32 m0, %[sl], 22528\n\t" "s_nop 0\n\t"
    "global_load_lds_dwordx4 v198, s[64:65] offset:0\n\t" "s_add_u32 m0, %[sl], 34816\n\t" "s_nop 0\n\t" "global_load_lds_dwordx4 v199, s[64:65] offset:0\n\t" "s_waitcnt vmcnt(12)\n\t"
    "v_mfma_f32_16x16x32_bf16 v[116:119], v[112:115], a[160:163], v[116:119]\n\t" "ds_read_b128 a[176:179], v202 offset:4096\n\t" "ds_read_b128 a[180:183], v202 offset:24576\n\t"
    "v_mfma_f32_16x16x32_bf16 v[128:131], a[160:163], a[160:163], v[128:131]\n\t" "ds_read_b128 a[184:187], v203 offset:4096\n\t" "ds_read_b128 a[188:191], v203 offset:24576\n\t"
    "v_mfma_f32_16x16x32_bf16 v[120:123], v[112:115], a[164:167], v[120:123]\n\t" "v_add_f32 v81, v81, v85\n\t" "v_add_f32 v82, v82, v86\n\t" "v_add_f32 v83, v83, v87\n\t" "v_add_f32 v104, v104, v108\n\t"
    "v_mfma_f32_16x16x32_bf16 v[132:135], a[164:167], a[164:167], v[132:135]\n\t" "v_add_f32 v105, v105, v109\n\t" "v_add_f32 v106, v106, v110\n\t" "v_add_f32 v107, v107, v111\n\t"
    "v_add_f32 v88, v88, v92\n\t" "v_mfma_f32_16x16x32_bf16 v[116:119], v[112:115], a[168:171], v[116:119]\n\t" "v_add_f32 v89, v89, v93\n\t" "v_add_f32 v90, v90, v94\n\t" "v_add_f32 v91, v91, v95\n\t"
    "v_add_f32 v96, v96, v104\n\t" "v_mfma_f32_16x16x32_bf16 v[128:131], a[168:171], a[168:171], v[128:131]\n\t" "v_add_f32 v97, v97, v105\n\t" "v_add_f32 v98, v98, v106\n\t"
    "v_add_f32 v99, v99, v107\n\t" "v_add_f32 v80, v80, v88\n\t" "s_waitcnt lgkmcnt(0)\n\t" "s_add_u32 m0, %[sl], 24448\n\t" "v_mfma_f32_16x16x32_bf16 v[120:123], v[112:115], a[172:175], v[120:123]\n\t"
    "global_load_lds_dwordx4 v196, s[64:65] offset:128\n\t" "s_add_u32 m0, %[sl], 36736\n\t" "v_mfma_f32_16x16x32_bf16 v[132:135], a[172:175], a[172:175], v[132:135]\n\t"
    "global_load_lds_dwordx4 v197, s[64:65] offset:128\n\t" "s_add_u32 m0, %[sl], 44928\n\t" "s_nop 0\n\t" "global_load_lds_dwordx4 v198, s[64:65] offset:128\n\t" "s_add_u32 m0, %[sl], 57216\n\t"
    "s_nop 0\n\t" "global_load_lds_dwordx4 v199, s[64:65] offset:128\n\t" "s_waitcnt vmcnt(12)\n\t" "v_mfma_f32_16x16x32_bf16 v[116:119], v[112:115], a[176:179], v[116:119]\n\t"
    "ds_read_b128 a[192:195], v202 offset:26624\n\t" "ds_read_b128 a[196:199], v202 offset:28672\n\t" "v_add_f32 v81, v81, v89\n\t" "v_add_f32 v82, v82, v90\n\t" "v_add_f32 v83, v83, v91\n\t"
    "v_cvt_pk_bf16_f32 v96, v96, v97\n\t" "v_mfma_f32_16x16x32_bf16 v[128:131], a[176:179], a[176:179], v[128:131]\n\t" "ds_read_b128 a[200:203], v203 offset:26624\n\t"
    "ds_read_b128 a[204:207], v203 offset:28672\n\t" "v_cvt_pk_bf16_f32 v98, v98, v99\n\t" "v_cvt_pk_bf16_f32 v80, v80, v81\n\t" "v_cvt_pk_bf16_f32 v81, v82, v83\n\t" "v_xor_b32 v82, 16, v232\n\t"
    "v_mfma_f32_16x16x32_bf16 v[120:123], v[112:115], a[180:183], v[120:123]\n\t" "v_and_b32 v97, 0xffff0000, v96\n\t" "v_lshlrev_b32 v96, 16, v96\n\t" "v_and_b32 v99, 0xffff0000, v98\n\t"
    "v_lshlrev_b32 v98, 16, v98\n\t" "v_mfma_f32_16x16x32_bf16 v[132:135], a[180:183], a[180:183], v[132:135]\n\t" "v_add_f32 v96, 1.0, v96\n\t" "v_add_f32 v97, 1.0, v97\n\t" "v_add_f32 v98, 1.0, v98\n\t"
    "v_add_f32 v99, 1.0, v99\n\t" "v_mfma_f32_16x16x32_bf16 v[116:119], v[112:115], a[184:187], v[116:119]\n\t" "v_cvt_pk_bf16_f32 v96, v96, v97\n\t" "v_cvt_pk_bf16_f32 v97, v98, v99\n\t"
    "v_mov_b32 v101, 0\n\t" "v_mov_b32 v103, 0\n\t" "v_mfma_f32_16x16x32_bf16 v[128:131], a[184:187], a[184:187], v[128:131]\n\t" "v_mov_b32 v104, 0\n\t" "v_mov_b32 v106, 0\n\t"
    "v_and_b32 v100, 0xffff, v96\n\t" "v_and_b32 v102, 0xffff0000, v96\n\t" "s_waitcnt lgkmcnt(0)\n\t" "v_mfma_f32_16x16x32_bf16 v[120:123], v[112:115], a[188:191], v[120:123]\n\t"
    "v_and_b32 v105, 0xffff, v97\n\t" "v_and_b32 v107, 0xffff0000, v97\n\t" "v_mfma_f32_16x16x32_bf16 v[132:135], a[188:191], a[188:191], v[132:135]\n\t" "s_waitcnt vmcnt(8)\n\t"
    "v_mfma_f32_16x16x32_bf16 v[116:119], v[112:115], a[192:195], v[116:119]\n\t" "ds_read_b128 a[208:211], v202 offset:49152\n\t" "ds_read_b128 a[212:215], v202 offset:51200\n\t"
    "v_mfma_f32_16x16x32_bf16 v[128:131], a[192:195], a[192:195], v[128:131]\n\t" "ds_read_b128 a[216:219], v203 offset:49152\n\t" "ds_read_b128 a[220:223], v203 offset:51200\n\t"
    "v_mfma_f32_16x16x32_bf16 v[120:123], v[112:115], a[196:199], v[120:123]\n\t" "v_mfma_f32_16x16x32_bf16 v[132:135], a[196:199], a[196:199], v[132:135]\n\t"
    "v_mfma_f32_16x16x32_bf16 v[116:119], v[112:115], a[200:203], v[116:119]\n\t" "v_mfma_f32_16x16x32_bf16 v[128:131], a[200:203], a[200:203], v[128:131]\n\t" "s_waitcnt lgkmcnt(0)\n\t"
    "v_mfma_f32_16x16x32_bf16 v[120:123], v[112:115], a[204:207], v[120:123]\n\t" "v_mfma_f32_16x16x32_bf16 v[132:135], a[204:207], a[204:207], v[132:135]\n\t" "s_waitcnt vmcnt(4)\n\t"
    "v_mfma_f32_16x16x32_bf16 v[116:119], v[112:115], a[208:211], v[116:119]\n\t" "ds_read_b128 a[224:227], v202 offset:0\n\t" "ds_read_b128 a[228:231], v202 offset:2048\n\t"
    "v_mfma_f32_16x16x32_bf16 v[128:131], a[208:211], a[208:211], v[128:131]\n\t" "ds_read_b128 a[232:235], v203 offset:0\n\t" "ds_read_b128 a[236:239], v203 offset:2048\n\t"
    "v_mfma_f32_16x16x32_bf16 v[120:123], v[112:115], a[212:215], v[120:123]\n\t" "v_mfma_f32_16x16x32_bf16 v[132:135], a[212:215], a[212:215], v[132:135]\n\t"
    "v_mfma_f32_16x16x32_bf16 v[116:119], v[112:115], a[216:219], v[116:119]\n\t" "v_mfma_f32_16x16x32_bf16 v[128:131], a[216:219], a[216:219], v[128:131]\n\t" "s_waitcnt lgkmcnt(0)\n\t"
    "v_mfma_f32_16x16x32_bf16 v[120:123], v[112:115], a[220:223], v[120:123]\n\t" "v_mfma_f32_16x16x32_bf16 v[132:135], a[220:223], a[220:223], v[132:135]\n\t" "s_waitcnt vmcnt(0)\n\t"
    "v_mfma_f32_16x16x32_bf16 v[116:119], v[112:115], a[224:227], v[116:119]\n\t" "ds_read_b128 a[240:243], v202 offset:4096\n\t" "ds_read_b128 a[244:247], v202 offset:24576\n\t"
    "v_mfma_f32_16x16x32_bf16 v[128:131], a[224:227], a[224:227], v[128:131]\n\t" "ds_read_b128 a[248:251], v203 offset:4096\n\t" "ds_read_b128 a[252:255], v203 offset:24576\n\t"
    "v_mfma_f32_16x16x32_bf16 v[120:123], v[112:115], a[228:231], v[120:123]\n\t" "ds_write2_b32 v235, v142, v124 offset1:1\n\t"
    "v_mfma_f32_16x16x32_bf16 v[132:135], a[228:231], a[228:231], v[132:135]\n\t" "ds_write2_b64 v231, v[100:101], v[102:103] offset0:0 offset1:8\n\t"
    "v_mfma_f32_16x16x32_bf16 v[116:119], v[112:115], a[232:235], v[116:119]\n\t" "ds_write2_b64 v231, v[104:105], v[106:107] offset0:16 offset1:24\n\t"
    "v_mfma_f32_16x16x32_bf16 v[128:131], a[232:235], a[232:235], v[128:131]\n\t" "ds_write_b64 v82, v[80:81]\n\t" "s_waitcnt lgkmcnt(4)\n\t"
    "v_mfma_f32_16x16x32_bf16 v[120:123], v[112:115], a[236:239], v[120:123]\n\t" "ds_write_b64 v232, v[96:97]\n\t" "v_mfma_f32_16x16x32_bf16 v[132:135], a[236:239], a[236:239], v[132:135]\n\t"
    "s_add_u32 m0, %[sl], 120832\n\t" "s_nop 0\n\t" "global_load_lds_dwordx4 v0, %[sb] offset:-2048\n\t" "global_load_lds_dwordx4 v0, %[sb] offset:0\n\t"
    "global_load_lds_dwordx4 v0, %[sb] offset:2048\n\t" "v_add_u32 v0, 0x30000, v0\n\t" "s_waitcnt lgkmcnt(0)\n\t" "s_barrier\n\t" "ds_read_b128 v[220:223], v2 offset:0\n\t"
    "ds_read_b128 v[44:47], v18 offset:0\n\t" "v_mfma_f32_16x16x32_bf16 v[116:119], v[112:115], a[240:243], v[116:119]\n\t" "ds_read_b128 v[242:245], v18 offset:256\n\t"
    "ds_read_b128 v[224:227], v2 offset:256\n\t" "v_mfma_f32_16x16x32_bf16 v[128:131], a[240:243], a[240:243], v[128:131]\n\t" "ds_read2_b64 v[148:151], v246 offset0:0 offset1:16\n\t"
    "ds_read2_b64 v[152:155], v246 offset0:32 offset1:48\n\t" "v_mfma_f32_16x16x32_bf16 v[120:123], v[112:115], a[244:247], v[120:123]\n\t" "ds_read_b64_tr_b16 v[156:157], v8 offset:12288\n\t"
    "ds_read_b64_tr_b16 v[158:159], v8 offset:12800\n\t" "v_mfma_f32_16x16x32_bf16 v[132:135], a[244:247], a[244:247], v[132:135]\n\t" "ds_read_b64_tr_b16 v[160:161], v9 offset:12288\n\t"
    "ds_read_b64_tr_b16 v[162:163], v9 offset:12800\n\t" "v_mfma_f32_16x16x32_bf16 v[116:119], v[112:115], a[248:251], v[116:119]\n\t" "ds_read_b64_tr_b16 v[164:165], v10 offset:12288\n\t"
    "ds_read_b64_tr_b16 v[166:167], v10 offset:12800\n\t" "v_mfma_f32_16x16x32_bf16 v[128:131], a[248:251], a[248:251], v[128:131]\n\t" "ds_read_b64_tr_b16 v[168:169], v8 offset:14336\n\t"
    "ds_read_b64_tr_b16 v[170:171], v8 offset:14848\n\t" "v_mfma_f32_16x16x32_bf16 v[120:123], v[112:115], a[252:255], v[120:123]\n\t" "ds_read_b64_tr_b16 v[172:173], v9 offset:14336\n\t"
    "s_waitcnt lgkmcnt(11)\n\t" "ds_read_b64_tr_b16 v[174:175], v9 offset:14848\n\t" "v_mfma_f32_16x16x32_bf16 v[132:135], a[252:255], a[252:255], v[132:135]\n\t"
    "ds_read_b64_tr_b16 v[176:177], v10 offset:14336\n\t" "ds_read_b64_tr_b16 v[178:179], v10 offset:14848\n\t" "v_mfma_f32_4x4x4_16b_bf16 v[204:207], v[220:221], a[0:1], 0\n\t"
    "v_mfma_f32_4x4x4_16b_bf16 v[208:211], v[222:223], a[2:3], 0\n\t" "v_mfma_f32_4x4x4_16b_bf16 v[212:215], v[220:221], a[4:5], 0\n\t" "v_mfma_f32_4x4x4_16b_bf16 v[216:219], v[222:223], a[6:7], 0\n\t"
    "v_mfma_f32_4x4x4_16b_bf16 v[56:59], v[44:45], v[48:49], 0\n\t" "v_mfma_f32_4x4x4_16b_bf16 v[60:63], v[46:47], v[50:51], 0\n\t" "v_mfma_f32_4x4x4_16b_bf16 v[64:67], v[242:243], v[52:53], 0\n\t"
    "v_mfma_f32_4x4x4_16b_bf16 v[68:71], v[244:245], v[54:55], 0\n\t" "s_waitcnt lgkmcnt(12)\n\t" "v_add_f32 v148, v148, v150\n\t" "v_add_f32 v152, v152, v154\n\t" "v_add_f32 v149, v149, v151\n\t"
    "v_add_f32 v153, v153, v155\n\t" "v_add_f32 v241, v148, v152\n\t" "v_add_f32 v238, v149, v153\n\t" "v_cvt_pk_bf16_f32 v48, v56, v57\n\t" "v_cvt_pk_bf16_f32 v49, v58, v59\n\t"
    "v_cvt_pk_bf16_f32 v50, v60, v61\n\t" "v_cvt_pk_bf16_f32 v51, v62, v63\n\t" "v_cvt_pk_bf16_f32 v52, v64, v65\n\t" "v_cvt_pk_bf16_f32 v53, v66, v67\n\t" "v_cvt_pk_bf16_f32 v54, v68, v69\n\t"
    "v_cvt_pk_bf16_f32 v55, v70, v71\n\t" "s_mov_b64 exec, s[60:61]\n\t" "ds_write_b128 v19, v[48:51] offset:0\n\t" "ds_write_b128 v19, v[52:55] offset:1024\n\t" "s_mov_b64 exec, -1\n\t"
    "v_cvt_pk_bf16_f32 v188, v204, v205\n\t" "v_cvt_pk_bf16_f32 v189, v206, v207\n\t" "v_cvt_pk_bf16_f32 v190, v208, v209\n\t" "v_cvt_pk_bf16_f32 v191, v210, v211\n\t"
    "v_cvt_pk_bf16_f32 v192, v212, v213\n\t" "v_cvt_pk_bf16_f32 v193, v214, v215\n\t" "v_cvt_pk_bf16_f32 v194, v216, v217\n\t" "v_cvt_pk_bf16_f32 v195, v218, v219\n\t" "s_nop 1\n\t"
    "s_waitcnt lgkmcnt(6)\n\t" "v_mfma_f32_16x16x32_bf16 v[56:59], v[156:159], v[188:191], 0\n\t" "ds_read_b64_tr_b16 v[180:181], v7 offset:12288\n\t" "s_add_u32 m0, %[sl], 71680\n\t"
    "v_mfma_f32_16x16x32_bf16 v[60:63], v[156:159], v[192:195], 0\n\t" "ds_read_b64_tr_b16 v[182:183], v7 offset:12800\n\t" "v_mfma_f32_16x16x32_bf16 v[64:67], v[160:163], v[188:191], 0\n\t"
    "ds_read_b64_tr_b16 v[184:185], v7 offset:14336\n\t" "global_load_lds_dwordx4 v0, %[sb] offset:-2048\n\t" "v_mfma_f32_16x16x32_bf16 v[68:71], v[160:163], v[192:195], 0\n\t"
    "ds_read_b64_tr_b16 v[186:187], v7 offset:14848\n\t" "v_mfma_f32_4x4x4_16b_bf16 v[204:207], v[224:225], a[8:9], 0\n\t" "v_mfma_f32_4x4x4_16b_bf16 v[208:211], v[226:227], a[10:11], 0\n\t"
    "v_mfma_f32_4x4x4_16b_bf16 v[212:215], v[224:225], a[12:13], 0\n\t" "v_mfma_f32_4x4x4_16b_bf16 v[216:219], v[226:227], a[14:15], 0\n\t" "v_cndmask_b32 v239, 0, v128, s[46:47]\n\t"
    "v_mfma_f32_16x16x32_bf16 v[72:75], v[164:167], v[188:191], 0\n\t" "ds_read_b64_tr_b16 v[156:157], v7 offset:16384\n\t" "v_cndmask_b32 v239, v239, v129, s[48:49]\n\t"
    "v_mfma_f32_16x16x32_bf16 v[76:79], v[164:167], v[192:195], 0\n\t" "ds_read_b64_tr_b16 v[158:159], v7 offset:16896\n\t" "global_load_lds_dwordx4 v0, %[sb] offset:0\n\t"
    "v_mfma_f32_16x16x32_bf16 v[88:91], v[168:171], v[188:191], 0\n\t" "s_waitcnt lgkmcnt(6)\n\t" "s_barrier\n\t" "ds_read_b128 v[228:231], v1 offset:0\n\t"
    "ds_read_b64_tr_b16 v[160:161], v8 offset:16384\n\t" "v_cvt_pk_bf16_f32 v196, v204, v205\n\t" "v_mfma_f32_16x16x32_bf16 v[92:95], v[168:171], v[192:195], 0\n\t"
    "ds_read_b64_tr_b16 v[162:163], v8 offset:16896\n\t" "v_cvt_pk_bf16_f32 v197, v206, v207\n\t" "s_waitcnt lgkmcnt(5)\n\t" "v_mfma_f32_16x16x32_bf16 v[96:99], v[172:175], v[188:191], 0\n\t"
    "ds_read_b64_tr_b16 v[164:165], v9 offset:16384\n\t" "v_cndmask_b32 v239, v239, v130, s[50:51]\n\t" "v_mfma_f32_16x16x32_bf16 v[100:103], v[172:175], v[192:195], 0\n\t"
    "ds_read_b64_tr_b16 v[166:167], v9 offset:16896\n\t" "v_cvt_pk_bf16_f32 v198, v208, v209\n\t" "v_mfma_f32_16x16x32_bf16 v[104:107], v[176:179], v[188:191], 0\n\t"
    "ds_read_b64_tr_b16 v[168:169], v10 offset:16384\n\t" "v_cvt_pk_bf16_f32 v199, v210, v211\n\t" "v_mfma_f32_16x16x32_bf16 v[108:111], v[176:179], v[192:195], 0\n\t"
    "ds_read_b64_tr_b16 v[170:171], v10 offset:16896\n\t" "ds_read_b128 v[220:223], v2 offset:512\n\t" "v_mfma_f32_16x16x32_bf16 v[48:51], v[180:183], v[188:191], 0\n\t"
    "ds_read_b64_tr_b16 v[172:173], v7 offset:24576\n\t" "v_cvt_pk_bf16_f32 v200, v212, v213\n\t" "v_mfma_f32_16x16x32_bf16 v[52:55], v[180:183], v[192:195], 0\n\t"
    "ds_read_b64_tr_b16 v[174:175], v7 offset:25088\n\t" "v_cvt_pk_bf16_f32 v201, v214, v215\n\t" "s_waitcnt lgkmcnt(9)\n\t" "v_mfma_f32_16x16x32_bf16 v[152:155], v[228:231], v[180:183], 0\n\t"
    "global_load_lds_dwordx4 v0, %[sb] offset:2048\n\t" "s_cmp_eq_u32 %[rot], 7\n\t" "s_cselect_b32 s42, s43, 0x30000\n\t" "v_add_u32 v0, s42, v0\n\t"
    "v_mfma_f32_16x16x32_bf16 v[80:83], v[184:187], v[188:191], 0\n\t" "ds_read_b64_tr_b16 v[176:177], v8 offset:24576\n\t" "v_cvt_pk_bf16_f32 v202, v216, v217\n\t"
    "v_mfma_f32_16x16x32_bf16 v[84:87], v[184:187], v[192:195], 0\n\t" "ds_read_b64_tr_b16 v[178:179], v8 offset:25088\n\t" "v_cvt_pk_bf16_f32 v203, v218, v219\n\t"
    "v_mfma_f32_16x16x32_bf16 v[144:147], v[184:187], v[228:231], 0\n\t" "v_cndmask_b32 v239, v239, v131, s[52:53]\n\t" "v_mov_b32 v236, v116\n\t" "v_cndmask_b32 v240, 0, v132, s[46:47]\n\t"
    "v_cndmask_b32 v240, v240, v133, s[48:49]\n\t" "v_cndmask_b32 v240, v240, v134, s[50:51]\n\t" "v_cndmask_b32 v240, v240, v135, s[52:53]\n\t" "v_mov_b32 v237, v120\n\t" "s_waitcnt lgkmcnt(5)\n\t"
    "v_mfma_f32_16x16x32_bf16 v[112:115], v[188:191], v[156:159], 0\n\t" "ds_read_b64_tr_b16 v[180:181], v9 offset:24576\n\t" "s_add_u32 m0, %[sl], 83968\n\t"
    "v_mfma_f32_16x16x32_bf16 v[116:119], v[192:195], v[156:159], 0\n\t" "ds_read_b64_tr_b16 v[182:183], v9 offset:25088\n\t" "global_load_lds_dwordx4 v0, %[sb] offset:-2048\n\t"
    "v_mfma_f32_16x16x32_bf16 v[148:151], v[228:231], v[156:159], 0\n\t" "ds_read_b128 v[232:235], v1 offset:1024\n\t" "global_load_lds_dwordx4 v0, %[sb] offset:0\n\t"
    "v_mfma_f32_16x16x32_bf16 v[120:123], v[188:191], v[160:163], 0\n\t" "ds_read_b64_tr_b16 v[184:185], v10 offset:24576\n\t" "v_mfma_f32_16x16x32_bf16 v[124:127], v[192:195], v[160:163], 0\n\t"
    "ds_read_b64_tr_b16 v[186:187], v10 offset:25088\n\t" "v_mfma_f32_16x16x32_bf16 v[128:131], v[188:191], v[164:167], 0\n\t" "ds_read_b64_tr_b16 v[156:157], v7 offset:26624\n\t"
    "v_mfma_f32_16x16x32_bf16 v[132:135], v[192:195], v[164:167], 0\n\t" "ds_read_b64_tr_b16 v[158:159], v7 offset:27136\n\t" "global_load_lds_dwordx4 v0, %[sb] offset:2048\n\t"
    "v_mfma_f32_16x16x32_bf16 v[136:139], v[188:191], v[168:171], 0\n\t" "ds_read_b64_tr_b16 v[160:161], v8 offset:26624\n\t" "v_add_u32 v0, 0x30000, v0\n\t"
    "v_mfma_f32_16x16x32_bf16 v[140:143], v[192:195], v[168:171], 0\n\t" "ds_read_b64_tr_b16 v[162:163], v8 offset:27136\n\t" "s_waitcnt lgkmcnt(4)\n\t"
    "v_mfma_f32_16x16x32_bf16 v[48:51], v[172:175], v[196:199], v[48:51]\n\t" "ds_read_b64_tr_b16 v[164:165], v9 offset:26624\n\t" "s_add_u32 m0, %[sl], 22528\n\t"
    "v_mfma_f32_16x16x32_bf16 v[52:55], v[172:175], v[200:203], v[52:55]\n\t" "ds_read_b64_tr_b16 v[166:167], v9 offset:27136\n\t"
    "v_mfma_f32_16x16x32_bf16 v[152:155], v[232:235], v[172:175], v[152:155]\n\t" "global_load_lds_dwordx4 v0, %[sb] offset:-2048\n\t"
    "v_mfma_f32_16x16x32_bf16 v[56:59], v[176:179], v[196:199], v[56:59]\n\t" "ds_read_b64_tr_b16 v[168:169], v10 offset:26624\n\t" "v_mfma_f32_4x4x4_16b_bf16 v[204:207], v[220:221], a[16:17], 0\n\t"
    "v_mfma_f32_4x4x4_16b_bf16 v[208:211], v[222:223], a[18:19], 0\n\t" "v_mfma_f32_4x4x4_16b_bf16 v[212:215], v[220:221], a[20:21], 0\n\t"
    "v_mfma_f32_4x4x4_16b_bf16 v[216:219], v[222:223], a[22:23], 0\n\t" "v_mfma_f32_16x16x32_bf16 v[60:63], v[176:179], v[200:203], v[60:63]\n\t" "ds_read_b64_tr_b16 v[170:171], v10 offset:27136\n\t"
    "v_mfma_f32_16x16x32_bf16 v[64:67], v[180:183], v[196:199], v[64:67]\n\t" "ds_read_b64_tr_b16 v[172:173], v7 offset:28672\n\t" "global_load_lds_dwordx4 v0, %[sb] offset:0\n\t"
    "v_mfma_f32_16x16x32_bf16 v[68:71], v[180:183], v[200:203], v[68:71]\n\t" "ds_read_b64_tr_b16 v[174:175], v7 offset:29184\n\t" "v_cvt_pk_bf16_f32 v188, v204, v205\n\t"
    "v_mfma_f32_16x16x32_bf16 v[72:75], v[184:187], v[196:199], v[72:75]\n\t" "ds_read_b64_tr_b16 v[176:177], v8 offset:28672\n\t" "v_cvt_pk_bf16_f32 v189, v206, v207\n\t"
    "v_mfma_f32_16x16x32_bf16 v[76:79], v[184:187], v[200:203], v[76:79]\n\t" "ds_read_b64_tr_b16 v[178:179], v8 offset:29184\n\t" "s_waitcnt lgkmcnt(4)\n\t"
    "v_mfma_f32_16x16x32_bf16 v[80:83], v[156:159], v[196:199], v[80:83]\n\t" "ds_read_b64_tr_b16 v[180:181], v9 offset:28672\n\t" "v_cvt_pk_bf16_f32 v190, v208, v209\n\t"
    "v_mfma_f32_16x16x32_bf16 v[84:87], v[156:159], v[200:203], v[84:87]\n\t" "ds_read_b64_tr_b16 v[182:183], v9 offset:29184\n\t" "v_cvt_pk_bf16_f32 v191, v210, v211\n\t"
    "v_mfma_f32_16x16x32_bf16 v[144:147], v[156:159], v[232:235], v[144:147]\n\t" "ds_read_b128 v[224:227], v2 offset:768\n\t" "global_load_lds_dwordx4 v0, %[sb] offset:2048\n\t"
    "v_mfma_f32_16x16x32_bf16 v[88:91], v[160:163], v[196:199], v[88:91]\n\t" "ds_read_b64_tr_b16 v[184:185], v10 offset:28672\n\t" "v_cvt_pk_bf16_f32 v192, v212, v213\n\t"
    "v_mfma_f32_16x16x32_bf16 v[92:95], v[160:163], v[200:203], v[92:95]\n\t" "ds_read_b64_tr_b16 v[186:187], v10 offset:29184\n\t" "v_cvt_pk_bf16_f32 v193, v214, v215\n\t"
    "v_mfma_f32_16x16x32_bf16 v[96:99], v[164:167], v[196:199], v[96:99]\n\t" "s_waitcnt vmcnt(9)\n\t" "s_barrier\n\t" "ds_read_b64_tr_b16 v[156:157], v7 offset:36864\n\t"
    "v_mfma_f32_16x16x32_bf16 v[100:103], v[164:167], v[200:203], v[100:103]\n\t" "ds_read_b64_tr_b16 v[158:159], v7 offset:37376\n\t" "v_cvt_pk_bf16_f32 v194, v216, v217\n\t"
    "v_mfma_f32_16x16x32_bf16 v[104:107], v[168:171], v[196:199], v[104:107]\n\t" "ds_read_b64_tr_b16 v[160:161], v8 offset:36864\n\t" "v_cvt_pk_bf16_f32 v195, v218, v219\n\t"
    "v_mfma_f32_16x16x32_bf16 v[108:111], v[168:171], v[200:203], v[108:111]\n\t" "ds_read_b64_tr_b16 v[162:163], v8 offset:37376\n\t" "s_waitcnt lgkmcnt(4)\n\t"
    "v_mfma_f32_16x16x32_bf16 v[112:115], v[196:199], v[172:175], v[112:115]\n\t" "ds_read_b64_tr_b16 v[164:165], v9 offset:36864\n\t" "v_add_u32 v0, 0x30000, v0\n\t"
    "v_mfma_f32_16x16x32_bf16 v[116:119], v[200:203], v[172:175], v[116:119]\n\t" "ds_read_b64_tr_b16 v[166:167], v9 offset:37376\n\t" "s_add_u32 m0, %[sl], 34816\n\t"
    "v_mfma_f32_16x16x32_bf16 v[148:151], v[232:235], v[172:175], v[148:151]\n\t" "ds_read_b128 v[228:231], v1 offset:2048\n\t" "global_load_lds_dwordx4 v0, %[sb] offset:-2048\n\t"
    "v_mfma_f32_16x16x32_bf16 v[120:123], v[196:199], v[176:179], v[120:123]\n\t" "ds_read_b64_tr_b16 v[168:169], v10 offset:36864\n\t" "global_load_lds_dwordx4 v0, %[sb] offset:0\n\t"
    "v_mfma_f32_16x16x32_bf16 v[124:127], v[200:203], v[176:179], v[124:127]\n\t" "ds_read_b64_tr_b16 v[170:171], v10 offset:37376\n\t"
    "v_mfma_f32_16x16x32_bf16 v[128:131], v[196:199], v[180:183], v[128:131]\n\t" "ds_read_b64_tr_b16 v[172:173], v7 offset:38912\n\t"
    "v_mfma_f32_16x16x32_bf16 v[132:135], v[200:203], v[180:183], v[132:135]\n\t" "ds_read_b64_tr_b16 v[174:175], v7 offset:39424\n\t" "global_load_lds_dwordx4 v0, %[sb] offset:2048\n\t"
    "v_mfma_f32_16x16x32_bf16 v[136:139], v[196:199], v[184:187], v[136:139]\n\t" "ds_read_b64_tr_b16 v[176:177], v8 offset:38912\n\t" "v_add_u32 v0, 0x30000, v0\n\t"
    "v_mfma_f32_16x16x32_bf16 v[140:143], v[200:203], v[184:187], v[140:143]\n\t" "ds_read_b64_tr_b16 v[178:179], v8 offset:39424\n\t" "s_waitcnt lgkmcnt(4)\n\t"
    "v_mfma_f32_16x16x32_bf16 v[48:51], v[156:159], v[188:191], v[48:51]\n\t" "ds_read_b64_tr_b16 v[180:181], v9 offset:38912\n\t" "s_add_u32 m0, %[sl], 47104\n\t"
    "v_mfma_f32_16x16x32_bf16 v[52:55], v[156:159], v[192:195], v[52:55]\n\t" "ds_read_b64_tr_b16 v[182:183], v9 offset:39424\n\t"
    "v_mfma_f32_16x16x32_bf16 v[152:155], v[228:231], v[156:159], v[152:155]\n\t" "global_load_lds_dwordx4 v0, %[sb] offset:-2048\n\t"
    "v_mfma_f32_16x16x32_bf16 v[56:59], v[160:163], v[188:191], v[56:59]\n\t" "ds_read_b64_tr_b16 v[184:185], v10 offset:38912\n\t" "v_mfma_f32_4x4x4_16b_bf16 v[204:207], v[224:225], a[24:25], 0\n\t"
    "v_mfma_f32_4x4x4_16b_bf16 v[208:211], v[226:227], a[26:27], 0\n\t" "v_mfma_f32_4x4x4_16b_bf16 v[212:215], v[224:225], a[28:29], 0\n\t"
    "v_mfma_f32_4x4x4_16b_bf16 v[216:219], v[226:227], a[30:31], 0\n\t" "v_mfma_f32_16x16x32_bf16 v[60:63], v[160:163], v[192:195], v[60:63]\n\t" "ds_read_b64_tr_b16 v[186:187], v10 offset:39424\n\t"
    "v_mfma_f32_16x16x32_bf16 v[64:67], v[164:167], v[188:191], v[64:67]\n\t" "ds_read_b64_tr_b16 v[156:157], v7 offset:40960\n\t" "global_load_lds_dwordx4 v0, %[sb] offset:0\n\t"
    "v_mfma_f32_16x16x32_bf16 v[68:71], v[164:167], v[192:195], v[68:71]\n\t" "ds_read_b64_tr_b16 v[158:159], v7 offset:41472\n\t" "v_cvt_pk_bf16_f32 v196, v204, v205\n\t"
    "v_mfma_f32_16x16x32_bf16 v[72:75], v[168:171], v[188:191], v[72:75]\n\t" "ds_read_b64_tr_b16 v[160:161], v8 offset:40960\n\t" "v_cvt_pk_bf16_f32 v197, v206, v207\n\t"
    "v_mfma_f32_16x16x32_bf16 v[76:79], v[168:171], v[192:195], v[76:79]\n\t" "ds_read_b64_tr_b16 v[162:163], v8 offset:41472\n\t" "s_waitcnt lgkmcnt(4)\n\t"
    "v_mfma_f32_16x16x32_bf16 v[80:83], v[172:175], v[188:191], v[80:83]\n\t" "ds_read_b64_tr_b16 v[164:165], v9 offset:40960\n\t" "v_cvt_pk_bf16_f32 v198, v208, v209\n\t"
    "v_mfma_f32_16x16x32_bf16 v[84:87], v[172:175], v[192:195], v[84:87]\n\t" "ds_read_b64_tr_b16 v[166:167], v9 offset:41472\n\t" "v_cvt_pk_bf16_f32 v199, v210, v211\n\t"
    "v_mfma_f32_16x16x32_bf16 v[144:147], v[172:175], v[228:231], v[144:147]\n\t" "ds_read_b128 v[220:223], v2 offset:1024\n\t" "global_load_lds_dwordx4 v0, %[sb] offset:2048\n\t"
    "v_mfma_f32_16x16x32_bf16 v[88:91], v[176:179], v[188:191], v[88:91]\n\t" "ds_read_b64_tr_b16 v[168:169], v10 offset:40960\n\t" "v_cvt_pk_bf16_f32 v200, v212, v213\n\t"
    "v_mfma_f32_16x16x32_bf16 v[92:95], v[176:179], v[192:195], v[92:95]\n\t" "ds_read_b64_tr_b16 v[170:171], v10 offset:41472\n\t" "v_cvt_pk_bf16_f32 v201, v214, v215\n\t"
    "v_mfma_f32_16x16x32_bf16 v[96:99], v[180:183], v[188:191], v[96:99]\n\t" "s_waitcnt vmcnt(12)\n\t" "s_barrier\n\t" "ds_read_b64_tr_b16 v[172:173], v3 offset:49152\n\t"
    "v_mfma_f32_16x16x32_bf16 v[100:103], v[180:183], v[192:195], v[100:103]\n\t" "ds_read_b64_tr_b16 v[174:175], v3 offset:49664\n\t" "v_cvt_pk_bf16_f32 v202, v216, v217\n\t"
    "v_mfma_f32_16x16x32_bf16 v[104:107], v[184:187], v[188:191], v[104:107]\n\t" "ds_read_b64_tr_b16 v[176:177], v4 offset:49152\n\t" "v_cvt_pk_bf16_f32 v203, v218, v219\n\t"
    "v_mfma_f32_16x16x32_bf16 v[108:111], v[184:187], v[192:195], v[108:111]\n\t" "ds_read_b64_tr_b16 v[178:179], v4 offset:49664\n\t" "s_waitcnt lgkmcnt(4)\n\t"
    "v_mfma_f32_16x16x32_bf16 v[112:115], v[188:191], v[156:159], v[112:115]\n\t" "ds_read_b64_tr_b16 v[180:181], v5 offset:49152\n\t" "s_cmp_eq_u32 %[rot], 6\n\t" "s_cselect_b32 s42, s43, 0x30000\n\t"
    "v_add_u32 v0, s42, v0\n\t" "v_mfma_f32_16x16x32_bf16 v[116:119], v[192:195], v[156:159], v[116:119]\n\t" "ds_read_b64_tr_b16 v[182:183], v5 offset:49664\n\t" "s_add_u32 m0, %[sl], 59392\n\t"
    "v_mfma_f32_16x16x32_bf16 v[148:151], v[228:231], v[156:159], v[148:151]\n\t" "ds_read_b128 v[232:235], v1 offset:3072\n\t" "global_load_lds_dwordx4 v0, %[sb] offset:-2048\n\t"
    "v_mfma_f32_16x16x32_bf16 v[120:123], v[188:191], v[160:163], v[120:123]\n\t" "ds_read_b64_tr_b16 v[184:185], v6 offset:49152\n\t" "global_load_lds_dwordx4 v0, %[sb] offset:0\n\t"
    "v_mfma_f32_16x16x32_bf16 v[124:127], v[192:195], v[160:163], v[124:127]\n\t" "ds_read_b64_tr_b16 v[186:187], v6 offset:49664\n\t"
    "v_mfma_f32_16x16x32_bf16 v[128:131], v[188:191], v[164:167], v[128:131]\n\t" "ds_read_b64_tr_b16 v[156:157], v3 offset:51200\n\t"
    "v_mfma_f32_16x16x32_bf16 v[132:135], v[192:195], v[164:167], v[132:135]\n\t" "ds_read_b64_tr_b16 v[158:159], v3 offset:51712\n\t" "global_load_lds_dwordx4 v0, %[sb] offset:2048\n\t"
    "v_mfma_f32_16x16x32_bf16 v[136:139], v[188:191], v[168:171], v[136:139]\n\t" "ds_read_b64_tr_b16 v[160:161], v4 offset:51200\n\t" "v_add_u32 v0, 0x30000, v0\n\t"
    "v_mfma_f32_16x16x32_bf16 v[140:143], v[192:195], v[168:171], v[140:143]\n\t" "ds_read_b64_tr_b16 v[162:163], v4 offset:51712\n\t" "s_waitcnt lgkmcnt(4)\n\t"
    "v_mfma_f32_16x16x32_bf16 v[48:51], v[172:175], v[196:199], v[48:51]\n\t" "ds_read_b64_tr_b16 v[164:165], v5 offset:51200\n\t" "s_add_u32 m0, %[sl], 96256\n\t"
    "v_mfma_f32_16x16x32_bf16 v[52:55], v[172:175], v[200:203], v[52:55]\n\t" "ds_read_b64_tr_b16 v[166:167], v5 offset:51712\n\t" "ds_read_b128 v[44:47], v18 offset:2048\n\t"
    "v_mfma_f32_16x16x32_bf16 v[152:155], v[232:235], v[172:175], v[152:155]\n\t" "global_load_lds_dwordx4 v0, %[sb] offset:-2048\n\t" "ds_read_b128 v[242:245], v18 offset:2304\n\t"
    "v_mfma_f32_16x16x32_bf16 v[56:59], v[176:179], v[196:199], v[56:59]\n\t" "ds_read_b64_tr_b16 v[168:169], v6 offset:51200\n\t" "v_mfma_f32_4x4x4_16b_bf16 v[204:207], v[220:221], a[32:33], 0\n\t"
    "v_mfma_f32_4x4x4_16b_bf16 v[208:211], v[222:223], a[34:35], 0\n\t" "v_mfma_f32_4x4x4_16b_bf16 v[212:215], v[220:221], a[36:37], 0\n\t"
    "v_mfma_f32_4x4x4_16b_bf16 v[216:219], v[222:223], a[38:39], 0\n\t" "v_mfma_f32_16x16x32_bf16 v[60:63], v[176:179], v[200:203], v[60:63]\n\t" "ds_read_b64_tr_b16 v[170:171], v6 offset:51712\n\t"
    "v_mfma_f32_16x16x32_bf16 v[64:67], v[180:183], v[196:199], v[64:67]\n\t" "ds_read_b64_tr_b16 v[172:173], v3 offset:53248\n\t" "v_mfma_f32_16x16x32_bf16 v[68:71], v[180:183], v[200:203], v[68:71]\n\t"
    "ds_read_b64_tr_b16 v[174:175], v3 offset:53760\n\t" "v_cvt_pk_bf16_f32 v188, v204, v205\n\t" "v_mfma_f32_16x16x32_bf16 v[72:75], v[184:187], v[196:199], v[72:75]\n\t"
    "ds_read_b64_tr_b16 v[176:177], v4 offset:53248\n\t" "v_cvt_pk_bf16_f32 v189, v206, v207\n\t" "v_mfma_f32_16x16x32_bf16 v[76:79], v[184:187], v[200:203], v[76:79]\n\t"
    "ds_read_b64_tr_b16 v[178:179], v4 offset:53760\n\t" "s_waitcnt lgkmcnt(4)\n\t" "v_mfma_f32_16x16x32_bf16 v[80:83], v[156:159], v[196:199], v[80:83]\n\t"
    "ds_read_b64_tr_b16 v[180:181], v5 offset:53248\n\t" "v_cvt_pk_bf16_f32 v190, v208, v209\n\t" "v_mfma_f32_16x16x32_bf16 v[84:87], v[156:159], v[200:203], v[84:87]\n\t"
    "ds_read_b64_tr_b16 v[182:183], v5 offset:53760\n\t" "v_cvt_pk_bf16_f32 v191, v210, v211\n\t" "v_mfma_f32_16x16x32_bf16 v[144:147], v[156:159], v[232:235], v[144:147]\n\t"
    "ds_read_b128 v[224:227], v2 offset:1280\n\t" "global_load_lds_dwordx4 v0, %[sb] offset:0\n\t" "v_mfma_f32_16x16x32_bf16 v[88:91], v[160:163], v[196:199], v[88:91]\n\t"
    "ds_read_b64_tr_b16 v[184:185], v6 offset:53248\n\t" "v_cvt_pk_bf16_f32 v192, v212, v213\n\t" "v_mfma_f32_16x16x32_bf16 v[92:95], v[160:163], v[200:203], v[92:95]\n\t"
    "ds_read_b64_tr_b16 v[186:187], v6 offset:53760\n\t" "v_cvt_pk_bf16_f32 v193, v214, v215\n\t" "v_mfma_f32_16x16x32_bf16 v[96:99], v[164:167], v[196:199], v[96:99]\n\t" "s_waitcnt vmcnt(11)\n\t"
    "s_barrier\n\t" "ds_read_b64_tr_b16 v[156:157], v7 offset:0\n\t" "v_mfma_f32_16x16x32_bf16 v[100:103], v[164:167], v[200:203], v[100:103]\n\t" "ds_read_b64_tr_b16 v[158:159], v7 offset:512\n\t"
    "v_cvt_pk_bf16_f32 v194, v216, v217\n\t" "v_mfma_f32_16x16x32_bf16 v[104:107], v[168:171], v[196:199], v[104:107]\n\t" "ds_read_b64_tr_b16 v[160:161], v8 offset:0\n\t"
    "v_cvt_pk_bf16_f32 v195, v218, v219\n\t" "v_mfma_f32_16x16x32_bf16 v[108:111], v[168:171], v[200:203], v[108:111]\n\t" "ds_read_b64_tr_b16 v[162:163], v8 offset:512\n\t"
    "v_mfma_f32_4x4x4_16b_bf16 v[204:207], v[44:45], v[20:21], 0\n\t" "v_mfma_f32_4x4x4_16b_bf16 v[208:211], v[46:47], v[22:23], 0\n\t" "v_mfma_f32_4x4x4_16b_bf16 v[212:215], v[242:243], v[24:25], 0\n\t"
    "v_mfma_f32_4x4x4_16b_bf16 v[216:219], v[244:245], v[26:27], 0\n\t" "s_waitcnt lgkmcnt(4)\n\t" "v_mfma_f32_16x16x32_bf16 v[112:115], v[196:199], v[172:175], v[112:115]\n\t"
    "ds_read_b64_tr_b16 v[164:165], v9 offset:0\n\t" "v_mfma_f32_16x16x32_bf16 v[116:119], v[200:203], v[172:175], v[116:119]\n\t" "ds_read_b64_tr_b16 v[166:167], v9 offset:512\n\t"
    "v_mfma_f32_16x16x32_bf16 v[148:151], v[232:235], v[172:175], v[148:151]\n\t" "ds_read_b128 v[228:231], v1 offset:4096\n\t" "global_load_lds_dwordx4 v0, %[sb] offset:2048\n\t"
    "v_mfma_f32_16x16x32_bf16 v[120:123], v[196:199], v[176:179], v[120:123]\n\t" "ds_read_b64_tr_b16 v[168:169], v10 offset:0\n\t" "v_add_u32 v0, 0x30000, v0\n\t"
    "v_mfma_f32_16x16x32_bf16 v[124:127], v[200:203], v[176:179], v[124:127]\n\t" "ds_read_b64_tr_b16 v[170:171], v10 offset:512\n\t" "v_cvt_pk_bf16_f32 v20, v204, v205\n\t"
    "v_mfma_f32_16x16x32_bf16 v[128:131], v[196:199], v[180:183], v[128:131]\n\t" "ds_read_b64_tr_b16 v[172:173], v7 offset:2048\n\t" "v_cvt_pk_bf16_f32 v21, v206, v207\n\t"
    "v_mfma_f32_16x16x32_bf16 v[132:135], v[200:203], v[180:183], v[132:135]\n\t" "ds_read_b64_tr_b16 v[174:175], v7 offset:2560\n\t" "v_cvt_pk_bf16_f32 v22, v208, v209\n\t"
    "v_mfma_f32_16x16x32_bf16 v[136:139], v[196:199], v[184:187], v[136:139]\n\t" "ds_read_b64_tr_b16 v[176:177], v8 offset:2048\n\t" "v_cvt_pk_bf16_f32 v23, v210, v211\n\t"
    "v_mfma_f32_16x16x32_bf16 v[140:143], v[200:203], v[184:187], v[140:143]\n\t" "ds_read_b64_tr_b16 v[178:179], v8 offset:2560\n\t" "s_waitcnt lgkmcnt(4)\n\t"
    "v_mfma_f32_16x16x32_bf16 v[48:51], v[156:159], v[188:191], v[48:51]\n\t" "ds_read_b64_tr_b16 v[180:181], v9 offset:2048\n\t" "s_add_u32 m0, %[sl], 108544\n\t"
    "v_mfma_f32_16x16x32_bf16 v[52:55], v[156:159], v[192:195], v[52:55]\n\t" "ds_read_b64_tr_b16 v[182:183], v9 offset:2560\n\t" "v_cvt_pk_bf16_f32 v24, v212, v213\n\t"
    "v_mfma_f32_16x16x32_bf16 v[152:155], v[228:231], v[156:159], v[152:155]\n\t" "v_cvt_pk_bf16_f32 v25, v214, v215\n\t" "v_cvt_pk_bf16_f32 v26, v216, v217\n\t" "v_cvt_pk_bf16_f32 v27, v218, v219\n\t"
    "s_mov_b64 exec, s[60:61]\n\t" "ds_write_b128 v19, v[20:23] offset:8192\n\t" "ds_write_b128 v19, v[24:27] offset:9216\n\t" "s_mov_b64 exec, -1\n\t"
    "v_mfma_f32_16x16x32_bf16 v[56:59], v[160:163], v[188:191], v[56:59]\n\t" "ds_read_b64_tr_b16 v[184:185], v10 offset:2048\n\t" "global_load_lds_dwordx4 v0, %[sb] offset:-2048\n\t"
    "v_mfma_f32_4x4x4_16b_bf16 v[204:207], v[224:225], a[40:41], 0\n\t" "v_mfma_f32_4x4x4_16b_bf16 v[208:211], v[226:227], a[42:43], 0\n\t"
    "v_mfma_f32_4x4x4_16b_bf16 v[212:215], v[224:225], a[44:45], 0\n\t" "v_mfma_f32_4x4x4_16b_bf16 v[216:219], v[226:227], a[46:47], 0\n\t"
    "v_mfma_f32_16x16x32_bf16 v[60:63], v[160:163], v[192:195], v[60:63]\n\t" "ds_read_b64_tr_b16 v[186:187], v10 offset:2560\n\t" "v_mfma_f32_16x16x32_bf16 v[64:67], v[164:167], v[188:191], v[64:67]\n\t"
    "ds_read_b64_tr_b16 v[156:157], v7 offset:4096\n\t" "v_mfma_f32_16x16x32_bf16 v[68:71], v[164:167], v[192:195], v[68:71]\n\t" "ds_read_b64_tr_b16 v[158:159], v7 offset:4608\n\t"
    "v_cvt_pk_bf16_f32 v196, v204, v205\n\t" "v_mfma_f32_16x16x32_bf16 v[72:75], v[168:171], v[188:191], v[72:75]\n\t" "ds_read_b64_tr_b16 v[160:161], v8 offset:4096\n\t"
    "v_cvt_pk_bf16_f32 v197, v206, v207\n\t" "v_mfma_f32_16x16x32_bf16 v[76:79], v[168:171], v[192:195], v[76:79]\n\t" "ds_read_b64_tr_b16 v[162:163], v8 offset:4608\n\t" "s_waitcnt lgkmcnt(4)\n\t"
    "v_mfma_f32_16x16x32_bf16 v[80:83], v[172:175], v[188:191], v[80:83]\n\t" "ds_read_b64_tr_b16 v[164:165], v9 offset:4096\n\t" "v_cvt_pk_bf16_f32 v198, v208, v209\n\t"
    "v_mfma_f32_16x16x32_bf16 v[84:87], v[172:175], v[192:195], v[84:87]\n\t" "ds_read_b64_tr_b16 v[166:167], v9 offset:4608\n\t" "v_cvt_pk_bf16_f32 v199, v210, v211\n\t"
    "v_mfma_f32_16x16x32_bf16 v[144:147], v[172:175], v[228:231], v[144:147]\n\t" "ds_read_b128 v[220:223], v2 offset:1536\n\t" "global_load_lds_dwordx4 v0, %[sb] offset:0\n\t"
    "v_mfma_f32_16x16x32_bf16 v[88:91], v[176:179], v[188:191], v[88:91]\n\t" "ds_read_b64_tr_b16 v[168:169], v10 offset:4096\n\t" "v_cvt_pk_bf16_f32 v200, v212, v213\n\t"
    "v_mfma_f32_16x16x32_bf16 v[92:95], v[176:179], v[192:195], v[92:95]\n\t" "ds_read_b64_tr_b16 v[170:171], v10 offset:4608\n\t" "v_cvt_pk_bf16_f32 v201, v214, v215\n\t"
    "v_mfma_f32_16x16x32_bf16 v[96:99], v[180:183], v[188:191], v[96:99]\n\t" "ds_read_b64_tr_b16 v[172:173], v3 offset:0\n\t" "v_mfma_f32_16x16x32_bf16 v[100:103], v[180:183], v[192:195], v[100:103]\n\t"
    "ds_read_b64_tr_b16 v[174:175], v3 offset:512\n\t" "v_cvt_pk_bf16_f32 v202, v216, v217\n\t" "v_mfma_f32_16x16x32_bf16 v[104:107], v[184:187], v[188:191], v[104:107]\n\t"
    "ds_read_b64_tr_b16 v[176:177], v4 offset:0\n\t" "v_cvt_pk_bf16_f32 v203, v218, v219\n\t" "v_mfma_f32_16x16x32_bf16 v[108:111], v[184:187], v[192:195], v[108:111]\n\t"
    "ds_read_b64_tr_b16 v[178:179], v4 offset:512\n\t" "s_waitcnt lgkmcnt(4)\n\t" "v_mfma_f32_16x16x32_bf16 v[112:115], v[188:191], v[156:159], v[112:115]\n\t"
    "ds_read_b64_tr_b16 v[180:181], v5 offset:0\n\t" "v_mfma_f32_16x16x32_bf16 v[116:119], v[192:195], v[156:159], v[116:119]\n\t" "ds_read_b64_tr_b16 v[182:183], v5 offset:512\n\t"
    "v_mfma_f32_16x16x32_bf16 v[148:151], v[228:231], v[156:159], v[148:151]\n\t" "ds_read_b128 v[232:235], v1 offset:5120\n\t" "global_load_lds_dwordx4 v0, %[sb] offset:2048\n\t"
    "v_mfma_f32_16x16x32_bf16 v[120:123], v[188:191], v[160:163], v[120:123]\n\t" "ds_read_b64_tr_b16 v[184:185], v6 offset:0\n\t" "v_add_u32 v0, 0x30000, v0\n\t"
    "v_mfma_f32_16x16x32_bf16 v[124:127], v[192:195], v[160:163], v[124:127]\n\t" "ds_read_b64_tr_b16 v[186:187], v6 offset:512\n\t"
    "v_mfma_f32_16x16x32_bf16 v[128:131], v[188:191], v[164:167], v[128:131]\n\t" "ds_read_b64_tr_b16 v[156:157], v3 offset:2048\n\t"
    "v_mfma_f32_16x16x32_bf16 v[132:135], v[192:195], v[164:167], v[132:135]\n\t" "ds_read_b64_tr_b16 v[158:159], v3 offset:2560\n\t"
    "v_mfma_f32_16x16x32_bf16 v[136:139], v[188:191], v[168:171], v[136:139]\n\t" "ds_read_b64_tr_b16 v[160:161], v4 offset:2048\n\t"
    "v_mfma_f32_16x16x32_bf16 v[140:143], v[192:195], v[168:171], v[140:143]\n\t" "ds_read_b64_tr_b16 v[162:163], v4 offset:2560\n\t" "s_waitcnt lgkmcnt(4)\n\t"
    "v_mfma_f32_16x16x32_bf16 v[48:51], v[172:175], v[196:199], v[48:51]\n\t" "ds_read_b64_tr_b16 v[164:165], v5 offset:2048\n\t" "s_add_u32 m0, %[sl], 120832\n\t"
    "v_mfma_f32_16x16x32_bf16 v[52:55], v[172:175], v[200:203], v[52:55]\n\t" "ds_read_b64_tr_b16 v[166:167], v5 offset:2560\n\t" "ds_read_b128 v[44:47], v18 offset:4096\n\t"
    "v_mfma_f32_16x16x32_bf16 v[152:155], v[232:235], v[172:175], v[152:155]\n\t" "global_load_lds_dwordx4 v0, %[sb] offset:-2048\n\t" "ds_read_b128 v[242:245], v18 offset:4352\n\t"
    "v_mfma_f32_16x16x32_bf16 v[56:59], v[176:179], v[196:199], v[56:59]\n\t" "ds_read_b64_tr_b16 v[168:169], v6 offset:2048\n\t" "v_mov_b32 v20, v239\n\t"
    "v_mfma_f32_4x4x4_16b_bf16 v[204:207], v[220:221], a[48:49], 0\n\t" "v_mfma_f32_4x4x4_16b_bf16 v[208:211], v[222:223], a[50:51], 0\n\t"
    "v_mfma_f32_4x4x4_16b_bf16 v[212:215], v[220:221], a[52:53], 0\n\t" "v_mfma_f32_4x4x4_16b_bf16 v[216:219], v[222:223], a[54:55], 0\n\t" "v_mov_b32 v21, v240\n\t"
    "v_mfma_f32_16x16x32_bf16 v[60:63], v[176:179], v[200:203], v[60:63]\n\t" "ds_read_b64_tr_b16 v[170:171], v6 offset:2560\n\t" "v_mfma_f32_16x16x32_bf16 v[64:67], v[180:183], v[196:199], v[64:67]\n\t"
    "ds_read_b64_tr_b16 v[172:173], v3 offset:4096\n\t" "v_mfma_f32_16x16x32_bf16 v[68:71], v[180:183], v[200:203], v[68:71]\n\t" "ds_read_b64_tr_b16 v[174:175], v3 offset:4608\n\t"
    "v_cvt_pk_bf16_f32 v188, v204, v205\n\t" "v_mfma_f32_16x16x32_bf16 v[72:75], v[184:187], v[196:199], v[72:75]\n\t" "ds_read_b64_tr_b16 v[176:177], v4 offset:4096\n\t"
    "v_cvt_pk_bf16_f32 v189, v206, v207\n\t" "v_mfma_f32_16x16x32_bf16 v[76:79], v[184:187], v[200:203], v[76:79]\n\t" "ds_read_b64_tr_b16 v[178:179], v4 offset:4608\n\t" "s_waitcnt lgkmcnt(4)\n\t"
    "v_mfma_f32_16x16x32_bf16 v[80:83], v[156:159], v[196:199], v[80:83]\n\t" "ds_read_b64_tr_b16 v[180:181], v5 offset:4096\n\t" "v_cvt_pk_bf16_f32 v190, v208, v209\n\t"
    "v_mfma_f32_16x16x32_bf16 v[84:87], v[156:159], v[200:203], v[84:87]\n\t" "ds_read_b64_tr_b16 v[182:183], v5 offset:4608\n\t" "v_cvt_pk_bf16_f32 v191, v210, v211\n\t"
    "v_mfma_f32_16x16x32_bf16 v[144:147], v[156:159], v[232:235], v[144:147]\n\t" "ds_read_b128 v[224:227], v2 offset:1792\n\t" "global_load_lds_dwordx4 v0, %[sb] offset:0\n\t"
    "v_mfma_f32_16x16x32_bf16 v[88:91], v[160:163], v[196:199], v[88:91]\n\t" "ds_read_b64_tr_b16 v[184:185], v6 offset:4096\n\t" "v_cvt_pk_bf16_f32 v192, v212, v213\n\t"
    "v_mfma_f32_16x16x32_bf16 v[92:95], v[160:163], v[200:203], v[92:95]\n\t" "ds_read_b64_tr_b16 v[186:187], v6 offset:4608\n\t" "v_cvt_pk_bf16_f32 v193, v214, v215\n\t"
    "v_mfma_f32_16x16x32_bf16 v[96:99], v[164:167], v[196:199], v[96:99]\n\t" "s_waitcnt vmcnt(11)\n\t" "s_barrier\n\t" "ds_read_b64_tr_b16 v[156:157], v3 offset:12288\n\t"
    "v_mfma_f32_16x16x32_bf16 v[100:103], v[164:167], v[200:203], v[100:103]\n\t" "ds_read_b64_tr_b16 v[158:159], v3 offset:12800\n\t" "v_cvt_pk_bf16_f32 v194, v216, v217\n\t"
    "v_mfma_f32_16x16x32_bf16 v[104:107], v[168:171], v[196:199], v[104:107]\n\t" "ds_read_b64_tr_b16 v[160:161], v4 offset:12288\n\t" "v_cvt_pk_bf16_f32 v195, v218, v219\n\t"
    "v_mfma_f32_16x16x32_bf16 v[108:111], v[168:171], v[200:203], v[108:111]\n\t" "ds_read_b64_tr_b16 v[162:163], v4 offset:12800\n\t" "v_mfma_f32_4x4x4_16b_bf16 v[204:207], v[44:45], v[28:29], 0\n\t"
    "v_mfma_f32_4x4x4_16b_bf16 v[208:211], v[46:47], v[30:31], 0\n\t" "v_mfma_f32_4x4x4_16b_bf16 v[212:215], v[242:243], v[32:33], 0\n\t"
    "v_mfma_f32_4x4x4_16b_bf16 v[216:219], v[244:245], v[34:35], 0\n\t" "s_waitcnt lgkmcnt(4)\n\t" "v_mfma_f32_16x16x32_bf16 v[112:115], v[196:199], v[172:175], v[112:115]\n\t"
    "ds_read_b64_tr_b16 v[164:165], v5 offset:12288\n\t" "v_mfma_f32_16x16x32_bf16 v[116:119], v[200:203], v[172:175], v[116:119]\n\t" "ds_read_b64_tr_b16 v[166:167], v5 offset:12800\n\t"
    "v_mfma_f32_16x16x32_bf16 v[148:151], v[232:235], v[172:175], v[148:151]\n\t" "ds_read_b128 v[228:231], v1 offset:6144\n\t" "global_load_lds_dwordx4 v0, %[sb] offset:2048\n\t"
    "v_mfma_f32_16x16x32_bf16 v[120:123], v[196:199], v[176:179], v[120:123]\n\t" "ds_read_b64_tr_b16 v[168:169], v6 offset:12288\n\t" "s_cmp_eq_u32 %[rot], 5\n\t" "s_cselect_b32 s42, s43, 0x30000\n\t"
    "v_add_u32 v0, s42, v0\n\t" "v_mfma_f32_16x16x32_bf16 v[124:127], v[200:203], v[176:179], v[124:127]\n\t" "ds_read_b64_tr_b16 v[170:171], v6 offset:12800\n\t" "v_cvt_pk_bf16_f32 v28, v204, v205\n\t"
    "v_mfma_f32_16x16x32_bf16 v[128:131], v[196:199], v[180:183], v[128:131]\n\t" "ds_read_b64_tr_b16 v[172:173], v3 offset:14336\n\t" "v_cvt_pk_bf16_f32 v29, v206, v207\n\t"
    "v_mfma_f32_16x16x32_bf16 v[132:135], v[200:203], v[180:183], v[132:135]\n\t" "ds_read_b64_tr_b16 v[174:175], v3 offset:14848\n\t" "v_cvt_pk_bf16_f32 v30, v208, v209\n\t"
    "v_mfma_f32_16x16x32_bf16 v[136:139], v[196:199], v[184:187], v[136:139]\n\t" "ds_read_b64_tr_b16 v[176:177], v4 offset:14336\n\t" "v_cvt_pk_bf16_f32 v31, v210, v211\n\t"
    "v_mfma_f32_16x16x32_bf16 v[140:143], v[200:203], v[184:187], v[140:143]\n\t" "ds_read_b64_tr_b16 v[178:179], v4 offset:14848\n\t" "s_waitcnt lgkmcnt(4)\n\t"
    "v_mfma_f32_16x16x32_bf16 v[48:51], v[156:159], v[188:191], v[48:51]\n\t" "ds_read_b64_tr_b16 v[180:181], v5 offset:14336\n\t" "s_add_u32 m0, %[sl], 71680\n\t"
    "v_mfma_f32_16x16x32_bf16 v[52:55], v[156:159], v[192:195], v[52:55]\n\t" "ds_read_b64_tr_b16 v[182:183], v5 offset:14848\n\t" "v_cvt_pk_bf16_f32 v32, v212, v213\n\t"
    "v_mfma_f32_16x16x32_bf16 v[152:155], v[228:231], v[156:159], v[152:155]\n\t" "v_cvt_pk_bf16_f32 v33, v214, v215\n\t" "v_cvt_pk_bf16_f32 v34, v216, v217\n\t" "v_cvt_pk_bf16_f32 v35, v218, v219\n\t"
    "s_mov_b64 exec, s[60:61]\n\t" "ds_write_b128 v19, v[28:31] offset:16384\n\t" "ds_write_b128 v19, v[32:35] offset:17408\n\t" "s_mov_b64 exec, -1\n\t"
    "v_mfma_f32_16x16x32_bf16 v[56:59], v[160:163], v[188:191], v[56:59]\n\t" "ds_read_b64_tr_b16 v[184:185], v6 offset:14336\n\t" "global_load_lds_dwordx4 v0, %[sb] offset:-2048\n\t"
    "v_mfma_f32_4x4x4_16b_bf16 v[204:207], v[224:225], a[56:57], 0\n\t" "v_mfma_f32_4x4x4_16b_bf16 v[208:211], v[226:227], a[58:59], 0\n\t"
    "v_mfma_f32_4x4x4_16b_bf16 v[212:215], v[224:225], a[60:61], 0\n\t" "v_mfma_f32_4x4x4_16b_bf16 v[216:219], v[226:227], a[62:63], 0\n\t" "v_permlane16_swap_b32 v239, v20\n\t"
    "v_mfma_f32_16x16x32_bf16 v[60:63], v[160:163], v[192:195], v[60:63]\n\t" "ds_read_b64_tr_b16 v[186:187], v6 offset:14848\n\t" "v_permlane16_swap_b32 v240, v21\n\t"
    "v_mfma_f32_16x16x32_bf16 v[64:67], v[164:167], v[188:191], v[64:67]\n\t" "ds_read_b64_tr_b16 v[156:157], v3 offset:16384\n\t" "v_add_f32 v239, v239, v20\n\t"
    "v_mfma_f32_16x16x32_bf16 v[68:71], v[164:167], v[192:195], v[68:71]\n\t" "ds_read_b64_tr_b16 v[158:159], v3 offset:16896\n\t" "v_cvt_pk_bf16_f32 v196, v204, v205\n\t"
    "v_mfma_f32_16x16x32_bf16 v[72:75], v[168:171], v[188:191], v[72:75]\n\t" "ds_read_b64_tr_b16 v[160:161], v4 offset:16384\n\t" "v_cvt_pk_bf16_f32 v197, v206, v207\n\t"
    "v_mfma_f32_16x16x32_bf16 v[76:79], v[168:171], v[192:195], v[76:79]\n\t" "ds_read_b64_tr_b16 v[162:163], v4 offset:16896\n\t" "s_waitcnt lgkmcnt(4)\n\t"
    "v_mfma_f32_16x16x32_bf16 v[80:83], v[172:175], v[188:191], v[80:83]\n\t" "ds_read_b64_tr_b16 v[164:165], v5 offset:16384\n\t" "v_cvt_pk_bf16_f32 v198, v208, v209\n\t"
    "v_mfma_f32_16x16x32_bf16 v[84:87], v[172:175], v[192:195], v[84:87]\n\t" "ds_read_b64_tr_b16 v[166:167], v5 offset:16896\n\t" "v_cvt_pk_bf16_f32 v199, v210, v211\n\t"
    "v_mfma_f32_16x16x32_bf16 v[144:147], v[172:175], v[228:231], v[144:147]\n\t" "ds_read_b128 v[220:223], v2 offset:2048\n\t" "global_load_lds_dwordx4 v0, %[sb] offset:0\n\t"
    "v_mfma_f32_16x16x32_bf16 v[88:91], v[176:179], v[188:191], v[88:91]\n\t" "ds_read_b64_tr_b16 v[168:169], v6 offset:16384\n\t" "v_cvt_pk_bf16_f32 v200, v212, v213\n\t"
    "v_mfma_f32_16x16x32_bf16 v[92:95], v[176:179], v[192:195], v[92:95]\n\t" "ds_read_b64_tr_b16 v[170:171], v6 offset:16896\n\t" "v_cvt_pk_bf16_f32 v201, v214, v215\n\t"
    "v_mfma_f32_16x16x32_bf16 v[96:99], v[180:183], v[188:191], v[96:99]\n\t" "ds_read_b64_tr_b16 v[172:173], v3 offset:24576\n\t" "v_add_f32 v240, v240, v21\n\t"
    "v_mfma_f32_16x16x32_bf16 v[100:103], v[180:183], v[192:195], v[100:103]\n\t" "ds_read_b64_tr_b16 v[174:175], v3 offset:25088\n\t" "v_cvt_pk_bf16_f32 v202, v216, v217\n\t"
    "v_mfma_f32_16x16x32_bf16 v[104:107], v[184:187], v[188:191], v[104:107]\n\t" "ds_read_b64_tr_b16 v[176:177], v4 offset:24576\n\t" "v_cvt_pk_bf16_f32 v203, v218, v219\n\t"
    "v_mfma_f32_16x16x32_bf16 v[108:111], v[184:187], v[192:195], v[108:111]\n\t" "ds_read_b64_tr_b16 v[178:179], v4 offset:25088\n\t" "s_waitcnt lgkmcnt(4)\n\t"
    "v_mfma_f32_16x16x32_bf16 v[112:115], v[188:191], v[156:159], v[112:115]\n\t" "ds_read_b64_tr_b16 v[180:181], v5 offset:24576\n\t" "v_mov_b32 v20, v239\n\t"
    "v_mfma_f32_16x16x32_bf16 v[116:119], v[192:195], v[156:159], v[116:119]\n\t" "ds_read_b64_tr_b16 v[182:183], v5 offset:25088\n\t" "v_mov_b32 v21, v240\n\t"
    "v_mfma_f32_16x16x32_bf16 v[148:151], v[228:231], v[156:159], v[148:151]\n\t" "ds_read_b128 v[232:235], v1 offset:7168\n\t" "global_load_lds_dwordx4 v0, %[sb] offset:2048\n\t"
    "v_mfma_f32_16x16x32_bf16 v[120:123], v[188:191], v[160:163], v[120:123]\n\t" "ds_read_b64_tr_b16 v[184:185], v6 offset:24576\n\t" "v_add_u32 v0, 0x30000, v0\n\t"
    "v_mfma_f32_16x16x32_bf16 v[124:127], v[192:195], v[160:163], v[124:127]\n\t" "ds_read_b64_tr_b16 v[186:187], v6 offset:25088\n\t"
    "v_mfma_f32_16x16x32_bf16 v[128:131], v[188:191], v[164:167], v[128:131]\n\t" "ds_read_b64_tr_b16 v[156:157], v3 offset:26624\n\t"
    "v_mfma_f32_16x16x32_bf16 v[132:135], v[192:195], v[164:167], v[132:135]\n\t" "ds_read_b64_tr_b16 v[158:159], v3 offset:27136\n\t" "v_permlane32_swap_b32 v239, v20\n\t"
    "v_mfma_f32_16x16x32_bf16 v[136:139], v[188:191], v[168:171], v[136:139]\n\t" "ds_read_b64_tr_b16 v[160:161], v4 offset:26624\n\t" "v_permlane32_swap_b32 v240, v21\n\t"
    "v_mfma_f32_16x16x32_bf16 v[140:143], v[192:195], v[168:171], v[140:143]\n\t" "ds_read_b64_tr_b16 v[162:163], v4 offset:27136\n\t" "s_waitcnt lgkmcnt(4)\n\t"
    "v_mfma_f32_16x16x32_bf16 v[48:51], v[172:175], v[196:199], v[48:51]\n\t" "ds_read_b64_tr_b16 v[164:165], v5 offset:26624\n\t" "s_add_u32 m0, %[sl], 83968\n\t"
    "v_mfma_f32_16x16x32_bf16 v[52:55], v[172:175], v[200:203], v[52:55]\n\t" "ds_read_b64_tr_b16 v[166:167], v5 offset:27136\n\t" "ds_read_b128 v[44:47], v18 offset:6144\n\t"
    "v_mfma_f32_16x16x32_bf16 v[152:155], v[232:235], v[172:175], v[152:155]\n\t" "global_load_lds_dwordx4 v0, %[sb] offset:-2048\n\t" "ds_read_b128 v[242:245], v18 offset:6400\n\t"
    "v_mfma_f32_16x16x32_bf16 v[56:59], v[176:179], v[196:199], v[56:59]\n\t" "ds_read_b64_tr_b16 v[168:169], v6 offset:26624\n\t" "v_add_f32 v239, v239, v20\n\t"
    "v_mfma_f32_4x4x4_16b_bf16 v[204:207], v[220:221], a[64:65], 0\n\t" "v_mfma_f32_4x4x4_16b_bf16 v[208:211], v[222:223], a[66:67], 0\n\t"
    "v_mfma_f32_4x4x4_16b_bf16 v[212:215], v[220:221], a[68:69], 0\n\t" "v_mfma_f32_4x4x4_16b_bf16 v[216:219], v[222:223], a[70:71], 0\n\t" "v_add_f32 v240, v240, v21\n\t"
    "v_mfma_f32_16x16x32_bf16 v[60:63], v[176:179], v[200:203], v[60:63]\n\t" "ds_read_b64_tr_b16 v[170:171], v6 offset:27136\n\t" "v_mul_f32 v236, 0x3a800000, v236\n\t"
    "v_mfma_f32_16x16x32_bf16 v[64:67], v[180:183], v[196:199], v[64:67]\n\t" "ds_read_b64_tr_b16 v[172:173], v3 offset:28672\n\t" "v_mul_f32 v237, 0x3a800000, v237\n\t"
    "v_mfma_f32_16x16x32_bf16 v[68:71], v[180:183], v[200:203], v[68:71]\n\t" "ds_read_b64_tr_b16 v[174:175], v3 offset:29184\n\t" "v_cvt_pk_bf16_f32 v188, v204, v205\n\t"
    "v_mfma_f32_16x16x32_bf16 v[72:75], v[184:187], v[196:199], v[72:75]\n\t" "ds_read_b64_tr_b16 v[176:177], v4 offset:28672\n\t" "v_cvt_pk_bf16_f32 v189, v206, v207\n\t"
    "v_mfma_f32_16x16x32_bf16 v[76:79], v[184:187], v[200:203], v[76:79]\n\t" "ds_read_b64_tr_b16 v[178:179], v4 offset:29184\n\t" "s_waitcnt lgkmcnt(4)\n\t"
    "v_mfma_f32_16x16x32_bf16 v[80:83], v[156:159], v[196:199], v[80:83]\n\t" "ds_read_b64_tr_b16 v[180:181], v5 offset:28672\n\t" "v_cvt_pk_bf16_f32 v190, v208, v209\n\t"
    "v_mfma_f32_16x16x32_bf16 v[84:87], v[156:159], v[200:203], v[84:87]\n\t" "ds_read_b64_tr_b16 v[182:183], v5 offset:29184\n\t" "v_cvt_pk_bf16_f32 v191, v210, v211\n\t"
    "v_mfma_f32_16x16x32_bf16 v[144:147], v[156:159], v[232:235], v[144:147]\n\t" "ds_read_b128 v[224:227], v2 offset:2304\n\t" "global_load_lds_dwordx4 v0, %[sb] offset:0\n\t"
    "v_mfma_f32_16x16x32_bf16 v[88:91], v[160:163], v[196:199], v[88:91]\n\t" "ds_read_b64_tr_b16 v[184:185], v6 offset:28672\n\t" "v_cvt_pk_bf16_f32 v192, v212, v213\n\t"
    "v_mfma_f32_16x16x32_bf16 v[92:95], v[160:163], v[200:203], v[92:95]\n\t" "ds_read_b64_tr_b16 v[186:187], v6 offset:29184\n\t" "v_cvt_pk_bf16_f32 v193, v214, v215\n\t"
    "v_mfma_f32_16x16x32_bf16 v[96:99], v[164:167], v[196:199], v[96:99]\n\t" "s_waitcnt vmcnt(11)\n\t" "s_barrier\n\t" "ds_read_b64_tr_b16 v[156:157], v3 offset:36864\n\t"
    "v_mfma_f32_16x16x32_bf16 v[100:103], v[164:167], v[200:203], v[100:103]\n\t" "ds_read_b64_tr_b16 v[158:159], v3 offset:37376\n\t" "v_cvt_pk_bf16_f32 v194, v216, v217\n\t"
    "v_mfma_f32_16x16x32_bf16 v[104:107], v[168:171], v[196:199], v[104:107]\n\t" "ds_read_b64_tr_b16 v[160:161], v4 offset:36864\n\t" "v_cvt_pk_bf16_f32 v195, v218, v219\n\t"
    "v_mfma_f32_16x16x32_bf16 v[108:111], v[168:171], v[200:203], v[108:111]\n\t" "ds_read_b64_tr_b16 v[162:163], v4 offset:37376\n\t" "v_mfma_f32_4x4x4_16b_bf16 v[204:207], v[44:45], v[36:37], 0\n\t"
    "v_mfma_f32_4x4x4_16b_bf16 v[208:211], v[46:47], v[38:39], 0\n\t" "v_mfma_f32_4x4x4_16b_bf16 v[212:215], v[242:243], v[40:41], 0\n\t"
    "v_mfma_f32_4x4x4_16b_bf16 v[216:219], v[244:245], v[42:43], 0\n\t" "s_waitcnt lgkmcnt(4)\n\t" "v_mfma_f32_16x16x32_bf16 v[112:115], v[196:199], v[172:175], v[112:115]\n\t"
    "ds_read_b64_tr_b16 v[164:165], v5 offset:36864\n\t" "v_mfma_f32_16x16x32_bf16 v[116:119], v[200:203], v[172:175], v[116:119]\n\t" "ds_read_b64_tr_b16 v[166:167], v5 offset:37376\n\t"
    "v_mfma_f32_16x16x32_bf16 v[148:151], v[232:235], v[172:175], v[148:151]\n\t" "ds_read_b128 v[228:231], v1 offset:8192\n\t" "global_load_lds_dwordx4 v0, %[sb] offset:2048\n\t"
    "v_mfma_f32_16x16x32_bf16 v[120:123], v[196:199], v[176:179], v[120:123]\n\t" "ds_read_b64_tr_b16 v[168:169], v6 offset:36864\n\t" "v_add_u32 v0, 0x30000, v0\n\t"
    "v_mfma_f32_16x16x32_bf16 v[124:127], v[200:203], v[176:179], v[124:127]\n\t" "ds_read_b64_tr_b16 v[170:171], v6 offset:37376\n\t" "v_cvt_pk_bf16_f32 v36, v204, v205\n\t"
    "v_mfma_f32_16x16x32_bf16 v[128:131], v[196:199], v[180:183], v[128:131]\n\t" "ds_read_b64_tr_b16 v[172:173], v3 offset:38912\n\t" "v_cvt_pk_bf16_f32 v37, v206, v207\n\t"
    "v_mfma_f32_16x16x32_bf16 v[132:135], v[200:203], v[180:183], v[132:135]\n\t" "ds_read_b64_tr_b16 v[174:175], v3 offset:39424\n\t" "v_cvt_pk_bf16_f32 v38, v208, v209\n\t"
    "v_mfma_f32_16x16x32_bf16 v[136:139], v[196:199], v[184:187], v[136:139]\n\t" "ds_read_b64_tr_b16 v[176:177], v4 offset:38912\n\t" "v_cvt_pk_bf16_f32 v39, v210, v211\n\t"
    "v_mfma_f32_16x16x32_bf16 v[140:143], v[200:203], v[184:187], v[140:143]\n\t" "ds_read_b64_tr_b16 v[178:179], v4 offset:39424\n\t" "s_waitcnt lgkmcnt(4)\n\t"
    "v_mfma_f32_16x16x32_bf16 v[48:51], v[156:159], v[188:191], v[48:51]\n\t" "ds_read_b64_tr_b16 v[180:181], v5 offset:38912\n\t" "s_add_u32 m0, %[sl], 22528\n\t"
    "v_mfma_f32_16x16x32_bf16 v[52:55], v[156:159], v[192:195], v[52:55]\n\t" "ds_read_b64_tr_b16 v[182:183], v5 offset:39424\n\t" "v_cvt_pk_bf16_f32 v40, v212, v213\n\t"
    "v_mfma_f32_16x16x32_bf16 v[152:155], v[228:231], v[156:159], v[152:155]\n\t" "v_cvt_pk_bf16_f32 v41, v214, v215\n\t" "v_cvt_pk_bf16_f32 v42, v216, v217\n\t" "v_cvt_pk_bf16_f32 v43, v218, v219\n\t"
    "s_mov_b64 exec, s[60:61]\n\t" "ds_write_b128 v19, v[36:39] offset:24576\n\t" "ds_write_b128 v19, v[40:43] offset:25600\n\t" "s_mov_b64 exec, -1\n\t"
    "v_mfma_f32_16x16x32_bf16 v[56:59], v[160:163], v[188:191], v[56:59]\n\t" "ds_read_b64_tr_b16 v[184:185], v6 offset:38912\n\t" "global_load_lds_dwordx4 v0, %[sb] offset:-2048\n\t"
    "v_mfma_f32_4x4x4_16b_bf16 v[204:207], v[224:225], a[72:73], 0\n\t" "v_mfma_f32_4x4x4_16b_bf16 v[208:211], v[226:227], a[74:75], 0\n\t"
    "v_mfma_f32_4x4x4_16b_bf16 v[212:215], v[224:225], a[76:77], 0\n\t" "v_mfma_f32_4x4x4_16b_bf16 v[216:219], v[226:227], a[78:79], 0\n\t" "v_mul_f32 v238, 0x3a800000, v238\n\t"
    "v_mfma_f32_16x16x32_bf16 v[60:63], v[160:163], v[192:195], v[60:63]\n\t" "ds_read_b64_tr_b16 v[186:187], v6 offset:39424\n\t" "v_mul_f32 v239, 0x3a800000, v239\n\t"
    "v_mfma_f32_16x16x32_bf16 v[64:67], v[164:167], v[188:191], v[64:67]\n\t" "ds_read_b64_tr_b16 v[156:157], v3 offset:40960\n\t" "global_load_lds_dwordx4 v0, %[sb] offset:0\n\t"
    "v_mfma_f32_16x16x32_bf16 v[68:71], v[164:167], v[192:195], v[68:71]\n\t" "ds_read_b64_tr_b16 v[158:159], v3 offset:41472\n\t" "v_cvt_pk_bf16_f32 v196, v204, v205\n\t"
    "v_mfma_f32_16x16x32_bf16 v[72:75], v[168:171], v[188:191], v[72:75]\n\t" "ds_read_b64_tr_b16 v[160:161], v4 offset:40960\n\t" "v_cvt_pk_bf16_f32 v197, v206, v207\n\t"
    "v_mfma_f32_16x16x32_bf16 v[76:79], v[168:171], v[192:195], v[76:79]\n\t" "ds_read_b64_tr_b16 v[162:163], v4 offset:41472\n\t" "s_waitcnt lgkmcnt(4)\n\t"
    "v_mfma_f32_16x16x32_bf16 v[80:83], v[172:175], v[188:191], v[80:83]\n\t" "ds_read_b64_tr_b16 v[164:165], v5 offset:40960\n\t" "v_cvt_pk_bf16_f32 v198, v208, v209\n\t"
    "v_mfma_f32_16x16x32_bf16 v[84:87], v[172:175], v[192:195], v[84:87]\n\t" "ds_read_b64_tr_b16 v[166:167], v5 offset:41472\n\t" "v_cvt_pk_bf16_f32 v199, v210, v211\n\t"
    "v_mfma_f32_16x16x32_bf16 v[144:147], v[172:175], v[228:231], v[144:147]\n\t" "ds_read_b128 v[220:223], v2 offset:2560\n\t" "global_load_lds_dwordx4 v0, %[sb] offset:2048\n\t"
    "v_mfma_f32_16x16x32_bf16 v[88:91], v[176:179], v[188:191], v[88:91]\n\t" "ds_read_b64_tr_b16 v[168:169], v6 offset:40960\n\t" "v_cvt_pk_bf16_f32 v200, v212, v213\n\t"
    "v_mfma_f32_16x16x32_bf16 v[92:95], v[176:179], v[192:195], v[92:95]\n\t" "ds_read_b64_tr_b16 v[170:171], v6 offset:41472\n\t" "v_cvt_pk_bf16_f32 v201, v214, v215\n\t"
    "v_mfma_f32_16x16x32_bf16 v[96:99], v[180:183], v[188:191], v[96:99]\n\t" "ds_read_b64_tr_b16 v[172:173], v7 offset:12288\n\t" "v_add_u32 v0, 0x30000, v0\n\t"
    "v_mfma_f32_16x16x32_bf16 v[100:103], v[180:183], v[192:195], v[100:103]\n\t" "ds_read_b64_tr_b16 v[174:175], v7 offset:12800\n\t" "v_cvt_pk_bf16_f32 v202, v216, v217\n\t"
    "v_mfma_f32_16x16x32_bf16 v[104:107], v[184:187], v[188:191], v[104:107]\n\t" "ds_read_b64_tr_b16 v[176:177], v8 offset:12288\n\t" "v_cvt_pk_bf16_f32 v203, v218, v219\n\t"
    "v_mfma_f32_16x16x32_bf16 v[108:111], v[184:187], v[192:195], v[108:111]\n\t" "ds_read_b64_tr_b16 v[178:179], v8 offset:12800\n\t" "s_waitcnt lgkmcnt(4)\n\t"
    "v_mfma_f32_16x16x32_bf16 v[112:115], v[188:191], v[156:159], v[112:115]\n\t" "ds_read_b64_tr_b16 v[180:181], v9 offset:12288\n\t" "v_mul_f32 v240, 0x3a800000, v240\n\t"
    "v_mfma_f32_16x16x32_bf16 v[116:119], v[192:195], v[156:159], v[116:119]\n\t" "ds_read_b64_tr_b16 v[182:183], v9 offset:12800\n\t" "v_mul_f32 v241, 0x3a800000, v241\n\t"
    "v_mfma_f32_16x16x32_bf16 v[148:151], v[228:231], v[156:159], v[148:151]\n\t" "ds_read_b128 v[232:235], v1 offset:9216\n\t" "global_load_dwordx4 a[32:35], v11, %[sm] nt\n\t"
    "v_mfma_f32_16x16x32_bf16 v[120:123], v[188:191], v[160:163], v[120:123]\n\t" "ds_read_b64_tr_b16 v[184:185], v10 offset:12288\n\t" "v_add_u32 v11, 0x3000, v11\n\t"
    "v_mfma_f32_16x16x32_bf16 v[124:127], v[192:195], v[160:163], v[124:127]\n\t" "ds_read_b64_tr_b16 v[186:187], v10 offset:12800\n\t" "v_fma_f32 v239, -v236, v236, v239\n\t"
    "v_mfma_f32_16x16x32_bf16 v[128:131], v[188:191], v[164:167], v[128:131]\n\t" "ds_read_b64_tr_b16 v[156:157], v7 offset:14336\n\t" "v_fma_f32 v240, -v237, v237, v240\n\t"
    "v_mfma_f32_16x16x32_bf16 v[132:135], v[192:195], v[164:167], v[132:135]\n\t" "ds_read_b64_tr_b16 v[158:159], v7 offset:14848\n\t" "global_load_dwordx4 a[36:39], v11, %[sm] nt\n\t"
    "v_mfma_f32_16x16x32_bf16 v[136:139], v[188:191], v[168:171], v[136:139]\n\t" "ds_read_b64_tr_b16 v[160:161], v8 offset:14336\n\t" "v_fma_f32 v241, -v238, v238, v241\n\t"
    "v_mfma_f32_16x16x32_bf16 v[140:143], v[192:195], v[168:171], v[140:143]\n\t" "ds_read_b64_tr_b16 v[162:163], v8 offset:14848\n\t" "s_waitcnt lgkmcnt(4)\n\t"
    "v_mfma_f32_16x16x32_bf16 v[48:51], v[172:175], v[196:199], v[48:51]\n\t" "ds_read_b64_tr_b16 v[164:165], v9 offset:14336\n\t" "s_add_u32 m0, %[sl], 34816\n\t"
    "v_mfma_f32_16x16x32_bf16 v[52:55], v[172:175], v[200:203], v[52:55]\n\t" "ds_read_b64_tr_b16 v[166:167], v9 offset:14848\n\t" "v_max_f32 v239, 0, v239\n\t"
    "v_mfma_f32_16x16x32_bf16 v[152:155], v[232:235], v[172:175], v[152:155]\n\t" "global_load_lds_dwordx4 v0, %[sb] offset:-2048\n\t" "v_max_f32 v240, 0, v240\n\t"
    "v_mfma_f32_16x16x32_bf16 v[56:59], v[176:179], v[196:199], v[56:59]\n\t" "ds_read_b64_tr_b16 v[168:169], v10 offset:14336\n\t" "v_max_f32 v241, 0, v241\n\t"
    "v_mfma_f32_4x4x4_16b_bf16 v[204:207], v[220:221], a[80:81], 0\n\t" "v_mfma_f32_4x4x4_16b_bf16 v[208:211], v[222:223], a[82:83], 0\n\t"
    "v_mfma_f32_4x4x4_16b_bf16 v[212:215], v[220:221], a[84:85], 0\n\t" "v_mfma_f32_4x4x4_16b_bf16 v[216:219], v[222:223], a[86:87], 0\n\t" "v_add_f32 v239, 0x3727c5ac, v239\n\t"
    "v_mfma_f32_16x16x32_bf16 v[60:63], v[176:179], v[200:203], v[60:63]\n\t" "ds_read_b64_tr_b16 v[170:171], v10 offset:14848\n\t" "v_add_f32 v240, 0x3727c5ac, v240\n\t"
    "v_mfma_f32_16x16x32_bf16 v[64:67], v[180:183], v[196:199], v[64:67]\n\t" "ds_read_b64_tr_b16 v[172:173], v7 offset:16384\n\t" "global_load_lds_dwordx4 v0, %[sb] offset:0\n\t"
    "v_mfma_f32_16x16x32_bf16 v[68:71], v[180:183], v[200:203], v[68:71]\n\t" "ds_read_b64_tr_b16 v[174:175], v7 offset:16896\n\t" "v_cvt_pk_bf16_f32 v188, v204, v205\n\t"
    "v_mfma_f32_16x16x32_bf16 v[72:75], v[184:187], v[196:199], v[72:75]\n\t" "ds_read_b64_tr_b16 v[176:177], v8 offset:16384\n\t" "v_cvt_pk_bf16_f32 v189, v206, v207\n\t"
    "v_mfma_f32_16x16x32_bf16 v[76:79], v[184:187], v[200:203], v[76:79]\n\t" "ds_read_b64_tr_b16 v[178:179], v8 offset:16896\n\t" "s_waitcnt lgkmcnt(4)\n\t"
    "v_mfma_f32_16x16x32_bf16 v[80:83], v[156:159], v[196:199], v[80:83]\n\t" "ds_read_b64_tr_b16 v[180:181], v9 offset:16384\n\t" "v_cvt_pk_bf16_f32 v190, v208, v209\n\t"
    "v_mfma_f32_16x16x32_bf16 v[84:87], v[156:159], v[200:203], v[84:87]\n\t" "ds_read_b64_tr_b16 v[182:183], v9 offset:16896\n\t" "v_cvt_pk_bf16_f32 v191, v210, v211\n\t"
    "v_mfma_f32_16x16x32_bf16 v[144:147], v[156:159], v[232:235], v[144:147]\n\t" "ds_read_b128 v[224:227], v2 offset:2816\n\t" "global_load_lds_dwordx4 v0, %[sb] offset:2048\n\t"
    "v_mfma_f32_16x16x32_bf16 v[88:91], v[160:163], v[196:199], v[88:91]\n\t" "ds_read_b64_tr_b16 v[184:185], v10 offset:16384\n\t" "v_cvt_pk_bf16_f32 v192, v212, v213\n\t"
    "v_mfma_f32_16x16x32_bf16 v[92:95], v[160:163], v[200:203], v[92:95]\n\t" "ds_read_b64_tr_b16 v[186:187], v10 offset:16896\n\t" "v_cvt_pk_bf16_f32 v193, v214, v215\n\t"
    "v_mfma_f32_16x16x32_bf16 v[96:99], v[164:167], v[196:199], v[96:99]\n\t" "s_waitcnt vmcnt(14)\n\t" "s_barrier\n\t" "ds_read_b64_tr_b16 v[156:157], v7 offset:24576\n\t"
    "v_mfma_f32_16x16x32_bf16 v[100:103], v[164:167], v[200:203], v[100:103]\n\t" "ds_read_b64_tr_b16 v[158:159], v7 offset:25088\n\t" "v_cvt_pk_bf16_f32 v194, v216, v217\n\t"
    "v_mfma_f32_16x16x32_bf16 v[104:107], v[168:171], v[196:199], v[104:107]\n\t" "ds_read_b64_tr_b16 v[160:161], v8 offset:24576\n\t" "v_cvt_pk_bf16_f32 v195, v218, v219\n\t"
    "v_mfma_f32_16x16x32_bf16 v[108:111], v[168:171], v[200:203], v[108:111]\n\t" "ds_read_b64_tr_b16 v[162:163], v8 offset:25088\n\t" "s_waitcnt lgkmcnt(4)\n\t"
    "v_mfma_f32_16x16x32_bf16 v[112:115], v[196:199], v[172:175], v[112:115]\n\t" "ds_read_b64_tr_b16 v[164:165], v9 offset:24576\n\t" "s_cmp_eq_u32 %[rot], 4\n\t" "s_cselect_b32 s42, s43, 0x30000\n\t"
    "v_add_u32 v0, s42, v0\n\t" "v_mfma_f32_16x16x32_bf16 v[116:119], v[200:203], v[172:175], v[116:119]\n\t" "ds_read_b64_tr_b16 v[166:167], v9 offset:25088\n\t" "v_add_u32 v11, 0x3000, v11\n\t"
    "v_mfma_f32_16x16x32_bf16 v[148:151], v[232:235], v[172:175], v[148:151]\n\t" "ds_read_b128 v[228:231], v1 offset:10240\n\t" "global_load_dwordx4 a[40:43], v11, %[sm] nt\n\t"
    "v_mfma_f32_16x16x32_bf16 v[120:123], v[196:199], v[176:179], v[120:123]\n\t" "ds_read_b64_tr_b16 v[168:169], v10 offset:24576\n\t" "v_add_u32 v11, 0x3000, v11\n\t"
    "v_mfma_f32_16x16x32_bf16 v[124:127], v[200:203], v[176:179], v[124:127]\n\t" "ds_read_b64_tr_b16 v[170:171], v10 offset:25088\n\t" "v_add_f32 v241, 0x3727c5ac, v241\n\t"
    "v_mfma_f32_16x16x32_bf16 v[128:131], v[196:199], v[180:183], v[128:131]\n\t" "ds_read_b64_tr_b16 v[172:173], v7 offset:26624\n\t" "v_rsq_f32 v20, v239\n\t"
    "v_mfma_f32_16x16x32_bf16 v[132:135], v[200:203], v[180:183], v[132:135]\n\t" "ds_read_b64_tr_b16 v[174:175], v7 offset:27136\n\t" "global_load_dwordx4 a[44:47], v11, %[sm] nt\n\t"
    "v_mfma_f32_16x16x32_bf16 v[136:139], v[196:199], v[184:187], v[136:139]\n\t" "ds_read_b64_tr_b16 v[176:177], v8 offset:26624\n\t" "v_rsq_f32 v21, v240\n\t"
    "v_mfma_f32_16x16x32_bf16 v[140:143], v[200:203], v[184:187], v[140:143]\n\t" "ds_read_b64_tr_b16 v[178:179], v8 offset:27136\n\t" "s_waitcnt lgkmcnt(4)\n\t"
    "v_mfma_f32_16x16x32_bf16 v[48:51], v[156:159], v[188:191], v[48:51]\n\t" "ds_read_b64_tr_b16 v[180:181], v9 offset:26624\n\t" "s_add_u32 m0, %[sl], 47104\n\t"
    "v_mfma_f32_16x16x32_bf16 v[52:55], v[156:159], v[192:195], v[52:55]\n\t" "ds_read_b64_tr_b16 v[182:183], v9 offset:27136\n\t" "v_rsq_f32 v22, v241\n\t"
    "v_mfma_f32_16x16x32_bf16 v[152:155], v[228:231], v[156:159], v[152:155]\n\t" "global_load_lds_dwordx4 v0, %[sb] offset:-2048\n\t" "v_and_b32 v26, 15, v13\n\t"
    "v_mfma_f32_16x16x32_bf16 v[56:59], v[160:163], v[188:191], v[56:59]\n\t" "ds_read_b64_tr_b16 v[184:185], v10 offset:26624\n\t" "v_lshl_add_u32 v26, v26, 2, %[l0]\n\t"
    "v_mfma_f32_4x4x4_16b_bf16 v[204:207], v[224:225], a[88:89], 0\n\t" "v_mfma_f32_4x4x4_16b_bf16 v[208:211], v[226:227], a[90:91], 0\n\t"
    "v_mfma_f32_4x4x4_16b_bf16 v[212:215], v[224:225], a[92:93], 0\n\t" "v_mfma_f32_4x4x4_16b_bf16 v[216:219], v[226:227], a[94:95], 0\n\t" "v_add_u32 v27, %[w128], v26\n\t"
    "v_mfma_f32_16x16x32_bf16 v[60:63], v[160:163], v[192:195], v[60:63]\n\t" "ds_read_b64_tr_b16 v[186:187], v10 offset:27136\n\t" "v_mul_f32 v239, v239, v20\n\t"
    "v_mfma_f32_16x16x32_bf16 v[64:67], v[164:167], v[188:191], v[64:67]\n\t" "ds_read_b64_tr_b16 v[156:157], v7 offset:28672\n\t" "global_load_lds_dwordx4 v0, %[sb] offset:0\n\t"
    "v_mfma_f32_16x16x32_bf16 v[68:71], v[164:167], v[192:195], v[68:71]\n\t" "ds_read_b64_tr_b16 v[158:159], v7 offset:29184\n\t" "v_cvt_pk_bf16_f32 v196, v204, v205\n\t"
    "v_mfma_f32_16x16x32_bf16 v[72:75], v[168:171], v[188:191], v[72:75]\n\t" "ds_read_b64_tr_b16 v[160:161], v8 offset:28672\n\t" "v_cvt_pk_bf16_f32 v197, v206, v207\n\t"
    "v_mfma_f32_16x16x32_bf16 v[76:79], v[168:171], v[192:195], v[76:79]\n\t" "ds_read_b64_tr_b16 v[162:163], v8 offset:29184\n\t" "s_waitcnt lgkmcnt(4)\n\t"
    "v_mfma_f32_16x16x32_bf16 v[80:83], v[172:175], v[188:191], v[80:83]\n\t" "ds_read_b64_tr_b16 v[164:165], v9 offset:28672\n\t" "v_cvt_pk_bf16_f32 v198, v208, v209\n\t"
    "v_mfma_f32_16x16x32_bf16 v[84:87], v[172:175], v[192:195], v[84:87]\n\t" "ds_read_b64_tr_b16 v[166:167], v9 offset:29184\n\t" "v_cvt_pk_bf16_f32 v199, v210, v211\n\t"
    "v_mfma_f32_16x16x32_bf16 v[144:147], v[172:175], v[228:231], v[144:147]\n\t" "ds_read_b128 v[220:223], v2 offset:3072\n\t" "global_load_lds_dwordx4 v0, %[sb] offset:2048\n\t"
    "v_mfma_f32_16x16x32_bf16 v[88:91], v[176:179], v[188:191], v[88:91]\n\t" "ds_read_b64_tr_b16 v[168:169], v10 offset:28672\n\t" "v_cvt_pk_bf16_f32 v200, v212, v213\n\t"
    "v_mfma_f32_16x16x32_bf16 v[92:95], v[176:179], v[192:195], v[92:95]\n\t" "ds_read_b64_tr_b16 v[170:171], v10 offset:29184\n\t" "v_cvt_pk_bf16_f32 v201, v214, v215\n\t"
    "v_mfma_f32_16x16x32_bf16 v[96:99], v[180:183], v[188:191], v[96:99]\n\t" "ds_read_b64_tr_b16 v[172:173], v7 offset:36864\n\t" "v_add_u32 v0, 0x30000, v0\n\t"
    "v_mfma_f32_16x16x32_bf16 v[100:103], v[180:183], v[192:195], v[100:103]\n\t" "ds_read_b64_tr_b16 v[174:175], v7 offset:37376\n\t" "v_cvt_pk_bf16_f32 v202, v216, v217\n\t"
    "v_mfma_f32_16x16x32_bf16 v[104:107], v[184:187], v[188:191], v[104:107]\n\t" "ds_read_b64_tr_b16 v[176:177], v8 offset:36864\n\t" "v_cvt_pk_bf16_f32 v203, v218, v219\n\t"
    "v_mfma_f32_16x16x32_bf16 v[108:111], v[184:187], v[192:195], v[108:111]\n\t" "ds_read_b64_tr_b16 v[178:179], v8 offset:37376\n\t" "s_waitcnt lgkmcnt(4)\n\t"
    "v_mfma_f32_16x16x32_bf16 v[112:115], v[188:191], v[156:159], v[112:115]\n\t" "ds_read_b64_tr_b16 v[180:181], v9 offset:36864\n\t" "v_add_u32 v11, 0x3000, v11\n\t"
    "v_mfma_f32_16x16x32_bf16 v[116:119], v[192:195], v[156:159], v[116:119]\n\t" "ds_read_b64_tr_b16 v[182:183], v9 offset:37376\n\t" "v_mul_f32 v240, v240, v21\n\t"
    "v_mfma_f32_16x16x32_bf16 v[148:151], v[228:231], v[156:159], v[148:151]\n\t" "ds_read_b128 v[232:235], v1 offset:11264\n\t" "global_load_dwordx4 a[48:51], v11, %[sm] nt\n\t"
    "v_mfma_f32_16x16x32_bf16 v[120:123], v[188:191], v[160:163], v[120:123]\n\t" "ds_read_b64_tr_b16 v[184:185], v10 offset:36864\n\t" "v_add_u32 v11, 0x3000, v11\n\t"
    "v_mfma_f32_16x16x32_bf16 v[124:127], v[192:195], v[160:163], v[124:127]\n\t" "ds_read_b64_tr_b16 v[186:187], v10 offset:37376\n\t" "v_mul_f32 v241, v241, v22\n\t"
    "v_mfma_f32_16x16x32_bf16 v[128:131], v[188:191], v[164:167], v[128:131]\n\t" "ds_read_b64_tr_b16 v[156:157], v7 offset:38912\n\t" "ds_write_b32 v27, v20 offset:17472\n\t"
    "v_mfma_f32_16x16x32_bf16 v[132:135], v[192:195], v[164:167], v[132:135]\n\t" "ds_read_b64_tr_b16 v[158:159], v7 offset:39424\n\t" "global_load_dwordx4 a[52:55], v11, %[sm] nt\n\t"
    "v_mfma_f32_16x16x32_bf16 v[136:139], v[188:191], v[168:171], v[136:139]\n\t" "ds_read_b64_tr_b16 v[160:161], v8 offset:38912\n\t" "ds_write_b32 v27, v21 offset:17536\n\t"
    "v_mfma_f32_16x16x32_bf16 v[140:143], v[192:195], v[168:171], v[140:143]\n\t" "ds_read_b64_tr_b16 v[162:163], v8 offset:39424\n\t" "s_waitcnt lgkmcnt(6)\n\t"
    "v_mfma_f32_16x16x32_bf16 v[48:51], v[172:175], v[196:199], v[48:51]\n\t" "ds_read_b64_tr_b16 v[164:165], v9 offset:38912\n\t" "s_add_u32 m0, %[sl], 59392\n\t"
    "v_mfma_f32_16x16x32_bf16 v[52:55], v[172:175], v[200:203], v[52:55]\n\t" "ds_read_b64_tr_b16 v[166:167], v9 offset:39424\n\t" "ds_write_b32 v26, v22 offset:17408\n\t"
    "v_mfma_f32_16x16x32_bf16 v[152:155], v[232:235], v[172:175], v[152:155]\n\t" "global_load_lds_dwordx4 v0, %[sb] offset:-2048\n\t" "s_mov_b64 s[46:47], 0xffff\n\t"
    "v_mfma_f32_16x16x32_bf16 v[56:59], v[176:179], v[196:199], v[56:59]\n\t" "ds_read_b64_tr_b16 v[168:169], v10 offset:38912\n\t" "s_mov_b32 s48, -1\n\t"
    "v_mfma_f32_4x4x4_16b_bf16 v[204:207], v[220:221], a[96:97], 0\n\t" "v_mfma_f32_4x4x4_16b_bf16 v[208:211], v[222:223], a[98:99], 0\n\t"
    "v_mfma_f32_4x4x4_16b_bf16 v[212:215], v[220:221], a[100:101], 0\n\t" "v_mfma_f32_4x4x4_16b_bf16 v[216:219], v[222:223], a[102:103], 0\n\t" "s_mov_b32 s49, 0\n\t"
    "v_mfma_f32_16x16x32_bf16 v[60:63], v[176:179], v[200:203], v[60:63]\n\t" "ds_read_b64_tr_b16 v[170:171], v10 offset:39424\n\t" "v_and_b32 v23, 0xffff0000, v236\n\t"
    "v_mfma_f32_16x16x32_bf16 v[64:67], v[180:183], v[196:199], v[64:67]\n\t" "ds_read_b64_tr_b16 v[172:173], v7 offset:40960\n\t" "global_load_lds_dwordx4 v0, %[sb] offset:0\n\t"
    "v_mfma_f32_16x16x32_bf16 v[68:71], v[180:183], v[200:203], v[68:71]\n\t" "ds_read_b64_tr_b16 v[174:175], v7 offset:41472\n\t" "v_cvt_pk_bf16_f32 v188, v204, v205\n\t"
    "v_mfma_f32_16x16x32_bf16 v[72:75], v[184:187], v[196:199], v[72:75]\n\t" "ds_read_b64_tr_b16 v[176:177], v8 offset:40960\n\t" "v_cvt_pk_bf16_f32 v189, v206, v207\n\t"
    "v_mfma_f32_16x16x32_bf16 v[76:79], v[184:187], v[200:203], v[76:79]\n\t" "ds_read_b64_tr_b16 v[178:179], v8 offset:41472\n\t" "s_waitcnt lgkmcnt(4)\n\t"
    "v_mfma_f32_16x16x32_bf16 v[80:83], v[156:159], v[196:199], v[80:83]\n\t" "ds_read_b64_tr_b16 v[180:181], v9 offset:40960\n\t" "v_cvt_pk_bf16_f32 v190, v208, v209\n\t"
    "v_mfma_f32_16x16x32_bf16 v[84:87], v[156:159], v[200:203], v[84:87]\n\t" "ds_read_b64_tr_b16 v[182:183], v9 offset:41472\n\t" "v_cvt_pk_bf16_f32 v191, v210, v211\n\t"
    "v_mfma_f32_16x16x32_bf16 v[144:147], v[156:159], v[232:235], v[144:147]\n\t" "ds_read_b128 v[224:227], v2 offset:3328\n\t" "global_load_lds_dwordx4 v0, %[sb] offset:2048\n\t"
    "v_mfma_f32_16x16x32_bf16 v[88:91], v[160:163], v[196:199], v[88:91]\n\t" "ds_read_b64_tr_b16 v[184:185], v10 offset:40960\n\t" "v_cvt_pk_bf16_f32 v192, v212, v213\n\t"
    "v_mfma_f32_16x16x32_bf16 v[92:95], v[160:163], v[200:203], v[92:95]\n\t" "ds_read_b64_tr_b16 v[186:187], v10 offset:41472\n\t" "v_cvt_pk_bf16_f32 v193, v214, v215\n\t"
    "v_mfma_f32_16x16x32_bf16 v[96:99], v[164:167], v[196:199], v[96:99]\n\t" "s_waitcnt vmcnt(18)\n\t" "s_barrier\n\t" "ds_read_b64_tr_b16 v[156:157], v3 offset:49152\n\t"
    "v_mfma_f32_16x16x32_bf16 v[100:103], v[164:167], v[200:203], v[100:103]\n\t" "ds_read_b64_tr_b16 v[158:159], v3 offset:49664\n\t" "v_cvt_pk_bf16_f32 v194, v216, v217\n\t"
    "v_mfma_f32_16x16x32_bf16 v[104:107], v[168:171], v[196:199], v[104:107]\n\t" "ds_read_b64_tr_b16 v[160:161], v4 offset:49152\n\t" "v_cvt_pk_bf16_f32 v195, v218, v219\n\t"
    "v_mfma_f32_16x16x32_bf16 v[108:111], v[168:171], v[200:203], v[108:111]\n\t" "ds_read_b64_tr_b16 v[162:163], v4 offset:49664\n\t" "s_waitcnt lgkmcnt(4)\n\t"
    "v_mfma_f32_16x16x32_bf16 v[112:115], v[196:199], v[172:175], v[112:115]\n\t" "ds_read_b64_tr_b16 v[164:165], v5 offset:49152\n\t" "v_add_u32 v0, 0x30000, v0\n\t"
    "v_mfma_f32_16x16x32_bf16 v[116:119], v[200:203], v[172:175], v[116:119]\n\t" "ds_read_b64_tr_b16 v[166:167], v5 offset:49664\n\t" "v_add_u32 v11, 0x3000, v11\n\t"
    "v_mfma_f32_16x16x32_bf16 v[148:151], v[232:235], v[172:175], v[148:151]\n\t" "ds_read_b128 v[228:231], v1 offset:12288\n\t" "global_load_dwordx4 a[56:59], v11, %[sm] nt\n\t"
    "v_mfma_f32_16x16x32_bf16 v[120:123], v[196:199], v[176:179], v[120:123]\n\t" "ds_read_b64_tr_b16 v[168:169], v6 offset:49152\n\t" "v_add_u32 v11, 0x3000, v11\n\t"
    "v_mfma_f32_16x16x32_bf16 v[124:127], v[200:203], v[176:179], v[124:127]\n\t" "ds_read_b64_tr_b16 v[170:171], v6 offset:49664\n\t" "v_sub_f32 v24, v236, v23\n\t"
    "v_mfma_f32_16x16x32_bf16 v[128:131], v[196:199], v[180:183], v[128:131]\n\t" "ds_read_b64_tr_b16 v[172:173], v3 offset:51200\n\t" "v_cndmask_b32 v23, v24, v23, s[46:47]\n\t"
    "v_mfma_f32_16x16x32_bf16 v[132:135], v[200:203], v[180:183], v[132:135]\n\t" "ds_read_b64_tr_b16 v[174:175], v3 offset:51712\n\t" "global_load_dwordx4 a[60:63], v11, %[sm] nt\n\t"
    "v_mfma_f32_16x16x32_bf16 v[136:139], v[196:199], v[184:187], v[136:139]\n\t" "ds_read_b64_tr_b16 v[176:177], v4 offset:51200\n\t" "v_cvt_pk_bf16_f32 v23, -v23, -v23\n\t"
    "v_mfma_f32_16x16x32_bf16 v[140:143], v[200:203], v[184:187], v[140:143]\n\t" "ds_read_b64_tr_b16 v[178:179], v4 offset:51712\n\t" "s_waitcnt lgkmcnt(4)\n\t"
    "v_mfma_f32_16x16x32_bf16 v[48:51], v[156:159], v[188:191], v[48:51]\n\t" "ds_read_b64_tr_b16 v[180:181], v5 offset:51200\n\t" "s_add_u32 m0, %[sl], 96256\n\t"
    "v_mfma_f32_16x16x32_bf16 v[52:55], v[156:159], v[192:195], v[52:55]\n\t" "ds_read_b64_tr_b16 v[182:183], v5 offset:51712\n\t" "v_cndmask_b32 v246, 0, v23, s[48:49]\n\t"
    "v_mfma_f32_16x16x32_bf16 v[152:155], v[228:231], v[156:159], v[152:155]\n\t" "global_load_lds_dwordx4 v0, %[sb] offset:-2048\n\t" "v_and_b32 v23, 0xffff0000, v239\n\t"
    "v_mfma_f32_16x16x32_bf16 v[56:59], v[160:163], v[188:191], v[56:59]\n\t" "ds_read_b64_tr_b16 v[184:185], v6 offset:51200\n\t" "v_sub_f32 v24, v239, v23\n\t"
    "v_mfma_f32_4x4x4_16b_bf16 v[204:207], v[224:225], a[104:105], 0\n\t" "v_mfma_f32_4x4x4_16b_bf16 v[208:211], v[226:227], a[106:107], 0\n\t"
    "v_mfma_f32_4x4x4_16b_bf16 v[212:215], v[224:225], a[108:109], 0\n\t" "v_mfma_f32_4x4x4_16b_bf16 v[216:219], v[226:227], a[110:111], 0\n\t" "v_cndmask_b32 v23, v24, v23, s[46:47]\n\t"
    "v_mfma_f32_16x16x32_bf16 v[60:63], v[160:163], v[192:195], v[60:63]\n\t" "ds_read_b64_tr_b16 v[186:187], v6 offset:51712\n\t" "v_cvt_pk_bf16_f32 v23, v23, v23\n\t"
    "v_mfma_f32_16x16x32_bf16 v[64:67], v[164:167], v[188:191], v[64:67]\n\t" "ds_read_b64_tr_b16 v[156:157], v3 offset:53248\n\t" "v_cndmask_b32 v247, 0, v23, s[48:49]\n\t"
    "v_mfma_f32_16x16x32_bf16 v[68:71], v[164:167], v[192:195], v[68:71]\n\t" "ds_read_b64_tr_b16 v[158:159], v3 offset:53760\n\t" "v_cvt_pk_bf16_f32 v196, v204, v205\n\t"
    "v_mfma_f32_16x16x32_bf16 v[72:75], v[168:171], v[188:191], v[72:75]\n\t" "ds_read_b64_tr_b16 v[160:161], v4 offset:53248\n\t" "v_cvt_pk_bf16_f32 v197, v206, v207\n\t"
    "v_mfma_f32_16x16x32_bf16 v[76:79], v[168:171], v[192:195], v[76:79]\n\t" "ds_read_b64_tr_b16 v[162:163], v4 offset:53760\n\t" "s_waitcnt lgkmcnt(4)\n\t"
    "v_mfma_f32_16x16x32_bf16 v[80:83], v[172:175], v[188:191], v[80:83]\n\t" "ds_read_b64_tr_b16 v[164:165], v5 offset:53248\n\t" "v_cvt_pk_bf16_f32 v198, v208, v209\n\t"
    "v_mfma_f32_16x16x32_bf16 v[84:87], v[172:175], v[192:195], v[84:87]\n\t" "ds_read_b64_tr_b16 v[166:167], v5 offset:53760\n\t" "v_cvt_pk_bf16_f32 v199, v210, v211\n\t"
    "v_mfma_f32_16x16x32_bf16 v[144:147], v[172:175], v[228:231], v[144:147]\n\t" "ds_read_b128 v[220:223], v2 offset:3584\n\t" "global_load_lds_dwordx4 v0, %[sb] offset:0\n\t"
    "v_mfma_f32_16x16x32_bf16 v[88:91], v[176:179], v[188:191], v[88:91]\n\t" "ds_read_b64_tr_b16 v[168:169], v6 offset:53248\n\t" "v_cvt_pk_bf16_f32 v200, v212, v213\n\t"
    "v_mfma_f32_16x16x32_bf16 v[92:95], v[176:179], v[192:195], v[92:95]\n\t" "ds_read_b64_tr_b16 v[170:171], v6 offset:53760\n\t" "v_cvt_pk_bf16_f32 v201, v214, v215\n\t"
    "v_mfma_f32_16x16x32_bf16 v[96:99], v[180:183], v[188:191], v[96:99]\n\t" "ds_read_b64_tr_b16 v[172:173], v7 offset:0\n\t" "v_and_b32 v23, 0xffff0000, v237\n\t"
    "v_mfma_f32_16x16x32_bf16 v[100:103], v[180:183], v[192:195], v[100:103]\n\t" "ds_read_b64_tr_b16 v[174:175], v7 offset:512\n\t" "v_cvt_pk_bf16_f32 v202, v216, v217\n\t"
    "v_mfma_f32_16x16x32_bf16 v[104:107], v[184:187], v[188:191], v[104:107]\n\t" "ds_read_b64_tr_b16 v[176:177], v8 offset:0\n\t" "v_cvt_pk_bf16_f32 v203, v218, v219\n\t"
    "v_mfma_f32_16x16x32_bf16 v[108:111], v[184:187], v[192:195], v[108:111]\n\t" "ds_read_b64_tr_b16 v[178:179], v8 offset:512\n\t" "s_waitcnt lgkmcnt(4)\n\t"
    "v_mfma_f32_16x16x32_bf16 v[112:115], v[188:191], v[156:159], v[112:115]\n\t" "ds_read_b64_tr_b16 v[180:181], v9 offset:0\n\t" "v_sub_f32 v24, v237, v23\n\t"
    "v_mfma_f32_16x16x32_bf16 v[116:119], v[192:195], v[156:159], v[116:119]\n\t" "ds_read_b64_tr_b16 v[182:183], v9 offset:512\n\t" "v_cndmask_b32 v23, v24, v23, s[46:47]\n\t"
    "v_mfma_f32_16x16x32_bf16 v[148:151], v[228:231], v[156:159], v[148:151]\n\t" "ds_read_b128 v[232:235], v1 offset:13312\n\t" "global_load_lds_dwordx4 v0, %[sb] offset:2048\n\t"
    "v_mfma_f32_16x16x32_bf16 v[120:123], v[188:191], v[160:163], v[120:123]\n\t" "ds_read_b64_tr_b16 v[184:185], v10 offset:0\n\t" "v_add_u32 v0, 0x30000, v0\n\t"
    "v_mfma_f32_16x16x32_bf16 v[124:127], v[192:195], v[160:163], v[124:127]\n\t" "ds_read_b64_tr_b16 v[186:187], v10 offset:512\n\t" "v_cvt_pk_bf16_f32 v23, -v23, -v23\n\t"
    "v_mfma_f32_16x16x32_bf16 v[128:131], v[188:191], v[164:167], v[128:131]\n\t" "ds_read_b64_tr_b16 v[156:157], v7 offset:2048\n\t" "v_cndmask_b32 v248, 0, v23, s[48:49]\n\t"
    "v_mfma_f32_16x16x32_bf16 v[132:135], v[192:195], v[164:167], v[132:135]\n\t" "ds_read_b64_tr_b16 v[158:159], v7 offset:2560\n\t" "s_mov_b64 exec, 0xffff\n\t"
    "global_load_dword a64, v[14:15], off\n\t" "s_mov_b64 exec, -1\n\t" "v_mfma_f32_16x16x32_bf16 v[136:139], v[188:191], v[168:171], v[136:139]\n\t" "ds_read_b64_tr_b16 v[160:161], v8 offset:2048\n\t"
    "v_and_b32 v23, 0xffff0000, v240\n\t" "v_mfma_f32_16x16x32_bf16 v[140:143], v[192:195], v[168:171], v[140:143]\n\t" "ds_read_b64_tr_b16 v[162:163], v8 offset:2560\n\t" "s_waitcnt lgkmcnt(4)\n\t"
    "v_mfma_f32_16x16x32_bf16 v[48:51], v[172:175], v[196:199], v[48:51]\n\t" "ds_read_b64_tr_b16 v[164:165], v9 offset:2048\n\t" "s_add_u32 m0, %[sl], 108544\n\t"
    "v_mfma_f32_16x16x32_bf16 v[52:55], v[172:175], v[200:203], v[52:55]\n\t" "ds_read_b64_tr_b16 v[166:167], v9 offset:2560\n\t" "v_sub_f32 v24, v240, v23\n\t"
    "v_mfma_f32_16x16x32_bf16 v[152:155], v[232:235], v[172:175], v[152:155]\n\t" "global_load_lds_dwordx4 v0, %[sb] offset:-2048\n\t" "v_cndmask_b32 v23, v24, v23, s[46:47]\n\t"
    "v_mfma_f32_16x16x32_bf16 v[56:59], v[176:179], v[196:199], v[56:59]\n\t" "ds_read_b64_tr_b16 v[168:169], v10 offset:2048\n\t" "v_cvt_pk_bf16_f32 v23, v23, v23\n\t"
    "v_mfma_f32_4x4x4_16b_bf16 v[204:207], v[220:221], a[112:113], 0\n\t" "v_mfma_f32_4x4x4_16b_bf16 v[208:211], v[222:223], a[114:115], 0\n\t"
    "v_mfma_f32_4x4x4_16b_bf16 v[212:215], v[220:221], a[116:117], 0\n\t" "v_mfma_f32_4x4x4_16b_bf16 v[216:219], v[222:223], a[118:119], 0\n\t" "v_cndmask_b32 v249, 0, v23, s[48:49]\n\t"
    "v_mfma_f32_16x16x32_bf16 v[60:63], v[176:179], v[200:203], v[60:63]\n\t" "ds_read_b64_tr_b16 v[170:171], v10 offset:2560\n\t" "v_and_b32 v23, 0xffff0000, v238\n\t"
    "v_mfma_f32_16x16x32_bf16 v[64:67], v[180:183], v[196:199], v[64:67]\n\t" "ds_read_b64_tr_b16 v[172:173], v7 offset:4096\n\t" "v_sub_f32 v24, v238, v23\n\t"
    "v_mfma_f32_16x16x32_bf16 v[68:71], v[180:183], v[200:203], v[68:71]\n\t" "ds_read_b64_tr_b16 v[174:175], v7 offset:4608\n\t" "v_cvt_pk_bf16_f32 v188, v204, v205\n\t"
    "v_mfma_f32_16x16x32_bf16 v[72:75], v[184:187], v[196:199], v[72:75]\n\t" "ds_read_b64_tr_b16 v[176:177], v8 offset:4096\n\t" "v_cvt_pk_bf16_f32 v189, v206, v207\n\t"
    "v_mfma_f32_16x16x32_bf16 v[76:79], v[184:187], v[200:203], v[76:79]\n\t" "ds_read_b64_tr_b16 v[178:179], v8 offset:4608\n\t" "s_waitcnt lgkmcnt(4)\n\t"
    "v_mfma_f32_16x16x32_bf16 v[80:83], v[156:159], v[196:199], v[80:83]\n\t" "ds_read_b64_tr_b16 v[180:181], v9 offset:4096\n\t" "v_cvt_pk_bf16_f32 v190, v208, v209\n\t"
    "v_mfma_f32_16x16x32_bf16 v[84:87], v[156:159], v[200:203], v[84:87]\n\t" "ds_read_b64_tr_b16 v[182:183], v9 offset:4608\n\t" "v_cvt_pk_bf16_f32 v191, v210, v211\n\t"
    "v_mfma_f32_16x16x32_bf16 v[144:147], v[156:159], v[232:235], v[144:147]\n\t" "ds_read_b128 v[224:227], v2 offset:3840\n\t" "global_load_lds_dwordx4 v0, %[sb] offset:0\n\t"
    "v_mfma_f32_16x16x32_bf16 v[88:91], v[160:163], v[196:199], v[88:91]\n\t" "ds_read_b64_tr_b16 v[184:185], v10 offset:4096\n\t" "v_cvt_pk_bf16_f32 v192, v212, v213\n\t"
    "v_mfma_f32_16x16x32_bf16 v[92:95], v[160:163], v[200:203], v[92:95]\n\t" "ds_read_b64_tr_b16 v[186:187], v10 offset:4608\n\t" "v_cvt_pk_bf16_f32 v193, v214, v215\n\t"
    "v_mfma_f32_16x16x32_bf16 v[96:99], v[164:167], v[196:199], v[96:99]\n\t" "s_waitcnt vmcnt(18)\n\t" "s_barrier\n\t" "ds_read_b64_tr_b16 v[156:157], v3 offset:0\n\t"
    "v_mfma_f32_16x16x32_bf16 v[100:103], v[164:167], v[200:203], v[100:103]\n\t" "ds_read_b64_tr_b16 v[158:159], v3 offset:512\n\t" "v_cvt_pk_bf16_f32 v194, v216, v217\n\t"
    "v_mfma_f32_16x16x32_bf16 v[104:107], v[168:171], v[196:199], v[104:107]\n\t" "ds_read_b64_tr_b16 v[160:161], v4 offset:0\n\t" "v_cvt_pk_bf16_f32 v195, v218, v219\n\t"
    "v_mfma_f32_16x16x32_bf16 v[108:111], v[168:171], v[200:203], v[108:111]\n\t" "ds_read_b64_tr_b16 v[162:163], v4 offset:512\n\t" "s_waitcnt lgkmcnt(4)\n\t"
    "v_mfma_f32_16x16x32_bf16 v[112:115], v[196:199], v[172:175], v[112:115]\n\t" "ds_read_b64_tr_b16 v[164:165], v5 offset:0\n\t" "v_cndmask_b32 v23, v24, v23, s[46:47]\n\t"
    "v_mfma_f32_16x16x32_bf16 v[116:119], v[200:203], v[172:175], v[116:119]\n\t" "ds_read_b64_tr_b16 v[166:167], v5 offset:512\n\t" "v_cvt_pk_bf16_f32 v23, -v23, -v23\n\t"
    "v_mfma_f32_16x16x32_bf16 v[148:151], v[232:235], v[172:175], v[148:151]\n\t" "ds_read_b128 v[228:231], v1 offset:14336\n\t" "global_load_lds_dwordx4 v0, %[sb] offset:2048\n\t"
    "v_mfma_f32_16x16x32_bf16 v[120:123], v[196:199], v[176:179], v[120:123]\n\t" "ds_read_b64_tr_b16 v[168:169], v6 offset:0\n\t" "s_cmp_eq_u32 %[rot], 3\n\t" "s_cselect_b32 s42, s43, 0x30000\n\t"
    "v_add_u32 v0, s42, v0\n\t" "v_mfma_f32_16x16x32_bf16 v[124:127], v[200:203], v[176:179], v[124:127]\n\t" "ds_read_b64_tr_b16 v[170:171], v6 offset:512\n\t" "v_cndmask_b32 v250, 0, v23, s[48:49]\n\t"
    "v_mfma_f32_16x16x32_bf16 v[128:131], v[196:199], v[180:183], v[128:131]\n\t" "ds_read_b64_tr_b16 v[172:173], v3 offset:2048\n\t" "v_and_b32 v23, 0xffff0000, v241\n\t"
    "v_mfma_f32_16x16x32_bf16 v[132:135], v[200:203], v[180:183], v[132:135]\n\t" "ds_read_b64_tr_b16 v[174:175], v3 offset:2560\n\t" "v_sub_f32 v24, v241, v23\n\t"
    "v_mfma_f32_16x16x32_bf16 v[136:139], v[196:199], v[184:187], v[136:139]\n\t" "ds_read_b64_tr_b16 v[176:177], v4 offset:2048\n\t" "v_cndmask_b32 v23, v24, v23, s[46:47]\n\t"
    "v_mfma_f32_16x16x32_bf16 v[140:143], v[200:203], v[184:187], v[140:143]\n\t" "ds_read_b64_tr_b16 v[178:179], v4 offset:2560\n\t" "s_waitcnt lgkmcnt(4)\n\t"
    "v_mfma_f32_16x16x32_bf16 v[48:51], v[156:159], v[188:191], v[48:51]\n\t" "ds_read_b64_tr_b16 v[180:181], v5 offset:2048\n\t" "s_add_u32 m0, %[sl], 120832\n\t"
    "v_mfma_f32_16x16x32_bf16 v[52:55], v[156:159], v[192:195], v[52:55]\n\t" "ds_read_b64_tr_b16 v[182:183], v5 offset:2560\n\t" "v_cvt_pk_bf16_f32 v23, v23, v23\n\t"
    "v_mfma_f32_16x16x32_bf16 v[152:155], v[228:231], v[156:159], v[152:155]\n\t" "global_load_lds_dwordx4 v0, %[sb] offset:-2048\n\t" "v_cndmask_b32 v251, 0, v23, s[48:49]\n\t"
    "v_mfma_f32_16x16x32_bf16 v[56:59], v[160:163], v[188:191], v[56:59]\n\t" "ds_read_b64_tr_b16 v[184:185], v6 offset:2048\n\t" "v_mul_f32 v240, 0x3e8293ee, v20\n\t"
    "v_mfma_f32_4x4x4_16b_bf16 v[204:207], v[224:225], a[120:121], 0\n\t" "v_mfma_f32_4x4x4_16b_bf16 v[208:211], v[226:227], a[122:123], 0\n\t"
    "v_mfma_f32_4x4x4_16b_bf16 v[212:215], v[224:225], a[124:125], 0\n\t" "v_mfma_f32_4x4x4_16b_bf16 v[216:219], v[226:227], a[126:127], 0\n\t" "v_mul_f32 v241, 0x3e8293ee, v21\n\t"
    "v_mfma_f32_16x16x32_bf16 v[60:63], v[160:163], v[192:195], v[60:63]\n\t" "ds_read_b64_tr_b16 v[186:187], v6 offset:2560\n\t" "v_mfma_f32_16x16x32_bf16 v[64:67], v[164:167], v[188:191], v[64:67]\n\t"
    "ds_read_b64_tr_b16 v[156:157], v3 offset:4096\n\t" "v_mfma_f32_16x16x32_bf16 v[68:71], v[164:167], v[192:195], v[68:71]\n\t" "ds_read_b64_tr_b16 v[158:159], v3 offset:4608\n\t"
    "v_cvt_pk_bf16_f32 v196, v204, v205\n\t" "v_mfma_f32_16x16x32_bf16 v[72:75], v[168:171], v[188:191], v[72:75]\n\t" "ds_read_b64_tr_b16 v[160:161], v4 offset:4096\n\t"
    "v_cvt_pk_bf16_f32 v197, v206, v207\n\t" "v_mfma_f32_16x16x32_bf16 v[76:79], v[168:171], v[192:195], v[76:79]\n\t" "ds_read_b64_tr_b16 v[162:163], v4 offset:4608\n\t" "s_waitcnt lgkmcnt(4)\n\t"
    "v_mfma_f32_16x16x32_bf16 v[80:83], v[172:175], v[188:191], v[80:83]\n\t" "ds_read_b64_tr_b16 v[164:165], v5 offset:4096\n\t" "v_cvt_pk_bf16_f32 v198, v208, v209\n\t"
    "v_mfma_f32_16x16x32_bf16 v[84:87], v[172:175], v[192:195], v[84:87]\n\t" "ds_read_b64_tr_b16 v[166:167], v5 offset:4608\n\t" "v_cvt_pk_bf16_f32 v199, v210, v211\n\t"
    "v_mfma_f32_16x16x32_bf16 v[144:147], v[172:175], v[228:231], v[144:147]\n\t" "ds_read_b128 v[220:223], v2 offset:4096\n\t" "global_load_lds_dwordx4 v0, %[sb] offset:0\n\t"
    "v_mfma_f32_16x16x32_bf16 v[88:91], v[176:179], v[188:191], v[88:91]\n\t" "ds_read_b64_tr_b16 v[168:169], v6 offset:4096\n\t" "v_cvt_pk_bf16_f32 v200, v212, v213\n\t"
    "v_mfma_f32_16x16x32_bf16 v[92:95], v[176:179], v[192:195], v[92:95]\n\t" "ds_read_b64_tr_b16 v[170:171], v6 offset:4608\n\t" "v_cvt_pk_bf16_f32 v201, v214, v215\n\t"
    "v_mfma_f32_16x16x32_bf16 v[96:99], v[180:183], v[188:191], v[96:99]\n\t" "ds_read_b64_tr_b16 v[172:173], v3 offset:12288\n\t"
    "v_mfma_f32_16x16x32_bf16 v[100:103], v[180:183], v[192:195], v[100:103]\n\t" "ds_read_b64_tr_b16 v[174:175], v3 offset:12800\n\t" "v_cvt_pk_bf16_f32 v202, v216, v217\n\t"
    "v_mfma_f32_16x16x32_bf16 v[104:107], v[184:187], v[188:191], v[104:107]\n\t" "ds_read_b64_tr_b16 v[176:177], v4 offset:12288\n\t" "v_cvt_pk_bf16_f32 v203, v218, v219\n\t"
    "v_mfma_f32_16x16x32_bf16 v[108:111], v[184:187], v[192:195], v[108:111]\n\t" "ds_read_b64_tr_b16 v[178:179], v4 offset:12800\n\t" "s_waitcnt lgkmcnt(4)\n\t"
    "v_mfma_f32_16x16x32_bf16 v[112:115], v[188:191], v[156:159], v[112:115]\n\t" "ds_read_b64_tr_b16 v[180:181], v5 offset:12288\n\t"
    "v_mfma_f32_16x16x32_bf16 v[116:119], v[192:195], v[156:159], v[116:119]\n\t" "ds_read_b64_tr_b16 v[182:183], v5 offset:12800\n\t"
    "v_mfma_f32_16x16x32_bf16 v[148:151], v[228:231], v[156:159], v[148:151]\n\t" "ds_read_b128 v[232:235], v1 offset:15360\n\t" "global_load_lds_dwordx4 v0, %[sb] offset:2048\n\t"
    "v_mfma_f32_16x16x32_bf16 v[120:123], v[188:191], v[160:163], v[120:123]\n\t" "ds_read_b64_tr_b16 v[184:185], v6 offset:12288\n\t" "v_add_u32 v0, 0x30000, v0\n\t"
    "v_mfma_f32_16x16x32_bf16 v[124:127], v[192:195], v[160:163], v[124:127]\n\t" "ds_read_b64_tr_b16 v[186:187], v6 offset:12800\n\t"
    "v_mfma_f32_16x16x32_bf16 v[128:131], v[188:191], v[164:167], v[128:131]\n\t" "ds_read_b64_tr_b16 v[156:157], v3 offset:14336\n\t"
    "v_mfma_f32_16x16x32_bf16 v[132:135], v[192:195], v[164:167], v[132:135]\n\t" "ds_read_b64_tr_b16 v[158:159], v3 offset:14848\n\t"
    "v_mfma_f32_16x16x32_bf16 v[136:139], v[188:191], v[168:171], v[136:139]\n\t" "ds_read_b64_tr_b16 v[160:161], v4 offset:14336\n\t"
    "v_mfma_f32_16x16x32_bf16 v[140:143], v[192:195], v[168:171], v[140:143]\n\t" "ds_read_b64_tr_b16 v[162:163], v4 offset:14848\n\t" "s_waitcnt lgkmcnt(4)\n\t"
    "v_mfma_f32_16x16x32_bf16 v[48:51], v[172:175], v[196:199], v[48:51]\n\t" "ds_read_b64_tr_b16 v[164:165], v5 offset:14336\n\t" "s_add_u32 m0, %[sl], 71680\n\t"
    "v_mfma_f32_16x16x32_bf16 v[52:55], v[172:175], v[200:203], v[52:55]\n\t" "ds_read_b64_tr_b16 v[166:167], v5 offset:14848\n\t"
    "v_mfma_f32_16x16x32_bf16 v[152:155], v[232:235], v[172:175], v[152:155]\n\t" "global_load_lds_dwordx4 v0, %[sb] offset:-2048\n\t"
    "v_mfma_f32_16x16x32_bf16 v[56:59], v[176:179], v[196:199], v[56:59]\n\t" "ds_read_b64_tr_b16 v[168:169], v6 offset:14336\n\t" "v_mfma_f32_4x4x4_16b_bf16 v[204:207], v[220:221], a[128:129], 0\n\t"
    "v_mfma_f32_4x4x4_16b_bf16 v[208:211], v[222:223], a[130:131], 0\n\t" "v_mfma_f32_4x4x4_16b_bf16 v[212:215], v[220:221], a[132:133], 0\n\t"
    "v_mfma_f32_4x4x4_16b_bf16 v[216:219], v[222:223], a[134:135], 0\n\t" "v_mfma_f32_16x16x32_bf16 v[60:63], v[176:179], v[200:203], v[60:63]\n\t" "ds_read_b64_tr_b16 v[170:171], v6 offset:14848\n\t"
    "v_mfma_f32_16x16x32_bf16 v[64:67], v[180:183], v[196:199], v[64:67]\n\t" "ds_read_b64_tr_b16 v[172:173], v3 offset:16384\n\t" "v_mfma_f32_16x16x32_bf16 v[68:71], v[180:183], v[200:203], v[68:71]\n\t"
    "ds_read_b64_tr_b16 v[174:175], v3 offset:16896\n\t" "v_cvt_pk_bf16_f32 v188, v204, v205\n\t" "v_mfma_f32_16x16x32_bf16 v[72:75], v[184:187], v[196:199], v[72:75]\n\t"
    "ds_read_b64_tr_b16 v[176:177], v4 offset:16384\n\t" "v_cvt_pk_bf16_f32 v189, v206, v207\n\t" "v_mfma_f32_16x16x32_bf16 v[76:79], v[184:187], v[200:203], v[76:79]\n\t"
    "ds_read_b64_tr_b16 v[178:179], v4 offset:16896\n\t" "s_waitcnt lgkmcnt(4)\n\t" "v_mfma_f32_16x16x32_bf16 v[80:83], v[156:159], v[196:199], v[80:83]\n\t"
    "ds_read_b64_tr_b16 v[180:181], v5 offset:16384\n\t" "v_cvt_pk_bf16_f32 v190, v208, v209\n\t" "v_mfma_f32_16x16x32_bf16 v[84:87], v[156:159], v[200:203], v[84:87]\n\t"
    "ds_read_b64_tr_b16 v[182:183], v5 offset:16896\n\t" "v_cvt_pk_bf16_f32 v191, v210, v211\n\t" "v_mfma_f32_16x16x32_bf16 v[144:147], v[156:159], v[232:235], v[144:147]\n\t"
    "ds_read_b128 v[224:227], v2 offset:4352\n\t" "global_load_lds_dwordx4 v0, %[sb] offset:0\n\t" "v_mfma_f32_16x16x32_bf16 v[88:91], v[160:163], v[196:199], v[88:91]\n\t"
    "ds_read_b64_tr_b16 v[184:185], v6 offset:16384\n\t" "v_cvt_pk_bf16_f32 v192, v212, v213\n\t" "v_mfma_f32_16x16x32_bf16 v[92:95], v[160:163], v[200:203], v[92:95]\n\t"
    "ds_read_b64_tr_b16 v[186:187], v6 offset:16896\n\t" "v_cvt_pk_bf16_f32 v193, v214, v215\n\t" "v_mfma_f32_16x16x32_bf16 v[96:99], v[164:167], v[196:199], v[96:99]\n\t" "s_waitcnt vmcnt(14)\n\t"
    "s_barrier\n\t" "ds_read_b64_tr_b16 v[156:157], v3 offset:24576\n\t" "v_mfma_f32_16x16x32_bf16 v[100:103], v[164:167], v[200:203], v[100:103]\n\t" "ds_read_b64_tr_b16 v[158:159], v3 offset:25088\n\t"
    "v_cvt_pk_bf16_f32 v194, v216, v217\n\t" "v_mfma_f32_16x16x32_bf16 v[104:107], v[168:171], v[196:199], v[104:107]\n\t" "ds_read_b64_tr_b16 v[160:161], v4 offset:24576\n\t"
    "v_cvt_pk_bf16_f32 v195, v218, v219\n\t" "v_mfma_f32_16x16x32_bf16 v[108:111], v[168:171], v[200:203], v[108:111]\n\t" "ds_read_b64_tr_b16 v[162:163], v4 offset:25088\n\t" "s_waitcnt lgkmcnt(4)\n\t"
    "v_mfma_f32_16x16x32_bf16 v[112:115], v[196:199], v[172:175], v[112:115]\n\t" "ds_read_b64_tr_b16 v[164:165], v5 offset:24576\n\t"
    "v_mfma_f32_16x16x32_bf16 v[116:119], v[200:203], v[172:175], v[116:119]\n\t" "ds_read_b64_tr_b16 v[166:167], v5 offset:25088\n\t"
    "v_mfma_f32_16x16x32_bf16 v[148:151], v[232:235], v[172:175], v[148:151]\n\t" "ds_read_b128 v[228:231], v1 offset:16384\n\t" "global_load_lds_dwordx4 v0, %[sb] offset:2048\n\t"
    "v_mfma_f32_16x16x32_bf16 v[120:123], v[196:199], v[176:179], v[120:123]\n\t" "ds_read_b64_tr_b16 v[168:169], v6 offset:24576\n\t" "v_add_u32 v0, 0x30000, v0\n\t"
    "v_mfma_f32_16x16x32_bf16 v[124:127], v[200:203], v[176:179], v[124:127]\n\t" "ds_read_b64_tr_b16 v[170:171], v6 offset:25088\n\t"
    "v_mfma_f32_16x16x32_bf16 v[128:131], v[196:199], v[180:183], v[128:131]\n\t" "ds_read_b64_tr_b16 v[172:173], v3 offset:26624\n\t"
    "v_mfma_f32_16x16x32_bf16 v[132:135], v[200:203], v[180:183], v[132:135]\n\t" "ds_read_b64_tr_b16 v[174:175], v3 offset:27136\n\t"
    "v_mfma_f32_16x16x32_bf16 v[136:139], v[196:199], v[184:187], v[136:139]\n\t" "ds_read_b64_tr_b16 v[176:177], v4 offset:26624\n\t"
    "v_mfma_f32_16x16x32_bf16 v[140:143], v[200:203], v[184:187], v[140:143]\n\t" "ds_read_b64_tr_b16 v[178:179], v4 offset:27136\n\t" "s_waitcnt lgkmcnt(4)\n\t"
    "v_mfma_f32_16x16x32_bf16 v[48:51], v[156:159], v[188:191], v[48:51]\n\t" "ds_read_b64_tr_b16 v[180:181], v5 offset:26624\n\t" "s_add_u32 m0, %[sl], 83968\n\t"
    "v_mfma_f32_16x16x32_bf16 v[52:55], v[156:159], v[192:195], v[52:55]\n\t" "ds_read_b64_tr_b16 v[182:183], v5 offset:27136\n\t"
    "v_mfma_f32_16x16x32_bf16 v[152:155], v[228:231], v[156:159], v[152:155]\n\t" "global_load_lds_dwordx4 v0, %[sb] offset:-2048\n\t"
    "v_mfma_f32_16x16x32_bf16 v[56:59], v[160:163], v[188:191], v[56:59]\n\t" "ds_read_b64_tr_b16 v[184:185], v6 offset:26624\n\t" "v_mfma_f32_4x4x4_16b_bf16 v[204:207], v[224:225], a[136:137], 0\n\t"
    "v_mfma_f32_4x4x4_16b_bf16 v[208:211], v[226:227], a[138:139], 0\n\t" "v_mfma_f32_4x4x4_16b_bf16 v[212:215], v[224:225], a[140:141], 0\n\t"
    "v_mfma_f32_4x4x4_16b_bf16 v[216:219], v[226:227], a[142:143], 0\n\t" "v_mfma_f32_16x16x32_bf16 v[60:63], v[160:163], v[192:195], v[60:63]\n\t" "ds_read_b64_tr_b16 v[186:187], v6 offset:27136\n\t"
    "v_mfma_f32_16x16x32_bf16 v[64:67], v[164:167], v[188:191], v[64:67]\n\t" "ds_read_b64_tr_b16 v[156:157], v3 offset:28672\n\t" "v_mfma_f32_16x16x32_bf16 v[68:71], v[164:167], v[192:195], v[68:71]\n\t"
    "ds_read_b64_tr_b16 v[158:159], v3 offset:29184\n\t" "v_cvt_pk_bf16_f32 v196, v204, v205\n\t" "v_mfma_f32_16x16x32_bf16 v[72:75], v[168:171], v[188:191], v[72:75]\n\t"
    "ds_read_b64_tr_b16 v[160:161], v4 offset:28672\n\t" "v_cvt_pk_bf16_f32 v197, v206, v207\n\t" "v_mfma_f32_16x16x32_bf16 v[76:79], v[168:171], v[192:195], v[76:79]\n\t"
    "ds_read_b64_tr_b16 v[162:163], v4 offset:29184\n\t" "s_waitcnt lgkmcnt(4)\n\t" "v_mfma_f32_16x16x32_bf16 v[80:83], v[172:175], v[188:191], v[80:83]\n\t"
    "ds_read_b64_tr_b16 v[164:165], v5 offset:28672\n\t" "v_cvt_pk_bf16_f32 v198, v208, v209\n\t" "v_mfma_f32_16x16x32_bf16 v[84:87], v[172:175], v[192:195], v[84:87]\n\t"
    "ds_read_b64_tr_b16 v[166:167], v5 offset:29184\n\t" "v_cvt_pk_bf16_f32 v199, v210, v211\n\t" "v_mfma_f32_16x16x32_bf16 v[144:147], v[172:175], v[228:231], v[144:147]\n\t"
    "ds_read_b128 v[220:223], v2 offset:4608\n\t" "global_load_lds_dwordx4 v0, %[sb] offset:0\n\t" "v_mfma_f32_16x16x32_bf16 v[88:91], v[176:179], v[188:191], v[88:91]\n\t"
    "ds_read_b64_tr_b16 v[168:169], v6 offset:28672\n\t" "v_cvt_pk_bf16_f32 v200, v212, v213\n\t" "v_mfma_f32_16x16x32_bf16 v[92:95], v[176:179], v[192:195], v[92:95]\n\t"
    "ds_read_b64_tr_b16 v[170:171], v6 offset:29184\n\t" "v_cvt_pk_bf16_f32 v201, v214, v215\n\t" "v_mfma_f32_16x16x32_bf16 v[96:99], v[180:183], v[188:191], v[96:99]\n\t"
    "ds_read_b64_tr_b16 v[172:173], v3 offset:36864\n\t" "v_mfma_f32_16x16x32_bf16 v[100:103], v[180:183], v[192:195], v[100:103]\n\t" "ds_read_b64_tr_b16 v[174:175], v3 offset:37376\n\t"
    "v_cvt_pk_bf16_f32 v202, v216, v217\n\t" "v_mfma_f32_16x16x32_bf16 v[104:107], v[184:187], v[188:191], v[104:107]\n\t" "ds_read_b64_tr_b16 v[176:177], v4 offset:36864\n\t"
    "v_cvt_pk_bf16_f32 v203, v218, v219\n\t" "v_mfma_f32_16x16x32_bf16 v[108:111], v[184:187], v[192:195], v[108:111]\n\t" "ds_read_b64_tr_b16 v[178:179], v4 offset:37376\n\t" "s_waitcnt lgkmcnt(4)\n\t"
    "v_mfma_f32_16x16x32_bf16 v[112:115], v[188:191], v[156:159], v[112:115]\n\t" "ds_read_b64_tr_b16 v[180:181], v5 offset:36864\n\t"
    "v_mfma_f32_16x16x32_bf16 v[116:119], v[192:195], v[156:159], v[116:119]\n\t" "ds_read_b64_tr_b16 v[182:183], v5 offset:37376\n\t"
    "v_mfma_f32_16x16x32_bf16 v[148:151], v[228:231], v[156:159], v[148:151]\n\t" "ds_read_b128 v[232:235], v1 offset:17408\n\t" "global_load_lds_dwordx4 v0, %[sb] offset:2048\n\t"
    "v_mfma_f32_16x16x32_bf16 v[120:123], v[188:191], v[160:163], v[120:123]\n\t" "ds_read_b64_tr_b16 v[184:185], v6 offset:36864\n\t" "v_add_u32 v0, 0x30000, v0\n\t"
    "v_mfma_f32_16x16x32_bf16 v[124:127], v[192:195], v[160:163], v[124:127]\n\t" "ds_read_b64_tr_b16 v[186:187], v6 offset:37376\n\t"
    "v_mfma_f32_16x16x32_bf16 v[128:131], v[188:191], v[164:167], v[128:131]\n\t" "ds_read_b64_tr_b16 v[156:157], v3 offset:38912\n\t"
    "v_mfma_f32_16x16x32_bf16 v[132:135], v[192:195], v[164:167], v[132:135]\n\t" "ds_read_b64_tr_b16 v[158:159], v3 offset:39424\n\t"
    "v_mfma_f32_16x16x32_bf16 v[136:139], v[188:191], v[168:171], v[136:139]\n\t" "ds_read_b64_tr_b16 v[160:161], v4 offset:38912\n\t"
    "v_mfma_f32_16x16x32_bf16 v[140:143], v[192:195], v[168:171], v[140:143]\n\t" "ds_read_b64_tr_b16 v[162:163], v4 offset:39424\n\t" "s_waitcnt lgkmcnt(4)\n\t"
    "v_mfma_f32_16x16x32_bf16 v[48:51], v[172:175], v[196:199], v[48:51]\n\t" "ds_read_b64_tr_b16 v[164:165], v5 offset:38912\n\t" "s_add_u32 m0, %[sl], 22528\n\t"
    "v_mfma_f32_16x16x32_bf16 v[52:55], v[172:175], v[200:203], v[52:55]\n\t" "ds_read_b64_tr_b16 v[166:167], v5 offset:39424\n\t"
    "v_mfma_f32_16x16x32_bf16 v[152:155], v[232:235], v[172:175], v[152:155]\n\t" "global_load_lds_dwordx4 v0, %[sb] offset:-2048\n\t"
    "v_mfma_f32_16x16x32_bf16 v[56:59], v[176:179], v[196:199], v[56:59]\n\t" "ds_read_b64_tr_b16 v[168:169], v6 offset:38912\n\t" "v_mfma_f32_4x4x4_16b_bf16 v[204:207], v[220:221], a[144:145], 0\n\t"
    "v_mfma_f32_4x4x4_16b_bf16 v[208:211], v[222:223], a[146:147], 0\n\t" "v_mfma_f32_4x4x4_16b_bf16 v[212:215], v[220:221], a[148:149], 0\n\t"
    "v_mfma_f32_4x4x4_16b_bf16 v[216:219], v[222:223], a[150:151], 0\n\t" "v_mfma_f32_16x16x32_bf16 v[60:63], v[176:179], v[200:203], v[60:63]\n\t" "ds_read_b64_tr_b16 v[170:171], v6 offset:39424\n\t"
    "v_mfma_f32_16x16x32_bf16 v[64:67], v[180:183], v[196:199], v[64:67]\n\t" "ds_read_b64_tr_b16 v[172:173], v3 offset:40960\n\t" "v_mfma_f32_16x16x32_bf16 v[68:71], v[180:183], v[200:203], v[68:71]\n\t"
    "ds_read_b64_tr_b16 v[174:175], v3 offset:41472\n\t" "v_cvt_pk_bf16_f32 v188, v204, v205\n\t" "v_mfma_f32_16x16x32_bf16 v[72:75], v[184:187], v[196:199], v[72:75]\n\t"
    "ds_read_b64_tr_b16 v[176:177], v4 offset:40960\n\t" "v_cvt_pk_bf16_f32 v189, v206, v207\n\t" "v_mfma_f32_16x16x32_bf16 v[76:79], v[184:187], v[200:203], v[76:79]\n\t"
    "ds_read_b64_tr_b16 v[178:179], v4 offset:41472\n\t" "s_waitcnt lgkmcnt(4)\n\t" "v_mfma_f32_16x16x32_bf16 v[80:83], v[156:159], v[196:199], v[80:83]\n\t"
    "ds_read_b64_tr_b16 v[180:181], v5 offset:40960\n\t" "v_cvt_pk_bf16_f32 v190, v208, v209\n\t" "v_mfma_f32_16x16x32_bf16 v[84:87], v[156:159], v[200:203], v[84:87]\n\t"
    "ds_read_b64_tr_b16 v[182:183], v5 offset:41472\n\t" "v_cvt_pk_bf16_f32 v191, v210, v211\n\t" "v_mfma_f32_16x16x32_bf16 v[144:147], v[156:159], v[232:235], v[144:147]\n\t"
    "ds_read_b128 v[224:227], v2 offset:4864\n\t" "global_load_lds_dwordx4 v0, %[sb] offset:0\n\t" "v_mfma_f32_16x16x32_bf16 v[88:91], v[160:163], v[196:199], v[88:91]\n\t"
    "ds_read_b64_tr_b16 v[184:185], v6 offset:40960\n\t" "v_cvt_pk_bf16_f32 v192, v212, v213\n\t" "v_mfma_f32_16x16x32_bf16 v[92:95], v[160:163], v[200:203], v[92:95]\n\t"
    "ds_read_b64_tr_b16 v[186:187], v6 offset:41472\n\t" "v_cvt_pk_bf16_f32 v193, v214, v215\n\t" "v_mfma_f32_16x16x32_bf16 v[96:99], v[164:167], v[196:199], v[96:99]\n\t" "s_waitcnt vmcnt(11)\n\t"
    "s_barrier\n\t" "ds_read_b64_tr_b16 v[156:157], v7 offset:12288\n\t" "v_mfma_f32_16x16x32_bf16 v[100:103], v[164:167], v[200:203], v[100:103]\n\t" "ds_read_b64_tr_b16 v[158:159], v7 offset:12800\n\t"
    "v_cvt_pk_bf16_f32 v194, v216, v217\n\t" "v_mfma_f32_16x16x32_bf16 v[104:107], v[168:171], v[196:199], v[104:107]\n\t" "ds_read_b64_tr_b16 v[160:161], v8 offset:12288\n\t"
    "v_cvt_pk_bf16_f32 v195, v218, v219\n\t" "v_mfma_f32_16x16x32_bf16 v[108:111], v[168:171], v[200:203], v[108:111]\n\t" "ds_read_b64_tr_b16 v[162:163], v8 offset:12800\n\t" "s_waitcnt lgkmcnt(4)\n\t"
    "v_mfma_f32_16x16x32_bf16 v[112:115], v[196:199], v[172:175], v[112:115]\n\t" "ds_read_b64_tr_b16 v[164:165], v9 offset:12288\n\t"
    "v_mfma_f32_16x16x32_bf16 v[116:119], v[200:203], v[172:175], v[116:119]\n\t" "ds_read_b64_tr_b16 v[166:167], v9 offset:12800\n\t"
    "v_mfma_f32_16x16x32_bf16 v[148:151], v[232:235], v[172:175], v[148:151]\n\t" "ds_read_b128 v[228:231], v1 offset:18432\n\t" "global_load_lds_dwordx4 v0, %[sb] offset:2048\n\t"
    "v_mfma_f32_16x16x32_bf16 v[120:123], v[196:199], v[176:179], v[120:123]\n\t" "ds_read_b64_tr_b16 v[168:169], v10 offset:12288\n\t" "s_cmp_eq_u32 %[rot], 2\n\t" "s_cselect_b32 s42, s43, 0x30000\n\t"
    "v_add_u32 v0, s42, v0\n\t" "v_mfma_f32_16x16x32_bf16 v[124:127], v[200:203], v[176:179], v[124:127]\n\t" "ds_read_b64_tr_b16 v[170:171], v10 offset:12800\n\t"
    "v_mfma_f32_16x16x32_bf16 v[128:131], v[196:199], v[180:183], v[128:131]\n\t" "ds_read_b64_tr_b16 v[172:173], v7 offset:14336\n\t"
    "v_mfma_f32_16x16x32_bf16 v[132:135], v[200:203], v[180:183], v[132:135]\n\t" "ds_read_b64_tr_b16 v[174:175], v7 offset:14848\n\t"
    "v_mfma_f32_16x16x32_bf16 v[136:139], v[196:199], v[184:187], v[136:139]\n\t" "ds_read_b64_tr_b16 v[176:177], v8 offset:14336\n\t"
    "v_mfma_f32_16x16x32_bf16 v[140:143], v[200:203], v[184:187], v[140:143]\n\t" "ds_read_b64_tr_b16 v[178:179], v8 offset:14848\n\t" "s_waitcnt lgkmcnt(4)\n\t"
    "v_mfma_f32_16x16x32_bf16 v[48:51], v[156:159], v[188:191], v[48:51]\n\t" "ds_read_b64_tr_b16 v[180:181], v9 offset:14336\n\t" "s_add_u32 m0, %[sl], 34816\n\t"
    "v_mfma_f32_16x16x32_bf16 v[52:55], v[156:159], v[192:195], v[52:55]\n\t" "ds_read_b64_tr_b16 v[182:183], v9 offset:14848\n\t" "s_mov_b32 s50, 0xaaaaaaaa\n\t"
    "v_mfma_f32_16x16x32_bf16 v[152:155], v[228:231], v[156:159], v[152:155]\n\t" "global_load_lds_dwordx4 v0, %[sb] offset:-2048\n\t" "s_mov_b32 s51, s50\n\t"
    "v_mfma_f32_16x16x32_bf16 v[56:59], v[160:163], v[188:191], v[56:59]\n\t" "ds_read_b64_tr_b16 v[184:185], v10 offset:14336\n\t" "s_mov_b32 s52, 0xfff0ffff\n\t"
    "v_mfma_f32_4x4x4_16b_bf16 v[204:207], v[224:225], a[152:153], 0\n\t" "v_mfma_f32_4x4x4_16b_bf16 v[208:211], v[226:227], a[154:155], 0\n\t"
    "v_mfma_f32_4x4x4_16b_bf16 v[212:215], v[224:225], a[156:157], 0\n\t" "v_mfma_f32_4x4x4_16b_bf16 v[216:219], v[226:227], a[158:159], 0\n\t" "s_mov_b32 s53, 0xf000ff00\n\t"
    "v_mfma_f32_16x16x32_bf16 v[60:63], v[160:163], v[192:195], v[60:63]\n\t" "ds_read_b64_tr_b16 v[186:187], v10 offset:14848\n\t" "s_mov_b32 s54, 0xffc0fffc\n\t"
    "v_mfma_f32_16x16x32_bf16 v[64:67], v[164:167], v[188:191], v[64:67]\n\t" "ds_read_b64_tr_b16 v[156:157], v7 offset:16384\n\t" "s_mov_b32 s55, 0xc000fc00\n\t"
    "v_mfma_f32_16x16x32_bf16 v[68:71], v[164:167], v[192:195], v[68:71]\n\t" "ds_read_b64_tr_b16 v[158:159], v7 offset:16896\n\t" "v_cvt_pk_bf16_f32 v196, v204, v205\n\t"
    "v_mfma_f32_16x16x32_bf16 v[72:75], v[168:171], v[188:191], v[72:75]\n\t" "ds_read_b64_tr_b16 v[160:161], v8 offset:16384\n\t" "v_cvt_pk_bf16_f32 v197, v206, v207\n\t"
    "v_mfma_f32_16x16x32_bf16 v[76:79], v[168:171], v[192:195], v[76:79]\n\t" "ds_read_b64_tr_b16 v[162:163], v8 offset:16896\n\t" "s_waitcnt lgkmcnt(4)\n\t"
    "v_mfma_f32_16x16x32_bf16 v[80:83], v[172:175], v[188:191], v[80:83]\n\t" "ds_read_b64_tr_b16 v[164:165], v9 offset:16384\n\t" "v_cvt_pk_bf16_f32 v198, v208, v209\n\t"
    "v_mfma_f32_16x16x32_bf16 v[84:87], v[172:175], v[192:195], v[84:87]\n\t" "ds_read_b64_tr_b16 v[166:167], v9 offset:16896\n\t" "v_cvt_pk_bf16_f32 v199, v210, v211\n\t"
    "v_mfma_f32_16x16x32_bf16 v[144:147], v[172:175], v[228:231], v[144:147]\n\t" "ds_read_b128 v[220:223], v2 offset:5120\n\t" "global_load_lds_dwordx4 v0, %[sb] offset:0\n\t"
    "v_mfma_f32_16x16x32_bf16 v[88:91], v[176:179], v[188:191], v[88:91]\n\t" "ds_read_b64_tr_b16 v[168:169], v10 offset:16384\n\t" "v_cvt_pk_bf16_f32 v200, v212, v213\n\t"
    "v_mfma_f32_16x16x32_bf16 v[92:95], v[176:179], v[192:195], v[92:95]\n\t" "ds_read_b64_tr_b16 v[170:171], v10 offset:16896\n\t" "v_cvt_pk_bf16_f32 v201, v214, v215\n\t"
    "v_mfma_f32_16x16x32_bf16 v[96:99], v[180:183], v[188:191], v[96:99]\n\t" "ds_read_b64_tr_b16 v[172:173], v7 offset:24576\n\t" "s_cmp_eq_u32 %[rw], 0\n\t" "s_cselect_b64 s[58:59], -1, 0\n\t"
    "v_mfma_f32_16x16x32_bf16 v[100:103], v[180:183], v[192:195], v[100:103]\n\t" "ds_read_b64_tr_b16 v[174:175], v7 offset:25088\n\t" "v_cvt_pk_bf16_f32 v202, v216, v217\n\t"
    "v_mfma_f32_16x16x32_bf16 v[104:107], v[184:187], v[188:191], v[104:107]\n\t" "ds_read_b64_tr_b16 v[176:177], v8 offset:24576\n\t" "v_cvt_pk_bf16_f32 v203, v218, v219\n\t"
    "v_mfma_f32_16x16x32_bf16 v[108:111], v[184:187], v[192:195], v[108:111]\n\t" "ds_read_b64_tr_b16 v[178:179], v8 offset:25088\n\t" "s_waitcnt lgkmcnt(4)\n\t"
    "v_mfma_f32_16x16x32_bf16 v[112:115], v[188:191], v[156:159], v[112:115]\n\t" "ds_read_b64_tr_b16 v[180:181], v9 offset:24576\n\t" "s_andn2_b64 s[56:57], s[58:59], s[52:53]\n\t"
    "v_mfma_f32_16x16x32_bf16 v[116:119], v[192:195], v[156:159], v[116:119]\n\t" "ds_read_b64_tr_b16 v[182:183], v9 offset:25088\n\t" "s_andn2_b64 s[58:59], s[58:59], s[54:55]\n\t"
    "v_mfma_f32_16x16x32_bf16 v[148:151], v[228:231], v[156:159], v[148:151]\n\t" "ds_read_b128 v[232:235], v1 offset:19456\n\t" "global_load_lds_dwordx4 v0, %[sb] offset:2048\n\t"
    "v_mfma_f32_16x16x32_bf16 v[120:123], v[188:191], v[160:163], v[120:123]\n\t" "ds_read_b64_tr_b16 v[184:185], v10 offset:24576\n\t" "v_add_u32 v0, 0x30000, v0\n\t"
    "v_mfma_f32_16x16x32_bf16 v[124:127], v[192:195], v[160:163], v[124:127]\n\t" "ds_read_b64_tr_b16 v[186:187], v10 offset:25088\n\t" "s_mov_b32 s48, 0x00030003\n\t"
    "v_mfma_f32_16x16x32_bf16 v[128:131], v[188:191], v[164:167], v[128:131]\n\t" "ds_read_b64_tr_b16 v[156:157], v7 offset:26624\n\t" "s_mov_b32 s49, s48\n\t"
    "v_mfma_f32_16x16x32_bf16 v[132:135], v[192:195], v[164:167], v[132:135]\n\t" "ds_read_b64_tr_b16 v[158:159], v7 offset:27136\n\t" "v_and_b32 v46, 63, v13\n\t"
    "v_mfma_f32_16x16x32_bf16 v[136:139], v[188:191], v[168:171], v[136:139]\n\t" "ds_read_b64_tr_b16 v[160:161], v8 offset:26624\n\t" "v_and_b32 v47, 15, v13\n\t"
    "v_mfma_f32_16x16x32_bf16 v[140:143], v[192:195], v[168:171], v[140:143]\n\t" "ds_read_b64_tr_b16 v[162:163], v8 offset:27136\n\t" "s_waitcnt lgkmcnt(4)\n\t"
    "v_mfma_f32_16x16x32_bf16 v[48:51], v[172:175], v[196:199], v[48:51]\n\t" "ds_read_b64_tr_b16 v[164:165], v9 offset:26624\n\t" "s_add_u32 m0, %[sl], 47104\n\t"
    "v_mfma_f32_16x16x32_bf16 v[52:55], v[172:175], v[200:203], v[52:55]\n\t" "ds_read_b64_tr_b16 v[166:167], v9 offset:27136\n\t" "v_and_b32 v45, 48, v13\n\t"
    "v_mfma_f32_16x16x32_bf16 v[152:155], v[232:235], v[172:175], v[152:155]\n\t" "global_load_lds_dwordx4 v0, %[sb] offset:-2048\n\t" "v_and_b32 v44, 1, v13\n\t"
    "v_mfma_f32_16x16x32_bf16 v[56:59], v[176:179], v[196:199], v[56:59]\n\t" "ds_read_b64_tr_b16 v[168:169], v10 offset:26624\n\t" "v_lshl_add_u32 v44, v44, 2, v45\n\t"
    "v_mfma_f32_4x4x4_16b_bf16 v[204:207], v[220:221], a[160:161], 0\n\t" "v_mfma_f32_4x4x4_16b_bf16 v[208:211], v[222:223], a[162:163], 0\n\t"
    "v_mfma_f32_4x4x4_16b_bf16 v[212:215], v[220:221], a[164:165], 0\n\t" "v_mfma_f32_4x4x4_16b_bf16 v[216:219], v[222:223], a[166:167], 0\n\t" "v_add_u32 v44, %[w128], v44\n\t"
    "v_mfma_f32_16x16x32_bf16 v[60:63], v[176:179], v[200:203], v[60:63]\n\t" "ds_read_b64_tr_b16 v[170:171], v10 offset:27136\n\t" "v_add_u32 v44, %[l0], v44\n\t"
    "v_mfma_f32_16x16x32_bf16 v[64:67], v[180:183], v[196:199], v[64:67]\n\t" "ds_read_b64_tr_b16 v[172:173], v7 offset:28672\n\t" "v_add_u32 v43, 64, v44\n\t"
    "v_mfma_f32_16x16x32_bf16 v[68:71], v[180:183], v[200:203], v[68:71]\n\t" "ds_read_b64_tr_b16 v[174:175], v7 offset:29184\n\t" "v_cvt_pk_bf16_f32 v188, v204, v205\n\t"
    "v_mfma_f32_16x16x32_bf16 v[72:75], v[184:187], v[196:199], v[72:75]\n\t" "ds_read_b64_tr_b16 v[176:177], v8 offset:28672\n\t" "v_cvt_pk_bf16_f32 v189, v206, v207\n\t"
    "v_mfma_f32_16x16x32_bf16 v[76:79], v[184:187], v[200:203], v[76:79]\n\t" "ds_read_b64_tr_b16 v[178:179], v8 offset:29184\n\t" "s_waitcnt lgkmcnt(4)\n\t"
    "v_mfma_f32_16x16x32_bf16 v[80:83], v[156:159], v[196:199], v[80:83]\n\t" "ds_read_b64_tr_b16 v[180:181], v9 offset:28672\n\t" "v_cvt_pk_bf16_f32 v190, v208, v209\n\t"
    "v_mfma_f32_16x16x32_bf16 v[84:87], v[156:159], v[200:203], v[84:87]\n\t" "ds_read_b64_tr_b16 v[182:183], v9 offset:29184\n\t" "v_cvt_pk_bf16_f32 v191, v210, v211\n\t"
    "v_mfma_f32_16x16x32_bf16 v[144:147], v[156:159], v[232:235], v[144:147]\n\t" "ds_read_b128 v[224:227], v2 offset:5376\n\t" "global_load_lds_dwordx4 v0, %[sb] offset:0\n\t"
    "v_mfma_f32_16x16x32_bf16 v[88:91], v[160:163], v[196:199], v[88:91]\n\t" "ds_read_b64_tr_b16 v[184:185], v10 offset:28672\n\t" "v_cvt_pk_bf16_f32 v192, v212, v213\n\t"
    "v_mfma_f32_16x16x32_bf16 v[92:95], v[160:163], v[200:203], v[92:95]\n\t" "ds_read_b64_tr_b16 v[186:187], v10 offset:29184\n\t" "v_cvt_pk_bf16_f32 v193, v214, v215\n\t"
    "v_mfma_f32_16x16x32_bf16 v[96:99], v[164:167], v[196:199], v[96:99]\n\t" "s_waitcnt vmcnt(11)\n\t" "s_barrier\n\t" "ds_read_b64_tr_b16 v[156:157], v7 offset:36864\n\t"
    "v_mfma_f32_16x16x32_bf16 v[100:103], v[164:167], v[200:203], v[100:103]\n\t" "ds_read_b64_tr_b16 v[158:159], v7 offset:37376\n\t" "v_cvt_pk_bf16_f32 v194, v216, v217\n\t"
    "v_mfma_f32_16x16x32_bf16 v[104:107], v[168:171], v[196:199], v[104:107]\n\t" "ds_read_b64_tr_b16 v[160:161], v8 offset:36864\n\t" "v_cvt_pk_bf16_f32 v195, v218, v219\n\t"
    "v_mfma_f32_16x16x32_bf16 v[108:111], v[168:171], v[200:203], v[108:111]\n\t" "ds_read_b64_tr_b16 v[162:163], v8 offset:37376\n\t" "s_waitcnt lgkmcnt(4)\n\t"
    "v_mfma_f32_16x16x32_bf16 v[112:115], v[196:199], v[172:175], v[112:115]\n\t" "ds_read_b64_tr_b16 v[164:165], v9 offset:36864\n\t" "v_cndmask_b32 v42, v44, v43, s[52:53]\n\t"
    "v_mfma_f32_16x16x32_bf16 v[116:119], v[200:203], v[172:175], v[116:119]\n\t" "ds_read_b64_tr_b16 v[166:167], v9 offset:37376\n\t" "v_cndmask_b32 v43, v44, v43, s[54:55]\n\t"
    "v_mfma_f32_16x16x32_bf16 v[148:151], v[232:235], v[172:175], v[148:151]\n\t" "ds_read_b128 v[228:231], v1 offset:20480\n\t" "global_load_lds_dwordx4 v0, %[sb] offset:2048\n\t"
    "v_mfma_f32_16x16x32_bf16 v[120:123], v[196:199], v[176:179], v[120:123]\n\t" "ds_read_b64_tr_b16 v[168:169], v10 offset:36864\n\t" "v_add_u32 v0, 0x30000, v0\n\t"
    "v_mfma_f32_16x16x32_bf16 v[124:127], v[200:203], v[176:179], v[124:127]\n\t" "ds_read_b64_tr_b16 v[170:171], v10 offset:37376\n\t" "ds_read_b32 v24, v42 offset:17408\n\t"
    "v_mfma_f32_16x16x32_bf16 v[128:131], v[196:199], v[180:183], v[128:131]\n\t" "ds_read_b64_tr_b16 v[172:173], v7 offset:38912\n\t" "ds_read_b32 v25, v43 offset:17416\n\t"
    "v_mfma_f32_16x16x32_bf16 v[132:135], v[200:203], v[180:183], v[132:135]\n\t" "ds_read_b64_tr_b16 v[174:175], v7 offset:39424\n\t" "ds_read_b32 v26, v42 offset:17472\n\t"
    "v_mfma_f32_16x16x32_bf16 v[136:139], v[196:199], v[184:187], v[136:139]\n\t" "ds_read_b64_tr_b16 v[176:177], v8 offset:38912\n\t" "s_waitcnt lgkmcnt(11)\n\t" "ds_read_b32 v27, v43 offset:17480\n\t"
    "v_mfma_f32_16x16x32_bf16 v[140:143], v[200:203], v[184:187], v[140:143]\n\t" "ds_read_b64_tr_b16 v[178:179], v8 offset:39424\n\t" "s_waitcnt lgkmcnt(8)\n\t"
    "v_mfma_f32_16x16x32_bf16 v[48:51], v[156:159], v[188:191], v[48:51]\n\t" "ds_read_b64_tr_b16 v[180:181], v9 offset:38912\n\t" "s_add_u32 m0, %[sl], 59392\n\t"
    "v_mfma_f32_16x16x32_bf16 v[52:55], v[156:159], v[192:195], v[52:55]\n\t" "ds_read_b64_tr_b16 v[182:183], v9 offset:39424\n\t" "v_mov_b32 v40, 0x0c0c0100\n\t"
    "v_mfma_f32_16x16x32_bf16 v[152:155], v[228:231], v[156:159], v[152:155]\n\t" "global_load_lds_dwordx4 v0, %[sb] offset:-2048\n\t" "v_mov_b32 v41, 0x01000c0c\n\t"
    "v_mfma_f32_16x16x32_bf16 v[56:59], v[160:163], v[188:191], v[56:59]\n\t" "ds_read_b64_tr_b16 v[184:185], v10 offset:38912\n\t" "v_cndmask_b32 v40, v40, v41, s[50:51]\n\t"
    "v_mfma_f32_4x4x4_16b_bf16 v[204:207], v[224:225], a[168:169], 0\n\t" "v_mfma_f32_4x4x4_16b_bf16 v[208:211], v[226:227], a[170:171], 0\n\t"
    "v_mfma_f32_4x4x4_16b_bf16 v[212:215], v[224:225], a[172:173], 0\n\t" "v_mfma_f32_4x4x4_16b_bf16 v[216:219], v[226:227], a[174:175], 0\n\t" "v_mov_b32 v39, 0x0c0c0302\n\t"
    "v_mfma_f32_16x16x32_bf16 v[60:63], v[160:163], v[192:195], v[60:63]\n\t" "ds_read_b64_tr_b16 v[186:187], v10 offset:39424\n\t" "v_mov_b32 v41, 0x03020c0c\n\t"
    "v_mfma_f32_16x16x32_bf16 v[64:67], v[164:167], v[188:191], v[64:67]\n\t" "ds_read_b64_tr_b16 v[156:157], v7 offset:40960\n\t" "v_cndmask_b32 v41, v39, v41, s[50:51]\n\t"
    "v_mfma_f32_16x16x32_bf16 v[68:71], v[164:167], v[192:195], v[68:71]\n\t" "ds_read_b64_tr_b16 v[158:159], v7 offset:41472\n\t" "v_cvt_pk_bf16_f32 v196, v204, v205\n\t"
    "v_mfma_f32_16x16x32_bf16 v[72:75], v[168:171], v[188:191], v[72:75]\n\t" "ds_read_b64_tr_b16 v[160:161], v8 offset:40960\n\t" "v_cvt_pk_bf16_f32 v197, v206, v207\n\t"
    "v_mfma_f32_16x16x32_bf16 v[76:79], v[168:171], v[192:195], v[76:79]\n\t" "s_waitcnt lgkmcnt(11)\n\t" "ds_read_b64_tr_b16 v[162:163], v8 offset:41472\n\t" "s_waitcnt lgkmcnt(4)\n\t"
    "v_mfma_f32_16x16x32_bf16 v[80:83], v[172:175], v[188:191], v[80:83]\n\t" "ds_read_b64_tr_b16 v[164:165], v9 offset:40960\n\t" "v_cvt_pk_bf16_f32 v198, v208, v209\n\t"
    "v_mfma_f32_16x16x32_bf16 v[84:87], v[172:175], v[192:195], v[84:87]\n\t" "ds_read_b64_tr_b16 v[166:167], v9 offset:41472\n\t" "v_cvt_pk_bf16_f32 v199, v210, v211\n\t"
    "v_mfma_f32_16x16x32_bf16 v[144:147], v[172:175], v[228:231], v[144:147]\n\t" "ds_read_b128 v[220:223], v2 offset:5632\n\t" "global_load_lds_dwordx4 v0, %[sb] offset:0\n\t"
    "v_mfma_f32_16x16x32_bf16 v[88:91], v[176:179], v[188:191], v[88:91]\n\t" "ds_read_b64_tr_b16 v[168:169], v10 offset:40960\n\t" "v_cvt_pk_bf16_f32 v200, v212, v213\n\t"
    "v_mfma_f32_16x16x32_bf16 v[92:95], v[176:179], v[192:195], v[92:95]\n\t" "ds_read_b64_tr_b16 v[170:171], v10 offset:41472\n\t" "v_cvt_pk_bf16_f32 v201, v214, v215\n\t"
    "v_mfma_f32_16x16x32_bf16 v[96:99], v[180:183], v[188:191], v[96:99]\n\t" "ds_read_b64_tr_b16 v[172:173], v3 offset:49152\n\t" "v_mov_b32 v39, 0x0c0c0c0c\n\t"
    "v_mfma_f32_16x16x32_bf16 v[100:103], v[180:183], v[192:195], v[100:103]\n\t" "ds_read_b64_tr_b16 v[174:175], v3 offset:49664\n\t" "v_cvt_pk_bf16_f32 v202, v216, v217\n\t"
    "v_mfma_f32_16x16x32_bf16 v[104:107], v[184:187], v[188:191], v[104:107]\n\t" "ds_read_b64_tr_b16 v[176:177], v4 offset:49152\n\t" "v_cvt_pk_bf16_f32 v203, v218, v219\n\t"
    "v_mfma_f32_16x16x32_bf16 v[108:111], v[184:187], v[192:195], v[108:111]\n\t" "ds_read_b64_tr_b16 v[178:179], v4 offset:49664\n\t" "s_waitcnt lgkmcnt(4)\n\t"
    "v_mfma_f32_16x16x32_bf16 v[112:115], v[188:191], v[156:159], v[112:115]\n\t" "ds_read_b64_tr_b16 v[180:181], v5 offset:49152\n\t" "v_cndmask_b32 v20, v40, v39, s[52:53]\n\t"
    "v_mfma_f32_16x16x32_bf16 v[116:119], v[192:195], v[156:159], v[116:119]\n\t" "ds_read_b64_tr_b16 v[182:183], v5 offset:49664\n\t" "v_cndmask_b32 v21, v41, v39, s[54:55]\n\t"
    "v_mfma_f32_16x16x32_bf16 v[148:151], v[228:231], v[156:159], v[148:151]\n\t" "ds_read_b128 v[232:235], v1 offset:21504\n\t" "global_load_lds_dwordx4 v0, %[sb] offset:2048\n\t"
    "v_mfma_f32_16x16x32_bf16 v[120:123], v[188:191], v[160:163], v[120:123]\n\t" "ds_read_b64_tr_b16 v[184:185], v6 offset:49152\n\t" "v_add_u32 v0, 0x30000, v0\n\t"
    "v_mfma_f32_16x16x32_bf16 v[124:127], v[192:195], v[160:163], v[124:127]\n\t" "ds_read_b64_tr_b16 v[186:187], v6 offset:49664\n\t" "v_cndmask_b32 v22, v39, v40, s[52:53]\n\t"
    "v_mfma_f32_16x16x32_bf16 v[128:131], v[188:191], v[164:167], v[128:131]\n\t" "ds_read_b64_tr_b16 v[156:157], v3 offset:51200\n\t" "v_cndmask_b32 v23, v39, v41, s[54:55]\n\t"
    "v_mfma_f32_16x16x32_bf16 v[132:135], v[192:195], v[164:167], v[132:135]\n\t" "ds_read_b64_tr_b16 v[158:159], v3 offset:51712\n\t" "s_mov_b32 s60, 0xcccccccc\n\t"
    "v_mfma_f32_16x16x32_bf16 v[136:139], v[188:191], v[168:171], v[136:139]\n\t" "ds_read_b64_tr_b16 v[160:161], v4 offset:51200\n\t" "s_mov_b32 s61, s60\n\t"
    "v_mfma_f32_16x16x32_bf16 v[140:143], v[192:195], v[168:171], v[140:143]\n\t" "ds_read_b64_tr_b16 v[162:163], v4 offset:51712\n\t" "s_waitcnt lgkmcnt(4)\n\t"
    "v_mfma_f32_16x16x32_bf16 v[48:51], v[172:175], v[196:199], v[48:51]\n\t" "ds_read_b64_tr_b16 v[164:165], v5 offset:51200\n\t" "s_add_u32 m0, %[sl], 96256\n\t"
    "v_mfma_f32_16x16x32_bf16 v[52:55], v[172:175], v[200:203], v[52:55]\n\t" "ds_read_b64_tr_b16 v[166:167], v5 offset:51712\n\t" "v_cndmask_b32 v14, v40, v39, s[60:61]\n\t"
    "v_mfma_f32_16x16x32_bf16 v[152:155], v[232:235], v[172:175], v[152:155]\n\t" "global_load_lds_dwordx4 v0, %[sb] offset:-2048\n\t" "v_cndmask_b32 v15, v41, v39, s[60:61]\n\t"
    "v_mfma_f32_16x16x32_bf16 v[56:59], v[176:179], v[196:199], v[56:59]\n\t" "ds_read_b64_tr_b16 v[168:169], v6 offset:51200\n\t" "v_cndmask_b32 v17, v39, v40, s[60:61]\n\t"
    "v_mfma_f32_4x4x4_16b_bf16 v[204:207], v[220:221], a[176:177], 0\n\t" "v_mfma_f32_4x4x4_16b_bf16 v[208:211], v[222:223], a[178:179], 0\n\t"
    "v_mfma_f32_4x4x4_16b_bf16 v[212:215], v[220:221], a[180:181], 0\n\t" "v_mfma_f32_4x4x4_16b_bf16 v[216:219], v[222:223], a[182:183], 0\n\t" "v_cndmask_b32 v19, v39, v41, s[60:61]\n\t"
    "v_mfma_f32_16x16x32_bf16 v[60:63], v[176:179], v[200:203], v[60:63]\n\t" "ds_read_b64_tr_b16 v[170:171], v6 offset:51712\n\t" "v_and_b32 v11, 7, v13\n\t"
    "v_mfma_f32_16x16x32_bf16 v[64:67], v[180:183], v[196:199], v[64:67]\n\t" "ds_read_b64_tr_b16 v[172:173], v3 offset:53248\n\t" "v_lshrrev_b32 v18, 1, v45\n\t"
    "v_mfma_f32_16x16x32_bf16 v[68:71], v[180:183], v[200:203], v[68:71]\n\t" "ds_read_b64_tr_b16 v[174:175], v3 offset:53760\n\t" "v_cvt_pk_bf16_f32 v188, v204, v205\n\t"
    "v_mfma_f32_16x16x32_bf16 v[72:75], v[184:187], v[196:199], v[72:75]\n\t" "ds_read_b64_tr_b16 v[176:177], v4 offset:53248\n\t" "v_cvt_pk_bf16_f32 v189, v206, v207\n\t"
    "v_mfma_f32_16x16x32_bf16 v[76:79], v[184:187], v[200:203], v[76:79]\n\t" "ds_read_b64_tr_b16 v[178:179], v4 offset:53760\n\t" "s_waitcnt lgkmcnt(4)\n\t"
    "v_mfma_f32_16x16x32_bf16 v[80:83], v[156:159], v[196:199], v[80:83]\n\t" "ds_read_b64_tr_b16 v[180:181], v5 offset:53248\n\t" "v_cvt_pk_bf16_f32 v190, v208, v209\n\t"
    "v_mfma_f32_16x16x32_bf16 v[84:87], v[156:159], v[200:203], v[84:87]\n\t" "ds_read_b64_tr_b16 v[182:183], v5 offset:53760\n\t" "v_cvt_pk_bf16_f32 v191, v210, v211\n\t"
    "v_mfma_f32_16x16x32_bf16 v[144:147], v[156:159], v[232:235], v[144:147]\n\t" "ds_read_b128 v[224:227], v2 offset:5888\n\t" "global_load_lds_dwordx4 v0, %[sb] offset:0\n\t"
    "v_mfma_f32_16x16x32_bf16 v[88:91], v[160:163], v[196:199], v[88:91]\n\t" "ds_read_b64_tr_b16 v[184:185], v6 offset:53248\n\t" "v_cvt_pk_bf16_f32 v192, v212, v213\n\t"
    "v_mfma_f32_16x16x32_bf16 v[92:95], v[160:163], v[200:203], v[92:95]\n\t" "ds_read_b64_tr_b16 v[186:187], v6 offset:53760\n\t" "v_cvt_pk_bf16_f32 v193, v214, v215\n\t"
    "v_mfma_f32_16x16x32_bf16 v[96:99], v[164:167], v[196:199], v[96:99]\n\t" "s_waitcnt vmcnt(11)\n\t" "s_barrier\n\t" "ds_read_b64_tr_b16 v[156:157], v7 offset:0\n\t"
    "v_mfma_f32_16x16x32_bf16 v[100:103], v[164:167], v[200:203], v[100:103]\n\t" "ds_read_b64_tr_b16 v[158:159], v7 offset:512\n\t" "v_cvt_pk_bf16_f32 v194, v216, v217\n\t"
    "v_mfma_f32_16x16x32_bf16 v[104:107], v[168:171], v[196:199], v[104:107]\n\t" "ds_read_b64_tr_b16 v[160:161], v8 offset:0\n\t" "v_cvt_pk_bf16_f32 v195, v218, v219\n\t"
    "v_mfma_f32_16x16x32_bf16 v[108:111], v[168:171], v[200:203], v[108:111]\n\t" "ds_read_b64_tr_b16 v[162:163], v8 offset:512\n\t" "s_waitcnt lgkmcnt(4)\n\t"
    "v_mfma_f32_16x16x32_bf16 v[112:115], v[196:199], v[172:175], v[112:115]\n\t" "ds_read_b64_tr_b16 v[164:165], v9 offset:0\n\t" "v_lshl_add_u32 v11, v11, 5, v18\n\t"
    "v_mfma_f32_16x16x32_bf16 v[116:119], v[200:203], v[172:175], v[116:119]\n\t" "ds_read_b64_tr_b16 v[166:167], v9 offset:512\n\t" "v_add_u32 v11, %[lr], v11\n\t"
    "v_mfma_f32_16x16x32_bf16 v[148:151], v[232:235], v[172:175], v[148:151]\n\t" "ds_read_b128 v[228:231], v1 offset:22528\n\t" "global_load_lds_dwordx4 v0, %[sb] offset:2048\n\t"
    "v_mfma_f32_16x16x32_bf16 v[120:123], v[196:199], v[176:179], v[120:123]\n\t" "ds_read_b64_tr_b16 v[168:169], v10 offset:0\n\t" "s_cmp_eq_u32 %[rot], 1\n\t" "s_cselect_b32 s42, s43, 0x30000\n\t"
    "v_add_u32 v0, s42, v0\n\t" "v_mfma_f32_16x16x32_bf16 v[124:127], v[200:203], v[176:179], v[124:127]\n\t" "ds_read_b64_tr_b16 v[170:171], v10 offset:512\n\t" "v_mul_f32 v28, v240, v24\n\t"
    "v_mfma_f32_16x16x32_bf16 v[128:131], v[196:199], v[180:183], v[128:131]\n\t" "ds_read_b64_tr_b16 v[172:173], v7 offset:2048\n\t" "v_mul_f32 v29, v240, v25\n\t"
    "v_mfma_f32_16x16x32_bf16 v[132:135], v[200:203], v[180:183], v[132:135]\n\t" "ds_read_b64_tr_b16 v[174:175], v7 offset:2560\n\t" "v_mul_f32 v30, v241, v26\n\t"
    "v_mfma_f32_16x16x32_bf16 v[136:139], v[196:199], v[184:187], v[136:139]\n\t" "ds_read_b64_tr_b16 v[176:177], v8 offset:2048\n\t" "v_mul_f32 v31, v241, v27\n\t"
    "v_mfma_f32_16x16x32_bf16 v[140:143], v[200:203], v[184:187], v[140:143]\n\t" "ds_read_b64_tr_b16 v[178:179], v8 offset:2560\n\t" "s_waitcnt lgkmcnt(4)\n\t"
    "v_mfma_f32_16x16x32_bf16 v[48:51], v[156:159], v[188:191], v[48:51]\n\t" "ds_read_b64_tr_b16 v[180:181], v9 offset:2048\n\t" "s_add_u32 m0, %[sl], 108544\n\t"
    "v_mfma_f32_16x16x32_bf16 v[52:55], v[156:159], v[192:195], v[52:55]\n\t" "ds_read_b64_tr_b16 v[182:183], v9 offset:2560\n\t" "v_mov_b32 v39, 0xceabfb70\n\t"
    "v_mfma_f32_16x16x32_bf16 v[152:155], v[228:231], v[156:159], v[152:155]\n\t" "global_load_lds_dwordx4 v0, %[sb] offset:-2048\n\t" "v_cndmask_b32 v28, v28, 0, s[56:57]\n\t"
    "v_mfma_f32_16x16x32_bf16 v[56:59], v[160:163], v[188:191], v[56:59]\n\t" "ds_read_b64_tr_b16 v[184:185], v10 offset:2048\n\t" "v_cndmask_b32 v29, v29, 0, s[58:59]\n\t"
    "v_mfma_f32_4x4x4_16b_bf16 v[204:207], v[224:225], a[184:185], 0\n\t" "v_mfma_f32_4x4x4_16b_bf16 v[208:211], v[226:227], a[186:187], 0\n\t"
    "v_mfma_f32_4x4x4_16b_bf16 v[212:215], v[224:225], a[188:189], 0\n\t" "v_mfma_f32_4x4x4_16b_bf16 v[216:219], v[226:227], a[190:191], 0\n\t" "v_cndmask_b32 v32, 0, v39, s[56:57]\n\t"
    "v_mfma_f32_16x16x32_bf16 v[60:63], v[160:163], v[192:195], v[60:63]\n\t" "ds_read_b64_tr_b16 v[186:187], v10 offset:2560\n\t" "v_cndmask_b32 v33, 0, v39, s[58:59]\n\t"
    "v_mfma_f32_16x16x32_bf16 v[64:67], v[164:167], v[188:191], v[64:67]\n\t" "ds_read_b64_tr_b16 v[156:157], v7 offset:4096\n\t" "v_lshl_add_u32 v43, v46, 5, %[lh]\n\t"
    "v_mfma_f32_16x16x32_bf16 v[68:71], v[164:167], v[192:195], v[68:71]\n\t" "ds_read_b64_tr_b16 v[158:159], v7 offset:4608\n\t" "v_cvt_pk_bf16_f32 v196, v204, v205\n\t"
    "v_mfma_f32_16x16x32_bf16 v[72:75], v[168:171], v[188:191], v[72:75]\n\t" "ds_read_b64_tr_b16 v[160:161], v8 offset:4096\n\t" "v_cvt_pk_bf16_f32 v197, v206, v207\n\t"
    "v_mfma_f32_16x16x32_bf16 v[76:79], v[168:171], v[192:195], v[76:79]\n\t" "ds_read_b64_tr_b16 v[162:163], v8 offset:4608\n\t" "s_waitcnt lgkmcnt(4)\n\t"
    "v_mfma_f32_16x16x32_bf16 v[80:83], v[172:175], v[188:191], v[80:83]\n\t" "ds_read_b64_tr_b16 v[164:165], v9 offset:4096\n\t" "v_cvt_pk_bf16_f32 v198, v208, v209\n\t"
    "v_mfma_f32_16x16x32_bf16 v[84:87], v[172:175], v[192:195], v[84:87]\n\t" "ds_read_b64_tr_b16 v[166:167], v9 offset:4608\n\t" "v_cvt_pk_bf16_f32 v199, v210, v211\n\t"
    "v_mfma_f32_16x16x32_bf16 v[144:147], v[172:175], v[228:231], v[144:147]\n\t" "ds_read_b128 v[220:223], v2 offset:6144\n\t" "global_load_lds_dwordx4 v0, %[sb] offset:0\n\t"
    "v_mfma_f32_16x16x32_bf16 v[88:91], v[176:179], v[188:191], v[88:91]\n\t" "ds_read_b64_tr_b16 v[168:169], v10 offset:4096\n\t" "v_cvt_pk_bf16_f32 v200, v212, v213\n\t"
    "v_mfma_f32_16x16x32_bf16 v[92:95], v[176:179], v[192:195], v[92:95]\n\t" "ds_read_b64_tr_b16 v[170:171], v10 offset:4608\n\t" "v_cvt_pk_bf16_f32 v201, v214, v215\n\t"
    "v_mfma_f32_16x16x32_bf16 v[96:99], v[180:183], v[188:191], v[96:99]\n\t" "ds_read_b64_tr_b16 v[172:173], v3 offset:0\n\t" "v_lshl_add_u32 v36, v47, 3, %[lt]\n\t"
    "v_mfma_f32_16x16x32_bf16 v[100:103], v[180:183], v[192:195], v[100:103]\n\t" "ds_read_b64_tr_b16 v[174:175], v3 offset:512\n\t" "v_cvt_pk_bf16_f32 v202, v216, v217\n\t"
    "v_mfma_f32_16x16x32_bf16 v[104:107], v[184:187], v[188:191], v[104:107]\n\t" "ds_read_b64_tr_b16 v[176:177], v4 offset:0\n\t" "v_cvt_pk_bf16_f32 v203, v218, v219\n\t"
    "v_mfma_f32_16x16x32_bf16 v[108:111], v[184:187], v[192:195], v[108:111]\n\t" "ds_read_b64_tr_b16 v[178:179], v4 offset:512\n\t" "s_waitcnt lgkmcnt(4)\n\t"
    "v_mfma_f32_16x16x32_bf16 v[112:115], v[188:191], v[156:159], v[112:115]\n\t" "ds_read_b64_tr_b16 v[180:181], v5 offset:0\n\t" "v_lshl_add_u32 v35, v46, 3, %[lh]\n\t"
    "v_mfma_f32_16x16x32_bf16 v[116:119], v[192:195], v[156:159], v[116:119]\n\t" "ds_read_b64_tr_b16 v[182:183], v5 offset:512\n\t" "v_cndmask_b32 v35, v35, v36, s[46:47]\n\t"
    "v_mfma_f32_16x16x32_bf16 v[148:151], v[228:231], v[156:159], v[148:151]\n\t" "ds_read_b128 v[232:235], v1 offset:23552\n\t" "global_load_lds_dwordx4 v0, %[sb] offset:2048\n\t"
    "v_mfma_f32_16x16x32_bf16 v[120:123], v[188:191], v[160:163], v[120:123]\n\t" "ds_read_b64_tr_b16 v[184:185], v6 offset:0\n\t" "v_add_u32 v0, 0x30000, v0\n\t"
    "v_mfma_f32_16x16x32_bf16 v[124:127], v[192:195], v[160:163], v[124:127]\n\t" "ds_read_b64_tr_b16 v[186:187], v6 offset:512\n\t" "v_lshl_add_u32 v34, v45, 1, %[lt]\n\t"
    "v_mfma_f32_16x16x32_bf16 v[128:131], v[188:191], v[164:167], v[128:131]\n\t" "ds_read_b64_tr_b16 v[156:157], v3 offset:2048\n\t" "v_lshl_add_u32 v34, v47, 2, v34\n\t"
    "v_mfma_f32_16x16x32_bf16 v[132:135], v[192:195], v[164:167], v[132:135]\n\t" "ds_read_b64_tr_b16 v[158:159], v3 offset:2560\n\t" "v_add_u32 v34, 0x200, v34\n\t"
    "v_mfma_f32_16x16x32_bf16 v[136:139], v[188:191], v[168:171], v[136:139]\n\t" "ds_read_b64_tr_b16 v[160:161], v4 offset:2048\n\t" "v_cndmask_b32 v34, v43, v34, s[48:49]\n\t"
    "v_mfma_f32_16x16x32_bf16 v[140:143], v[192:195], v[168:171], v[140:143]\n\t" "ds_read_b64_tr_b16 v[162:163], v4 offset:2560\n\t" "s_waitcnt lgkmcnt(4)\n\t"
    "v_mfma_f32_16x16x32_bf16 v[48:51], v[172:175], v[196:199], v[48:51]\n\t" "ds_read_b64_tr_b16 v[164:165], v5 offset:2048\n\t" "s_add_u32 m0, %[sl], 120832\n\t"
    "v_mfma_f32_16x16x32_bf16 v[52:55], v[172:175], v[200:203], v[52:55]\n\t" "ds_read_b64_tr_b16 v[166:167], v5 offset:2560\n\t" "v_xor_b32 v37, 0x80, v36\n\t"
    "v_mfma_f32_16x16x32_bf16 v[152:155], v[232:235], v[172:175], v[152:155]\n\t" "global_load_lds_dwordx4 v0, %[sb] offset:-2048\n\t" "v_xor_b32 v38, 0x100, v36\n\t"
    "v_mfma_f32_16x16x32_bf16 v[56:59], v[176:179], v[196:199], v[56:59]\n\t" "ds_read_b64_tr_b16 v[168:169], v6 offset:2048\n\t" "v_xor_b32 v39, 0x180, v36\n\t"
    "v_mfma_f32_4x4x4_16b_bf16 v[204:207], v[220:221], a[192:193], 0\n\t" "v_mfma_f32_4x4x4_16b_bf16 v[208:211], v[222:223], a[194:195], 0\n\t"
    "v_mfma_f32_4x4x4_16b_bf16 v[212:215], v[220:221], a[196:197], 0\n\t" "v_mfma_f32_4x4x4_16b_bf16 v[216:219], v[222:223], a[198:199], 0\n\t" "v_add_u32 v40, %[xw], v43\n\t"
    "v_mfma_f32_16x16x32_bf16 v[60:63], v[176:179], v[200:203], v[60:63]\n\t" "ds_read_b64_tr_b16 v[170:171], v6 offset:2560\n\t" "v_xor_b32 v41, 0x800, v40\n\t"
    "v_mfma_f32_16x16x32_bf16 v[64:67], v[180:183], v[196:199], v[64:67]\n\t" "ds_read_b64_tr_b16 v[172:173], v3 offset:4096\n\t" "v_add_u32 v42, %[xh], v43\n\t"
    "v_mfma_f32_16x16x32_bf16 v[68:71], v[180:183], v[200:203], v[68:71]\n\t" "ds_read_b64_tr_b16 v[174:175], v3 offset:4608\n\t" "v_cvt_pk_bf16_f32 v188, v204, v205\n\t"
    "v_mfma_f32_16x16x32_bf16 v[72:75], v[184:187], v[196:199], v[72:75]\n\t" "ds_read_b64_tr_b16 v[176:177], v4 offset:4096\n\t" "v_cvt_pk_bf16_f32 v189, v206, v207\n\t"
    "v_mfma_f32_16x16x32_bf16 v[76:79], v[184:187], v[200:203], v[76:79]\n\t" "ds_read_b64_tr_b16 v[178:179], v4 offset:4608\n\t" "s_waitcnt lgkmcnt(4)\n\t"
    "v_mfma_f32_16x16x32_bf16 v[80:83], v[156:159], v[196:199], v[80:83]\n\t" "ds_read_b64_tr_b16 v[180:181], v5 offset:4096\n\t" "v_cvt_pk_bf16_f32 v190, v208, v209\n\t"
    "v_mfma_f32_16x16x32_bf16 v[84:87], v[156:159], v[200:203], v[84:87]\n\t" "ds_read_b64_tr_b16 v[182:183], v5 offset:4608\n\t" "v_cvt_pk_bf16_f32 v191, v210, v211\n\t"
    "v_mfma_f32_16x16x32_bf16 v[144:147], v[156:159], v[232:235], v[144:147]\n\t" "ds_read_b128 v[224:227], v2 offset:6400\n\t" "global_load_lds_dwordx4 v0, %[sb] offset:0\n\t"
    "v_mfma_f32_16x16x32_bf16 v[88:91], v[160:163], v[196:199], v[88:91]\n\t" "ds_read_b64_tr_b16 v[184:185], v6 offset:4096\n\t" "v_cvt_pk_bf16_f32 v192, v212, v213\n\t"
    "v_mfma_f32_16x16x32_bf16 v[92:95], v[160:163], v[200:203], v[92:95]\n\t" "ds_read_b64_tr_b16 v[186:187], v6 offset:4608\n\t" "v_cvt_pk_bf16_f32 v193, v214, v215\n\t"
    "v_mfma_f32_16x16x32_bf16 v[96:99], v[164:167], v[196:199], v[96:99]\n\t" "s_waitcnt vmcnt(11)\n\t" "s_barrier\n\t" "ds_read_b64_tr_b16 v[156:157], v3 offset:12288\n\t"
    "v_mfma_f32_16x16x32_bf16 v[100:103], v[164:167], v[200:203], v[100:103]\n\t" "ds_read_b64_tr_b16 v[158:159], v3 offset:12800\n\t" "v_cvt_pk_bf16_f32 v194, v216, v217\n\t"
    "v_mfma_f32_16x16x32_bf16 v[104:107], v[168:171], v[196:199], v[104:107]\n\t" "ds_read_b64_tr_b16 v[160:161], v4 offset:12288\n\t" "v_cvt_pk_bf16_f32 v195, v218, v219\n\t"
    "v_mfma_f32_16x16x32_bf16 v[108:111], v[168:171], v[200:203], v[108:111]\n\t" "ds_read_b64_tr_b16 v[162:163], v4 offset:12800\n\t" "s_waitcnt lgkmcnt(4)\n\t"
    "v_mfma_f32_16x16x32_bf16 v[112:115], v[196:199], v[172:175], v[112:115]\n\t" "ds_read_b64_tr_b16 v[164:165], v5 offset:12288\n\t" "v_add_u32 v43, %[w4k], v43\n\t"
    "v_mfma_f32_16x16x32_bf16 v[116:119], v[200:203], v[172:175], v[116:119]\n\t" "ds_read_b64_tr_b16 v[166:167], v5 offset:12800\n\t" "v_lshl_add_u32 v44, v46, 4, %[ox]\n\t"
    "v_mfma_f32_16x16x32_bf16 v[148:151], v[232:235], v[172:175], v[148:151]\n\t" "ds_read_b128 v[228:231], v1 offset:24576\n\t" "global_load_lds_dwordx4 v0, %[sb] offset:2048\n\t"
    "v_mfma_f32_16x16x32_bf16 v[120:123], v[196:199], v[176:179], v[120:123]\n\t" "ds_read_b64_tr_b16 v[168:169], v6 offset:12288\n\t" "v_add_u32 v0, 0x30000, v0\n\t"
    "v_mfma_f32_16x16x32_bf16 v[124:127], v[200:203], v[176:179], v[124:127]\n\t" "ds_read_b64_tr_b16 v[170:171], v6 offset:12800\n\t" "v_xor_b32 v45, 0x800, v44\n\t"
    "v_mfma_f32_16x16x32_bf16 v[128:131], v[196:199], v[180:183], v[128:131]\n\t" "ds_read_b64_tr_b16 v[172:173], v3 offset:14336\n\t"
    "v_mfma_f32_16x16x32_bf16 v[132:135], v[200:203], v[180:183], v[132:135]\n\t" "ds_read_b64_tr_b16 v[174:175], v3 offset:14848\n\t"
    "v_mfma_f32_16x16x32_bf16 v[136:139], v[196:199], v[184:187], v[136:139]\n\t" "ds_read_b64_tr_b16 v[176:177], v4 offset:14336\n\t"
    "v_mfma_f32_16x16x32_bf16 v[140:143], v[200:203], v[184:187], v[140:143]\n\t" "ds_read_b64_tr_b16 v[178:179], v4 offset:14848\n\t" "s_waitcnt lgkmcnt(4)\n\t"
    "v_mfma_f32_16x16x32_bf16 v[48:51], v[156:159], v[188:191], v[48:51]\n\t" "ds_read_b64_tr_b16 v[180:181], v5 offset:14336\n\t" "s_add_u32 m0, %[sl], 71680\n\t"
    "v_mfma_f32_16x16x32_bf16 v[52:55], v[156:159], v[192:195], v[52:55]\n\t" "ds_read_b64_tr_b16 v[182:183], v5 offset:14848\n\t"
    "v_mfma_f32_16x16x32_bf16 v[152:155], v[228:231], v[156:159], v[152:155]\n\t" "global_load_lds_dwordx4 v0, %[sb] offset:-2048\n\t"
    "v_mfma_f32_16x16x32_bf16 v[56:59], v[160:163], v[188:191], v[56:59]\n\t" "ds_read_b64_tr_b16 v[184:185], v6 offset:14336\n\t" "v_mfma_f32_4x4x4_16b_bf16 v[204:207], v[224:225], a[200:201], 0\n\t"
    "v_mfma_f32_4x4x4_16b_bf16 v[208:211], v[226:227], a[202:203], 0\n\t" "v_mfma_f32_4x4x4_16b_bf16 v[212:215], v[224:225], a[204:205], 0\n\t"
    "v_mfma_f32_4x4x4_16b_bf16 v[216:219], v[226:227], a[206:207], 0\n\t" "v_mfma_f32_16x16x32_bf16 v[60:63], v[160:163], v[192:195], v[60:63]\n\t" "ds_read_b64_tr_b16 v[186:187], v6 offset:14848\n\t"
    "v_mfma_f32_16x16x32_bf16 v[64:67], v[164:167], v[188:191], v[64:67]\n\t" "ds_read_b64_tr_b16 v[156:157], v3 offset:16384\n\t" "global_load_lds_dwordx4 v0, %[sb] offset:0\n\t"
    "v_mfma_f32_16x16x32_bf16 v[68:71], v[164:167], v[192:195], v[68:71]\n\t" "ds_read_b64_tr_b16 v[158:159], v3 offset:16896\n\t" "v_cvt_pk_bf16_f32 v196, v204, v205\n\t"
    "v_mfma_f32_16x16x32_bf16 v[72:75], v[168:171], v[188:191], v[72:75]\n\t" "ds_read_b64_tr_b16 v[160:161], v4 offset:16384\n\t" "v_cvt_pk_bf16_f32 v197, v206, v207\n\t"
    "v_mfma_f32_16x16x32_bf16 v[76:79], v[168:171], v[192:195], v[76:79]\n\t" "ds_read_b64_tr_b16 v[162:163], v4 offset:16896\n\t" "s_waitcnt lgkmcnt(4)\n\t"
    "v_mfma_f32_16x16x32_bf16 v[80:83], v[172:175], v[188:191], v[80:83]\n\t" "ds_read_b64_tr_b16 v[164:165], v5 offset:16384\n\t" "v_cvt_pk_bf16_f32 v198, v208, v209\n\t"
    "v_mfma_f32_16x16x32_bf16 v[84:87], v[172:175], v[192:195], v[84:87]\n\t" "ds_read_b64_tr_b16 v[166:167], v5 offset:16896\n\t" "v_cvt_pk_bf16_f32 v199, v210, v211\n\t"
    "v_mfma_f32_16x16x32_bf16 v[144:147], v[172:175], v[228:231], v[144:147]\n\t" "ds_read_b128 v[220:223], v2 offset:6656\n\t" "global_load_lds_dwordx4 v0, %[sb] offset:2048\n\t"
    "v_mfma_f32_16x16x32_bf16 v[88:91], v[176:179], v[188:191], v[88:91]\n\t" "ds_read_b64_tr_b16 v[168:169], v6 offset:16384\n\t" "v_cvt_pk_bf16_f32 v200, v212, v213\n\t"
    "v_mfma_f32_16x16x32_bf16 v[92:95], v[176:179], v[192:195], v[92:95]\n\t" "ds_read_b64_tr_b16 v[170:171], v6 offset:16896\n\t" "v_cvt_pk_bf16_f32 v201, v214, v215\n\t"
    "v_mfma_f32_16x16x32_bf16 v[96:99], v[180:183], v[188:191], v[96:99]\n\t" "ds_read_b64_tr_b16 v[172:173], v3 offset:24576\n\t" "v_add_u32 v0, 0x30000, v0\n\t"
    "v_mfma_f32_16x16x32_bf16 v[100:103], v[180:183], v[192:195], v[100:103]\n\t" "ds_read_b64_tr_b16 v[174:175], v3 offset:25088\n\t" "v_cvt_pk_bf16_f32 v202, v216, v217\n\t"
    "v_mfma_f32_16x16x32_bf16 v[104:107], v[184:187], v[188:191], v[104:107]\n\t" "ds_read_b64_tr_b16 v[176:177], v4 offset:24576\n\t" "v_cvt_pk_bf16_f32 v203, v218, v219\n\t"
    "v_mfma_f32_16x16x32_bf16 v[108:111], v[184:187], v[192:195], v[108:111]\n\t" "ds_read_b64_tr_b16 v[178:179], v4 offset:25088\n\t" "s_waitcnt lgkmcnt(4)\n\t"
    "v_mfma_f32_16x16x32_bf16 v[112:115], v[188:191], v[156:159], v[112:115]\n\t" "ds_read_b64_tr_b16 v[180:181], v5 offset:24576\n\t" "s_add_u32 m0, %[sl1], 0\n\t"
    "v_mfma_f32_16x16x32_bf16 v[116:119], v[192:195], v[156:159], v[116:119]\n\t" "ds_read_b64_tr_b16 v[182:183], v5 offset:25088\n\t"
    "v_mfma_f32_16x16x32_bf16 v[148:151], v[228:231], v[156:159], v[148:151]\n\t" "ds_read_b128 v[232:235], v1 offset:25600\n\t" "global_load_lds_dwordx4 v12, %[so]\n\t"
    "v_mfma_f32_16x16x32_bf16 v[120:123], v[188:191], v[160:163], v[120:123]\n\t" "ds_read_b64_tr_b16 v[184:185], v6 offset:24576\n\t" "v_add_u32 v12, 0x20000, v12\n\t"
    "v_mfma_f32_16x16x32_bf16 v[124:127], v[192:195], v[160:163], v[124:127]\n\t" "ds_read_b64_tr_b16 v[186:187], v6 offset:25088\n\t" "s_add_u32 m0, %[sl1], 8192\n\t"
    "v_mfma_f32_16x16x32_bf16 v[128:131], v[188:191], v[164:167], v[128:131]\n\t" "ds_read_b64_tr_b16 v[156:157], v3 offset:26624\n\t"
    "v_mfma_f32_16x16x32_bf16 v[132:135], v[192:195], v[164:167], v[132:135]\n\t" "ds_read_b64_tr_b16 v[158:159], v3 offset:27136\n\t" "global_load_lds_dwordx4 v12, %[so]\n\t"
    "v_mfma_f32_16x16x32_bf16 v[136:139], v[188:191], v[168:171], v[136:139]\n\t" "ds_read_b64_tr_b16 v[160:161], v4 offset:26624\n\t"
    "v_mfma_f32_16x16x32_bf16 v[140:143], v[192:195], v[168:171], v[140:143]\n\t" "ds_read_b64_tr_b16 v[162:163], v4 offset:27136\n\t" "s_waitcnt lgkmcnt(4)\n\t"
    "v_mfma_f32_16x16x32_bf16 v[48:51], v[172:175], v[196:199], v[48:51]\n\t" "ds_read_b64_tr_b16 v[164:165], v5 offset:26624\n\t" "s_add_u32 m0, %[sl], 83968\n\t"
    "v_mfma_f32_16x16x32_bf16 v[52:55], v[172:175], v[200:203], v[52:55]\n\t" "ds_read_b64_tr_b16 v[166:167], v5 offset:27136\n\t"
    "v_mfma_f32_16x16x32_bf16 v[152:155], v[232:235], v[172:175], v[152:155]\n\t" "global_load_lds_dwordx4 v0, %[sb] offset:-2048\n\t"
    "v_mfma_f32_16x16x32_bf16 v[56:59], v[176:179], v[196:199], v[56:59]\n\t" "ds_read_b64_tr_b16 v[168:169], v6 offset:26624\n\t" "v_mfma_f32_4x4x4_16b_bf16 v[204:207], v[220:221], a[208:209], 0\n\t"
    "v_mfma_f32_4x4x4_16b_bf16 v[208:211], v[222:223], a[210:211], 0\n\t" "v_mfma_f32_4x4x4_16b_bf16 v[212:215], v[220:221], a[212:213], 0\n\t"
    "v_mfma_f32_4x4x4_16b_bf16 v[216:219], v[222:223], a[214:215], 0\n\t" "v_mfma_f32_16x16x32_bf16 v[60:63], v[176:179], v[200:203], v[60:63]\n\t" "ds_read_b64_tr_b16 v[170:171], v6 offset:27136\n\t"
    "v_mfma_f32_16x16x32_bf16 v[64:67], v[180:183], v[196:199], v[64:67]\n\t" "ds_read_b64_tr_b16 v[172:173], v3 offset:28672\n\t" "v_mfma_f32_16x16x32_bf16 v[68:71], v[180:183], v[200:203], v[68:71]\n\t"
    "ds_read_b64_tr_b16 v[174:175], v3 offset:29184\n\t" "v_cvt_pk_bf16_f32 v188, v204, v205\n\t" "v_mfma_f32_16x16x32_bf16 v[72:75], v[184:187], v[196:199], v[72:75]\n\t"
    "ds_read_b64_tr_b16 v[176:177], v4 offset:28672\n\t" "v_cvt_pk_bf16_f32 v189, v206, v207\n\t" "v_mfma_f32_16x16x32_bf16 v[76:79], v[184:187], v[200:203], v[76:79]\n\t"
    "ds_read_b64_tr_b16 v[178:179], v4 offset:29184\n\t" "s_waitcnt lgkmcnt(4)\n\t" "v_mfma_f32_16x16x32_bf16 v[80:83], v[156:159], v[196:199], v[80:83]\n\t"
    "ds_read_b64_tr_b16 v[180:181], v5 offset:28672\n\t" "v_cvt_pk_bf16_f32 v190, v208, v209\n\t" "v_mfma_f32_16x16x32_bf16 v[84:87], v[156:159], v[200:203], v[84:87]\n\t"
    "ds_read_b64_tr_b16 v[182:183], v5 offset:29184\n\t" "v_cvt_pk_bf16_f32 v191, v210, v211\n\t" "v_mfma_f32_16x16x32_bf16 v[144:147], v[156:159], v[232:235], v[144:147]\n\t"
    "ds_read_b128 v[224:227], v2 offset:6912\n\t" "global_load_lds_dwordx4 v0, %[sb] offset:0\n\t" "v_mfma_f32_16x16x32_bf16 v[88:91], v[160:163], v[196:199], v[88:91]\n\t"
    "ds_read_b64_tr_b16 v[184:185], v6 offset:28672\n\t" "v_cvt_pk_bf16_f32 v192, v212, v213\n\t" "v_mfma_f32_16x16x32_bf16 v[92:95], v[160:163], v[200:203], v[92:95]\n\t"
    "ds_read_b64_tr_b16 v[186:187], v6 offset:29184\n\t" "v_cvt_pk_bf16_f32 v193, v214, v215\n\t" "v_mfma_f32_16x16x32_bf16 v[96:99], v[164:167], v[196:199], v[96:99]\n\t" "s_waitcnt vmcnt(13)\n\t"
    "s_barrier\n\t" "ds_read_b64_tr_b16 v[156:157], v3 offset:36864\n\t" "v_mfma_f32_16x16x32_bf16 v[100:103], v[164:167], v[200:203], v[100:103]\n\t" "ds_read_b64_tr_b16 v[158:159], v3 offset:37376\n\t"
    "v_cvt_pk_bf16_f32 v194, v216, v217\n\t" "v_mfma_f32_16x16x32_bf16 v[104:107], v[168:171], v[196:199], v[104:107]\n\t" "ds_read_b64_tr_b16 v[160:161], v4 offset:36864\n\t"
    "v_cvt_pk_bf16_f32 v195, v218, v219\n\t" "v_mfma_f32_16x16x32_bf16 v[108:111], v[168:171], v[200:203], v[108:111]\n\t" "ds_read_b64_tr_b16 v[162:163], v4 offset:37376\n\t" "s_waitcnt lgkmcnt(4)\n\t"
    "v_mfma_f32_16x16x32_bf16 v[112:115], v[196:199], v[172:175], v[112:115]\n\t" "ds_read_b64_tr_b16 v[164:165], v5 offset:36864\n\t"
    "v_mfma_f32_16x16x32_bf16 v[116:119], v[200:203], v[172:175], v[116:119]\n\t" "ds_read_b64_tr_b16 v[166:167], v5 offset:37376\n\t"
    "v_mfma_f32_16x16x32_bf16 v[148:151], v[232:235], v[172:175], v[148:151]\n\t" "ds_read_b128 v[228:231], v1 offset:26624\n\t" "global_load_lds_dwordx4 v0, %[sb] offset:2048\n\t"
    "v_mfma_f32_16x16x32_bf16 v[120:123], v[196:199], v[176:179], v[120:123]\n\t" "ds_read_b64_tr_b16 v[168:169], v6 offset:36864\n\t" "v_add_u32 v12, 0x10000, v12\n\t"
    "v_mfma_f32_16x16x32_bf16 v[124:127], v[200:203], v[176:179], v[124:127]\n\t" "ds_read_b64_tr_b16 v[170:171], v6 offset:37376\n\t" "s_add_u32 m0, %[sl1], 12288\n\t"
    "v_mfma_f32_16x16x32_bf16 v[128:131], v[196:199], v[180:183], v[128:131]\n\t" "ds_read_b64_tr_b16 v[172:173], v3 offset:38912\n\t"
    "v_mfma_f32_16x16x32_bf16 v[132:135], v[200:203], v[180:183], v[132:135]\n\t" "ds_read_b64_tr_b16 v[174:175], v3 offset:39424\n\t" "global_load_lds_dwordx4 v12, %[so]\n\t"
    "v_mfma_f32_16x16x32_bf16 v[136:139], v[196:199], v[184:187], v[136:139]\n\t" "ds_read_b64_tr_b16 v[176:177], v4 offset:38912\n\t"
    "v_mfma_f32_16x16x32_bf16 v[140:143], v[200:203], v[184:187], v[140:143]\n\t" "ds_read_b64_tr_b16 v[178:179], v4 offset:39424\n\t" "s_waitcnt lgkmcnt(4)\n\t"
    "v_mfma_f32_16x16x32_bf16 v[48:51], v[156:159], v[188:191], v[48:51]\n\t" "ds_read_b64_tr_b16 v[180:181], v5 offset:38912\n\t" "v_add_u32 v12, 0x20000, v12\n\t"
    "v_mfma_f32_16x16x32_bf16 v[52:55], v[156:159], v[192:195], v[52:55]\n\t" "ds_read_b64_tr_b16 v[182:183], v5 offset:39424\n\t" "s_add_u32 m0, %[sl1], 20480\n\t"
    "v_mfma_f32_16x16x32_bf16 v[152:155], v[228:231], v[156:159], v[152:155]\n\t" "global_load_lds_dwordx4 v12, %[so]\n\t" "v_add_u32 v12, 0x10000, v12\n\t"
    "v_mfma_f32_16x16x32_bf16 v[56:59], v[160:163], v[188:191], v[56:59]\n\t" "ds_read_b64_tr_b16 v[184:185], v6 offset:38912\n\t" "v_mfma_f32_4x4x4_16b_bf16 v[204:207], v[224:225], a[216:217], 0\n\t"
    "v_mfma_f32_4x4x4_16b_bf16 v[208:211], v[226:227], a[218:219], 0\n\t" "v_mfma_f32_4x4x4_16b_bf16 v[212:215], v[224:225], a[220:221], 0\n\t"
    "v_mfma_f32_4x4x4_16b_bf16 v[216:219], v[226:227], a[222:223], 0\n\t" "v_mfma_f32_16x16x32_bf16 v[60:63], v[160:163], v[192:195], v[60:63]\n\t" "ds_read_b64_tr_b16 v[186:187], v6 offset:39424\n\t"
    "v_mfma_f32_16x16x32_bf16 v[64:67], v[164:167], v[188:191], v[64:67]\n\t" "ds_read_b64_tr_b16 v[156:157], v3 offset:40960\n\t" "v_mfma_f32_16x16x32_bf16 v[68:71], v[164:167], v[192:195], v[68:71]\n\t"
    "ds_read_b64_tr_b16 v[158:159], v3 offset:41472\n\t" "v_cvt_pk_bf16_f32 v196, v204, v205\n\t" "v_mfma_f32_16x16x32_bf16 v[72:75], v[168:171], v[188:191], v[72:75]\n\t"
    "ds_read_b64_tr_b16 v[160:161], v4 offset:40960\n\t" "v_cvt_pk_bf16_f32 v197, v206, v207\n\t" "v_mfma_f32_16x16x32_bf16 v[76:79], v[168:171], v[192:195], v[76:79]\n\t"
    "ds_read_b64_tr_b16 v[162:163], v4 offset:41472\n\t" "s_waitcnt lgkmcnt(4)\n\t" "v_mfma_f32_16x16x32_bf16 v[80:83], v[172:175], v[188:191], v[80:83]\n\t"
    "ds_read_b64_tr_b16 v[164:165], v5 offset:40960\n\t" "v_cvt_pk_bf16_f32 v198, v208, v209\n\t" "v_mfma_f32_16x16x32_bf16 v[84:87], v[172:175], v[192:195], v[84:87]\n\t"
    "ds_read_b64_tr_b16 v[166:167], v5 offset:41472\n\t" "v_cvt_pk_bf16_f32 v199, v210, v211\n\t" "v_mfma_f32_16x16x32_bf16 v[144:147], v[172:175], v[228:231], v[144:147]\n\t"
    "ds_read_b128 v[220:223], v2 offset:7168\n\t" "s_add_u32 m0, %[sl1], 24576\n\t" "v_mfma_f32_16x16x32_bf16 v[88:91], v[176:179], v[188:191], v[88:91]\n\t"
    "ds_read_b64_tr_b16 v[168:169], v6 offset:40960\n\t" "v_cvt_pk_bf16_f32 v200, v212, v213\n\t" "v_mfma_f32_16x16x32_bf16 v[92:95], v[176:179], v[192:195], v[92:95]\n\t"
    "ds_read_b64_tr_b16 v[170:171], v6 offset:41472\n\t" "v_cvt_pk_bf16_f32 v201, v214, v215\n\t" "v_mfma_f32_16x16x32_bf16 v[96:99], v[180:183], v[188:191], v[96:99]\n\t"
    "ds_read_b64_tr_b16 v[172:173], v7 offset:12288\n\t" "global_load_lds_dwordx4 v12, %[so]\n\t" "v_mfma_f32_16x16x32_bf16 v[100:103], v[180:183], v[192:195], v[100:103]\n\t"
    "ds_read_b64_tr_b16 v[174:175], v7 offset:12800\n\t" "v_cvt_pk_bf16_f32 v202, v216, v217\n\t" "v_mfma_f32_16x16x32_bf16 v[104:107], v[184:187], v[188:191], v[104:107]\n\t"
    "ds_read_b64_tr_b16 v[176:177], v8 offset:12288\n\t" "v_cvt_pk_bf16_f32 v203, v218, v219\n\t" "v_mfma_f32_16x16x32_bf16 v[108:111], v[184:187], v[192:195], v[108:111]\n\t"
    "ds_read_b64_tr_b16 v[178:179], v8 offset:12800\n\t" "s_waitcnt lgkmcnt(4)\n\t" "v_mfma_f32_16x16x32_bf16 v[112:115], v[188:191], v[156:159], v[112:115]\n\t"
    "ds_read_b64_tr_b16 v[180:181], v9 offset:12288\n\t" "v_add_u32 v12, 0x10000, v12\n\t" "v_mfma_f32_16x16x32_bf16 v[116:119], v[192:195], v[156:159], v[116:119]\n\t"
    "ds_read_b64_tr_b16 v[182:183], v9 offset:12800\n\t" "s_add_u32 m0, %[sl1], 28672\n\t" "v_mfma_f32_16x16x32_bf16 v[148:151], v[228:231], v[156:159], v[148:151]\n\t"
    "ds_read_b128 v[232:235], v1 offset:27648\n\t" "global_load_lds_dwordx4 v12, %[so]\n\t" "v_mfma_f32_16x16x32_bf16 v[120:123], v[188:191], v[160:163], v[120:123]\n\t"
    "ds_read_b64_tr_b16 v[184:185], v10 offset:12288\n\t" "v_mfma_f32_16x16x32_bf16 v[124:127], v[192:195], v[160:163], v[124:127]\n\t" "ds_read_b64_tr_b16 v[186:187], v10 offset:12800\n\t"
    "v_mfma_f32_16x16x32_bf16 v[128:131], v[188:191], v[164:167], v[128:131]\n\t" "ds_read_b64_tr_b16 v[156:157], v7 offset:14336\n\t"
    "v_mfma_f32_16x16x32_bf16 v[132:135], v[192:195], v[164:167], v[132:135]\n\t" "ds_read_b64_tr_b16 v[158:159], v7 offset:14848\n\t"
    "v_mfma_f32_16x16x32_bf16 v[136:139], v[188:191], v[168:171], v[136:139]\n\t" "ds_read_b64_tr_b16 v[160:161], v8 offset:14336\n\t"
    "v_mfma_f32_16x16x32_bf16 v[140:143], v[192:195], v[168:171], v[140:143]\n\t" "ds_read_b64_tr_b16 v[162:163], v8 offset:14848\n\t" "s_waitcnt lgkmcnt(4)\n\t"
    "v_mfma_f32_16x16x32_bf16 v[48:51], v[172:175], v[196:199], v[48:51]\n\t" "ds_read_b64_tr_b16 v[164:165], v9 offset:14336\n\t" "v_add_u32 v12, 0x10000, v12\n\t"
    "v_mfma_f32_16x16x32_bf16 v[52:55], v[172:175], v[200:203], v[52:55]\n\t" "ds_read_b64_tr_b16 v[166:167], v9 offset:14848\n\t" "s_add_u32 m0, %[sl1], 32768\n\t"
    "v_mfma_f32_16x16x32_bf16 v[152:155], v[232:235], v[172:175], v[152:155]\n\t" "global_load_lds_dwordx4 v12, %[so]\n\t" "v_add_u32 v12, 0x10000, v12\n\t"
    "v_mfma_f32_16x16x32_bf16 v[56:59], v[176:179], v[196:199], v[56:59]\n\t" "ds_read_b64_tr_b16 v[168:169], v10 offset:14336\n\t" "v_mfma_f32_4x4x4_16b_bf16 v[204:207], v[220:221], a[224:225], 0\n\t"
    "v_mfma_f32_4x4x4_16b_bf16 v[208:211], v[222:223], a[226:227], 0\n\t" "v_mfma_f32_4x4x4_16b_bf16 v[212:215], v[220:221], a[228:229], 0\n\t"
    "v_mfma_f32_4x4x4_16b_bf16 v[216:219], v[222:223], a[230:231], 0\n\t" "v_mfma_f32_16x16x32_bf16 v[60:63], v[176:179], v[200:203], v[60:63]\n\t" "ds_read_b64_tr_b16 v[170:171], v10 offset:14848\n\t"
    "v_mfma_f32_16x16x32_bf16 v[64:67], v[180:183], v[196:199], v[64:67]\n\t" "ds_read_b64_tr_b16 v[172:173], v7 offset:16384\n\t" "v_mfma_f32_16x16x32_bf16 v[68:71], v[180:183], v[200:203], v[68:71]\n\t"
    "ds_read_b64_tr_b16 v[174:175], v7 offset:16896\n\t" "v_cvt_pk_bf16_f32 v188, v204, v205\n\t" "v_mfma_f32_16x16x32_bf16 v[72:75], v[184:187], v[196:199], v[72:75]\n\t"
    "ds_read_b64_tr_b16 v[176:177], v8 offset:16384\n\t" "v_cvt_pk_bf16_f32 v189, v206, v207\n\t" "v_mfma_f32_16x16x32_bf16 v[76:79], v[184:187], v[200:203], v[76:79]\n\t"
    "ds_read_b64_tr_b16 v[178:179], v8 offset:16896\n\t" "s_waitcnt lgkmcnt(4)\n\t" "v_mfma_f32_16x16x32_bf16 v[80:83], v[156:159], v[196:199], v[80:83]\n\t"
    "ds_read_b64_tr_b16 v[180:181], v9 offset:16384\n\t" "v_cvt_pk_bf16_f32 v190, v208, v209\n\t" "v_mfma_f32_16x16x32_bf16 v[84:87], v[156:159], v[200:203], v[84:87]\n\t"
    "ds_read_b64_tr_b16 v[182:183], v9 offset:16896\n\t" "v_cvt_pk_bf16_f32 v191, v210, v211\n\t" "v_mfma_f32_16x16x32_bf16 v[144:147], v[156:159], v[232:235], v[144:147]\n\t"
    "ds_read_b128 v[224:227], v2 offset:7424\n\t" "s_add_u32 m0, %[sl1], 36864\n\t" "v_mfma_f32_16x16x32_bf16 v[88:91], v[160:163], v[196:199], v[88:91]\n\t"
    "ds_read_b64_tr_b16 v[184:185], v10 offset:16384\n\t" "v_cvt_pk_bf16_f32 v192, v212, v213\n\t" "v_mfma_f32_16x16x32_bf16 v[92:95], v[160:163], v[200:203], v[92:95]\n\t"
    "ds_read_b64_tr_b16 v[186:187], v10 offset:16896\n\t" "v_cvt_pk_bf16_f32 v193, v214, v215\n\t" "v_mfma_f32_16x16x32_bf16 v[96:99], v[164:167], v[196:199], v[96:99]\n\t" "s_waitcnt vmcnt(13)\n\t"
    "s_barrier\n\t" "ds_read_b64_tr_b16 v[156:157], v7 offset:24576\n\t" "v_mfma_f32_16x16x32_bf16 v[100:103], v[164:167], v[200:203], v[100:103]\n\t" "ds_read_b64_tr_b16 v[158:159], v7 offset:25088\n\t"
    "v_cvt_pk_bf16_f32 v194, v216, v217\n\t" "v_mfma_f32_16x16x32_bf16 v[104:107], v[168:171], v[196:199], v[104:107]\n\t" "ds_read_b64_tr_b16 v[160:161], v8 offset:24576\n\t"
    "v_cvt_pk_bf16_f32 v195, v218, v219\n\t" "v_mfma_f32_16x16x32_bf16 v[108:111], v[168:171], v[200:203], v[108:111]\n\t" "ds_read_b64_tr_b16 v[162:163], v8 offset:25088\n\t" "s_waitcnt lgkmcnt(4)\n\t"
    "v_mfma_f32_16x16x32_bf16 v[112:115], v[196:199], v[172:175], v[112:115]\n\t" "ds_read_b64_tr_b16 v[164:165], v9 offset:24576\n\t" "global_load_lds_dwordx4 v12, %[so]\n\t"
    "v_mfma_f32_16x16x32_bf16 v[116:119], v[200:203], v[172:175], v[116:119]\n\t" "ds_read_b64_tr_b16 v[166:167], v9 offset:25088\n\t" "v_add_u32 v12, 0x10000, v12\n\t"
    "v_mfma_f32_16x16x32_bf16 v[148:151], v[232:235], v[172:175], v[148:151]\n\t" "ds_read_b128 v[228:231], v1 offset:28672\n\t" "s_add_u32 m0, %[sl1], 40960\n\t"
    "v_mfma_f32_16x16x32_bf16 v[120:123], v[196:199], v[176:179], v[120:123]\n\t" "ds_read_b64_tr_b16 v[168:169], v10 offset:24576\n\t" "global_load_lds_dwordx4 v12, %[so]\n\t"
    "v_mfma_f32_16x16x32_bf16 v[124:127], v[200:203], v[176:179], v[124:127]\n\t" "ds_read_b64_tr_b16 v[170:171], v10 offset:25088\n\t"
    "v_mfma_f32_16x16x32_bf16 v[128:131], v[196:199], v[180:183], v[128:131]\n\t" "ds_read_b64_tr_b16 v[172:173], v7 offset:26624\n\t"
    "v_mfma_f32_16x16x32_bf16 v[132:135], v[200:203], v[180:183], v[132:135]\n\t" "ds_read_b64_tr_b16 v[174:175], v7 offset:27136\n\t"
    "v_mfma_f32_16x16x32_bf16 v[136:139], v[196:199], v[184:187], v[136:139]\n\t" "ds_read_b64_tr_b16 v[176:177], v8 offset:26624\n\t"
    "v_mfma_f32_16x16x32_bf16 v[140:143], v[200:203], v[184:187], v[140:143]\n\t" "ds_read_b64_tr_b16 v[178:179], v8 offset:27136\n\t" "s_waitcnt lgkmcnt(4)\n\t"
    "v_mfma_f32_16x16x32_bf16 v[48:51], v[156:159], v[188:191], v[48:51]\n\t" "ds_read_b64_tr_b16 v[180:181], v9 offset:26624\n\t" "v_add_u32 v12, 0xfffa0000, v12\n\t"
    "v_mfma_f32_16x16x32_bf16 v[52:55], v[156:159], v[192:195], v[52:55]\n\t" "ds_read_b64_tr_b16 v[182:183], v9 offset:27136\n\t" "s_add_u32 m0, %[sl1], 16384\n\t"
    "v_mfma_f32_16x16x32_bf16 v[152:155], v[228:231], v[156:159], v[152:155]\n\t" "global_load_lds_dwordx4 v12, %[so]\n\t" "v_add_u32 v12, 0x70000, v12\n\t"
    "v_mfma_f32_16x16x32_bf16 v[56:59], v[160:163], v[188:191], v[56:59]\n\t" "ds_read_b64_tr_b16 v[184:185], v10 offset:26624\n\t" "v_mfma_f32_4x4x4_16b_bf16 v[204:207], v[224:225], a[232:233], 0\n\t"
    "v_mfma_f32_4x4x4_16b_bf16 v[208:211], v[226:227], a[234:235], 0\n\t" "v_mfma_f32_4x4x4_16b_bf16 v[212:215], v[224:225], a[236:237], 0\n\t"
    "v_mfma_f32_4x4x4_16b_bf16 v[216:219], v[226:227], a[238:239], 0\n\t" "v_mfma_f32_16x16x32_bf16 v[60:63], v[160:163], v[192:195], v[60:63]\n\t" "ds_read_b64_tr_b16 v[186:187], v10 offset:27136\n\t"
    "v_mfma_f32_16x16x32_bf16 v[64:67], v[164:167], v[188:191], v[64:67]\n\t" "ds_read_b64_tr_b16 v[156:157], v7 offset:28672\n\t" "v_mfma_f32_16x16x32_bf16 v[68:71], v[164:167], v[192:195], v[68:71]\n\t"
    "ds_read_b64_tr_b16 v[158:159], v7 offset:29184\n\t" "v_cvt_pk_bf16_f32 v196, v204, v205\n\t" "v_mfma_f32_16x16x32_bf16 v[72:75], v[168:171], v[188:191], v[72:75]\n\t"
    "ds_read_b64_tr_b16 v[160:161], v8 offset:28672\n\t" "v_cvt_pk_bf16_f32 v197, v206, v207\n\t" "v_mfma_f32_16x16x32_bf16 v[76:79], v[168:171], v[192:195], v[76:79]\n\t"
    "ds_read_b64_tr_b16 v[162:163], v8 offset:29184\n\t" "s_waitcnt lgkmcnt(4)\n\t" "v_mfma_f32_16x16x32_bf16 v[80:83], v[172:175], v[188:191], v[80:83]\n\t"
    "ds_read_b64_tr_b16 v[164:165], v9 offset:28672\n\t" "v_cvt_pk_bf16_f32 v198, v208, v209\n\t" "v_mfma_f32_16x16x32_bf16 v[84:87], v[172:175], v[192:195], v[84:87]\n\t"
    "ds_read_b64_tr_b16 v[166:167], v9 offset:29184\n\t" "v_cvt_pk_bf16_f32 v199, v210, v211\n\t" "v_mfma_f32_16x16x32_bf16 v[144:147], v[172:175], v[228:231], v[144:147]\n\t"
    "ds_read_b128 v[220:223], v2 offset:7680\n\t" "s_add_u32 m0, %[sl1], 45056\n\t" "v_mfma_f32_16x16x32_bf16 v[88:91], v[176:179], v[188:191], v[88:91]\n\t"
    "ds_read_b64_tr_b16 v[168:169], v10 offset:28672\n\t" "v_cvt_pk_bf16_f32 v200, v212, v213\n\t" "v_mfma_f32_16x16x32_bf16 v[92:95], v[176:179], v[192:195], v[92:95]\n\t"
    "ds_read_b64_tr_b16 v[170:171], v10 offset:29184\n\t" "v_cvt_pk_bf16_f32 v201, v214, v215\n\t" "v_mfma_f32_16x16x32_bf16 v[96:99], v[180:183], v[188:191], v[96:99]\n\t"
    "ds_read_b64_tr_b16 v[172:173], v7 offset:36864\n\t" "global_load_lds_dwordx4 v12, %[so]\n\t" "v_mfma_f32_16x16x32_bf16 v[100:103], v[180:183], v[192:195], v[100:103]\n\t"
    "ds_read_b64_tr_b16 v[174:175], v7 offset:37376\n\t" "v_cvt_pk_bf16_f32 v202, v216, v217\n\t" "v_mfma_f32_16x16x32_bf16 v[104:107], v[184:187], v[188:191], v[104:107]\n\t"
    "ds_read_b64_tr_b16 v[176:177], v8 offset:36864\n\t" "v_cvt_pk_bf16_f32 v203, v218, v219\n\t" "v_mfma_f32_16x16x32_bf16 v[108:111], v[184:187], v[192:195], v[108:111]\n\t"
    "ds_read_b64_tr_b16 v[178:179], v8 offset:37376\n\t" "s_waitcnt lgkmcnt(4)\n\t" "v_mfma_f32_16x16x32_bf16 v[112:115], v[188:191], v[156:159], v[112:115]\n\t"
    "ds_read_b64_tr_b16 v[180:181], v9 offset:36864\n\t" "v_add_u32 v12, 0x10000, v12\n\t" "v_mfma_f32_16x16x32_bf16 v[116:119], v[192:195], v[156:159], v[116:119]\n\t"
    "ds_read_b64_tr_b16 v[182:183], v9 offset:37376\n\t" "s_add_u32 m0, %[sl1], 49152\n\t" "v_mfma_f32_16x16x32_bf16 v[148:151], v[228:231], v[156:159], v[148:151]\n\t"
    "ds_read_b128 v[232:235], v1 offset:29696\n\t" "global_load_lds_dwordx4 v12, %[so]\n\t" "v_mfma_f32_16x16x32_bf16 v[120:123], v[188:191], v[160:163], v[120:123]\n\t"
    "ds_read_b64_tr_b16 v[184:185], v10 offset:36864\n\t" "v_add_u32 v12, 0x10000, v12\n\t" "v_mfma_f32_16x16x32_bf16 v[124:127], v[192:195], v[160:163], v[124:127]\n\t"
    "ds_read_b64_tr_b16 v[186:187], v10 offset:37376\n\t" "s_add_u32 m0, %[sl1], 53248\n\t" "v_mfma_f32_16x16x32_bf16 v[128:131], v[188:191], v[164:167], v[128:131]\n\t"
    "ds_read_b64_tr_b16 v[156:157], v7 offset:38912\n\t" "v_mfma_f32_16x16x32_bf16 v[132:135], v[192:195], v[164:167], v[132:135]\n\t" "ds_read_b64_tr_b16 v[158:159], v7 offset:39424\n\t"
    "global_load_lds_dwordx4 v12, %[so]\n\t" "v_mfma_f32_16x16x32_bf16 v[136:139], v[188:191], v[168:171], v[136:139]\n\t" "ds_read_b64_tr_b16 v[160:161], v8 offset:38912\n\t"
    "v_mfma_f32_16x16x32_bf16 v[140:143], v[192:195], v[168:171], v[140:143]\n\t" "ds_read_b64_tr_b16 v[162:163], v8 offset:39424\n\t" "s_waitcnt lgkmcnt(4)\n\t"
    "v_mfma_f32_16x16x32_bf16 v[48:51], v[172:175], v[196:199], v[48:51]\n\t" "ds_read_b64_tr_b16 v[164:165], v9 offset:38912\n\t" "v_add_u32 v12, 0x10000, v12\n\t"
    "v_mfma_f32_16x16x32_bf16 v[52:55], v[172:175], v[200:203], v[52:55]\n\t" "ds_read_b64_tr_b16 v[166:167], v9 offset:39424\n\t" "s_add_u32 m0, %[sl1], 57344\n\t"
    "v_mfma_f32_16x16x32_bf16 v[152:155], v[232:235], v[172:175], v[152:155]\n\t" "global_load_lds_dwordx4 v12, %[so]\n\t" "v_add_u32 v12, 0x10000, v12\n\t"
    "v_mfma_f32_16x16x32_bf16 v[56:59], v[176:179], v[196:199], v[56:59]\n\t" "ds_read_b64_tr_b16 v[168:169], v10 offset:38912\n\t" "v_mfma_f32_4x4x4_16b_bf16 v[204:207], v[220:221], a[240:241], 0\n\t"
    "v_mfma_f32_4x4x4_16b_bf16 v[208:211], v[222:223], a[242:243], 0\n\t" "v_mfma_f32_4x4x4_16b_bf16 v[212:215], v[220:221], a[244:245], 0\n\t"
    "v_mfma_f32_4x4x4_16b_bf16 v[216:219], v[222:223], a[246:247], 0\n\t" "v_mfma_f32_16x16x32_bf16 v[60:63], v[176:179], v[200:203], v[60:63]\n\t" "ds_read_b64_tr_b16 v[170:171], v10 offset:39424\n\t"
    "v_mfma_f32_16x16x32_bf16 v[64:67], v[180:183], v[196:199], v[64:67]\n\t" "ds_read_b64_tr_b16 v[172:173], v7 offset:40960\n\t" "v_mfma_f32_16x16x32_bf16 v[68:71], v[180:183], v[200:203], v[68:71]\n\t"
    "ds_read_b64_tr_b16 v[174:175], v7 offset:41472\n\t" "v_cvt_pk_bf16_f32 v188, v204, v205\n\t" "v_mfma_f32_16x16x32_bf16 v[72:75], v[184:187], v[196:199], v[72:75]\n\t"
    "ds_read_b64_tr_b16 v[176:177], v8 offset:40960\n\t" "v_cvt_pk_bf16_f32 v189, v206, v207\n\t" "v_mfma_f32_16x16x32_bf16 v[76:79], v[184:187], v[200:203], v[76:79]\n\t"
    "ds_read_b64_tr_b16 v[178:179], v8 offset:41472\n\t" "s_waitcnt lgkmcnt(4)\n\t" "v_mfma_f32_16x16x32_bf16 v[80:83], v[156:159], v[196:199], v[80:83]\n\t"
    "ds_read_b64_tr_b16 v[180:181], v9 offset:40960\n\t" "v_cvt_pk_bf16_f32 v190, v208, v209\n\t" "v_mfma_f32_16x16x32_bf16 v[84:87], v[156:159], v[200:203], v[84:87]\n\t"
    "ds_read_b64_tr_b16 v[182:183], v9 offset:41472\n\t" "v_cvt_pk_bf16_f32 v191, v210, v211\n\t" "v_mfma_f32_16x16x32_bf16 v[144:147], v[156:159], v[232:235], v[144:147]\n\t"
    "ds_read_b128 v[224:227], v2 offset:7936\n\t" "s_add_u32 m0, %[sl1], 61440\n\t" "v_mfma_f32_16x16x32_bf16 v[88:91], v[160:163], v[196:199], v[88:91]\n\t"
    "ds_read_b64_tr_b16 v[184:185], v10 offset:40960\n\t" "v_cvt_pk_bf16_f32 v192, v212, v213\n\t" "v_mfma_f32_16x16x32_bf16 v[92:95], v[160:163], v[200:203], v[92:95]\n\t"
    "ds_read_b64_tr_b16 v[186:187], v10 offset:41472\n\t" "v_cvt_pk_bf16_f32 v193, v214, v215\n\t" "v_mfma_f32_16x16x32_bf16 v[96:99], v[164:167], v[196:199], v[96:99]\n\t" "s_waitcnt vmcnt(12)\n\t"
    "s_barrier\n\t" "ds_read_b64_tr_b16 v[156:157], v3 offset:51200\n\t" "v_mfma_f32_16x16x32_bf16 v[100:103], v[164:167], v[200:203], v[100:103]\n\t" "ds_read_b64_tr_b16 v[158:159], v3 offset:51712\n\t"
    "v_cvt_pk_bf16_f32 v194, v216, v217\n\t" "v_mfma_f32_16x16x32_bf16 v[104:107], v[168:171], v[196:199], v[104:107]\n\t" "ds_read_b64_tr_b16 v[160:161], v3 offset:53248\n\t"
    "v_cvt_pk_bf16_f32 v195, v218, v219\n\t" "v_mfma_f32_16x16x32_bf16 v[108:111], v[168:171], v[200:203], v[108:111]\n\t" "ds_read_b64_tr_b16 v[162:163], v3 offset:53760\n\t" "s_waitcnt lgkmcnt(4)\n\t"
    "v_mfma_f32_16x16x32_bf16 v[112:115], v[196:199], v[172:175], v[112:115]\n\t" "ds_read_b64_tr_b16 v[164:165], v3 offset:49152\n\t" "global_load_lds_dwordx4 v12, %[so]\n\t"
    "v_mfma_f32_16x16x32_bf16 v[116:119], v[200:203], v[172:175], v[116:119]\n\t" "ds_read_b64_tr_b16 v[166:167], v3 offset:49664\n\t" "v_add_u32 v12, 0x10000, v12\n\t"
    "v_mfma_f32_16x16x32_bf16 v[148:151], v[232:235], v[172:175], v[148:151]\n\t" "ds_read_b128 v[228:231], v1 offset:30720\n\t" "s_add_u32 m0, %[sl1], 65536\n\t"
    "v_mfma_f32_16x16x32_bf16 v[120:123], v[196:199], v[176:179], v[120:123]\n\t" "ds_read_b64_tr_b16 v[168:169], v7 offset:2048\n\t" "ds_read_b128 v[232:235], v1 offset:31744\n\t"
    "v_mfma_f32_16x16x32_bf16 v[124:127], v[200:203], v[176:179], v[124:127]\n\t" "ds_read_b64_tr_b16 v[170:171], v7 offset:2560\n\t" "global_load_lds_dwordx4 v12, %[so]\n\t"
    "v_mfma_f32_16x16x32_bf16 v[128:131], v[196:199], v[180:183], v[128:131]\n\t" "ds_read_b64_tr_b16 v[172:173], v7 offset:4096\n\t"
    "v_mfma_f32_16x16x32_bf16 v[132:135], v[200:203], v[180:183], v[132:135]\n\t" "ds_read_b64_tr_b16 v[174:175], v7 offset:4608\n\t"
    "v_mfma_f32_16x16x32_bf16 v[136:139], v[196:199], v[184:187], v[136:139]\n\t" "ds_read_b64_tr_b16 v[176:177], v7 offset:0\n\t"
    "v_mfma_f32_16x16x32_bf16 v[140:143], v[200:203], v[184:187], v[140:143]\n\t" "ds_read_b64_tr_b16 v[178:179], v7 offset:512\n\t" "s_waitcnt lgkmcnt(4)\n\t"
    "v_mfma_f32_16x16x32_bf16 v[80:83], v[156:159], v[188:191], v[80:83]\n\t" "ds_read_b64_tr_b16 v[180:181], v4 offset:49152\n\t" "v_add_u32 v12, 0x70000, v12\n\t"
    "v_mfma_f32_16x16x32_bf16 v[84:87], v[156:159], v[192:195], v[84:87]\n\t" "ds_read_b64_tr_b16 v[182:183], v4 offset:49664\n\t" "s_add_u32 m0, %[sl1], 94208\n\t"
    "v_mfma_f32_16x16x32_bf16 v[144:147], v[156:159], v[228:231], v[144:147]\n\t" "global_load_lds_dwordx4 v12, %[so]\n\t" "v_add_u32 v12, 0x10000, v12\n\t"
    "v_mfma_f32_16x16x32_bf16 v[112:115], v[188:191], v[160:163], v[112:115]\n\t" "ds_read_b64_tr_b16 v[184:185], v5 offset:49152\n\t" "v_mfma_f32_4x4x4_16b_bf16 v[204:207], v[224:225], a[248:249], 0\n\t"
    "v_mfma_f32_4x4x4_16b_bf16 v[208:211], v[226:227], a[250:251], 0\n\t" "v_mfma_f32_4x4x4_16b_bf16 v[212:215], v[224:225], a[252:253], 0\n\t"
    "v_mfma_f32_4x4x4_16b_bf16 v[216:219], v[226:227], a[254:255], 0\n\t" "v_mfma_f32_16x16x32_bf16 v[116:119], v[192:195], v[160:163], v[116:119]\n\t" "ds_read_b64_tr_b16 v[186:187], v5 offset:49664\n\t"
    "v_mfma_f32_16x16x32_bf16 v[148:151], v[228:231], v[160:163], v[148:151]\n\t" "v_mfma_f32_16x16x32_bf16 v[48:51], v[164:167], v[188:191], v[48:51]\n\t"
    "ds_read_b64_tr_b16 v[156:157], v6 offset:49152\n\t" "v_cvt_pk_bf16_f32 v196, v204, v205\n\t" "v_mfma_f32_16x16x32_bf16 v[52:55], v[164:167], v[192:195], v[52:55]\n\t"
    "ds_read_b64_tr_b16 v[158:159], v6 offset:49664\n\t" "v_cvt_pk_bf16_f32 v197, v206, v207\n\t" "v_mfma_f32_16x16x32_bf16 v[152:155], v[228:231], v[164:167], v[152:155]\n\t"
    "s_add_u32 m0, %[sl1], 98304\n\t" "v_mfma_f32_16x16x32_bf16 v[144:147], v[168:171], v[232:235], v[144:147]\n\t" "ds_read_b64_tr_b16 v[160:161], v4 offset:51200\n\t"
    "ds_read_b64_tr_b16 v[162:163], v4 offset:51712\n\t" "v_cvt_pk_bf16_f32 v198, v208, v209\n\t" "s_waitcnt lgkmcnt(4)\n\t" "v_mfma_f32_16x16x32_bf16 v[148:151], v[232:235], v[172:175], v[148:151]\n\t"
    "ds_read_b64_tr_b16 v[164:165], v5 offset:51200\n\t" "ds_read_b64_tr_b16 v[166:167], v5 offset:51712\n\t" "v_cvt_pk_bf16_f32 v199, v210, v211\n\t"
    "v_mfma_f32_16x16x32_bf16 v[152:155], v[232:235], v[176:179], v[152:155]\n\t" "ds_read_b64_tr_b16 v[168:169], v6 offset:51200\n\t" "ds_read_b64_tr_b16 v[170:171], v6 offset:51712\n\t"
    "v_mfma_f32_16x16x32_bf16 v[56:59], v[180:183], v[188:191], v[56:59]\n\t" "ds_read_b64_tr_b16 v[172:173], v4 offset:53248\n\t" "v_cvt_pk_bf16_f32 v200, v212, v213\n\t"
    "v_mfma_f32_16x16x32_bf16 v[60:63], v[180:183], v[192:195], v[60:63]\n\t" "ds_read_b64_tr_b16 v[174:175], v4 offset:53760\n\t" "v_cvt_pk_bf16_f32 v201, v214, v215\n\t"
    "v_mfma_f32_16x16x32_bf16 v[64:67], v[184:187], v[188:191], v[64:67]\n\t" "ds_read_b64_tr_b16 v[176:177], v5 offset:53248\n\t" "global_load_lds_dwordx4 v12, %[so]\n\t"
    "v_mfma_f32_16x16x32_bf16 v[68:71], v[184:187], v[192:195], v[68:71]\n\t" "ds_read_b64_tr_b16 v[178:179], v5 offset:53760\n\t" "v_cvt_pk_bf16_f32 v202, v216, v217\n\t" "s_waitcnt lgkmcnt(4)\n\t"
    "v_mfma_f32_16x16x32_bf16 v[72:75], v[156:159], v[188:191], v[72:75]\n\t" "ds_read_b64_tr_b16 v[180:181], v6 offset:53248\n\t" "v_cvt_pk_bf16_f32 v203, v218, v219\n\t"
    "v_mfma_f32_16x16x32_bf16 v[76:79], v[156:159], v[192:195], v[76:79]\n\t" "ds_read_b64_tr_b16 v[182:183], v6 offset:53760\n\t" "v_add_u32 v12, 0x10000, v12\n\t"
    "v_mfma_f32_16x16x32_bf16 v[88:91], v[160:163], v[188:191], v[88:91]\n\t" "ds_read_b64_tr_b16 v[184:185], v7 offset:2048\n\t" "s_add_u32 m0, %[sl1], 102400\n\t"
    "v_mfma_f32_16x16x32_bf16 v[92:95], v[160:163], v[192:195], v[92:95]\n\t" "ds_read_b64_tr_b16 v[186:187], v7 offset:2560\n\t" "v_and_b32 v242, 0xffff0000, v144\n\t"
    "v_mfma_f32_16x16x32_bf16 v[96:99], v[164:167], v[188:191], v[96:99]\n\t" "ds_read_b64_tr_b16 v[156:157], v8 offset:2048\n\t" "global_load_lds_dwordx4 v12, %[so]\n\t"
    "v_mfma_f32_16x16x32_bf16 v[100:103], v[164:167], v[192:195], v[100:103]\n\t" "ds_read_b64_tr_b16 v[158:159], v8 offset:2560\n\t" "v_sub_f32 v18, v144, v242\n\t"
    "v_mfma_f32_16x16x32_bf16 v[104:107], v[168:171], v[188:191], v[104:107]\n\t" "ds_read_b64_tr_b16 v[160:161], v9 offset:2048\n\t" "v_cvt_pk_bf16_f32 v242, v242, v18\n\t"
    "v_mfma_f32_16x16x32_bf16 v[108:111], v[168:171], v[192:195], v[108:111]\n\t" "ds_read_b64_tr_b16 v[162:163], v9 offset:2560\n\t" "s_waitcnt lgkmcnt(4)\n\t"
    "v_mfma_f32_16x16x32_bf16 v[120:123], v[188:191], v[172:175], v[120:123]\n\t" "ds_read_b64_tr_b16 v[164:165], v10 offset:2048\n\t" "v_and_b32 v243, 0xffff0000, v145\n\t"
    "v_mfma_f32_16x16x32_bf16 v[124:127], v[192:195], v[172:175], v[124:127]\n\t" "ds_read_b64_tr_b16 v[166:167], v10 offset:2560\n\t" "v_sub_f32 v18, v145, v243\n\t"
    "v_mfma_f32_16x16x32_bf16 v[128:131], v[188:191], v[176:179], v[128:131]\n\t" "ds_read_b64_tr_b16 v[168:169], v7 offset:4096\n\t" "v_cvt_pk_bf16_f32 v243, v243, v18\n\t"
    "v_mfma_f32_16x16x32_bf16 v[132:135], v[192:195], v[176:179], v[132:135]\n\t" "ds_read_b64_tr_b16 v[170:171], v7 offset:4608\n\t" "ds_write2_b32 v34, v242, v243 offset0:0 offset1:2\n\t"
    "v_mfma_f32_16x16x32_bf16 v[136:139], v[188:191], v[180:183], v[136:139]\n\t" "ds_read_b64_tr_b16 v[172:173], v8 offset:4096\n\t" "v_and_b32 v244, 0xffff0000, v146\n\t"
    "v_mfma_f32_16x16x32_bf16 v[140:143], v[192:195], v[180:183], v[140:143]\n\t" "ds_read_b64_tr_b16 v[174:175], v8 offset:4608\n\t" "v_sub_f32 v18, v146, v244\n\t"
    "v_mfma_f32_16x16x32_bf16 v[80:83], v[184:187], v[196:199], v[80:83]\n\t" "ds_read_b64_tr_b16 v[176:177], v9 offset:4096\n\t" "v_cvt_pk_bf16_f32 v244, v244, v18\n\t"
    "v_mfma_f32_16x16x32_bf16 v[84:87], v[184:187], v[200:203], v[84:87]\n\t" "ds_read_b64_tr_b16 v[178:179], v9 offset:4608\n\t" "s_waitcnt lgkmcnt(5)\n\t"
    "v_mfma_f32_16x16x32_bf16 v[88:91], v[156:159], v[196:199], v[88:91]\n\t" "ds_read_b64_tr_b16 v[180:181], v10 offset:4096\n\t" "v_and_b32 v245, 0xffff0000, v147\n\t"
    "v_mfma_f32_16x16x32_bf16 v[92:95], v[156:159], v[200:203], v[92:95]\n\t" "ds_read_b64_tr_b16 v[182:183], v10 offset:4608\n\t" "v_sub_f32 v18, v147, v245\n\t"
    "v_mfma_f32_16x16x32_bf16 v[96:99], v[160:163], v[196:199], v[96:99]\n\t" "ds_read_b64_tr_b16 v[184:185], v7 offset:0\n\t" "v_cvt_pk_bf16_f32 v245, v245, v18\n\t"
    "v_mfma_f32_16x16x32_bf16 v[100:103], v[160:163], v[200:203], v[100:103]\n\t" "ds_read_b64_tr_b16 v[186:187], v7 offset:512\n\t" "ds_write2_b32 v34, v244, v245 offset0:4 offset1:6\n\t"
    "v_mfma_f32_16x16x32_bf16 v[104:107], v[164:167], v[196:199], v[104:107]\n\t" "ds_read_b64_tr_b16 v[156:157], v8 offset:0\n\t" "v_and_b32 v238, 0xffff0000, v148\n\t"
    "v_mfma_f32_16x16x32_bf16 v[108:111], v[164:167], v[200:203], v[108:111]\n\t" "ds_read_b64_tr_b16 v[158:159], v8 offset:512\n\t" "v_sub_f32 v18, v148, v238\n\t"
    "v_mfma_f32_16x16x32_bf16 v[112:115], v[196:199], v[168:171], v[112:115]\n\t" "ds_read_b64_tr_b16 v[160:161], v9 offset:0\n\t" "v_cvt_pk_bf16_f32 v238, v238, v18\n\t"
    "v_mfma_f32_16x16x32_bf16 v[116:119], v[200:203], v[168:171], v[116:119]\n\t" "ds_read_b64_tr_b16 v[162:163], v9 offset:512\n\t" "s_waitcnt lgkmcnt(5)\n\t"
    "v_mfma_f32_16x16x32_bf16 v[120:123], v[196:199], v[172:175], v[120:123]\n\t" "ds_read_b64_tr_b16 v[164:165], v10 offset:0\n\t" "v_and_b32 v239, 0xffff0000, v149\n\t"
    "v_mfma_f32_16x16x32_bf16 v[124:127], v[200:203], v[172:175], v[124:127]\n\t" "ds_read_b64_tr_b16 v[166:167], v10 offset:512\n\t" "global_load_dword a64, v16, %[pf]\n\t"
    "v_mfma_f32_16x16x32_bf16 v[128:131], v[196:199], v[176:179], v[128:131]\n\t" "v_sub_f32 v18, v149, v239\n\t" "v_cvt_pk_bf16_f32 v239, v239, v18\n\t"
    "v_mfma_f32_16x16x32_bf16 v[132:135], v[200:203], v[176:179], v[132:135]\n\t" "v_and_b32 v240, 0xffff0000, v152\n\t" "v_sub_f32 v18, v152, v240\n\t"
    "v_mfma_f32_16x16x32_bf16 v[136:139], v[196:199], v[180:183], v[136:139]\n\t" "v_cvt_pk_bf16_f32 v240, v240, v18\n\t" "v_and_b32 v241, 0xffff0000, v153\n\t"
    "v_mfma_f32_16x16x32_bf16 v[140:143], v[200:203], v[180:183], v[140:143]\n\t" "v_sub_f32 v18, v153, v241\n\t" "v_cvt_pk_bf16_f32 v241, v241, v18\n\t"
    "v_mfma_f32_16x16x32_bf16 v[48:51], v[184:187], v[196:199], v[48:51]\n\t" "ds_write2st64_b64 v35, v[238:239], v[240:241] offset0:2 offset1:0\n\t"
    "v_mfma_f32_16x16x32_bf16 v[52:55], v[184:187], v[200:203], v[52:55]\n\t" "s_waitcnt lgkmcnt(1)\n\t" "v_mfma_f32_16x16x32_bf16 v[56:59], v[156:159], v[196:199], v[56:59]\n\t"
    "s_waitcnt lgkmcnt(0)\n\t" "s_barrier\n\t" "v_mfma_f32_16x16x32_bf16 v[60:63], v[156:159], v[200:203], v[60:63]\n\t" "ds_read2st64_b64 v[188:191], v36 offset0:1 offset1:2\n\t"
    "ds_read2st64_b64 v[192:195], v37 offset0:1 offset1:2\n\t" "v_mfma_f32_16x16x32_bf16 v[64:67], v[160:163], v[196:199], v[64:67]\n\t" "ds_read2st64_b64 v[220:223], v38 offset0:1 offset1:2\n\t"
    "ds_read2st64_b64 v[224:227], v39 offset0:1 offset1:2\n\t" "v_mfma_f32_16x16x32_bf16 v[68:71], v[160:163], v[200:203], v[68:71]\n\t" "ds_read_b64 v[228:229], v36\n\t" "ds_read_b64 v[230:231], v37\n\t"
    "v_mfma_f32_16x16x32_bf16 v[72:75], v[164:167], v[196:199], v[72:75]\n\t" "ds_read_b64 v[232:233], v38\n\t" "ds_read_b64 v[234:235], v39\n\t"
    "v_mfma_f32_16x16x32_bf16 v[76:79], v[164:167], v[200:203], v[76:79]\n\t" "s_waitcnt lgkmcnt(4)\n\t" "v_mfma_f32_16x16x16_bf16 v[84:87], v[188:189], v[248:249], v[84:87]\n\t" "s_nop 1\n\t"
    "v_mfma_f32_16x16x16_bf16 v[92:95], v[192:193], v[248:249], v[92:95]\n\t" "s_nop 1\n\t" "v_mfma_f32_16x16x16_bf16 v[100:103], v[220:221], v[248:249], v[100:103]\n\t" "s_nop 1\n\t"
    "v_mfma_f32_16x16x16_bf16 v[108:111], v[224:225], v[248:249], v[108:111]\n\t" "v_cvt_pk_bf16_f32 v164, v84, v85\n\t" "v_cvt_pk_bf16_f32 v165, v86, v87\n\t"
    "v_mfma_f32_16x16x16_bf16 v[144:147], v[188:189], v[250:251], v[144:147]\n\t" "v_cvt_pk_bf16_f32 v166, v92, v93\n\t" "v_cvt_pk_bf16_f32 v167, v94, v95\n\t"
    "v_mfma_f32_16x16x16_bf16 v[116:119], v[248:249], v[190:191], v[116:119]\n\t" "ds_write2_b64 v40, v[164:165], v[166:167] offset0:1 offset1:0\n\t" "v_cvt_pk_bf16_f32 v168, v100, v101\n\t"
    "v_mfma_f32_16x16x16_bf16 v[124:127], v[248:249], v[194:195], v[124:127]\n\t" "v_cvt_pk_bf16_f32 v169, v102, v103\n\t" "v_cvt_pk_bf16_f32 v170, v108, v109\n\t"
    "v_mfma_f32_16x16x16_bf16 v[132:135], v[248:249], v[222:223], v[132:135]\n\t" "v_cvt_pk_bf16_f32 v171, v110, v111\n\t" "ds_write2_b64 v41, v[168:169], v[170:171] offset0:1 offset1:0\n\t"
    "v_mfma_f32_16x16x16_bf16 v[140:143], v[248:249], v[226:227], v[140:143]\n\t" "v_cvt_pk_bf16_f32 v144, v144, v145\n\t" "v_cvt_pk_bf16_f32 v145, v146, v147\n\t"
    "v_mfma_f32_16x16x16_bf16 v[148:151], v[250:251], v[190:191], v[148:151]\n\t" "v_cvt_pk_bf16_f32 v200, v116, v117\n\t" "v_cvt_pk_bf16_f32 v201, v118, v119\n\t"
    "v_mfma_f32_16x16x16_bf16 v[80:83], v[188:189], v[246:247], v[80:83]\n\t" "v_cvt_pk_bf16_f32 v206, v124, v125\n\t" "v_cvt_pk_bf16_f32 v207, v126, v127\n\t"
    "v_mfma_f32_16x16x16_bf16 v[88:91], v[192:193], v[246:247], v[88:91]\n\t" "ds_write2_b64 v40, v[200:201], v[206:207] offset0:3 offset1:2\n\t" "v_cvt_pk_bf16_f32 v212, v132, v133\n\t"
    "v_mfma_f32_16x16x16_bf16 v[96:99], v[220:221], v[246:247], v[96:99]\n\t" "v_cvt_pk_bf16_f32 v213, v134, v135\n\t" "v_cvt_pk_bf16_f32 v218, v140, v141\n\t"
    "v_mfma_f32_16x16x16_bf16 v[104:107], v[224:225], v[246:247], v[104:107]\n\t" "v_cvt_pk_bf16_f32 v219, v142, v143\n\t" "ds_write2_b64 v41, v[212:213], v[218:219] offset0:3 offset1:2\n\t"
    "s_waitcnt lgkmcnt(4)\n\t" "v_mfma_f32_16x16x16_bf16 v[52:55], v[228:229], v[248:249], v[52:55]\n\t" "v_cvt_pk_bf16_f32 v148, v148, v149\n\t" "v_cvt_pk_bf16_f32 v149, v150, v151\n\t"
    "v_mfma_f32_16x16x16_bf16 v[60:63], v[230:231], v[248:249], v[60:63]\n\t" "ds_write2_b64 v42, v[144:145], v[148:149] offset0:0 offset1:2\n\t" "v_cvt_pk_bf16_f32 v156, v80, v81\n\t"
    "v_mfma_f32_16x16x16_bf16 v[68:71], v[232:233], v[248:249], v[68:71]\n\t" "v_cvt_pk_bf16_f32 v157, v82, v83\n\t" "v_cvt_pk_bf16_f32 v158, v88, v89\n\t"
    "v_mfma_f32_16x16x16_bf16 v[76:79], v[234:235], v[248:249], v[76:79]\n\t" "v_cvt_pk_bf16_f32 v159, v90, v91\n\t" "v_cvt_pk_bf16_f32 v160, v96, v97\n\t"
    "v_mfma_f32_16x16x16_bf16 v[48:51], v[228:229], v[246:247], v[48:51]\n\t" "v_cvt_pk_bf16_f32 v161, v98, v99\n\t" "v_cvt_pk_bf16_f32 v162, v104, v105\n\t"
    "v_mfma_f32_16x16x16_bf16 v[56:59], v[230:231], v[246:247], v[56:59]\n\t" "v_cvt_pk_bf16_f32 v163, v106, v107\n\t" "s_waitcnt lgkmcnt(0)\n\t" "s_barrier\n\t"
    "v_mfma_f32_16x16x16_bf16 v[64:67], v[232:233], v[246:247], v[64:67]\n\t" "ds_read_b128 v[144:147], v43 offset:0\n\t" "ds_read_b128 v[148:151], v43 offset:2048\n\t"
    "v_mfma_f32_16x16x16_bf16 v[72:75], v[234:235], v[246:247], v[72:75]\n\t" "ds_read_b64 v[196:197], v43 offset:16\n\t" "ds_read_b64 v[202:203], v43 offset:24\n\t"
    "v_mfma_f32_16x16x16_bf16 v[112:115], v[246:247], v[190:191], v[112:115]\n\t" "ds_read_b64 v[208:209], v43 offset:2064\n\t" "ds_read_b64 v[214:215], v43 offset:2072\n\t"
    "v_mfma_f32_16x16x16_bf16 v[120:123], v[246:247], v[194:195], v[120:123]\n\t" "v_cvt_pk_bf16_f32 v180, v52, v53\n\t" "v_cvt_pk_bf16_f32 v181, v54, v55\n\t"
    "v_mfma_f32_16x16x16_bf16 v[128:131], v[246:247], v[222:223], v[128:131]\n\t" "v_cvt_pk_bf16_f32 v182, v60, v61\n\t" "v_cvt_pk_bf16_f32 v183, v62, v63\n\t"
    "v_mfma_f32_16x16x16_bf16 v[136:139], v[246:247], v[226:227], v[136:139]\n\t" "v_cvt_pk_bf16_f32 v184, v68, v69\n\t" "v_cvt_pk_bf16_f32 v185, v70, v71\n\t"
    "v_mfma_f32_16x16x32_bf16 v[80:83], v[156:159], v[180:183], 0\n\t" "v_cvt_pk_bf16_f32 v186, v76, v77\n\t" "v_cvt_pk_bf16_f32 v187, v78, v79\n\t"
    "v_mfma_f32_16x16x32_bf16 v[84:87], v[164:167], v[180:183], 0\n\t" "v_cvt_pk_bf16_f32 v172, v48, v49\n\t" "v_cvt_pk_bf16_f32 v173, v50, v51\n\t"
    "v_mfma_f32_16x16x32_bf16 v[88:91], v[160:163], v[184:187], 0\n\t" "v_cvt_pk_bf16_f32 v174, v56, v57\n\t" "v_cvt_pk_bf16_f32 v175, v58, v59\n\t"
    "v_mfma_f32_16x16x32_bf16 v[92:95], v[168:171], v[184:187], 0\n\t" "v_cvt_pk_bf16_f32 v176, v64, v65\n\t" "v_cvt_pk_bf16_f32 v177, v66, v67\n\t" "s_waitcnt lgkmcnt(4)\n\t"
    "v_mfma_f32_16x16x32_bf16 v[96:99], v[144:147], v[172:175], 0\n\t" "v_cvt_pk_bf16_f32 v178, v72, v73\n\t" "v_cvt_pk_bf16_f32 v179, v74, v75\n\t"
    "v_mfma_f32_16x16x32_bf16 v[100:103], v[156:159], v[172:175], 0\n\t" "v_cvt_pk_bf16_f32 v198, v112, v113\n\t" "v_cvt_pk_bf16_f32 v199, v114, v115\n\t"
    "v_mfma_f32_16x16x32_bf16 v[104:107], v[148:151], v[176:179], 0\n\t" "v_cvt_pk_bf16_f32 v204, v120, v121\n\t" "v_cvt_pk_bf16_f32 v205, v122, v123\n\t"
    "v_mfma_f32_16x16x32_bf16 v[108:111], v[160:163], v[176:179], 0\n\t" "v_cvt_pk_bf16_f32 v210, v128, v129\n\t" "v_cvt_pk_bf16_f32 v211, v130, v131\n\t" "v_cvt_pk_bf16_f32 v216, v136, v137\n\t"
    "v_cvt_pk_bf16_f32 v217, v138, v139\n\t" "v_cndmask_b32 v48, v80, v81, s[50:51]\n\t" "v_cndmask_b32 v64, v84, v85, s[50:51]\n\t" "v_cndmask_b32 v49, v82, v83, s[50:51]\n\t"
    "v_cndmask_b32 v65, v86, v87, s[50:51]\n\t" "v_cndmask_b32 v48, v48, v64, s[52:53]\n\t" "v_cndmask_b32 v49, v49, v65, s[54:55]\n\t" "v_mul_f32 v48, v48, v30\n\t" "v_mul_f32 v49, v49, v31\n\t"
    "v_max_f32 v56, v48, v49\n\t" "v_cndmask_b32 v50, v88, v89, s[50:51]\n\t" "v_cndmask_b32 v64, v92, v93, s[50:51]\n\t" "v_cndmask_b32 v51, v90, v91, s[50:51]\n\t"
    "v_cndmask_b32 v65, v94, v95, s[50:51]\n\t" "v_cndmask_b32 v50, v50, v64, s[52:53]\n\t" "v_cndmask_b32 v51, v51, v65, s[54:55]\n\t" "v_mul_f32 v50, v50, v30\n\t" "v_mul_f32 v51, v51, v31\n\t"
    "v_max_f32 v57, v50, v51\n\t" "v_cndmask_b32 v52, v96, v97, s[50:51]\n\t" "v_cndmask_b32 v64, v100, v101, s[50:51]\n\t" "v_cndmask_b32 v53, v98, v99, s[50:51]\n\t"
    "v_cndmask_b32 v65, v102, v103, s[50:51]\n\t" "v_cndmask_b32 v52, v52, v64, s[52:53]\n\t" "v_cndmask_b32 v53, v53, v65, s[54:55]\n\t" "v_fma_f32 v52, v52, v28, v32\n\t"
    "v_fma_f32 v53, v53, v29, v33\n\t" "v_max_f32 v58, v52, v53\n\t" "v_cndmask_b32 v54, v104, v105, s[50:51]\n\t" "v_cndmask_b32 v64, v108, v109, s[50:51]\n\t"
    "v_cndmask_b32 v55, v106, v107, s[50:51]\n\t" "v_cndmask_b32 v65, v110, v111, s[50:51]\n\t" "v_cndmask_b32 v54, v54, v64, s[52:53]\n\t" "v_cndmask_b32 v55, v55, v65, s[54:55]\n\t"
    "v_fma_f32 v54, v54, v28, v32\n\t" "v_fma_f32 v55, v55, v29, v33\n\t" "v_max_f32 v59, v54, v55\n\t" "v_permlane16_swap_b32 v56, v57\n\t" "s_nop 0\n\t" "v_permlane16_swap_b32 v58, v59\n\t"
    "v_max_f32 v56, v56, v57\n\t" "v_max_f32 v58, v58, v59\n\t" "s_nop 0\n\t" "s_nop 0\n\t" "v_permlane32_swap_b32 v56, v58\n\t" "v_max_f32 v56, v56, v58\n\t" "v_mov_b32 v58, v56\n\t" "s_nop 0\n\t"
    "s_nop 0\n\t" "v_permlane32_swap_b32 v56, v58\n\t" "v_mov_b32 v57, v56\n\t" "v_mov_b32 v59, v58\n\t" "s_nop 0\n\t" "v_permlane16_swap_b32 v56, v57\n\t" "v_permlane16_swap_b32 v58, v59\n\t"
    "v_sub_f32 v48, v48, v56\n\t" "v_sub_f32 v49, v49, v56\n\t" "v_sub_f32 v50, v50, v57\n\t" "v_sub_f32 v51, v51, v57\n\t" "v_sub_f32 v52, v52, v58\n\t" "v_sub_f32 v53, v53, v58\n\t"
    "v_sub_f32 v54, v54, v59\n\t" "v_sub_f32 v55, v55, v59\n\t" "v_exp_f32 v48, v48\n\t" "v_exp_f32 v49, v49\n\t" "v_exp_f32 v50, v50\n\t" "v_exp_f32 v51, v51\n\t" "v_exp_f32 v52, v52\n\t"
    "v_exp_f32 v53, v53\n\t" "v_exp_f32 v54, v54\n\t" "v_exp_f32 v55, v55\n\t" "v_add_f32 v56, v48, v49\n\t" "v_add_f32 v57, v50, v51\n\t" "v_add_f32 v58, v52, v53\n\t" "v_add_f32 v59, v54, v55\n\t"
    "v_permlane16_swap_b32 v56, v57\n\t" "v_mul_f32 v48, v48, v26\n\t" "v_permlane16_swap_b32 v58, v59\n\t" "v_add_f32 v56, v56, v57\n\t" "v_add_f32 v58, v58, v59\n\t" "v_mul_f32 v49, v49, v27\n\t"
    "v_mul_f32 v50, v50, v26\n\t" "v_permlane32_swap_b32 v56, v58\n\t" "v_add_f32 v56, v56, v58\n\t" "v_rcp_f32 v56, v56\n\t" "v_mul_f32 v51, v51, v27\n\t" "v_mov_b32 v58, v56\n\t"
    "v_mul_f32 v52, v52, v24\n\t" "v_mul_f32 v53, v53, v25\n\t" "v_permlane32_swap_b32 v56, v58\n\t" "v_mov_b32 v57, v56\n\t" "v_mov_b32 v59, v58\n\t" "v_mul_f32 v54, v54, v24\n\t"
    "v_permlane16_swap_b32 v56, v57\n\t" "v_permlane16_swap_b32 v58, v59\n\t" "v_mul_f32 v55, v55, v25\n\t" "s_waitcnt lgkmcnt(0)\n\t" "v_mul_f32 v48, v48, v56\n\t" "v_mul_f32 v49, v49, v56\n\t"
    "v_cvt_pk_bf16_f32 v48, v48, v49\n\t" "v_perm_b32 v112, v48, v48, v20\n\t" "v_perm_b32 v113, v48, v48, v21\n\t" "v_perm_b32 v114, v48, v48, v22\n\t" "v_perm_b32 v115, v48, v48, v23\n\t"
    "v_mul_f32 v50, v50, v57\n\t" "v_mul_f32 v51, v51, v57\n\t" "v_cvt_pk_bf16_f32 v50, v50, v51\n\t" "v_perm_b32 v116, v50, v50, v20\n\t" "v_perm_b32 v117, v50, v50, v21\n\t"
    "v_perm_b32 v118, v50, v50, v22\n\t" "v_perm_b32 v119, v50, v50, v23\n\t" "v_mfma_f32_16x16x32_bf16 v[80:83], v[198:201], v[112:115], 0\n\t"
    "v_mfma_f32_16x16x32_bf16 v[84:87], v[204:207], v[112:115], 0\n\t" "v_mul_f32 v52, v52, v58\n\t" "v_mul_f32 v53, v53, v58\n\t" "v_cvt_pk_bf16_f32 v52, v52, v53\n\t"
    "v_perm_b32 v120, v52, v52, v20\n\t" "v_perm_b32 v121, v52, v52, v21\n\t" "v_perm_b32 v122, v52, v52, v22\n\t" "v_perm_b32 v123, v52, v52, v23\n\t"
    "v_mfma_f32_16x16x32_bf16 v[88:91], v[210:213], v[116:119], 0\n\t" "v_mfma_f32_16x16x32_bf16 v[92:95], v[216:219], v[116:119], 0\n\t" "v_mul_f32 v54, v54, v59\n\t" "v_mul_f32 v55, v55, v59\n\t"
    "v_cvt_pk_bf16_f32 v54, v54, v55\n\t" "v_perm_b32 v124, v54, v54, v20\n\t" "v_perm_b32 v125, v54, v54, v21\n\t" "v_perm_b32 v126, v54, v54, v22\n\t" "v_perm_b32 v127, v54, v54, v23\n\t"
    "v_mfma_f32_16x16x32_bf16 v[96:99], v[196:199], v[120:123], 0\n\t" "v_mfma_f32_16x16x32_bf16 v[100:103], v[202:205], v[120:123], 0\n\t" "v_cvt_pk_bf16_f32 v128, v80, v81\n\t"
    "v_cvt_pk_bf16_f32 v129, v82, v83\n\t" "v_cvt_pk_bf16_f32 v130, v84, v85\n\t" "v_cvt_pk_bf16_f32 v131, v86, v87\n\t" "global_store_dwordx4 v44, v[128:131], %[so2] offset:1024 sc1\n\t"
    "v_mfma_f32_16x16x32_bf16 v[104:107], v[208:211], v[124:127], 0\n\t" "v_mfma_f32_16x16x32_bf16 v[108:111], v[214:217], v[124:127], 0\n\t" "v_cvt_pk_bf16_f32 v132, v88, v89\n\t"
    "v_cvt_pk_bf16_f32 v133, v90, v91\n\t" "v_cvt_pk_bf16_f32 v134, v92, v93\n\t" "v_cvt_pk_bf16_f32 v135, v94, v95\n\t" "global_store_dwordx4 v45, v[132:135], %[so2] offset:1024 sc1\n\t"
    "v_cvt_pk_bf16_f32 v136, v96, v97\n\t" "v_cvt_pk_bf16_f32 v137, v98, v99\n\t" "v_cvt_pk_bf16_f32 v138, v100, v101\n\t" "v_cvt_pk_bf16_f32 v139, v102, v103\n\t"
    "global_store_dwordx4 v44, v[136:139], %[so2] offset:0 sc1\n\t" "v_cvt_pk_bf16_f32 v140, v104, v105\n\t" "v_cvt_pk_bf16_f32 v141, v106, v107\n\t" "v_cvt_pk_bf16_f32 v142, v108, v109\n\t"
    "v_cvt_pk_bf16_f32 v143, v110, v111\n\t" "global_store_dwordx4 v45, v[140:143], %[so2] offset:0 sc1\n\t" "v_perm_b32 v156, v252, v252, v14\n\t" "v_perm_b32 v157, v252, v252, v17\n\t"
    "v_perm_b32 v158, v252, v252, v15\n\t" "v_perm_b32 v159, v252, v252, v19\n\t" "v_mfma_f32_4x4x4_16b_bf16 v[176:179], v[156:157], a[32:33], 0\n\t"
    "v_mfma_f32_4x4x4_16b_bf16 v[180:183], v[156:157], a[34:35], 0\n\t" "v_perm_b32 v160, v253, v253, v14\n\t" "v_perm_b32 v161, v253, v253, v17\n\t"
    "v_mfma_f32_4x4x4_16b_bf16 v[176:179], v[158:159], a[36:37], v[176:179]\n\t" "v_mfma_f32_4x4x4_16b_bf16 v[180:183], v[158:159], a[38:39], v[180:183]\n\t" "v_perm_b32 v162, v253, v253, v15\n\t"
    "v_perm_b32 v163, v253, v253, v19\n\t" "v_mfma_f32_4x4x4_16b_bf16 v[176:179], v[160:161], a[40:41], v[176:179]\n\t" "v_mfma_f32_4x4x4_16b_bf16 v[180:183], v[160:161], a[42:43], v[180:183]\n\t"
    "v_perm_b32 v164, v254, v254, v14\n\t" "v_perm_b32 v165, v254, v254, v17\n\t" "v_mfma_f32_4x4x4_16b_bf16 v[176:179], v[162:163], a[44:45], v[176:179]\n\t"
    "v_mfma_f32_4x4x4_16b_bf16 v[180:183], v[162:163], a[46:47], v[180:183]\n\t" "v_perm_b32 v166, v254, v254, v15\n\t" "v_perm_b32 v167, v254, v254, v19\n\t"
    "v_mfma_f32_4x4x4_16b_bf16 v[176:179], v[164:165], a[48:49], v[176:179]\n\t" "v_mfma_f32_4x4x4_16b_bf16 v[180:183], v[164:165], a[50:51], v[180:183]\n\t" "v_perm_b32 v168, v255, v255, v14\n\t"
    "v_perm_b32 v169, v255, v255, v17\n\t" "v_mfma_f32_4x4x4_16b_bf16 v[176:179], v[166:167], a[52:53], v[176:179]\n\t" "v_mfma_f32_4x4x4_16b_bf16 v[180:183], v[166:167], a[54:55], v[180:183]\n\t"
    "v_perm_b32 v170, v255, v255, v15\n\t" "v_perm_b32 v171, v255, v255, v19\n\t" "v_mfma_f32_4x4x4_16b_bf16 v[176:179], v[168:169], a[56:57], v[176:179]\n\t"
    "v_mfma_f32_4x4x4_16b_bf16 v[180:183], v[168:169], a[58:59], v[180:183]\n\t" "s_nop 0\n\t" "v_mfma_f32_4x4x4_16b_bf16 v[176:179], v[170:171], a[60:61], v[176:179]\n\t"
    "v_mfma_f32_4x4x4_16b_bf16 v[180:183], v[170:171], a[62:63], v[180:183]\n\t" "s_nop 0\n\t" "s_nop 3\n\t" "v_permlane32_swap_b32 v176, v180\n\t" "v_permlane32_swap_b32 v177, v181\n\t"
    "v_permlane32_swap_b32 v178, v182\n\t" "v_permlane32_swap_b32 v179, v183\n\t" "v_add_f32 v176, v176, v180\n\t" "v_add_f32 v178, v178, v182\n\t" "v_add_f32 v177, v177, v181\n\t"
    "v_add_f32 v179, v179, v183\n\t" "v_permlane16_swap_b32 v176, v178\n\t" "s_nop 0\n\t" "v_permlane16_swap_b32 v177, v179\n\t" "v_add_f32 v176, v176, v178\n\t" "v_add_f32 v177, v177, v179\n\t"
    "s_nop 0\n\t" "v_add_f32_dpp v176, v176, v176 row_ror:8 row_mask:0xf bank_mask:0xf\n\t" "v_add_f32_dpp v177, v177, v177 row_ror:8 row_mask:0xf bank_mask:0xf\n\t" "ds_write_b64 v11, v[176:177]\n\t"
    "v_mov_b64 v[178:179], s[62:63]\n\t" "v_mov_b32 v180, 0\n\t" "s_waitcnt vmcnt(0) lgkmcnt(0)\n\t" "s_barrier\n\t" "s_cmp_eq_u32 %[w128], 0\n\t" "s_cselect_b64 exec, 1, 0\n\t"
    "global_store_dwordx2 v180, v[178:179], %[sf] sc1\n\t" "s_mov_b64 exec, -1\n\t" "v_add_u32 v12, 0xffe80000, v12\n\t" "s_add_u32 m0, %[sl1], 4096\n\t" "s_nop 0\n\t"
    "global_load_lds_dwordx4 v12, %[so]\n\t" "v_add_u32 v12, 0x100000, v12\n\t" "s_add_u32 m0, %[sl1], 69632\n\t" "s_nop 0\n\t" "global_load_lds_dwordx4 v12, %[so]\n\t" "v_add_u32 v12, 0x10000, v12\n\t"
    "s_add_u32 m0, %[sl1], 73728\n\t" "s_nop 0\n\t" "global_load_lds_dwordx4 v12, %[so]\n\t" "v_add_u32 v12, 0x10000, v12\n\t" "s_add_u32 m0, %[sl1], 77824\n\t" "s_nop 0\n\t"
    "global_load_lds_dwordx4 v12, %[so]\n\t" "s_branch .Lend_%=\n\t" ".Lslow_%=:\n\t" "s_sleep 2\n\t" "s_mov_b64 s[40:41], %[sg]\n\t"
    "global_load_dwordx4 v[80:83], v230, s[40:41] offset:-4096 sc1\n\t" "global_load_dwordx4 v[96:99], v230, s[40:41] offset:0 sc1\n\t" "s_add_u32 s40, s40, 8192\n\t" "s_addc_u32 s41, s41, 0\n\t"
    "global_load_dwordx4 v[84:87], v230, s[40:41] offset:-4096 sc1\n\t" "global_load_dwordx4 v[100:103], v230, s[40:41] offset:0 sc1\n\t" "s_add_u32 s40, s40, 8192\n\t" "s_addc_u32 s41, s41, 0\n\t"
    "global_load_dwordx4 v[88:91], v230, s[40:41] offset:-4096 sc1\n\t" "global_load_dwordx4 v[104:107], v230, s[40:41] offset:0 sc1\n\t" "s_add_u32 s40, s40, 8192\n\t" "s_addc_u32 s41, s41, 0\n\t"
    "global_load_dwordx4 v[92:95], v230, s[40:41] offset:-4096 sc1\n\t" "global_load_dwordx4 v[108:111], v230, s[40:41] offset:0 sc1\n\t" "s_waitcnt vmcnt(0)\n\t" "v_xad_u32 v144, v80, v81, s62\n\t"
    "v_xad_u32 v145, v84, v85, s62\n\t" "v_xad_u32 v146, v88, v89, s62\n\t" "v_xad_u32 v147, v92, v93, s62\n\t" "v_xad_u32 v148, v96, v97, s62\n\t" "v_xad_u32 v149, v100, v101, s62\n\t"
    "v_xad_u32 v150, v104, v105, s62\n\t" "v_xad_u32 v151, v108, v109, s62\n\t" "v_xad_u32 v144, v82, v81, v144\n\t" "v_xad_u32 v145, v86, v85, v145\n\t" "v_xad_u32 v146, v90, v89, v146\n\t"
    "v_xad_u32 v147, v94, v93, v147\n\t" "v_xad_u32 v148, v98, v97, v148\n\t" "v_xad_u32 v149, v102, v101, v149\n\t" "v_xad_u32 v150, v106, v105, v150\n\t" "v_xad_u32 v151, v110, v109, v151\n\t"
    "v_xor_b32 v140, v144, v83\n\t" "v_bitop3_b32 v140, v140, v145, v87 bitop3:0xf6\n\t" "v_bitop3_b32 v140, v140, v146, v91 bitop3:0xf6\n\t" "v_bitop3_b32 v140, v140, v147, v95 bitop3:0xf6\n\t"
    "v_bitop3_b32 v140, v140, v148, v99 bitop3:0xf6\n\t" "v_bitop3_b32 v140, v140, v149, v103 bitop3:0xf6\n\t" "v_bitop3_b32 v140, v140, v150, v107 bitop3:0xf6\n\t"
    "v_bitop3_b32 v140, v140, v151, v111 bitop3:0xf6\n\t" "v_cmp_ne_u32 vcc, 0, v140\n\t" "v_perm_b32 v83, v80, v81, s57\n\t" "v_perm_b32 v87, v84, v85, s57\n\t" "v_perm_b32 v91, v88, v89, s57\n\t"
    "v_perm_b32 v95, v92, v93, s57\n\t" "v_perm_b32 v99, v96, v97, s57\n\t" "v_perm_b32 v103, v100, v101, s57\n\t" "v_perm_b32 v107, v104, v105, s57\n\t" "v_perm_b32 v111, v108, v109, s57\n\t"
    "s_cbranch_vccz .Lgo_%=\n\t" "s_sub_u32 s44, s44, 1\n\t" "s_cbranch_scc0 .Lslow_%=\n\t" "s_branch .Lgo_%=\n\t" ".Lend_%=:\n\t"
    : "+{v0}"(vb), "+{v1}"(vh), "+{v2}"(vdt), "+{v11}"(vm), "+{v12}"(vbo), "+{v13}"(tid), "+{v[14:15]}"(t2), "+{v16}"(t3), "+{v18}"(vhd), "+{v19}"(vhx), "+{v196}"(d0), "+{v197}"(d1), "+{v198}"(d2), "+{v199}"(d3), "+{v200}"(dh0), "+{v201}"(dh1), "+{v202}"(xr0), "+{v203}"(xr1), "+{v230}"(vg), "+{v231}"(vtw), "+{v232}"(vhw), "+{v234}"(vs8), "+{v235}"(vph), "+{v246}"(vpr), "+{v3}"(vr0), "+{v4}"(vr1), "+{v5}"(vr2), "+{v6}"(vr3), "+{v7}"(vr4), "+{v8}"(vr5), "+{v9}"(vr6), "+{v10}"(vr7), "+{v205}"(tv), "+{s[62:63]}"(tag), "={v236}"(mu[0]), "={v237}"(mu[1])
    : [l0] "s"(l0), [lh] "s"(lh), [lt] "s"(lt), [lr] "s"(lr), [sl] "s"(sl), [sl1] "s"(sl1), [sb] "s"(sb), [rot] "s"(rot), [xl] "s"(xl), [xh_] "s"(xh_), [k0] "s"(k0), [kh] "s"(kh), [sg] "s"(sg), [sm] "s"(sm), [so] "s"(so), [so2] "s"(so2), [sf] "s"(sf), [pf] "s"(pf), [ps] "s"(ps), [w128] "s"(w128), [w4k] "s"(w4k), [xw] "s"(xw), [xh] "s"(xh), [ox] "s"(ox), [rw] "s"(rw)
    : "v17", "v20", "v21", "v22", "v23", "v24", "v25", "v26", "v27", "v28", "v29", "v30", "v31", "v32", "v33", "v34", "v35", "v36", "v37", "v38", "v39", "v40", "v41", "v42", "v43", "v44", "v45", "v46", "v47", "v48", "v49", "v50", "v51", "v52", "v53", "v54", "v55", "v56", "v57", "v58", "v59", "v60", "v61", "v62", "v63", "v64", "v65", "v66", "v67", "v68", "v69", "v70", "v71", "v72", "v73", "v74", "v75", "v76", "v77", "v78", "v79", "v80", "v81", "v82", "v83", "v84", "v85", "v86", "v87", "v88", "v89", "v90", "v91", "v92", "v93", "v94", "v95", "v96", "v97", "v98", "v99", "v100", "v101", "v102", "v103", "v104", "v105", "v106", "v107", "v108", "v109", "v110", "v111", "v112", "v113", "v114", "v115", "v116", "v117", "v118", "v119", "v120", "v121", "v122", "v123", "v124", "v125", "v126", "v127", "v128", "v129", "v130", "v131", "v132", "v133", "v134", "v135", "v136", "v137", "v138", "v139", "v140", "v141", "v142", "v143", "v144", "v145", "v146", "v147", "v148", "v149", "v150", "v151", "v152", "v153", "v154", "v155", "v156", "v157", "v158", "v159", "v160", "v161", "v162", "v163", "v164", "v165", "v166", "v167", "v168", "v169", "v170", "v171", "v172", "v173", "v174", "v175", "v176", "v177", "v178", "v179", "v180", "v181", "v182", "v183", "v184", "v185", "v186", "v187", "v188", "v189", "v190", "v191", "v192", "v193", "v194", "v195", "v204", "v206", "v207", "v208", "v209", "v210", "v211", "v212", "v213", "v214", "v215", "v216", "v217", "v218", "v219", "v220", "v221", "v222", "v223", "v224", "v225", "v226", "v227", "v228", "v229", "v233", "v238", "v239", "v240", "v241", "v242", "v243", "v244", "v245", "v247", "v248", "v249", "v250", "v251", "v252", "v253", "v254", "v255", "a0", "a1", "a2", "a3", "a4", "a5", "a6", "a7", "a8", "a9", "a10", "a11", "a12", "a13", "a14", "a15", "a16", "a17", "a18", "a19", "a20", "a21", "a22", "a23", "a24", "a25", "a26", "a27", "a28", "a29", "a30", "a31", "a32", "a33", "a34", "a35", "a36", "a37", "a38", "a39", "a40", "a41", "a42", "a43", "a44", "a45", "a46", "a47", "a48", "a49", "a50", "a51", "a52", "a53", "a54", "a55", "a56", "a57", "a58", "a59", "a60", "a61", "a62", "a63", "a64", "a65", "a66", "a67", "a68", "a69", "a70", "a71", "a72", "a73", "a74", "a75", "a76", "a77", "a78", "a79", "a80", "a81", "a82", "a83", "a84", "a85", "a86", "a87", "a88", "a89", "a90", "a91", "a92", "a93", "a94", "a95", "a96", "a97", "a98", "a99", "a100", "a101", "a102", "a103", "a104", "a105", "a106", "a107", "a108", "a109", "a110", "a111", "a112", "a113", "a114", "a115", "a116", "a117", "a118", "a119", "a120", "a121", "a122", "a123", "a124", "a125", "a126", "a127", "a128", "a129", "a130", "a131", "a132", "a133", "a134", "a135", "a136", "a137", "a138", "a139", "a140", "a141", "a142", "a143", "a144", "a145", "a146", "a147", "a148", "a149", "a150", "a151", "a152", "a153", "a154", "a155", "a156", "a157", "a158", "a159", "a160", "a161", "a162", "a163", "a164", "a165", "a166", "a167", "a168", "a169", "a170", "a171", "a172", "a173", "a174", "a175", "a176", "a177", "a178", "a179", "a180", "a181", "a182", "a183", "a184", "a185", "a186", "a187", "a188", "a189", "a190", "a191", "a192", "a193", "a194", "a195", "a196", "a197", "a198", "a199", "a200", "a201", "a202", "a203", "a204", "a205", "a206", "a207", "a208", "a209", "a210", "a211", "a212", "a213", "a214", "a215", "a216", "a217", "a218", "a219", "a220", "a221", "a222", "a223", "a224", "a225", "a226", "a227", "a228", "a229", "a230", "a231", "a232", "a233", "a234", "a235", "a236", "a237", "a238", "a239", "a240", "a241", "a242", "a243", "a244", "a245", "a246", "a247", "a248", "a249", "a250", "a251", "a252", "a253", "a254", "a255", "s40", "s41", "s42", "s43", "s44", "s45", "s46", "s47", "s48", "s49", "s50", "s51", "s52", "s53", "s54", "s55", "s56", "s57", "s58", "s59", "s60", "s61", "s64", "s65", "vcc", "scc", "memory");
  } }
  asm volatile("" : "+s"(tag)); asm volatile("" : "+s"(bx), "+s"(wid), "+s"(ws));
  constexpr int GB = 49152, RAW = 0, TAB = 16384, ZB = TAB + 3584;
#define GW() const unsigned sl = ldsb + GB + wid * 3584; const char* sb = (const char*)(((wid >> 1) ? wu : wg) + ((xcd >> 1) * 8 + (loc >> 2)) * 112) + (size_t)(xcd & 1) * 16 * 32 * 7168
  unsigned vb[4];
  { unsigned vr[16]; f32x4 xr[8]; const unsigned va = (tid & 63) * 16; const char* sa;
    { IDX();
    const int lane = tid & 63, fr = lane & 15, par = xcd & 1; u32x4 id[2];
#pragma unroll
    for (int j = 0; j < 4; ++j) { vr[j] = ldsb + f_vr(lane, j) + (wid & 1) * 2048; vr[4 + j] = vr[j] + 65536; vr[8 + j] = vr[j] ^ 2048; vr[12 + j] = vr[8 + j] + 65536; }
#pragma unroll
    for (int j = 0; j < 8; ++j) id[j >> 2][j & 3] = (lane >> 1) - (fr >> 3) * 12 == (j >> 2) * 16 + (j & 3) ? 0x3f80u << (fr & 1) * 16 : 0u;
    const unsigned vf = fr * 64, rr = ldsb + HALO + 22528 + va;
    const char* sq = ws + (unsigned)(W_TAGS + (OF + rb * 128) * 8); sa = ws + (unsigned)(W_O + (rb * 4 + wid) * 65536);
    const char* su = ws + (unsigned)(wid == 0 ? W_TAGS + EP * 8 : wid == 3 ? W_MODP + gc * 1024 + gk * 256 : W_UVP + ((xcd >> 1) * 8 + (loc >> 2)) * 16384 + (par * 4 + (loc & 3)) * 2048 + (wid - 1) * 1024);
    asm volatile( "s_add_u32 m0, %[sl1], 81920\n\t" "v_add_u32 v12, 0x10000, v12\n\t" "global_load_lds_dwordx4 v12, %[so]\n\t" "s_add_u32 m0, %[sl1], 86016\n\t" "v_add_u32 v12, 0x10000, v12\n\t"
    "global_load_lds_dwordx4 v12, %[so]\n\t" "s_add_u32 m0, %[sl1], 90112\n\t" "v_add_u32 v12, 0x10000, v12\n\t" "global_load_lds_dwordx4 v12, %[so]\n\t" "s_add_u32 m0, %[sl1], 106496\n\t"
    "v_add_u32 v12, 0x40000, v12\n\t" "global_load_lds_dwordx4 v12, %[so]\n\t" "s_add_u32 m0, %[sl1], 110592\n\t" "v_add_u32 v12, 0x10000, v12\n\t" "global_load_lds_dwordx4 v12, %[so]\n\t"
    "s_add_u32 m0, %[sl1], 114688\n\t" "v_add_u32 v12, 0x10000, v12\n\t" "global_load_lds_dwordx4 v12, %[so]\n\t" "s_add_u32 m0, %[sl1], 118784\n\t" "v_add_u32 v12, 0x10000, v12\n\t"
    "global_load_lds_dwordx4 v12, %[so]\n\t" "s_add_u32 m0, %[sl1], 122880\n\t" "v_add_u32 v12, 0x10000, v12\n\t" "global_load_lds_dwordx4 v12, %[so]\n\t" "s_add_u32 m0, %[sl1], 126976\n\t"
    "v_add_u32 v12, 0x10000, v12\n\t" "global_load_lds_dwordx4 v12, %[so]\n\t" "v_sub_f32 v156, 0, v236\n\t" "v_sub_f32 v157, 0, v236\n\t" "v_sub_f32 v158, 0, v236\n\t" "v_sub_f32 v159, 0, v236\n\t"
    "v_sub_f32 v160, 0, v237\n\t" "v_sub_f32 v161, 0, v237\n\t" "v_sub_f32 v162, 0, v237\n\t" "v_sub_f32 v163, 0, v237\n\t" "v_mov_b64 v[164:165], %[tag]\n\t" "v_mov_b32 v166, 0\n\t"
    "s_cmp_eq_u32 %[par], 0\n\t" "s_cbranch_scc0 .Lx1_%=\n\t" "v_mfma_f32_16x16x32_bf16 v[80:83], v[40:43], a[0:3], v[156:159]\n\t" "v_mfma_f32_16x16x32_bf16 v[84:87], v[40:43], a[4:7], v[160:163]\n\t"
    "v_mfma_f32_16x16x32_bf16 v[88:91], v[44:47], a[0:3], v[156:159]\n\t" "v_mfma_f32_16x16x32_bf16 v[92:95], v[44:47], a[4:7], v[160:163]\n\t"
    "v_mfma_f32_16x16x32_bf16 v[96:99], v[40:43], a[8:11], v[156:159]\n\t" "v_mfma_f32_16x16x32_bf16 v[100:103], v[40:43], a[12:15], v[160:163]\n\t"
    "v_mfma_f32_16x16x32_bf16 v[104:107], v[44:47], a[8:11], v[156:159]\n\t" "v_mfma_f32_16x16x32_bf16 v[108:111], v[44:47], a[12:15], v[160:163]\n\t" "s_branch .Lx2_%=\n\t" ".Lx1_%=:\n\t"
    "v_mfma_f32_16x16x32_bf16 v[80:83], v[40:43], a[16:19], v[156:159]\n\t" "v_mfma_f32_16x16x32_bf16 v[84:87], v[40:43], a[20:23], v[160:163]\n\t"
    "v_mfma_f32_16x16x32_bf16 v[88:91], v[44:47], a[16:19], v[156:159]\n\t" "v_mfma_f32_16x16x32_bf16 v[92:95], v[44:47], a[20:23], v[160:163]\n\t"
    "v_mfma_f32_16x16x32_bf16 v[96:99], v[40:43], a[24:27], v[156:159]\n\t" "v_mfma_f32_16x16x32_bf16 v[100:103], v[40:43], a[28:31], v[160:163]\n\t"
    "v_mfma_f32_16x16x32_bf16 v[104:107], v[44:47], a[24:27], v[156:159]\n\t" "v_mfma_f32_16x16x32_bf16 v[108:111], v[44:47], a[28:31], v[160:163]\n\t" ".Lx2_%=:\n\t" "s_mov_b32 s46, 0x07060503\n\t"
    "s_mov_b32 s47, 0x07060502\n\t" "s_mov_b32 s48, 0x07060501\n\t" "v_mov_b32 v168, -1\n\t" "v_mov_b32 v169, -1\n\t" "v_mov_b32 v170, -1\n\t" "v_mov_b32 v171, -1\n\t" "s_cmp_eq_u32 %[wid], 3\n\t"
    "s_cbranch_scc0 .Lnu_%=\n\t" "ds_read_b128 v[172:175], v35 offset:0\n\t" "ds_read_b128 v[176:179], v35 offset:256\n\t" "ds_read_b128 v[180:183], v35 offset:512\n\t"
    "ds_read_b128 v[184:187], v35 offset:768\n\t" "s_waitcnt lgkmcnt(0)\n\t" "v_add_f32 v172, v172, v176\n\t" "v_add_f32 v173, v173, v177\n\t" "v_add_f32 v174, v174, v178\n\t"
    "v_add_f32 v175, v175, v179\n\t" "v_add_f32 v180, v180, v184\n\t" "v_add_f32 v181, v181, v185\n\t" "v_add_f32 v182, v182, v186\n\t" "v_add_f32 v183, v183, v187\n\t" "v_add_f32 v172, v172, v180\n\t"
    "v_add_f32 v173, v173, v181\n\t" "v_add_f32 v174, v174, v182\n\t" "v_add_f32 v175, v175, v183\n\t" "v_perm_b32 v168, v172, v175, s46\n\t" "v_perm_b32 v169, v173, v175, s47\n\t"
    "v_perm_b32 v170, v174, v175, s48\n\t" "v_xad_u32 v171, v168, v169, v164\n\t" "s_mov_b64 exec, 0xffff\n\t" "v_xad_u32 v171, v170, v169, v171\n\t" ".Lnu_%=:\n\t" "s_cmp_eq_u32 %[wid], 0\n\t"
    "s_cbranch_scc1 .Lw0_%=\n\t" "global_store_dwordx4 v0, v[168:171], %[su] sc1\n\t" "s_branch .Lw1_%=\n\t" ".Lw0_%=:\n\t" "s_cmp_eq_u32 %[bx], 0\n\t" "s_cselect_b64 exec, 1, 0\n\t"
    "global_store_dwordx2 v166, v[164:165], %[su]\n\t" ".Lw1_%=:\n\t" "s_mov_b64 exec, -1\n\t" "s_mov_b64 s[40:41], %[sa]\n\t" "s_mov_b32 s44, 0x400000\n\t" ".Lpo_%=:\n\t"
    "global_load_dwordx2 v[172:173], v14, %[sq] sc1\n\t" "s_waitcnt vmcnt(0)\n\t" "v_cmp_ne_u64 vcc, %[tag], v[172:173]\n\t" "s_nop 4\n\t" "s_cbranch_vccz .Lro_%=\n\t" "s_sleep 1\n\t"
    "s_sub_u32 s44, s44, 1\n\t" "s_cbranch_scc0 .Lpo_%=\n\t" ".Lro_%=:\n\t" "global_load_dwordx4 a[64:67], v0, s[40:41] offset:0 sc1\n\t"
    "global_load_dwordx4 a[68:71], v0, s[40:41] offset:1024 sc1\n\t" "s_barrier\n\t" "ds_read_b64_tr_b16 a[128:129], v16 offset:0\n\t" "ds_read_b64_tr_b16 a[130:131], v24 offset:0\n\t"
    "ds_read_b64_tr_b16 a[132:133], v17 offset:0\n\t" "ds_read_b64_tr_b16 a[134:135], v25 offset:0\n\t" "ds_read_b64_tr_b16 a[136:137], v18 offset:0\n\t" "ds_read_b64_tr_b16 a[138:139], v26 offset:0\n\t"
    "ds_read_b64_tr_b16 a[140:141], v19 offset:0\n\t" "ds_read_b64_tr_b16 a[142:143], v27 offset:0\n\t" "global_load_dwordx4 a[72:75], v0, s[40:41] offset:2048 sc1\n\t"
    "global_load_dwordx4 a[76:79], v0, s[40:41] offset:3072 sc1\n\t" "s_add_u32 s40, s40, 4096\n\t" "s_addc_u32 s41, s41, 0\n\t" "global_load_dwordx4 a[80:83], v0, s[40:41] offset:0 sc1\n\t"
    "global_load_dwordx4 a[84:87], v0, s[40:41] offset:1024 sc1\n\t" "global_load_dwordx4 a[88:91], v0, s[40:41] offset:2048 sc1\n\t" "global_load_dwordx4 a[92:95], v0, s[40:41] offset:3072 sc1\n\t"
    "s_add_u32 s40, s40, 4096\n\t" "s_addc_u32 s41, s41, 0\n\t" "global_load_dwordx4 a[96:99], v0, s[40:41] offset:0 sc1\n\t" "global_load_dwordx4 a[100:103], v0, s[40:41] offset:1024 sc1\n\t"
    "global_load_dwordx4 a[104:107], v0, s[40:41] offset:2048 sc1\n\t" "global_load_dwordx4 a[108:111], v0, s[40:41] offset:3072 sc1\n\t" "s_add_u32 s40, s40, 4096\n\t" "s_addc_u32 s41, s41, 0\n\t"

    : "+{v12}"(vbo), "=&{v[80:83]}"(xr[0]), "=&{v[84:87]}"(xr[1]), "=&{v[88:91]}"(xr[2]), "=&{v[92:95]}"(xr[3]), "=&{v[96:99]}"(xr[4]), "=&{v[100:103]}"(xr[5]), "=&{v[104:107]}"(xr[6]), "=&{v[108:111]}"(xr[7])
    : "{v0}"(va), "{v14}"(vf), "{v35}"(rr), "{v236}"(mu[0]), "{v237}"(mu[1]), "{v16}"(vr[0]), "{v17}"(vr[1]), "{v18}"(vr[2]), "{v19}"(vr[3]), "{v20}"(vr[4]), "{v21}"(vr[5]), "{v22}"(vr[6]), "{v23}"(vr[7]), "{v24}"(vr[8]), "{v25}"(vr[9]), "{v26}"(vr[10]), "{v27}"(vr[11]), "{v28}"(vr[12]), "{v29}"(vr[13]), "{v30}"(vr[14]), "{v31}"(vr[15]), "{v[40:43]}"(id[0]), "{v[44:47]}"(id[1]), [tag] "s"(tag), [par] "s"(par), [wid] "s"(wid), [bx] "s"(bx), [sl1] "s"(sl1), [so] "s"(so), [su] "s"(su), [sa] "s"(sa), [sq] "s"(sq)
    : "v156", "v157", "v158", "v159", "v160", "v161", "v162", "v163", "v164", "v165", "v166", "v167", "v168", "v169", "v170", "v171", "v172", "v173", "v174", "v175", "v176", "v177", "v178", "v179", "v180", "v181", "v182", "v183", "v184", "v185", "v186", "v187", "v188", "v189", "v190", "v191", "v192", "v193", "v194", "v195", "a0", "a1", "a2", "a3", "a4", "a5", "a6", "a7", "a8", "a9", "a10", "a11", "a12", "a13", "a14", "a15", "a16", "a17", "a18", "a19", "a20", "a21", "a22", "a23", "a24", "a25", "a26", "a27", "a28", "a29", "a30", "a31", "a32", "a33", "a34", "a35", "a36", "a37", "a38", "a39", "a40", "a41", "a42", "a43", "a44", "a45", "a46", "a47", "a48", "a49", "a50", "a51", "a52", "a53", "a54", "a55", "a56", "a57", "a58", "a59", "a60", "a61", "a62", "a63", "a64", "a65", "a66", "a67", "a68", "a69", "a70", "a71", "a72", "a73", "a74", "a75", "a76", "a77", "a78", "a79", "a80", "a81", "a82", "a83", "a84", "a85", "a86", "a87", "a88", "a89", "a90", "a91", "a92", "a93", "a94", "a95", "a96", "a97", "a98", "a99", "a100", "a101", "a102", "a103", "a104", "a105", "a106", "a107", "a108", "a109", "a110", "a111", "a112", "a113", "a114", "a115", "a116", "a117", "a118", "a119", "a120", "a121", "a122", "a123", "a124", "a125", "a126", "a127", "a128", "a129", "a130", "a131", "a132", "a133", "a134", "a135", "a136", "a137", "a138", "a139", "a140", "a141", "a142", "a143", "a144", "a145", "a146", "a147", "a148", "a149", "a150", "a151", "a152", "a153", "a154", "a155", "a156", "a157", "a158", "a159", "s40", "s41", "s42", "s43", "s44", "s45", "s46", "s47", "s48", "s49", "s50", "s78", "s79", "s82", "s83", "vcc", "scc", "memory");
    }
    asm volatile("" : "+s"(bx), "+s"(wid), "+s"(ws)); asm volatile("" : "+v"(tid));
    IDX(); GW();
    const int lane = tid & 63, fr = lane & 15, fq = lane >> 4, col = fr * 4 + fq, role = wid == 1 ? 1 : (rb & 1) ? 3 : wid;
#pragma unroll
    for (int i = 0; i < 4; ++i) { const int idx = min(i * 64 + lane, 223), r = (idx * 293) >> 12; vb[i] = (unsigned)((wid & 1) * 16 * 7168 + r * (7168 - 224) + idx * 16); }
    const unsigned tw = ldsb + HALO + 1024 + wid * 512 + col * 4, tr = ldsb + HALO + 1024 + wid * 512 + fq * 16, vs = (lane & 31) * 8, zb = ldsb + ZB + tid * 4;
    const unsigned ve = wid == 1 ? col * 4 : (fr >> 3) * 128 + (fr & 3) * 16 + (fr & 4) * 2 + fq * 2;
    const char* sg0 = ws + (unsigned)(W_MODP + co * 1024); const char* sg1 = sg0 + 32768; const char* sg2 = sg0 + (unsigned)(16384 + (wid & 1) * 32768); const char* sf = ws + (unsigned)(W_TAGS + (YF + (rb * 16 + cb) * 8) * 8);
    const char* se = ws + (unsigned)(wid == 1 ? W_GB + bx * 256 : W_EF + rb * 2048 + co * 256 + (wid ? 0 : 64));
    const char* sy = ws + (unsigned)(W_Y + ((rb * 2 + (wid >> 1)) * 32 + 2 * co + 1) * 4096 + (wid & 1) * 2048); const char* ss = ws + (unsigned)(W_STATS + (cb * S_ + rb * 128 + wid * 32) * 8); const char* sx = ws + (unsigned)(W_X1 + (bx * 4 + wid) * 4096);
    asm volatile("s_mov_b64 s[40:41], %[sa]\n\t" "s_mov_b32 s49, 0x04000c0c\n\t" "s_mov_b32 s50, 0x0706000c\n\t" "s_mov_b32 s44, 0x400000\n\t" "v_mov_b64 v[164:165], %[tag]\n\t" "s_waitcnt vmcnt(10)\n\t"
    "s_waitcnt lgkmcnt(4)\n\t" "v_mfma_f32_16x16x32_bf16 v[48:51], a[128:131], a[64:67], 0\n\t" "ds_read_b64_tr_b16 a[144:145], v16 offset:4096\n\t"
    "global_load_dwordx4 a[112:115], v0, s[40:41] offset:0 sc1\n\t" "v_mfma_f32_16x16x32_bf16 v[52:55], a[128:131], a[68:71], 0\n\t" "ds_read_b64_tr_b16 a[146:147], v24 offset:4096\n\t"
    "v_mfma_f32_16x16x32_bf16 v[56:59], a[132:135], a[64:67], 0\n\t" "ds_read_b64_tr_b16 a[148:149], v17 offset:4096\n\t" "global_load_dwordx4 a[116:119], v0, s[40:41] offset:1024 sc1\n\t"
    "v_mfma_f32_16x16x32_bf16 v[60:63], a[132:135], a[68:71], 0\n\t" "ds_read_b64_tr_b16 a[150:151], v25 offset:4096\n\t" "s_waitcnt lgkmcnt(4)\n\t"
    "v_mfma_f32_16x16x32_bf16 v[64:67], a[136:139], a[64:67], 0\n\t" "ds_read_b64_tr_b16 a[152:153], v18 offset:4096\n\t" "v_mfma_f32_16x16x32_bf16 v[68:71], a[136:139], a[68:71], 0\n\t"
    "ds_read_b64_tr_b16 a[154:155], v26 offset:4096\n\t" "v_mfma_f32_16x16x32_bf16 v[72:75], a[140:143], a[64:67], 0\n\t" "ds_read_b64_tr_b16 a[156:157], v19 offset:4096\n\t"
    "global_load_dwordx4 a[120:123], v0, s[40:41] offset:2048 sc1\n\t" "v_mfma_f32_16x16x32_bf16 v[76:79], a[140:143], a[68:71], 0\n\t" "ds_read_b64_tr_b16 a[158:159], v27 offset:4096\n\t"
    "s_waitcnt lgkmcnt(4)\n\t" "s_waitcnt vmcnt(11)\n\t" "v_mfma_f32_16x16x32_bf16 v[48:51], a[144:147], a[72:75], v[48:51]\n\t" "ds_read_b64_tr_b16 a[128:129], v16 offset:8192\n\t"
    "global_load_dwordx4 a[124:127], v0, s[40:41] offset:3072 sc1\n\t" "s_add_u32 s40, s40, 4096\n\t" "s_addc_u32 s41, s41, 0\n\t" "v_mfma_f32_16x16x32_bf16 v[52:55], a[144:147], a[76:79], v[52:55]\n\t"
    "ds_read_b64_tr_b16 a[130:131], v24 offset:8192\n\t" "v_mfma_f32_16x16x32_bf16 v[56:59], a[148:151], a[72:75], v[56:59]\n\t" "ds_read_b64_tr_b16 a[132:133], v17 offset:8192\n\t"
    "global_load_dwordx4 a[64:67], v0, s[40:41] offset:0 sc1\n\t" "v_mfma_f32_16x16x32_bf16 v[60:63], a[148:151], a[76:79], v[60:63]\n\t" "ds_read_b64_tr_b16 a[134:135], v25 offset:8192\n\t"
    "s_waitcnt lgkmcnt(4)\n\t" "v_mfma_f32_16x16x32_bf16 v[64:67], a[152:155], a[72:75], v[64:67]\n\t" "ds_read_b64_tr_b16 a[136:137], v18 offset:8192\n\t"
    "v_mfma_f32_16x16x32_bf16 v[68:71], a[152:155], a[76:79], v[68:71]\n\t" "ds_read_b64_tr_b16 a[138:139], v26 offset:8192\n\t" "v_mfma_f32_16x16x32_bf16 v[72:75], a[156:159], a[72:75], v[72:75]\n\t"
    "ds_read_b64_tr_b16 a[140:141], v19 offset:8192\n\t" "global_load_dwordx4 a[68:71], v0, s[40:41] offset:1024 sc1\n\t" "v_mfma_f32_16x16x32_bf16 v[76:79], a[156:159], a[76:79], v[76:79]\n\t"
    "ds_read_b64_tr_b16 a[142:143], v27 offset:8192\n\t" "s_waitcnt lgkmcnt(4)\n\t" "s_waitcnt vmcnt(12)\n\t" "v_mfma_f32_16x16x32_bf16 v[48:51], a[128:131], a[80:83], v[48:51]\n\t"
    "ds_read_b64_tr_b16 a[144:145], v16 offset:12288\n\t" "v_mfma_f32_16x16x32_bf16 v[52:55], a[128:131], a[84:87], v[52:55]\n\t" "ds_read_b64_tr_b16 a[146:147], v24 offset:12288\n\t"
    "v_mfma_f32_16x16x32_bf16 v[56:59], a[132:135], a[80:83], v[56:59]\n\t" "ds_read_b64_tr_b16 a[148:149], v17 offset:12288\n\t" "global_load_dwordx4 a[72:75], v0, s[40:41] offset:2048 sc1\n\t"
    "v_mfma_f32_16x16x32_bf16 v[60:63], a[132:135], a[84:87], v[60:63]\n\t" "ds_read_b64_tr_b16 a[150:151], v25 offset:12288\n\t" "s_waitcnt lgkmcnt(4)\n\t"
    "v_mfma_f32_16x16x32_bf16 v[64:67], a[136:139], a[80:83], v[64:67]\n\t" "ds_read_b64_tr_b16 a[152:153], v18 offset:12288\n\t" "v_mfma_f32_16x16x32_bf16 v[68:71], a[136:139], a[84:87], v[68:71]\n\t"
    "ds_read_b64_tr_b16 a[154:155], v26 offset:12288\n\t" "v_mfma_f32_16x16x32_bf16 v[72:75], a[140:143], a[80:83], v[72:75]\n\t" "ds_read_b64_tr_b16 a[156:157], v19 offset:12288\n\t"
    "global_load_dwordx4 a[76:79], v0, s[40:41] offset:3072 sc1\n\t" "s_add_u32 s40, s40, 4096\n\t" "s_addc_u32 s41, s41, 0\n\t" "v_mfma_f32_16x16x32_bf16 v[76:79], a[140:143], a[84:87], v[76:79]\n\t"
    "ds_read_b64_tr_b16 a[158:159], v27 offset:12288\n\t" "s_waitcnt lgkmcnt(4)\n\t" "s_waitcnt vmcnt(12)\n\t" "v_mfma_f32_16x16x32_bf16 v[48:51], a[144:147], a[88:91], v[48:51]\n\t"
    "ds_read_b64_tr_b16 a[128:129], v16 offset:16384\n\t" "v_mfma_f32_16x16x32_bf16 v[52:55], a[144:147], a[92:95], v[52:55]\n\t" "ds_read_b64_tr_b16 a[130:131], v24 offset:16384\n\t"
    "v_mfma_f32_16x16x32_bf16 v[56:59], a[148:151], a[88:91], v[56:59]\n\t" "ds_read_b64_tr_b16 a[132:133], v17 offset:16384\n\t" "global_load_dwordx4 a[80:83], v0, s[40:41] offset:0 sc1\n\t"
    "v_mfma_f32_16x16x32_bf16 v[60:63], a[148:151], a[92:95], v[60:63]\n\t" "ds_read_b64_tr_b16 a[134:135], v25 offset:16384\n\t" "s_waitcnt lgkmcnt(4)\n\t"
    "v_mfma_f32_16x16x32_bf16 v[64:67], a[152:155], a[88:91], v[64:67]\n\t" "ds_read_b64_tr_b16 a[136:137], v18 offset:16384\n\t" "v_mfma_f32_16x16x32_bf16 v[68:71], a[152:155], a[92:95], v[68:71]\n\t"
    "ds_read_b64_tr_b16 a[138:139], v26 offset:16384\n\t" "v_mfma_f32_16x16x32_bf16 v[72:75], a[156:159], a[88:91], v[72:75]\n\t" "ds_read_b64_tr_b16 a[140:141], v19 offset:16384\n\t"
    "global_load_dwordx4 a[84:87], v0, s[40:41] offset:1024 sc1\n\t" "v_mfma_f32_16x16x32_bf16 v[76:79], a[156:159], a[92:95], v[76:79]\n\t" "ds_read_b64_tr_b16 a[142:143], v27 offset:16384\n\t"
    "s_waitcnt lgkmcnt(4)\n\t" "s_waitcnt vmcnt(12)\n\t" "v_mfma_f32_16x16x32_bf16 v[48:51], a[128:131], a[96:99], v[48:51]\n\t" "ds_read_b64_tr_b16 a[144:145], v16 offset:20480\n\t"
    "v_mfma_f32_16x16x32_bf16 v[52:55], a[128:131], a[100:103], v[52:55]\n\t" "ds_read_b64_tr_b16 a[146:147], v24 offset:20480\n\t" "v_mfma_f32_16x16x32_bf16 v[56:59], a[132:135], a[96:99], v[56:59]\n\t"
    "ds_read_b64_tr_b16 a[148:149], v17 offset:20480\n\t" "global_load_dwordx4 a[88:91], v0, s[40:41] offset:2048 sc1\n\t" "v_mfma_f32_16x16x32_bf16 v[60:63], a[132:135], a[100:103], v[60:63]\n\t"
    "ds_read_b64_tr_b16 a[150:151], v25 offset:20480\n\t" "s_waitcnt lgkmcnt(4)\n\t" "v_mfma_f32_16x16x32_bf16 v[64:67], a[136:139], a[96:99], v[64:67]\n\t"
    "ds_read_b64_tr_b16 a[152:153], v18 offset:20480\n\t" "v_mfma_f32_16x16x32_bf16 v[68:71], a[136:139], a[100:103], v[68:71]\n\t" "ds_read_b64_tr_b16 a[154:155], v26 offset:20480\n\t"
    "v_mfma_f32_16x16x32_bf16 v[72:75], a[140:143], a[96:99], v[72:75]\n\t" "ds_read_b64_tr_b16 a[156:157], v19 offset:20480\n\t" "global_load_dwordx4 a[92:95], v0, s[40:41] offset:3072 sc1\n\t"
    "s_add_u32 s40, s40, 4096\n\t" "s_addc_u32 s41, s41, 0\n\t" "v_mfma_f32_16x16x32_bf16 v[76:79], a[140:143], a[100:103], v[76:79]\n\t" "ds_read_b64_tr_b16 a[158:159], v27 offset:20480\n\t"
    "s_waitcnt lgkmcnt(4)\n\t" "s_waitcnt vmcnt(12)\n\t" "v_mfma_f32_16x16x32_bf16 v[48:51], a[144:147], a[104:107], v[48:51]\n\t" "ds_read_b64_tr_b16 a[128:129], v16 offset:24576\n\t"
    "v_mfma_f32_16x16x32_bf16 v[52:55], a[144:147], a[108:111], v[52:55]\n\t" "ds_read_b64_tr_b16 a[130:131], v24 offset:24576\n\t"
    "v_mfma_f32_16x16x32_bf16 v[56:59], a[148:151], a[104:107], v[56:59]\n\t" "ds_read_b64_tr_b16 a[132:133], v17 offset:24576\n\t" "global_load_dwordx4 a[96:99], v0, s[40:41] offset:0 sc1\n\t"
    "v_mfma_f32_16x16x32_bf16 v[60:63], a[148:151], a[108:111], v[60:63]\n\t" "ds_read_b64_tr_b16 a[134:135], v25 offset:24576\n\t" "s_waitcnt lgkmcnt(4)\n\t"
    "v_mfma_f32_16x16x32_bf16 v[64:67], a[152:155], a[104:107], v[64:67]\n\t" "ds_read_b64_tr_b16 a[136:137], v18 offset:24576\n\t"
    "v_mfma_f32_16x16x32_bf16 v[68:71], a[152:155], a[108:111], v[68:71]\n\t" "ds_read_b64_tr_b16 a[138:139], v26 offset:24576\n\t"
    "v_mfma_f32_16x16x32_bf16 v[72:75], a[156:159], a[104:107], v[72:75]\n\t" "ds_read_b64_tr_b16 a[140:141], v19 offset:24576\n\t" "global_load_dwordx4 a[100:103], v0, s[40:41] offset:1024 sc1\n\t"
    "v_mfma_f32_16x16x32_bf16 v[76:79], a[156:159], a[108:111], v[76:79]\n\t" "ds_read_b64_tr_b16 a[142:143], v27 offset:24576\n\t" "s_waitcnt lgkmcnt(4)\n\t" "s_waitcnt vmcnt(12)\n\t"
    "v_mfma_f32_16x16x32_bf16 v[48:51], a[128:131], a[112:115], v[48:51]\n\t" "ds_read_b64_tr_b16 a[144:145], v16 offset:28672\n\t"
    "v_mfma_f32_16x16x32_bf16 v[52:55], a[128:131], a[116:119], v[52:55]\n\t" "ds_read_b64_tr_b16 a[146:147], v24 offset:28672\n\t"
    "v_mfma_f32_16x16x32_bf16 v[56:59], a[132:135], a[112:115], v[56:59]\n\t" "ds_read_b64_tr_b16 a[148:149], v17 offset:28672\n\t" "global_load_dwordx4 a[104:107], v0, s[40:41] offset:2048 sc1\n\t"
    "v_mfma_f32_16x16x32_bf16 v[60:63], a[132:135], a[116:119], v[60:63]\n\t" "ds_read_b64_tr_b16 a[150:151], v25 offset:28672\n\t" "s_waitcnt lgkmcnt(4)\n\t"
    "v_mfma_f32_16x16x32_bf16 v[64:67], a[136:139], a[112:115], v[64:67]\n\t" "ds_read_b64_tr_b16 a[152:153], v18 offset:28672\n\t"
    "v_mfma_f32_16x16x32_bf16 v[68:71], a[136:139], a[116:119], v[68:71]\n\t" "ds_read_b64_tr_b16 a[154:155], v26 offset:28672\n\t"
    "v_mfma_f32_16x16x32_bf16 v[72:75], a[140:143], a[112:115], v[72:75]\n\t" "ds_read_b64_tr_b16 a[156:157], v19 offset:28672\n\t" "global_load_dwordx4 a[108:111], v0, s[40:41] offset:3072 sc1\n\t"
    "s_add_u32 s40, s40, 4096\n\t" "s_addc_u32 s41, s41, 0\n\t" "v_mfma_f32_16x16x32_bf16 v[76:79], a[140:143], a[116:119], v[76:79]\n\t" "ds_read_b64_tr_b16 a[158:159], v27 offset:28672\n\t"
    "s_waitcnt lgkmcnt(4)\n\t" "s_waitcnt vmcnt(12)\n\t" "v_mfma_f32_16x16x32_bf16 v[48:51], a[144:147], a[120:123], v[48:51]\n\t" "ds_read_b64_tr_b16 a[128:129], v16 offset:32768\n\t"
    "v_mfma_f32_16x16x32_bf16 v[52:55], a[144:147], a[124:127], v[52:55]\n\t" "ds_read_b64_tr_b16 a[130:131], v24 offset:32768\n\t"
    "v_mfma_f32_16x16x32_bf16 v[56:59], a[148:151], a[120:123], v[56:59]\n\t" "ds_read_b64_tr_b16 a[132:133], v17 offset:32768\n\t" "global_load_dwordx4 a[112:115], v0, s[40:41] offset:0 sc1\n\t"
    "v_mfma_f32_16x16x32_bf16 v[60:63], a[148:151], a[124:127], v[60:63]\n\t" "ds_read_b64_tr_b16 a[134:135], v25 offset:32768\n\t" "s_waitcnt lgkmcnt(4)\n\t"
    "v_mfma_f32_16x16x32_bf16 v[64:67], a[152:155], a[120:123], v[64:67]\n\t" "ds_read_b64_tr_b16 a[136:137], v18 offset:32768\n\t"
    "v_mfma_f32_16x16x32_bf16 v[68:71], a[152:155], a[124:127], v[68:71]\n\t" "ds_read_b64_tr_b16 a[138:139], v26 offset:32768\n\t"
    "v_mfma_f32_16x16x32_bf16 v[72:75], a[156:159], a[120:123], v[72:75]\n\t" "ds_read_b64_tr_b16 a[140:141], v19 offset:32768\n\t" "global_load_dwordx4 a[116:119], v0, s[40:41] offset:1024 sc1\n\t"
    "v_mfma_f32_16x16x32_bf16 v[76:79], a[156:159], a[124:127], v[76:79]\n\t" "ds_read_b64_tr_b16 a[142:143], v27 offset:32768\n\t" "s_waitcnt lgkmcnt(4)\n\t" "s_waitcnt vmcnt(12)\n\t"
    "v_mfma_f32_16x16x32_bf16 v[48:51], a[128:131], a[64:67], v[48:51]\n\t" "ds_read_b64_tr_b16 a[144:145], v16 offset:36864\n\t" "v_mfma_f32_16x16x32_bf16 v[52:55], a[128:131], a[68:71], v[52:55]\n\t"
    "ds_read_b64_tr_b16 a[146:147], v24 offset:36864\n\t" "v_mfma_f32_16x16x32_bf16 v[56:59], a[132:135], a[64:67], v[56:59]\n\t" "ds_read_b64_tr_b16 a[148:149], v17 offset:36864\n\t"
    "global_load_dwordx4 a[120:123], v0, s[40:41] offset:2048 sc1\n\t" "v_mfma_f32_16x16x32_bf16 v[60:63], a[132:135], a[68:71], v[60:63]\n\t" "ds_read_b64_tr_b16 a[150:151], v25 offset:36864\n\t"
    "s_waitcnt lgkmcnt(4)\n\t" "v_mfma_f32_16x16x32_bf16 v[64:67], a[136:139], a[64:67], v[64:67]\n\t" "ds_read_b64_tr_b16 a[152:153], v18 offset:36864\n\t"
    "v_mfma_f32_16x16x32_bf16 v[68:71], a[136:139], a[68:71], v[68:71]\n\t" "ds_read_b64_tr_b16 a[154:155], v26 offset:36864\n\t" "v_mfma_f32_16x16x32_bf16 v[72:75], a[140:143], a[64:67], v[72:75]\n\t"
    "ds_read_b64_tr_b16 a[156:157], v19 offset:36864\n\t" "global_load_dwordx4 a[124:127], v0, s[40:41] offset:3072 sc1\n\t" "s_add_u32 s40, s40, 4096\n\t" "s_addc_u32 s41, s41, 0\n\t"
    "v_mfma_f32_16x16x32_bf16 v[76:79], a[140:143], a[68:71], v[76:79]\n\t" "ds_read_b64_tr_b16 a[158:159], v27 offset:36864\n\t" "s_waitcnt lgkmcnt(4)\n\t" "s_waitcnt vmcnt(12)\n\t"
    "v_mfma_f32_16x16x32_bf16 v[48:51], a[144:147], a[72:75], v[48:51]\n\t" "ds_read_b64_tr_b16 a[128:129], v16 offset:40960\n\t" "v_mfma_f32_16x16x32_bf16 v[52:55], a[144:147], a[76:79], v[52:55]\n\t"
    "ds_read_b64_tr_b16 a[130:131], v24 offset:40960\n\t" "v_mfma_f32_16x16x32_bf16 v[56:59], a[148:151], a[72:75], v[56:59]\n\t" "ds_read_b64_tr_b16 a[132:133], v17 offset:40960\n\t"
    "global_load_dwordx4 a[64:67], v0, s[40:41] offset:0 sc1\n\t" "v_mfma_f32_16x16x32_bf16 v[60:63], a[148:151], a[76:79], v[60:63]\n\t" "ds_read_b64_tr_b16 a[134:135], v25 offset:40960\n\t"
    "s_waitcnt lgkmcnt(4)\n\t" "v_mfma_f32_16x16x32_bf16 v[64:67], a[152:155], a[72:75], v[64:67]\n\t" "ds_read_b64_tr_b16 a[136:137], v18 offset:40960\n\t"
    "v_mfma_f32_16x16x32_bf16 v[68:71], a[152:155], a[76:79], v[68:71]\n\t" "ds_read_b64_tr_b16 a[138:139], v26 offset:40960\n\t" "v_mfma_f32_16x16x32_bf16 v[72:75], a[156:159], a[72:75], v[72:75]\n\t"
    "ds_read_b64_tr_b16 a[140:141], v19 offset:40960\n\t" "global_load_dwordx4 a[68:71], v0, s[40:41] offset:1024 sc1\n\t" "v_mfma_f32_16x16x32_bf16 v[76:79], a[156:159], a[76:79], v[76:79]\n\t"
    "ds_read_b64_tr_b16 a[142:143], v27 offset:40960\n\t" "s_waitcnt lgkmcnt(4)\n\t" "s_waitcnt vmcnt(12)\n\t" "v_mfma_f32_16x16x32_bf16 v[48:51], a[128:131], a[80:83], v[48:51]\n\t"
    "ds_read_b64_tr_b16 a[144:145], v16 offset:45056\n\t" "v_mfma_f32_16x16x32_bf16 v[52:55], a[128:131], a[84:87], v[52:55]\n\t" "ds_read_b64_tr_b16 a[146:147], v24 offset:45056\n\t"
    "global_load_dwordx4 v[112:115], v0, %[sg0] sc1\n\t" "v_mfma_f32_16x16x32_bf16 v[56:59], a[132:135], a[80:83], v[56:59]\n\t" "ds_read_b64_tr_b16 a[148:149], v17 offset:45056\n\t"
    "global_load_dwordx4 a[72:75], v0, s[40:41] offset:2048 sc1\n\t" "v_mfma_f32_16x16x32_bf16 v[60:63], a[132:135], a[84:87], v[60:63]\n\t" "ds_read_b64_tr_b16 a[150:151], v25 offset:45056\n\t"
    "global_load_dwordx4 v[116:119], v0, %[sg1] sc1\n\t" "s_waitcnt lgkmcnt(4)\n\t" "v_mfma_f32_16x16x32_bf16 v[64:67], a[136:139], a[80:83], v[64:67]\n\t"
    "ds_read_b64_tr_b16 a[152:153], v18 offset:45056\n\t" "v_mfma_f32_16x16x32_bf16 v[68:71], a[136:139], a[84:87], v[68:71]\n\t" "ds_read_b64_tr_b16 a[154:155], v26 offset:45056\n\t"
    "global_load_dwordx4 v[120:123], v0, %[sg2] sc1\n\t" "v_mfma_f32_16x16x32_bf16 v[72:75], a[140:143], a[80:83], v[72:75]\n\t" "ds_read_b64_tr_b16 a[156:157], v19 offset:45056\n\t"
    "global_load_dwordx4 a[76:79], v0, s[40:41] offset:3072 sc1\n\t" "s_add_u32 s40, s40, 4096\n\t" "s_addc_u32 s41, s41, 0\n\t" "v_mfma_f32_16x16x32_bf16 v[76:79], a[140:143], a[84:87], v[76:79]\n\t"
    "ds_read_b64_tr_b16 a[158:159], v27 offset:45056\n\t" "s_waitcnt lgkmcnt(4)\n\t" "s_waitcnt vmcnt(15)\n\t" "v_mfma_f32_16x16x32_bf16 v[48:51], a[144:147], a[88:91], v[48:51]\n\t"
    "ds_read_b64_tr_b16 a[128:129], v16 offset:49152\n\t" "v_mfma_f32_16x16x32_bf16 v[52:55], a[144:147], a[92:95], v[52:55]\n\t" "ds_read_b64_tr_b16 a[130:131], v24 offset:49152\n\t"
    "v_mfma_f32_16x16x32_bf16 v[56:59], a[148:151], a[88:91], v[56:59]\n\t" "ds_read_b64_tr_b16 a[132:133], v17 offset:49152\n\t" "global_load_dwordx4 a[80:83], v0, s[40:41] offset:0 sc1\n\t"
    "v_mfma_f32_16x16x32_bf16 v[60:63], a[148:151], a[92:95], v[60:63]\n\t" "ds_read_b64_tr_b16 a[134:135], v25 offset:49152\n\t" "s_waitcnt lgkmcnt(4)\n\t"
    "v_mfma_f32_16x16x32_bf16 v[64:67], a[152:155], a[88:91], v[64:67]\n\t" "ds_read_b64_tr_b16 a[136:137], v18 offset:49152\n\t" "v_mfma_f32_16x16x32_bf16 v[68:71], a[152:155], a[92:95], v[68:71]\n\t"
    "ds_read_b64_tr_b16 a[138:139], v26 offset:49152\n\t" "v_mfma_f32_16x16x32_bf16 v[72:75], a[156:159], a[88:91], v[72:75]\n\t" "ds_read_b64_tr_b16 a[140:141], v19 offset:49152\n\t"
    "global_load_dwordx4 a[84:87], v0, s[40:41] offset:1024 sc1\n\t" "v_mfma_f32_16x16x32_bf16 v[76:79], a[156:159], a[92:95], v[76:79]\n\t" "ds_read_b64_tr_b16 a[142:143], v27 offset:49152\n\t"
    "s_waitcnt lgkmcnt(4)\n\t" "s_waitcnt vmcnt(15)\n\t" "v_mfma_f32_16x16x32_bf16 v[48:51], a[128:131], a[96:99], v[48:51]\n\t" "ds_read_b64_tr_b16 a[144:145], v16 offset:53248\n\t"
    "v_mfma_f32_16x16x32_bf16 v[52:55], a[128:131], a[100:103], v[52:55]\n\t" "ds_read_b64_tr_b16 a[146:147], v24 offset:53248\n\t" "v_mfma_f32_16x16x32_bf16 v[56:59], a[132:135], a[96:99], v[56:59]\n\t"
    "ds_read_b64_tr_b16 a[148:149], v17 offset:53248\n\t" "global_load_dwordx4 a[88:91], v0, s[40:41] offset:2048 sc1\n\t" "v_mfma_f32_16x16x32_bf16 v[60:63], a[132:135], a[100:103], v[60:63]\n\t"
    "ds_read_b64_tr_b16 a[150:151], v25 offset:53248\n\t" "s_waitcnt lgkmcnt(4)\n\t" "v_mfma_f32_16x16x32_bf16 v[64:67], a[136:139], a[96:99], v[64:67]\n\t"
    "ds_read_b64_tr_b16 a[152:153], v18 offset:53248\n\t" "v_mfma_f32_16x16x32_bf16 v[68:71], a[136:139], a[100:103], v[68:71]\n\t" "ds_read_b64_tr_b16 a[154:155], v26 offset:53248\n\t"
    "v_mfma_f32_16x16x32_bf16 v[72:75], a[140:143], a[96:99], v[72:75]\n\t" "ds_read_b64_tr_b16 a[156:157], v19 offset:53248\n\t" "global_load_dwordx4 a[92:95], v0, s[40:41] offset:3072 sc1\n\t"
    "s_add_u32 s40, s40, 4096\n\t" "s_addc_u32 s41, s41, 0\n\t" "v_mfma_f32_16x16x32_bf16 v[76:79], a[140:143], a[100:103], v[76:79]\n\t" "ds_read_b64_tr_b16 a[158:159], v27 offset:53248\n\t"
    "s_waitcnt lgkmcnt(4)\n\t" "s_waitcnt vmcnt(15)\n\t" "v_mfma_f32_16x16x32_bf16 v[48:51], a[144:147], a[104:107], v[48:51]\n\t" "ds_read_b64_tr_b16 a[128:129], v16 offset:57344\n\t"
    "v_mfma_f32_16x16x32_bf16 v[52:55], a[144:147], a[108:111], v[52:55]\n\t" "ds_read_b64_tr_b16 a[130:131], v24 offset:57344\n\t"
    "v_mfma_f32_16x16x32_bf16 v[56:59], a[148:151], a[104:107], v[56:59]\n\t" "ds_read_b64_tr_b16 a[132:133], v17 offset:57344\n\t" "global_load_dwordx4 a[96:99], v0, s[40:41] offset:0 sc1\n\t"
    "v_mfma_f32_16x16x32_bf16 v[60:63], a[148:151], a[108:111], v[60:63]\n\t" "ds_read_b64_tr_b16 a[134:135], v25 offset:57344\n\t" "s_waitcnt lgkmcnt(4)\n\t"
    "v_mfma_f32_16x16x32_bf16 v[64:67], a[152:155], a[104:107], v[64:67]\n\t" "ds_read_b64_tr_b16 a[136:137], v18 offset:57344\n\t"
    "v_mfma_f32_16x16x32_bf16 v[68:71], a[152:155], a[108:111], v[68:71]\n\t" "ds_read_b64_tr_b16 a[138:139], v26 offset:57344\n\t"
    "v_mfma_f32_16x16x32_bf16 v[72:75], a[156:159], a[104:107], v[72:75]\n\t" "ds_read_b64_tr_b16 a[140:141], v19 offset:57344\n\t" "global_load_dwordx4 a[100:103], v0, s[40:41] offset:1024 sc1\n\t"
    "v_mfma_f32_16x16x32_bf16 v[76:79], a[156:159], a[108:111], v[76:79]\n\t" "ds_read_b64_tr_b16 a[142:143], v27 offset:57344\n\t" "s_waitcnt lgkmcnt(4)\n\t" "s_waitcnt vmcnt(15)\n\t"
    "v_mfma_f32_16x16x32_bf16 v[48:51], a[128:131], a[112:115], v[48:51]\n\t" "ds_read_b64_tr_b16 a[144:145], v16 offset:61440\n\t"
    "v_mfma_f32_16x16x32_bf16 v[52:55], a[128:131], a[116:119], v[52:55]\n\t" "ds_read_b64_tr_b16 a[146:147], v24 offset:61440\n\t"
    "v_mfma_f32_16x16x32_bf16 v[56:59], a[132:135], a[112:115], v[56:59]\n\t" "ds_read_b64_tr_b16 a[148:149], v17 offset:61440\n\t" "global_load_dwordx4 a[104:107], v0, s[40:41] offset:2048 sc1\n\t"
    "v_mfma_f32_16x16x32_bf16 v[60:63], a[132:135], a[116:119], v[60:63]\n\t" "ds_read_b64_tr_b16 a[150:151], v25 offset:61440\n\t" "s_waitcnt lgkmcnt(4)\n\t"
    "v_mfma_f32_16x16x32_bf16 v[64:67], a[136:139], a[112:115], v[64:67]\n\t" "ds_read_b64_tr_b16 a[152:153], v18 offset:61440\n\t"
    "v_mfma_f32_16x16x32_bf16 v[68:71], a[136:139], a[116:119], v[68:71]\n\t" "ds_read_b64_tr_b16 a[154:155], v26 offset:61440\n\t"
    "v_mfma_f32_16x16x32_bf16 v[72:75], a[140:143], a[112:115], v[72:75]\n\t" "ds_read_b64_tr_b16 a[156:157], v19 offset:61440\n\t" "global_load_dwordx4 a[108:111], v0, s[40:41] offset:3072 sc1\n\t"
    "s_add_u32 s40, s40, 4096\n\t" "s_addc_u32 s41, s41, 0\n\t" "v_mfma_f32_16x16x32_bf16 v[76:79], a[140:143], a[116:119], v[76:79]\n\t" "ds_read_b64_tr_b16 a[158:159], v27 offset:61440\n\t"
    "s_waitcnt lgkmcnt(4)\n\t" "s_waitcnt vmcnt(15)\n\t" "v_mfma_f32_16x16x32_bf16 v[48:51], a[144:147], a[120:123], v[48:51]\n\t" "ds_read_b64_tr_b16 a[128:129], v20 offset:0\n\t"
    "v_mfma_f32_16x16x32_bf16 v[52:55], a[144:147], a[124:127], v[52:55]\n\t" "ds_read_b64_tr_b16 a[130:131], v28 offset:0\n\t" "v_mfma_f32_16x16x32_bf16 v[56:59], a[148:151], a[120:123], v[56:59]\n\t"
    "ds_read_b64_tr_b16 a[132:133], v21 offset:0\n\t" "global_load_dwordx4 a[112:115], v0, s[40:41] offset:0 sc1\n\t" "v_mfma_f32_16x16x32_bf16 v[60:63], a[148:151], a[124:127], v[60:63]\n\t"
    "ds_read_b64_tr_b16 a[134:135], v29 offset:0\n\t" "s_waitcnt lgkmcnt(4)\n\t" "v_mfma_f32_16x16x32_bf16 v[64:67], a[152:155], a[120:123], v[64:67]\n\t" "ds_read_b64_tr_b16 a[136:137], v22 offset:0\n\t"
    "v_mfma_f32_16x16x32_bf16 v[68:71], a[152:155], a[124:127], v[68:71]\n\t" "ds_read_b64_tr_b16 a[138:139], v30 offset:0\n\t" "v_mfma_f32_16x16x32_bf16 v[72:75], a[156:159], a[120:123], v[72:75]\n\t"
    "ds_read_b64_tr_b16 a[140:141], v23 offset:0\n\t" "global_load_dwordx4 a[116:119], v0, s[40:41] offset:1024 sc1\n\t" "v_mfma_f32_16x16x32_bf16 v[76:79], a[156:159], a[124:127], v[76:79]\n\t"
    "ds_read_b64_tr_b16 a[142:143], v31 offset:0\n\t" "s_waitcnt lgkmcnt(4)\n\t" "s_waitcnt vmcnt(15)\n\t" "v_mfma_f32_16x16x32_bf16 v[48:51], a[128:131], a[64:67], v[48:51]\n\t"
    "ds_read_b64_tr_b16 a[144:145], v20 offset:4096\n\t" "s_waitcnt vmcnt(11)\n\t" "v_xad_u32 v156, v112, v113, v164\n\t" "v_mfma_f32_16x16x32_bf16 v[52:55], a[128:131], a[68:71], v[52:55]\n\t"
    "ds_read_b64_tr_b16 a[146:147], v28 offset:4096\n\t" "v_xad_u32 v157, v116, v117, v164\n\t" "v_xad_u32 v158, v120, v121, v164\n\t"
    "v_mfma_f32_16x16x32_bf16 v[56:59], a[132:135], a[64:67], v[56:59]\n\t" "ds_read_b64_tr_b16 a[148:149], v21 offset:4096\n\t" "global_load_dwordx4 a[120:123], v0, s[40:41] offset:2048 sc1\n\t"
    "v_xad_u32 v156, v114, v113, v156\n\t" "v_mfma_f32_16x16x32_bf16 v[60:63], a[132:135], a[68:71], v[60:63]\n\t" "ds_read_b64_tr_b16 a[150:151], v29 offset:4096\n\t"
    "v_xad_u32 v157, v118, v117, v157\n\t" "v_xad_u32 v158, v122, v121, v158\n\t" "s_waitcnt lgkmcnt(4)\n\t" "v_mfma_f32_16x16x32_bf16 v[64:67], a[136:139], a[64:67], v[64:67]\n\t"
    "ds_read_b64_tr_b16 a[152:153], v22 offset:4096\n\t" "v_xor_b32 v156, v156, v115\n\t" "v_bitop3_b32 v156, v156, v157, v119 bitop3:0xf6\n\t"
    "v_mfma_f32_16x16x32_bf16 v[68:71], a[136:139], a[68:71], v[68:71]\n\t" "ds_read_b64_tr_b16 a[154:155], v30 offset:4096\n\t" "v_bitop3_b32 v156, v156, v158, v123 bitop3:0xf6\n\t"
    "v_cmp_ne_u32 vcc, 0, v156\n\t" "v_mfma_f32_16x16x32_bf16 v[72:75], a[140:143], a[64:67], v[72:75]\n\t" "ds_read_b64_tr_b16 a[156:157], v23 offset:4096\n\t"
    "global_load_dwordx4 a[124:127], v0, s[40:41] offset:3072 sc1\n\t" "s_add_u32 s40, s40, 4096\n\t" "s_addc_u32 s41, s41, 0\n\t" "v_mfma_f32_16x16x32_bf16 v[76:79], a[140:143], a[68:71], v[76:79]\n\t"
    "ds_read_b64_tr_b16 a[158:159], v31 offset:4096\n\t" "v_perm_b32 v115, v112, v113, s49\n\t" "v_perm_b32 v119, v116, v117, s49\n\t" "s_waitcnt lgkmcnt(4)\n\t" "s_waitcnt vmcnt(12)\n\t"
    "v_mfma_f32_16x16x32_bf16 v[48:51], a[144:147], a[72:75], v[48:51]\n\t" "ds_read_b64_tr_b16 a[128:129], v20 offset:8192\n\t" "v_perm_b32 v123, v120, v121, s49\n\t" "s_nop 1\n\t"
    "v_mfma_f32_16x16x32_bf16 v[52:55], a[144:147], a[76:79], v[52:55]\n\t" "ds_read_b64_tr_b16 a[130:131], v28 offset:8192\n\t" "s_cbranch_vccnz .Lslow_%=\n\t" ".Lgo_%=:\n\t"
    "v_mfma_f32_16x16x32_bf16 v[56:59], a[148:151], a[72:75], v[56:59]\n\t" "ds_read_b64_tr_b16 a[132:133], v21 offset:8192\n\t" "global_load_dwordx4 a[64:67], v0, s[40:41] offset:0 sc1\n\t"
    "v_perm_b32 v115, v115, v114, s50\n\t" "v_mfma_f32_16x16x32_bf16 v[60:63], a[148:151], a[76:79], v[60:63]\n\t" "ds_read_b64_tr_b16 a[134:135], v29 offset:8192\n\t"
    "v_perm_b32 v119, v119, v118, s50\n\t" "v_perm_b32 v123, v123, v122, s50\n\t" "s_waitcnt lgkmcnt(4)\n\t" "v_mfma_f32_16x16x32_bf16 v[64:67], a[152:155], a[72:75], v[64:67]\n\t"
    "ds_read_b64_tr_b16 a[136:137], v22 offset:8192\n\t" "v_permlane32_swap_b32 v112, v114\n\t" "v_permlane32_swap_b32 v116, v118\n\t"
    "v_mfma_f32_16x16x32_bf16 v[68:71], a[152:155], a[76:79], v[68:71]\n\t" "ds_read_b64_tr_b16 a[138:139], v30 offset:8192\n\t" "v_permlane32_swap_b32 v120, v122\n\t"
    "v_permlane32_swap_b32 v113, v115\n\t" "v_mfma_f32_16x16x32_bf16 v[72:75], a[156:159], a[72:75], v[72:75]\n\t" "ds_read_b64_tr_b16 a[140:141], v23 offset:8192\n\t"
    "global_load_dwordx4 a[68:71], v0, s[40:41] offset:1024 sc1\n\t" "v_permlane32_swap_b32 v117, v119\n\t" "v_mfma_f32_16x16x32_bf16 v[76:79], a[156:159], a[76:79], v[76:79]\n\t"
    "ds_read_b64_tr_b16 a[142:143], v31 offset:8192\n\t" "v_permlane32_swap_b32 v121, v123\n\t" "v_add_f32 v112, v112, v114\n\t" "s_waitcnt lgkmcnt(4)\n\t" "s_waitcnt vmcnt(12)\n\t"
    "v_mfma_f32_16x16x32_bf16 v[48:51], a[128:131], a[80:83], v[48:51]\n\t" "ds_read_b64_tr_b16 a[144:145], v20 offset:12288\n\t" "v_add_f32 v116, v116, v118\n\t" "v_add_f32 v120, v120, v122\n\t"
    "v_mfma_f32_16x16x32_bf16 v[52:55], a[128:131], a[84:87], v[52:55]\n\t" "ds_read_b64_tr_b16 a[146:147], v28 offset:12288\n\t" "v_add_f32 v113, v113, v115\n\t" "v_add_f32 v117, v117, v119\n\t"
    "v_mfma_f32_16x16x32_bf16 v[56:59], a[132:135], a[80:83], v[56:59]\n\t" "ds_read_b64_tr_b16 a[148:149], v21 offset:12288\n\t" "global_load_dwordx4 a[72:75], v0, s[40:41] offset:2048 sc1\n\t"
    "v_add_f32 v121, v121, v123\n\t" "v_mfma_f32_16x16x32_bf16 v[60:63], a[132:135], a[84:87], v[60:63]\n\t" "ds_read_b64_tr_b16 a[150:151], v29 offset:12288\n\t" "v_permlane16_swap_b32 v112, v113\n\t"
    "v_permlane16_swap_b32 v116, v117\n\t" "s_waitcnt lgkmcnt(4)\n\t" "v_mfma_f32_16x16x32_bf16 v[64:67], a[136:139], a[80:83], v[64:67]\n\t" "ds_read_b64_tr_b16 a[152:153], v22 offset:12288\n\t"
    "v_permlane16_swap_b32 v120, v121\n\t" "v_add_f32 v112, v112, v113\n\t" "v_mfma_f32_16x16x32_bf16 v[68:71], a[136:139], a[84:87], v[68:71]\n\t" "ds_read_b64_tr_b16 a[154:155], v30 offset:12288\n\t"
    "v_add_f32 v116, v116, v117\n\t" "v_add_f32 v120, v120, v121\n\t" "v_mfma_f32_16x16x32_bf16 v[72:75], a[140:143], a[80:83], v[72:75]\n\t" "ds_read_b64_tr_b16 a[156:157], v23 offset:12288\n\t"
    "global_load_dwordx4 a[76:79], v0, s[40:41] offset:3072 sc1\n\t" "s_add_u32 s40, s40, 4096\n\t" "s_addc_u32 s41, s41, 0\n\t" "v_mfma_f32_16x16x32_bf16 v[76:79], a[140:143], a[84:87], v[76:79]\n\t"
    "ds_read_b64_tr_b16 a[158:159], v31 offset:12288\n\t" "v_cvt_pk_bf16_f32 v112, v112, v112\n\t" "v_cvt_pk_bf16_f32 v116, v116, v116\n\t" "s_waitcnt lgkmcnt(4)\n\t" "s_waitcnt vmcnt(12)\n\t"
    "v_mfma_f32_16x16x32_bf16 v[48:51], a[144:147], a[88:91], v[48:51]\n\t" "ds_read_b64_tr_b16 a[128:129], v20 offset:16384\n\t" "v_cvt_pk_bf16_f32 v120, v120, v120\n\t"
    "v_and_b32 v112, 0xffff0000, v112\n\t" "v_mfma_f32_16x16x32_bf16 v[52:55], a[144:147], a[92:95], v[52:55]\n\t" "ds_read_b64_tr_b16 a[130:131], v28 offset:16384\n\t"
    "v_and_b32 v116, 0xffff0000, v116\n\t" "v_and_b32 v120, 0xffff0000, v120\n\t" "v_mfma_f32_16x16x32_bf16 v[56:59], a[148:151], a[88:91], v[56:59]\n\t"
    "ds_read_b64_tr_b16 a[132:133], v21 offset:16384\n\t" "global_load_dwordx4 a[80:83], v0, s[40:41] offset:0 sc1\n\t" "v_add_f32 v116, 1.0, v116\n\t"
    "v_mfma_f32_16x16x32_bf16 v[60:63], a[148:151], a[92:95], v[60:63]\n\t" "ds_read_b64_tr_b16 a[134:135], v29 offset:16384\n\t" "v_cvt_pk_bf16_f32 v116, v116, v116\n\t"
    "v_and_b32 v116, 0xffff0000, v116\n\t" "s_waitcnt lgkmcnt(4)\n\t" "v_mfma_f32_16x16x32_bf16 v[64:67], a[152:155], a[88:91], v[64:67]\n\t" "ds_read_b64_tr_b16 a[136:137], v22 offset:16384\n\t"
    "ds_write2st64_b32 v15, v112, v116 offset1:1\n\t" "s_cmp_eq_u32 %[role], 0\n\t" "s_cselect_b64 exec, -1, 0\n\t" "global_store_short_d16_hi v33, v120, %[se] sc1\n\t" "s_mov_b64 exec, -1\n\t"
    "v_mfma_f32_16x16x32_bf16 v[68:71], a[152:155], a[92:95], v[68:71]\n\t" "ds_read_b64_tr_b16 a[138:139], v30 offset:16384\n\t" "s_cmp_eq_u32 %[role], 1\n\t" "s_cselect_b64 exec, -1, 0\n\t"
    "global_store_dword v33, v120, %[se] sc1\n\t" "s_mov_b64 exec, -1\n\t" "v_mfma_f32_16x16x32_bf16 v[72:75], a[156:159], a[88:91], v[72:75]\n\t" "ds_read_b64_tr_b16 a[140:141], v23 offset:16384\n\t"
    "global_load_dwordx4 a[84:87], v0, s[40:41] offset:1024 sc1\n\t" "s_cmp_eq_u32 %[role], 2\n\t" "s_cselect_b64 exec, -1, 0\n\t" "global_store_short_d16_hi v33, v116, %[se] sc1\n\t"
    "s_mov_b64 exec, -1\n\t" "v_mfma_f32_16x16x32_bf16 v[76:79], a[156:159], a[92:95], v[76:79]\n\t" "ds_read_b64_tr_b16 a[142:143], v31 offset:16384\n\t" "ds_read_b128 v[124:127], v32 offset:0\n\t"
    "ds_read_b128 v[128:131], v32 offset:64\n\t" "s_waitcnt lgkmcnt(7)\n\t" "s_waitcnt vmcnt(12)\n\t" "v_mfma_f32_16x16x32_bf16 v[48:51], a[128:131], a[96:99], v[48:51]\n\t"
    "ds_read_b64_tr_b16 a[144:145], v20 offset:20480\n\t" "ds_read_b128 v[132:135], v32 offset:128\n\t" "ds_read_b128 v[136:139], v32 offset:192\n\t"
    "v_mfma_f32_16x16x32_bf16 v[52:55], a[128:131], a[100:103], v[52:55]\n\t" "ds_read_b64_tr_b16 a[146:147], v28 offset:20480\n\t" "ds_read_b128 v[140:143], v32 offset:256\n\t"
    "ds_read_b128 v[144:147], v32 offset:320\n\t" "v_mfma_f32_16x16x32_bf16 v[56:59], a[132:135], a[96:99], v[56:59]\n\t" "ds_read_b64_tr_b16 a[148:149], v21 offset:20480\n\t"
    "global_load_dwordx4 a[88:91], v0, s[40:41] offset:2048 sc1\n\t" "ds_read_b128 v[148:151], v32 offset:384\n\t" "v_mfma_f32_16x16x32_bf16 v[60:63], a[132:135], a[100:103], v[60:63]\n\t"
    "s_waitcnt lgkmcnt(11)\n\t" "ds_read_b64_tr_b16 a[150:151], v29 offset:20480\n\t" "ds_read_b128 v[152:155], v32 offset:448\n\t" "s_waitcnt lgkmcnt(12)\n\t"
    "v_mfma_f32_16x16x32_bf16 v[64:67], a[136:139], a[96:99], v[64:67]\n\t" "ds_read_b64_tr_b16 a[152:153], v22 offset:20480\n\t" "v_mfma_f32_16x16x32_bf16 v[68:71], a[136:139], a[100:103], v[68:71]\n\t"
    "ds_read_b64_tr_b16 a[154:155], v30 offset:20480\n\t" "v_mfma_f32_16x16x32_bf16 v[72:75], a[140:143], a[96:99], v[72:75]\n\t" "ds_read_b64_tr_b16 a[156:157], v23 offset:20480\n\t"
    "global_load_dwordx4 a[92:95], v0, s[40:41] offset:3072 sc1\n\t" "s_add_u32 s40, s40, 4096\n\t" "s_addc_u32 s41, s41, 0\n\t" "v_mfma_f32_16x16x32_bf16 v[76:79], a[140:143], a[100:103], v[76:79]\n\t"
    "s_waitcnt lgkmcnt(11)\n\t" "ds_read_b64_tr_b16 a[158:159], v31 offset:20480\n\t" "s_waitcnt lgkmcnt(5)\n\t" "s_waitcnt vmcnt(12)\n\t"
    "v_mfma_f32_16x16x32_bf16 v[48:51], a[144:147], a[104:107], v[48:51]\n\t" "ds_read_b64_tr_b16 a[128:129], v20 offset:24576\n\t"
    "v_mfma_f32_16x16x32_bf16 v[52:55], a[144:147], a[108:111], v[52:55]\n\t" "ds_read_b64_tr_b16 a[130:131], v28 offset:24576\n\t"
    "v_mfma_f32_16x16x32_bf16 v[56:59], a[148:151], a[104:107], v[56:59]\n\t" "ds_read_b64_tr_b16 a[132:133], v21 offset:24576\n\t" "global_load_dwordx4 a[96:99], v0, s[40:41] offset:0 sc1\n\t"
    "v_mfma_f32_16x16x32_bf16 v[60:63], a[148:151], a[108:111], v[60:63]\n\t" "ds_read_b64_tr_b16 a[134:135], v29 offset:24576\n\t" "s_waitcnt lgkmcnt(4)\n\t"
    "v_mfma_f32_16x16x32_bf16 v[64:67], a[152:155], a[104:107], v[64:67]\n\t" "ds_read_b64_tr_b16 a[136:137], v22 offset:24576\n\t"
    "v_mfma_f32_16x16x32_bf16 v[68:71], a[152:155], a[108:111], v[68:71]\n\t" "ds_read_b64_tr_b16 a[138:139], v30 offset:24576\n\t"
    "v_mfma_f32_16x16x32_bf16 v[72:75], a[156:159], a[104:107], v[72:75]\n\t" "ds_read_b64_tr_b16 a[140:141], v23 offset:24576\n\t" "global_load_dwordx4 a[100:103], v0, s[40:41] offset:1024 sc1\n\t"
    "v_mfma_f32_16x16x32_bf16 v[76:79], a[156:159], a[108:111], v[76:79]\n\t" "ds_read_b64_tr_b16 a[142:143], v31 offset:24576\n\t" "s_waitcnt lgkmcnt(4)\n\t" "s_waitcnt vmcnt(12)\n\t"
    "v_mfma_f32_16x16x32_bf16 v[48:51], a[128:131], a[112:115], v[48:51]\n\t" "ds_read_b64_tr_b16 a[144:145], v20 offset:28672\n\t"
    "v_mfma_f32_16x16x32_bf16 v[52:55], a[128:131], a[116:119], v[52:55]\n\t" "ds_read_b64_tr_b16 a[146:147], v28 offset:28672\n\t"
    "v_mfma_f32_16x16x32_bf16 v[56:59], a[132:135], a[112:115], v[56:59]\n\t" "ds_read_b64_tr_b16 a[148:149], v21 offset:28672\n\t" "global_load_dwordx4 a[104:107], v0, s[40:41] offset:2048 sc1\n\t"
    "v_mfma_f32_16x16x32_bf16 v[60:63], a[132:135], a[116:119], v[60:63]\n\t" "ds_read_b64_tr_b16 a[150:151], v29 offset:28672\n\t" "s_waitcnt lgkmcnt(4)\n\t"
    "v_mfma_f32_16x16x32_bf16 v[64:67], a[136:139], a[112:115], v[64:67]\n\t" "ds_read_b64_tr_b16 a[152:153], v22 offset:28672\n\t"
    "v_mfma_f32_16x16x32_bf16 v[68:71], a[136:139], a[116:119], v[68:71]\n\t" "ds_read_b64_tr_b16 a[154:155], v30 offset:28672\n\t"
    "v_mfma_f32_16x16x32_bf16 v[72:75], a[140:143], a[112:115], v[72:75]\n\t" "ds_read_b64_tr_b16 a[156:157], v23 offset:28672\n\t" "global_load_dwordx4 a[108:111], v0, s[40:41] offset:3072 sc1\n\t"
    "s_add_u32 s40, s40, 4096\n\t" "s_addc_u32 s41, s41, 0\n\t" "v_mfma_f32_16x16x32_bf16 v[76:79], a[140:143], a[116:119], v[76:79]\n\t" "ds_read_b64_tr_b16 a[158:159], v31 offset:28672\n\t"
    "s_waitcnt lgkmcnt(4)\n\t" "s_waitcnt vmcnt(12)\n\t" "v_mfma_f32_16x16x32_bf16 v[48:51], a[144:147], a[120:123], v[48:51]\n\t" "ds_read_b64_tr_b16 a[128:129], v20 offset:32768\n\t"
    "v_mfma_f32_16x16x32_bf16 v[52:55], a[144:147], a[124:127], v[52:55]\n\t" "ds_read_b64_tr_b16 a[130:131], v28 offset:32768\n\t"
    "v_mfma_f32_16x16x32_bf16 v[56:59], a[148:151], a[120:123], v[56:59]\n\t" "ds_read_b64_tr_b16 a[132:133], v21 offset:32768\n\t" "global_load_dwordx4 a[112:115], v0, s[40:41] offset:0 sc1\n\t"
    "v_mfma_f32_16x16x32_bf16 v[60:63], a[148:151], a[124:127], v[60:63]\n\t" "ds_read_b64_tr_b16 a[134:135], v29 offset:32768\n\t" "s_waitcnt lgkmcnt(4)\n\t"
    "v_mfma_f32_16x16x32_bf16 v[64:67], a[152:155], a[120:123], v[64:67]\n\t" "ds_read_b64_tr_b16 a[136:137], v22 offset:32768\n\t"
    "v_mfma_f32_16x16x32_bf16 v[68:71], a[152:155], a[124:127], v[68:71]\n\t" "ds_read_b64_tr_b16 a[138:139], v30 offset:32768\n\t"
    "v_mfma_f32_16x16x32_bf16 v[72:75], a[156:159], a[120:123], v[72:75]\n\t" "ds_read_b64_tr_b16 a[140:141], v23 offset:32768\n\t" "global_load_dwordx4 a[116:119], v0, s[40:41] offset:1024 sc1\n\t"
    "v_mfma_f32_16x16x32_bf16 v[76:79], a[156:159], a[124:127], v[76:79]\n\t" "ds_read_b64_tr_b16 a[142:143], v31 offset:32768\n\t" "s_waitcnt lgkmcnt(4)\n\t" "s_waitcnt vmcnt(12)\n\t"
    "v_mfma_f32_16x16x32_bf16 v[48:51], a[128:131], a[64:67], v[48:51]\n\t" "ds_read_b64_tr_b16 a[144:145], v20 offset:36864\n\t" "v_mfma_f32_16x16x32_bf16 v[52:55], a[128:131], a[68:71], v[52:55]\n\t"
    "ds_read_b64_tr_b16 a[146:147], v28 offset:36864\n\t" "v_mfma_f32_16x16x32_bf16 v[56:59], a[132:135], a[64:67], v[56:59]\n\t" "ds_read_b64_tr_b16 a[148:149], v21 offset:36864\n\t"
    "global_load_dwordx4 a[120:123], v0, s[40:41] offset:2048 sc1\n\t" "v_mfma_f32_16x16x32_bf16 v[60:63], a[132:135], a[68:71], v[60:63]\n\t" "ds_read_b64_tr_b16 a[150:151], v29 offset:36864\n\t"
    "s_waitcnt lgkmcnt(4)\n\t" "v_mfma_f32_16x16x32_bf16 v[64:67], a[136:139], a[64:67], v[64:67]\n\t" "ds_read_b64_tr_b16 a[152:153], v22 offset:36864\n\t"
    "v_mfma_f32_16x16x32_bf16 v[68:71], a[136:139], a[68:71], v[68:71]\n\t" "ds_read_b64_tr_b16 a[154:155], v30 offset:36864\n\t" "v_mfma_f32_16x16x32_bf16 v[72:75], a[140:143], a[64:67], v[72:75]\n\t"
    "ds_read_b64_tr_b16 a[156:157], v23 offset:36864\n\t" "global_load_dwordx4 a[124:127], v0, s[40:41] offset:3072 sc1\n\t" "s_add_u32 s40, s40, 4096\n\t" "s_addc_u32 s41, s41, 0\n\t"
    "v_mfma_f32_16x16x32_bf16 v[76:79], a[140:143], a[68:71], v[76:79]\n\t" "ds_read_b64_tr_b16 a[158:159], v31 offset:36864\n\t" "s_waitcnt lgkmcnt(4)\n\t" "s_waitcnt vmcnt(12)\n\t"
    "v_mfma_f32_16x16x32_bf16 v[48:51], a[144:147], a[72:75], v[48:51]\n\t" "ds_read_b64_tr_b16 a[128:129], v20 offset:40960\n\t" "v_mfma_f32_16x16x32_bf16 v[52:55], a[144:147], a[76:79], v[52:55]\n\t"
    "ds_read_b64_tr_b16 a[130:131], v28 offset:40960\n\t" "v_mfma_f32_16x16x32_bf16 v[56:59], a[148:151], a[72:75], v[56:59]\n\t" "ds_read_b64_tr_b16 a[132:133], v21 offset:40960\n\t"
    "s_mov_b64 s[78:79], %[sb]\n\t" "s_mov_b32 s82, -1\n\t" "s_mov_b32 s83, 0\n\t" "s_add_u32 m0, %[sl], 86016\n\t" "s_nop 0\n\t" "global_load_lds_dwordx4 v1, s[78:79]\n\t"
    "v_mfma_f32_16x16x32_bf16 v[60:63], a[148:151], a[76:79], v[60:63]\n\t" "ds_read_b64_tr_b16 a[134:135], v29 offset:40960\n\t" "s_waitcnt lgkmcnt(4)\n\t"
    "v_mfma_f32_16x16x32_bf16 v[64:67], a[152:155], a[72:75], v[64:67]\n\t" "ds_read_b64_tr_b16 a[136:137], v22 offset:40960\n\t" "v_mfma_f32_16x16x32_bf16 v[68:71], a[152:155], a[76:79], v[68:71]\n\t"
    "ds_read_b64_tr_b16 a[138:139], v30 offset:40960\n\t" "v_mfma_f32_16x16x32_bf16 v[72:75], a[156:159], a[72:75], v[72:75]\n\t" "ds_read_b64_tr_b16 a[140:141], v23 offset:40960\n\t"
    "s_add_u32 m0, %[sl], 87040\n\t" "s_nop 0\n\t" "global_load_lds_dwordx4 v2, s[78:79]\n\t" "v_mfma_f32_16x16x32_bf16 v[76:79], a[156:159], a[76:79], v[76:79]\n\t"
    "ds_read_b64_tr_b16 a[142:143], v31 offset:40960\n\t" "s_waitcnt lgkmcnt(4)\n\t" "s_waitcnt vmcnt(12)\n\t" "v_mfma_f32_16x16x32_bf16 v[48:51], a[128:131], a[80:83], v[48:51]\n\t"
    "ds_read_b64_tr_b16 a[144:145], v20 offset:45056\n\t" "v_mfma_f32_16x16x32_bf16 v[52:55], a[128:131], a[84:87], v[52:55]\n\t" "ds_read_b64_tr_b16 a[146:147], v28 offset:45056\n\t"
    "v_mfma_f32_16x16x32_bf16 v[56:59], a[132:135], a[80:83], v[56:59]\n\t" "ds_read_b64_tr_b16 a[148:149], v21 offset:45056\n\t" "s_add_u32 m0, %[sl], 88064\n\t" "s_nop 0\n\t"
    "global_load_lds_dwordx4 v3, s[78:79]\n\t" "v_mfma_f32_16x16x32_bf16 v[60:63], a[132:135], a[84:87], v[60:63]\n\t" "ds_read_b64_tr_b16 a[150:151], v29 offset:45056\n\t" "s_waitcnt lgkmcnt(4)\n\t"
    "v_mfma_f32_16x16x32_bf16 v[64:67], a[136:139], a[80:83], v[64:67]\n\t" "ds_read_b64_tr_b16 a[152:153], v22 offset:45056\n\t" "v_mfma_f32_16x16x32_bf16 v[68:71], a[136:139], a[84:87], v[68:71]\n\t"
    "ds_read_b64_tr_b16 a[154:155], v30 offset:45056\n\t" "v_mfma_f32_16x16x32_bf16 v[72:75], a[140:143], a[80:83], v[72:75]\n\t" "ds_read_b64_tr_b16 a[156:157], v23 offset:45056\n\t"
    "s_add_u32 m0, %[sl], 89088\n\t" "s_nop 0\n\t" "s_mov_b64 exec, s[82:83]\n\t" "global_load_lds_dwordx4 v4, s[78:79]\n\t" "s_mov_b64 exec, -1\n\t" "s_add_u32 s78, s78, 229376\n\t"
    "s_addc_u32 s79, s79, 0\n\t" "v_mfma_f32_16x16x32_bf16 v[76:79], a[140:143], a[84:87], v[76:79]\n\t" "ds_read_b64_tr_b16 a[158:159], v31 offset:45056\n\t" "s_waitcnt lgkmcnt(4)\n\t"
    "s_waitcnt vmcnt(12)\n\t" "v_mfma_f32_16x16x32_bf16 v[48:51], a[144:147], a[88:91], v[48:51]\n\t" "ds_read_b64_tr_b16 a[128:129], v20 offset:49152\n\t"
    "v_mfma_f32_16x16x32_bf16 v[52:55], a[144:147], a[92:95], v[52:55]\n\t" "ds_read_b64_tr_b16 a[130:131], v28 offset:49152\n\t" "v_mfma_f32_16x16x32_bf16 v[56:59], a[148:151], a[88:91], v[56:59]\n\t"
    "ds_read_b64_tr_b16 a[132:133], v21 offset:49152\n\t" "s_add_u32 m0, %[sl], 100352\n\t" "s_nop 0\n\t" "global_load_lds_dwordx4 v1, s[78:79]\n\t"
    "v_mfma_f32_16x16x32_bf16 v[60:63], a[148:151], a[92:95], v[60:63]\n\t" "ds_read_b64_tr_b16 a[134:135], v29 offset:49152\n\t" "s_waitcnt lgkmcnt(4)\n\t"
    "v_mfma_f32_16x16x32_bf16 v[64:67], a[152:155], a[88:91], v[64:67]\n\t" "ds_read_b64_tr_b16 a[136:137], v22 offset:49152\n\t" "v_mfma_f32_16x16x32_bf16 v[68:71], a[152:155], a[92:95], v[68:71]\n\t"
    "ds_read_b64_tr_b16 a[138:139], v30 offset:49152\n\t" "v_mfma_f32_16x16x32_bf16 v[72:75], a[156:159], a[88:91], v[72:75]\n\t" "ds_read_b64_tr_b16 a[140:141], v23 offset:49152\n\t"
    "s_add_u32 m0, %[sl], 101376\n\t" "s_nop 0\n\t" "global_load_lds_dwordx4 v2, s[78:79]\n\t" "v_mfma_f32_16x16x32_bf16 v[76:79], a[156:159], a[92:95], v[76:79]\n\t"
    "ds_read_b64_tr_b16 a[142:143], v31 offset:49152\n\t" "s_waitcnt lgkmcnt(4)\n\t" "s_waitcnt vmcnt(12)\n\t" "v_mfma_f32_16x16x32_bf16 v[48:51], a[128:131], a[96:99], v[48:51]\n\t"
    "ds_read_b64_tr_b16 a[144:145], v20 offset:53248\n\t" "v_mfma_f32_16x16x32_bf16 v[52:55], a[128:131], a[100:103], v[52:55]\n\t" "ds_read_b64_tr_b16 a[146:147], v28 offset:53248\n\t"
    "v_mfma_f32_16x16x32_bf16 v[56:59], a[132:135], a[96:99], v[56:59]\n\t" "ds_read_b64_tr_b16 a[148:149], v21 offset:53248\n\t" "s_add_u32 m0, %[sl], 102400\n\t" "s_nop 0\n\t"
    "global_load_lds_dwordx4 v3, s[78:79]\n\t" "v_mfma_f32_16x16x32_bf16 v[60:63], a[132:135], a[100:103], v[60:63]\n\t" "ds_read_b64_tr_b16 a[150:151], v29 offset:53248\n\t" "s_waitcnt lgkmcnt(4)\n\t"
    "v_mfma_f32_16x16x32_bf16 v[64:67], a[136:139], a[96:99], v[64:67]\n\t" "ds_read_b64_tr_b16 a[152:153], v22 offset:53248\n\t" "v_mfma_f32_16x16x32_bf16 v[68:71], a[136:139], a[100:103], v[68:71]\n\t"
    "ds_read_b64_tr_b16 a[154:155], v30 offset:53248\n\t" "v_mfma_f32_16x16x32_bf16 v[72:75], a[140:143], a[96:99], v[72:75]\n\t" "ds_read_b64_tr_b16 a[156:157], v23 offset:53248\n\t"
    "s_add_u32 m0, %[sl], 103424\n\t" "s_nop 0\n\t" "s_mov_b64 exec, s[82:83]\n\t" "global_load_lds_dwordx4 v4, s[78:79]\n\t" "s_mov_b64 exec, -1\n\t" "s_add_u32 s78, s78, 229376\n\t"
    "s_addc_u32 s79, s79, 0\n\t" "v_mfma_f32_16x16x32_bf16 v[76:79], a[140:143], a[100:103], v[76:79]\n\t" "ds_read_b64_tr_b16 a[158:159], v31 offset:53248\n\t" "s_waitcnt lgkmcnt(4)\n\t"
    "s_waitcnt vmcnt(12)\n\t" "v_mfma_f32_16x16x32_bf16 v[48:51], a[144:147], a[104:107], v[48:51]\n\t" "ds_read_b64_tr_b16 a[128:129], v20 offset:57344\n\t"
    "v_mfma_f32_16x16x32_bf16 v[52:55], a[144:147], a[108:111], v[52:55]\n\t" "ds_read_b64_tr_b16 a[130:131], v28 offset:57344\n\t"
    "v_mfma_f32_16x16x32_bf16 v[56:59], a[148:151], a[104:107], v[56:59]\n\t" "ds_read_b64_tr_b16 a[132:133], v21 offset:57344\n\t"
    "v_mfma_f32_16x16x32_bf16 v[60:63], a[148:151], a[108:111], v[60:63]\n\t" "ds_read_b64_tr_b16 a[134:135], v29 offset:57344\n\t" "s_waitcnt lgkmcnt(4)\n\t"
    "v_mfma_f32_16x16x32_bf16 v[64:67], a[152:155], a[104:107], v[64:67]\n\t" "ds_read_b64_tr_b16 a[136:137], v22 offset:57344\n\t"
    "v_mfma_f32_16x16x32_bf16 v[68:71], a[152:155], a[108:111], v[68:71]\n\t" "ds_read_b64_tr_b16 a[138:139], v30 offset:57344\n\t"
    "v_mfma_f32_16x16x32_bf16 v[72:75], a[156:159], a[104:107], v[72:75]\n\t" "ds_read_b64_tr_b16 a[140:141], v23 offset:57344\n\t"
    "v_mfma_f32_16x16x32_bf16 v[76:79], a[156:159], a[108:111], v[76:79]\n\t" "ds_read_b64_tr_b16 a[142:143], v31 offset:57344\n\t" "s_waitcnt lgkmcnt(4)\n\t" "s_waitcnt vmcnt(10)\n\t"
    "v_mfma_f32_16x16x32_bf16 v[48:51], a[128:131], a[112:115], v[48:51]\n\t" "ds_read_b64_tr_b16 a[144:145], v20 offset:61440\n\t"
    "v_mfma_f32_16x16x32_bf16 v[52:55], a[128:131], a[116:119], v[52:55]\n\t" "ds_read_b64_tr_b16 a[146:147], v28 offset:61440\n\t"
    "v_mfma_f32_16x16x32_bf16 v[56:59], a[132:135], a[112:115], v[56:59]\n\t" "ds_read_b64_tr_b16 a[148:149], v21 offset:61440\n\t"
    "v_mfma_f32_16x16x32_bf16 v[60:63], a[132:135], a[116:119], v[60:63]\n\t" "ds_read_b64_tr_b16 a[150:151], v29 offset:61440\n\t" "s_waitcnt lgkmcnt(4)\n\t"
    "v_mfma_f32_16x16x32_bf16 v[64:67], a[136:139], a[112:115], v[64:67]\n\t" "ds_read_b64_tr_b16 a[152:153], v22 offset:61440\n\t"
    "v_mfma_f32_16x16x32_bf16 v[68:71], a[136:139], a[116:119], v[68:71]\n\t" "ds_read_b64_tr_b16 a[154:155], v30 offset:61440\n\t"
    "v_mfma_f32_16x16x32_bf16 v[72:75], a[140:143], a[112:115], v[72:75]\n\t" "ds_read_b64_tr_b16 a[156:157], v23 offset:61440\n\t"
    "v_mfma_f32_16x16x32_bf16 v[76:79], a[140:143], a[116:119], v[76:79]\n\t" "ds_read_b64_tr_b16 a[158:159], v31 offset:61440\n\t" "s_waitcnt lgkmcnt(4)\n\t" "s_waitcnt vmcnt(8)\n\t"
    "v_mfma_f32_16x16x32_bf16 v[48:51], a[144:147], a[120:123], v[48:51]\n\t" "v_mfma_f32_16x16x32_bf16 v[52:55], a[144:147], a[124:127], v[52:55]\n\t"
    "v_mfma_f32_16x16x32_bf16 v[56:59], a[148:151], a[120:123], v[56:59]\n\t" "v_mfma_f32_16x16x32_bf16 v[60:63], a[148:151], a[124:127], v[60:63]\n\t" "s_waitcnt lgkmcnt(0)\n\t"
    "v_mfma_f32_16x16x32_bf16 v[64:67], a[152:155], a[120:123], v[64:67]\n\t" "v_mfma_f32_16x16x32_bf16 v[68:71], a[152:155], a[124:127], v[68:71]\n\t"
    "v_mfma_f32_16x16x32_bf16 v[72:75], a[156:159], a[120:123], v[72:75]\n\t" "v_mfma_f32_16x16x32_bf16 v[76:79], a[156:159], a[124:127], v[76:79]\n\t" "s_nop 1\n\t"
    "v_fma_f32 v48, v124, v48, v80\n\t" "v_fma_f32 v49, v125, v49, v81\n\t" "v_fma_f32 v50, v126, v50, v82\n\t" "v_fma_f32 v51, v127, v51, v83\n\t" "v_fma_f32 v56, v128, v56, v88\n\t"
    "v_fma_f32 v57, v129, v57, v89\n\t" "v_fma_f32 v58, v130, v58, v90\n\t" "v_fma_f32 v59, v131, v59, v91\n\t" "v_mul_f32 v156, v48, v140\n\t" "v_mul_f32 v157, v49, v141\n\t"
    "v_mul_f32 v158, v50, v142\n\t" "v_mul_f32 v159, v51, v143\n\t" "v_mul_f32 v160, v56, v144\n\t" "v_mul_f32 v161, v57, v145\n\t" "v_mul_f32 v162, v58, v146\n\t" "v_mul_f32 v163, v59, v147\n\t"
    "v_cvt_pk_bf16_f32 v164, v156, v157\n\t" "v_cvt_pk_bf16_f32 v165, v158, v159\n\t" "v_cvt_pk_bf16_f32 v166, v160, v161\n\t" "v_cvt_pk_bf16_f32 v167, v162, v163\n\t"
    "global_store_dwordx4 v0, v[164:167], %[sy] offset:-4096 sc1\n\t" "v_fma_f32 v52, v124, v52, v84\n\t" "v_fma_f32 v53, v125, v53, v85\n\t" "v_fma_f32 v54, v126, v54, v86\n\t"
    "v_fma_f32 v55, v127, v55, v87\n\t" "v_fma_f32 v60, v128, v60, v92\n\t" "v_fma_f32 v61, v129, v61, v93\n\t" "v_fma_f32 v62, v130, v62, v94\n\t" "v_fma_f32 v63, v131, v63, v95\n\t"
    "v_mul_f32 v172, v52, v140\n\t" "v_mul_f32 v173, v53, v141\n\t" "v_mul_f32 v174, v54, v142\n\t" "v_mul_f32 v175, v55, v143\n\t" "v_mul_f32 v176, v60, v144\n\t" "v_mul_f32 v177, v61, v145\n\t"
    "v_mul_f32 v178, v62, v146\n\t" "v_mul_f32 v179, v63, v147\n\t" "v_cvt_pk_bf16_f32 v180, v172, v173\n\t" "v_cvt_pk_bf16_f32 v181, v174, v175\n\t" "v_cvt_pk_bf16_f32 v182, v176, v177\n\t"
    "v_cvt_pk_bf16_f32 v183, v178, v179\n\t" "global_store_dwordx4 v0, v[180:183], %[sy] offset:-3072 sc1\n\t" "v_fma_f32 v64, v132, v64, v96\n\t" "v_fma_f32 v65, v133, v65, v97\n\t"
    "v_fma_f32 v66, v134, v66, v98\n\t" "v_fma_f32 v67, v135, v67, v99\n\t" "v_fma_f32 v72, v136, v72, v104\n\t" "v_fma_f32 v73, v137, v73, v105\n\t" "v_fma_f32 v74, v138, v74, v106\n\t"
    "v_fma_f32 v75, v139, v75, v107\n\t" "v_mul_f32 v156, v64, v148\n\t" "v_mul_f32 v157, v65, v149\n\t" "v_mul_f32 v158, v66, v150\n\t" "v_mul_f32 v159, v67, v151\n\t" "v_mul_f32 v160, v72, v152\n\t"
    "v_mul_f32 v161, v73, v153\n\t" "v_mul_f32 v162, v74, v154\n\t" "v_mul_f32 v163, v75, v155\n\t" "v_cvt_pk_bf16_f32 v168, v156, v157\n\t" "v_cvt_pk_bf16_f32 v169, v158, v159\n\t"
    "v_cvt_pk_bf16_f32 v170, v160, v161\n\t" "v_cvt_pk_bf16_f32 v171, v162, v163\n\t" "global_store_dwordx4 v0, v[168:171], %[sy] offset:0 sc1\n\t" "v_fma_f32 v68, v132, v68, v100\n\t"
    "v_fma_f32 v69, v133, v69, v101\n\t" "v_fma_f32 v70, v134, v70, v102\n\t" "v_fma_f32 v71, v135, v71, v103\n\t" "v_fma_f32 v76, v136, v76, v108\n\t" "v_fma_f32 v77, v137, v77, v109\n\t"
    "v_fma_f32 v78, v138, v78, v110\n\t" "v_fma_f32 v79, v139, v79, v111\n\t" "v_mul_f32 v172, v68, v148\n\t" "v_mul_f32 v173, v69, v149\n\t" "v_mul_f32 v174, v70, v150\n\t"
    "v_mul_f32 v175, v71, v151\n\t" "v_mul_f32 v176, v76, v152\n\t" "v_mul_f32 v177, v77, v153\n\t" "v_mul_f32 v178, v78, v154\n\t" "v_mul_f32 v179, v79, v155\n\t" "v_cvt_pk_bf16_f32 v184, v172, v173\n\t"
    "v_cvt_pk_bf16_f32 v185, v174, v175\n\t" "v_cvt_pk_bf16_f32 v186, v176, v177\n\t" "v_cvt_pk_bf16_f32 v187, v178, v179\n\t" "global_store_dwordx4 v0, v[184:187], %[sy] offset:1024 sc1\n\t"
    "v_mul_f32 v189, v48, v48\n\t" "v_mul_f32 v191, v52, v52\n\t" "v_add_f32 v188, v49, v48\n\t" "v_fmac_f32 v189, v49, v49\n\t" "v_add_f32 v190, v53, v52\n\t" "v_fmac_f32 v191, v53, v53\n\t"
    "v_add_f32 v188, v50, v188\n\t" "v_fmac_f32 v189, v50, v50\n\t" "v_add_f32 v190, v54, v190\n\t" "v_fmac_f32 v191, v54, v54\n\t" "v_add_f32 v188, v51, v188\n\t" "v_fmac_f32 v189, v51, v51\n\t"
    "v_add_f32 v190, v55, v190\n\t" "v_fmac_f32 v191, v55, v55\n\t" "v_add_f32 v188, v56, v188\n\t" "v_fmac_f32 v189, v56, v56\n\t" "v_add_f32 v190, v60, v190\n\t" "v_fmac_f32 v191, v60, v60\n\t"
    "v_add_f32 v188, v57, v188\n\t" "v_fmac_f32 v189, v57, v57\n\t" "v_add_f32 v190, v61, v190\n\t" "v_fmac_f32 v191, v61, v61\n\t" "v_add_f32 v188, v58, v188\n\t" "v_fmac_f32 v189, v58, v58\n\t"
    "v_add_f32 v190, v62, v190\n\t" "v_fmac_f32 v191, v62, v62\n\t" "v_add_f32 v188, v59, v188\n\t" "v_fmac_f32 v189, v59, v59\n\t" "v_add_f32 v190, v63, v190\n\t" "v_fmac_f32 v191, v63, v63\n\t"
    "v_add_f32 v188, v64, v188\n\t" "v_fmac_f32 v189, v64, v64\n\t" "v_add_f32 v190, v68, v190\n\t" "v_fmac_f32 v191, v68, v68\n\t" "v_add_f32 v188, v65, v188\n\t" "v_fmac_f32 v189, v65, v65\n\t"
    "v_add_f32 v190, v69, v190\n\t" "v_fmac_f32 v191, v69, v69\n\t" "v_add_f32 v188, v66, v188\n\t" "v_fmac_f32 v189, v66, v66\n\t" "v_add_f32 v190, v70, v190\n\t" "v_fmac_f32 v191, v70, v70\n\t"
    "v_add_f32 v188, v67, v188\n\t" "v_fmac_f32 v189, v67, v67\n\t" "v_add_f32 v190, v71, v190\n\t" "v_fmac_f32 v191, v71, v71\n\t" "v_add_f32 v188, v72, v188\n\t" "v_fmac_f32 v189, v72, v72\n\t"
    "v_add_f32 v190, v76, v190\n\t" "v_fmac_f32 v191, v76, v76\n\t" "v_add_f32 v188, v73, v188\n\t" "v_fmac_f32 v189, v73, v73\n\t" "v_add_f32 v190, v77, v190\n\t" "v_fmac_f32 v191, v77, v77\n\t"
    "v_add_f32 v188, v74, v188\n\t" "v_fmac_f32 v189, v74, v74\n\t" "v_add_f32 v190, v78, v190\n\t" "v_fmac_f32 v191, v78, v78\n\t" "v_add_f32 v188, v75, v188\n\t" "v_fmac_f32 v189, v75, v75\n\t"
    "v_add_f32 v190, v79, v190\n\t" "v_fmac_f32 v191, v79, v79\n\t" "v_add_f32 v48, v48, v236\n\t" "v_add_f32 v49, v49, v236\n\t" "v_permlane16_swap_b32 v188, v190\n\t"
    "v_permlane16_swap_b32 v189, v191\n\t" "v_add_f32 v188, v188, v190\n\t" "v_add_f32 v189, v189, v191\n\t" "v_mov_b32 v190, v188\n\t" "v_mov_b32 v191, v189\n\t" "v_add_f32 v50, v50, v236\n\t"
    "v_add_f32 v51, v51, v236\n\t" "v_permlane32_swap_b32 v188, v190\n\t" "v_permlane32_swap_b32 v189, v191\n\t" "v_add_f32 v188, v188, v190\n\t" "v_add_f32 v189, v189, v191\n\t"
    "s_mov_b32 exec_hi, 0\n\t" "global_store_dwordx2 v34, v[188:189], %[ss] sc1\n\t" "s_mov_b64 exec, -1\n\t" "v_add_f32 v56, v56, v236\n\t" "v_add_f32 v57, v57, v236\n\t"
    "v_add_f32 v58, v58, v236\n\t" "v_add_f32 v59, v59, v236\n\t" "v_add_f32 v64, v64, v236\n\t" "v_add_f32 v65, v65, v236\n\t" "v_add_f32 v66, v66, v236\n\t" "v_add_f32 v67, v67, v236\n\t"
    "v_add_f32 v72, v72, v236\n\t" "v_add_f32 v73, v73, v236\n\t" "v_add_f32 v74, v74, v236\n\t" "v_add_f32 v75, v75, v236\n\t" "v_add_f32 v52, v52, v237\n\t" "v_add_f32 v53, v53, v237\n\t"
    "v_add_f32 v54, v54, v237\n\t" "v_add_f32 v55, v55, v237\n\t" "v_add_f32 v60, v60, v237\n\t" "v_add_f32 v61, v61, v237\n\t" "v_add_f32 v62, v62, v237\n\t" "v_add_f32 v63, v63, v237\n\t"
    "v_add_f32 v68, v68, v237\n\t" "v_add_f32 v69, v69, v237\n\t" "v_add_f32 v70, v70, v237\n\t" "v_add_f32 v71, v71, v237\n\t" "v_add_f32 v76, v76, v237\n\t" "v_add_f32 v77, v77, v237\n\t"
    "v_add_f32 v78, v78, v237\n\t" "v_add_f32 v79, v79, v237\n\t" "v_cvt_pk_bf16_f32 v80, v48, v49\n\t" "v_cvt_pk_bf16_f32 v81, v50, v51\n\t" "v_cvt_pk_bf16_f32 v82, v56, v57\n\t"
    "v_cvt_pk_bf16_f32 v83, v58, v59\n\t" "global_store_dwordx4 v0, v[80:83], %[sx] offset:0\n\t" "v_cvt_pk_bf16_f32 v84, v64, v65\n\t" "v_cvt_pk_bf16_f32 v85, v66, v67\n\t"
    "v_cvt_pk_bf16_f32 v86, v72, v73\n\t" "v_cvt_pk_bf16_f32 v87, v74, v75\n\t" "global_store_dwordx4 v0, v[84:87], %[sx] offset:1024\n\t" "v_cvt_pk_bf16_f32 v88, v52, v53\n\t"
    "v_cvt_pk_bf16_f32 v89, v54, v55\n\t" "v_cvt_pk_bf16_f32 v90, v60, v61\n\t" "v_cvt_pk_bf16_f32 v91, v62, v63\n\t" "global_store_dwordx4 v0, v[88:91], %[sx] offset:2048\n\t"
    "v_cvt_pk_bf16_f32 v92, v68, v69\n\t" "v_cvt_pk_bf16_f32 v93, v70, v71\n\t" "v_cvt_pk_bf16_f32 v94, v76, v77\n\t" "v_cvt_pk_bf16_f32 v95, v78, v79\n\t"
    "global_store_dwordx4 v0, v[92:95], %[sx] offset:3072\n\t" "v_mov_b64 v[192:193], %[tag]\n\t" "v_mov_b32 v194, 0\n\t" "s_waitcnt vmcnt(4)\n\t" "s_barrier\n\t" "s_cmp_eq_u32 %[wid], 0\n\t"
    "s_cselect_b64 exec, 1, 0\n\t" "global_store_dwordx2 v194, v[192:193], %[sf] sc1\n\t" "s_mov_b64 exec, -1\n\t" "ds_write_b32 v36, v194\n\t" "s_add_u32 m0, %[sl], 0\n\t" "s_nop 0\n\t"
    "global_load_lds_dwordx4 v1, s[78:79]\n\t" "s_add_u32 m0, %[sl], 1024\n\t" "s_nop 0\n\t" "global_load_lds_dwordx4 v2, s[78:79]\n\t" "s_add_u32 m0, %[sl], 2048\n\t" "s_nop 0\n\t"
    "global_load_lds_dwordx4 v3, s[78:79]\n\t" "s_add_u32 m0, %[sl], 3072\n\t" "s_nop 0\n\t" "s_mov_b64 exec, s[82:83]\n\t" "global_load_lds_dwordx4 v4, s[78:79]\n\t" "s_mov_b64 exec, -1\n\t"
    "s_add_u32 s78, s78, 229376\n\t" "s_addc_u32 s79, s79, 0\n\t" "s_branch .Lend_%=\n\t" ".Lslow_%=:\n\t" "s_sleep 2\n\t" "global_load_dwordx4 v[112:115], v0, %[sg0] sc1\n\t"
    "global_load_dwordx4 v[116:119], v0, %[sg1] sc1\n\t" "global_load_dwordx4 v[120:123], v0, %[sg2] sc1\n\t" "s_waitcnt vmcnt(0)\n\t" "v_xad_u32 v156, v112, v113, v164\n\t"
    "v_xad_u32 v157, v116, v117, v164\n\t" "v_xad_u32 v158, v120, v121, v164\n\t" "v_xad_u32 v156, v114, v113, v156\n\t" "v_xad_u32 v157, v118, v117, v157\n\t" "v_xad_u32 v158, v122, v121, v158\n\t"
    "v_xor_b32 v156, v156, v115\n\t" "v_bitop3_b32 v156, v156, v157, v119 bitop3:0xf6\n\t" "v_bitop3_b32 v156, v156, v158, v123 bitop3:0xf6\n\t" "v_cmp_ne_u32 vcc, 0, v156\n\t"
    "v_perm_b32 v115, v112, v113, s49\n\t" "v_perm_b32 v119, v116, v117, s49\n\t" "v_perm_b32 v123, v120, v121, s49\n\t" "s_nop 1\n\t" "s_cbranch_vccz .Lgo_%=\n\t" "s_sub_u32 s44, s44, 1\n\t"
    "s_cbranch_scc0 .Lslow_%=\n\t" "s_branch .Lgo_%=\n\t" ".Lend_%=:\n\t"
    : "+{v[80:83]}"(xr[0]), "+{v[84:87]}"(xr[1]), "+{v[88:91]}"(xr[2]), "+{v[92:95]}"(xr[3]), "+{v[96:99]}"(xr[4]), "+{v[100:103]}"(xr[5]), "+{v[104:107]}"(xr[6]), "+{v[108:111]}"(xr[7])
    : "{v0}"(va), "{v15}"(tw), "{v32}"(tr), "{v33}"(ve), "{v34}"(vs), "{v36}"(zb), "{v236}"(mu[0]), "{v237}"(mu[1]), "{v1}"(vb[0]), "{v2}"(vb[1]), "{v3}"(vb[2]), "{v4}"(vb[3]), "{v16}"(vr[0]), "{v17}"(vr[1]), "{v18}"(vr[2]), "{v19}"(vr[3]), "{v20}"(vr[4]), "{v21}"(vr[5]), "{v22}"(vr[6]), "{v23}"(vr[7]), "{v24}"(vr[8]), "{v25}"(vr[9]), "{v26}"(vr[10]), "{v27}"(vr[11]), "{v28}"(vr[12]), "{v29}"(vr[13]), "{v30}"(vr[14]), "{v31}"(vr[15]), [sa] "s"(sa + 12288), [tag] "s"(tag), [wid] "s"(wid), [role] "s"(role), [sf] "s"(sf), [sg0] "s"(sg0), [sg1] "s"(sg1), [sg2] "s"(sg2), [se] "s"(se), [sy] "s"(sy), [ss] "s"(ss), [sx] "s"(sx), [sb] "s"(sb), [sl] "s"(sl)
    : "v48", "v49", "v50", "v51", "v52", "v53", "v54", "v55", "v56", "v57", "v58", "v59", "v60", "v61", "v62", "v63", "v64", "v65", "v66", "v67", "v68", "v69", "v70", "v71", "v72", "v73", "v74", "v75", "v76", "v77", "v78", "v79", "v112", "v113", "v114", "v115", "v116", "v117", "v118", "v119", "v120", "v121", "v122", "v123", "v124", "v125", "v126", "v127", "v128", "v129", "v130", "v131", "v132", "v133", "v134", "v135", "v136", "v137", "v138", "v139", "v140", "v141", "v142", "v143", "v144", "v145", "v146", "v147", "v148", "v149", "v150", "v151", "v152", "v153", "v154", "v155", "v156", "v157", "v158", "v159", "v160", "v161", "v162", "v163", "v164", "v165", "v166", "v167", "v168", "v169", "v170", "v171", "v172", "v173", "v174", "v175", "v176", "v177", "v178", "v179", "v180", "v181", "v182", "v183", "v184", "v185", "v186", "v187", "v188", "v189", "v190", "v191", "v192", "v193", "v194", "v195", "a0", "a1", "a2", "a3", "a4", "a5", "a6", "a7", "a8", "a9", "a10", "a11", "a12", "a13", "a14", "a15", "a16", "a17", "a18", "a19", "a20", "a21", "a22", "a23", "a24", "a25", "a26", "a27", "a28", "a29", "a30", "a31", "a32", "a33", "a34", "a35", "a36", "a37", "a38", "a39", "a40", "a41", "a42", "a43", "a44", "a45", "a46", "a47", "a48", "a49", "a50", "a51", "a52", "a53", "a54", "a55", "a56", "a57", "a58", "a59", "a60", "a61", "a62", "a63", "a64", "a65", "a66", "a67", "a68", "a69", "a70", "a71", "a72", "a73", "a74", "a75", "a76", "a77", "a78", "a79", "a80", "a81", "a82", "a83", "a84", "a85", "a86", "a87", "a88", "a89", "a90", "a91", "a92", "a93", "a94", "a95", "a96", "a97", "a98", "a99", "a100", "a101", "a102", "a103", "a104", "a105", "a106", "a107", "a108", "a109", "a110", "a111", "a112", "a113", "a114", "a115", "a116", "a117", "a118", "a119", "a120", "a121", "a122", "a123", "a124", "a125", "a126", "a127", "a128", "a129", "a130", "a131", "a132", "a133", "a134", "a135", "a136", "a137", "a138", "a139", "a140", "a141", "a142", "a143", "a144", "a145", "a146", "a147", "a148", "a149", "a150", "a151", "a152", "a153", "a154", "a155", "a156", "a157", "a158", "a159", "s40", "s41", "s42", "s43", "s44", "s45", "s46", "s47", "s48", "s49", "s50", "s78", "s79", "s82", "s83", "vcc", "scc", "memory");
  }
  asm volatile("" : "+s"(bx), "+s"(wid), "+s"(ws)); asm volatile("" : "+s"(tag)); asm volatile("" : "+v"(tid));
  {
  const int xcd = bx & 7, loc = bx >> 3;
  const int lane = tid & 63, rot = xcd & 1, rb = (xcd & 1) * 4 + (loc & 3), cb = (xcd >> 1) * 8 + (loc >> 2);
  const u16* ef = (const u16*)(ws + W_EF) + rb * 2048; const f32x2* stats = (const f32x2*)(ws + W_STATS); u16* hh = (u16*)(ws + W_HH); float* uvp = (float*)(ws + W_UVP);
  GW(); const unsigned va = lane * 16; const char* sa = ws + W_Y + (size_t)(rb * 4 + wid) * 131072 + rot * 16 * 4096; const char* sq = ws + W_TAGS + YF * 8; const char* scnt = ws + (unsigned)(W_TAGS + (HF + (rb * 32 + cb) * 8) * 8);
  const unsigned vf = (lane < 32 ? rb * 32 + lane : ((xcd >> 1) * 4 + (lane & 3)) * 16 + (lane & 4) * 2 + (loc >> 2)) * 64;
  auto grp = [&](int i) { const int q = i + (wid < 3 ? wid : 0); return i == 3 ? 3 : q < 3 ? q : q - 3; };
  unsigned vr[8], gt[4], go[4];
  const unsigned ct = ldsb + (lane < 16 ? TAB + lane * 16 : ZB);
#pragma unroll
  for (int m = 0; m < 4; ++m) {
    vr[2 * m] = ldsb + GB + (lane >> 2) * 224 + (lane & 3) * 8 + (m < 3 ? grp(m) * 64 + (cb & 1) * 32 : (cb & 1) ? 0 : 192); vr[2 * m + 1] = vr[2 * m] + 4 * 14336;
    gt[m] = grp(m) * 1024;
    go[m] = m < 3 ? ((cb & 1) * 4 + grp(m)) * 2048 : 3 * 2048 + (cb & 1) * 1024;
  }
  const unsigned vs = (unsigned)((lane >> 4) * 4 * S_ + (lane & 15)) * 8, ve = (lane & 1) * 64 + (lane >> 4) * 16;
  const char* se = (const char*)ef + ((rb + 1) & 7) * 512;
  const char* ss = (const char*)(stats + rb * 256 + wid * 64);
  {
    const unsigned vp = ((lane & 1) * 4 + (lane >> 4)) * 144 + rb * 16, cr0 = ldsb + RAW + (tid >> 2) * 144 + (tid & 3) * 4, cr1 = cr0 + (wid < 3 ? 64 * 144 : 0), srl = ldsb + RAW + wid * 4096;
    const unsigned cw = ldsb + TAB + (tid >> 5) * 256 + (tid & 15) * 16 + ((tid >> 4) & 1) * 8;
    const size_t su = (size_t)uvp + cb * 16384, sh = (size_t)hh + ((size_t)(rb * 8 + wid * 2) * 112 + (cb >> 1) * 7) * 2048;
    const unsigned shl = (unsigned)sh, shh = (unsigned)(sh >> 32);
    const size_t su0 = su + grp(0) * 4608, su6 = su + 6 * 2304, sr = su + wid * 4096;
    const unsigned long long m01 = wid < 3 ? 0x0003000300030003ull : 0ull, m6 = wid < 3 ? 0ull : 0x0003000300030003ull;
    const char* tp0 = (const char*)pfw + min((bx * 4 + wid) * 7168, 1024 * 7168 - 57 * 128);
    const int wa = rot ? -31 * 4096 : 4096, wb = rot ? -31 * 32 * 7168 : 32 * 7168, e1 = (rb & 1) ? -1 : (rb >> 1) & 1, e0 = (rb & 1) ? (rb >> 1) & 1 : -1;
    const unsigned vbd = f_vb(wid, lane), sld = ldsb + wid * 1024;
    const char* sbd = (const char*)(pfw + (size_t)(xcd & 1) * 1792 * D_ + (loc >> 2) * 128);
    asm volatile("s_mov_b32 s80, 0\n\t" "s_cmp_eq_u32 s80, 1\n\t" "s_cselect_b32 s86, %[wa], 4096\n\t" "s_cselect_b32 s88, %[wb], 229376\n\t" "s_ashr_i32 s87, s86, 31\n\t" "s_ashr_i32 s89, s88, 31\n\t"
    "s_mov_b64 s[76:77], %[sa]\n\t" "s_mov_b64 s[78:79], %[sb]\n\t" "s_mov_b32 s82, -1\n\t" "s_mov_b32 s83, 0\n\t" "s_mov_b32 s94, 0x10000\n\t" "s_mov_b64 s[84:85], %[tp0]\n\t"
    "v_accvgpr_write_b32 a200, 0\n\t" "v_accvgpr_write_b32 a201, 0\n\t" "v_accvgpr_write_b32 a202, 0\n\t" "v_accvgpr_write_b32 a203, 0\n\t" "v_accvgpr_write_b32 a204, 0\n\t"
    "v_accvgpr_write_b32 a205, 0\n\t" "v_accvgpr_write_b32 a206, 0\n\t" "v_accvgpr_write_b32 a207, 0\n\t" "v_accvgpr_write_b32 a208, 0\n\t" "v_accvgpr_write_b32 a209, 0\n\t"
    "v_accvgpr_write_b32 a210, 0\n\t" "v_accvgpr_write_b32 a211, 0\n\t" "v_accvgpr_write_b32 a212, 0\n\t" "v_accvgpr_write_b32 a213, 0\n\t" "v_accvgpr_write_b32 a214, 0\n\t"
    "v_accvgpr_write_b32 a215, 0\n\t" "s_mov_b32 s81, 0x400000\n\t" ".Lpy_%=:\n\t" "global_load_dwordx2 v[24:25], %[vf], %[sq] sc1\n\t" "s_waitcnt vmcnt(0)\n\t" "v_cmp_ne_u64 vcc, %[tag], v[24:25]\n\t"
    "s_nop 4\n\t" "s_cbranch_vccz .Lry_%=\n\t" "s_sleep 1\n\t" "s_sub_u32 s81, s81, 1\n\t" "s_cbranch_scc0 .Lpy_%=\n\t" ".Lry_%=:\n\t" "global_load_dwordx4 a[0:3], %[va], s[76:77] offset:0 sc1\n\t"
    "global_load_dwordx4 a[4:7], %[va], s[76:77] offset:1024 sc1\n\t" "global_load_dwordx4 a[8:11], %[va], s[76:77] offset:2048 sc1\n\t" "global_load_dwordx4 a[12:15], %[va], s[76:77] offset:3072 sc1\n\t"
    "s_add_u32 s76, s76, 4096\n\t" "s_addc_u32 s77, s77, 0\n\t" "ds_read_b64_tr_b16 a[128:129], %[vr1] offset:28672\n\t" "ds_read_b64_tr_b16 a[130:131], %[vr1] offset:32256\n\t"
    "ds_read_b64_tr_b16 a[132:133], %[vr1] offset:28704\n\t" "ds_read_b64_tr_b16 a[134:135], %[vr1] offset:32288\n\t" "ds_read_b64_tr_b16 a[136:137], %[vr3] offset:28672\n\t"
    "ds_read_b64_tr_b16 a[138:139], %[vr3] offset:32256\n\t" "ds_read_b64_tr_b16 a[140:141], %[vr3] offset:28704\n\t" "ds_read_b64_tr_b16 a[142:143], %[vr3] offset:32288\n\t"
    "ds_read_b64_tr_b16 a[144:145], %[vr5] offset:28672\n\t" "ds_read_b64_tr_b16 a[146:147], %[vr5] offset:32256\n\t" "ds_read_b64_tr_b16 a[148:149], %[vr5] offset:28704\n\t"
    "ds_read_b64_tr_b16 a[150:151], %[vr5] offset:32288\n\t" "ds_read_b64_tr_b16 a[152:153], %[vr7] offset:28672\n\t" "ds_read_b64_tr_b16 a[154:155], %[vr7] offset:32256\n\t"
    "global_load_dwordx4 a[16:19], %[va], s[76:77] offset:0 sc1\n\t" "global_load_dwordx4 a[20:23], %[va], s[76:77] offset:1024 sc1\n\t" "global_load_dwordx4 a[24:27], %[va], s[76:77] offset:2048 sc1\n\t"
    "global_load_dwordx4 a[28:31], %[va], s[76:77] offset:3072 sc1\n\t" "s_add_u32 s76, s76, 4096\n\t" "s_addc_u32 s77, s77, 0\n\t" "global_load_dwordx4 a[32:35], %[va], s[76:77] offset:0 sc1\n\t"
    "global_load_dwordx4 a[36:39], %[va], s[76:77] offset:1024 sc1\n\t" "global_load_dwordx4 a[40:43], %[va], s[76:77] offset:2048 sc1\n\t"
    "global_load_dwordx4 a[44:47], %[va], s[76:77] offset:3072 sc1\n\t" "s_add_u32 s76, s76, 4096\n\t" "s_addc_u32 s77, s77, 0\n\t" "s_waitcnt vmcnt(8)\n\t" "s_waitcnt lgkmcnt(12)\n\t"
    "v_mfma_f32_16x16x32_bf16 v[32:35], a[128:131], a[0:3], 0\n\t" "ds_read_b64_tr_b16 a[156:157], %[vr1] offset:35840\n\t" "s_mov_b64 s[90:91], %[ss]\n\t"
    "v_mfma_f32_16x16x32_bf16 v[36:39], a[128:131], a[4:7], 0\n\t" "ds_read_b64_tr_b16 a[158:159], %[vr1] offset:39424\n\t" "v_accvgpr_read_b32 v30, %[vs]\n\t"
    "v_mfma_f32_16x16x32_bf16 v[40:43], a[128:131], a[8:11], 0\n\t" "global_load_dwordx4 a[48:51], %[va], s[76:77] offset:0 sc1\n\t" "v_accvgpr_read_b32 v31, %[ve]\n\t"
    "v_mfma_f32_16x16x32_bf16 v[44:47], a[128:131], a[12:15], 0\n\t" "s_waitcnt lgkmcnt(12)\n\t" "v_mfma_f32_16x16x32_bf16 v[48:51], a[132:135], a[0:3], 0\n\t"
    "ds_read_b64_tr_b16 a[128:129], %[vr1] offset:35872\n\t" "global_load_dwordx4 a[184:187], v31, %[se] offset:0 sc1\n\t" "v_mfma_f32_16x16x32_bf16 v[52:55], a[132:135], a[4:7], 0\n\t"
    "ds_read_b64_tr_b16 a[130:131], %[vr1] offset:39456\n\t" "v_mfma_f32_16x16x32_bf16 v[56:59], a[132:135], a[8:11], 0\n\t" "global_load_dwordx4 a[188:191], v31, %[se] offset:128 sc1\n\t"
    "v_mfma_f32_16x16x32_bf16 v[60:63], a[132:135], a[12:15], 0\n\t" "s_waitcnt lgkmcnt(12)\n\t" "s_add_u32 m0, %[sl], 14336\n\t" "v_mfma_f32_16x16x32_bf16 v[64:67], a[136:139], a[0:3], 0\n\t"
    "global_load_lds_dwordx4 %[vb0], s[78:79]\n\t" "ds_read_b64_tr_b16 a[132:133], %[vr3] offset:35840\n\t" "global_load_dwordx4 a[192:195], v31, %[se] offset:256 sc1\n\t"
    "v_mfma_f32_16x16x32_bf16 v[68:71], a[136:139], a[4:7], 0\n\t" "ds_read_b64_tr_b16 a[134:135], %[vr3] offset:39424\n\t" "v_mfma_f32_16x16x32_bf16 v[72:75], a[136:139], a[8:11], 0\n\t"
    "global_load_dwordx4 a[196:199], v31, %[se] offset:384 sc1\n\t" "v_mfma_f32_16x16x32_bf16 v[76:79], a[136:139], a[12:15], 0\n\t" "s_waitcnt lgkmcnt(12)\n\t"
    "v_mfma_f32_16x16x32_bf16 v[80:83], a[140:143], a[0:3], 0\n\t" "ds_read_b64_tr_b16 a[136:137], %[vr3] offset:35872\n\t" "global_load_dwordx2 a[216:217], v30, s[90:91] offset:0 sc1\n\t"
    "v_mfma_f32_16x16x32_bf16 v[84:87], a[140:143], a[4:7], 0\n\t" "ds_read_b64_tr_b16 a[138:139], %[vr3] offset:39456\n\t" "v_mfma_f32_16x16x32_bf16 v[88:91], a[140:143], a[8:11], 0\n\t"
    "global_load_dwordx4 a[52:55], %[va], s[76:77] offset:1024 sc1\n\t" "v_mfma_f32_16x16x32_bf16 v[92:95], a[140:143], a[12:15], 0\n\t" "s_waitcnt lgkmcnt(12)\n\t"
    "v_mfma_f32_16x16x32_bf16 v[96:99], a[144:147], a[0:3], 0\n\t" "ds_read_b64_tr_b16 a[140:141], %[vr5] offset:35840\n\t" "v_mfma_f32_16x16x32_bf16 v[100:103], a[144:147], a[4:7], 0\n\t"
    "ds_read_b64_tr_b16 a[142:143], %[vr5] offset:39424\n\t" "v_mfma_f32_16x16x32_bf16 v[104:107], a[144:147], a[8:11], 0\n\t" "v_mfma_f32_16x16x32_bf16 v[108:111], a[144:147], a[12:15], 0\n\t"
    "s_waitcnt lgkmcnt(12)\n\t" "s_add_u32 m0, %[sl], 15360\n\t" "v_mfma_f32_16x16x32_bf16 v[112:115], a[148:151], a[0:3], 0\n\t" "global_load_lds_dwordx4 %[vb1], s[78:79]\n\t"
    "ds_read_b64_tr_b16 a[144:145], %[vr5] offset:35872\n\t" "v_mfma_f32_16x16x32_bf16 v[116:119], a[148:151], a[4:7], 0\n\t" "ds_read_b64_tr_b16 a[146:147], %[vr5] offset:39456\n\t"
    "v_mfma_f32_16x16x32_bf16 v[120:123], a[148:151], a[8:11], 0\n\t" "v_mfma_f32_16x16x32_bf16 v[124:127], a[148:151], a[12:15], 0\n\t" "s_waitcnt lgkmcnt(12)\n\t"
    "v_mfma_f32_16x16x32_bf16 v[128:131], a[152:155], a[0:3], 0\n\t" "ds_read_b64_tr_b16 a[148:149], %[vr7] offset:35840\n\t" "global_load_dwordx2 a[218:219], v30, s[90:91] offset:128 sc1\n\t"
    "v_mfma_f32_16x16x32_bf16 v[132:135], a[152:155], a[4:7], 0\n\t" "ds_read_b64_tr_b16 a[150:151], %[vr7] offset:39424\n\t" "v_mfma_f32_16x16x32_bf16 v[136:139], a[152:155], a[8:11], 0\n\t"
    "global_load_dwordx4 a[56:59], %[va], s[76:77] offset:2048 sc1\n\t" "v_mfma_f32_16x16x32_bf16 v[140:143], a[152:155], a[12:15], 0\n\t" "s_waitcnt lgkmcnt(12)\n\t"
    "v_mfma_f32_16x16x32_bf16 v[144:147], a[156:159], a[0:3], 0\n\t" "s_waitcnt vmcnt(15)\n\t" "s_barrier\n\t" "ds_read_b64_tr_b16 a[152:153], %[vr1] offset:43008\n\t"
    "v_mfma_f32_16x16x32_bf16 v[148:151], a[156:159], a[4:7], 0\n\t" "ds_read_b64_tr_b16 a[154:155], %[vr1] offset:46592\n\t" "v_mfma_f32_16x16x32_bf16 v[152:155], a[156:159], a[8:11], 0\n\t"
    "v_mfma_f32_16x16x32_bf16 v[156:159], a[156:159], a[12:15], 0\n\t" "s_waitcnt lgkmcnt(12)\n\t" "s_add_u32 m0, %[sl], 16384\n\t" "v_mfma_f32_16x16x32_bf16 v[160:163], a[128:131], a[0:3], 0\n\t"
    "global_load_lds_dwordx4 %[vb2], s[78:79]\n\t" "ds_read_b64_tr_b16 a[156:157], %[vr1] offset:43040\n\t" "v_mfma_f32_16x16x32_bf16 v[164:167], a[128:131], a[4:7], 0\n\t"
    "ds_read_b64_tr_b16 a[158:159], %[vr1] offset:46624\n\t" "v_mfma_f32_16x16x32_bf16 v[168:171], a[128:131], a[8:11], 0\n\t" "v_mfma_f32_16x16x32_bf16 v[172:175], a[128:131], a[12:15], 0\n\t"
    "s_waitcnt lgkmcnt(12)\n\t" "v_mfma_f32_16x16x32_bf16 v[176:179], a[132:135], a[0:3], 0\n\t" "ds_read_b64_tr_b16 a[128:129], %[vr3] offset:43008\n\t"
    "global_load_dwordx2 a[220:221], v30, s[90:91] offset:256 sc1\n\t" "v_mfma_f32_16x16x32_bf16 v[180:183], a[132:135], a[4:7], 0\n\t" "ds_read_b64_tr_b16 a[130:131], %[vr3] offset:46592\n\t"
    "v_mfma_f32_16x16x32_bf16 v[184:187], a[132:135], a[8:11], 0\n\t" "global_load_dwordx4 a[60:63], %[va], s[76:77] offset:3072 sc1\n\t" "s_add_u32 s76, s76, 4096\n\t" "s_addc_u32 s77, s77, 0\n\t"
    "v_mfma_f32_16x16x32_bf16 v[188:191], a[132:135], a[12:15], 0\n\t" "s_waitcnt lgkmcnt(12)\n\t" "v_mfma_f32_16x16x32_bf16 v[192:195], a[136:139], a[0:3], 0\n\t"
    "ds_read_b64_tr_b16 a[132:133], %[vr3] offset:43040\n\t" "v_mfma_f32_16x16x32_bf16 v[196:199], a[136:139], a[4:7], 0\n\t" "ds_read_b64_tr_b16 a[134:135], %[vr3] offset:46624\n\t"
    "v_mfma_f32_16x16x32_bf16 v[200:203], a[136:139], a[8:11], 0\n\t" "v_mfma_f32_16x16x32_bf16 v[204:207], a[136:139], a[12:15], 0\n\t" "s_waitcnt lgkmcnt(12)\n\t" "s_add_u32 m0, %[sl], 17408\n\t"
    "v_mfma_f32_16x16x32_bf16 v[208:211], a[140:143], a[0:3], 0\n\t" "s_mov_b64 exec, s[82:83]\n\t" "global_load_lds_dwordx4 %[vb3], s[78:79]\n\t" "s_mov_b64 exec, -1\n\t" "s_add_u32 s78, s78, 229376\n\t"
    "s_addc_u32 s79, s79, 0\n\t" "ds_read_b64_tr_b16 a[136:137], %[vr5] offset:43008\n\t" "v_mfma_f32_16x16x32_bf16 v[212:215], a[140:143], a[4:7], 0\n\t"
    "ds_read_b64_tr_b16 a[138:139], %[vr5] offset:46592\n\t" "v_mfma_f32_16x16x32_bf16 v[216:219], a[140:143], a[8:11], 0\n\t" "v_mfma_f32_16x16x32_bf16 v[220:223], a[140:143], a[12:15], 0\n\t"
    "s_waitcnt lgkmcnt(12)\n\t" "v_mfma_f32_16x16x32_bf16 v[224:227], a[144:147], a[0:3], 0\n\t" "ds_read_b64_tr_b16 a[140:141], %[vr5] offset:43040\n\t"
    "global_load_dwordx2 a[222:223], v30, s[90:91] offset:384 sc1\n\t" "v_mfma_f32_16x16x32_bf16 v[228:231], a[144:147], a[4:7], 0\n\t" "ds_read_b64_tr_b16 a[142:143], %[vr5] offset:46624\n\t"
    "v_mfma_f32_16x16x32_bf16 v[232:235], a[144:147], a[8:11], 0\n\t" "v_mfma_f32_16x16x32_bf16 v[236:239], a[144:147], a[12:15], 0\n\t" "s_waitcnt lgkmcnt(12)\n\t"
    "v_mfma_f32_16x16x32_bf16 v[240:243], a[148:151], a[0:3], 0\n\t" "ds_read_b64_tr_b16 a[144:145], %[vr7] offset:43008\n\t" "v_mfma_f32_16x16x32_bf16 v[244:247], a[148:151], a[4:7], 0\n\t"
    "ds_read_b64_tr_b16 a[146:147], %[vr7] offset:46592\n\t" "v_mfma_f32_16x16x32_bf16 v[248:251], a[148:151], a[8:11], 0\n\t" "v_mfma_f32_16x16x32_bf16 v[252:255], a[148:151], a[12:15], 0\n\t"
    "s_waitcnt lgkmcnt(12)\n\t" "v_mfma_f32_16x16x32_bf16 v[32:35], a[152:155], a[16:19], v[32:35]\n\t" "ds_read_b64_tr_b16 a[148:149], %[vr1] offset:50176\n\t"
    "v_mfma_f32_16x16x32_bf16 v[36:39], a[152:155], a[20:23], v[36:39]\n\t" "ds_read_b64_tr_b16 a[150:151], %[vr1] offset:53760\n\t" "v_mfma_f32_16x16x32_bf16 v[40:43], a[152:155], a[24:27], v[40:43]\n\t"
    "global_load_dwordx4 a[64:67], %[va], s[76:77] offset:0 sc1\n\t" "v_mfma_f32_16x16x32_bf16 v[44:47], a[152:155], a[28:31], v[44:47]\n\t" "s_waitcnt lgkmcnt(12)\n\t"
    "v_mfma_f32_16x16x32_bf16 v[48:51], a[156:159], a[16:19], v[48:51]\n\t" "ds_read_b64_tr_b16 a[152:153], %[vr1] offset:50208\n\t" "s_add_u32 s90, s90, 16384\n\t" "s_addc_u32 s91, s91, 0\n\t"
    "v_mfma_f32_16x16x32_bf16 v[52:55], a[156:159], a[20:23], v[52:55]\n\t" "ds_read_b64_tr_b16 a[154:155], %[vr1] offset:53792\n\t" "global_load_dwordx2 a[224:225], v30, s[90:91] offset:0 sc1\n\t"
    "v_mfma_f32_16x16x32_bf16 v[56:59], a[156:159], a[24:27], v[56:59]\n\t" "v_mfma_f32_16x16x32_bf16 v[60:63], a[156:159], a[28:31], v[60:63]\n\t" "s_waitcnt lgkmcnt(12)\n\t"
    "s_add_u32 m0, %[sl], 28672\n\t" "v_mfma_f32_16x16x32_bf16 v[64:67], a[128:131], a[16:19], v[64:67]\n\t" "global_load_lds_dwordx4 %[vb0], s[78:79]\n\t"
    "ds_read_b64_tr_b16 a[156:157], %[vr3] offset:50176\n\t" "v_mfma_f32_16x16x32_bf16 v[68:71], a[128:131], a[20:23], v[68:71]\n\t" "ds_read_b64_tr_b16 a[158:159], %[vr3] offset:53760\n\t"
    "v_mfma_f32_16x16x32_bf16 v[72:75], a[128:131], a[24:27], v[72:75]\n\t" "v_mfma_f32_16x16x32_bf16 v[76:79], a[128:131], a[28:31], v[76:79]\n\t" "s_waitcnt lgkmcnt(12)\n\t"
    "v_mfma_f32_16x16x32_bf16 v[80:83], a[132:135], a[16:19], v[80:83]\n\t" "ds_read_b64_tr_b16 a[128:129], %[vr3] offset:50208\n\t" "v_mfma_f32_16x16x32_bf16 v[84:87], a[132:135], a[20:23], v[84:87]\n\t"
    "ds_read_b64_tr_b16 a[130:131], %[vr3] offset:53792\n\t" "v_mfma_f32_16x16x32_bf16 v[88:91], a[132:135], a[24:27], v[88:91]\n\t" "global_load_dwordx4 a[68:71], %[va], s[76:77] offset:1024 sc1\n\t"
    "v_mfma_f32_16x16x32_bf16 v[92:95], a[132:135], a[28:31], v[92:95]\n\t" "s_waitcnt lgkmcnt(12)\n\t" "v_mfma_f32_16x16x32_bf16 v[96:99], a[136:139], a[16:19], v[96:99]\n\t"
    "ds_read_b64_tr_b16 a[132:133], %[vr5] offset:50176\n\t" "v_mfma_f32_16x16x32_bf16 v[100:103], a[136:139], a[20:23], v[100:103]\n\t" "ds_read_b64_tr_b16 a[134:135], %[vr5] offset:53760\n\t"
    "global_load_dwordx2 a[226:227], v30, s[90:91] offset:128 sc1\n\t" "v_mfma_f32_16x16x32_bf16 v[104:107], a[136:139], a[24:27], v[104:107]\n\t"
    "v_mfma_f32_16x16x32_bf16 v[108:111], a[136:139], a[28:31], v[108:111]\n\t" "s_waitcnt lgkmcnt(12)\n\t" "s_add_u32 m0, %[sl], 29696\n\t"
    "v_mfma_f32_16x16x32_bf16 v[112:115], a[140:143], a[16:19], v[112:115]\n\t" "global_load_lds_dwordx4 %[vb1], s[78:79]\n\t" "ds_read_b64_tr_b16 a[136:137], %[vr5] offset:50208\n\t"
    "v_mfma_f32_16x16x32_bf16 v[116:119], a[140:143], a[20:23], v[116:119]\n\t" "ds_read_b64_tr_b16 a[138:139], %[vr5] offset:53792\n\t"
    "v_mfma_f32_16x16x32_bf16 v[120:123], a[140:143], a[24:27], v[120:123]\n\t" "v_mfma_f32_16x16x32_bf16 v[124:127], a[140:143], a[28:31], v[124:127]\n\t" "s_waitcnt lgkmcnt(12)\n\t"
    "v_mfma_f32_16x16x32_bf16 v[128:131], a[144:147], a[16:19], v[128:131]\n\t" "ds_read_b64_tr_b16 a[140:141], %[vr7] offset:50176\n\t"
    "v_mfma_f32_16x16x32_bf16 v[132:135], a[144:147], a[20:23], v[132:135]\n\t" "ds_read_b64_tr_b16 a[142:143], %[vr7] offset:53760\n\t"
    "v_mfma_f32_16x16x32_bf16 v[136:139], a[144:147], a[24:27], v[136:139]\n\t" "global_load_dwordx4 a[72:75], %[va], s[76:77] offset:2048 sc1\n\t"
    "v_mfma_f32_16x16x32_bf16 v[140:143], a[144:147], a[28:31], v[140:143]\n\t" "s_waitcnt lgkmcnt(12)\n\t" "v_mfma_f32_16x16x32_bf16 v[144:147], a[148:151], a[16:19], v[144:147]\n\t"
    "s_waitcnt vmcnt(23)\n\t" "s_barrier\n\t" "ds_read_b64_tr_b16 a[144:145], %[vr0] offset:0\n\t" "v_mfma_f32_16x16x32_bf16 v[148:151], a[148:151], a[20:23], v[148:151]\n\t"
    "ds_read_b64_tr_b16 a[146:147], %[vr0] offset:3584\n\t" "global_load_dwordx2 a[228:229], v30, s[90:91] offset:256 sc1\n\t" "v_mfma_f32_16x16x32_bf16 v[152:155], a[148:151], a[24:27], v[152:155]\n\t"
    "v_mfma_f32_16x16x32_bf16 v[156:159], a[148:151], a[28:31], v[156:159]\n\t" "s_waitcnt lgkmcnt(12)\n\t" "s_add_u32 m0, %[sl], 30720\n\t"
    "v_mfma_f32_16x16x32_bf16 v[160:163], a[152:155], a[16:19], v[160:163]\n\t" "global_load_lds_dwordx4 %[vb2], s[78:79]\n\t" "ds_read_b64_tr_b16 a[148:149], %[vr0] offset:32\n\t"
    "v_mfma_f32_16x16x32_bf16 v[164:167], a[152:155], a[20:23], v[164:167]\n\t" "ds_read_b64_tr_b16 a[150:151], %[vr0] offset:3616\n\t"
    "v_mfma_f32_16x16x32_bf16 v[168:171], a[152:155], a[24:27], v[168:171]\n\t" "v_mfma_f32_16x16x32_bf16 v[172:175], a[152:155], a[28:31], v[172:175]\n\t" "s_waitcnt lgkmcnt(12)\n\t"
    "v_mfma_f32_16x16x32_bf16 v[176:179], a[156:159], a[16:19], v[176:179]\n\t" "ds_read_b64_tr_b16 a[152:153], %[vr2] offset:0\n\t"
    "v_mfma_f32_16x16x32_bf16 v[180:183], a[156:159], a[20:23], v[180:183]\n\t" "ds_read_b64_tr_b16 a[154:155], %[vr2] offset:3584\n\t"
    "v_mfma_f32_16x16x32_bf16 v[184:187], a[156:159], a[24:27], v[184:187]\n\t" "global_load_dwordx4 a[76:79], %[va], s[76:77] offset:3072 sc1\n\t" "s_add_u32 s76, s76, 4096\n\t"
    "s_addc_u32 s77, s77, 0\n\t" "v_mfma_f32_16x16x32_bf16 v[188:191], a[156:159], a[28:31], v[188:191]\n\t" "s_waitcnt lgkmcnt(12)\n\t"
    "v_mfma_f32_16x16x32_bf16 v[192:195], a[128:131], a[16:19], v[192:195]\n\t" "ds_read_b64_tr_b16 a[156:157], %[vr2] offset:32\n\t"
    "v_mfma_f32_16x16x32_bf16 v[196:199], a[128:131], a[20:23], v[196:199]\n\t" "ds_read_b64_tr_b16 a[158:159], %[vr2] offset:3616\n\t" "global_load_dwordx2 a[230:231], v30, s[90:91] offset:384 sc1\n\t"
    "v_mfma_f32_16x16x32_bf16 v[200:203], a[128:131], a[24:27], v[200:203]\n\t" "v_mfma_f32_16x16x32_bf16 v[204:207], a[128:131], a[28:31], v[204:207]\n\t" "s_waitcnt lgkmcnt(12)\n\t"
    "s_add_u32 m0, %[sl], 31744\n\t" "v_mfma_f32_16x16x32_bf16 v[208:211], a[132:135], a[16:19], v[208:211]\n\t" "s_mov_b64 exec, s[82:83]\n\t" "global_load_lds_dwordx4 %[vb3], s[78:79]\n\t"
    "s_mov_b64 exec, -1\n\t" "s_add_u32 s78, s78, 229376\n\t" "s_addc_u32 s79, s79, 0\n\t" "ds_read_b64_tr_b16 a[128:129], %[vr4] offset:0\n\t"
    "v_mfma_f32_16x16x32_bf16 v[212:215], a[132:135], a[20:23], v[212:215]\n\t" "ds_read_b64_tr_b16 a[130:131], %[vr4] offset:3584\n\t"
    "v_mfma_f32_16x16x32_bf16 v[216:219], a[132:135], a[24:27], v[216:219]\n\t" "v_mfma_f32_16x16x32_bf16 v[220:223], a[132:135], a[28:31], v[220:223]\n\t" "s_waitcnt lgkmcnt(12)\n\t"
    "v_mfma_f32_16x16x32_bf16 v[224:227], a[136:139], a[16:19], v[224:227]\n\t" "ds_read_b64_tr_b16 a[132:133], %[vr4] offset:32\n\t"
    "v_mfma_f32_16x16x32_bf16 v[228:231], a[136:139], a[20:23], v[228:231]\n\t" "ds_read_b64_tr_b16 a[134:135], %[vr4] offset:3616\n\t"
    "v_mfma_f32_16x16x32_bf16 v[232:235], a[136:139], a[24:27], v[232:235]\n\t" "v_mfma_f32_16x16x32_bf16 v[236:239], a[136:139], a[28:31], v[236:239]\n\t" "s_waitcnt lgkmcnt(12)\n\t"
    "v_mfma_f32_16x16x32_bf16 v[240:243], a[140:143], a[16:19], v[240:243]\n\t" "ds_read_b64_tr_b16 a[136:137], %[vr6] offset:0\n\t"
    "v_mfma_f32_16x16x32_bf16 v[244:247], a[140:143], a[20:23], v[244:247]\n\t" "ds_read_b64_tr_b16 a[138:139], %[vr6] offset:3584\n\t" "s_add_u32 s90, s90, 16384\n\t" "s_addc_u32 s91, s91, 0\n\t"
    "v_mfma_f32_16x16x32_bf16 v[248:251], a[140:143], a[24:27], v[248:251]\n\t" "global_load_dwordx2 a[232:233], v30, s[90:91] offset:0 sc1\n\t"
    "v_mfma_f32_16x16x32_bf16 v[252:255], a[140:143], a[28:31], v[252:255]\n\t" "s_waitcnt lgkmcnt(12)\n\t" "v_mfma_f32_16x16x32_bf16 v[32:35], a[144:147], a[32:35], v[32:35]\n\t"
    "ds_read_b64_tr_b16 a[140:141], %[vr0] offset:7168\n\t" "v_mfma_f32_16x16x32_bf16 v[36:39], a[144:147], a[36:39], v[36:39]\n\t" "ds_read_b64_tr_b16 a[142:143], %[vr0] offset:10752\n\t"
    "v_mfma_f32_16x16x32_bf16 v[40:43], a[144:147], a[40:43], v[40:43]\n\t" "global_load_dwordx4 a[80:83], %[va], s[76:77] offset:0 sc1\n\t"
    "v_mfma_f32_16x16x32_bf16 v[44:47], a[144:147], a[44:47], v[44:47]\n\t" "s_waitcnt lgkmcnt(12)\n\t" "v_mfma_f32_16x16x32_bf16 v[48:51], a[148:151], a[32:35], v[48:51]\n\t"
    "ds_read_b64_tr_b16 a[144:145], %[vr0] offset:7200\n\t" "v_mfma_f32_16x16x32_bf16 v[52:55], a[148:151], a[36:39], v[52:55]\n\t" "ds_read_b64_tr_b16 a[146:147], %[vr0] offset:10784\n\t"
    "v_mfma_f32_16x16x32_bf16 v[56:59], a[148:151], a[40:43], v[56:59]\n\t" "v_mfma_f32_16x16x32_bf16 v[60:63], a[148:151], a[44:47], v[60:63]\n\t" "s_waitcnt lgkmcnt(12)\n\t"
    "s_add_u32 m0, %[sl], 43008\n\t" "v_mfma_f32_16x16x32_bf16 v[64:67], a[152:155], a[32:35], v[64:67]\n\t" "global_load_lds_dwordx4 %[vb0], s[78:79]\n\t"
    "ds_read_b64_tr_b16 a[148:149], %[vr2] offset:7168\n\t" "v_mfma_f32_16x16x32_bf16 v[68:71], a[152:155], a[36:39], v[68:71]\n\t" "ds_read_b64_tr_b16 a[150:151], %[vr2] offset:10752\n\t"
    "v_mfma_f32_16x16x32_bf16 v[72:75], a[152:155], a[40:43], v[72:75]\n\t" "global_load_dwordx2 a[234:235], v30, s[90:91] offset:128 sc1\n\t"
    "v_mfma_f32_16x16x32_bf16 v[76:79], a[152:155], a[44:47], v[76:79]\n\t" "s_waitcnt lgkmcnt(12)\n\t" "v_mfma_f32_16x16x32_bf16 v[80:83], a[156:159], a[32:35], v[80:83]\n\t"
    "ds_read_b64_tr_b16 a[152:153], %[vr2] offset:7200\n\t" "v_mfma_f32_16x16x32_bf16 v[84:87], a[156:159], a[36:39], v[84:87]\n\t" "ds_read_b64_tr_b16 a[154:155], %[vr2] offset:10784\n\t"
    "v_mfma_f32_16x16x32_bf16 v[88:91], a[156:159], a[40:43], v[88:91]\n\t" "global_load_dwordx4 a[84:87], %[va], s[76:77] offset:1024 sc1\n\t"
    "v_mfma_f32_16x16x32_bf16 v[92:95], a[156:159], a[44:47], v[92:95]\n\t" "s_waitcnt lgkmcnt(12)\n\t" "v_mfma_f32_16x16x32_bf16 v[96:99], a[128:131], a[32:35], v[96:99]\n\t"
    "ds_read_b64_tr_b16 a[156:157], %[vr4] offset:7168\n\t" "v_mfma_f32_16x16x32_bf16 v[100:103], a[128:131], a[36:39], v[100:103]\n\t" "ds_read_b64_tr_b16 a[158:159], %[vr4] offset:10752\n\t"
    "v_mfma_f32_16x16x32_bf16 v[104:107], a[128:131], a[40:43], v[104:107]\n\t" "v_mfma_f32_16x16x32_bf16 v[108:111], a[128:131], a[44:47], v[108:111]\n\t" "s_waitcnt lgkmcnt(12)\n\t"
    "s_add_u32 m0, %[sl], 44032\n\t" "v_mfma_f32_16x16x32_bf16 v[112:115], a[132:135], a[32:35], v[112:115]\n\t" "global_load_lds_dwordx4 %[vb1], s[78:79]\n\t"
    "ds_read_b64_tr_b16 a[128:129], %[vr4] offset:7200\n\t" "v_mfma_f32_16x16x32_bf16 v[116:119], a[132:135], a[36:39], v[116:119]\n\t" "ds_read_b64_tr_b16 a[130:131], %[vr4] offset:10784\n\t"
    "v_mfma_f32_16x16x32_bf16 v[120:123], a[132:135], a[40:43], v[120:123]\n\t" "global_load_dwordx2 a[236:237], v30, s[90:91] offset:256 sc1\n\t"
    "v_mfma_f32_16x16x32_bf16 v[124:127], a[132:135], a[44:47], v[124:127]\n\t" "s_waitcnt lgkmcnt(12)\n\t" "v_mfma_f32_16x16x32_bf16 v[128:131], a[136:139], a[32:35], v[128:131]\n\t"
    "ds_read_b64_tr_b16 a[132:133], %[vr6] offset:7168\n\t" "v_mfma_f32_16x16x32_bf16 v[132:135], a[136:139], a[36:39], v[132:135]\n\t" "ds_read_b64_tr_b16 a[134:135], %[vr6] offset:10752\n\t"
    "v_mfma_f32_16x16x32_bf16 v[136:139], a[136:139], a[40:43], v[136:139]\n\t" "global_load_dwordx4 a[88:91], %[va], s[76:77] offset:2048 sc1\n\t"
    "v_mfma_f32_16x16x32_bf16 v[140:143], a[136:139], a[44:47], v[140:143]\n\t" "s_waitcnt lgkmcnt(12)\n\t" "v_mfma_f32_16x16x32_bf16 v[144:147], a[140:143], a[32:35], v[144:147]\n\t"
    "s_waitcnt vmcnt(21)\n\t" "s_barrier\n\t" "ds_read_b64_tr_b16 a[136:137], %[vr0] offset:14336\n\t" "v_mfma_f32_16x16x32_bf16 v[148:151], a[140:143], a[36:39], v[148:151]\n\t"
    "ds_read_b64_tr_b16 a[138:139], %[vr0] offset:17920\n\t" "v_mfma_f32_16x16x32_bf16 v[152:155], a[140:143], a[40:43], v[152:155]\n\t"
    "v_mfma_f32_16x16x32_bf16 v[156:159], a[140:143], a[44:47], v[156:159]\n\t" "s_waitcnt lgkmcnt(12)\n\t" "s_add_u32 m0, %[sl], 45056\n\t"
    "v_mfma_f32_16x16x32_bf16 v[160:163], a[144:147], a[32:35], v[160:163]\n\t" "global_load_lds_dwordx4 %[vb2], s[78:79]\n\t" "ds_read_b64_tr_b16 a[140:141], %[vr0] offset:14368\n\t"
    "v_mfma_f32_16x16x32_bf16 v[164:167], a[144:147], a[36:39], v[164:167]\n\t" "ds_read_b64_tr_b16 a[142:143], %[vr0] offset:17952\n\t"
    "v_mfma_f32_16x16x32_bf16 v[168:171], a[144:147], a[40:43], v[168:171]\n\t" "global_load_dwordx2 a[238:239], v30, s[90:91] offset:384 sc1\n\t"
    "v_mfma_f32_16x16x32_bf16 v[172:175], a[144:147], a[44:47], v[172:175]\n\t" "s_waitcnt lgkmcnt(12)\n\t" "v_mfma_f32_16x16x32_bf16 v[176:179], a[148:151], a[32:35], v[176:179]\n\t"
    "ds_read_b64_tr_b16 a[144:145], %[vr2] offset:14336\n\t" "v_mfma_f32_16x16x32_bf16 v[180:183], a[148:151], a[36:39], v[180:183]\n\t" "ds_read_b64_tr_b16 a[146:147], %[vr2] offset:17920\n\t"
    "v_mfma_f32_16x16x32_bf16 v[184:187], a[148:151], a[40:43], v[184:187]\n\t" "global_load_dwordx4 a[92:95], %[va], s[76:77] offset:3072 sc1\n\t" "s_add_u32 s76, s76, 4096\n\t"
    "s_addc_u32 s77, s77, 0\n\t" "v_mfma_f32_16x16x32_bf16 v[188:191], a[148:151], a[44:47], v[188:191]\n\t" "s_waitcnt lgkmcnt(12)\n\t"
    "v_mfma_f32_16x16x32_bf16 v[192:195], a[152:155], a[32:35], v[192:195]\n\t" "ds_read_b64_tr_b16 a[148:149], %[vr2] offset:14368\n\t"
    "v_mfma_f32_16x16x32_bf16 v[196:199], a[152:155], a[36:39], v[196:199]\n\t" "ds_read_b64_tr_b16 a[150:151], %[vr2] offset:17952\n\t"
    "v_mfma_f32_16x16x32_bf16 v[200:203], a[152:155], a[40:43], v[200:203]\n\t" "v_mfma_f32_16x16x32_bf16 v[204:207], a[152:155], a[44:47], v[204:207]\n\t" "s_waitcnt lgkmcnt(12)\n\t"
    "s_add_u32 m0, %[sl], 46080\n\t" "v_mfma_f32_16x16x32_bf16 v[208:211], a[156:159], a[32:35], v[208:211]\n\t" "s_mov_b64 exec, s[82:83]\n\t" "global_load_lds_dwordx4 %[vb3], s[78:79]\n\t"
    "s_mov_b64 exec, -1\n\t" "s_add_u32 s78, s78, 229376\n\t" "s_addc_u32 s79, s79, 0\n\t" "ds_read_b64_tr_b16 a[152:153], %[vr4] offset:14336\n\t"
    "v_mfma_f32_16x16x32_bf16 v[212:215], a[156:159], a[36:39], v[212:215]\n\t" "ds_read_b64_tr_b16 a[154:155], %[vr4] offset:17920\n\t"
    "v_mfma_f32_16x16x32_bf16 v[216:219], a[156:159], a[40:43], v[216:219]\n\t" "s_add_u32 s90, s90, 16384\n\t" "s_addc_u32 s91, s91, 0\n\t"
    "v_mfma_f32_16x16x32_bf16 v[220:223], a[156:159], a[44:47], v[220:223]\n\t" "global_load_dwordx2 a[240:241], v30, s[90:91] offset:0 sc1\n\t" "s_waitcnt lgkmcnt(12)\n\t"
    "v_mfma_f32_16x16x32_bf16 v[224:227], a[128:131], a[32:35], v[224:227]\n\t" "ds_read_b64_tr_b16 a[156:157], %[vr4] offset:14368\n\t"
    "v_mfma_f32_16x16x32_bf16 v[228:231], a[128:131], a[36:39], v[228:231]\n\t" "ds_read_b64_tr_b16 a[158:159], %[vr4] offset:17952\n\t"
    "v_mfma_f32_16x16x32_bf16 v[232:235], a[128:131], a[40:43], v[232:235]\n\t" "v_mfma_f32_16x16x32_bf16 v[236:239], a[128:131], a[44:47], v[236:239]\n\t" "s_waitcnt lgkmcnt(12)\n\t"
    "v_mfma_f32_16x16x32_bf16 v[240:243], a[132:135], a[32:35], v[240:243]\n\t" "ds_read_b64_tr_b16 a[128:129], %[vr6] offset:14336\n\t"
    "v_mfma_f32_16x16x32_bf16 v[244:247], a[132:135], a[36:39], v[244:247]\n\t" "ds_read_b64_tr_b16 a[130:131], %[vr6] offset:17920\n\t"
    "v_mfma_f32_16x16x32_bf16 v[248:251], a[132:135], a[40:43], v[248:251]\n\t" "v_mfma_f32_16x16x32_bf16 v[252:255], a[132:135], a[44:47], v[252:255]\n\t" "s_waitcnt lgkmcnt(12)\n\t"
    "v_mfma_f32_16x16x32_bf16 v[32:35], a[136:139], a[48:51], v[32:35]\n\t" "ds_read_b64_tr_b16 a[132:133], %[vr0] offset:21504\n\t" "v_mfma_f32_16x16x32_bf16 v[36:39], a[136:139], a[52:55], v[36:39]\n\t"
    "ds_read_b64_tr_b16 a[134:135], %[vr0] offset:25088\n\t" "v_mfma_f32_16x16x32_bf16 v[40:43], a[136:139], a[56:59], v[40:43]\n\t" "global_load_dwordx4 a[96:99], %[va], s[76:77] offset:0 sc1\n\t"
    "v_mfma_f32_16x16x32_bf16 v[44:47], a[136:139], a[60:63], v[44:47]\n\t" "global_load_dwordx2 a[242:243], v30, s[90:91] offset:128 sc1\n\t" "s_waitcnt lgkmcnt(12)\n\t"
    "v_mfma_f32_16x16x32_bf16 v[48:51], a[140:143], a[48:51], v[48:51]\n\t" "ds_read_b64_tr_b16 a[136:137], %[vr0] offset:21536\n\t" "v_mfma_f32_16x16x32_bf16 v[52:55], a[140:143], a[52:55], v[52:55]\n\t"
    "ds_read_b64_tr_b16 a[138:139], %[vr0] offset:25120\n\t" "v_mfma_f32_16x16x32_bf16 v[56:59], a[140:143], a[56:59], v[56:59]\n\t" "v_mfma_f32_16x16x32_bf16 v[60:63], a[140:143], a[60:63], v[60:63]\n\t"
    "s_waitcnt lgkmcnt(12)\n\t" "s_add_u32 m0, %[sl], 57344\n\t" "v_mfma_f32_16x16x32_bf16 v[64:67], a[144:147], a[48:51], v[64:67]\n\t" "global_load_lds_dwordx4 %[vb0], s[78:79]\n\t"
    "ds_read_b64_tr_b16 a[140:141], %[vr2] offset:21504\n\t" "v_mfma_f32_16x16x32_bf16 v[68:71], a[144:147], a[52:55], v[68:71]\n\t" "ds_read_b64_tr_b16 a[142:143], %[vr2] offset:25088\n\t"
    "v_mfma_f32_16x16x32_bf16 v[72:75], a[144:147], a[56:59], v[72:75]\n\t" "v_mfma_f32_16x16x32_bf16 v[76:79], a[144:147], a[60:63], v[76:79]\n\t" "s_waitcnt lgkmcnt(12)\n\t"
    "v_mfma_f32_16x16x32_bf16 v[80:83], a[148:151], a[48:51], v[80:83]\n\t" "ds_read_b64_tr_b16 a[144:145], %[vr2] offset:21536\n\t" "v_mfma_f32_16x16x32_bf16 v[84:87], a[148:151], a[52:55], v[84:87]\n\t"
    "ds_read_b64_tr_b16 a[146:147], %[vr2] offset:25120\n\t" "v_mfma_f32_16x16x32_bf16 v[88:91], a[148:151], a[56:59], v[88:91]\n\t" "global_load_dwordx4 a[100:103], %[va], s[76:77] offset:1024 sc1\n\t"
    "v_mfma_f32_16x16x32_bf16 v[92:95], a[148:151], a[60:63], v[92:95]\n\t" "global_load_dwordx2 a[244:245], v30, s[90:91] offset:256 sc1\n\t" "s_waitcnt lgkmcnt(12)\n\t"
    "v_mfma_f32_16x16x32_bf16 v[96:99], a[152:155], a[48:51], v[96:99]\n\t" "ds_read_b64_tr_b16 a[148:149], %[vr4] offset:21504\n\t"
    "v_mfma_f32_16x16x32_bf16 v[100:103], a[152:155], a[52:55], v[100:103]\n\t" "ds_read_b64_tr_b16 a[150:151], %[vr4] offset:25088\n\t"
    "v_mfma_f32_16x16x32_bf16 v[104:107], a[152:155], a[56:59], v[104:107]\n\t" "v_mfma_f32_16x16x32_bf16 v[108:111], a[152:155], a[60:63], v[108:111]\n\t" "s_waitcnt lgkmcnt(12)\n\t"
    "s_add_u32 m0, %[sl], 58368\n\t" "v_mfma_f32_16x16x32_bf16 v[112:115], a[156:159], a[48:51], v[112:115]\n\t" "global_load_lds_dwordx4 %[vb1], s[78:79]\n\t"
    "ds_read_b64_tr_b16 a[152:153], %[vr4] offset:21536\n\t" "v_mfma_f32_16x16x32_bf16 v[116:119], a[156:159], a[52:55], v[116:119]\n\t" "ds_read_b64_tr_b16 a[154:155], %[vr4] offset:25120\n\t"
    "v_mfma_f32_16x16x32_bf16 v[120:123], a[156:159], a[56:59], v[120:123]\n\t" "v_mfma_f32_16x16x32_bf16 v[124:127], a[156:159], a[60:63], v[124:127]\n\t" "s_waitcnt lgkmcnt(12)\n\t"
    "v_mfma_f32_16x16x32_bf16 v[128:131], a[128:131], a[48:51], v[128:131]\n\t" "ds_read_b64_tr_b16 a[156:157], %[vr6] offset:21504\n\t"
    "v_mfma_f32_16x16x32_bf16 v[132:135], a[128:131], a[52:55], v[132:135]\n\t" "ds_read_b64_tr_b16 a[158:159], %[vr6] offset:25088\n\t"
    "v_mfma_f32_16x16x32_bf16 v[136:139], a[128:131], a[56:59], v[136:139]\n\t" "global_load_dwordx4 a[104:107], %[va], s[76:77] offset:2048 sc1\n\t"
    "v_mfma_f32_16x16x32_bf16 v[140:143], a[128:131], a[60:63], v[140:143]\n\t" "global_load_dwordx2 a[246:247], v30, s[90:91] offset:384 sc1\n\t" "s_waitcnt lgkmcnt(12)\n\t"
    "v_mfma_f32_16x16x32_bf16 v[144:147], a[132:135], a[48:51], v[144:147]\n\t" "s_waitcnt vmcnt(21)\n\t" "s_barrier\n\t" "ds_read_b64_tr_b16 a[128:129], %[vr0] offset:28672\n\t"
    "v_mfma_f32_16x16x32_bf16 v[148:151], a[132:135], a[52:55], v[148:151]\n\t" "ds_read_b64_tr_b16 a[130:131], %[vr0] offset:32256\n\t"
    "v_mfma_f32_16x16x32_bf16 v[152:155], a[132:135], a[56:59], v[152:155]\n\t" "v_mfma_f32_16x16x32_bf16 v[156:159], a[132:135], a[60:63], v[156:159]\n\t" "s_waitcnt lgkmcnt(12)\n\t"
    "s_add_u32 m0, %[sl], 59392\n\t" "v_mfma_f32_16x16x32_bf16 v[160:163], a[136:139], a[48:51], v[160:163]\n\t" "global_load_lds_dwordx4 %[vb2], s[78:79]\n\t"
    "ds_read_b64_tr_b16 a[132:133], %[vr0] offset:28704\n\t" "v_mfma_f32_16x16x32_bf16 v[164:167], a[136:139], a[52:55], v[164:167]\n\t" "ds_read_b64_tr_b16 a[134:135], %[vr0] offset:32288\n\t"
    "v_mfma_f32_16x16x32_bf16 v[168:171], a[136:139], a[56:59], v[168:171]\n\t" "v_mfma_f32_16x16x32_bf16 v[172:175], a[136:139], a[60:63], v[172:175]\n\t" "s_waitcnt lgkmcnt(12)\n\t"
    "v_mfma_f32_16x16x32_bf16 v[176:179], a[140:143], a[48:51], v[176:179]\n\t" "ds_read_b64_tr_b16 a[136:137], %[vr2] offset:28672\n\t"
    "v_mfma_f32_16x16x32_bf16 v[180:183], a[140:143], a[52:55], v[180:183]\n\t" "ds_read_b64_tr_b16 a[138:139], %[vr2] offset:32256\n\t"
    "v_mfma_f32_16x16x32_bf16 v[184:187], a[140:143], a[56:59], v[184:187]\n\t" "global_load_dwordx4 a[108:111], %[va], s[76:77] offset:3072 sc1\n\t" "s_add_u32 s76, s76, 4096\n\t"
    "s_addc_u32 s77, s77, 0\n\t" "v_mfma_f32_16x16x32_bf16 v[188:191], a[140:143], a[60:63], v[188:191]\n\t" "s_waitcnt lgkmcnt(12)\n\t"
    "v_mfma_f32_16x16x32_bf16 v[192:195], a[144:147], a[48:51], v[192:195]\n\t" "ds_read_b64_tr_b16 a[140:141], %[vr2] offset:28704\n\t"
    "v_mfma_f32_16x16x32_bf16 v[196:199], a[144:147], a[52:55], v[196:199]\n\t" "ds_read_b64_tr_b16 a[142:143], %[vr2] offset:32288\n\t"
    "v_mfma_f32_16x16x32_bf16 v[200:203], a[144:147], a[56:59], v[200:203]\n\t" "v_mfma_f32_16x16x32_bf16 v[204:207], a[144:147], a[60:63], v[204:207]\n\t" "s_waitcnt lgkmcnt(12)\n\t"
    "s_add_u32 m0, %[sl], 60416\n\t" "v_mfma_f32_16x16x32_bf16 v[208:211], a[148:151], a[48:51], v[208:211]\n\t" "s_mov_b64 exec, s[82:83]\n\t" "global_load_lds_dwordx4 %[vb3], s[78:79]\n\t"
    "s_mov_b64 exec, -1\n\t" "s_add_u32 s78, s78, 229376\n\t" "s_addc_u32 s79, s79, 0\n\t" "ds_read_b64_tr_b16 a[144:145], %[vr4] offset:28672\n\t"
    "v_mfma_f32_16x16x32_bf16 v[212:215], a[148:151], a[52:55], v[212:215]\n\t" "ds_read_b64_tr_b16 a[146:147], %[vr4] offset:32256\n\t"
    "v_mfma_f32_16x16x32_bf16 v[216:219], a[148:151], a[56:59], v[216:219]\n\t" "v_mfma_f32_16x16x32_bf16 v[220:223], a[148:151], a[60:63], v[220:223]\n\t" "s_waitcnt lgkmcnt(12)\n\t"
    "v_mfma_f32_16x16x32_bf16 v[224:227], a[152:155], a[48:51], v[224:227]\n\t" "ds_read_b64_tr_b16 a[148:149], %[vr4] offset:28704\n\t"
    "v_mfma_f32_16x16x32_bf16 v[228:231], a[152:155], a[52:55], v[228:231]\n\t" "ds_read_b64_tr_b16 a[150:151], %[vr4] offset:32288\n\t"
    "v_mfma_f32_16x16x32_bf16 v[232:235], a[152:155], a[56:59], v[232:235]\n\t" "v_mfma_f32_16x16x32_bf16 v[236:239], a[152:155], a[60:63], v[236:239]\n\t" "s_waitcnt lgkmcnt(12)\n\t"
    "v_mfma_f32_16x16x32_bf16 v[240:243], a[156:159], a[48:51], v[240:243]\n\t" "ds_read_b64_tr_b16 a[152:153], %[vr6] offset:28672\n\t"
    "v_mfma_f32_16x16x32_bf16 v[244:247], a[156:159], a[52:55], v[244:247]\n\t" "ds_read_b64_tr_b16 a[154:155], %[vr6] offset:32256\n\t"
    "v_mfma_f32_16x16x32_bf16 v[248:251], a[156:159], a[56:59], v[248:251]\n\t" "v_mfma_f32_16x16x32_bf16 v[252:255], a[156:159], a[60:63], v[252:255]\n\t" ".Lloop_%=:\n\t" "s_cmp_eq_u32 s80, 1\n\t"
    "s_cselect_b32 s86, %[wa], 4096\n\t" "s_cselect_b32 s88, %[wb], 229376\n\t" "s_ashr_i32 s87, s86, 31\n\t" "s_ashr_i32 s89, s88, 31\n\t" "s_cmp_eq_u32 s80, %[e1]\n\t" "s_cbranch_scc1 .LE1_%=\n\t"
    "s_waitcnt lgkmcnt(12)\n\t" "v_mfma_f32_16x16x32_bf16 v[32:35], a[128:131], a[64:67], v[32:35]\n\t" "ds_read_b64_tr_b16 a[156:157], %[vr0] offset:35840\n\t"
    "v_mfma_f32_16x16x32_bf16 v[36:39], a[128:131], a[68:71], v[36:39]\n\t" "ds_read_b64_tr_b16 a[158:159], %[vr0] offset:39424\n\t" "v_mfma_f32_16x16x32_bf16 v[40:43], a[128:131], a[72:75], v[40:43]\n\t"
    "global_load_dwordx4 a[112:115], %[va], s[76:77] offset:0 sc1\n\t" "v_mfma_f32_16x16x32_bf16 v[44:47], a[128:131], a[76:79], v[44:47]\n\t" "s_waitcnt lgkmcnt(12)\n\t"
    "v_mfma_f32_16x16x32_bf16 v[48:51], a[132:135], a[64:67], v[48:51]\n\t" "ds_read_b64_tr_b16 a[128:129], %[vr0] offset:35872\n\t" "v_mfma_f32_16x16x32_bf16 v[52:55], a[132:135], a[68:71], v[52:55]\n\t"
    "ds_read_b64_tr_b16 a[130:131], %[vr0] offset:39456\n\t" "s_load_dwordx2 s[98:99], s[84:85], 60\n\t" "v_mfma_f32_16x16x32_bf16 v[56:59], a[132:135], a[72:75], v[56:59]\n\t"
    "v_mfma_f32_16x16x32_bf16 v[60:63], a[132:135], a[76:79], v[60:63]\n\t" "s_waitcnt lgkmcnt(12)\n\t" "s_add_u32 m0, %[sl], 71680\n\t"
    "v_mfma_f32_16x16x32_bf16 v[64:67], a[136:139], a[64:67], v[64:67]\n\t" "global_load_lds_dwordx4 %[vb0], s[78:79]\n\t" "ds_read_b64_tr_b16 a[132:133], %[vr2] offset:35840\n\t"
    "v_mfma_f32_16x16x32_bf16 v[68:71], a[136:139], a[68:71], v[68:71]\n\t" "ds_read_b64_tr_b16 a[134:135], %[vr2] offset:39424\n\t" "v_mfma_f32_16x16x32_bf16 v[72:75], a[136:139], a[72:75], v[72:75]\n\t"
    "v_mfma_f32_16x16x32_bf16 v[76:79], a[136:139], a[76:79], v[76:79]\n\t" "s_waitcnt lgkmcnt(12)\n\t" "v_mfma_f32_16x16x32_bf16 v[80:83], a[140:143], a[64:67], v[80:83]\n\t"
    "ds_read_b64_tr_b16 a[136:137], %[vr2] offset:35872\n\t" "v_mfma_f32_16x16x32_bf16 v[84:87], a[140:143], a[68:71], v[84:87]\n\t" "ds_read_b64_tr_b16 a[138:139], %[vr2] offset:39456\n\t"
    "v_mfma_f32_16x16x32_bf16 v[88:91], a[140:143], a[72:75], v[88:91]\n\t" "global_load_dwordx4 a[116:119], %[va], s[76:77] offset:1024 sc1\n\t"
    "v_mfma_f32_16x16x32_bf16 v[92:95], a[140:143], a[76:79], v[92:95]\n\t" "s_waitcnt lgkmcnt(12)\n\t" "v_mfma_f32_16x16x32_bf16 v[96:99], a[144:147], a[64:67], v[96:99]\n\t"
    "ds_read_b64_tr_b16 a[140:141], %[vr4] offset:35840\n\t" "v_mfma_f32_16x16x32_bf16 v[100:103], a[144:147], a[68:71], v[100:103]\n\t" "ds_read_b64_tr_b16 a[142:143], %[vr4] offset:39424\n\t"
    "v_mfma_f32_16x16x32_bf16 v[104:107], a[144:147], a[72:75], v[104:107]\n\t" "v_mfma_f32_16x16x32_bf16 v[108:111], a[144:147], a[76:79], v[108:111]\n\t" "s_waitcnt lgkmcnt(12)\n\t"
    "s_add_u32 m0, %[sl], 72704\n\t" "v_mfma_f32_16x16x32_bf16 v[112:115], a[148:151], a[64:67], v[112:115]\n\t" "global_load_lds_dwordx4 %[vb1], s[78:79]\n\t"
    "ds_read_b64_tr_b16 a[144:145], %[vr4] offset:35872\n\t" "v_mfma_f32_16x16x32_bf16 v[116:119], a[148:151], a[68:71], v[116:119]\n\t" "ds_read_b64_tr_b16 a[146:147], %[vr4] offset:39456\n\t"
    "v_mfma_f32_16x16x32_bf16 v[120:123], a[148:151], a[72:75], v[120:123]\n\t" "v_mfma_f32_16x16x32_bf16 v[124:127], a[148:151], a[76:79], v[124:127]\n\t" "s_load_dwordx2 s[98:99], s[84:85], 188\n\t"
    "s_waitcnt lgkmcnt(12)\n\t" "v_mfma_f32_16x16x32_bf16 v[128:131], a[152:155], a[64:67], v[128:131]\n\t" "ds_read_b64_tr_b16 a[148:149], %[vr6] offset:35840\n\t"
    "v_mfma_f32_16x16x32_bf16 v[132:135], a[152:155], a[68:71], v[132:135]\n\t" "ds_read_b64_tr_b16 a[150:151], %[vr6] offset:39424\n\t"
    "v_mfma_f32_16x16x32_bf16 v[136:139], a[152:155], a[72:75], v[136:139]\n\t" "global_load_dwordx4 a[120:123], %[va], s[76:77] offset:2048 sc1\n\t"
    "v_mfma_f32_16x16x32_bf16 v[140:143], a[152:155], a[76:79], v[140:143]\n\t" "s_waitcnt lgkmcnt(12)\n\t" "v_mfma_f32_16x16x32_bf16 v[144:147], a[156:159], a[64:67], v[144:147]\n\t"
    "s_waitcnt vmcnt(13)\n\t" "s_barrier\n\t" "ds_read_b64_tr_b16 a[152:153], %[vr0] offset:43008\n\t" "v_mfma_f32_16x16x32_bf16 v[148:151], a[156:159], a[68:71], v[148:151]\n\t"
    "ds_read_b64_tr_b16 a[154:155], %[vr0] offset:46592\n\t" "v_mfma_f32_16x16x32_bf16 v[152:155], a[156:159], a[72:75], v[152:155]\n\t"
    "v_mfma_f32_16x16x32_bf16 v[156:159], a[156:159], a[76:79], v[156:159]\n\t" "s_waitcnt lgkmcnt(12)\n\t" "s_add_u32 m0, %[sl], 73728\n\t"
    "v_mfma_f32_16x16x32_bf16 v[160:163], a[128:131], a[64:67], v[160:163]\n\t" "global_load_lds_dwordx4 %[vb2], s[78:79]\n\t" "ds_read_b64_tr_b16 a[156:157], %[vr0] offset:43040\n\t"
    "v_mfma_f32_16x16x32_bf16 v[164:167], a[128:131], a[68:71], v[164:167]\n\t" "ds_read_b64_tr_b16 a[158:159], %[vr0] offset:46624\n\t"
    "v_mfma_f32_16x16x32_bf16 v[168:171], a[128:131], a[72:75], v[168:171]\n\t" "v_mfma_f32_16x16x32_bf16 v[172:175], a[128:131], a[76:79], v[172:175]\n\t" "s_waitcnt lgkmcnt(12)\n\t"
    "v_mfma_f32_16x16x32_bf16 v[176:179], a[132:135], a[64:67], v[176:179]\n\t" "ds_read_b64_tr_b16 a[128:129], %[vr2] offset:43008\n\t"
    "v_mfma_f32_16x16x32_bf16 v[180:183], a[132:135], a[68:71], v[180:183]\n\t" "ds_read_b64_tr_b16 a[130:131], %[vr2] offset:46592\n\t"
    "v_mfma_f32_16x16x32_bf16 v[184:187], a[132:135], a[72:75], v[184:187]\n\t" "global_load_dwordx4 a[124:127], %[va], s[76:77] offset:3072 sc1\n\t" "s_add_u32 s76, s76, s86\n\t"
    "s_addc_u32 s77, s77, s87\n\t" "v_mfma_f32_16x16x32_bf16 v[188:191], a[132:135], a[76:79], v[188:191]\n\t" "s_waitcnt lgkmcnt(12)\n\t"
    "v_mfma_f32_16x16x32_bf16 v[192:195], a[136:139], a[64:67], v[192:195]\n\t" "ds_read_b64_tr_b16 a[132:133], %[vr2] offset:43040\n\t"
    "v_mfma_f32_16x16x32_bf16 v[196:199], a[136:139], a[68:71], v[196:199]\n\t" "ds_read_b64_tr_b16 a[134:135], %[vr2] offset:46624\n\t" "s_load_dwordx2 s[98:99], s[84:85], 316\n\t"
    "s_add_u32 s84, s84, 384\n\t" "s_addc_u32 s85, s85, 0\n\t" "v_mfma_f32_16x16x32_bf16 v[200:203], a[136:139], a[72:75], v[200:203]\n\t"
    "v_mfma_f32_16x16x32_bf16 v[204:207], a[136:139], a[76:79], v[204:207]\n\t" "s_waitcnt lgkmcnt(12)\n\t" "s_add_u32 m0, %[sl], 74752\n\t"
    "v_mfma_f32_16x16x32_bf16 v[208:211], a[140:143], a[64:67], v[208:211]\n\t" "s_mov_b64 exec, s[82:83]\n\t" "global_load_lds_dwordx4 %[vb3], s[78:79]\n\t" "s_mov_b64 exec, -1\n\t"
    "s_add_u32 s78, s78, s88\n\t" "s_addc_u32 s79, s79, s89\n\t" "ds_read_b64_tr_b16 a[136:137], %[vr4] offset:43008\n\t" "v_mfma_f32_16x16x32_bf16 v[212:215], a[140:143], a[68:71], v[212:215]\n\t"
    "ds_read_b64_tr_b16 a[138:139], %[vr4] offset:46592\n\t" "v_mfma_f32_16x16x32_bf16 v[216:219], a[140:143], a[72:75], v[216:219]\n\t"
    "v_mfma_f32_16x16x32_bf16 v[220:223], a[140:143], a[76:79], v[220:223]\n\t" "s_waitcnt lgkmcnt(12)\n\t" "v_mfma_f32_16x16x32_bf16 v[224:227], a[144:147], a[64:67], v[224:227]\n\t"
    "ds_read_b64_tr_b16 a[140:141], %[vr4] offset:43040\n\t" "v_mfma_f32_16x16x32_bf16 v[228:231], a[144:147], a[68:71], v[228:231]\n\t" "ds_read_b64_tr_b16 a[142:143], %[vr4] offset:46624\n\t"
    "v_mfma_f32_16x16x32_bf16 v[232:235], a[144:147], a[72:75], v[232:235]\n\t" "v_mfma_f32_16x16x32_bf16 v[236:239], a[144:147], a[76:79], v[236:239]\n\t" "s_waitcnt lgkmcnt(12)\n\t"
    "v_mfma_f32_16x16x32_bf16 v[240:243], a[148:151], a[64:67], v[240:243]\n\t" "ds_read_b64_tr_b16 a[144:145], %[vr6] offset:43008\n\t"
    "v_mfma_f32_16x16x32_bf16 v[244:247], a[148:151], a[68:71], v[244:247]\n\t" "ds_read_b64_tr_b16 a[146:147], %[vr6] offset:46592\n\t"
    "v_mfma_f32_16x16x32_bf16 v[248:251], a[148:151], a[72:75], v[248:251]\n\t" "v_mfma_f32_16x16x32_bf16 v[252:255], a[148:151], a[76:79], v[252:255]\n\t" "s_waitcnt lgkmcnt(12)\n\t"
    "v_mfma_f32_16x16x32_bf16 v[32:35], a[152:155], a[80:83], v[32:35]\n\t" "ds_read_b64_tr_b16 a[148:149], %[vr0] offset:50176\n\t" "v_mfma_f32_16x16x32_bf16 v[36:39], a[152:155], a[84:87], v[36:39]\n\t"
    "ds_read_b64_tr_b16 a[150:151], %[vr0] offset:53760\n\t" "v_mfma_f32_16x16x32_bf16 v[40:43], a[152:155], a[88:91], v[40:43]\n\t" "global_load_dwordx4 a[0:3], %[va], s[76:77] offset:0 sc1\n\t"
    "v_mfma_f32_16x16x32_bf16 v[44:47], a[152:155], a[92:95], v[44:47]\n\t" "s_waitcnt lgkmcnt(12)\n\t" "v_mfma_f32_16x16x32_bf16 v[48:51], a[156:159], a[80:83], v[48:51]\n\t"
    "ds_read_b64_tr_b16 a[152:153], %[vr0] offset:50208\n\t" "v_mfma_f32_16x16x32_bf16 v[52:55], a[156:159], a[84:87], v[52:55]\n\t" "ds_read_b64_tr_b16 a[154:155], %[vr0] offset:53792\n\t"
    "s_load_dwordx2 s[98:99], s[84:85], 60\n\t" "v_mfma_f32_16x16x32_bf16 v[56:59], a[156:159], a[88:91], v[56:59]\n\t" "v_mfma_f32_16x16x32_bf16 v[60:63], a[156:159], a[92:95], v[60:63]\n\t"
    "s_waitcnt lgkmcnt(12)\n\t" "s_add_u32 m0, %[sl], 86016\n\t" "v_mfma_f32_16x16x32_bf16 v[64:67], a[128:131], a[80:83], v[64:67]\n\t" "global_load_lds_dwordx4 %[vb0], s[78:79]\n\t"
    "ds_read_b64_tr_b16 a[156:157], %[vr2] offset:50176\n\t" "v_mfma_f32_16x16x32_bf16 v[68:71], a[128:131], a[84:87], v[68:71]\n\t" "ds_read_b64_tr_b16 a[158:159], %[vr2] offset:53760\n\t"
    "v_mfma_f32_16x16x32_bf16 v[72:75], a[128:131], a[88:91], v[72:75]\n\t" "v_mfma_f32_16x16x32_bf16 v[76:79], a[128:131], a[92:95], v[76:79]\n\t" "s_waitcnt lgkmcnt(12)\n\t"
    "v_mfma_f32_16x16x32_bf16 v[80:83], a[132:135], a[80:83], v[80:83]\n\t" "ds_read_b64_tr_b16 a[128:129], %[vr2] offset:50208\n\t" "v_mfma_f32_16x16x32_bf16 v[84:87], a[132:135], a[84:87], v[84:87]\n\t"
    "ds_read_b64_tr_b16 a[130:131], %[vr2] offset:53792\n\t" "v_mfma_f32_16x16x32_bf16 v[88:91], a[132:135], a[88:91], v[88:91]\n\t" "global_load_dwordx4 a[4:7], %[va], s[76:77] offset:1024 sc1\n\t"
    "v_mfma_f32_16x16x32_bf16 v[92:95], a[132:135], a[92:95], v[92:95]\n\t" "s_waitcnt lgkmcnt(12)\n\t" "v_mfma_f32_16x16x32_bf16 v[96:99], a[136:139], a[80:83], v[96:99]\n\t"
    "ds_read_b64_tr_b16 a[132:133], %[vr4] offset:50176\n\t" "v_mfma_f32_16x16x32_bf16 v[100:103], a[136:139], a[84:87], v[100:103]\n\t" "ds_read_b64_tr_b16 a[134:135], %[vr4] offset:53760\n\t"
    "v_mfma_f32_16x16x32_bf16 v[104:107], a[136:139], a[88:91], v[104:107]\n\t" "v_mfma_f32_16x16x32_bf16 v[108:111], a[136:139], a[92:95], v[108:111]\n\t" "s_waitcnt lgkmcnt(12)\n\t"
    "s_add_u32 m0, %[sl], 87040\n\t" "v_mfma_f32_16x16x32_bf16 v[112:115], a[140:143], a[80:83], v[112:115]\n\t" "global_load_lds_dwordx4 %[vb1], s[78:79]\n\t"
    "ds_read_b64_tr_b16 a[136:137], %[vr4] offset:50208\n\t" "v_mfma_f32_16x16x32_bf16 v[116:119], a[140:143], a[84:87], v[116:119]\n\t" "ds_read_b64_tr_b16 a[138:139], %[vr4] offset:53792\n\t"
    "v_mfma_f32_16x16x32_bf16 v[120:123], a[140:143], a[88:91], v[120:123]\n\t" "v_mfma_f32_16x16x32_bf16 v[124:127], a[140:143], a[92:95], v[124:127]\n\t" "s_load_dwordx2 s[98:99], s[84:85], 188\n\t"
    "s_waitcnt lgkmcnt(12)\n\t" "v_mfma_f32_16x16x32_bf16 v[128:131], a[144:147], a[80:83], v[128:131]\n\t" "ds_read_b64_tr_b16 a[140:141], %[vr6] offset:50176\n\t"
    "v_mfma_f32_16x16x32_bf16 v[132:135], a[144:147], a[84:87], v[132:135]\n\t" "ds_read_b64_tr_b16 a[142:143], %[vr6] offset:53760\n\t"
    "v_mfma_f32_16x16x32_bf16 v[136:139], a[144:147], a[88:91], v[136:139]\n\t" "global_load_dwordx4 a[8:11], %[va], s[76:77] offset:2048 sc1\n\t"
    "v_mfma_f32_16x16x32_bf16 v[140:143], a[144:147], a[92:95], v[140:143]\n\t" "s_waitcnt lgkmcnt(12)\n\t" "v_mfma_f32_16x16x32_bf16 v[144:147], a[148:151], a[80:83], v[144:147]\n\t"
    "s_waitcnt vmcnt(13)\n\t" "s_barrier\n\t" "ds_read_b64_tr_b16 a[144:145], %[vr1] offset:0\n\t" "v_mfma_f32_16x16x32_bf16 v[148:151], a[148:151], a[84:87], v[148:151]\n\t"
    "ds_read_b64_tr_b16 a[146:147], %[vr1] offset:3584\n\t" "v_mfma_f32_16x16x32_bf16 v[152:155], a[148:151], a[88:91], v[152:155]\n\t"
    "v_mfma_f32_16x16x32_bf16 v[156:159], a[148:151], a[92:95], v[156:159]\n\t" "s_waitcnt lgkmcnt(12)\n\t" "s_add_u32 m0, %[sl], 88064\n\t"
    "v_mfma_f32_16x16x32_bf16 v[160:163], a[152:155], a[80:83], v[160:163]\n\t" "global_load_lds_dwordx4 %[vb2], s[78:79]\n\t" "ds_read_b64_tr_b16 a[148:149], %[vr1] offset:32\n\t"
    "v_mfma_f32_16x16x32_bf16 v[164:167], a[152:155], a[84:87], v[164:167]\n\t" "ds_read_b64_tr_b16 a[150:151], %[vr1] offset:3616\n\t"
    "v_mfma_f32_16x16x32_bf16 v[168:171], a[152:155], a[88:91], v[168:171]\n\t" "v_mfma_f32_16x16x32_bf16 v[172:175], a[152:155], a[92:95], v[172:175]\n\t" "s_waitcnt lgkmcnt(12)\n\t"
    "v_mfma_f32_16x16x32_bf16 v[176:179], a[156:159], a[80:83], v[176:179]\n\t" "ds_read_b64_tr_b16 a[152:153], %[vr3] offset:0\n\t"
    "v_mfma_f32_16x16x32_bf16 v[180:183], a[156:159], a[84:87], v[180:183]\n\t" "ds_read_b64_tr_b16 a[154:155], %[vr3] offset:3584\n\t"
    "v_mfma_f32_16x16x32_bf16 v[184:187], a[156:159], a[88:91], v[184:187]\n\t" "global_load_dwordx4 a[12:15], %[va], s[76:77] offset:3072 sc1\n\t" "s_add_u32 s76, s76, 4096\n\t"
    "s_addc_u32 s77, s77, 0\n\t" "v_mfma_f32_16x16x32_bf16 v[188:191], a[156:159], a[92:95], v[188:191]\n\t" "s_waitcnt lgkmcnt(12)\n\t"
    "v_mfma_f32_16x16x32_bf16 v[192:195], a[128:131], a[80:83], v[192:195]\n\t" "ds_read_b64_tr_b16 a[156:157], %[vr3] offset:32\n\t"
    "v_mfma_f32_16x16x32_bf16 v[196:199], a[128:131], a[84:87], v[196:199]\n\t" "ds_read_b64_tr_b16 a[158:159], %[vr3] offset:3616\n\t" "s_load_dwordx2 s[98:99], s[84:85], 316\n\t"
    "s_add_u32 s84, s84, 384\n\t" "s_addc_u32 s85, s85, 0\n\t" "v_mfma_f32_16x16x32_bf16 v[200:203], a[128:131], a[88:91], v[200:203]\n\t"
    "v_mfma_f32_16x16x32_bf16 v[204:207], a[128:131], a[92:95], v[204:207]\n\t" "s_waitcnt lgkmcnt(12)\n\t" "s_add_u32 m0, %[sl], 89088\n\t"
    "v_mfma_f32_16x16x32_bf16 v[208:211], a[132:135], a[80:83], v[208:211]\n\t" "s_mov_b64 exec, s[82:83]\n\t" "global_load_lds_dwordx4 %[vb3], s[78:79]\n\t" "s_mov_b64 exec, -1\n\t"
    "s_add_u32 s78, s78, 229376\n\t" "s_addc_u32 s79, s79, 0\n\t" "ds_read_b64_tr_b16 a[128:129], %[vr5] offset:0\n\t" "v_mfma_f32_16x16x32_bf16 v[212:215], a[132:135], a[84:87], v[212:215]\n\t"
    "ds_read_b64_tr_b16 a[130:131], %[vr5] offset:3584\n\t" "v_mfma_f32_16x16x32_bf16 v[216:219], a[132:135], a[88:91], v[216:219]\n\t"
    "v_mfma_f32_16x16x32_bf16 v[220:223], a[132:135], a[92:95], v[220:223]\n\t" "s_waitcnt lgkmcnt(12)\n\t" "v_mfma_f32_16x16x32_bf16 v[224:227], a[136:139], a[80:83], v[224:227]\n\t"
    "ds_read_b64_tr_b16 a[132:133], %[vr5] offset:32\n\t" "v_mfma_f32_16x16x32_bf16 v[228:231], a[136:139], a[84:87], v[228:231]\n\t" "ds_read_b64_tr_b16 a[134:135], %[vr5] offset:3616\n\t"
    "v_mfma_f32_16x16x32_bf16 v[232:235], a[136:139], a[88:91], v[232:235]\n\t" "v_mfma_f32_16x16x32_bf16 v[236:239], a[136:139], a[92:95], v[236:239]\n\t" "s_waitcnt lgkmcnt(12)\n\t"
    "v_mfma_f32_16x16x32_bf16 v[240:243], a[140:143], a[80:83], v[240:243]\n\t" "ds_read_b64_tr_b16 a[136:137], %[vr7] offset:0\n\t"
    "v_mfma_f32_16x16x32_bf16 v[244:247], a[140:143], a[84:87], v[244:247]\n\t" "ds_read_b64_tr_b16 a[138:139], %[vr7] offset:3584\n\t"
    "v_mfma_f32_16x16x32_bf16 v[248:251], a[140:143], a[88:91], v[248:251]\n\t" "v_mfma_f32_16x16x32_bf16 v[252:255], a[140:143], a[92:95], v[252:255]\n\t" "s_waitcnt lgkmcnt(12)\n\t"
    "v_mfma_f32_16x16x32_bf16 v[32:35], a[144:147], a[96:99], v[32:35]\n\t" "ds_read_b64_tr_b16 a[140:141], %[vr1] offset:7168\n\t"
    "v_mfma_f32_16x16x32_bf16 v[36:39], a[144:147], a[100:103], v[36:39]\n\t" "ds_read_b64_tr_b16 a[142:143], %[vr1] offset:10752\n\t"
    "v_mfma_f32_16x16x32_bf16 v[40:43], a[144:147], a[104:107], v[40:43]\n\t" "global_load_dwordx4 a[16:19], %[va], s[76:77] offset:0 sc1\n\t"
    "v_mfma_f32_16x16x32_bf16 v[44:47], a[144:147], a[108:111], v[44:47]\n\t" "s_waitcnt lgkmcnt(12)\n\t" "v_mfma_f32_16x16x32_bf16 v[48:51], a[148:151], a[96:99], v[48:51]\n\t"
    "ds_read_b64_tr_b16 a[144:145], %[vr1] offset:7200\n\t" "v_mfma_f32_16x16x32_bf16 v[52:55], a[148:151], a[100:103], v[52:55]\n\t" "ds_read_b64_tr_b16 a[146:147], %[vr1] offset:10784\n\t"
    "s_load_dwordx2 s[98:99], s[84:85], 60\n\t" "v_mfma_f32_16x16x32_bf16 v[56:59], a[148:151], a[104:107], v[56:59]\n\t" "v_mfma_f32_16x16x32_bf16 v[60:63], a[148:151], a[108:111], v[60:63]\n\t"
    "s_waitcnt lgkmcnt(12)\n\t" "s_add_u32 m0, %[sl], 100352\n\t" "v_mfma_f32_16x16x32_bf16 v[64:67], a[152:155], a[96:99], v[64:67]\n\t" "global_load_lds_dwordx4 %[vb0], s[78:79]\n\t"
    "ds_read_b64_tr_b16 a[148:149], %[vr3] offset:7168\n\t" "v_mfma_f32_16x16x32_bf16 v[68:71], a[152:155], a[100:103], v[68:71]\n\t" "ds_read_b64_tr_b16 a[150:151], %[vr3] offset:10752\n\t"
    "v_mfma_f32_16x16x32_bf16 v[72:75], a[152:155], a[104:107], v[72:75]\n\t" "v_mfma_f32_16x16x32_bf16 v[76:79], a[152:155], a[108:111], v[76:79]\n\t" "s_waitcnt lgkmcnt(12)\n\t"
    "v_mfma_f32_16x16x32_bf16 v[80:83], a[156:159], a[96:99], v[80:83]\n\t" "ds_read_b64_tr_b16 a[152:153], %[vr3] offset:7200\n\t"
    "v_mfma_f32_16x16x32_bf16 v[84:87], a[156:159], a[100:103], v[84:87]\n\t" "ds_read_b64_tr_b16 a[154:155], %[vr3] offset:10784\n\t"
    "v_mfma_f32_16x16x32_bf16 v[88:91], a[156:159], a[104:107], v[88:91]\n\t" "global_load_dwordx4 a[20:23], %[va], s[76:77] offset:1024 sc1\n\t"
    "v_mfma_f32_16x16x32_bf16 v[92:95], a[156:159], a[108:111], v[92:95]\n\t" "s_waitcnt lgkmcnt(12)\n\t" "v_mfma_f32_16x16x32_bf16 v[96:99], a[128:131], a[96:99], v[96:99]\n\t"
    "ds_read_b64_tr_b16 a[156:157], %[vr5] offset:7168\n\t" "v_mfma_f32_16x16x32_bf16 v[100:103], a[128:131], a[100:103], v[100:103]\n\t" "ds_read_b64_tr_b16 a[158:159], %[vr5] offset:10752\n\t"
    "v_mfma_f32_16x16x32_bf16 v[104:107], a[128:131], a[104:107], v[104:107]\n\t" "v_mfma_f32_16x16x32_bf16 v[108:111], a[128:131], a[108:111], v[108:111]\n\t" "s_waitcnt lgkmcnt(12)\n\t"
    "s_add_u32 m0, %[sl], 101376\n\t" "v_mfma_f32_16x16x32_bf16 v[112:115], a[132:135], a[96:99], v[112:115]\n\t" "global_load_lds_dwordx4 %[vb1], s[78:79]\n\t"
    "ds_read_b64_tr_b16 a[128:129], %[vr5] offset:7200\n\t" "v_mfma_f32_16x16x32_bf16 v[116:119], a[132:135], a[100:103], v[116:119]\n\t" "ds_read_b64_tr_b16 a[130:131], %[vr5] offset:10784\n\t"
    "v_mfma_f32_16x16x32_bf16 v[120:123], a[132:135], a[104:107], v[120:123]\n\t" "v_mfma_f32_16x16x32_bf16 v[124:127], a[132:135], a[108:111], v[124:127]\n\t" "s_load_dwordx2 s[98:99], s[84:85], 188\n\t"
    "s_waitcnt lgkmcnt(12)\n\t" "v_mfma_f32_16x16x32_bf16 v[128:131], a[136:139], a[96:99], v[128:131]\n\t" "ds_read_b64_tr_b16 a[132:133], %[vr7] offset:7168\n\t"
    "v_mfma_f32_16x16x32_bf16 v[132:135], a[136:139], a[100:103], v[132:135]\n\t" "ds_read_b64_tr_b16 a[134:135], %[vr7] offset:10752\n\t"
    "v_mfma_f32_16x16x32_bf16 v[136:139], a[136:139], a[104:107], v[136:139]\n\t" "global_load_dwordx4 a[24:27], %[va], s[76:77] offset:2048 sc1\n\t"
    "v_mfma_f32_16x16x32_bf16 v[140:143], a[136:139], a[108:111], v[140:143]\n\t" "s_waitcnt lgkmcnt(12)\n\t" "v_mfma_f32_16x16x32_bf16 v[144:147], a[140:143], a[96:99], v[144:147]\n\t"
    "s_waitcnt vmcnt(13)\n\t" "s_barrier\n\t" "ds_read_b64_tr_b16 a[136:137], %[vr1] offset:14336\n\t" "v_mfma_f32_16x16x32_bf16 v[148:151], a[140:143], a[100:103], v[148:151]\n\t"
    "ds_read_b64_tr_b16 a[138:139], %[vr1] offset:17920\n\t" "v_mfma_f32_16x16x32_bf16 v[152:155], a[140:143], a[104:107], v[152:155]\n\t"
    "v_mfma_f32_16x16x32_bf16 v[156:159], a[140:143], a[108:111], v[156:159]\n\t" "s_waitcnt lgkmcnt(12)\n\t" "s_add_u32 m0, %[sl], 102400\n\t"
    "v_mfma_f32_16x16x32_bf16 v[160:163], a[144:147], a[96:99], v[160:163]\n\t" "global_load_lds_dwordx4 %[vb2], s[78:79]\n\t" "ds_read_b64_tr_b16 a[140:141], %[vr1] offset:14368\n\t"
    "v_mfma_f32_16x16x32_bf16 v[164:167], a[144:147], a[100:103], v[164:167]\n\t" "ds_read_b64_tr_b16 a[142:143], %[vr1] offset:17952\n\t"
    "v_mfma_f32_16x16x32_bf16 v[168:171], a[144:147], a[104:107], v[168:171]\n\t" "v_mfma_f32_16x16x32_bf16 v[172:175], a[144:147], a[108:111], v[172:175]\n\t" "s_waitcnt lgkmcnt(12)\n\t"
    "v_mfma_f32_16x16x32_bf16 v[176:179], a[148:151], a[96:99], v[176:179]\n\t" "ds_read_b64_tr_b16 a[144:145], %[vr3] offset:14336\n\t"
    "v_mfma_f32_16x16x32_bf16 v[180:183], a[148:151], a[100:103], v[180:183]\n\t" "ds_read_b64_tr_b16 a[146:147], %[vr3] offset:17920\n\t"
    "v_mfma_f32_16x16x32_bf16 v[184:187], a[148:151], a[104:107], v[184:187]\n\t" "global_load_dwordx4 a[28:31], %[va], s[76:77] offset:3072 sc1\n\t" "s_add_u32 s76, s76, 4096\n\t"
    "s_addc_u32 s77, s77, 0\n\t" "v_mfma_f32_16x16x32_bf16 v[188:191], a[148:151], a[108:111], v[188:191]\n\t" "s_waitcnt lgkmcnt(12)\n\t"
    "v_mfma_f32_16x16x32_bf16 v[192:195], a[152:155], a[96:99], v[192:195]\n\t" "ds_read_b64_tr_b16 a[148:149], %[vr3] offset:14368\n\t"
    "v_mfma_f32_16x16x32_bf16 v[196:199], a[152:155], a[100:103], v[196:199]\n\t" "ds_read_b64_tr_b16 a[150:151], %[vr3] offset:17952\n\t" "s_load_dwordx2 s[98:99], s[84:85], 316\n\t"
    "s_add_u32 s84, s84, 384\n\t" "s_addc_u32 s85, s85, 0\n\t" "v_mfma_f32_16x16x32_bf16 v[200:203], a[152:155], a[104:107], v[200:203]\n\t"
    "v_mfma_f32_16x16x32_bf16 v[204:207], a[152:155], a[108:111], v[204:207]\n\t" "s_waitcnt lgkmcnt(12)\n\t" "s_add_u32 m0, %[sl], 103424\n\t"
    "v_mfma_f32_16x16x32_bf16 v[208:211], a[156:159], a[96:99], v[208:211]\n\t" "s_mov_b64 exec, s[82:83]\n\t" "global_load_lds_dwordx4 %[vb3], s[78:79]\n\t" "s_mov_b64 exec, -1\n\t"
    "s_add_u32 s78, s78, 229376\n\t" "s_addc_u32 s79, s79, 0\n\t" "ds_read_b64_tr_b16 a[152:153], %[vr5] offset:14336\n\t" "v_mfma_f32_16x16x32_bf16 v[212:215], a[156:159], a[100:103], v[212:215]\n\t"
    "ds_read_b64_tr_b16 a[154:155], %[vr5] offset:17920\n\t" "v_mfma_f32_16x16x32_bf16 v[216:219], a[156:159], a[104:107], v[216:219]\n\t"
    "v_mfma_f32_16x16x32_bf16 v[220:223], a[156:159], a[108:111], v[220:223]\n\t" "s_waitcnt lgkmcnt(12)\n\t" "v_mfma_f32_16x16x32_bf16 v[224:227], a[128:131], a[96:99], v[224:227]\n\t"
    "ds_read_b64_tr_b16 a[156:157], %[vr5] offset:14368\n\t" "v_mfma_f32_16x16x32_bf16 v[228:231], a[128:131], a[100:103], v[228:231]\n\t" "ds_read_b64_tr_b16 a[158:159], %[vr5] offset:17952\n\t"
    "v_mfma_f32_16x16x32_bf16 v[232:235], a[128:131], a[104:107], v[232:235]\n\t" "v_mfma_f32_16x16x32_bf16 v[236:239], a[128:131], a[108:111], v[236:239]\n\t" "s_waitcnt lgkmcnt(12)\n\t"
    "v_mfma_f32_16x16x32_bf16 v[240:243], a[132:135], a[96:99], v[240:243]\n\t" "ds_read_b64_tr_b16 a[128:129], %[vr7] offset:14336\n\t"
    "v_mfma_f32_16x16x32_bf16 v[244:247], a[132:135], a[100:103], v[244:247]\n\t" "ds_read_b64_tr_b16 a[130:131], %[vr7] offset:17920\n\t"
    "v_mfma_f32_16x16x32_bf16 v[248:251], a[132:135], a[104:107], v[248:251]\n\t" "v_mfma_f32_16x16x32_bf16 v[252:255], a[132:135], a[108:111], v[252:255]\n\t" "s_waitcnt lgkmcnt(12)\n\t"
    "v_mfma_f32_16x16x32_bf16 v[32:35], a[136:139], a[112:115], v[32:35]\n\t" "ds_read_b64_tr_b16 a[132:133], %[vr1] offset:21504\n\t"
    "v_mfma_f32_16x16x32_bf16 v[36:39], a[136:139], a[116:119], v[36:39]\n\t" "ds_read_b64_tr_b16 a[134:135], %[vr1] offset:25088\n\t"
    "v_mfma_f32_16x16x32_bf16 v[40:43], a[136:139], a[120:123], v[40:43]\n\t" "global_load_dwordx4 a[32:35], %[va], s[76:77] offset:0 sc1\n\t"
    "v_mfma_f32_16x16x32_bf16 v[44:47], a[136:139], a[124:127], v[44:47]\n\t" "s_waitcnt lgkmcnt(12)\n\t" "v_mfma_f32_16x16x32_bf16 v[48:51], a[140:143], a[112:115], v[48:51]\n\t"
    "ds_read_b64_tr_b16 a[136:137], %[vr1] offset:21536\n\t" "v_mfma_f32_16x16x32_bf16 v[52:55], a[140:143], a[116:119], v[52:55]\n\t" "ds_read_b64_tr_b16 a[138:139], %[vr1] offset:25120\n\t"
    "s_load_dwordx2 s[98:99], s[84:85], 60\n\t" "v_mfma_f32_16x16x32_bf16 v[56:59], a[140:143], a[120:123], v[56:59]\n\t" "v_mfma_f32_16x16x32_bf16 v[60:63], a[140:143], a[124:127], v[60:63]\n\t"
    "s_waitcnt lgkmcnt(12)\n\t" "s_add_u32 m0, %[sl], 0\n\t" "v_mfma_f32_16x16x32_bf16 v[64:67], a[144:147], a[112:115], v[64:67]\n\t" "global_load_lds_dwordx4 %[vb0], s[78:79]\n\t"
    "ds_read_b64_tr_b16 a[140:141], %[vr3] offset:21504\n\t" "v_mfma_f32_16x16x32_bf16 v[68:71], a[144:147], a[116:119], v[68:71]\n\t" "ds_read_b64_tr_b16 a[142:143], %[vr3] offset:25088\n\t"
    "v_mfma_f32_16x16x32_bf16 v[72:75], a[144:147], a[120:123], v[72:75]\n\t" "v_mfma_f32_16x16x32_bf16 v[76:79], a[144:147], a[124:127], v[76:79]\n\t" "s_waitcnt lgkmcnt(12)\n\t"
    "v_mfma_f32_16x16x32_bf16 v[80:83], a[148:151], a[112:115], v[80:83]\n\t" "ds_read_b64_tr_b16 a[144:145], %[vr3] offset:21536\n\t"
    "v_mfma_f32_16x16x32_bf16 v[84:87], a[148:151], a[116:119], v[84:87]\n\t" "ds_read_b64_tr_b16 a[146:147], %[vr3] offset:25120\n\t"
    "v_mfma_f32_16x16x32_bf16 v[88:91], a[148:151], a[120:123], v[88:91]\n\t" "global_load_dwordx4 a[36:39], %[va], s[76:77] offset:1024 sc1\n\t"
    "v_mfma_f32_16x16x32_bf16 v[92:95], a[148:151], a[124:127], v[92:95]\n\t" "s_waitcnt lgkmcnt(12)\n\t" "v_mfma_f32_16x16x32_bf16 v[96:99], a[152:155], a[112:115], v[96:99]\n\t"
    "ds_read_b64_tr_b16 a[148:149], %[vr5] offset:21504\n\t" "v_mfma_f32_16x16x32_bf16 v[100:103], a[152:155], a[116:119], v[100:103]\n\t" "ds_read_b64_tr_b16 a[150:151], %[vr5] offset:25088\n\t"
    "v_mfma_f32_16x16x32_bf16 v[104:107], a[152:155], a[120:123], v[104:107]\n\t" "v_mfma_f32_16x16x32_bf16 v[108:111], a[152:155], a[124:127], v[108:111]\n\t" "s_waitcnt lgkmcnt(12)\n\t"
    "s_add_u32 m0, %[sl], 1024\n\t" "v_mfma_f32_16x16x32_bf16 v[112:115], a[156:159], a[112:115], v[112:115]\n\t" "global_load_lds_dwordx4 %[vb1], s[78:79]\n\t"
    "ds_read_b64_tr_b16 a[152:153], %[vr5] offset:21536\n\t" "v_mfma_f32_16x16x32_bf16 v[116:119], a[156:159], a[116:119], v[116:119]\n\t" "ds_read_b64_tr_b16 a[154:155], %[vr5] offset:25120\n\t"
    "v_mfma_f32_16x16x32_bf16 v[120:123], a[156:159], a[120:123], v[120:123]\n\t" "v_mfma_f32_16x16x32_bf16 v[124:127], a[156:159], a[124:127], v[124:127]\n\t" "s_load_dwordx2 s[98:99], s[84:85], 188\n\t"
    "s_waitcnt lgkmcnt(12)\n\t" "v_mfma_f32_16x16x32_bf16 v[128:131], a[128:131], a[112:115], v[128:131]\n\t" "ds_read_b64_tr_b16 a[156:157], %[vr7] offset:21504\n\t"
    "v_mfma_f32_16x16x32_bf16 v[132:135], a[128:131], a[116:119], v[132:135]\n\t" "ds_read_b64_tr_b16 a[158:159], %[vr7] offset:25088\n\t"
    "v_mfma_f32_16x16x32_bf16 v[136:139], a[128:131], a[120:123], v[136:139]\n\t" "global_load_dwordx4 a[40:43], %[va], s[76:77] offset:2048 sc1\n\t"
    "v_mfma_f32_16x16x32_bf16 v[140:143], a[128:131], a[124:127], v[140:143]\n\t" "s_waitcnt lgkmcnt(12)\n\t" "v_mfma_f32_16x16x32_bf16 v[144:147], a[132:135], a[112:115], v[144:147]\n\t"
    "s_waitcnt vmcnt(13)\n\t" "s_barrier\n\t" "ds_read_b64_tr_b16 a[128:129], %[vr1] offset:28672\n\t" "v_mfma_f32_16x16x32_bf16 v[148:151], a[132:135], a[116:119], v[148:151]\n\t"
    "ds_read_b64_tr_b16 a[130:131], %[vr1] offset:32256\n\t" "v_mfma_f32_16x16x32_bf16 v[152:155], a[132:135], a[120:123], v[152:155]\n\t"
    "v_mfma_f32_16x16x32_bf16 v[156:159], a[132:135], a[124:127], v[156:159]\n\t" "s_waitcnt lgkmcnt(12)\n\t" "s_add_u32 m0, %[sl], 2048\n\t"
    "v_mfma_f32_16x16x32_bf16 v[160:163], a[136:139], a[112:115], v[160:163]\n\t" "global_load_lds_dwordx4 %[vb2], s[78:79]\n\t" "ds_read_b64_tr_b16 a[132:133], %[vr1] offset:28704\n\t"
    "v_mfma_f32_16x16x32_bf16 v[164:167], a[136:139], a[116:119], v[164:167]\n\t" "ds_read_b64_tr_b16 a[134:135], %[vr1] offset:32288\n\t"
    "v_mfma_f32_16x16x32_bf16 v[168:171], a[136:139], a[120:123], v[168:171]\n\t" "v_mfma_f32_16x16x32_bf16 v[172:175], a[136:139], a[124:127], v[172:175]\n\t" "s_waitcnt lgkmcnt(12)\n\t"
    "v_mfma_f32_16x16x32_bf16 v[176:179], a[140:143], a[112:115], v[176:179]\n\t" "ds_read_b64_tr_b16 a[136:137], %[vr3] offset:28672\n\t"
    "v_mfma_f32_16x16x32_bf16 v[180:183], a[140:143], a[116:119], v[180:183]\n\t" "ds_read_b64_tr_b16 a[138:139], %[vr3] offset:32256\n\t"
    "v_mfma_f32_16x16x32_bf16 v[184:187], a[140:143], a[120:123], v[184:187]\n\t" "global_load_dwordx4 a[44:47], %[va], s[76:77] offset:3072 sc1\n\t" "s_add_u32 s76, s76, 4096\n\t"
    "s_addc_u32 s77, s77, 0\n\t" "v_mfma_f32_16x16x32_bf16 v[188:191], a[140:143], a[124:127], v[188:191]\n\t" "s_waitcnt lgkmcnt(12)\n\t"
    "v_mfma_f32_16x16x32_bf16 v[192:195], a[144:147], a[112:115], v[192:195]\n\t" "ds_read_b64_tr_b16 a[140:141], %[vr3] offset:28704\n\t"
    "v_mfma_f32_16x16x32_bf16 v[196:199], a[144:147], a[116:119], v[196:199]\n\t" "ds_read_b64_tr_b16 a[142:143], %[vr3] offset:32288\n\t" "s_load_dwordx2 s[98:99], s[84:85], 316\n\t"
    "s_add_u32 s84, s84, 384\n\t" "s_addc_u32 s85, s85, 0\n\t" "v_mfma_f32_16x16x32_bf16 v[200:203], a[144:147], a[120:123], v[200:203]\n\t"
    "v_mfma_f32_16x16x32_bf16 v[204:207], a[144:147], a[124:127], v[204:207]\n\t" "s_waitcnt lgkmcnt(12)\n\t" "s_add_u32 m0, %[sl], 3072\n\t"
    "v_mfma_f32_16x16x32_bf16 v[208:211], a[148:151], a[112:115], v[208:211]\n\t" "s_mov_b64 exec, s[82:83]\n\t" "global_load_lds_dwordx4 %[vb3], s[78:79]\n\t" "s_mov_b64 exec, -1\n\t"
    "s_add_u32 s78, s78, 229376\n\t" "s_addc_u32 s79, s79, 0\n\t" "ds_read_b64_tr_b16 a[144:145], %[vr5] offset:28672\n\t" "v_mfma_f32_16x16x32_bf16 v[212:215], a[148:151], a[116:119], v[212:215]\n\t"
    "ds_read_b64_tr_b16 a[146:147], %[vr5] offset:32256\n\t" "v_mfma_f32_16x16x32_bf16 v[216:219], a[148:151], a[120:123], v[216:219]\n\t"
    "v_mfma_f32_16x16x32_bf16 v[220:223], a[148:151], a[124:127], v[220:223]\n\t" "s_waitcnt lgkmcnt(12)\n\t" "v_mfma_f32_16x16x32_bf16 v[224:227], a[152:155], a[112:115], v[224:227]\n\t"
    "ds_read_b64_tr_b16 a[148:149], %[vr5] offset:28704\n\t" "v_mfma_f32_16x16x32_bf16 v[228:231], a[152:155], a[116:119], v[228:231]\n\t" "ds_read_b64_tr_b16 a[150:151], %[vr5] offset:32288\n\t"
    "v_mfma_f32_16x16x32_bf16 v[232:235], a[152:155], a[120:123], v[232:235]\n\t" "v_mfma_f32_16x16x32_bf16 v[236:239], a[152:155], a[124:127], v[236:239]\n\t" "s_waitcnt lgkmcnt(12)\n\t"
    "v_mfma_f32_16x16x32_bf16 v[240:243], a[156:159], a[112:115], v[240:243]\n\t" "ds_read_b64_tr_b16 a[152:153], %[vr7] offset:28672\n\t"
    "v_mfma_f32_16x16x32_bf16 v[244:247], a[156:159], a[116:119], v[244:247]\n\t" "ds_read_b64_tr_b16 a[154:155], %[vr7] offset:32256\n\t"
    "v_mfma_f32_16x16x32_bf16 v[248:251], a[156:159], a[120:123], v[248:251]\n\t" "v_mfma_f32_16x16x32_bf16 v[252:255], a[156:159], a[124:127], v[252:255]\n\t" ".Ld1_%=:\n\t" "s_cmp_eq_u32 s80, %[e0]\n\t"
    "s_cbranch_scc1 .LE0_%=\n\t" "s_waitcnt lgkmcnt(12)\n\t" "v_mfma_f32_16x16x32_bf16 v[32:35], a[128:131], a[0:3], v[32:35]\n\t" "ds_read_b64_tr_b16 a[156:157], %[vr1] offset:35840\n\t"
    "v_mfma_f32_16x16x32_bf16 v[36:39], a[128:131], a[4:7], v[36:39]\n\t" "ds_read_b64_tr_b16 a[158:159], %[vr1] offset:39424\n\t" "v_mfma_f32_16x16x32_bf16 v[40:43], a[128:131], a[8:11], v[40:43]\n\t"
    "global_load_dwordx4 a[48:51], %[va], s[76:77] offset:0 sc1\n\t" "v_mfma_f32_16x16x32_bf16 v[44:47], a[128:131], a[12:15], v[44:47]\n\t" "s_waitcnt lgkmcnt(12)\n\t"
    "v_mfma_f32_16x16x32_bf16 v[48:51], a[132:135], a[0:3], v[48:51]\n\t" "ds_read_b64_tr_b16 a[128:129], %[vr1] offset:35872\n\t" "v_mfma_f32_16x16x32_bf16 v[52:55], a[132:135], a[4:7], v[52:55]\n\t"
    "ds_read_b64_tr_b16 a[130:131], %[vr1] offset:39456\n\t" "s_load_dwordx2 s[98:99], s[84:85], 60\n\t" "v_mfma_f32_16x16x32_bf16 v[56:59], a[132:135], a[8:11], v[56:59]\n\t"
    "v_mfma_f32_16x16x32_bf16 v[60:63], a[132:135], a[12:15], v[60:63]\n\t" "s_waitcnt lgkmcnt(12)\n\t" "s_add_u32 m0, %[sl], 14336\n\t"
    "v_mfma_f32_16x16x32_bf16 v[64:67], a[136:139], a[0:3], v[64:67]\n\t" "global_load_lds_dwordx4 %[vb0], s[78:79]\n\t" "ds_read_b64_tr_b16 a[132:133], %[vr3] offset:35840\n\t"
    "v_mfma_f32_16x16x32_bf16 v[68:71], a[136:139], a[4:7], v[68:71]\n\t" "ds_read_b64_tr_b16 a[134:135], %[vr3] offset:39424\n\t" "v_mfma_f32_16x16x32_bf16 v[72:75], a[136:139], a[8:11], v[72:75]\n\t"
    "v_mfma_f32_16x16x32_bf16 v[76:79], a[136:139], a[12:15], v[76:79]\n\t" "s_waitcnt lgkmcnt(12)\n\t" "v_mfma_f32_16x16x32_bf16 v[80:83], a[140:143], a[0:3], v[80:83]\n\t"
    "ds_read_b64_tr_b16 a[136:137], %[vr3] offset:35872\n\t" "v_mfma_f32_16x16x32_bf16 v[84:87], a[140:143], a[4:7], v[84:87]\n\t" "ds_read_b64_tr_b16 a[138:139], %[vr3] offset:39456\n\t"
    "v_mfma_f32_16x16x32_bf16 v[88:91], a[140:143], a[8:11], v[88:91]\n\t" "global_load_dwordx4 a[52:55], %[va], s[76:77] offset:1024 sc1\n\t"
    "v_mfma_f32_16x16x32_bf16 v[92:95], a[140:143], a[12:15], v[92:95]\n\t" "s_waitcnt lgkmcnt(12)\n\t" "v_mfma_f32_16x16x32_bf16 v[96:99], a[144:147], a[0:3], v[96:99]\n\t"
    "ds_read_b64_tr_b16 a[140:141], %[vr5] offset:35840\n\t" "v_mfma_f32_16x16x32_bf16 v[100:103], a[144:147], a[4:7], v[100:103]\n\t" "ds_read_b64_tr_b16 a[142:143], %[vr5] offset:39424\n\t"
    "v_mfma_f32_16x16x32_bf16 v[104:107], a[144:147], a[8:11], v[104:107]\n\t" "v_mfma_f32_16x16x32_bf16 v[108:111], a[144:147], a[12:15], v[108:111]\n\t" "s_waitcnt lgkmcnt(12)\n\t"
    "s_add_u32 m0, %[sl], 15360\n\t" "v_mfma_f32_16x16x32_bf16 v[112:115], a[148:151], a[0:3], v[112:115]\n\t" "global_load_lds_dwordx4 %[vb1], s[78:79]\n\t"
    "ds_read_b64_tr_b16 a[144:145], %[vr5] offset:35872\n\t" "v_mfma_f32_16x16x32_bf16 v[116:119], a[148:151], a[4:7], v[116:119]\n\t" "ds_read_b64_tr_b16 a[146:147], %[vr5] offset:39456\n\t"
    "v_mfma_f32_16x16x32_bf16 v[120:123], a[148:151], a[8:11], v[120:123]\n\t" "v_mfma_f32_16x16x32_bf16 v[124:127], a[148:151], a[12:15], v[124:127]\n\t" "s_load_dwordx2 s[98:99], s[84:85], 188\n\t"
    "s_waitcnt lgkmcnt(12)\n\t" "v_mfma_f32_16x16x32_bf16 v[128:131], a[152:155], a[0:3], v[128:131]\n\t" "ds_read_b64_tr_b16 a[148:149], %[vr7] offset:35840\n\t"
    "v_mfma_f32_16x16x32_bf16 v[132:135], a[152:155], a[4:7], v[132:135]\n\t" "ds_read_b64_tr_b16 a[150:151], %[vr7] offset:39424\n\t"
    "v_mfma_f32_16x16x32_bf16 v[136:139], a[152:155], a[8:11], v[136:139]\n\t" "global_load_dwordx4 a[56:59], %[va], s[76:77] offset:2048 sc1\n\t"
    "v_mfma_f32_16x16x32_bf16 v[140:143], a[152:155], a[12:15], v[140:143]\n\t" "s_waitcnt lgkmcnt(12)\n\t" "v_mfma_f32_16x16x32_bf16 v[144:147], a[156:159], a[0:3], v[144:147]\n\t"
    "s_waitcnt vmcnt(13)\n\t" "s_barrier\n\t" "ds_read_b64_tr_b16 a[152:153], %[vr1] offset:43008\n\t" "v_mfma_f32_16x16x32_bf16 v[148:151], a[156:159], a[4:7], v[148:151]\n\t"
    "ds_read_b64_tr_b16 a[154:155], %[vr1] offset:46592\n\t" "v_mfma_f32_16x16x32_bf16 v[152:155], a[156:159], a[8:11], v[152:155]\n\t"
    "v_mfma_f32_16x16x32_bf16 v[156:159], a[156:159], a[12:15], v[156:159]\n\t" "s_waitcnt lgkmcnt(12)\n\t" "s_add_u32 m0, %[sl], 16384\n\t"
    "v_mfma_f32_16x16x32_bf16 v[160:163], a[128:131], a[0:3], v[160:163]\n\t" "global_load_lds_dwordx4 %[vb2], s[78:79]\n\t" "ds_read_b64_tr_b16 a[156:157], %[vr1] offset:43040\n\t"
    "v_mfma_f32_16x16x32_bf16 v[164:167], a[128:131], a[4:7], v[164:167]\n\t" "ds_read_b64_tr_b16 a[158:159], %[vr1] offset:46624\n\t"
    "v_mfma_f32_16x16x32_bf16 v[168:171], a[128:131], a[8:11], v[168:171]\n\t" "v_mfma_f32_16x16x32_bf16 v[172:175], a[128:131], a[12:15], v[172:175]\n\t" "s_waitcnt lgkmcnt(12)\n\t"
    "v_mfma_f32_16x16x32_bf16 v[176:179], a[132:135], a[0:3], v[176:179]\n\t" "ds_read_b64_tr_b16 a[128:129], %[vr3] offset:43008\n\t"
    "v_mfma_f32_16x16x32_bf16 v[180:183], a[132:135], a[4:7], v[180:183]\n\t" "ds_read_b64_tr_b16 a[130:131], %[vr3] offset:46592\n\t"
    "v_mfma_f32_16x16x32_bf16 v[184:187], a[132:135], a[8:11], v[184:187]\n\t" "global_load_dwordx4 a[60:63], %[va], s[76:77] offset:3072 sc1\n\t" "s_add_u32 s76, s76, 4096\n\t"
    "s_addc_u32 s77, s77, 0\n\t" "v_mfma_f32_16x16x32_bf16 v[188:191], a[132:135], a[12:15], v[188:191]\n\t" "s_waitcnt lgkmcnt(12)\n\t"
    "v_mfma_f32_16x16x32_bf16 v[192:195], a[136:139], a[0:3], v[192:195]\n\t" "ds_read_b64_tr_b16 a[132:133], %[vr3] offset:43040\n\t"
    "v_mfma_f32_16x16x32_bf16 v[196:199], a[136:139], a[4:7], v[196:199]\n\t" "ds_read_b64_tr_b16 a[134:135], %[vr3] offset:46624\n\t" "s_load_dwordx2 s[98:99], s[84:85], 316\n\t"
    "s_add_u32 s84, s84, 384\n\t" "s_addc_u32 s85, s85, 0\n\t" "v_mfma_f32_16x16x32_bf16 v[200:203], a[136:139], a[8:11], v[200:203]\n\t"
    "v_mfma_f32_16x16x32_bf16 v[204:207], a[136:139], a[12:15], v[204:207]\n\t" "s_waitcnt lgkmcnt(12)\n\t" "s_add_u32 m0, %[sl], 17408\n\t"
    "v_mfma_f32_16x16x32_bf16 v[208:211], a[140:143], a[0:3], v[208:211]\n\t" "s_mov_b64 exec, s[82:83]\n\t" "global_load_lds_dwordx4 %[vb3], s[78:79]\n\t" "s_mov_b64 exec, -1\n\t"
    "s_add_u32 s78, s78, 229376\n\t" "s_addc_u32 s79, s79, 0\n\t" "ds_read_b64_tr_b16 a[136:137], %[vr5] offset:43008\n\t" "v_mfma_f32_16x16x32_bf16 v[212:215], a[140:143], a[4:7], v[212:215]\n\t"
    "ds_read_b64_tr_b16 a[138:139], %[vr5] offset:46592\n\t" "v_mfma_f32_16x16x32_bf16 v[216:219], a[140:143], a[8:11], v[216:219]\n\t"
    "v_mfma_f32_16x16x32_bf16 v[220:223], a[140:143], a[12:15], v[220:223]\n\t" "s_waitcnt lgkmcnt(12)\n\t" "v_mfma_f32_16x16x32_bf16 v[224:227], a[144:147], a[0:3], v[224:227]\n\t"
    "ds_read_b64_tr_b16 a[140:141], %[vr5] offset:43040\n\t" "v_mfma_f32_16x16x32_bf16 v[228:231], a[144:147], a[4:7], v[228:231]\n\t" "ds_read_b64_tr_b16 a[142:143], %[vr5] offset:46624\n\t"
    "v_mfma_f32_16x16x32_bf16 v[232:235], a[144:147], a[8:11], v[232:235]\n\t" "v_mfma_f32_16x16x32_bf16 v[236:239], a[144:147], a[12:15], v[236:239]\n\t" "s_waitcnt lgkmcnt(12)\n\t"
    "v_mfma_f32_16x16x32_bf16 v[240:243], a[148:151], a[0:3], v[240:243]\n\t" "ds_read_b64_tr_b16 a[144:145], %[vr7] offset:43008\n\t"
    "v_mfma_f32_16x16x32_bf16 v[244:247], a[148:151], a[4:7], v[244:247]\n\t" "ds_read_b64_tr_b16 a[146:147], %[vr7] offset:46592\n\t"
    "v_mfma_f32_16x16x32_bf16 v[248:251], a[148:151], a[8:11], v[248:251]\n\t" "v_mfma_f32_16x16x32_bf16 v[252:255], a[148:151], a[12:15], v[252:255]\n\t" "s_waitcnt lgkmcnt(12)\n\t"
    "v_mfma_f32_16x16x32_bf16 v[32:35], a[152:155], a[16:19], v[32:35]\n\t" "ds_read_b64_tr_b16 a[148:149], %[vr1] offset:50176\n\t" "v_mfma_f32_16x16x32_bf16 v[36:39], a[152:155], a[20:23], v[36:39]\n\t"
    "ds_read_b64_tr_b16 a[150:151], %[vr1] offset:53760\n\t" "v_mfma_f32_16x16x32_bf16 v[40:43], a[152:155], a[24:27], v[40:43]\n\t" "global_load_dwordx4 a[64:67], %[va], s[76:77] offset:0 sc1\n\t"
    "v_mfma_f32_16x16x32_bf16 v[44:47], a[152:155], a[28:31], v[44:47]\n\t" "s_waitcnt lgkmcnt(12)\n\t" "v_mfma_f32_16x16x32_bf16 v[48:51], a[156:159], a[16:19], v[48:51]\n\t"
    "ds_read_b64_tr_b16 a[152:153], %[vr1] offset:50208\n\t" "v_mfma_f32_16x16x32_bf16 v[52:55], a[156:159], a[20:23], v[52:55]\n\t" "ds_read_b64_tr_b16 a[154:155], %[vr1] offset:53792\n\t"
    "s_load_dwordx2 s[98:99], s[84:85], 60\n\t" "v_mfma_f32_16x16x32_bf16 v[56:59], a[156:159], a[24:27], v[56:59]\n\t" "v_mfma_f32_16x16x32_bf16 v[60:63], a[156:159], a[28:31], v[60:63]\n\t"
    "s_waitcnt lgkmcnt(12)\n\t" "s_add_u32 m0, %[sl], 28672\n\t" "v_mfma_f32_16x16x32_bf16 v[64:67], a[128:131], a[16:19], v[64:67]\n\t" "global_load_lds_dwordx4 %[vb0], s[78:79]\n\t"
    "ds_read_b64_tr_b16 a[156:157], %[vr3] offset:50176\n\t" "v_mfma_f32_16x16x32_bf16 v[68:71], a[128:131], a[20:23], v[68:71]\n\t" "ds_read_b64_tr_b16 a[158:159], %[vr3] offset:53760\n\t"
    "v_mfma_f32_16x16x32_bf16 v[72:75], a[128:131], a[24:27], v[72:75]\n\t" "v_mfma_f32_16x16x32_bf16 v[76:79], a[128:131], a[28:31], v[76:79]\n\t" "s_waitcnt lgkmcnt(12)\n\t"
    "v_mfma_f32_16x16x32_bf16 v[80:83], a[132:135], a[16:19], v[80:83]\n\t" "ds_read_b64_tr_b16 a[128:129], %[vr3] offset:50208\n\t" "v_mfma_f32_16x16x32_bf16 v[84:87], a[132:135], a[20:23], v[84:87]\n\t"
    "ds_read_b64_tr_b16 a[130:131], %[vr3] offset:53792\n\t" "v_mfma_f32_16x16x32_bf16 v[88:91], a[132:135], a[24:27], v[88:91]\n\t" "global_load_dwordx4 a[68:71], %[va], s[76:77] offset:1024 sc1\n\t"
    "v_mfma_f32_16x16x32_bf16 v[92:95], a[132:135], a[28:31], v[92:95]\n\t" "s_waitcnt lgkmcnt(12)\n\t" "v_mfma_f32_16x16x32_bf16 v[96:99], a[136:139], a[16:19], v[96:99]\n\t"
    "ds_read_b64_tr_b16 a[132:133], %[vr5] offset:50176\n\t" "v_mfma_f32_16x16x32_bf16 v[100:103], a[136:139], a[20:23], v[100:103]\n\t" "ds_read_b64_tr_b16 a[134:135], %[vr5] offset:53760\n\t"
    "v_mfma_f32_16x16x32_bf16 v[104:107], a[136:139], a[24:27], v[104:107]\n\t" "v_mfma_f32_16x16x32_bf16 v[108:111], a[136:139], a[28:31], v[108:111]\n\t" "s_waitcnt lgkmcnt(12)\n\t"
    "s_add_u32 m0, %[sl], 29696\n\t" "v_mfma_f32_16x16x32_bf16 v[112:115], a[140:143], a[16:19], v[112:115]\n\t" "global_load_lds_dwordx4 %[vb1], s[78:79]\n\t"
    "ds_read_b64_tr_b16 a[136:137], %[vr5] offset:50208\n\t" "v_mfma_f32_16x16x32_bf16 v[116:119], a[140:143], a[20:23], v[116:119]\n\t" "ds_read_b64_tr_b16 a[138:139], %[vr5] offset:53792\n\t"
    "v_mfma_f32_16x16x32_bf16 v[120:123], a[140:143], a[24:27], v[120:123]\n\t" "v_mfma_f32_16x16x32_bf16 v[124:127], a[140:143], a[28:31], v[124:127]\n\t" "s_load_dwordx2 s[98:99], s[84:85], 188\n\t"
    "s_waitcnt lgkmcnt(12)\n\t" "v_mfma_f32_16x16x32_bf16 v[128:131], a[144:147], a[16:19], v[128:131]\n\t" "ds_read_b64_tr_b16 a[140:141], %[vr7] offset:50176\n\t"
    "v_mfma_f32_16x16x32_bf16 v[132:135], a[144:147], a[20:23], v[132:135]\n\t" "ds_read_b64_tr_b16 a[142:143], %[vr7] offset:53760\n\t"
    "v_mfma_f32_16x16x32_bf16 v[136:139], a[144:147], a[24:27], v[136:139]\n\t" "global_load_dwordx4 a[72:75], %[va], s[76:77] offset:2048 sc1\n\t"
    "v_mfma_f32_16x16x32_bf16 v[140:143], a[144:147], a[28:31], v[140:143]\n\t" "s_waitcnt lgkmcnt(12)\n\t" "v_mfma_f32_16x16x32_bf16 v[144:147], a[148:151], a[16:19], v[144:147]\n\t"
    "s_waitcnt vmcnt(13)\n\t" "s_barrier\n\t" "ds_read_b64_tr_b16 a[144:145], %[vr0] offset:0\n\t" "v_mfma_f32_16x16x32_bf16 v[148:151], a[148:151], a[20:23], v[148:151]\n\t"
    "ds_read_b64_tr_b16 a[146:147], %[vr0] offset:3584\n\t" "v_mfma_f32_16x16x32_bf16 v[152:155], a[148:151], a[24:27], v[152:155]\n\t"
    "v_mfma_f32_16x16x32_bf16 v[156:159], a[148:151], a[28:31], v[156:159]\n\t" "s_waitcnt lgkmcnt(12)\n\t" "s_add_u32 m0, %[sl], 30720\n\t"
    "v_mfma_f32_16x16x32_bf16 v[160:163], a[152:155], a[16:19], v[160:163]\n\t" "global_load_lds_dwordx4 %[vb2], s[78:79]\n\t" "ds_read_b64_tr_b16 a[148:149], %[vr0] offset:32\n\t"
    "v_mfma_f32_16x16x32_bf16 v[164:167], a[152:155], a[20:23], v[164:167]\n\t" "ds_read_b64_tr_b16 a[150:151], %[vr0] offset:3616\n\t"
    "v_mfma_f32_16x16x32_bf16 v[168:171], a[152:155], a[24:27], v[168:171]\n\t" "v_mfma_f32_16x16x32_bf16 v[172:175], a[152:155], a[28:31], v[172:175]\n\t" "s_waitcnt lgkmcnt(12)\n\t"
    "v_mfma_f32_16x16x32_bf16 v[176:179], a[156:159], a[16:19], v[176:179]\n\t" "ds_read_b64_tr_b16 a[152:153], %[vr2] offset:0\n\t"
    "v_mfma_f32_16x16x32_bf16 v[180:183], a[156:159], a[20:23], v[180:183]\n\t" "ds_read_b64_tr_b16 a[154:155], %[vr2] offset:3584\n\t"
    "v_mfma_f32_16x16x32_bf16 v[184:187], a[156:159], a[24:27], v[184:187]\n\t" "global_load_dwordx4 a[76:79], %[va], s[76:77] offset:3072 sc1\n\t" "s_add_u32 s76, s76, 4096\n\t"
    "s_addc_u32 s77, s77, 0\n\t" "v_mfma_f32_16x16x32_bf16 v[188:191], a[156:159], a[28:31], v[188:191]\n\t" "s_waitcnt lgkmcnt(12)\n\t"
    "v_mfma_f32_16x16x32_bf16 v[192:195], a[128:131], a[16:19], v[192:195]\n\t" "ds_read_b64_tr_b16 a[156:157], %[vr2] offset:32\n\t"
    "v_mfma_f32_16x16x32_bf16 v[196:199], a[128:131], a[20:23], v[196:199]\n\t" "ds_read_b64_tr_b16 a[158:159], %[vr2] offset:3616\n\t" "s_load_dwordx2 s[98:99], s[84:85], 316\n\t"
    "s_add_u32 s84, s84, 384\n\t" "s_addc_u32 s85, s85, 0\n\t" "v_mfma_f32_16x16x32_bf16 v[200:203], a[128:131], a[24:27], v[200:203]\n\t"
    "v_mfma_f32_16x16x32_bf16 v[204:207], a[128:131], a[28:31], v[204:207]\n\t" "s_waitcnt lgkmcnt(12)\n\t" "s_add_u32 m0, %[sl], 31744\n\t"
    "v_mfma_f32_16x16x32_bf16 v[208:211], a[132:135], a[16:19], v[208:211]\n\t" "s_mov_b64 exec, s[82:83]\n\t" "global_load_lds_dwordx4 %[vb3], s[78:79]\n\t" "s_mov_b64 exec, -1\n\t"
    "s_add_u32 s78, s78, 229376\n\t" "s_addc_u32 s79, s79, 0\n\t" "ds_read_b64_tr_b16 a[128:129], %[vr4] offset:0\n\t" "v_mfma_f32_16x16x32_bf16 v[212:215], a[132:135], a[20:23], v[212:215]\n\t"
    "ds_read_b64_tr_b16 a[130:131], %[vr4] offset:3584\n\t" "v_mfma_f32_16x16x32_bf16 v[216:219], a[132:135], a[24:27], v[216:219]\n\t"
    "v_mfma_f32_16x16x32_bf16 v[220:223], a[132:135], a[28:31], v[220:223]\n\t" "s_waitcnt lgkmcnt(12)\n\t" "v_mfma_f32_16x16x32_bf16 v[224:227], a[136:139], a[16:19], v[224:227]\n\t"
    "ds_read_b64_tr_b16 a[132:133], %[vr4] offset:32\n\t" "v_mfma_f32_16x16x32_bf16 v[228:231], a[136:139], a[20:23], v[228:231]\n\t" "ds_read_b64_tr_b16 a[134:135], %[vr4] offset:3616\n\t"
    "v_mfma_f32_16x16x32_bf16 v[232:235], a[136:139], a[24:27], v[232:235]\n\t" "v_mfma_f32_16x16x32_bf16 v[236:239], a[136:139], a[28:31], v[236:239]\n\t" "s_waitcnt lgkmcnt(12)\n\t"
    "v_mfma_f32_16x16x32_bf16 v[240:243], a[140:143], a[16:19], v[240:243]\n\t" "ds_read_b64_tr_b16 a[136:137], %[vr6] offset:0\n\t"
    "v_mfma_f32_16x16x32_bf16 v[244:247], a[140:143], a[20:23], v[244:247]\n\t" "ds_read_b64_tr_b16 a[138:139], %[vr6] offset:3584\n\t"
    "v_mfma_f32_16x16x32_bf16 v[248:251], a[140:143], a[24:27], v[248:251]\n\t" "v_mfma_f32_16x16x32_bf16 v[252:255], a[140:143], a[28:31], v[252:255]\n\t" "s_waitcnt lgkmcnt(12)\n\t"
    "v_mfma_f32_16x16x32_bf16 v[32:35], a[144:147], a[32:35], v[32:35]\n\t" "ds_read_b64_tr_b16 a[140:141], %[vr0] offset:7168\n\t" "v_mfma_f32_16x16x32_bf16 v[36:39], a[144:147], a[36:39], v[36:39]\n\t"
    "ds_read_b64_tr_b16 a[142:143], %[vr0] offset:10752\n\t" "v_mfma_f32_16x16x32_bf16 v[40:43], a[144:147], a[40:43], v[40:43]\n\t" "global_load_dwordx4 a[80:83], %[va], s[76:77] offset:0 sc1\n\t"
    "v_mfma_f32_16x16x32_bf16 v[44:47], a[144:147], a[44:47], v[44:47]\n\t" "s_waitcnt lgkmcnt(12)\n\t" "v_mfma_f32_16x16x32_bf16 v[48:51], a[148:151], a[32:35], v[48:51]\n\t"
    "ds_read_b64_tr_b16 a[144:145], %[vr0] offset:7200\n\t" "v_mfma_f32_16x16x32_bf16 v[52:55], a[148:151], a[36:39], v[52:55]\n\t" "ds_read_b64_tr_b16 a[146:147], %[vr0] offset:10784\n\t"
    "s_load_dwordx2 s[98:99], s[84:85], 60\n\t" "v_mfma_f32_16x16x32_bf16 v[56:59], a[148:151], a[40:43], v[56:59]\n\t" "v_mfma_f32_16x16x32_bf16 v[60:63], a[148:151], a[44:47], v[60:63]\n\t"
    "s_waitcnt lgkmcnt(12)\n\t" "s_add_u32 m0, %[sl], 43008\n\t" "v_mfma_f32_16x16x32_bf16 v[64:67], a[152:155], a[32:35], v[64:67]\n\t" "global_load_lds_dwordx4 %[vb0], s[78:79]\n\t"
    "ds_read_b64_tr_b16 a[148:149], %[vr2] offset:7168\n\t" "v_mfma_f32_16x16x32_bf16 v[68:71], a[152:155], a[36:39], v[68:71]\n\t" "ds_read_b64_tr_b16 a[150:151], %[vr2] offset:10752\n\t"
    "v_mfma_f32_16x16x32_bf16 v[72:75], a[152:155], a[40:43], v[72:75]\n\t" "v_mfma_f32_16x16x32_bf16 v[76:79], a[152:155], a[44:47], v[76:79]\n\t" "s_waitcnt lgkmcnt(12)\n\t"
    "v_mfma_f32_16x16x32_bf16 v[80:83], a[156:159], a[32:35], v[80:83]\n\t" "ds_read_b64_tr_b16 a[152:153], %[vr2] offset:7200\n\t" "v_mfma_f32_16x16x32_bf16 v[84:87], a[156:159], a[36:39], v[84:87]\n\t"
    "ds_read_b64_tr_b16 a[154:155], %[vr2] offset:10784\n\t" "v_mfma_f32_16x16x32_bf16 v[88:91], a[156:159], a[40:43], v[88:91]\n\t" "global_load_dwordx4 a[84:87], %[va], s[76:77] offset:1024 sc1\n\t"
    "v_mfma_f32_16x16x32_bf16 v[92:95], a[156:159], a[44:47], v[92:95]\n\t" "s_waitcnt lgkmcnt(12)\n\t" "v_mfma_f32_16x16x32_bf16 v[96:99], a[128:131], a[32:35], v[96:99]\n\t"
    "ds_read_b64_tr_b16 a[156:157], %[vr4] offset:7168\n\t" "v_mfma_f32_16x16x32_bf16 v[100:103], a[128:131], a[36:39], v[100:103]\n\t" "ds_read_b64_tr_b16 a[158:159], %[vr4] offset:10752\n\t"
    "v_mfma_f32_16x16x32_bf16 v[104:107], a[128:131], a[40:43], v[104:107]\n\t" "v_mfma_f32_16x16x32_bf16 v[108:111], a[128:131], a[44:47], v[108:111]\n\t" "s_waitcnt lgkmcnt(12)\n\t"
    "s_add_u32 m0, %[sl], 44032\n\t" "v_mfma_f32_16x16x32_bf16 v[112:115], a[132:135], a[32:35], v[112:115]\n\t" "global_load_lds_dwordx4 %[vb1], s[78:79]\n\t"
    "ds_read_b64_tr_b16 a[128:129], %[vr4] offset:7200\n\t" "v_mfma_f32_16x16x32_bf16 v[116:119], a[132:135], a[36:39], v[116:119]\n\t" "ds_read_b64_tr_b16 a[130:131], %[vr4] offset:10784\n\t"
    "v_mfma_f32_16x16x32_bf16 v[120:123], a[132:135], a[40:43], v[120:123]\n\t" "v_mfma_f32_16x16x32_bf16 v[124:127], a[132:135], a[44:47], v[124:127]\n\t" "s_load_dwordx2 s[98:99], s[84:85], 188\n\t"
    "s_waitcnt lgkmcnt(12)\n\t" "v_mfma_f32_16x16x32_bf16 v[128:131], a[136:139], a[32:35], v[128:131]\n\t" "ds_read_b64_tr_b16 a[132:133], %[vr6] offset:7168\n\t"
    "v_mfma_f32_16x16x32_bf16 v[132:135], a[136:139], a[36:39], v[132:135]\n\t" "ds_read_b64_tr_b16 a[134:135], %[vr6] offset:10752\n\t"
    "v_mfma_f32_16x16x32_bf16 v[136:139], a[136:139], a[40:43], v[136:139]\n\t" "global_load_dwordx4 a[88:91], %[va], s[76:77] offset:2048 sc1\n\t"
    "v_mfma_f32_16x16x32_bf16 v[140:143], a[136:139], a[44:47], v[140:143]\n\t" "s_waitcnt lgkmcnt(12)\n\t" "v_mfma_f32_16x16x32_bf16 v[144:147], a[140:143], a[32:35], v[144:147]\n\t"
    "s_waitcnt vmcnt(13)\n\t" "s_barrier\n\t" "ds_read_b64_tr_b16 a[136:137], %[vr0] offset:14336\n\t" "v_mfma_f32_16x16x32_bf16 v[148:151], a[140:143], a[36:39], v[148:151]\n\t"
    "ds_read_b64_tr_b16 a[138:139], %[vr0] offset:17920\n\t" "v_mfma_f32_16x16x32_bf16 v[152:155], a[140:143], a[40:43], v[152:155]\n\t"
    "v_mfma_f32_16x16x32_bf16 v[156:159], a[140:143], a[44:47], v[156:159]\n\t" "s_waitcnt lgkmcnt(12)\n\t" "s_add_u32 m0, %[sl], 45056\n\t"
    "v_mfma_f32_16x16x32_bf16 v[160:163], a[144:147], a[32:35], v[160:163]\n\t" "global_load_lds_dwordx4 %[vb2], s[78:79]\n\t" "ds_read_b64_tr_b16 a[140:141], %[vr0] offset:14368\n\t"
    "v_mfma_f32_16x16x32_bf16 v[164:167], a[144:147], a[36:39], v[164:167]\n\t" "ds_read_b64_tr_b16 a[142:143], %[vr0] offset:17952\n\t"
    "v_mfma_f32_16x16x32_bf16 v[168:171], a[144:147], a[40:43], v[168:171]\n\t" "v_mfma_f32_16x16x32_bf16 v[172:175], a[144:147], a[44:47], v[172:175]\n\t" "s_waitcnt lgkmcnt(12)\n\t"
    "v_mfma_f32_16x16x32_bf16 v[176:179], a[148:151], a[32:35], v[176:179]\n\t" "ds_read_b64_tr_b16 a[144:145], %[vr2] offset:14336\n\t"
    "v_mfma_f32_16x16x32_bf16 v[180:183], a[148:151], a[36:39], v[180:183]\n\t" "ds_read_b64_tr_b16 a[146:147], %[vr2] offset:17920\n\t"
    "v_mfma_f32_16x16x32_bf16 v[184:187], a[148:151], a[40:43], v[184:187]\n\t" "global_load_dwordx4 a[92:95], %[va], s[76:77] offset:3072 sc1\n\t" "s_add_u32 s76, s76, 4096\n\t"
    "s_addc_u32 s77, s77, 0\n\t" "v_mfma_f32_16x16x32_bf16 v[188:191], a[148:151], a[44:47], v[188:191]\n\t" "s_waitcnt lgkmcnt(12)\n\t"
    "v_mfma_f32_16x16x32_bf16 v[192:195], a[152:155], a[32:35], v[192:195]\n\t" "ds_read_b64_tr_b16 a[148:149], %[vr2] offset:14368\n\t"
    "v_mfma_f32_16x16x32_bf16 v[196:199], a[152:155], a[36:39], v[196:199]\n\t" "ds_read_b64_tr_b16 a[150:151], %[vr2] offset:17952\n\t" "s_load_dwordx2 s[98:99], s[84:85], 316\n\t"
    "s_add_u32 s84, s84, 384\n\t" "s_addc_u32 s85, s85, 0\n\t" "v_mfma_f32_16x16x32_bf16 v[200:203], a[152:155], a[40:43], v[200:203]\n\t"
    "v_mfma_f32_16x16x32_bf16 v[204:207], a[152:155], a[44:47], v[204:207]\n\t" "s_waitcnt lgkmcnt(12)\n\t" "s_add_u32 m0, %[sl], 46080\n\t"
    "v_mfma_f32_16x16x32_bf16 v[208:211], a[156:159], a[32:35], v[208:211]\n\t" "s_mov_b64 exec, s[82:83]\n\t" "global_load_lds_dwordx4 %[vb3], s[78:79]\n\t" "s_mov_b64 exec, -1\n\t"
    "s_add_u32 s78, s78, 229376\n\t" "s_addc_u32 s79, s79, 0\n\t" "ds_read_b64_tr_b16 a[152:153], %[vr4] offset:14336\n\t" "v_mfma_f32_16x16x32_bf16 v[212:215], a[156:159], a[36:39], v[212:215]\n\t"
    "ds_read_b64_tr_b16 a[154:155], %[vr4] offset:17920\n\t" "v_mfma_f32_16x16x32_bf16 v[216:219], a[156:159], a[40:43], v[216:219]\n\t"
    "v_mfma_f32_16x16x32_bf16 v[220:223], a[156:159], a[44:47], v[220:223]\n\t" "s_waitcnt lgkmcnt(12)\n\t" "v_mfma_f32_16x16x32_bf16 v[224:227], a[128:131], a[32:35], v[224:227]\n\t"
    "ds_read_b64_tr_b16 a[156:157], %[vr4] offset:14368\n\t" "v_mfma_f32_16x16x32_bf16 v[228:231], a[128:131], a[36:39], v[228:231]\n\t" "ds_read_b64_tr_b16 a[158:159], %[vr4] offset:17952\n\t"
    "v_mfma_f32_16x16x32_bf16 v[232:235], a[128:131], a[40:43], v[232:235]\n\t" "v_mfma_f32_16x16x32_bf16 v[236:239], a[128:131], a[44:47], v[236:239]\n\t" "s_waitcnt lgkmcnt(12)\n\t"
    "v_mfma_f32_16x16x32_bf16 v[240:243], a[132:135], a[32:35], v[240:243]\n\t" "ds_read_b64_tr_b16 a[128:129], %[vr6] offset:14336\n\t"
    "v_mfma_f32_16x16x32_bf16 v[244:247], a[132:135], a[36:39], v[244:247]\n\t" "ds_read_b64_tr_b16 a[130:131], %[vr6] offset:17920\n\t"
    "v_mfma_f32_16x16x32_bf16 v[248:251], a[132:135], a[40:43], v[248:251]\n\t" "v_mfma_f32_16x16x32_bf16 v[252:255], a[132:135], a[44:47], v[252:255]\n\t" "s_waitcnt lgkmcnt(12)\n\t"
    "v_mfma_f32_16x16x32_bf16 v[32:35], a[136:139], a[48:51], v[32:35]\n\t" "ds_read_b64_tr_b16 a[132:133], %[vr0] offset:21504\n\t" "v_mfma_f32_16x16x32_bf16 v[36:39], a[136:139], a[52:55], v[36:39]\n\t"
    "ds_read_b64_tr_b16 a[134:135], %[vr0] offset:25088\n\t" "v_mfma_f32_16x16x32_bf16 v[40:43], a[136:139], a[56:59], v[40:43]\n\t" "global_load_dwordx4 a[96:99], %[va], s[76:77] offset:0 sc1\n\t"
    "v_mfma_f32_16x16x32_bf16 v[44:47], a[136:139], a[60:63], v[44:47]\n\t" "s_waitcnt lgkmcnt(12)\n\t" "v_mfma_f32_16x16x32_bf16 v[48:51], a[140:143], a[48:51], v[48:51]\n\t"
    "ds_read_b64_tr_b16 a[136:137], %[vr0] offset:21536\n\t" "v_mfma_f32_16x16x32_bf16 v[52:55], a[140:143], a[52:55], v[52:55]\n\t" "ds_read_b64_tr_b16 a[138:139], %[vr0] offset:25120\n\t"
    "s_load_dwordx2 s[98:99], s[84:85], 60\n\t" "v_mfma_f32_16x16x32_bf16 v[56:59], a[140:143], a[56:59], v[56:59]\n\t" "v_mfma_f32_16x16x32_bf16 v[60:63], a[140:143], a[60:63], v[60:63]\n\t"
    "s_waitcnt lgkmcnt(12)\n\t" "s_add_u32 m0, %[sl], 57344\n\t" "v_mfma_f32_16x16x32_bf16 v[64:67], a[144:147], a[48:51], v[64:67]\n\t" "global_load_lds_dwordx4 %[vb0], s[78:79]\n\t"
    "ds_read_b64_tr_b16 a[140:141], %[vr2] offset:21504\n\t" "v_mfma_f32_16x16x32_bf16 v[68:71], a[144:147], a[52:55], v[68:71]\n\t" "ds_read_b64_tr_b16 a[142:143], %[vr2] offset:25088\n\t"
    "v_mfma_f32_16x16x32_bf16 v[72:75], a[144:147], a[56:59], v[72:75]\n\t" "v_mfma_f32_16x16x32_bf16 v[76:79], a[144:147], a[60:63], v[76:79]\n\t" "s_waitcnt lgkmcnt(12)\n\t"
    "v_mfma_f32_16x16x32_bf16 v[80:83], a[148:151], a[48:51], v[80:83]\n\t" "ds_read_b64_tr_b16 a[144:145], %[vr2] offset:21536\n\t" "v_mfma_f32_16x16x32_bf16 v[84:87], a[148:151], a[52:55], v[84:87]\n\t"
    "ds_read_b64_tr_b16 a[146:147], %[vr2] offset:25120\n\t" "v_mfma_f32_16x16x32_bf16 v[88:91], a[148:151], a[56:59], v[88:91]\n\t" "global_load_dwordx4 a[100:103], %[va], s[76:77] offset:1024 sc1\n\t"
    "v_mfma_f32_16x16x32_bf16 v[92:95], a[148:151], a[60:63], v[92:95]\n\t" "s_waitcnt lgkmcnt(12)\n\t" "v_mfma_f32_16x16x32_bf16 v[96:99], a[152:155], a[48:51], v[96:99]\n\t"
    "ds_read_b64_tr_b16 a[148:149], %[vr4] offset:21504\n\t" "v_mfma_f32_16x16x32_bf16 v[100:103], a[152:155], a[52:55], v[100:103]\n\t" "ds_read_b64_tr_b16 a[150:151], %[vr4] offset:25088\n\t"
    "v_mfma_f32_16x16x32_bf16 v[104:107], a[152:155], a[56:59], v[104:107]\n\t" "v_mfma_f32_16x16x32_bf16 v[108:111], a[152:155], a[60:63], v[108:111]\n\t" "s_waitcnt lgkmcnt(12)\n\t"
    "s_add_u32 m0, %[sl], 58368\n\t" "v_mfma_f32_16x16x32_bf16 v[112:115], a[156:159], a[48:51], v[112:115]\n\t" "global_load_lds_dwordx4 %[vb1], s[78:79]\n\t"
    "ds_read_b64_tr_b16 a[152:153], %[vr4] offset:21536\n\t" "v_mfma_f32_16x16x32_bf16 v[116:119], a[156:159], a[52:55], v[116:119]\n\t" "ds_read_b64_tr_b16 a[154:155], %[vr4] offset:25120\n\t"
    "v_mfma_f32_16x16x32_bf16 v[120:123], a[156:159], a[56:59], v[120:123]\n\t" "v_mfma_f32_16x16x32_bf16 v[124:127], a[156:159], a[60:63], v[124:127]\n\t" "s_load_dwordx2 s[98:99], s[84:85], 188\n\t"
    "s_waitcnt lgkmcnt(12)\n\t" "v_mfma_f32_16x16x32_bf16 v[128:131], a[128:131], a[48:51], v[128:131]\n\t" "ds_read_b64_tr_b16 a[156:157], %[vr6] offset:21504\n\t"
    "v_mfma_f32_16x16x32_bf16 v[132:135], a[128:131], a[52:55], v[132:135]\n\t" "ds_read_b64_tr_b16 a[158:159], %[vr6] offset:25088\n\t"
    "v_mfma_f32_16x16x32_bf16 v[136:139], a[128:131], a[56:59], v[136:139]\n\t" "global_load_dwordx4 a[104:107], %[va], s[76:77] offset:2048 sc1\n\t"
    "v_mfma_f32_16x16x32_bf16 v[140:143], a[128:131], a[60:63], v[140:143]\n\t" "s_waitcnt lgkmcnt(12)\n\t" "v_mfma_f32_16x16x32_bf16 v[144:147], a[132:135], a[48:51], v[144:147]\n\t"
    "s_waitcnt vmcnt(13)\n\t" "s_barrier\n\t" "ds_read_b64_tr_b16 a[128:129], %[vr0] offset:28672\n\t" "v_mfma_f32_16x16x32_bf16 v[148:151], a[132:135], a[52:55], v[148:151]\n\t"
    "ds_read_b64_tr_b16 a[130:131], %[vr0] offset:32256\n\t" "v_mfma_f32_16x16x32_bf16 v[152:155], a[132:135], a[56:59], v[152:155]\n\t"
    "v_mfma_f32_16x16x32_bf16 v[156:159], a[132:135], a[60:63], v[156:159]\n\t" "s_waitcnt lgkmcnt(12)\n\t" "s_add_u32 m0, %[sl], 59392\n\t"
    "v_mfma_f32_16x16x32_bf16 v[160:163], a[136:139], a[48:51], v[160:163]\n\t" "global_load_lds_dwordx4 %[vb2], s[78:79]\n\t" "ds_read_b64_tr_b16 a[132:133], %[vr0] offset:28704\n\t"
    "v_mfma_f32_16x16x32_bf16 v[164:167], a[136:139], a[52:55], v[164:167]\n\t" "ds_read_b64_tr_b16 a[134:135], %[vr0] offset:32288\n\t"
    "v_mfma_f32_16x16x32_bf16 v[168:171], a[136:139], a[56:59], v[168:171]\n\t" "v_mfma_f32_16x16x32_bf16 v[172:175], a[136:139], a[60:63], v[172:175]\n\t" "s_waitcnt lgkmcnt(12)\n\t"
    "v_mfma_f32_16x16x32_bf16 v[176:179], a[140:143], a[48:51], v[176:179]\n\t" "ds_read_b64_tr_b16 a[136:137], %[vr2] offset:28672\n\t"
    "v_mfma_f32_16x16x32_bf16 v[180:183], a[140:143], a[52:55], v[180:183]\n\t" "ds_read_b64_tr_b16 a[138:139], %[vr2] offset:32256\n\t"
    "v_mfma_f32_16x16x32_bf16 v[184:187], a[140:143], a[56:59], v[184:187]\n\t" "global_load_dwordx4 a[108:111], %[va], s[76:77] offset:3072 sc1\n\t" "s_add_u32 s76, s76, 4096\n\t"
    "s_addc_u32 s77, s77, 0\n\t" "v_mfma_f32_16x16x32_bf16 v[188:191], a[140:143], a[60:63], v[188:191]\n\t" "s_waitcnt lgkmcnt(12)\n\t"
    "v_mfma_f32_16x16x32_bf16 v[192:195], a[144:147], a[48:51], v[192:195]\n\t" "ds_read_b64_tr_b16 a[140:141], %[vr2] offset:28704\n\t"
    "v_mfma_f32_16x16x32_bf16 v[196:199], a[144:147], a[52:55], v[196:199]\n\t" "ds_read_b64_tr_b16 a[142:143], %[vr2] offset:32288\n\t" "s_load_dwordx2 s[98:99], s[84:85], 316\n\t"
    "s_add_u32 s84, s84, 384\n\t" "s_addc_u32 s85, s85, 0\n\t" "v_mfma_f32_16x16x32_bf16 v[200:203], a[144:147], a[56:59], v[200:203]\n\t"
    "v_mfma_f32_16x16x32_bf16 v[204:207], a[144:147], a[60:63], v[204:207]\n\t" "s_waitcnt lgkmcnt(12)\n\t" "s_add_u32 m0, %[sl], 60416\n\t"
    "v_mfma_f32_16x16x32_bf16 v[208:211], a[148:151], a[48:51], v[208:211]\n\t" "s_mov_b64 exec, s[82:83]\n\t" "global_load_lds_dwordx4 %[vb3], s[78:79]\n\t" "s_mov_b64 exec, -1\n\t"
    "s_add_u32 s78, s78, 229376\n\t" "s_addc_u32 s79, s79, 0\n\t" "ds_read_b64_tr_b16 a[144:145], %[vr4] offset:28672\n\t" "v_mfma_f32_16x16x32_bf16 v[212:215], a[148:151], a[52:55], v[212:215]\n\t"
    "ds_read_b64_tr_b16 a[146:147], %[vr4] offset:32256\n\t" "v_mfma_f32_16x16x32_bf16 v[216:219], a[148:151], a[56:59], v[216:219]\n\t"
    "v_mfma_f32_16x16x32_bf16 v[220:223], a[148:151], a[60:63], v[220:223]\n\t" "s_waitcnt lgkmcnt(12)\n\t" "v_mfma_f32_16x16x32_bf16 v[224:227], a[152:155], a[48:51], v[224:227]\n\t"
    "ds_read_b64_tr_b16 a[148:149], %[vr4] offset:28704\n\t" "v_mfma_f32_16x16x32_bf16 v[228:231], a[152:155], a[52:55], v[228:231]\n\t" "ds_read_b64_tr_b16 a[150:151], %[vr4] offset:32288\n\t"
    "v_mfma_f32_16x16x32_bf16 v[232:235], a[152:155], a[56:59], v[232:235]\n\t" "v_mfma_f32_16x16x32_bf16 v[236:239], a[152:155], a[60:63], v[236:239]\n\t" "s_waitcnt lgkmcnt(12)\n\t"
    "v_mfma_f32_16x16x32_bf16 v[240:243], a[156:159], a[48:51], v[240:243]\n\t" "ds_read_b64_tr_b16 a[152:153], %[vr6] offset:28672\n\t"
    "v_mfma_f32_16x16x32_bf16 v[244:247], a[156:159], a[52:55], v[244:247]\n\t" "ds_read_b64_tr_b16 a[154:155], %[vr6] offset:32256\n\t"
    "v_mfma_f32_16x16x32_bf16 v[248:251], a[156:159], a[56:59], v[248:251]\n\t" "v_mfma_f32_16x16x32_bf16 v[252:255], a[156:159], a[60:63], v[252:255]\n\t" ".Ld0_%=:\n\t" "s_add_u32 s80, s80, 1\n\t"
    "s_cmp_lt_u32 s80, 2\n\t" "s_cbranch_scc1 .Lloop_%=\n\t" "s_branch .Ltail_%=\n\t" ".LE0_%=:\n\t" "s_waitcnt lgkmcnt(12)\n\t" "v_mfma_f32_16x16x32_bf16 v[32:35], a[128:131], a[0:3], v[32:35]\n\t"
    "ds_read_b64_tr_b16 a[156:157], %[vr1] offset:35840\n\t" "v_mfma_f32_16x16x32_bf16 v[36:39], a[128:131], a[4:7], v[36:39]\n\t" "ds_read_b64_tr_b16 a[158:159], %[vr1] offset:39424\n\t"
    "v_mfma_f32_16x16x32_bf16 a[160:163], a[128:131], a[184:187], 0\n\t" "global_load_dwordx4 a[48:51], %[va], s[76:77] offset:0 sc1\n\t"
    "v_mfma_f32_16x16x32_bf16 v[40:43], a[128:131], a[8:11], v[40:43]\n\t" "v_mfma_f32_16x16x32_bf16 v[44:47], a[128:131], a[12:15], v[44:47]\n\t" "s_waitcnt lgkmcnt(12)\n\t"
    "v_mfma_f32_16x16x32_bf16 v[48:51], a[132:135], a[0:3], v[48:51]\n\t" "ds_read_b64_tr_b16 a[128:129], %[vr1] offset:35872\n\t" "s_load_dwordx2 s[98:99], s[84:85], 60\n\t"
    "v_mfma_f32_16x16x32_bf16 v[52:55], a[132:135], a[4:7], v[52:55]\n\t" "ds_read_b64_tr_b16 a[130:131], %[vr1] offset:39456\n\t" "v_mfma_f32_16x16x32_bf16 a[164:167], a[132:135], a[184:187], 0\n\t"
    "s_add_u32 m0, %[sl], 14336\n\t" "v_mfma_f32_16x16x32_bf16 v[56:59], a[132:135], a[8:11], v[56:59]\n\t" "global_load_lds_dwordx4 %[vb0], s[78:79]\n\t"
    "v_mfma_f32_16x16x32_bf16 v[60:63], a[132:135], a[12:15], v[60:63]\n\t" "s_waitcnt lgkmcnt(12)\n\t" "v_mfma_f32_16x16x32_bf16 v[64:67], a[136:139], a[0:3], v[64:67]\n\t"
    "ds_read_b64_tr_b16 a[132:133], %[vr3] offset:35840\n\t" "v_mfma_f32_16x16x32_bf16 v[68:71], a[136:139], a[4:7], v[68:71]\n\t" "ds_read_b64_tr_b16 a[134:135], %[vr3] offset:39424\n\t"
    "v_mfma_f32_16x16x32_bf16 v[72:75], a[136:139], a[8:11], v[72:75]\n\t" "v_mfma_f32_16x16x32_bf16 v[76:79], a[136:139], a[12:15], v[76:79]\n\t" "s_waitcnt lgkmcnt(12)\n\t"
    "v_mfma_f32_16x16x32_bf16 v[80:83], a[140:143], a[0:3], v[80:83]\n\t" "global_load_dwordx4 a[52:55], %[va], s[76:77] offset:1024 sc1\n\t" "ds_read_b64_tr_b16 a[136:137], %[vr3] offset:35872\n\t"
    "v_mfma_f32_16x16x32_bf16 v[84:87], a[140:143], a[4:7], v[84:87]\n\t" "ds_read_b64_tr_b16 a[138:139], %[vr3] offset:39456\n\t" "v_mfma_f32_16x16x32_bf16 v[88:91], a[140:143], a[8:11], v[88:91]\n\t"
    "v_mfma_f32_16x16x32_bf16 v[92:95], a[140:143], a[12:15], v[92:95]\n\t" "s_waitcnt lgkmcnt(12)\n\t" "v_mfma_f32_16x16x32_bf16 v[96:99], a[144:147], a[0:3], v[96:99]\n\t"
    "ds_read_b64_tr_b16 a[140:141], %[vr5] offset:35840\n\t" "v_mfma_f32_16x16x32_bf16 v[100:103], a[144:147], a[4:7], v[100:103]\n\t" "ds_read_b64_tr_b16 a[142:143], %[vr5] offset:39424\n\t"
    "s_add_u32 m0, %[sl], 15360\n\t" "v_mfma_f32_16x16x32_bf16 v[104:107], a[144:147], a[8:11], v[104:107]\n\t" "global_load_lds_dwordx4 %[vb1], s[78:79]\n\t"
    "v_mfma_f32_16x16x32_bf16 v[108:111], a[144:147], a[12:15], v[108:111]\n\t" "s_waitcnt lgkmcnt(12)\n\t" "v_mfma_f32_16x16x32_bf16 v[112:115], a[148:151], a[0:3], v[112:115]\n\t"
    "ds_read_b64_tr_b16 a[144:145], %[vr5] offset:35872\n\t" "v_mfma_f32_16x16x32_bf16 v[116:119], a[148:151], a[4:7], v[116:119]\n\t" "ds_read_b64_tr_b16 a[146:147], %[vr5] offset:39456\n\t"
    "s_load_dwordx2 s[98:99], s[84:85], 188\n\t" "v_mfma_f32_16x16x32_bf16 v[120:123], a[148:151], a[8:11], v[120:123]\n\t" "v_mfma_f32_16x16x32_bf16 v[124:127], a[148:151], a[12:15], v[124:127]\n\t"
    "s_waitcnt lgkmcnt(12)\n\t" "v_mfma_f32_16x16x32_bf16 v[128:131], a[152:155], a[0:3], v[128:131]\n\t" "global_load_dwordx4 a[56:59], %[va], s[76:77] offset:2048 sc1\n\t"
    "ds_read_b64_tr_b16 a[148:149], %[vr7] offset:35840\n\t" "v_mfma_f32_16x16x32_bf16 v[132:135], a[152:155], a[4:7], v[132:135]\n\t" "ds_read_b64_tr_b16 a[150:151], %[vr7] offset:39424\n\t"
    "v_mfma_f32_16x16x32_bf16 a[168:171], a[152:155], a[184:187], 0\n\t" "v_mfma_f32_16x16x32_bf16 v[136:139], a[152:155], a[8:11], v[136:139]\n\t"
    "v_mfma_f32_16x16x32_bf16 v[140:143], a[152:155], a[12:15], v[140:143]\n\t" "s_waitcnt lgkmcnt(12)\n\t" "v_mfma_f32_16x16x32_bf16 v[144:147], a[156:159], a[0:3], v[144:147]\n\t"
    "s_waitcnt vmcnt(13)\n\t" "s_barrier\n\t" "ds_read_b64_tr_b16 a[152:153], %[vr1] offset:43008\n\t" "s_add_u32 m0, %[sl], 16384\n\t"
    "v_mfma_f32_16x16x32_bf16 v[148:151], a[156:159], a[4:7], v[148:151]\n\t" "global_load_lds_dwordx4 %[vb2], s[78:79]\n\t" "ds_read_b64_tr_b16 a[154:155], %[vr1] offset:46592\n\t"
    "v_mfma_f32_16x16x32_bf16 a[172:175], a[156:159], a[184:187], 0\n\t" "v_mfma_f32_16x16x32_bf16 v[152:155], a[156:159], a[8:11], v[152:155]\n\t"
    "v_mfma_f32_16x16x32_bf16 v[156:159], a[156:159], a[12:15], v[156:159]\n\t" "s_waitcnt lgkmcnt(12)\n\t" "v_mfma_f32_16x16x32_bf16 v[160:163], a[128:131], a[0:3], v[160:163]\n\t"
    "ds_read_b64_tr_b16 a[156:157], %[vr1] offset:43040\n\t" "v_mfma_f32_16x16x32_bf16 v[164:167], a[128:131], a[4:7], v[164:167]\n\t" "ds_read_b64_tr_b16 a[158:159], %[vr1] offset:46624\n\t"
    "v_mfma_f32_16x16x32_bf16 a[176:179], a[128:131], a[184:187], 0\n\t" "global_load_dwordx4 a[60:63], %[va], s[76:77] offset:3072 sc1\n\t" "s_add_u32 s76, s76, 4096\n\t" "s_addc_u32 s77, s77, 0\n\t"
    "v_mfma_f32_16x16x32_bf16 v[168:171], a[128:131], a[8:11], v[168:171]\n\t" "v_mfma_f32_16x16x32_bf16 v[172:175], a[128:131], a[12:15], v[172:175]\n\t" "s_waitcnt lgkmcnt(12)\n\t"
    "v_mfma_f32_16x16x32_bf16 v[176:179], a[132:135], a[0:3], v[176:179]\n\t" "ds_read_b64_tr_b16 a[128:129], %[vr3] offset:43008\n\t" "s_load_dwordx2 s[98:99], s[84:85], 316\n\t"
    "s_add_u32 s84, s84, 384\n\t" "s_addc_u32 s85, s85, 0\n\t" "v_mfma_f32_16x16x32_bf16 v[180:183], a[132:135], a[4:7], v[180:183]\n\t" "ds_read_b64_tr_b16 a[130:131], %[vr3] offset:46592\n\t"
    "v_mfma_f32_16x16x32_bf16 v[184:187], a[132:135], a[8:11], v[184:187]\n\t" "s_add_u32 m0, %[sl], 17408\n\t" "v_mfma_f32_16x16x32_bf16 v[188:191], a[132:135], a[12:15], v[188:191]\n\t"
    "s_mov_b64 exec, s[82:83]\n\t" "global_load_lds_dwordx4 %[vb3], s[78:79]\n\t" "s_mov_b64 exec, -1\n\t" "s_add_u32 s78, s78, 229376\n\t" "s_addc_u32 s79, s79, 0\n\t" "s_waitcnt lgkmcnt(12)\n\t"
    "v_mfma_f32_16x16x32_bf16 v[192:195], a[136:139], a[0:3], v[192:195]\n\t" "ds_read_b64_tr_b16 a[132:133], %[vr3] offset:43040\n\t"
    "v_mfma_f32_16x16x32_bf16 v[196:199], a[136:139], a[4:7], v[196:199]\n\t" "ds_read_b64_tr_b16 a[134:135], %[vr3] offset:46624\n\t"
    "v_mfma_f32_16x16x32_bf16 v[200:203], a[136:139], a[8:11], v[200:203]\n\t" "v_mfma_f32_16x16x32_bf16 v[204:207], a[136:139], a[12:15], v[204:207]\n\t" "s_waitcnt lgkmcnt(12)\n\t"
    "v_mfma_f32_16x16x32_bf16 v[208:211], a[140:143], a[0:3], v[208:211]\n\t" "ds_read_b64_tr_b16 a[136:137], %[vr5] offset:43008\n\t"
    "v_mfma_f32_16x16x32_bf16 v[212:215], a[140:143], a[4:7], v[212:215]\n\t" "ds_read_b64_tr_b16 a[138:139], %[vr5] offset:46592\n\t"
    "v_mfma_f32_16x16x32_bf16 v[216:219], a[140:143], a[8:11], v[216:219]\n\t" "v_mfma_f32_16x16x32_bf16 v[220:223], a[140:143], a[12:15], v[220:223]\n\t" "s_waitcnt lgkmcnt(12)\n\t"
    "v_mfma_f32_16x16x32_bf16 v[224:227], a[144:147], a[0:3], v[224:227]\n\t" "ds_read_b64_tr_b16 a[140:141], %[vr5] offset:43040\n\t"
    "v_mfma_f32_16x16x32_bf16 v[228:231], a[144:147], a[4:7], v[228:231]\n\t" "ds_read_b64_tr_b16 a[142:143], %[vr5] offset:46624\n\t"
    "v_mfma_f32_16x16x32_bf16 v[232:235], a[144:147], a[8:11], v[232:235]\n\t" "v_mfma_f32_16x16x32_bf16 v[236:239], a[144:147], a[12:15], v[236:239]\n\t" "s_waitcnt lgkmcnt(12)\n\t"
    "v_mfma_f32_16x16x32_bf16 v[240:243], a[148:151], a[0:3], v[240:243]\n\t" "ds_read_b64_tr_b16 a[144:145], %[vr7] offset:43008\n\t"
    "v_mfma_f32_16x16x32_bf16 v[244:247], a[148:151], a[4:7], v[244:247]\n\t" "ds_read_b64_tr_b16 a[146:147], %[vr7] offset:46592\n\t" "v_mfma_f32_16x16x32_bf16 a[180:183], a[148:151], a[184:187], 0\n\t"
    "v_mfma_f32_16x16x32_bf16 v[248:251], a[148:151], a[8:11], v[248:251]\n\t" "v_mfma_f32_16x16x32_bf16 v[252:255], a[148:151], a[12:15], v[252:255]\n\t" "s_waitcnt lgkmcnt(12)\n\t"
    "v_mfma_f32_16x16x32_bf16 v[32:35], a[152:155], a[16:19], v[32:35]\n\t" "ds_read_b64_tr_b16 a[148:149], %[vr1] offset:50176\n\t" "v_mfma_f32_16x16x32_bf16 v[36:39], a[152:155], a[20:23], v[36:39]\n\t"
    "ds_read_b64_tr_b16 a[150:151], %[vr1] offset:53760\n\t" "v_mfma_f32_16x16x32_bf16 a[160:163], a[152:155], a[188:191], a[160:163]\n\t" "global_load_dwordx4 a[64:67], %[va], s[76:77] offset:0 sc1\n\t"
    "v_mfma_f32_16x16x32_bf16 v[40:43], a[152:155], a[24:27], v[40:43]\n\t" "v_mfma_f32_16x16x32_bf16 v[44:47], a[152:155], a[28:31], v[44:47]\n\t" "s_waitcnt lgkmcnt(12)\n\t"
    "v_mfma_f32_16x16x32_bf16 v[48:51], a[156:159], a[16:19], v[48:51]\n\t" "ds_read_b64_tr_b16 a[152:153], %[vr1] offset:50208\n\t" "s_load_dwordx2 s[98:99], s[84:85], 60\n\t"
    "v_mfma_f32_16x16x32_bf16 v[52:55], a[156:159], a[20:23], v[52:55]\n\t" "ds_read_b64_tr_b16 a[154:155], %[vr1] offset:53792\n\t"
    "v_mfma_f32_16x16x32_bf16 a[164:167], a[156:159], a[188:191], a[164:167]\n\t" "s_add_u32 m0, %[sl], 28672\n\t" "v_mfma_f32_16x16x32_bf16 v[56:59], a[156:159], a[24:27], v[56:59]\n\t"
    "global_load_lds_dwordx4 %[vb0], s[78:79]\n\t" "v_mfma_f32_16x16x32_bf16 v[60:63], a[156:159], a[28:31], v[60:63]\n\t" "s_waitcnt lgkmcnt(12)\n\t"
    "v_mfma_f32_16x16x32_bf16 v[64:67], a[128:131], a[16:19], v[64:67]\n\t" "ds_read_b64_tr_b16 a[156:157], %[vr3] offset:50176\n\t" "v_mfma_f32_16x16x32_bf16 v[68:71], a[128:131], a[20:23], v[68:71]\n\t"
    "ds_read_b64_tr_b16 a[158:159], %[vr3] offset:53760\n\t" "v_mfma_f32_16x16x32_bf16 v[72:75], a[128:131], a[24:27], v[72:75]\n\t" "v_mfma_f32_16x16x32_bf16 v[76:79], a[128:131], a[28:31], v[76:79]\n\t"
    "s_waitcnt lgkmcnt(12)\n\t" "v_mfma_f32_16x16x32_bf16 v[80:83], a[132:135], a[16:19], v[80:83]\n\t" "global_load_dwordx4 a[68:71], %[va], s[76:77] offset:1024 sc1\n\t"
    "ds_read_b64_tr_b16 a[128:129], %[vr3] offset:50208\n\t" "v_mfma_f32_16x16x32_bf16 v[84:87], a[132:135], a[20:23], v[84:87]\n\t" "ds_read_b64_tr_b16 a[130:131], %[vr3] offset:53792\n\t"
    "v_mfma_f32_16x16x32_bf16 v[88:91], a[132:135], a[24:27], v[88:91]\n\t" "v_mfma_f32_16x16x32_bf16 v[92:95], a[132:135], a[28:31], v[92:95]\n\t" "s_waitcnt lgkmcnt(12)\n\t"
    "v_mfma_f32_16x16x32_bf16 v[96:99], a[136:139], a[16:19], v[96:99]\n\t" "ds_read_b64_tr_b16 a[132:133], %[vr5] offset:50176\n\t"
    "v_mfma_f32_16x16x32_bf16 v[100:103], a[136:139], a[20:23], v[100:103]\n\t" "ds_read_b64_tr_b16 a[134:135], %[vr5] offset:53760\n\t" "s_add_u32 m0, %[sl], 29696\n\t"
    "v_mfma_f32_16x16x32_bf16 v[104:107], a[136:139], a[24:27], v[104:107]\n\t" "global_load_lds_dwordx4 %[vb1], s[78:79]\n\t" "v_mfma_f32_16x16x32_bf16 v[108:111], a[136:139], a[28:31], v[108:111]\n\t"
    "s_waitcnt lgkmcnt(12)\n\t" "v_mfma_f32_16x16x32_bf16 v[112:115], a[140:143], a[16:19], v[112:115]\n\t" "ds_read_b64_tr_b16 a[136:137], %[vr5] offset:50208\n\t"
    "v_mfma_f32_16x16x32_bf16 v[116:119], a[140:143], a[20:23], v[116:119]\n\t" "ds_read_b64_tr_b16 a[138:139], %[vr5] offset:53792\n\t" "s_load_dwordx2 s[98:99], s[84:85], 188\n\t"
    "v_mfma_f32_16x16x32_bf16 v[120:123], a[140:143], a[24:27], v[120:123]\n\t" "v_mfma_f32_16x16x32_bf16 v[124:127], a[140:143], a[28:31], v[124:127]\n\t" "s_waitcnt lgkmcnt(12)\n\t"
    "v_mfma_f32_16x16x32_bf16 v[128:131], a[144:147], a[16:19], v[128:131]\n\t" "global_load_dwordx4 a[72:75], %[va], s[76:77] offset:2048 sc1\n\t" "ds_read_b64_tr_b16 a[140:141], %[vr7] offset:50176\n\t"
    "v_mfma_f32_16x16x32_bf16 v[132:135], a[144:147], a[20:23], v[132:135]\n\t" "ds_read_b64_tr_b16 a[142:143], %[vr7] offset:53760\n\t"
    "v_mfma_f32_16x16x32_bf16 a[168:171], a[144:147], a[188:191], a[168:171]\n\t" "v_mfma_f32_16x16x32_bf16 v[136:139], a[144:147], a[24:27], v[136:139]\n\t"
    "v_mfma_f32_16x16x32_bf16 v[140:143], a[144:147], a[28:31], v[140:143]\n\t" "s_waitcnt lgkmcnt(12)\n\t" "v_mfma_f32_16x16x32_bf16 v[144:147], a[148:151], a[16:19], v[144:147]\n\t"
    "s_waitcnt vmcnt(13)\n\t" "s_barrier\n\t" "ds_read_b64_tr_b16 a[144:145], %[vr0] offset:0\n\t" "s_add_u32 m0, %[sl], 30720\n\t"
    "v_mfma_f32_16x16x32_bf16 v[148:151], a[148:151], a[20:23], v[148:151]\n\t" "global_load_lds_dwordx4 %[vb2], s[78:79]\n\t" "ds_read_b64_tr_b16 a[146:147], %[vr0] offset:3584\n\t"
    "v_mfma_f32_16x16x32_bf16 a[172:175], a[148:151], a[188:191], a[172:175]\n\t" "v_mfma_f32_16x16x32_bf16 v[152:155], a[148:151], a[24:27], v[152:155]\n\t"
    "v_mfma_f32_16x16x32_bf16 v[156:159], a[148:151], a[28:31], v[156:159]\n\t" "s_waitcnt lgkmcnt(12)\n\t" "v_mfma_f32_16x16x32_bf16 v[160:163], a[152:155], a[16:19], v[160:163]\n\t"
    "ds_read_b64_tr_b16 a[148:149], %[vr0] offset:32\n\t" "v_mfma_f32_16x16x32_bf16 v[164:167], a[152:155], a[20:23], v[164:167]\n\t" "ds_read_b64_tr_b16 a[150:151], %[vr0] offset:3616\n\t"
    "v_mfma_f32_16x16x32_bf16 a[176:179], a[152:155], a[188:191], a[176:179]\n\t" "global_load_dwordx4 a[76:79], %[va], s[76:77] offset:3072 sc1\n\t" "s_add_u32 s76, s76, 4096\n\t"
    "s_addc_u32 s77, s77, 0\n\t" "v_mfma_f32_16x16x32_bf16 v[168:171], a[152:155], a[24:27], v[168:171]\n\t" "v_mfma_f32_16x16x32_bf16 v[172:175], a[152:155], a[28:31], v[172:175]\n\t"
    "s_waitcnt lgkmcnt(12)\n\t" "v_mfma_f32_16x16x32_bf16 v[176:179], a[156:159], a[16:19], v[176:179]\n\t" "ds_read_b64_tr_b16 a[152:153], %[vr2] offset:0\n\t"
    "s_load_dwordx2 s[98:99], s[84:85], 316\n\t" "s_add_u32 s84, s84, 384\n\t" "s_addc_u32 s85, s85, 0\n\t" "v_mfma_f32_16x16x32_bf16 v[180:183], a[156:159], a[20:23], v[180:183]\n\t"
    "ds_read_b64_tr_b16 a[154:155], %[vr2] offset:3584\n\t" "v_mfma_f32_16x16x32_bf16 v[184:187], a[156:159], a[24:27], v[184:187]\n\t" "s_add_u32 m0, %[sl], 31744\n\t"
    "v_mfma_f32_16x16x32_bf16 v[188:191], a[156:159], a[28:31], v[188:191]\n\t" "s_mov_b64 exec, s[82:83]\n\t" "global_load_lds_dwordx4 %[vb3], s[78:79]\n\t" "s_mov_b64 exec, -1\n\t"
    "s_add_u32 s78, s78, 229376\n\t" "s_addc_u32 s79, s79, 0\n\t" "s_waitcnt lgkmcnt(12)\n\t" "v_mfma_f32_16x16x32_bf16 v[192:195], a[128:131], a[16:19], v[192:195]\n\t"
    "ds_read_b64_tr_b16 a[156:157], %[vr2] offset:32\n\t" "v_mfma_f32_16x16x32_bf16 v[196:199], a[128:131], a[20:23], v[196:199]\n\t" "ds_read_b64_tr_b16 a[158:159], %[vr2] offset:3616\n\t"
    "v_mfma_f32_16x16x32_bf16 v[200:203], a[128:131], a[24:27], v[200:203]\n\t" "v_mfma_f32_16x16x32_bf16 v[204:207], a[128:131], a[28:31], v[204:207]\n\t" "s_waitcnt lgkmcnt(12)\n\t"
    "v_mfma_f32_16x16x32_bf16 v[208:211], a[132:135], a[16:19], v[208:211]\n\t" "ds_read_b64_tr_b16 a[128:129], %[vr4] offset:0\n\t"
    "v_mfma_f32_16x16x32_bf16 v[212:215], a[132:135], a[20:23], v[212:215]\n\t" "ds_read_b64_tr_b16 a[130:131], %[vr4] offset:3584\n\t"
    "v_mfma_f32_16x16x32_bf16 v[216:219], a[132:135], a[24:27], v[216:219]\n\t" "v_mfma_f32_16x16x32_bf16 v[220:223], a[132:135], a[28:31], v[220:223]\n\t" "s_waitcnt lgkmcnt(12)\n\t"
    "v_mfma_f32_16x16x32_bf16 v[224:227], a[136:139], a[16:19], v[224:227]\n\t" "ds_read_b64_tr_b16 a[132:133], %[vr4] offset:32\n\t"
    "v_mfma_f32_16x16x32_bf16 v[228:231], a[136:139], a[20:23], v[228:231]\n\t" "ds_read_b64_tr_b16 a[134:135], %[vr4] offset:3616\n\t"
    "v_mfma_f32_16x16x32_bf16 v[232:235], a[136:139], a[24:27], v[232:235]\n\t" "v_mfma_f32_16x16x32_bf16 v[236:239], a[136:139], a[28:31], v[236:239]\n\t" "s_waitcnt lgkmcnt(12)\n\t"
    "v_mfma_f32_16x16x32_bf16 v[240:243], a[140:143], a[16:19], v[240:243]\n\t" "ds_read_b64_tr_b16 a[136:137], %[vr6] offset:0\n\t"
    "v_mfma_f32_16x16x32_bf16 v[244:247], a[140:143], a[20:23], v[244:247]\n\t" "ds_read_b64_tr_b16 a[138:139], %[vr6] offset:3584\n\t"
    "v_mfma_f32_16x16x32_bf16 a[180:183], a[140:143], a[188:191], a[180:183]\n\t" "v_mfma_f32_16x16x32_bf16 v[248:251], a[140:143], a[24:27], v[248:251]\n\t"
    "v_mfma_f32_16x16x32_bf16 v[252:255], a[140:143], a[28:31], v[252:255]\n\t" "s_waitcnt lgkmcnt(12)\n\t" "v_mfma_f32_16x16x32_bf16 v[32:35], a[144:147], a[32:35], v[32:35]\n\t"
    "ds_read_b64_tr_b16 a[140:141], %[vr0] offset:7168\n\t" "v_mfma_f32_16x16x32_bf16 v[36:39], a[144:147], a[36:39], v[36:39]\n\t" "ds_read_b64_tr_b16 a[142:143], %[vr0] offset:10752\n\t"
    "v_mfma_f32_16x16x32_bf16 a[160:163], a[144:147], a[192:195], a[160:163]\n\t" "global_load_dwordx4 a[80:83], %[va], s[76:77] offset:0 sc1\n\t"
    "v_mfma_f32_16x16x32_bf16 v[40:43], a[144:147], a[40:43], v[40:43]\n\t" "v_mfma_f32_16x16x32_bf16 v[44:47], a[144:147], a[44:47], v[44:47]\n\t" "s_waitcnt lgkmcnt(12)\n\t"
    "v_mfma_f32_16x16x32_bf16 v[48:51], a[148:151], a[32:35], v[48:51]\n\t" "ds_read_b64_tr_b16 a[144:145], %[vr0] offset:7200\n\t" "s_load_dwordx2 s[98:99], s[84:85], 60\n\t"
    "v_mfma_f32_16x16x32_bf16 v[52:55], a[148:151], a[36:39], v[52:55]\n\t" "ds_read_b64_tr_b16 a[146:147], %[vr0] offset:10784\n\t"
    "v_mfma_f32_16x16x32_bf16 a[164:167], a[148:151], a[192:195], a[164:167]\n\t" "s_add_u32 m0, %[sl], 43008\n\t" "v_mfma_f32_16x16x32_bf16 v[56:59], a[148:151], a[40:43], v[56:59]\n\t"
    "global_load_lds_dwordx4 %[vb0], s[78:79]\n\t" "v_mfma_f32_16x16x32_bf16 v[60:63], a[148:151], a[44:47], v[60:63]\n\t" "s_waitcnt lgkmcnt(12)\n\t"
    "v_mfma_f32_16x16x32_bf16 v[64:67], a[152:155], a[32:35], v[64:67]\n\t" "ds_read_b64_tr_b16 a[148:149], %[vr2] offset:7168\n\t" "v_mfma_f32_16x16x32_bf16 v[68:71], a[152:155], a[36:39], v[68:71]\n\t"
    "ds_read_b64_tr_b16 a[150:151], %[vr2] offset:10752\n\t" "v_mfma_f32_16x16x32_bf16 v[72:75], a[152:155], a[40:43], v[72:75]\n\t" "v_mfma_f32_16x16x32_bf16 v[76:79], a[152:155], a[44:47], v[76:79]\n\t"
    "s_waitcnt lgkmcnt(12)\n\t" "v_mfma_f32_16x16x32_bf16 v[80:83], a[156:159], a[32:35], v[80:83]\n\t" "global_load_dwordx4 a[84:87], %[va], s[76:77] offset:1024 sc1\n\t"
    "ds_read_b64_tr_b16 a[152:153], %[vr2] offset:7200\n\t" "v_mfma_f32_16x16x32_bf16 v[84:87], a[156:159], a[36:39], v[84:87]\n\t" "ds_read_b64_tr_b16 a[154:155], %[vr2] offset:10784\n\t"
    "v_mfma_f32_16x16x32_bf16 v[88:91], a[156:159], a[40:43], v[88:91]\n\t" "v_mfma_f32_16x16x32_bf16 v[92:95], a[156:159], a[44:47], v[92:95]\n\t" "s_waitcnt lgkmcnt(12)\n\t"
    "v_mfma_f32_16x16x32_bf16 v[96:99], a[128:131], a[32:35], v[96:99]\n\t" "ds_read_b64_tr_b16 a[156:157], %[vr4] offset:7168\n\t"
    "v_mfma_f32_16x16x32_bf16 v[100:103], a[128:131], a[36:39], v[100:103]\n\t" "ds_read_b64_tr_b16 a[158:159], %[vr4] offset:10752\n\t" "s_add_u32 m0, %[sl], 44032\n\t"
    "v_mfma_f32_16x16x32_bf16 v[104:107], a[128:131], a[40:43], v[104:107]\n\t" "global_load_lds_dwordx4 %[vb1], s[78:79]\n\t" "v_mfma_f32_16x16x32_bf16 v[108:111], a[128:131], a[44:47], v[108:111]\n\t"
    "s_waitcnt lgkmcnt(12)\n\t" "v_mfma_f32_16x16x32_bf16 v[112:115], a[132:135], a[32:35], v[112:115]\n\t" "ds_read_b64_tr_b16 a[128:129], %[vr4] offset:7200\n\t"
    "v_mfma_f32_16x16x32_bf16 v[116:119], a[132:135], a[36:39], v[116:119]\n\t" "ds_read_b64_tr_b16 a[130:131], %[vr4] offset:10784\n\t" "s_load_dwordx2 s[98:99], s[84:85], 188\n\t"
    "v_mfma_f32_16x16x32_bf16 v[120:123], a[132:135], a[40:43], v[120:123]\n\t" "v_mfma_f32_16x16x32_bf16 v[124:127], a[132:135], a[44:47], v[124:127]\n\t" "s_waitcnt lgkmcnt(12)\n\t"
    "v_mfma_f32_16x16x32_bf16 v[128:131], a[136:139], a[32:35], v[128:131]\n\t" "global_load_dwordx4 a[88:91], %[va], s[76:77] offset:2048 sc1\n\t" "ds_read_b64_tr_b16 a[132:133], %[vr6] offset:7168\n\t"
    "v_mfma_f32_16x16x32_bf16 v[132:135], a[136:139], a[36:39], v[132:135]\n\t" "ds_read_b64_tr_b16 a[134:135], %[vr6] offset:10752\n\t"
    "v_mfma_f32_16x16x32_bf16 a[168:171], a[136:139], a[192:195], a[168:171]\n\t" "v_mfma_f32_16x16x32_bf16 v[136:139], a[136:139], a[40:43], v[136:139]\n\t"
    "v_mfma_f32_16x16x32_bf16 v[140:143], a[136:139], a[44:47], v[140:143]\n\t" "s_waitcnt lgkmcnt(12)\n\t" "v_mfma_f32_16x16x32_bf16 v[144:147], a[140:143], a[32:35], v[144:147]\n\t"
    "s_waitcnt vmcnt(13)\n\t" "s_barrier\n\t" "ds_read_b64_tr_b16 a[136:137], %[vr0] offset:14336\n\t" "s_add_u32 m0, %[sl], 45056\n\t"
    "v_mfma_f32_16x16x32_bf16 v[148:151], a[140:143], a[36:39], v[148:151]\n\t" "global_load_lds_dwordx4 %[vb2], s[78:79]\n\t" "ds_read_b64_tr_b16 a[138:139], %[vr0] offset:17920\n\t"
    "v_mfma_f32_16x16x32_bf16 a[172:175], a[140:143], a[192:195], a[172:175]\n\t" "v_mfma_f32_16x16x32_bf16 v[152:155], a[140:143], a[40:43], v[152:155]\n\t"
    "v_mfma_f32_16x16x32_bf16 v[156:159], a[140:143], a[44:47], v[156:159]\n\t" "s_waitcnt lgkmcnt(12)\n\t" "v_mfma_f32_16x16x32_bf16 v[160:163], a[144:147], a[32:35], v[160:163]\n\t"
    "ds_read_b64_tr_b16 a[140:141], %[vr0] offset:14368\n\t" "v_mfma_f32_16x16x32_bf16 v[164:167], a[144:147], a[36:39], v[164:167]\n\t" "ds_read_b64_tr_b16 a[142:143], %[vr0] offset:17952\n\t"
    "v_mfma_f32_16x16x32_bf16 a[176:179], a[144:147], a[192:195], a[176:179]\n\t" "global_load_dwordx4 a[92:95], %[va], s[76:77] offset:3072 sc1\n\t" "s_add_u32 s76, s76, 4096\n\t"
    "s_addc_u32 s77, s77, 0\n\t" "v_mfma_f32_16x16x32_bf16 v[168:171], a[144:147], a[40:43], v[168:171]\n\t" "v_mfma_f32_16x16x32_bf16 v[172:175], a[144:147], a[44:47], v[172:175]\n\t"
    "s_waitcnt lgkmcnt(12)\n\t" "v_mfma_f32_16x16x32_bf16 v[176:179], a[148:151], a[32:35], v[176:179]\n\t" "ds_read_b64_tr_b16 a[144:145], %[vr2] offset:14336\n\t"
    "s_load_dwordx2 s[98:99], s[84:85], 316\n\t" "s_add_u32 s84, s84, 384\n\t" "s_addc_u32 s85, s85, 0\n\t" "v_mfma_f32_16x16x32_bf16 v[180:183], a[148:151], a[36:39], v[180:183]\n\t"
    "ds_read_b64_tr_b16 a[146:147], %[vr2] offset:17920\n\t" "v_mfma_f32_16x16x32_bf16 v[184:187], a[148:151], a[40:43], v[184:187]\n\t" "s_add_u32 m0, %[sl], 46080\n\t"
    "v_mfma_f32_16x16x32_bf16 v[188:191], a[148:151], a[44:47], v[188:191]\n\t" "s_mov_b64 exec, s[82:83]\n\t" "global_load_lds_dwordx4 %[vb3], s[78:79]\n\t" "s_mov_b64 exec, -1\n\t"
    "s_add_u32 s78, s78, 229376\n\t" "s_addc_u32 s79, s79, 0\n\t" "s_waitcnt lgkmcnt(12)\n\t" "v_mfma_f32_16x16x32_bf16 v[192:195], a[152:155], a[32:35], v[192:195]\n\t"
    "ds_read_b64_tr_b16 a[148:149], %[vr2] offset:14368\n\t" "v_mfma_f32_16x16x32_bf16 v[196:199], a[152:155], a[36:39], v[196:199]\n\t" "ds_read_b64_tr_b16 a[150:151], %[vr2] offset:17952\n\t"
    "v_mfma_f32_16x16x32_bf16 v[200:203], a[152:155], a[40:43], v[200:203]\n\t" "v_mfma_f32_16x16x32_bf16 v[204:207], a[152:155], a[44:47], v[204:207]\n\t" "s_waitcnt lgkmcnt(12)\n\t"
    "v_mfma_f32_16x16x32_bf16 v[208:211], a[156:159], a[32:35], v[208:211]\n\t" "ds_read_b64_tr_b16 a[152:153], %[vr4] offset:14336\n\t"
    "v_mfma_f32_16x16x32_bf16 v[212:215], a[156:159], a[36:39], v[212:215]\n\t" "ds_read_b64_tr_b16 a[154:155], %[vr4] offset:17920\n\t"
    "v_mfma_f32_16x16x32_bf16 v[216:219], a[156:159], a[40:43], v[216:219]\n\t" "v_mfma_f32_16x16x32_bf16 v[220:223], a[156:159], a[44:47], v[220:223]\n\t" "s_waitcnt lgkmcnt(12)\n\t"
    "v_mfma_f32_16x16x32_bf16 v[224:227], a[128:131], a[32:35], v[224:227]\n\t" "ds_read_b64_tr_b16 a[156:157], %[vr4] offset:14368\n\t"
    "v_mfma_f32_16x16x32_bf16 v[228:231], a[128:131], a[36:39], v[228:231]\n\t" "ds_read_b64_tr_b16 a[158:159], %[vr4] offset:17952\n\t"
    "v_mfma_f32_16x16x32_bf16 v[232:235], a[128:131], a[40:43], v[232:235]\n\t" "v_mfma_f32_16x16x32_bf16 v[236:239], a[128:131], a[44:47], v[236:239]\n\t" "s_waitcnt lgkmcnt(12)\n\t"
    "v_mfma_f32_16x16x32_bf16 v[240:243], a[132:135], a[32:35], v[240:243]\n\t" "ds_read_b64_tr_b16 a[128:129], %[vr6] offset:14336\n\t"
    "v_mfma_f32_16x16x32_bf16 v[244:247], a[132:135], a[36:39], v[244:247]\n\t" "ds_read_b64_tr_b16 a[130:131], %[vr6] offset:17920\n\t"
    "v_mfma_f32_16x16x32_bf16 a[180:183], a[132:135], a[192:195], a[180:183]\n\t" "v_mfma_f32_16x16x32_bf16 v[248:251], a[132:135], a[40:43], v[248:251]\n\t"
    "v_mfma_f32_16x16x32_bf16 v[252:255], a[132:135], a[44:47], v[252:255]\n\t" "s_waitcnt lgkmcnt(12)\n\t" "v_mfma_f32_16x16x32_bf16 v[32:35], a[136:139], a[48:51], v[32:35]\n\t"
    "ds_read_b64_tr_b16 a[132:133], %[vr0] offset:21504\n\t" "v_mfma_f32_16x16x32_bf16 v[36:39], a[136:139], a[52:55], v[36:39]\n\t" "ds_read_b64_tr_b16 a[134:135], %[vr0] offset:25088\n\t"
    "v_mfma_f32_16x16x32_bf16 a[160:163], a[136:139], a[196:199], a[160:163]\n\t" "global_load_dwordx4 a[96:99], %[va], s[76:77] offset:0 sc1\n\t"
    "v_mfma_f32_16x16x32_bf16 v[40:43], a[136:139], a[56:59], v[40:43]\n\t" "v_mfma_f32_16x16x32_bf16 v[44:47], a[136:139], a[60:63], v[44:47]\n\t" "s_waitcnt lgkmcnt(12)\n\t"
    "v_mfma_f32_16x16x32_bf16 v[48:51], a[140:143], a[48:51], v[48:51]\n\t" "ds_read_b64_tr_b16 a[136:137], %[vr0] offset:21536\n\t" "s_load_dwordx2 s[98:99], s[84:85], 60\n\t"
    "v_mfma_f32_16x16x32_bf16 v[52:55], a[140:143], a[52:55], v[52:55]\n\t" "ds_read_b64_tr_b16 a[138:139], %[vr0] offset:25120\n\t"
    "v_mfma_f32_16x16x32_bf16 a[164:167], a[140:143], a[196:199], a[164:167]\n\t" "s_add_u32 m0, %[sl], 57344\n\t" "v_mfma_f32_16x16x32_bf16 v[56:59], a[140:143], a[56:59], v[56:59]\n\t"
    "global_load_lds_dwordx4 %[vb0], s[78:79]\n\t" "v_mfma_f32_16x16x32_bf16 v[60:63], a[140:143], a[60:63], v[60:63]\n\t" "s_waitcnt lgkmcnt(12)\n\t"
    "v_mfma_f32_16x16x32_bf16 v[64:67], a[144:147], a[48:51], v[64:67]\n\t" "ds_read_b64_tr_b16 a[140:141], %[vr2] offset:21504\n\t" "v_mfma_f32_16x16x32_bf16 v[68:71], a[144:147], a[52:55], v[68:71]\n\t"
    "ds_read_b64_tr_b16 a[142:143], %[vr2] offset:25088\n\t" "v_mfma_f32_16x16x32_bf16 v[72:75], a[144:147], a[56:59], v[72:75]\n\t" "v_mfma_f32_16x16x32_bf16 v[76:79], a[144:147], a[60:63], v[76:79]\n\t"
    "s_waitcnt lgkmcnt(12)\n\t" "v_mfma_f32_16x16x32_bf16 v[80:83], a[148:151], a[48:51], v[80:83]\n\t" "global_load_dwordx4 a[100:103], %[va], s[76:77] offset:1024 sc1\n\t"
    "ds_read_b64_tr_b16 a[144:145], %[vr2] offset:21536\n\t" "v_mfma_f32_16x16x32_bf16 v[84:87], a[148:151], a[52:55], v[84:87]\n\t" "ds_read_b64_tr_b16 a[146:147], %[vr2] offset:25120\n\t"
    "v_mfma_f32_16x16x32_bf16 v[88:91], a[148:151], a[56:59], v[88:91]\n\t" "v_mfma_f32_16x16x32_bf16 v[92:95], a[148:151], a[60:63], v[92:95]\n\t" "s_waitcnt lgkmcnt(12)\n\t"
    "v_mfma_f32_16x16x32_bf16 v[96:99], a[152:155], a[48:51], v[96:99]\n\t" "ds_read_b64_tr_b16 a[148:149], %[vr4] offset:21504\n\t"
    "v_mfma_f32_16x16x32_bf16 v[100:103], a[152:155], a[52:55], v[100:103]\n\t" "ds_read_b64_tr_b16 a[150:151], %[vr4] offset:25088\n\t" "s_add_u32 m0, %[sl], 58368\n\t"
    "v_mfma_f32_16x16x32_bf16 v[104:107], a[152:155], a[56:59], v[104:107]\n\t" "global_load_lds_dwordx4 %[vb1], s[78:79]\n\t" "v_mfma_f32_16x16x32_bf16 v[108:111], a[152:155], a[60:63], v[108:111]\n\t"
    "s_waitcnt lgkmcnt(12)\n\t" "v_mfma_f32_16x16x32_bf16 v[112:115], a[156:159], a[48:51], v[112:115]\n\t" "ds_read_b64_tr_b16 a[152:153], %[vr4] offset:21536\n\t"
    "v_mfma_f32_16x16x32_bf16 v[116:119], a[156:159], a[52:55], v[116:119]\n\t" "ds_read_b64_tr_b16 a[154:155], %[vr4] offset:25120\n\t" "s_load_dwordx2 s[98:99], s[84:85], 188\n\t"
    "v_mfma_f32_16x16x32_bf16 v[120:123], a[156:159], a[56:59], v[120:123]\n\t" "v_mfma_f32_16x16x32_bf16 v[124:127], a[156:159], a[60:63], v[124:127]\n\t" "s_waitcnt lgkmcnt(12)\n\t"
    "v_mfma_f32_16x16x32_bf16 v[128:131], a[128:131], a[48:51], v[128:131]\n\t" "global_load_dwordx4 a[104:107], %[va], s[76:77] offset:2048 sc1\n\t"
    "ds_read_b64_tr_b16 a[156:157], %[vr6] offset:21504\n\t" "v_mfma_f32_16x16x32_bf16 v[132:135], a[128:131], a[52:55], v[132:135]\n\t" "ds_read_b64_tr_b16 a[158:159], %[vr6] offset:25088\n\t"
    "v_mfma_f32_16x16x32_bf16 a[168:171], a[128:131], a[196:199], a[168:171]\n\t" "v_mfma_f32_16x16x32_bf16 v[136:139], a[128:131], a[56:59], v[136:139]\n\t"
    "v_mfma_f32_16x16x32_bf16 v[140:143], a[128:131], a[60:63], v[140:143]\n\t" "s_waitcnt lgkmcnt(12)\n\t" "v_mfma_f32_16x16x32_bf16 v[144:147], a[132:135], a[48:51], v[144:147]\n\t"
    "s_waitcnt vmcnt(13)\n\t" "s_barrier\n\t" "ds_read_b64_tr_b16 a[128:129], %[vr0] offset:28672\n\t" "s_add_u32 m0, %[sl], 59392\n\t"
    "v_mfma_f32_16x16x32_bf16 v[148:151], a[132:135], a[52:55], v[148:151]\n\t" "global_load_lds_dwordx4 %[vb2], s[78:79]\n\t" "ds_read_b64_tr_b16 a[130:131], %[vr0] offset:32256\n\t"
    "v_mfma_f32_16x16x32_bf16 a[172:175], a[132:135], a[196:199], a[172:175]\n\t" "v_mfma_f32_16x16x32_bf16 v[152:155], a[132:135], a[56:59], v[152:155]\n\t"
    "v_mfma_f32_16x16x32_bf16 v[156:159], a[132:135], a[60:63], v[156:159]\n\t" "s_waitcnt lgkmcnt(12)\n\t" "v_mfma_f32_16x16x32_bf16 v[160:163], a[136:139], a[48:51], v[160:163]\n\t"
    "ds_read_b64_tr_b16 a[132:133], %[vr0] offset:28704\n\t" "v_mfma_f32_16x16x32_bf16 v[164:167], a[136:139], a[52:55], v[164:167]\n\t" "ds_read_b64_tr_b16 a[134:135], %[vr0] offset:32288\n\t"
    "v_mfma_f32_16x16x32_bf16 a[176:179], a[136:139], a[196:199], a[176:179]\n\t" "global_load_dwordx4 a[108:111], %[va], s[76:77] offset:3072 sc1\n\t" "s_add_u32 s76, s76, 4096\n\t"
    "s_addc_u32 s77, s77, 0\n\t" "v_mfma_f32_16x16x32_bf16 v[168:171], a[136:139], a[56:59], v[168:171]\n\t" "v_mfma_f32_16x16x32_bf16 v[172:175], a[136:139], a[60:63], v[172:175]\n\t"
    "s_waitcnt lgkmcnt(12)\n\t" "v_mfma_f32_16x16x32_bf16 v[176:179], a[140:143], a[48:51], v[176:179]\n\t" "ds_read_b64_tr_b16 a[136:137], %[vr2] offset:28672\n\t"
    "s_load_dwordx2 s[98:99], s[84:85], 316\n\t" "s_add_u32 s84, s84, 384\n\t" "s_addc_u32 s85, s85, 0\n\t" "v_mfma_f32_16x16x32_bf16 v[180:183], a[140:143], a[52:55], v[180:183]\n\t"
    "ds_read_b64_tr_b16 a[138:139], %[vr2] offset:32256\n\t" "v_mfma_f32_16x16x32_bf16 v[184:187], a[140:143], a[56:59], v[184:187]\n\t" "s_add_u32 m0, %[sl], 60416\n\t"
    "v_mfma_f32_16x16x32_bf16 v[188:191], a[140:143], a[60:63], v[188:191]\n\t" "s_mov_b64 exec, s[82:83]\n\t" "global_load_lds_dwordx4 %[vb3], s[78:79]\n\t" "s_mov_b64 exec, -1\n\t"
    "s_add_u32 s78, s78, 229376\n\t" "s_addc_u32 s79, s79, 0\n\t" "s_waitcnt lgkmcnt(12)\n\t" "v_mfma_f32_16x16x32_bf16 v[192:195], a[144:147], a[48:51], v[192:195]\n\t"
    "ds_read_b64_tr_b16 a[140:141], %[vr2] offset:28704\n\t" "v_mfma_f32_16x16x32_bf16 v[196:199], a[144:147], a[52:55], v[196:199]\n\t" "ds_read_b64_tr_b16 a[142:143], %[vr2] offset:32288\n\t"
    "v_mfma_f32_16x16x32_bf16 v[200:203], a[144:147], a[56:59], v[200:203]\n\t" "v_mfma_f32_16x16x32_bf16 v[204:207], a[144:147], a[60:63], v[204:207]\n\t" "s_waitcnt lgkmcnt(12)\n\t"
    "v_mfma_f32_16x16x32_bf16 v[208:211], a[148:151], a[48:51], v[208:211]\n\t" "ds_read_b64_tr_b16 a[144:145], %[vr4] offset:28672\n\t"
    "v_mfma_f32_16x16x32_bf16 v[212:215], a[148:151], a[52:55], v[212:215]\n\t" "ds_read_b64_tr_b16 a[146:147], %[vr4] offset:32256\n\t"
    "v_mfma_f32_16x16x32_bf16 v[216:219], a[148:151], a[56:59], v[216:219]\n\t" "v_mfma_f32_16x16x32_bf16 v[220:223], a[148:151], a[60:63], v[220:223]\n\t" "s_waitcnt lgkmcnt(12)\n\t"
    "v_mfma_f32_16x16x32_bf16 v[224:227], a[152:155], a[48:51], v[224:227]\n\t" "ds_read_b64_tr_b16 a[148:149], %[vr4] offset:28704\n\t"
    "v_mfma_f32_16x16x32_bf16 v[228:231], a[152:155], a[52:55], v[228:231]\n\t" "ds_read_b64_tr_b16 a[150:151], %[vr4] offset:32288\n\t"
    "v_mfma_f32_16x16x32_bf16 v[232:235], a[152:155], a[56:59], v[232:235]\n\t" "v_mfma_f32_16x16x32_bf16 v[236:239], a[152:155], a[60:63], v[236:239]\n\t" "s_waitcnt lgkmcnt(12)\n\t"
    "v_mfma_f32_16x16x32_bf16 v[240:243], a[156:159], a[48:51], v[240:243]\n\t" "ds_read_b64_tr_b16 a[152:153], %[vr6] offset:28672\n\t"
    "v_mfma_f32_16x16x32_bf16 v[244:247], a[156:159], a[52:55], v[244:247]\n\t" "ds_read_b64_tr_b16 a[154:155], %[vr6] offset:32256\n\t"
    "v_mfma_f32_16x16x32_bf16 a[180:183], a[156:159], a[196:199], a[180:183]\n\t" "v_mfma_f32_16x16x32_bf16 v[248:251], a[156:159], a[56:59], v[248:251]\n\t"
    "v_mfma_f32_16x16x32_bf16 v[252:255], a[156:159], a[60:63], v[252:255]\n\t" "v_accvgpr_read_b32 v31, %[cp]\n\t" "s_nop 7\n\t" "s_mov_b64 exec, %[m01]\n\t"
    "global_store_dwordx4 v31, a[160:163], %[su0] offset:0 sc1\n\t" "global_store_dwordx4 v31, a[164:167], %[su0] offset:2304 sc1\n\t" "global_store_dwordx4 v31, a[172:175], %[su0] offset:1152 sc1\n\t"
    "global_store_dwordx4 v31, a[176:179], %[su0] offset:3456 sc1\n\t" "s_mov_b64 exec, %[m6]\n\t" "global_store_dwordx4 v31, a[168:171], %[su6] offset:0 sc1\n\t"
    "global_store_dwordx4 v31, a[180:183], %[su6] offset:1152 sc1\n\t" "s_mov_b64 exec, -1\n\t" "s_branch .Ld0_%=\n\t" ".LE1_%=:\n\t" "s_waitcnt lgkmcnt(12)\n\t"
    "v_mfma_f32_16x16x32_bf16 v[32:35], a[128:131], a[64:67], v[32:35]\n\t" "ds_read_b64_tr_b16 a[156:157], %[vr0] offset:35840\n\t" "v_mfma_f32_16x16x32_bf16 v[36:39], a[128:131], a[68:71], v[36:39]\n\t"
    "ds_read_b64_tr_b16 a[158:159], %[vr0] offset:39424\n\t" "v_mfma_f32_16x16x32_bf16 a[160:163], a[128:131], a[184:187], 0\n\t" "global_load_dwordx4 a[112:115], %[va], s[76:77] offset:0 sc1\n\t"
    "v_mfma_f32_16x16x32_bf16 v[40:43], a[128:131], a[72:75], v[40:43]\n\t" "v_mfma_f32_16x16x32_bf16 v[44:47], a[128:131], a[76:79], v[44:47]\n\t" "s_waitcnt lgkmcnt(12)\n\t"
    "v_mfma_f32_16x16x32_bf16 v[48:51], a[132:135], a[64:67], v[48:51]\n\t" "ds_read_b64_tr_b16 a[128:129], %[vr0] offset:35872\n\t" "s_load_dwordx2 s[98:99], s[84:85], 60\n\t"
    "v_mfma_f32_16x16x32_bf16 v[52:55], a[132:135], a[68:71], v[52:55]\n\t" "ds_read_b64_tr_b16 a[130:131], %[vr0] offset:39456\n\t" "v_mfma_f32_16x16x32_bf16 a[164:167], a[132:135], a[184:187], 0\n\t"
    "s_add_u32 m0, %[sl], 71680\n\t" "v_mfma_f32_16x16x32_bf16 v[56:59], a[132:135], a[72:75], v[56:59]\n\t" "global_load_lds_dwordx4 %[vb0], s[78:79]\n\t"
    "v_mfma_f32_16x16x32_bf16 v[60:63], a[132:135], a[76:79], v[60:63]\n\t" "s_waitcnt lgkmcnt(12)\n\t" "v_mfma_f32_16x16x32_bf16 v[64:67], a[136:139], a[64:67], v[64:67]\n\t"
    "ds_read_b64_tr_b16 a[132:133], %[vr2] offset:35840\n\t" "v_mfma_f32_16x16x32_bf16 v[68:71], a[136:139], a[68:71], v[68:71]\n\t" "ds_read_b64_tr_b16 a[134:135], %[vr2] offset:39424\n\t"
    "v_mfma_f32_16x16x32_bf16 v[72:75], a[136:139], a[72:75], v[72:75]\n\t" "v_mfma_f32_16x16x32_bf16 v[76:79], a[136:139], a[76:79], v[76:79]\n\t" "s_waitcnt lgkmcnt(12)\n\t"
    "v_mfma_f32_16x16x32_bf16 v[80:83], a[140:143], a[64:67], v[80:83]\n\t" "global_load_dwordx4 a[116:119], %[va], s[76:77] offset:1024 sc1\n\t" "ds_read_b64_tr_b16 a[136:137], %[vr2] offset:35872\n\t"
    "v_mfma_f32_16x16x32_bf16 v[84:87], a[140:143], a[68:71], v[84:87]\n\t" "ds_read_b64_tr_b16 a[138:139], %[vr2] offset:39456\n\t" "v_mfma_f32_16x16x32_bf16 v[88:91], a[140:143], a[72:75], v[88:91]\n\t"
    "v_mfma_f32_16x16x32_bf16 v[92:95], a[140:143], a[76:79], v[92:95]\n\t" "s_waitcnt lgkmcnt(12)\n\t" "v_mfma_f32_16x16x32_bf16 v[96:99], a[144:147], a[64:67], v[96:99]\n\t"
    "ds_read_b64_tr_b16 a[140:141], %[vr4] offset:35840\n\t" "v_mfma_f32_16x16x32_bf16 v[100:103], a[144:147], a[68:71], v[100:103]\n\t" "ds_read_b64_tr_b16 a[142:143], %[vr4] offset:39424\n\t"
    "s_add_u32 m0, %[sl], 72704\n\t" "v_mfma_f32_16x16x32_bf16 v[104:107], a[144:147], a[72:75], v[104:107]\n\t" "global_load_lds_dwordx4 %[vb1], s[78:79]\n\t"
    "v_mfma_f32_16x16x32_bf16 v[108:111], a[144:147], a[76:79], v[108:111]\n\t" "s_waitcnt lgkmcnt(12)\n\t" "v_mfma_f32_16x16x32_bf16 v[112:115], a[148:151], a[64:67], v[112:115]\n\t"
    "ds_read_b64_tr_b16 a[144:145], %[vr4] offset:35872\n\t" "v_mfma_f32_16x16x32_bf16 v[116:119], a[148:151], a[68:71], v[116:119]\n\t" "ds_read_b64_tr_b16 a[146:147], %[vr4] offset:39456\n\t"
    "s_load_dwordx2 s[98:99], s[84:85], 188\n\t" "v_mfma_f32_16x16x32_bf16 v[120:123], a[148:151], a[72:75], v[120:123]\n\t" "v_mfma_f32_16x16x32_bf16 v[124:127], a[148:151], a[76:79], v[124:127]\n\t"
    "s_waitcnt lgkmcnt(12)\n\t" "v_mfma_f32_16x16x32_bf16 v[128:131], a[152:155], a[64:67], v[128:131]\n\t" "global_load_dwordx4 a[120:123], %[va], s[76:77] offset:2048 sc1\n\t"
    "ds_read_b64_tr_b16 a[148:149], %[vr6] offset:35840\n\t" "v_mfma_f32_16x16x32_bf16 v[132:135], a[152:155], a[68:71], v[132:135]\n\t" "ds_read_b64_tr_b16 a[150:151], %[vr6] offset:39424\n\t"
    "v_mfma_f32_16x16x32_bf16 a[168:171], a[152:155], a[184:187], 0\n\t" "v_mfma_f32_16x16x32_bf16 v[136:139], a[152:155], a[72:75], v[136:139]\n\t"
    "v_mfma_f32_16x16x32_bf16 v[140:143], a[152:155], a[76:79], v[140:143]\n\t" "s_waitcnt lgkmcnt(12)\n\t" "v_mfma_f32_16x16x32_bf16 v[144:147], a[156:159], a[64:67], v[144:147]\n\t"
    "s_waitcnt vmcnt(13)\n\t" "s_barrier\n\t" "ds_read_b64_tr_b16 a[152:153], %[vr0] offset:43008\n\t" "s_add_u32 m0, %[sl], 73728\n\t"
    "v_mfma_f32_16x16x32_bf16 v[148:151], a[156:159], a[68:71], v[148:151]\n\t" "global_load_lds_dwordx4 %[vb2], s[78:79]\n\t" "ds_read_b64_tr_b16 a[154:155], %[vr0] offset:46592\n\t"
    "v_mfma_f32_16x16x32_bf16 a[172:175], a[156:159], a[184:187], 0\n\t" "v_mfma_f32_16x16x32_bf16 v[152:155], a[156:159], a[72:75], v[152:155]\n\t"
    "v_mfma_f32_16x16x32_bf16 v[156:159], a[156:159], a[76:79], v[156:159]\n\t" "s_waitcnt lgkmcnt(12)\n\t" "v_mfma_f32_16x16x32_bf16 v[160:163], a[128:131], a[64:67], v[160:163]\n\t"
    "ds_read_b64_tr_b16 a[156:157], %[vr0] offset:43040\n\t" "v_mfma_f32_16x16x32_bf16 v[164:167], a[128:131], a[68:71], v[164:167]\n\t" "ds_read_b64_tr_b16 a[158:159], %[vr0] offset:46624\n\t"
    "v_mfma_f32_16x16x32_bf16 a[176:179], a[128:131], a[184:187], 0\n\t" "global_load_dwordx4 a[124:127], %[va], s[76:77] offset:3072 sc1\n\t" "s_add_u32 s76, s76, s86\n\t" "s_addc_u32 s77, s77, s87\n\t"
    "v_mfma_f32_16x16x32_bf16 v[168:171], a[128:131], a[72:75], v[168:171]\n\t" "v_mfma_f32_16x16x32_bf16 v[172:175], a[128:131], a[76:79], v[172:175]\n\t" "s_waitcnt lgkmcnt(12)\n\t"
    "v_mfma_f32_16x16x32_bf16 v[176:179], a[132:135], a[64:67], v[176:179]\n\t" "ds_read_b64_tr_b16 a[128:129], %[vr2] offset:43008\n\t" "s_load_dwordx2 s[98:99], s[84:85], 316\n\t"
    "s_add_u32 s84, s84, 384\n\t" "s_addc_u32 s85, s85, 0\n\t" "v_mfma_f32_16x16x32_bf16 v[180:183], a[132:135], a[68:71], v[180:183]\n\t" "ds_read_b64_tr_b16 a[130:131], %[vr2] offset:46592\n\t"
    "v_mfma_f32_16x16x32_bf16 v[184:187], a[132:135], a[72:75], v[184:187]\n\t" "s_add_u32 m0, %[sl], 74752\n\t" "v_mfma_f32_16x16x32_bf16 v[188:191], a[132:135], a[76:79], v[188:191]\n\t"
    "s_mov_b64 exec, s[82:83]\n\t" "global_load_lds_dwordx4 %[vb3], s[78:79]\n\t" "s_mov_b64 exec, -1\n\t" "s_add_u32 s78, s78, s88\n\t" "s_addc_u32 s79, s79, s89\n\t" "s_waitcnt lgkmcnt(12)\n\t"
    "v_mfma_f32_16x16x32_bf16 v[192:195], a[136:139], a[64:67], v[192:195]\n\t" "ds_read_b64_tr_b16 a[132:133], %[vr2] offset:43040\n\t"
    "v_mfma_f32_16x16x32_bf16 v[196:199], a[136:139], a[68:71], v[196:199]\n\t" "ds_read_b64_tr_b16 a[134:135], %[vr2] offset:46624\n\t"
    "v_mfma_f32_16x16x32_bf16 v[200:203], a[136:139], a[72:75], v[200:203]\n\t" "v_mfma_f32_16x16x32_bf16 v[204:207], a[136:139], a[76:79], v[204:207]\n\t" "s_waitcnt lgkmcnt(12)\n\t"
    "v_mfma_f32_16x16x32_bf16 v[208:211], a[140:143], a[64:67], v[208:211]\n\t" "ds_read_b64_tr_b16 a[136:137], %[vr4] offset:43008\n\t"
    "v_mfma_f32_16x16x32_bf16 v[212:215], a[140:143], a[68:71], v[212:215]\n\t" "ds_read_b64_tr_b16 a[138:139], %[vr4] offset:46592\n\t"
    "v_mfma_f32_16x16x32_bf16 v[216:219], a[140:143], a[72:75], v[216:219]\n\t" "v_mfma_f32_16x16x32_bf16 v[220:223], a[140:143], a[76:79], v[220:223]\n\t" "s_waitcnt lgkmcnt(12)\n\t"
    "v_mfma_f32_16x16x32_bf16 v[224:227], a[144:147], a[64:67], v[224:227]\n\t" "ds_read_b64_tr_b16 a[140:141], %[vr4] offset:43040\n\t"
    "v_mfma_f32_16x16x32_bf16 v[228:231], a[144:147], a[68:71], v[228:231]\n\t" "ds_read_b64_tr_b16 a[142:143], %[vr4] offset:46624\n\t"
    "v_mfma_f32_16x16x32_bf16 v[232:235], a[144:147], a[72:75], v[232:235]\n\t" "v_mfma_f32_16x16x32_bf16 v[236:239], a[144:147], a[76:79], v[236:239]\n\t" "s_waitcnt lgkmcnt(12)\n\t"
    "v_mfma_f32_16x16x32_bf16 v[240:243], a[148:151], a[64:67], v[240:243]\n\t" "ds_read_b64_tr_b16 a[144:145], %[vr6] offset:43008\n\t"
    "v_mfma_f32_16x16x32_bf16 v[244:247], a[148:151], a[68:71], v[244:247]\n\t" "ds_read_b64_tr_b16 a[146:147], %[vr6] offset:46592\n\t"
    "v_mfma_f32_16x16x32_bf16 a[180:183], a[148:151], a[184:187], 0\n\t" "v_mfma_f32_16x16x32_bf16 v[248:251], a[148:151], a[72:75], v[248:251]\n\t"
    "v_mfma_f32_16x16x32_bf16 v[252:255], a[148:151], a[76:79], v[252:255]\n\t" "s_waitcnt lgkmcnt(12)\n\t" "v_mfma_f32_16x16x32_bf16 v[32:35], a[152:155], a[80:83], v[32:35]\n\t"
    "ds_read_b64_tr_b16 a[148:149], %[vr0] offset:50176\n\t" "v_mfma_f32_16x16x32_bf16 v[36:39], a[152:155], a[84:87], v[36:39]\n\t" "ds_read_b64_tr_b16 a[150:151], %[vr0] offset:53760\n\t"
    "v_mfma_f32_16x16x32_bf16 a[160:163], a[152:155], a[188:191], a[160:163]\n\t" "global_load_dwordx4 a[0:3], %[va], s[76:77] offset:0 sc1\n\t"
    "v_mfma_f32_16x16x32_bf16 v[40:43], a[152:155], a[88:91], v[40:43]\n\t" "v_mfma_f32_16x16x32_bf16 v[44:47], a[152:155], a[92:95], v[44:47]\n\t" "s_waitcnt lgkmcnt(12)\n\t"
    "v_mfma_f32_16x16x32_bf16 v[48:51], a[156:159], a[80:83], v[48:51]\n\t" "ds_read_b64_tr_b16 a[152:153], %[vr0] offset:50208\n\t" "s_load_dwordx2 s[98:99], s[84:85], 60\n\t"
    "v_mfma_f32_16x16x32_bf16 v[52:55], a[156:159], a[84:87], v[52:55]\n\t" "ds_read_b64_tr_b16 a[154:155], %[vr0] offset:53792\n\t"
    "v_mfma_f32_16x16x32_bf16 a[164:167], a[156:159], a[188:191], a[164:167]\n\t" "s_add_u32 m0, %[sl], 86016\n\t" "v_mfma_f32_16x16x32_bf16 v[56:59], a[156:159], a[88:91], v[56:59]\n\t"
    "global_load_lds_dwordx4 %[vb0], s[78:79]\n\t" "v_mfma_f32_16x16x32_bf16 v[60:63], a[156:159], a[92:95], v[60:63]\n\t" "s_waitcnt lgkmcnt(12)\n\t"
    "v_mfma_f32_16x16x32_bf16 v[64:67], a[128:131], a[80:83], v[64:67]\n\t" "ds_read_b64_tr_b16 a[156:157], %[vr2] offset:50176\n\t" "v_mfma_f32_16x16x32_bf16 v[68:71], a[128:131], a[84:87], v[68:71]\n\t"
    "ds_read_b64_tr_b16 a[158:159], %[vr2] offset:53760\n\t" "v_mfma_f32_16x16x32_bf16 v[72:75], a[128:131], a[88:91], v[72:75]\n\t" "v_mfma_f32_16x16x32_bf16 v[76:79], a[128:131], a[92:95], v[76:79]\n\t"
    "s_waitcnt lgkmcnt(12)\n\t" "v_mfma_f32_16x16x32_bf16 v[80:83], a[132:135], a[80:83], v[80:83]\n\t" "global_load_dwordx4 a[4:7], %[va], s[76:77] offset:1024 sc1\n\t"
    "ds_read_b64_tr_b16 a[128:129], %[vr2] offset:50208\n\t" "v_mfma_f32_16x16x32_bf16 v[84:87], a[132:135], a[84:87], v[84:87]\n\t" "ds_read_b64_tr_b16 a[130:131], %[vr2] offset:53792\n\t"
    "v_mfma_f32_16x16x32_bf16 v[88:91], a[132:135], a[88:91], v[88:91]\n\t" "v_mfma_f32_16x16x32_bf16 v[92:95], a[132:135], a[92:95], v[92:95]\n\t" "s_waitcnt lgkmcnt(12)\n\t"
    "v_mfma_f32_16x16x32_bf16 v[96:99], a[136:139], a[80:83], v[96:99]\n\t" "ds_read_b64_tr_b16 a[132:133], %[vr4] offset:50176\n\t"
    "v_mfma_f32_16x16x32_bf16 v[100:103], a[136:139], a[84:87], v[100:103]\n\t" "ds_read_b64_tr_b16 a[134:135], %[vr4] offset:53760\n\t" "s_add_u32 m0, %[sl], 87040\n\t"
    "v_mfma_f32_16x16x32_bf16 v[104:107], a[136:139], a[88:91], v[104:107]\n\t" "global_load_lds_dwordx4 %[vb1], s[78:79]\n\t" "v_mfma_f32_16x16x32_bf16 v[108:111], a[136:139], a[92:95], v[108:111]\n\t"
    "s_waitcnt lgkmcnt(12)\n\t" "v_mfma_f32_16x16x32_bf16 v[112:115], a[140:143], a[80:83], v[112:115]\n\t" "ds_read_b64_tr_b16 a[136:137], %[vr4] offset:50208\n\t"
    "v_mfma_f32_16x16x32_bf16 v[116:119], a[140:143], a[84:87], v[116:119]\n\t" "ds_read_b64_tr_b16 a[138:139], %[vr4] offset:53792\n\t" "s_load_dwordx2 s[98:99], s[84:85], 188\n\t"
    "v_mfma_f32_16x16x32_bf16 v[120:123], a[140:143], a[88:91], v[120:123]\n\t" "v_mfma_f32_16x16x32_bf16 v[124:127], a[140:143], a[92:95], v[124:127]\n\t" "s_waitcnt lgkmcnt(12)\n\t"
    "v_mfma_f32_16x16x32_bf16 v[128:131], a[144:147], a[80:83], v[128:131]\n\t" "global_load_dwordx4 a[8:11], %[va], s[76:77] offset:2048 sc1\n\t" "ds_read_b64_tr_b16 a[140:141], %[vr6] offset:50176\n\t"
    "v_mfma_f32_16x16x32_bf16 v[132:135], a[144:147], a[84:87], v[132:135]\n\t" "ds_read_b64_tr_b16 a[142:143], %[vr6] offset:53760\n\t"
    "v_mfma_f32_16x16x32_bf16 a[168:171], a[144:147], a[188:191], a[168:171]\n\t" "v_mfma_f32_16x16x32_bf16 v[136:139], a[144:147], a[88:91], v[136:139]\n\t"
    "v_mfma_f32_16x16x32_bf16 v[140:143], a[144:147], a[92:95], v[140:143]\n\t" "s_waitcnt lgkmcnt(12)\n\t" "v_mfma_f32_16x16x32_bf16 v[144:147], a[148:151], a[80:83], v[144:147]\n\t"
    "s_waitcnt vmcnt(13)\n\t" "s_barrier\n\t" "ds_read_b64_tr_b16 a[144:145], %[vr1] offset:0\n\t" "s_add_u32 m0, %[sl], 88064\n\t"
    "v_mfma_f32_16x16x32_bf16 v[148:151], a[148:151], a[84:87], v[148:151]\n\t" "global_load_lds_dwordx4 %[vb2], s[78:79]\n\t" "ds_read_b64_tr_b16 a[146:147], %[vr1] offset:3584\n\t"
    "v_mfma_f32_16x16x32_bf16 a[172:175], a[148:151], a[188:191], a[172:175]\n\t" "v_mfma_f32_16x16x32_bf16 v[152:155], a[148:151], a[88:91], v[152:155]\n\t"
    "v_mfma_f32_16x16x32_bf16 v[156:159], a[148:151], a[92:95], v[156:159]\n\t" "s_waitcnt lgkmcnt(12)\n\t" "v_mfma_f32_16x16x32_bf16 v[160:163], a[152:155], a[80:83], v[160:163]\n\t"
    "ds_read_b64_tr_b16 a[148:149], %[vr1] offset:32\n\t" "v_mfma_f32_16x16x32_bf16 v[164:167], a[152:155], a[84:87], v[164:167]\n\t" "ds_read_b64_tr_b16 a[150:151], %[vr1] offset:3616\n\t"
    "v_mfma_f32_16x16x32_bf16 a[176:179], a[152:155], a[188:191], a[176:179]\n\t" "global_load_dwordx4 a[12:15], %[va], s[76:77] offset:3072 sc1\n\t" "s_add_u32 s76, s76, 4096\n\t"
    "s_addc_u32 s77, s77, 0\n\t" "v_mfma_f32_16x16x32_bf16 v[168:171], a[152:155], a[88:91], v[168:171]\n\t" "v_mfma_f32_16x16x32_bf16 v[172:175], a[152:155], a[92:95], v[172:175]\n\t"
    "s_waitcnt lgkmcnt(12)\n\t" "v_mfma_f32_16x16x32_bf16 v[176:179], a[156:159], a[80:83], v[176:179]\n\t" "ds_read_b64_tr_b16 a[152:153], %[vr3] offset:0\n\t"
    "s_load_dwordx2 s[98:99], s[84:85], 316\n\t" "s_add_u32 s84, s84, 384\n\t" "s_addc_u32 s85, s85, 0\n\t" "v_mfma_f32_16x16x32_bf16 v[180:183], a[156:159], a[84:87], v[180:183]\n\t"
    "ds_read_b64_tr_b16 a[154:155], %[vr3] offset:3584\n\t" "v_mfma_f32_16x16x32_bf16 v[184:187], a[156:159], a[88:91], v[184:187]\n\t" "s_add_u32 m0, %[sl], 89088\n\t"
    "v_mfma_f32_16x16x32_bf16 v[188:191], a[156:159], a[92:95], v[188:191]\n\t" "s_mov_b64 exec, s[82:83]\n\t" "global_load_lds_dwordx4 %[vb3], s[78:79]\n\t" "s_mov_b64 exec, -1\n\t"
    "s_add_u32 s78, s78, 229376\n\t" "s_addc_u32 s79, s79, 0\n\t" "s_waitcnt lgkmcnt(12)\n\t" "v_mfma_f32_16x16x32_bf16 v[192:195], a[128:131], a[80:83], v[192:195]\n\t"
    "ds_read_b64_tr_b16 a[156:157], %[vr3] offset:32\n\t" "v_mfma_f32_16x16x32_bf16 v[196:199], a[128:131], a[84:87], v[196:199]\n\t" "ds_read_b64_tr_b16 a[158:159], %[vr3] offset:3616\n\t"
    "v_mfma_f32_16x16x32_bf16 v[200:203], a[128:131], a[88:91], v[200:203]\n\t" "v_mfma_f32_16x16x32_bf16 v[204:207], a[128:131], a[92:95], v[204:207]\n\t" "s_waitcnt lgkmcnt(12)\n\t"
    "v_mfma_f32_16x16x32_bf16 v[208:211], a[132:135], a[80:83], v[208:211]\n\t" "ds_read_b64_tr_b16 a[128:129], %[vr5] offset:0\n\t"
    "v_mfma_f32_16x16x32_bf16 v[212:215], a[132:135], a[84:87], v[212:215]\n\t" "ds_read_b64_tr_b16 a[130:131], %[vr5] offset:3584\n\t"
    "v_mfma_f32_16x16x32_bf16 v[216:219], a[132:135], a[88:91], v[216:219]\n\t" "v_mfma_f32_16x16x32_bf16 v[220:223], a[132:135], a[92:95], v[220:223]\n\t" "s_waitcnt lgkmcnt(12)\n\t"
    "v_mfma_f32_16x16x32_bf16 v[224:227], a[136:139], a[80:83], v[224:227]\n\t" "ds_read_b64_tr_b16 a[132:133], %[vr5] offset:32\n\t"
    "v_mfma_f32_16x16x32_bf16 v[228:231], a[136:139], a[84:87], v[228:231]\n\t" "ds_read_b64_tr_b16 a[134:135], %[vr5] offset:3616\n\t"
    "v_mfma_f32_16x16x32_bf16 v[232:235], a[136:139], a[88:91], v[232:235]\n\t" "v_mfma_f32_16x16x32_bf16 v[236:239], a[136:139], a[92:95], v[236:239]\n\t" "s_waitcnt lgkmcnt(12)\n\t"
    "v_mfma_f32_16x16x32_bf16 v[240:243], a[140:143], a[80:83], v[240:243]\n\t" "ds_read_b64_tr_b16 a[136:137], %[vr7] offset:0\n\t"
    "v_mfma_f32_16x16x32_bf16 v[244:247], a[140:143], a[84:87], v[244:247]\n\t" "ds_read_b64_tr_b16 a[138:139], %[vr7] offset:3584\n\t"
    "v_mfma_f32_16x16x32_bf16 a[180:183], a[140:143], a[188:191], a[180:183]\n\t" "v_mfma_f32_16x16x32_bf16 v[248:251], a[140:143], a[88:91], v[248:251]\n\t"
    "v_mfma_f32_16x16x32_bf16 v[252:255], a[140:143], a[92:95], v[252:255]\n\t" "s_waitcnt lgkmcnt(12)\n\t" "v_mfma_f32_16x16x32_bf16 v[32:35], a[144:147], a[96:99], v[32:35]\n\t"
    "ds_read_b64_tr_b16 a[140:141], %[vr1] offset:7168\n\t" "v_mfma_f32_16x16x32_bf16 v[36:39], a[144:147], a[100:103], v[36:39]\n\t" "ds_read_b64_tr_b16 a[142:143], %[vr1] offset:10752\n\t"
    "v_mfma_f32_16x16x32_bf16 a[160:163], a[144:147], a[192:195], a[160:163]\n\t" "global_load_dwordx4 a[16:19], %[va], s[76:77] offset:0 sc1\n\t"
    "v_mfma_f32_16x16x32_bf16 v[40:43], a[144:147], a[104:107], v[40:43]\n\t" "v_mfma_f32_16x16x32_bf16 v[44:47], a[144:147], a[108:111], v[44:47]\n\t" "s_waitcnt lgkmcnt(12)\n\t"
    "v_mfma_f32_16x16x32_bf16 v[48:51], a[148:151], a[96:99], v[48:51]\n\t" "ds_read_b64_tr_b16 a[144:145], %[vr1] offset:7200\n\t" "s_load_dwordx2 s[98:99], s[84:85], 60\n\t"
    "v_mfma_f32_16x16x32_bf16 v[52:55], a[148:151], a[100:103], v[52:55]\n\t" "ds_read_b64_tr_b16 a[146:147], %[vr1] offset:10784\n\t"
    "v_mfma_f32_16x16x32_bf16 a[164:167], a[148:151], a[192:195], a[164:167]\n\t" "s_add_u32 m0, %[sl], 100352\n\t" "v_mfma_f32_16x16x32_bf16 v[56:59], a[148:151], a[104:107], v[56:59]\n\t"
    "global_load_lds_dwordx4 %[vb0], s[78:79]\n\t" "v_mfma_f32_16x16x32_bf16 v[60:63], a[148:151], a[108:111], v[60:63]\n\t" "s_waitcnt lgkmcnt(12)\n\t"
    "v_mfma_f32_16x16x32_bf16 v[64:67], a[152:155], a[96:99], v[64:67]\n\t" "ds_read_b64_tr_b16 a[148:149], %[vr3] offset:7168\n\t"
    "v_mfma_f32_16x16x32_bf16 v[68:71], a[152:155], a[100:103], v[68:71]\n\t" "ds_read_b64_tr_b16 a[150:151], %[vr3] offset:10752\n\t"
    "v_mfma_f32_16x16x32_bf16 v[72:75], a[152:155], a[104:107], v[72:75]\n\t" "v_mfma_f32_16x16x32_bf16 v[76:79], a[152:155], a[108:111], v[76:79]\n\t" "s_waitcnt lgkmcnt(12)\n\t"
    "v_mfma_f32_16x16x32_bf16 v[80:83], a[156:159], a[96:99], v[80:83]\n\t" "global_load_dwordx4 a[20:23], %[va], s[76:77] offset:1024 sc1\n\t" "ds_read_b64_tr_b16 a[152:153], %[vr3] offset:7200\n\t"
    "v_mfma_f32_16x16x32_bf16 v[84:87], a[156:159], a[100:103], v[84:87]\n\t" "ds_read_b64_tr_b16 a[154:155], %[vr3] offset:10784\n\t"
    "v_mfma_f32_16x16x32_bf16 v[88:91], a[156:159], a[104:107], v[88:91]\n\t" "v_mfma_f32_16x16x32_bf16 v[92:95], a[156:159], a[108:111], v[92:95]\n\t" "s_waitcnt lgkmcnt(12)\n\t"
    "v_mfma_f32_16x16x32_bf16 v[96:99], a[128:131], a[96:99], v[96:99]\n\t" "ds_read_b64_tr_b16 a[156:157], %[vr5] offset:7168\n\t"
    "v_mfma_f32_16x16x32_bf16 v[100:103], a[128:131], a[100:103], v[100:103]\n\t" "ds_read_b64_tr_b16 a[158:159], %[vr5] offset:10752\n\t" "s_add_u32 m0, %[sl], 101376\n\t"
    "v_mfma_f32_16x16x32_bf16 v[104:107], a[128:131], a[104:107], v[104:107]\n\t" "global_load_lds_dwordx4 %[vb1], s[78:79]\n\t"
    "v_mfma_f32_16x16x32_bf16 v[108:111], a[128:131], a[108:111], v[108:111]\n\t" "s_waitcnt lgkmcnt(12)\n\t" "v_mfma_f32_16x16x32_bf16 v[112:115], a[132:135], a[96:99], v[112:115]\n\t"
    "ds_read_b64_tr_b16 a[128:129], %[vr5] offset:7200\n\t" "v_mfma_f32_16x16x32_bf16 v[116:119], a[132:135], a[100:103], v[116:119]\n\t" "ds_read_b64_tr_b16 a[130:131], %[vr5] offset:10784\n\t"
    "s_load_dwordx2 s[98:99], s[84:85], 188\n\t" "v_mfma_f32_16x16x32_bf16 v[120:123], a[132:135], a[104:107], v[120:123]\n\t" "v_mfma_f32_16x16x32_bf16 v[124:127], a[132:135], a[108:111], v[124:127]\n\t"
    "s_waitcnt lgkmcnt(12)\n\t" "v_mfma_f32_16x16x32_bf16 v[128:131], a[136:139], a[96:99], v[128:131]\n\t" "global_load_dwordx4 a[24:27], %[va], s[76:77] offset:2048 sc1\n\t"
    "ds_read_b64_tr_b16 a[132:133], %[vr7] offset:7168\n\t" "v_mfma_f32_16x16x32_bf16 v[132:135], a[136:139], a[100:103], v[132:135]\n\t" "ds_read_b64_tr_b16 a[134:135], %[vr7] offset:10752\n\t"
    "v_mfma_f32_16x16x32_bf16 a[168:171], a[136:139], a[192:195], a[168:171]\n\t" "v_mfma_f32_16x16x32_bf16 v[136:139], a[136:139], a[104:107], v[136:139]\n\t"
    "v_mfma_f32_16x16x32_bf16 v[140:143], a[136:139], a[108:111], v[140:143]\n\t" "s_waitcnt lgkmcnt(12)\n\t" "v_mfma_f32_16x16x32_bf16 v[144:147], a[140:143], a[96:99], v[144:147]\n\t"
    "s_waitcnt vmcnt(13)\n\t" "s_barrier\n\t" "ds_read_b64_tr_b16 a[136:137], %[vr1] offset:14336\n\t" "s_add_u32 m0, %[sl], 102400\n\t"
    "v_mfma_f32_16x16x32_bf16 v[148:151], a[140:143], a[100:103], v[148:151]\n\t" "global_load_lds_dwordx4 %[vb2], s[78:79]\n\t" "ds_read_b64_tr_b16 a[138:139], %[vr1] offset:17920\n\t"
    "v_mfma_f32_16x16x32_bf16 a[172:175], a[140:143], a[192:195], a[172:175]\n\t" "v_mfma_f32_16x16x32_bf16 v[152:155], a[140:143], a[104:107], v[152:155]\n\t"
    "v_mfma_f32_16x16x32_bf16 v[156:159], a[140:143], a[108:111], v[156:159]\n\t" "s_waitcnt lgkmcnt(12)\n\t" "v_mfma_f32_16x16x32_bf16 v[160:163], a[144:147], a[96:99], v[160:163]\n\t"
    "ds_read_b64_tr_b16 a[140:141], %[vr1] offset:14368\n\t" "v_mfma_f32_16x16x32_bf16 v[164:167], a[144:147], a[100:103], v[164:167]\n\t" "ds_read_b64_tr_b16 a[142:143], %[vr1] offset:17952\n\t"
    "v_mfma_f32_16x16x32_bf16 a[176:179], a[144:147], a[192:195], a[176:179]\n\t" "global_load_dwordx4 a[28:31], %[va], s[76:77] offset:3072 sc1\n\t" "s_add_u32 s76, s76, 4096\n\t"
    "s_addc_u32 s77, s77, 0\n\t" "v_mfma_f32_16x16x32_bf16 v[168:171], a[144:147], a[104:107], v[168:171]\n\t" "v_mfma_f32_16x16x32_bf16 v[172:175], a[144:147], a[108:111], v[172:175]\n\t"
    "s_waitcnt lgkmcnt(12)\n\t" "v_mfma_f32_16x16x32_bf16 v[176:179], a[148:151], a[96:99], v[176:179]\n\t" "ds_read_b64_tr_b16 a[144:145], %[vr3] offset:14336\n\t"
    "s_load_dwordx2 s[98:99], s[84:85], 316\n\t" "s_add_u32 s84, s84, 384\n\t" "s_addc_u32 s85, s85, 0\n\t" "v_mfma_f32_16x16x32_bf16 v[180:183], a[148:151], a[100:103], v[180:183]\n\t"
    "ds_read_b64_tr_b16 a[146:147], %[vr3] offset:17920\n\t" "v_mfma_f32_16x16x32_bf16 v[184:187], a[148:151], a[104:107], v[184:187]\n\t" "s_add_u32 m0, %[sl], 103424\n\t"
    "v_mfma_f32_16x16x32_bf16 v[188:191], a[148:151], a[108:111], v[188:191]\n\t" "s_mov_b64 exec, s[82:83]\n\t" "global_load_lds_dwordx4 %[vb3], s[78:79]\n\t" "s_mov_b64 exec, -1\n\t"
    "s_add_u32 s78, s78, 229376\n\t" "s_addc_u32 s79, s79, 0\n\t" "s_waitcnt lgkmcnt(12)\n\t" "v_mfma_f32_16x16x32_bf16 v[192:195], a[152:155], a[96:99], v[192:195]\n\t"
    "ds_read_b64_tr_b16 a[148:149], %[vr3] offset:14368\n\t" "v_mfma_f32_16x16x32_bf16 v[196:199], a[152:155], a[100:103], v[196:199]\n\t" "ds_read_b64_tr_b16 a[150:151], %[vr3] offset:17952\n\t"
    "v_mfma_f32_16x16x32_bf16 v[200:203], a[152:155], a[104:107], v[200:203]\n\t" "v_mfma_f32_16x16x32_bf16 v[204:207], a[152:155], a[108:111], v[204:207]\n\t" "s_waitcnt lgkmcnt(12)\n\t"
    "v_mfma_f32_16x16x32_bf16 v[208:211], a[156:159], a[96:99], v[208:211]\n\t" "ds_read_b64_tr_b16 a[152:153], %[vr5] offset:14336\n\t"
    "v_mfma_f32_16x16x32_bf16 v[212:215], a[156:159], a[100:103], v[212:215]\n\t" "ds_read_b64_tr_b16 a[154:155], %[vr5] offset:17920\n\t"
    "v_mfma_f32_16x16x32_bf16 v[216:219], a[156:159], a[104:107], v[216:219]\n\t" "v_mfma_f32_16x16x32_bf16 v[220:223], a[156:159], a[108:111], v[220:223]\n\t" "s_waitcnt lgkmcnt(12)\n\t"
    "v_mfma_f32_16x16x32_bf16 v[224:227], a[128:131], a[96:99], v[224:227]\n\t" "ds_read_b64_tr_b16 a[156:157], %[vr5] offset:14368\n\t"
    "v_mfma_f32_16x16x32_bf16 v[228:231], a[128:131], a[100:103], v[228:231]\n\t" "ds_read_b64_tr_b16 a[158:159], %[vr5] offset:17952\n\t"
    "v_mfma_f32_16x16x32_bf16 v[232:235], a[128:131], a[104:107], v[232:235]\n\t" "v_mfma_f32_16x16x32_bf16 v[236:239], a[128:131], a[108:111], v[236:239]\n\t" "s_waitcnt lgkmcnt(12)\n\t"
    "v_mfma_f32_16x16x32_bf16 v[240:243], a[132:135], a[96:99], v[240:243]\n\t" "ds_read_b64_tr_b16 a[128:129], %[vr7] offset:14336\n\t"
    "v_mfma_f32_16x16x32_bf16 v[244:247], a[132:135], a[100:103], v[244:247]\n\t" "ds_read_b64_tr_b16 a[130:131], %[vr7] offset:17920\n\t"
    "v_mfma_f32_16x16x32_bf16 a[180:183], a[132:135], a[192:195], a[180:183]\n\t" "v_mfma_f32_16x16x32_bf16 v[248:251], a[132:135], a[104:107], v[248:251]\n\t"
    "v_mfma_f32_16x16x32_bf16 v[252:255], a[132:135], a[108:111], v[252:255]\n\t" "s_waitcnt lgkmcnt(12)\n\t" "v_mfma_f32_16x16x32_bf16 v[32:35], a[136:139], a[112:115], v[32:35]\n\t"
    "ds_read_b64_tr_b16 a[132:133], %[vr1] offset:21504\n\t" "v_mfma_f32_16x16x32_bf16 v[36:39], a[136:139], a[116:119], v[36:39]\n\t" "ds_read_b64_tr_b16 a[134:135], %[vr1] offset:25088\n\t"
    "v_mfma_f32_16x16x32_bf16 a[160:163], a[136:139], a[196:199], a[160:163]\n\t" "global_load_dwordx4 a[32:35], %[va], s[76:77] offset:0 sc1\n\t"
    "v_mfma_f32_16x16x32_bf16 v[40:43], a[136:139], a[120:123], v[40:43]\n\t" "v_mfma_f32_16x16x32_bf16 v[44:47], a[136:139], a[124:127], v[44:47]\n\t" "s_waitcnt lgkmcnt(12)\n\t"
    "v_mfma_f32_16x16x32_bf16 v[48:51], a[140:143], a[112:115], v[48:51]\n\t" "ds_read_b64_tr_b16 a[136:137], %[vr1] offset:21536\n\t" "s_load_dwordx2 s[98:99], s[84:85], 60\n\t"
    "v_mfma_f32_16x16x32_bf16 v[52:55], a[140:143], a[116:119], v[52:55]\n\t" "ds_read_b64_tr_b16 a[138:139], %[vr1] offset:25120\n\t"
    "v_mfma_f32_16x16x32_bf16 a[164:167], a[140:143], a[196:199], a[164:167]\n\t" "s_add_u32 m0, %[sl], 0\n\t" "v_mfma_f32_16x16x32_bf16 v[56:59], a[140:143], a[120:123], v[56:59]\n\t"
    "global_load_lds_dwordx4 %[vb0], s[78:79]\n\t" "v_mfma_f32_16x16x32_bf16 v[60:63], a[140:143], a[124:127], v[60:63]\n\t" "s_waitcnt lgkmcnt(12)\n\t"
    "v_mfma_f32_16x16x32_bf16 v[64:67], a[144:147], a[112:115], v[64:67]\n\t" "ds_read_b64_tr_b16 a[140:141], %[vr3] offset:21504\n\t"
    "v_mfma_f32_16x16x32_bf16 v[68:71], a[144:147], a[116:119], v[68:71]\n\t" "ds_read_b64_tr_b16 a[142:143], %[vr3] offset:25088\n\t"
    "v_mfma_f32_16x16x32_bf16 v[72:75], a[144:147], a[120:123], v[72:75]\n\t" "v_mfma_f32_16x16x32_bf16 v[76:79], a[144:147], a[124:127], v[76:79]\n\t" "s_waitcnt lgkmcnt(12)\n\t"
    "v_mfma_f32_16x16x32_bf16 v[80:83], a[148:151], a[112:115], v[80:83]\n\t" "global_load_dwordx4 a[36:39], %[va], s[76:77] offset:1024 sc1\n\t" "ds_read_b64_tr_b16 a[144:145], %[vr3] offset:21536\n\t"
    "v_mfma_f32_16x16x32_bf16 v[84:87], a[148:151], a[116:119], v[84:87]\n\t" "ds_read_b64_tr_b16 a[146:147], %[vr3] offset:25120\n\t"
    "v_mfma_f32_16x16x32_bf16 v[88:91], a[148:151], a[120:123], v[88:91]\n\t" "v_mfma_f32_16x16x32_bf16 v[92:95], a[148:151], a[124:127], v[92:95]\n\t" "s_waitcnt lgkmcnt(12)\n\t"
    "v_mfma_f32_16x16x32_bf16 v[96:99], a[152:155], a[112:115], v[96:99]\n\t" "ds_read_b64_tr_b16 a[148:149], %[vr5] offset:21504\n\t"
    "v_mfma_f32_16x16x32_bf16 v[100:103], a[152:155], a[116:119], v[100:103]\n\t" "ds_read_b64_tr_b16 a[150:151], %[vr5] offset:25088\n\t" "s_add_u32 m0, %[sl], 1024\n\t"
    "v_mfma_f32_16x16x32_bf16 v[104:107], a[152:155], a[120:123], v[104:107]\n\t" "global_load_lds_dwordx4 %[vb1], s[78:79]\n\t"
    "v_mfma_f32_16x16x32_bf16 v[108:111], a[152:155], a[124:127], v[108:111]\n\t" "s_waitcnt lgkmcnt(12)\n\t" "v_mfma_f32_16x16x32_bf16 v[112:115], a[156:159], a[112:115], v[112:115]\n\t"
    "ds_read_b64_tr_b16 a[152:153], %[vr5] offset:21536\n\t" "v_mfma_f32_16x16x32_bf16 v[116:119], a[156:159], a[116:119], v[116:119]\n\t" "ds_read_b64_tr_b16 a[154:155], %[vr5] offset:25120\n\t"
    "s_load_dwordx2 s[98:99], s[84:85], 188\n\t" "v_mfma_f32_16x16x32_bf16 v[120:123], a[156:159], a[120:123], v[120:123]\n\t" "v_mfma_f32_16x16x32_bf16 v[124:127], a[156:159], a[124:127], v[124:127]\n\t"
    "s_waitcnt lgkmcnt(12)\n\t" "v_mfma_f32_16x16x32_bf16 v[128:131], a[128:131], a[112:115], v[128:131]\n\t" "global_load_dwordx4 a[40:43], %[va], s[76:77] offset:2048 sc1\n\t"
    "ds_read_b64_tr_b16 a[156:157], %[vr7] offset:21504\n\t" "v_mfma_f32_16x16x32_bf16 v[132:135], a[128:131], a[116:119], v[132:135]\n\t" "ds_read_b64_tr_b16 a[158:159], %[vr7] offset:25088\n\t"
    "v_mfma_f32_16x16x32_bf16 a[168:171], a[128:131], a[196:199], a[168:171]\n\t" "v_mfma_f32_16x16x32_bf16 v[136:139], a[128:131], a[120:123], v[136:139]\n\t"
    "v_mfma_f32_16x16x32_bf16 v[140:143], a[128:131], a[124:127], v[140:143]\n\t" "s_waitcnt lgkmcnt(12)\n\t" "v_mfma_f32_16x16x32_bf16 v[144:147], a[132:135], a[112:115], v[144:147]\n\t"
    "s_waitcnt vmcnt(13)\n\t" "s_barrier\n\t" "ds_read_b64_tr_b16 a[128:129], %[vr1] offset:28672\n\t" "s_add_u32 m0, %[sl], 2048\n\t"
    "v_mfma_f32_16x16x32_bf16 v[148:151], a[132:135], a[116:119], v[148:151]\n\t" "global_load_lds_dwordx4 %[vb2], s[78:79]\n\t" "ds_read_b64_tr_b16 a[130:131], %[vr1] offset:32256\n\t"
    "v_mfma_f32_16x16x32_bf16 a[172:175], a[132:135], a[196:199], a[172:175]\n\t" "v_mfma_f32_16x16x32_bf16 v[152:155], a[132:135], a[120:123], v[152:155]\n\t"
    "v_mfma_f32_16x16x32_bf16 v[156:159], a[132:135], a[124:127], v[156:159]\n\t" "s_waitcnt lgkmcnt(12)\n\t" "v_mfma_f32_16x16x32_bf16 v[160:163], a[136:139], a[112:115], v[160:163]\n\t"
    "ds_read_b64_tr_b16 a[132:133], %[vr1] offset:28704\n\t" "v_mfma_f32_16x16x32_bf16 v[164:167], a[136:139], a[116:119], v[164:167]\n\t" "ds_read_b64_tr_b16 a[134:135], %[vr1] offset:32288\n\t"
    "v_mfma_f32_16x16x32_bf16 a[176:179], a[136:139], a[196:199], a[176:179]\n\t" "global_load_dwordx4 a[44:47], %[va], s[76:77] offset:3072 sc1\n\t" "s_add_u32 s76, s76, 4096\n\t"
    "s_addc_u32 s77, s77, 0\n\t" "v_mfma_f32_16x16x32_bf16 v[168:171], a[136:139], a[120:123], v[168:171]\n\t" "v_mfma_f32_16x16x32_bf16 v[172:175], a[136:139], a[124:127], v[172:175]\n\t"
    "s_waitcnt lgkmcnt(12)\n\t" "v_mfma_f32_16x16x32_bf16 v[176:179], a[140:143], a[112:115], v[176:179]\n\t" "ds_read_b64_tr_b16 a[136:137], %[vr3] offset:28672\n\t"
    "s_load_dwordx2 s[98:99], s[84:85], 316\n\t" "s_add_u32 s84, s84, 384\n\t" "s_addc_u32 s85, s85, 0\n\t" "v_mfma_f32_16x16x32_bf16 v[180:183], a[140:143], a[116:119], v[180:183]\n\t"
    "ds_read_b64_tr_b16 a[138:139], %[vr3] offset:32256\n\t" "v_mfma_f32_16x16x32_bf16 v[184:187], a[140:143], a[120:123], v[184:187]\n\t" "s_add_u32 m0, %[sl], 3072\n\t"
    "v_mfma_f32_16x16x32_bf16 v[188:191], a[140:143], a[124:127], v[188:191]\n\t" "s_mov_b64 exec, s[82:83]\n\t" "global_load_lds_dwordx4 %[vb3], s[78:79]\n\t" "s_mov_b64 exec, -1\n\t"
    "s_add_u32 s78, s78, 229376\n\t" "s_addc_u32 s79, s79, 0\n\t" "s_waitcnt lgkmcnt(12)\n\t" "v_mfma_f32_16x16x32_bf16 v[192:195], a[144:147], a[112:115], v[192:195]\n\t"
    "ds_read_b64_tr_b16 a[140:141], %[vr3] offset:28704\n\t" "v_mfma_f32_16x16x32_bf16 v[196:199], a[144:147], a[116:119], v[196:199]\n\t" "ds_read_b64_tr_b16 a[142:143], %[vr3] offset:32288\n\t"
    "v_mfma_f32_16x16x32_bf16 v[200:203], a[144:147], a[120:123], v[200:203]\n\t" "v_mfma_f32_16x16x32_bf16 v[204:207], a[144:147], a[124:127], v[204:207]\n\t" "s_waitcnt lgkmcnt(12)\n\t"
    "v_mfma_f32_16x16x32_bf16 v[208:211], a[148:151], a[112:115], v[208:211]\n\t" "ds_read_b64_tr_b16 a[144:145], %[vr5] offset:28672\n\t"
    "v_mfma_f32_16x16x32_bf16 v[212:215], a[148:151], a[116:119], v[212:215]\n\t" "ds_read_b64_tr_b16 a[146:147], %[vr5] offset:32256\n\t"
    "v_mfma_f32_16x16x32_bf16 v[216:219], a[148:151], a[120:123], v[216:219]\n\t" "v_mfma_f32_16x16x32_bf16 v[220:223], a[148:151], a[124:127], v[220:223]\n\t" "s_waitcnt lgkmcnt(12)\n\t"
    "v_mfma_f32_16x16x32_bf16 v[224:227], a[152:155], a[112:115], v[224:227]\n\t" "ds_read_b64_tr_b16 a[148:149], %[vr5] offset:28704\n\t"
    "v_mfma_f32_16x16x32_bf16 v[228:231], a[152:155], a[116:119], v[228:231]\n\t" "ds_read_b64_tr_b16 a[150:151], %[vr5] offset:32288\n\t"
    "v_mfma_f32_16x16x32_bf16 v[232:235], a[152:155], a[120:123], v[232:235]\n\t" "v_mfma_f32_16x16x32_bf16 v[236:239], a[152:155], a[124:127], v[236:239]\n\t" "s_waitcnt lgkmcnt(12)\n\t"
    "v_mfma_f32_16x16x32_bf16 v[240:243], a[156:159], a[112:115], v[240:243]\n\t" "ds_read_b64_tr_b16 a[152:153], %[vr7] offset:28672\n\t"
    "v_mfma_f32_16x16x32_bf16 v[244:247], a[156:159], a[116:119], v[244:247]\n\t" "ds_read_b64_tr_b16 a[154:155], %[vr7] offset:32256\n\t"
    "v_mfma_f32_16x16x32_bf16 a[180:183], a[156:159], a[196:199], a[180:183]\n\t" "v_mfma_f32_16x16x32_bf16 v[248:251], a[156:159], a[120:123], v[248:251]\n\t"
    "v_mfma_f32_16x16x32_bf16 v[252:255], a[156:159], a[124:127], v[252:255]\n\t" "v_accvgpr_read_b32 v31, %[cp]\n\t" "s_nop 7\n\t" "s_mov_b64 exec, %[m01]\n\t"
    "global_store_dwordx4 v31, a[160:163], %[su0] offset:0 sc1\n\t" "global_store_dwordx4 v31, a[164:167], %[su0] offset:2304 sc1\n\t" "global_store_dwordx4 v31, a[172:175], %[su0] offset:1152 sc1\n\t"
    "global_store_dwordx4 v31, a[176:179], %[su0] offset:3456 sc1\n\t" "s_mov_b64 exec, %[m6]\n\t" "global_store_dwordx4 v31, a[168:171], %[su6] offset:0 sc1\n\t"
    "global_store_dwordx4 v31, a[180:183], %[su6] offset:1152 sc1\n\t" "s_mov_b64 exec, -1\n\t" "s_branch .Ld1_%=\n\t" ".Lslow0_%=:\n\t" "s_sleep 2\n\t" "v_accvgpr_read_b32 v31, %[cr0]\n\t"
    "s_mov_b64 s[96:97], 0\n\t" "v_subrev_u32 v31, %[rb], v31\n\t" "global_load_dword v24, v31, %[su] offset:0 sc1\n\t" "global_load_dword v25, v31, %[su] offset:16 sc1\n\t"
    "global_load_dword v26, v31, %[su] offset:32 sc1\n\t" "global_load_dword v27, v31, %[su] offset:48 sc1\n\t" "global_load_dword v28, v31, %[su] offset:64 sc1\n\t"
    "global_load_dword v29, v31, %[su] offset:80 sc1\n\t" "global_load_dword v30, v31, %[su] offset:96 sc1\n\t" "global_load_dword v31, v31, %[su] offset:112 sc1\n\t" "s_waitcnt vmcnt(0)\n\t"
    "v_cmp_eq_u32 vcc, -1, v24\n\t" "s_nop 1\n\t" "s_or_b64 s[96:97], s[96:97], vcc\n\t" "v_cmp_eq_u32 vcc, -1, v25\n\t" "s_nop 1\n\t" "s_or_b64 s[96:97], s[96:97], vcc\n\t"
    "v_cmp_eq_u32 vcc, -1, v26\n\t" "s_nop 1\n\t" "s_or_b64 s[96:97], s[96:97], vcc\n\t" "v_cmp_eq_u32 vcc, -1, v27\n\t" "s_nop 1\n\t" "s_or_b64 s[96:97], s[96:97], vcc\n\t"
    "v_cmp_eq_u32 vcc, -1, v28\n\t" "s_nop 1\n\t" "s_or_b64 s[96:97], s[96:97], vcc\n\t" "v_cmp_eq_u32 vcc, -1, v29\n\t" "s_nop 1\n\t" "s_or_b64 s[96:97], s[96:97], vcc\n\t"
    "v_cmp_eq_u32 vcc, -1, v30\n\t" "s_nop 1\n\t" "s_or_b64 s[96:97], s[96:97], vcc\n\t" "v_cmp_eq_u32 vcc, -1, v31\n\t" "s_nop 1\n\t" "s_or_b64 s[96:97], s[96:97], vcc\n\t" "v_add_f32 v24, v24, v25\n\t"
    "v_add_f32 v26, v26, v27\n\t" "v_add_f32 v28, v28, v29\n\t" "v_add_f32 v30, v30, v31\n\t" "v_add_f32 v24, v24, v26\n\t" "v_add_f32 v28, v28, v30\n\t" "v_add_f32 v24, v24, v28\n\t"
    "s_cbranch_scc0 .Lgo0_%=\n\t" "s_sub_u32 s94, s94, 1\n\t" "s_cbranch_scc0 .Lslow0_%=\n\t" "s_branch .Lgo0_%=\n\t" ".Lslow1_%=:\n\t" "s_sleep 2\n\t" "v_accvgpr_read_b32 v31, %[cr1]\n\t"
    "s_mov_b64 s[96:97], 0\n\t" "v_subrev_u32 v31, %[rb], v31\n\t" "global_load_dword v24, v31, %[su] offset:0 sc1\n\t" "global_load_dword v25, v31, %[su] offset:16 sc1\n\t"
    "global_load_dword v26, v31, %[su] offset:32 sc1\n\t" "global_load_dword v27, v31, %[su] offset:48 sc1\n\t" "global_load_dword v28, v31, %[su] offset:64 sc1\n\t"
    "global_load_dword v29, v31, %[su] offset:80 sc1\n\t" "global_load_dword v30, v31, %[su] offset:96 sc1\n\t" "global_load_dword v31, v31, %[su] offset:112 sc1\n\t" "s_waitcnt vmcnt(0)\n\t"
    "v_cmp_eq_u32 vcc, -1, v24\n\t" "s_nop 1\n\t" "s_or_b64 s[96:97], s[96:97], vcc\n\t" "v_cmp_eq_u32 vcc, -1, v25\n\t" "s_nop 1\n\t" "s_or_b64 s[96:97], s[96:97], vcc\n\t"
    "v_cmp_eq_u32 vcc, -1, v26\n\t" "s_nop 1\n\t" "s_or_b64 s[96:97], s[96:97], vcc\n\t" "v_cmp_eq_u32 vcc, -1, v27\n\t" "s_nop 1\n\t" "s_or_b64 s[96:97], s[96:97], vcc\n\t"
    "v_cmp_eq_u32 vcc, -1, v28\n\t" "s_nop 1\n\t" "s_or_b64 s[96:97], s[96:97], vcc\n\t" "v_cmp_eq_u32 vcc, -1, v29\n\t" "s_nop 1\n\t" "s_or_b64 s[96:97], s[96:97], vcc\n\t"
    "v_cmp_eq_u32 vcc, -1, v30\n\t" "s_nop 1\n\t" "s_or_b64 s[96:97], s[96:97], vcc\n\t" "v_cmp_eq_u32 vcc, -1, v31\n\t" "s_nop 1\n\t" "s_or_b64 s[96:97], s[96:97], vcc\n\t" "v_add_f32 v24, v24, v25\n\t"
    "v_add_f32 v26, v26, v27\n\t" "v_add_f32 v28, v28, v29\n\t" "v_add_f32 v30, v30, v31\n\t" "v_add_f32 v24, v24, v26\n\t" "v_add_f32 v28, v28, v30\n\t" "v_add_f32 v24, v24, v28\n\t"
    "s_cbranch_scc0 .Lgo1_%=\n\t" "s_sub_u32 s94, s94, 1\n\t" "s_cbranch_scc0 .Lslow1_%=\n\t" "s_branch .Lgo1_%=\n\t" ".Ltail_%=:\n\t" "s_cmp_eq_u32 s80, 1\n\t" "s_cselect_b32 s86, %[wa], 4096\n\t"
    "s_cselect_b32 s88, %[wb], 229376\n\t" "s_ashr_i32 s87, s86, 31\n\t" "s_ashr_i32 s89, s88, 31\n\t" "s_waitcnt lgkmcnt(12)\n\t" "v_mfma_f32_16x16x32_bf16 v[32:35], a[128:131], a[64:67], v[32:35]\n\t"
    "ds_read_b64_tr_b16 a[156:157], %[vr0] offset:35840\n\t" "v_accvgpr_read_b32 v24, a216\n\t" "v_mfma_f32_16x16x32_bf16 v[36:39], a[128:131], a[68:71], v[36:39]\n\t"
    "global_load_dwordx4 a[112:115], %[va], s[76:77] offset:0 sc1\n\t" "ds_read_b64_tr_b16 a[158:159], %[vr0] offset:39424\n\t" "v_accvgpr_read_b32 v25, a217\n\t"
    "v_mfma_f32_16x16x32_bf16 v[40:43], a[128:131], a[72:75], v[40:43]\n\t" "v_accvgpr_read_b32 v28, a218\n\t" "v_mfma_f32_16x16x32_bf16 v[44:47], a[128:131], a[76:79], v[44:47]\n\t"
    "v_accvgpr_read_b32 v29, a219\n\t" "s_waitcnt lgkmcnt(12)\n\t" "v_mfma_f32_16x16x32_bf16 v[48:51], a[132:135], a[64:67], v[48:51]\n\t" "ds_read_b64_tr_b16 a[128:129], %[vr0] offset:35872\n\t"
    "v_accvgpr_read_b32 v26, a224\n\t" "s_add_u32 m0, %[sl], 71680\n\t" "v_mfma_f32_16x16x32_bf16 v[52:55], a[132:135], a[68:71], v[52:55]\n\t" "global_load_lds_dwordx4 %[vb0], s[78:79]\n\t"
    "ds_read_b64_tr_b16 a[130:131], %[vr0] offset:39456\n\t" "s_load_dwordx2 s[98:99], s[84:85], 60\n\t" "v_accvgpr_read_b32 v27, a225\n\t"
    "v_mfma_f32_16x16x32_bf16 v[56:59], a[132:135], a[72:75], v[56:59]\n\t" "v_accvgpr_read_b32 v30, a226\n\t" "v_mfma_f32_16x16x32_bf16 v[60:63], a[132:135], a[76:79], v[60:63]\n\t"
    "v_accvgpr_read_b32 v31, a227\n\t" "s_waitcnt lgkmcnt(12)\n\t" "v_mfma_f32_16x16x32_bf16 v[64:67], a[136:139], a[64:67], v[64:67]\n\t" "ds_read_b64_tr_b16 a[132:133], %[vr2] offset:35840\n\t"
    "v_add_f32 v24, v24, v26\n\t" "v_mfma_f32_16x16x32_bf16 v[68:71], a[136:139], a[68:71], v[68:71]\n\t" "global_load_dwordx4 a[116:119], %[va], s[76:77] offset:1024 sc1\n\t"
    "ds_read_b64_tr_b16 a[134:135], %[vr2] offset:39424\n\t" "v_add_f32 v25, v25, v27\n\t" "v_mfma_f32_16x16x32_bf16 v[72:75], a[136:139], a[72:75], v[72:75]\n\t" "v_add_f32 v28, v28, v30\n\t"
    "v_mfma_f32_16x16x32_bf16 v[76:79], a[136:139], a[76:79], v[76:79]\n\t" "v_add_f32 v29, v29, v31\n\t" "s_waitcnt lgkmcnt(12)\n\t"
    "v_mfma_f32_16x16x32_bf16 v[80:83], a[140:143], a[64:67], v[80:83]\n\t" "ds_read_b64_tr_b16 a[136:137], %[vr2] offset:35872\n\t" "v_accvgpr_read_b32 v26, a232\n\t" "s_add_u32 m0, %[sl], 72704\n\t"
    "v_mfma_f32_16x16x32_bf16 v[84:87], a[140:143], a[68:71], v[84:87]\n\t" "global_load_lds_dwordx4 %[vb1], s[78:79]\n\t" "ds_read_b64_tr_b16 a[138:139], %[vr2] offset:39456\n\t"
    "v_accvgpr_read_b32 v27, a233\n\t" "v_mfma_f32_16x16x32_bf16 v[88:91], a[140:143], a[72:75], v[88:91]\n\t" "v_accvgpr_read_b32 v30, a234\n\t"
    "v_mfma_f32_16x16x32_bf16 v[92:95], a[140:143], a[76:79], v[92:95]\n\t" "v_accvgpr_read_b32 v31, a235\n\t" "s_waitcnt lgkmcnt(12)\n\t"
    "v_mfma_f32_16x16x32_bf16 v[96:99], a[144:147], a[64:67], v[96:99]\n\t" "ds_read_b64_tr_b16 a[140:141], %[vr4] offset:35840\n\t" "v_add_f32 v24, v24, v26\n\t"
    "v_mfma_f32_16x16x32_bf16 v[100:103], a[144:147], a[68:71], v[100:103]\n\t" "global_load_dwordx4 a[120:123], %[va], s[76:77] offset:2048 sc1\n\t"
    "ds_read_b64_tr_b16 a[142:143], %[vr4] offset:39424\n\t" "v_add_f32 v25, v25, v27\n\t" "v_mfma_f32_16x16x32_bf16 v[104:107], a[144:147], a[72:75], v[104:107]\n\t" "v_add_f32 v28, v28, v30\n\t"
    "v_mfma_f32_16x16x32_bf16 v[108:111], a[144:147], a[76:79], v[108:111]\n\t" "v_add_f32 v29, v29, v31\n\t" "s_waitcnt lgkmcnt(12)\n\t"
    "v_mfma_f32_16x16x32_bf16 v[112:115], a[148:151], a[64:67], v[112:115]\n\t" "ds_read_b64_tr_b16 a[144:145], %[vr4] offset:35872\n\t" "v_accvgpr_read_b32 v26, a240\n\t" "s_add_u32 m0, %[sl], 73728\n\t"
    "v_mfma_f32_16x16x32_bf16 v[116:119], a[148:151], a[68:71], v[116:119]\n\t" "global_load_lds_dwordx4 %[vb2], s[78:79]\n\t" "ds_read_b64_tr_b16 a[146:147], %[vr4] offset:39456\n\t"
    "v_accvgpr_read_b32 v27, a241\n\t" "v_mfma_f32_16x16x32_bf16 v[120:123], a[148:151], a[72:75], v[120:123]\n\t" "v_accvgpr_read_b32 v30, a242\n\t"
    "v_mfma_f32_16x16x32_bf16 v[124:127], a[148:151], a[76:79], v[124:127]\n\t" "s_load_dwordx2 s[98:99], s[84:85], 188\n\t" "v_accvgpr_read_b32 v31, a243\n\t" "s_waitcnt lgkmcnt(12)\n\t"
    "v_mfma_f32_16x16x32_bf16 v[128:131], a[152:155], a[64:67], v[128:131]\n\t" "ds_read_b64_tr_b16 a[148:149], %[vr6] offset:35840\n\t" "v_add_f32 v24, v24, v26\n\t"
    "v_mfma_f32_16x16x32_bf16 v[132:135], a[152:155], a[68:71], v[132:135]\n\t" "global_load_dwordx4 a[124:127], %[va], s[76:77] offset:3072 sc1\n\t" "s_add_u32 s76, s76, s86\n\t"
    "s_addc_u32 s77, s77, s87\n\t" "ds_read_b64_tr_b16 a[150:151], %[vr6] offset:39424\n\t" "v_add_f32 v25, v25, v27\n\t" "v_mfma_f32_16x16x32_bf16 v[136:139], a[152:155], a[72:75], v[136:139]\n\t"
    "v_add_f32 v28, v28, v30\n\t" "v_mfma_f32_16x16x32_bf16 v[140:143], a[152:155], a[76:79], v[140:143]\n\t" "v_add_f32 v29, v29, v31\n\t" "s_waitcnt lgkmcnt(12)\n\t"
    "v_mfma_f32_16x16x32_bf16 v[144:147], a[156:159], a[64:67], v[144:147]\n\t" "s_waitcnt vmcnt(15)\n\t" "s_barrier\n\t" "ds_read_b64_tr_b16 a[152:153], %[vr0] offset:43008\n\t" "v_mov_b32 v26, v24\n\t"
    "s_add_u32 m0, %[sl], 74752\n\t" "v_mfma_f32_16x16x32_bf16 v[148:151], a[156:159], a[68:71], v[148:151]\n\t" "s_mov_b64 exec, s[82:83]\n\t" "global_load_lds_dwordx4 %[vb3], s[78:79]\n\t"
    "s_mov_b64 exec, -1\n\t" "s_add_u32 s78, s78, s88\n\t" "s_addc_u32 s79, s79, s89\n\t" "ds_read_b64_tr_b16 a[154:155], %[vr0] offset:46592\n\t" "v_mov_b32 v27, v25\n\t"
    "v_mfma_f32_16x16x32_bf16 v[152:155], a[156:159], a[72:75], v[152:155]\n\t" "v_mov_b32 v30, v28\n\t" "v_mfma_f32_16x16x32_bf16 v[156:159], a[156:159], a[76:79], v[156:159]\n\t"
    "v_mov_b32 v31, v29\n\t" "s_waitcnt lgkmcnt(12)\n\t" "v_mfma_f32_16x16x32_bf16 v[160:163], a[128:131], a[64:67], v[160:163]\n\t" "ds_read_b64_tr_b16 a[156:157], %[vr0] offset:43040\n\t" "s_nop 0\n\t"
    "v_mfma_f32_16x16x32_bf16 v[164:167], a[128:131], a[68:71], v[164:167]\n\t" "global_load_dwordx4 a[0:3], %[va], s[76:77] offset:0 sc1\n\t" "ds_read_b64_tr_b16 a[158:159], %[vr0] offset:46624\n\t"
    "s_nop 0\n\t" "v_mfma_f32_16x16x32_bf16 v[168:171], a[128:131], a[72:75], v[168:171]\n\t" "v_permlane16_swap_b32 v24, v26\n\t"
    "v_mfma_f32_16x16x32_bf16 v[172:175], a[128:131], a[76:79], v[172:175]\n\t" "v_permlane16_swap_b32 v25, v27\n\t" "s_waitcnt lgkmcnt(12)\n\t"
    "v_mfma_f32_16x16x32_bf16 v[176:179], a[132:135], a[64:67], v[176:179]\n\t" "ds_read_b64_tr_b16 a[128:129], %[vr2] offset:43008\n\t" "v_permlane16_swap_b32 v28, v30\n\t"
    "s_add_u32 m0, %[sl], 86016\n\t" "v_mfma_f32_16x16x32_bf16 v[180:183], a[132:135], a[68:71], v[180:183]\n\t" "global_load_lds_dwordx4 %[vb0], s[78:79]\n\t"
    "ds_read_b64_tr_b16 a[130:131], %[vr2] offset:46592\n\t" "v_permlane16_swap_b32 v29, v31\n\t" "v_mfma_f32_16x16x32_bf16 v[184:187], a[132:135], a[72:75], v[184:187]\n\t" "v_add_f32 v24, v24, v26\n\t"
    "v_mfma_f32_16x16x32_bf16 v[188:191], a[132:135], a[76:79], v[188:191]\n\t" "v_add_f32 v25, v25, v27\n\t" "s_waitcnt lgkmcnt(12)\n\t"
    "v_mfma_f32_16x16x32_bf16 v[192:195], a[136:139], a[64:67], v[192:195]\n\t" "ds_read_b64_tr_b16 a[132:133], %[vr2] offset:43040\n\t" "v_add_f32 v28, v28, v30\n\t"
    "v_mfma_f32_16x16x32_bf16 v[196:199], a[136:139], a[68:71], v[196:199]\n\t" "global_load_dwordx4 a[4:7], %[va], s[76:77] offset:1024 sc1\n\t" "ds_read_b64_tr_b16 a[134:135], %[vr2] offset:46624\n\t"
    "s_load_dwordx2 s[98:99], s[84:85], 316\n\t" "s_add_u32 s84, s84, 384\n\t" "s_addc_u32 s85, s85, 0\n\t" "v_add_f32 v29, v29, v31\n\t"
    "v_mfma_f32_16x16x32_bf16 v[200:203], a[136:139], a[72:75], v[200:203]\n\t" "v_mov_b32 v26, v24\n\t" "v_mfma_f32_16x16x32_bf16 v[204:207], a[136:139], a[76:79], v[204:207]\n\t"
    "v_mov_b32 v27, v25\n\t" "s_waitcnt lgkmcnt(12)\n\t" "v_mfma_f32_16x16x32_bf16 v[208:211], a[140:143], a[64:67], v[208:211]\n\t" "ds_read_b64_tr_b16 a[136:137], %[vr4] offset:43008\n\t"
    "v_mov_b32 v30, v28\n\t" "s_add_u32 m0, %[sl], 87040\n\t" "v_mfma_f32_16x16x32_bf16 v[212:215], a[140:143], a[68:71], v[212:215]\n\t" "global_load_lds_dwordx4 %[vb1], s[78:79]\n\t"
    "ds_read_b64_tr_b16 a[138:139], %[vr4] offset:46592\n\t" "v_mov_b32 v31, v29\n\t" "v_mfma_f32_16x16x32_bf16 v[216:219], a[140:143], a[72:75], v[216:219]\n\t" "s_nop 0\n\t"
    "v_mfma_f32_16x16x32_bf16 v[220:223], a[140:143], a[76:79], v[220:223]\n\t" "s_nop 0\n\t" "s_waitcnt lgkmcnt(12)\n\t" "v_mfma_f32_16x16x32_bf16 v[224:227], a[144:147], a[64:67], v[224:227]\n\t"
    "ds_read_b64_tr_b16 a[140:141], %[vr4] offset:43040\n\t" "v_permlane32_swap_b32 v24, v26\n\t" "v_mfma_f32_16x16x32_bf16 v[228:231], a[144:147], a[68:71], v[228:231]\n\t"
    "ds_read_b64_tr_b16 a[142:143], %[vr4] offset:46624\n\t" "v_permlane32_swap_b32 v25, v27\n\t" "v_mfma_f32_16x16x32_bf16 v[232:235], a[144:147], a[72:75], v[232:235]\n\t"
    "v_permlane32_swap_b32 v28, v30\n\t" "v_mfma_f32_16x16x32_bf16 v[236:239], a[144:147], a[76:79], v[236:239]\n\t" "v_permlane32_swap_b32 v29, v31\n\t" "s_waitcnt lgkmcnt(12)\n\t"
    "v_mfma_f32_16x16x32_bf16 v[240:243], a[148:151], a[64:67], v[240:243]\n\t" "ds_read_b64_tr_b16 a[144:145], %[vr6] offset:43008\n\t" "v_add_f32 v24, v24, v26\n\t"
    "v_mfma_f32_16x16x32_bf16 v[244:247], a[148:151], a[68:71], v[244:247]\n\t" "ds_read_b64_tr_b16 a[146:147], %[vr6] offset:46592\n\t" "v_add_f32 v25, v25, v27\n\t"
    "v_mfma_f32_16x16x32_bf16 v[248:251], a[148:151], a[72:75], v[248:251]\n\t" "v_add_f32 v28, v28, v30\n\t" "v_mfma_f32_16x16x32_bf16 v[252:255], a[148:151], a[76:79], v[252:255]\n\t"
    "v_add_f32 v29, v29, v31\n\t" "s_waitcnt lgkmcnt(12)\n\t" "v_mfma_f32_16x16x32_bf16 v[32:35], a[152:155], a[80:83], v[32:35]\n\t" "ds_read_b64_tr_b16 a[148:149], %[vr0] offset:50176\n\t"
    "v_mul_f32 v24, 0xba800000, v24\n\t" "v_mfma_f32_16x16x32_bf16 v[36:39], a[152:155], a[84:87], v[36:39]\n\t" "global_load_dwordx4 a[8:11], %[va], s[76:77] offset:2048 sc1\n\t"
    "ds_read_b64_tr_b16 a[150:151], %[vr0] offset:53760\n\t" "v_mul_f32 v25, 0x3a800000, v25\n\t" "v_mfma_f32_16x16x32_bf16 v[40:43], a[152:155], a[88:91], v[40:43]\n\t"
    "v_mul_f32 v28, 0xba800000, v28\n\t" "v_mfma_f32_16x16x32_bf16 v[44:47], a[152:155], a[92:95], v[44:47]\n\t" "v_mul_f32 v29, 0x3a800000, v29\n\t" "s_waitcnt lgkmcnt(12)\n\t"
    "v_mfma_f32_16x16x32_bf16 v[48:51], a[156:159], a[80:83], v[48:51]\n\t" "ds_read_b64_tr_b16 a[152:153], %[vr0] offset:50208\n\t" "v_fma_f32 v25, -v24, v24, v25\n\t" "s_add_u32 m0, %[sl], 88064\n\t"
    "v_mfma_f32_16x16x32_bf16 v[52:55], a[156:159], a[84:87], v[52:55]\n\t" "global_load_lds_dwordx4 %[vb2], s[78:79]\n\t" "ds_read_b64_tr_b16 a[154:155], %[vr0] offset:53792\n\t"
    "s_load_dwordx2 s[98:99], s[84:85], 60\n\t" "v_and_b32 v26, 0xffff0000, v24\n\t" "v_mfma_f32_16x16x32_bf16 v[56:59], a[156:159], a[88:91], v[56:59]\n\t" "v_fma_f32 v29, -v28, v28, v29\n\t"
    "v_mfma_f32_16x16x32_bf16 v[60:63], a[156:159], a[92:95], v[60:63]\n\t" "v_and_b32 v30, 0xffff0000, v28\n\t" "s_waitcnt lgkmcnt(12)\n\t"
    "v_mfma_f32_16x16x32_bf16 v[64:67], a[128:131], a[80:83], v[64:67]\n\t" "ds_read_b64_tr_b16 a[156:157], %[vr2] offset:50176\n\t" "v_max_f32 v25, 0, v25\n\t"
    "v_mfma_f32_16x16x32_bf16 v[68:71], a[128:131], a[84:87], v[68:71]\n\t" "global_load_dwordx4 a[12:15], %[va], s[76:77] offset:3072 sc1\n\t" "s_add_u32 s76, s76, 4096\n\t" "s_addc_u32 s77, s77, 0\n\t"
    "ds_read_b64_tr_b16 a[158:159], %[vr2] offset:53760\n\t" "v_sub_f32 v24, v24, v26\n\t" "v_mfma_f32_16x16x32_bf16 v[72:75], a[128:131], a[88:91], v[72:75]\n\t" "v_max_f32 v29, 0, v29\n\t"
    "v_mfma_f32_16x16x32_bf16 v[76:79], a[128:131], a[92:95], v[76:79]\n\t" "v_sub_f32 v28, v28, v30\n\t" "s_waitcnt lgkmcnt(12)\n\t"
    "v_mfma_f32_16x16x32_bf16 v[80:83], a[132:135], a[80:83], v[80:83]\n\t" "ds_read_b64_tr_b16 a[128:129], %[vr2] offset:50208\n\t" "v_add_f32 v25, 0x3727c5ac, v25\n\t" "s_add_u32 m0, %[sl], 89088\n\t"
    "v_mfma_f32_16x16x32_bf16 v[84:87], a[132:135], a[84:87], v[84:87]\n\t" "s_mov_b64 exec, s[82:83]\n\t" "global_load_lds_dwordx4 %[vb3], s[78:79]\n\t" "s_mov_b64 exec, -1\n\t"
    "s_add_u32 s78, s78, 229376\n\t" "s_addc_u32 s79, s79, 0\n\t" "ds_read_b64_tr_b16 a[130:131], %[vr2] offset:53792\n\t" "v_cvt_pk_bf16_f32 v26, v26, v26\n\t"
    "v_mfma_f32_16x16x32_bf16 v[88:91], a[132:135], a[88:91], v[88:91]\n\t" "v_add_f32 v29, 0x3727c5ac, v29\n\t" "v_mfma_f32_16x16x32_bf16 v[92:95], a[132:135], a[92:95], v[92:95]\n\t"
    "v_cvt_pk_bf16_f32 v30, v30, v30\n\t" "s_waitcnt lgkmcnt(12)\n\t" "v_mfma_f32_16x16x32_bf16 v[96:99], a[136:139], a[80:83], v[96:99]\n\t" "ds_read_b64_tr_b16 a[132:133], %[vr4] offset:50176\n\t"
    "v_rsq_f32 v27, v25\n\t" "v_mfma_f32_16x16x32_bf16 v[100:103], a[136:139], a[84:87], v[100:103]\n\t" "global_load_dwordx4 a[16:19], %[va], s[76:77] offset:0 sc1\n\t"
    "ds_read_b64_tr_b16 a[134:135], %[vr4] offset:53760\n\t" "v_cvt_pk_bf16_f32 v24, v24, v24\n\t" "v_mfma_f32_16x16x32_bf16 v[104:107], a[136:139], a[88:91], v[104:107]\n\t" "v_rsq_f32 v31, v29\n\t"
    "v_mfma_f32_16x16x32_bf16 v[108:111], a[136:139], a[92:95], v[108:111]\n\t" "v_cvt_pk_bf16_f32 v28, v28, v28\n\t" "s_waitcnt lgkmcnt(12)\n\t"
    "v_mfma_f32_16x16x32_bf16 v[112:115], a[140:143], a[80:83], v[112:115]\n\t" "ds_read_b64_tr_b16 a[136:137], %[vr4] offset:50208\n\t" "v_mul_f32 v20, v25, v27\n\t" "s_add_u32 m0, %[sl], 100352\n\t"
    "v_mfma_f32_16x16x32_bf16 v[116:119], a[140:143], a[84:87], v[116:119]\n\t" "global_load_lds_dwordx4 %[vb0], s[78:79]\n\t" "ds_read_b64_tr_b16 a[138:139], %[vr4] offset:53792\n\t"
    "v_mul_f32 v16, 0xbfb8aa3b, v27\n\t" "v_mfma_f32_16x16x32_bf16 v[120:123], a[140:143], a[88:91], v[120:123]\n\t" "v_mul_f32 v21, v29, v31\n\t"
    "v_mfma_f32_16x16x32_bf16 v[124:127], a[140:143], a[92:95], v[124:127]\n\t" "s_load_dwordx2 s[98:99], s[84:85], 188\n\t" "v_mul_f32 v17, 0xbfb8aa3b, v31\n\t" "s_waitcnt lgkmcnt(12)\n\t"
    "v_mfma_f32_16x16x32_bf16 v[128:131], a[144:147], a[80:83], v[128:131]\n\t" "ds_read_b64_tr_b16 a[140:141], %[vr6] offset:50176\n\t" "v_and_b32 v27, 0xffff0000, v20\n\t"
    "v_mfma_f32_16x16x32_bf16 v[132:135], a[144:147], a[84:87], v[132:135]\n\t" "global_load_dwordx4 a[20:23], %[va], s[76:77] offset:1024 sc1\n\t" "ds_read_b64_tr_b16 a[142:143], %[vr6] offset:53760\n\t"
    "s_nop 0\n\t" "v_mfma_f32_16x16x32_bf16 v[136:139], a[144:147], a[88:91], v[136:139]\n\t" "v_and_b32 v31, 0xffff0000, v21\n\t"
    "v_mfma_f32_16x16x32_bf16 v[140:143], a[144:147], a[92:95], v[140:143]\n\t" "v_sub_f32 v25, v20, v27\n\t" "s_waitcnt lgkmcnt(12)\n\t"
    "v_mfma_f32_16x16x32_bf16 v[144:147], a[148:151], a[80:83], v[144:147]\n\t" "s_waitcnt vmcnt(19)\n\t" "s_barrier\n\t" "ds_read_b64_tr_b16 a[144:145], %[vr1] offset:0\n\t"
    "v_cvt_pk_bf16_f32 v27, v27, v27\n\t" "s_add_u32 m0, %[sl], 101376\n\t" "v_mfma_f32_16x16x32_bf16 v[148:151], a[148:151], a[84:87], v[148:151]\n\t" "global_load_lds_dwordx4 %[vb1], s[78:79]\n\t"
    "ds_read_b64_tr_b16 a[146:147], %[vr1] offset:3584\n\t" "v_sub_f32 v29, v21, v31\n\t" "v_mfma_f32_16x16x32_bf16 v[152:155], a[148:151], a[88:91], v[152:155]\n\t" "v_cvt_pk_bf16_f32 v31, v31, v31\n\t"
    "v_mfma_f32_16x16x32_bf16 v[156:159], a[148:151], a[92:95], v[156:159]\n\t" "v_cvt_pk_bf16_f32 v25, v25, v25\n\t" "s_waitcnt lgkmcnt(12)\n\t"
    "v_mfma_f32_16x16x32_bf16 v[160:163], a[152:155], a[80:83], v[160:163]\n\t" "ds_read_b64_tr_b16 a[148:149], %[vr1] offset:32\n\t" "s_nop 0\n\t"
    "v_mfma_f32_16x16x32_bf16 v[164:167], a[152:155], a[84:87], v[164:167]\n\t" "global_load_dwordx4 a[24:27], %[va], s[76:77] offset:2048 sc1\n\t" "ds_read_b64_tr_b16 a[150:151], %[vr1] offset:3616\n\t"
    "v_cvt_pk_bf16_f32 v29, v29, v29\n\t" "v_mfma_f32_16x16x32_bf16 v[168:171], a[152:155], a[88:91], v[168:171]\n\t" "s_mov_b64 exec, 0xffff\n\t" "v_accvgpr_write_b32 a200, v26\n\t"
    "v_accvgpr_write_b32 a201, v24\n\t" "v_accvgpr_write_b32 a202, v27\n\t" "v_accvgpr_write_b32 a203, v25\n\t" "v_accvgpr_write_b32 a204, v30\n\t" "v_accvgpr_write_b32 a205, v28\n\t"
    "v_accvgpr_write_b32 a206, v31\n\t" "v_accvgpr_write_b32 a207, v29\n\t" "s_mov_b64 exec, -1\n\t" "v_mfma_f32_16x16x32_bf16 v[172:175], a[152:155], a[92:95], v[172:175]\n\t"
    "v_accvgpr_read_b32 v24, a220\n\t" "s_waitcnt lgkmcnt(12)\n\t" "v_mfma_f32_16x16x32_bf16 v[176:179], a[156:159], a[80:83], v[176:179]\n\t" "ds_read_b64_tr_b16 a[152:153], %[vr3] offset:0\n\t"
    "v_accvgpr_read_b32 v25, a221\n\t" "s_add_u32 m0, %[sl], 102400\n\t" "v_mfma_f32_16x16x32_bf16 v[180:183], a[156:159], a[84:87], v[180:183]\n\t" "global_load_lds_dwordx4 %[vb2], s[78:79]\n\t"
    "ds_read_b64_tr_b16 a[154:155], %[vr3] offset:3584\n\t" "v_accvgpr_read_b32 v28, a222\n\t" "v_mfma_f32_16x16x32_bf16 v[184:187], a[156:159], a[88:91], v[184:187]\n\t"
    "v_accvgpr_read_b32 v29, a223\n\t" "v_mfma_f32_16x16x32_bf16 v[188:191], a[156:159], a[92:95], v[188:191]\n\t" "v_accvgpr_read_b32 v26, a228\n\t" "s_waitcnt lgkmcnt(12)\n\t"
    "v_mfma_f32_16x16x32_bf16 v[192:195], a[128:131], a[80:83], v[192:195]\n\t" "ds_read_b64_tr_b16 a[156:157], %[vr3] offset:32\n\t" "v_accvgpr_read_b32 v27, a229\n\t"
    "v_mfma_f32_16x16x32_bf16 v[196:199], a[128:131], a[84:87], v[196:199]\n\t" "global_load_dwordx4 a[28:31], %[va], s[76:77] offset:3072 sc1\n\t" "s_add_u32 s76, s76, 4096\n\t"
    "s_addc_u32 s77, s77, 0\n\t" "ds_read_b64_tr_b16 a[158:159], %[vr3] offset:3616\n\t" "s_load_dwordx2 s[98:99], s[84:85], 316\n\t" "s_add_u32 s84, s84, 384\n\t" "s_addc_u32 s85, s85, 0\n\t"
    "v_accvgpr_read_b32 v30, a230\n\t" "v_mfma_f32_16x16x32_bf16 v[200:203], a[128:131], a[88:91], v[200:203]\n\t" "v_accvgpr_read_b32 v31, a231\n\t"
    "v_mfma_f32_16x16x32_bf16 v[204:207], a[128:131], a[92:95], v[204:207]\n\t" "v_add_f32 v24, v24, v26\n\t" "s_waitcnt lgkmcnt(12)\n\t"
    "v_mfma_f32_16x16x32_bf16 v[208:211], a[132:135], a[80:83], v[208:211]\n\t" "ds_read_b64_tr_b16 a[128:129], %[vr5] offset:0\n\t" "v_add_f32 v25, v25, v27\n\t" "s_add_u32 m0, %[sl], 103424\n\t"
    "v_mfma_f32_16x16x32_bf16 v[212:215], a[132:135], a[84:87], v[212:215]\n\t" "s_mov_b64 exec, s[82:83]\n\t" "global_load_lds_dwordx4 %[vb3], s[78:79]\n\t" "s_mov_b64 exec, -1\n\t"
    "s_add_u32 s78, s78, 229376\n\t" "s_addc_u32 s79, s79, 0\n\t" "ds_read_b64_tr_b16 a[130:131], %[vr5] offset:3584\n\t" "v_add_f32 v28, v28, v30\n\t"
    "v_mfma_f32_16x16x32_bf16 v[216:219], a[132:135], a[88:91], v[216:219]\n\t" "v_add_f32 v29, v29, v31\n\t" "v_mfma_f32_16x16x32_bf16 v[220:223], a[132:135], a[92:95], v[220:223]\n\t"
    "v_accvgpr_read_b32 v26, a236\n\t" "s_waitcnt lgkmcnt(12)\n\t" "v_mfma_f32_16x16x32_bf16 v[224:227], a[136:139], a[80:83], v[224:227]\n\t" "ds_read_b64_tr_b16 a[132:133], %[vr5] offset:32\n\t"
    "v_accvgpr_read_b32 v27, a237\n\t" "v_mfma_f32_16x16x32_bf16 v[228:231], a[136:139], a[84:87], v[228:231]\n\t" "ds_read_b64_tr_b16 a[134:135], %[vr5] offset:3616\n\t"
    "v_accvgpr_read_b32 v30, a238\n\t" "v_mfma_f32_16x16x32_bf16 v[232:235], a[136:139], a[88:91], v[232:235]\n\t" "v_accvgpr_read_b32 v31, a239\n\t"
    "v_mfma_f32_16x16x32_bf16 v[236:239], a[136:139], a[92:95], v[236:239]\n\t" "v_add_f32 v24, v24, v26\n\t" "s_waitcnt lgkmcnt(12)\n\t"
    "v_mfma_f32_16x16x32_bf16 v[240:243], a[140:143], a[80:83], v[240:243]\n\t" "ds_read_b64_tr_b16 a[136:137], %[vr7] offset:0\n\t" "v_add_f32 v25, v25, v27\n\t"
    "v_mfma_f32_16x16x32_bf16 v[244:247], a[140:143], a[84:87], v[244:247]\n\t" "ds_read_b64_tr_b16 a[138:139], %[vr7] offset:3584\n\t" "v_add_f32 v28, v28, v30\n\t"
    "v_mfma_f32_16x16x32_bf16 v[248:251], a[140:143], a[88:91], v[248:251]\n\t" "v_add_f32 v29, v29, v31\n\t" "v_mfma_f32_16x16x32_bf16 v[252:255], a[140:143], a[92:95], v[252:255]\n\t"
    "v_accvgpr_read_b32 v26, a244\n\t" "s_waitcnt lgkmcnt(12)\n\t" "v_mfma_f32_16x16x32_bf16 v[32:35], a[144:147], a[96:99], v[32:35]\n\t" "ds_read_b64_tr_b16 a[140:141], %[vr1] offset:7168\n\t"
    "v_accvgpr_read_b32 v27, a245\n\t" "v_mfma_f32_16x16x32_bf16 v[36:39], a[144:147], a[100:103], v[36:39]\n\t" "global_load_dwordx4 a[32:35], %[va], s[76:77] offset:0 sc1\n\t"
    "ds_read_b64_tr_b16 a[142:143], %[vr1] offset:10752\n\t" "v_accvgpr_read_b32 v30, a246\n\t" "v_mfma_f32_16x16x32_bf16 v[40:43], a[144:147], a[104:107], v[40:43]\n\t" "s_mov_b32 m0, %[srl]\n\t"
    "s_nop 0\n\t" "global_load_lds_dwordx4 %[va], %[sr] offset:0 sc1\n\t" "v_accvgpr_read_b32 v31, a247\n\t" "v_mfma_f32_16x16x32_bf16 v[44:47], a[144:147], a[108:111], v[44:47]\n\t"
    "v_add_f32 v24, v24, v26\n\t" "s_waitcnt lgkmcnt(12)\n\t" "v_mfma_f32_16x16x32_bf16 v[48:51], a[148:151], a[96:99], v[48:51]\n\t" "ds_read_b64_tr_b16 a[144:145], %[vr1] offset:7200\n\t"
    "v_add_f32 v25, v25, v27\n\t" "s_add_u32 m0, %[sl], 0\n\t" "v_mfma_f32_16x16x32_bf16 v[52:55], a[148:151], a[100:103], v[52:55]\n\t" "global_load_lds_dwordx4 %[vb0], s[78:79]\n\t"
    "ds_read_b64_tr_b16 a[146:147], %[vr1] offset:10784\n\t" "s_load_dwordx2 s[98:99], s[84:85], 60\n\t" "v_add_f32 v28, v28, v30\n\t"
    "v_mfma_f32_16x16x32_bf16 v[56:59], a[148:151], a[104:107], v[56:59]\n\t" "s_mov_b32 m0, %[srl]\n\t" "s_nop 0\n\t" "global_load_lds_dwordx4 %[va], %[sr] offset:1024 sc1\n\t"
    "v_add_f32 v29, v29, v31\n\t" "v_mfma_f32_16x16x32_bf16 v[60:63], a[148:151], a[108:111], v[60:63]\n\t" "v_mov_b32 v26, v24\n\t" "s_waitcnt lgkmcnt(12)\n\t"
    "v_mfma_f32_16x16x32_bf16 v[64:67], a[152:155], a[96:99], v[64:67]\n\t" "ds_read_b64_tr_b16 a[148:149], %[vr3] offset:7168\n\t" "v_mov_b32 v27, v25\n\t"
    "v_mfma_f32_16x16x32_bf16 v[68:71], a[152:155], a[100:103], v[68:71]\n\t" "global_load_dwordx4 a[36:39], %[va], s[76:77] offset:1024 sc1\n\t" "ds_read_b64_tr_b16 a[150:151], %[vr3] offset:10752\n\t"
    "v_mov_b32 v30, v28\n\t" "v_mfma_f32_16x16x32_bf16 v[72:75], a[152:155], a[104:107], v[72:75]\n\t" "s_mov_b32 m0, %[srl]\n\t" "s_nop 0\n\t" "global_load_lds_dwordx4 %[va], %[sr] offset:2048 sc1\n\t"
    "v_mov_b32 v31, v29\n\t" "v_mfma_f32_16x16x32_bf16 v[76:79], a[152:155], a[108:111], v[76:79]\n\t" "s_nop 0\n\t" "s_waitcnt lgkmcnt(12)\n\t"
    "v_mfma_f32_16x16x32_bf16 v[80:83], a[156:159], a[96:99], v[80:83]\n\t" "ds_read_b64_tr_b16 a[152:153], %[vr3] offset:7200\n\t" "s_nop 0\n\t" "s_add_u32 m0, %[sl], 1024\n\t"
    "v_mfma_f32_16x16x32_bf16 v[84:87], a[156:159], a[100:103], v[84:87]\n\t" "global_load_lds_dwordx4 %[vb1], s[78:79]\n\t" "ds_read_b64_tr_b16 a[154:155], %[vr3] offset:10784\n\t"
    "v_permlane16_swap_b32 v24, v26\n\t" "v_mfma_f32_16x16x32_bf16 v[88:91], a[156:159], a[104:107], v[88:91]\n\t" "s_mov_b32 m0, %[srl]\n\t" "s_nop 0\n\t"
    "global_load_lds_dwordx4 %[va], %[sr] offset:3072 sc1\n\t" "v_permlane16_swap_b32 v25, v27\n\t" "v_mfma_f32_16x16x32_bf16 v[92:95], a[156:159], a[108:111], v[92:95]\n\t"
    "v_permlane16_swap_b32 v28, v30\n\t" "s_waitcnt lgkmcnt(12)\n\t" "v_mfma_f32_16x16x32_bf16 v[96:99], a[128:131], a[96:99], v[96:99]\n\t" "ds_read_b64_tr_b16 a[156:157], %[vr5] offset:7168\n\t"
    "v_permlane16_swap_b32 v29, v31\n\t" "v_mfma_f32_16x16x32_bf16 v[100:103], a[128:131], a[100:103], v[100:103]\n\t" "global_load_dwordx4 a[40:43], %[va], s[76:77] offset:2048 sc1\n\t"
    "ds_read_b64_tr_b16 a[158:159], %[vr5] offset:10752\n\t" "v_add_f32 v24, v24, v26\n\t" "v_mfma_f32_16x16x32_bf16 v[104:107], a[128:131], a[104:107], v[104:107]\n\t" "v_add_f32 v25, v25, v27\n\t"
    "v_mfma_f32_16x16x32_bf16 v[108:111], a[128:131], a[108:111], v[108:111]\n\t" "v_add_f32 v28, v28, v30\n\t" "s_waitcnt lgkmcnt(12)\n\t"
    "v_mfma_f32_16x16x32_bf16 v[112:115], a[132:135], a[96:99], v[112:115]\n\t" "ds_read_b64_tr_b16 a[128:129], %[vr5] offset:7200\n\t" "v_add_f32 v29, v29, v31\n\t" "s_add_u32 m0, %[sl], 2048\n\t"
    "v_mfma_f32_16x16x32_bf16 v[116:119], a[132:135], a[100:103], v[116:119]\n\t" "global_load_lds_dwordx4 %[vb2], s[78:79]\n\t" "ds_read_b64_tr_b16 a[130:131], %[vr5] offset:10784\n\t"
    "v_mov_b32 v26, v24\n\t" "v_mfma_f32_16x16x32_bf16 v[120:123], a[132:135], a[104:107], v[120:123]\n\t" "v_mov_b32 v27, v25\n\t"
    "v_mfma_f32_16x16x32_bf16 v[124:127], a[132:135], a[108:111], v[124:127]\n\t" "s_load_dwordx2 s[98:99], s[84:85], 188\n\t" "v_mov_b32 v30, v28\n\t" "s_waitcnt lgkmcnt(12)\n\t"
    "v_mfma_f32_16x16x32_bf16 v[128:131], a[136:139], a[96:99], v[128:131]\n\t" "ds_read_b64_tr_b16 a[132:133], %[vr7] offset:7168\n\t" "v_mov_b32 v31, v29\n\t"
    "v_mfma_f32_16x16x32_bf16 v[132:135], a[136:139], a[100:103], v[132:135]\n\t" "global_load_dwordx4 a[44:47], %[va], s[76:77] offset:3072 sc1\n\t" "s_add_u32 s76, s76, 4096\n\t"
    "s_addc_u32 s77, s77, 0\n\t" "ds_read_b64_tr_b16 a[134:135], %[vr7] offset:10752\n\t" "s_nop 0\n\t" "v_mfma_f32_16x16x32_bf16 v[136:139], a[136:139], a[104:107], v[136:139]\n\t" "s_nop 0\n\t"
    "v_mfma_f32_16x16x32_bf16 v[140:143], a[136:139], a[108:111], v[140:143]\n\t" "v_permlane32_swap_b32 v24, v26\n\t" "s_waitcnt lgkmcnt(12)\n\t"
    "v_mfma_f32_16x16x32_bf16 v[144:147], a[140:143], a[96:99], v[144:147]\n\t" "s_waitcnt vmcnt(27)\n\t" "s_barrier\n\t" "ds_read_b64_tr_b16 a[136:137], %[vr1] offset:14336\n\t"
    "v_permlane32_swap_b32 v25, v27\n\t" "s_add_u32 m0, %[sl], 3072\n\t" "v_mfma_f32_16x16x32_bf16 v[148:151], a[140:143], a[100:103], v[148:151]\n\t" "s_mov_b64 exec, s[82:83]\n\t"
    "global_load_lds_dwordx4 %[vb3], s[78:79]\n\t" "s_mov_b64 exec, -1\n\t" "s_add_u32 s78, s78, 229376\n\t" "s_addc_u32 s79, s79, 0\n\t" "ds_read_b64_tr_b16 a[138:139], %[vr1] offset:17920\n\t"
    "v_permlane32_swap_b32 v28, v30\n\t" "v_mfma_f32_16x16x32_bf16 v[152:155], a[140:143], a[104:107], v[152:155]\n\t" "v_permlane32_swap_b32 v29, v31\n\t"
    "v_mfma_f32_16x16x32_bf16 v[156:159], a[140:143], a[108:111], v[156:159]\n\t" "v_add_f32 v24, v24, v26\n\t" "s_waitcnt lgkmcnt(12)\n\t"
    "v_mfma_f32_16x16x32_bf16 v[160:163], a[144:147], a[96:99], v[160:163]\n\t" "ds_read_b64_tr_b16 a[140:141], %[vr1] offset:14368\n\t" "v_add_f32 v25, v25, v27\n\t"
    "v_mfma_f32_16x16x32_bf16 v[164:167], a[144:147], a[100:103], v[164:167]\n\t" "global_load_dwordx4 a[48:51], %[va], s[76:77] offset:0 sc1\n\t" "ds_read_b64_tr_b16 a[142:143], %[vr1] offset:17952\n\t"
    "v_add_f32 v28, v28, v30\n\t" "v_mfma_f32_16x16x32_bf16 v[168:171], a[144:147], a[104:107], v[168:171]\n\t" "v_add_f32 v29, v29, v31\n\t"
    "v_mfma_f32_16x16x32_bf16 v[172:175], a[144:147], a[108:111], v[172:175]\n\t" "v_mul_f32 v24, 0xba800000, v24\n\t" "s_waitcnt lgkmcnt(12)\n\t"
    "v_mfma_f32_16x16x32_bf16 v[176:179], a[148:151], a[96:99], v[176:179]\n\t" "ds_read_b64_tr_b16 a[144:145], %[vr3] offset:14336\n\t" "v_mul_f32 v25, 0x3a800000, v25\n\t"
    "s_add_u32 m0, %[sl], 14336\n\t" "v_mfma_f32_16x16x32_bf16 v[180:183], a[148:151], a[100:103], v[180:183]\n\t" "global_load_lds_dwordx4 %[vb0], s[78:79]\n\t"
    "ds_read_b64_tr_b16 a[146:147], %[vr3] offset:17920\n\t" "v_mul_f32 v28, 0xba800000, v28\n\t" "v_mfma_f32_16x16x32_bf16 v[184:187], a[148:151], a[104:107], v[184:187]\n\t"
    "v_mul_f32 v29, 0x3a800000, v29\n\t" "v_mfma_f32_16x16x32_bf16 v[188:191], a[148:151], a[108:111], v[188:191]\n\t" "v_fma_f32 v25, -v24, v24, v25\n\t" "s_waitcnt lgkmcnt(12)\n\t"
    "v_mfma_f32_16x16x32_bf16 v[192:195], a[152:155], a[96:99], v[192:195]\n\t" "ds_read_b64_tr_b16 a[148:149], %[vr3] offset:14368\n\t" "v_and_b32 v26, 0xffff0000, v24\n\t"
    "v_mfma_f32_16x16x32_bf16 v[196:199], a[152:155], a[100:103], v[196:199]\n\t" "global_load_dwordx4 a[52:55], %[va], s[76:77] offset:1024 sc1\n\t"
    "ds_read_b64_tr_b16 a[150:151], %[vr3] offset:17952\n\t" "s_load_dwordx2 s[98:99], s[84:85], 316\n\t" "v_fma_f32 v29, -v28, v28, v29\n\t"
    "v_mfma_f32_16x16x32_bf16 v[200:203], a[152:155], a[104:107], v[200:203]\n\t" "v_and_b32 v30, 0xffff0000, v28\n\t" "v_mfma_f32_16x16x32_bf16 v[204:207], a[152:155], a[108:111], v[204:207]\n\t"
    "v_max_f32 v25, 0, v25\n\t" "s_waitcnt lgkmcnt(12)\n\t" "v_mfma_f32_16x16x32_bf16 v[208:211], a[156:159], a[96:99], v[208:211]\n\t" "ds_read_b64_tr_b16 a[152:153], %[vr5] offset:14336\n\t"
    "v_sub_f32 v24, v24, v26\n\t" "s_add_u32 m0, %[sl], 15360\n\t" "v_mfma_f32_16x16x32_bf16 v[212:215], a[156:159], a[100:103], v[212:215]\n\t" "global_load_lds_dwordx4 %[vb1], s[78:79]\n\t"
    "ds_read_b64_tr_b16 a[154:155], %[vr5] offset:17920\n\t" "v_max_f32 v29, 0, v29\n\t" "v_mfma_f32_16x16x32_bf16 v[216:219], a[156:159], a[104:107], v[216:219]\n\t" "v_sub_f32 v28, v28, v30\n\t"
    "v_mfma_f32_16x16x32_bf16 v[220:223], a[156:159], a[108:111], v[220:223]\n\t" "v_add_f32 v25, 0x3727c5ac, v25\n\t" "s_waitcnt lgkmcnt(12)\n\t"
    "v_mfma_f32_16x16x32_bf16 v[224:227], a[128:131], a[96:99], v[224:227]\n\t" "ds_read_b64_tr_b16 a[156:157], %[vr5] offset:14368\n\t" "v_cvt_pk_bf16_f32 v26, v26, v26\n\t"
    "v_mfma_f32_16x16x32_bf16 v[228:231], a[128:131], a[100:103], v[228:231]\n\t" "ds_read_b64_tr_b16 a[158:159], %[vr5] offset:17952\n\t" "v_add_f32 v29, 0x3727c5ac, v29\n\t"
    "v_mfma_f32_16x16x32_bf16 v[232:235], a[128:131], a[104:107], v[232:235]\n\t" "v_cvt_pk_bf16_f32 v30, v30, v30\n\t" "v_mfma_f32_16x16x32_bf16 v[236:239], a[128:131], a[108:111], v[236:239]\n\t"
    "v_rsq_f32 v27, v25\n\t" "s_waitcnt lgkmcnt(12)\n\t" "v_mfma_f32_16x16x32_bf16 v[240:243], a[132:135], a[96:99], v[240:243]\n\t" "ds_read_b64_tr_b16 a[128:129], %[vr7] offset:14336\n\t"
    "v_cvt_pk_bf16_f32 v24, v24, v24\n\t" "v_mfma_f32_16x16x32_bf16 v[244:247], a[132:135], a[100:103], v[244:247]\n\t" "ds_read_b64_tr_b16 a[130:131], %[vr7] offset:17920\n\t" "v_rsq_f32 v31, v29\n\t"
    "v_mfma_f32_16x16x32_bf16 v[248:251], a[132:135], a[104:107], v[248:251]\n\t" "v_cvt_pk_bf16_f32 v28, v28, v28\n\t" "v_mfma_f32_16x16x32_bf16 v[252:255], a[132:135], a[108:111], v[252:255]\n\t"
    "v_mul_f32 v22, v25, v27\n\t" "s_waitcnt lgkmcnt(12)\n\t" "v_mfma_f32_16x16x32_bf16 v[32:35], a[136:139], a[112:115], v[32:35]\n\t" "ds_read_b64_tr_b16 a[132:133], %[vr1] offset:21504\n\t"
    "v_mul_f32 v18, 0xbfb8aa3b, v27\n\t" "v_mfma_f32_16x16x32_bf16 v[36:39], a[136:139], a[116:119], v[36:39]\n\t" "global_load_dwordx4 a[56:59], %[va], s[76:77] offset:2048 sc1\n\t"
    "ds_read_b64_tr_b16 a[134:135], %[vr1] offset:25088\n\t" "v_mul_f32 v23, v29, v31\n\t" "v_mfma_f32_16x16x32_bf16 v[40:43], a[136:139], a[120:123], v[40:43]\n\t" "v_mul_f32 v19, 0xbfb8aa3b, v31\n\t"
    "v_mfma_f32_16x16x32_bf16 v[44:47], a[136:139], a[124:127], v[44:47]\n\t" "v_and_b32 v27, 0xffff0000, v22\n\t" "s_waitcnt lgkmcnt(12)\n\t"
    "v_mfma_f32_16x16x32_bf16 v[48:51], a[140:143], a[112:115], v[48:51]\n\t" "ds_read_b64_tr_b16 a[136:137], %[vr1] offset:21536\n\t" "s_nop 0\n\t" "s_add_u32 m0, %[sl], 16384\n\t"
    "v_mfma_f32_16x16x32_bf16 v[52:55], a[140:143], a[116:119], v[52:55]\n\t" "global_load_lds_dwordx4 %[vb2], s[78:79]\n\t" "ds_read_b64_tr_b16 a[138:139], %[vr1] offset:25120\n\t"
    "v_and_b32 v31, 0xffff0000, v23\n\t" "v_mfma_f32_16x16x32_bf16 v[56:59], a[140:143], a[120:123], v[56:59]\n\t" "v_sub_f32 v25, v22, v27\n\t"
    "v_mfma_f32_16x16x32_bf16 v[60:63], a[140:143], a[124:127], v[60:63]\n\t" "v_cvt_pk_bf16_f32 v27, v27, v27\n\t" "s_waitcnt lgkmcnt(12)\n\t"
    "v_mfma_f32_16x16x32_bf16 v[64:67], a[144:147], a[112:115], v[64:67]\n\t" "ds_read_b64_tr_b16 a[140:141], %[vr3] offset:21504\n\t" "v_sub_f32 v29, v23, v31\n\t"
    "v_mfma_f32_16x16x32_bf16 v[68:71], a[144:147], a[116:119], v[68:71]\n\t" "global_load_dwordx4 a[60:63], %[va], s[76:77] offset:3072 sc1\n\t" "s_add_u32 s76, s76, 4096\n\t"
    "s_addc_u32 s77, s77, 0\n\t" "ds_read_b64_tr_b16 a[142:143], %[vr3] offset:25088\n\t" "v_cvt_pk_bf16_f32 v31, v31, v31\n\t" "v_mfma_f32_16x16x32_bf16 v[72:75], a[144:147], a[120:123], v[72:75]\n\t"
    "v_cvt_pk_bf16_f32 v25, v25, v25\n\t" "v_mfma_f32_16x16x32_bf16 v[76:79], a[144:147], a[124:127], v[76:79]\n\t" "s_nop 0\n\t" "s_waitcnt lgkmcnt(12)\n\t"
    "v_mfma_f32_16x16x32_bf16 v[80:83], a[148:151], a[112:115], v[80:83]\n\t" "ds_read_b64_tr_b16 a[144:145], %[vr3] offset:21536\n\t" "v_cvt_pk_bf16_f32 v29, v29, v29\n\t"
    "s_add_u32 m0, %[sl], 17408\n\t" "v_mfma_f32_16x16x32_bf16 v[84:87], a[148:151], a[116:119], v[84:87]\n\t" "s_mov_b64 exec, s[82:83]\n\t" "global_load_lds_dwordx4 %[vb3], s[78:79]\n\t"
    "s_mov_b64 exec, -1\n\t" "s_add_u32 s78, s78, 229376\n\t" "s_addc_u32 s79, s79, 0\n\t" "ds_read_b64_tr_b16 a[146:147], %[vr3] offset:25120\n\t" "s_mov_b64 exec, 0xffff\n\t"
    "v_accvgpr_write_b32 a208, v26\n\t" "v_accvgpr_write_b32 a209, v24\n\t" "v_accvgpr_write_b32 a210, v27\n\t" "v_accvgpr_write_b32 a211, v25\n\t" "v_accvgpr_write_b32 a212, v30\n\t"
    "v_accvgpr_write_b32 a213, v28\n\t" "v_accvgpr_write_b32 a214, v31\n\t" "v_accvgpr_write_b32 a215, v29\n\t" "s_mov_b64 exec, -1\n\t"
    "v_mfma_f32_16x16x32_bf16 v[88:91], a[148:151], a[120:123], v[88:91]\n\t" "v_mfma_f32_16x16x32_bf16 v[92:95], a[148:151], a[124:127], v[92:95]\n\t" "s_waitcnt lgkmcnt(12)\n\t"
    "v_mfma_f32_16x16x32_bf16 v[96:99], a[152:155], a[112:115], v[96:99]\n\t" "ds_read_b64_tr_b16 a[148:149], %[vr5] offset:21504\n\t"
    "v_mfma_f32_16x16x32_bf16 v[100:103], a[152:155], a[116:119], v[100:103]\n\t" "global_load_dwordx4 a[64:67], %[va], s[76:77] offset:0 sc1\n\t" "ds_read_b64_tr_b16 a[150:151], %[vr5] offset:25088\n\t"
    "v_mfma_f32_16x16x32_bf16 v[104:107], a[152:155], a[120:123], v[104:107]\n\t" "v_mfma_f32_16x16x32_bf16 v[108:111], a[152:155], a[124:127], v[108:111]\n\t" "s_waitcnt lgkmcnt(12)\n\t"
    "v_mfma_f32_16x16x32_bf16 v[112:115], a[156:159], a[112:115], v[112:115]\n\t" "ds_read_b64_tr_b16 a[152:153], %[vr5] offset:21536\n\t" "s_add_u32 m0, %[sl], 28672\n\t"
    "v_mfma_f32_16x16x32_bf16 v[116:119], a[156:159], a[116:119], v[116:119]\n\t" "global_load_lds_dwordx4 %[vb0], s[78:79]\n\t" "ds_read_b64_tr_b16 a[154:155], %[vr5] offset:25120\n\t"
    "v_mfma_f32_16x16x32_bf16 v[120:123], a[156:159], a[120:123], v[120:123]\n\t" "v_mfma_f32_16x16x32_bf16 v[124:127], a[156:159], a[124:127], v[124:127]\n\t" "s_waitcnt lgkmcnt(12)\n\t"
    "v_mfma_f32_16x16x32_bf16 v[128:131], a[128:131], a[112:115], v[128:131]\n\t" "ds_read_b64_tr_b16 a[156:157], %[vr7] offset:21504\n\t"
    "v_mfma_f32_16x16x32_bf16 v[132:135], a[128:131], a[116:119], v[132:135]\n\t" "global_load_dwordx4 a[68:71], %[va], s[76:77] offset:1024 sc1\n\t"
    "ds_read_b64_tr_b16 a[158:159], %[vr7] offset:25088\n\t" "v_mfma_f32_16x16x32_bf16 v[136:139], a[128:131], a[120:123], v[136:139]\n\t"
    "v_mfma_f32_16x16x32_bf16 v[140:143], a[128:131], a[124:127], v[140:143]\n\t" "s_waitcnt lgkmcnt(12)\n\t" "v_mfma_f32_16x16x32_bf16 v[144:147], a[132:135], a[112:115], v[144:147]\n\t"
    "s_waitcnt vmcnt(15)\n\t" "s_barrier\n\t" "ds_read_b64_tr_b16 a[128:129], %[vr1] offset:28672\n\t" "s_add_u32 m0, %[sl], 29696\n\t"
    "v_mfma_f32_16x16x32_bf16 v[148:151], a[132:135], a[116:119], v[148:151]\n\t" "global_load_lds_dwordx4 %[vb1], s[78:79]\n\t" "ds_read_b64_tr_b16 a[130:131], %[vr1] offset:32256\n\t"
    "v_mfma_f32_16x16x32_bf16 v[152:155], a[132:135], a[120:123], v[152:155]\n\t" "v_mfma_f32_16x16x32_bf16 v[156:159], a[132:135], a[124:127], v[156:159]\n\t" "s_waitcnt lgkmcnt(12)\n\t"
    "v_mfma_f32_16x16x32_bf16 v[160:163], a[136:139], a[112:115], v[160:163]\n\t" "ds_read_b64_tr_b16 a[132:133], %[vr1] offset:28704\n\t"
    "v_mfma_f32_16x16x32_bf16 v[164:167], a[136:139], a[116:119], v[164:167]\n\t" "global_load_dwordx4 a[72:75], %[va], s[76:77] offset:2048 sc1\n\t"
    "ds_read_b64_tr_b16 a[134:135], %[vr1] offset:32288\n\t" "v_mfma_f32_16x16x32_bf16 v[168:171], a[136:139], a[120:123], v[168:171]\n\t"
    "v_mfma_f32_16x16x32_bf16 v[172:175], a[136:139], a[124:127], v[172:175]\n\t" "s_waitcnt lgkmcnt(12)\n\t" "v_mfma_f32_16x16x32_bf16 v[176:179], a[140:143], a[112:115], v[176:179]\n\t"
    "ds_read_b64_tr_b16 a[136:137], %[vr3] offset:28672\n\t" "s_add_u32 m0, %[sl], 30720\n\t" "v_mfma_f32_16x16x32_bf16 v[180:183], a[140:143], a[116:119], v[180:183]\n\t"
    "global_load_lds_dwordx4 %[vb2], s[78:79]\n\t" "ds_read_b64_tr_b16 a[138:139], %[vr3] offset:32256\n\t" "v_mfma_f32_16x16x32_bf16 v[184:187], a[140:143], a[120:123], v[184:187]\n\t"
    "v_mfma_f32_16x16x32_bf16 v[188:191], a[140:143], a[124:127], v[188:191]\n\t" "s_waitcnt lgkmcnt(12)\n\t" "v_mfma_f32_16x16x32_bf16 v[192:195], a[144:147], a[112:115], v[192:195]\n\t"
    "ds_read_b64_tr_b16 a[140:141], %[vr3] offset:28704\n\t" "v_mfma_f32_16x16x32_bf16 v[196:199], a[144:147], a[116:119], v[196:199]\n\t"
    "global_load_dwordx4 a[76:79], %[va], s[76:77] offset:3072 sc1\n\t" "s_add_u32 s76, s76, 4096\n\t" "s_addc_u32 s77, s77, 0\n\t" "ds_read_b64_tr_b16 a[142:143], %[vr3] offset:32288\n\t"
    "v_mfma_f32_16x16x32_bf16 v[200:203], a[144:147], a[120:123], v[200:203]\n\t" "v_mfma_f32_16x16x32_bf16 v[204:207], a[144:147], a[124:127], v[204:207]\n\t" "s_waitcnt lgkmcnt(12)\n\t"
    "v_mfma_f32_16x16x32_bf16 v[208:211], a[148:151], a[112:115], v[208:211]\n\t" "ds_read_b64_tr_b16 a[144:145], %[vr5] offset:28672\n\t" "s_add_u32 m0, %[sl], 31744\n\t"
    "v_mfma_f32_16x16x32_bf16 v[212:215], a[148:151], a[116:119], v[212:215]\n\t" "s_mov_b64 exec, s[82:83]\n\t" "global_load_lds_dwordx4 %[vb3], s[78:79]\n\t" "s_mov_b64 exec, -1\n\t"
    "s_add_u32 s78, s78, 229376\n\t" "s_addc_u32 s79, s79, 0\n\t" "ds_read_b64_tr_b16 a[146:147], %[vr5] offset:32256\n\t" "v_mfma_f32_16x16x32_bf16 v[216:219], a[148:151], a[120:123], v[216:219]\n\t"
    "v_mfma_f32_16x16x32_bf16 v[220:223], a[148:151], a[124:127], v[220:223]\n\t" "s_waitcnt lgkmcnt(12)\n\t" "v_mfma_f32_16x16x32_bf16 v[224:227], a[152:155], a[112:115], v[224:227]\n\t"
    "ds_read_b64_tr_b16 a[148:149], %[vr5] offset:28704\n\t" "v_mfma_f32_16x16x32_bf16 v[228:231], a[152:155], a[116:119], v[228:231]\n\t" "ds_read_b64_tr_b16 a[150:151], %[vr5] offset:32288\n\t"
    "v_mfma_f32_16x16x32_bf16 v[232:235], a[152:155], a[120:123], v[232:235]\n\t" "v_mfma_f32_16x16x32_bf16 v[236:239], a[152:155], a[124:127], v[236:239]\n\t" "s_waitcnt lgkmcnt(12)\n\t"
    "v_mfma_f32_16x16x32_bf16 v[240:243], a[156:159], a[112:115], v[240:243]\n\t" "ds_read_b64_tr_b16 a[152:153], %[vr7] offset:28672\n\t"
    "v_mfma_f32_16x16x32_bf16 v[244:247], a[156:159], a[116:119], v[244:247]\n\t" "ds_read_b64_tr_b16 a[154:155], %[vr7] offset:32256\n\t"
    "v_mfma_f32_16x16x32_bf16 v[248:251], a[156:159], a[120:123], v[248:251]\n\t" "v_mfma_f32_16x16x32_bf16 v[252:255], a[156:159], a[124:127], v[252:255]\n\t" "s_waitcnt lgkmcnt(12)\n\t"
    "v_mfma_f32_16x16x32_bf16 v[32:35], a[128:131], a[0:3], v[32:35]\n\t" "ds_read_b64_tr_b16 a[156:157], %[vr1] offset:35840\n\t" "v_accvgpr_read_b32 v31, %[cr0]\n\t"
    "v_mfma_f32_16x16x32_bf16 v[36:39], a[128:131], a[4:7], v[36:39]\n\t" "global_load_dwordx4 a[80:83], %[va], s[76:77] offset:0 sc1\n\t" "ds_read_b64_tr_b16 a[158:159], %[vr1] offset:39424\n\t"
    "v_mfma_f32_16x16x32_bf16 v[40:43], a[128:131], a[8:11], v[40:43]\n\t" "ds_read2_b32 v[24:25], v31 offset0:0 offset1:4\n\t" "v_mfma_f32_16x16x32_bf16 v[44:47], a[128:131], a[12:15], v[44:47]\n\t"
    "ds_read2_b32 v[26:27], v31 offset0:8 offset1:12\n\t" "ds_read2_b32 v[28:29], v31 offset0:16 offset1:20\n\t" "s_waitcnt lgkmcnt(15)\n\t"
    "v_mfma_f32_16x16x32_bf16 v[48:51], a[132:135], a[0:3], v[48:51]\n\t" "ds_read_b64_tr_b16 a[128:129], %[vr1] offset:35872\n\t" "ds_read2_b32 v[30:31], v31 offset0:24 offset1:28\n\t"
    "s_add_u32 m0, %[sl], 43008\n\t" "v_mfma_f32_16x16x32_bf16 v[52:55], a[132:135], a[4:7], v[52:55]\n\t" "global_load_lds_dwordx4 %[vb0], s[78:79]\n\t"
    "ds_read_b64_tr_b16 a[130:131], %[vr1] offset:39456\n\t" "v_mfma_f32_16x16x32_bf16 v[56:59], a[132:135], a[8:11], v[56:59]\n\t" "v_mfma_f32_16x16x32_bf16 v[60:63], a[132:135], a[12:15], v[60:63]\n\t"
    "s_waitcnt lgkmcnt(15)\n\t" "v_mfma_f32_16x16x32_bf16 v[64:67], a[136:139], a[0:3], v[64:67]\n\t" "ds_read_b64_tr_b16 a[132:133], %[vr3] offset:35840\n\t"
    "v_mfma_f32_16x16x32_bf16 v[68:71], a[136:139], a[4:7], v[68:71]\n\t" "global_load_dwordx4 a[84:87], %[va], s[76:77] offset:1024 sc1\n\t" "ds_read_b64_tr_b16 a[134:135], %[vr3] offset:39424\n\t"
    "v_mfma_f32_16x16x32_bf16 v[72:75], a[136:139], a[8:11], v[72:75]\n\t" "v_mfma_f32_16x16x32_bf16 v[76:79], a[136:139], a[12:15], v[76:79]\n\t" "s_waitcnt lgkmcnt(3)\n\t" "v_add_f32 v24, v24, v25\n\t"
    "v_mfma_f32_16x16x32_bf16 v[80:83], a[140:143], a[0:3], v[80:83]\n\t" "ds_read_b64_tr_b16 a[136:137], %[vr3] offset:35872\n\t" "v_add_f32 v26, v26, v27\n\t" "s_add_u32 m0, %[sl], 44032\n\t"
    "v_mfma_f32_16x16x32_bf16 v[84:87], a[140:143], a[4:7], v[84:87]\n\t" "global_load_lds_dwordx4 %[vb1], s[78:79]\n\t" "ds_read_b64_tr_b16 a[138:139], %[vr3] offset:39456\n\t"
    "v_add_f32 v28, v28, v29\n\t" "v_add_f32 v30, v30, v31\n\t" "v_mfma_f32_16x16x32_bf16 v[88:91], a[140:143], a[8:11], v[88:91]\n\t" "v_add_f32 v24, v24, v26\n\t"
    "v_mfma_f32_16x16x32_bf16 v[92:95], a[140:143], a[12:15], v[92:95]\n\t" "v_add_f32 v28, v28, v30\n\t" "v_mfma_f32_16x16x32_bf16 v[96:99], a[144:147], a[0:3], v[96:99]\n\t"
    "ds_read_b64_tr_b16 a[140:141], %[vr5] offset:35840\n\t" "v_add_f32 v24, v24, v28\n\t" "v_mfma_f32_16x16x32_bf16 v[100:103], a[144:147], a[4:7], v[100:103]\n\t"
    "global_load_dwordx4 a[88:91], %[va], s[76:77] offset:2048 sc1\n\t" "ds_read_b64_tr_b16 a[142:143], %[vr5] offset:39424\n\t" "v_mfma_f32_16x16x32_bf16 v[104:107], a[144:147], a[8:11], v[104:107]\n\t"
    "v_cmp_u_f32 vcc, v24, v24\n\t" "v_mfma_f32_16x16x32_bf16 v[108:111], a[144:147], a[12:15], v[108:111]\n\t" "v_mfma_f32_16x16x32_bf16 v[112:115], a[148:151], a[0:3], v[112:115]\n\t"
    "ds_read_b64_tr_b16 a[144:145], %[vr5] offset:35872\n\t" "s_add_u32 m0, %[sl], 45056\n\t" "v_mfma_f32_16x16x32_bf16 v[116:119], a[148:151], a[4:7], v[116:119]\n\t"
    "global_load_lds_dwordx4 %[vb2], s[78:79]\n\t" "ds_read_b64_tr_b16 a[146:147], %[vr5] offset:39456\n\t" "v_mfma_f32_16x16x32_bf16 v[120:123], a[148:151], a[8:11], v[120:123]\n\t"
    "v_mfma_f32_16x16x32_bf16 v[124:127], a[148:151], a[12:15], v[124:127]\n\t" "s_nop 0\n\t" "s_cbranch_vccnz .Lslow0_%=\n\t" ".Lgo0_%=:\n\t" "v_accvgpr_read_b32 v27, %[cw]\n\t"
    "v_mfma_f32_16x16x32_bf16 v[128:131], a[152:155], a[0:3], v[128:131]\n\t" "ds_read_b64_tr_b16 a[148:149], %[vr7] offset:35840\n\t" "v_and_b32 v25, 0xffff0000, v24\n\t"
    "v_mfma_f32_16x16x32_bf16 v[132:135], a[152:155], a[4:7], v[132:135]\n\t" "global_load_dwordx4 a[92:95], %[va], s[76:77] offset:3072 sc1\n\t" "s_add_u32 s76, s76, 4096\n\t"
    "s_addc_u32 s77, s77, 0\n\t" "ds_read_b64_tr_b16 a[150:151], %[vr7] offset:39424\n\t" "v_mfma_f32_16x16x32_bf16 v[136:139], a[152:155], a[8:11], v[136:139]\n\t" "v_add_u32 v27, 0x0, v27\n\t"
    "v_mfma_f32_16x16x32_bf16 v[140:143], a[152:155], a[12:15], v[140:143]\n\t" "v_sub_f32 v24, v24, v25\n\t" "v_mfma_f32_16x16x32_bf16 v[144:147], a[156:159], a[0:3], v[144:147]\n\t" "s_barrier\n\t"
    "ds_read_b64_tr_b16 a[152:153], %[vr1] offset:43008\n\t" "v_cvt_pk_bf16_f32 v24, v25, v24\n\t" "s_add_u32 m0, %[sl], 46080\n\t"
    "v_mfma_f32_16x16x32_bf16 v[148:151], a[156:159], a[4:7], v[148:151]\n\t" "s_mov_b64 exec, s[82:83]\n\t" "global_load_lds_dwordx4 %[vb3], s[78:79]\n\t" "s_mov_b64 exec, -1\n\t"
    "s_add_u32 s78, s78, 229376\n\t" "s_addc_u32 s79, s79, 0\n\t" "ds_read_b64_tr_b16 a[154:155], %[vr1] offset:46592\n\t" "v_mfma_f32_16x16x32_bf16 v[152:155], a[156:159], a[8:11], v[152:155]\n\t"
    "ds_write2_b32 v27, v24, v24 offset0:0 offset1:1\n\t" "v_mfma_f32_16x16x32_bf16 v[156:159], a[156:159], a[12:15], v[156:159]\n\t" "v_accvgpr_read_b32 v31, %[cr1]\n\t" "s_waitcnt lgkmcnt(13)\n\t"
    "v_mfma_f32_16x16x32_bf16 v[160:163], a[128:131], a[0:3], v[160:163]\n\t" "ds_read_b64_tr_b16 a[156:157], %[vr1] offset:43040\n\t" "ds_read2_b32 v[24:25], v31 offset0:0 offset1:4\n\t"
    "v_mfma_f32_16x16x32_bf16 v[164:167], a[128:131], a[4:7], v[164:167]\n\t" "global_load_dwordx4 a[96:99], %[va], s[76:77] offset:0 sc1\n\t" "ds_read_b64_tr_b16 a[158:159], %[vr1] offset:46624\n\t"
    "ds_read2_b32 v[26:27], v31 offset0:8 offset1:12\n\t" "ds_read2_b32 v[28:29], v31 offset0:16 offset1:20\n\t" "v_mfma_f32_16x16x32_bf16 v[168:171], a[128:131], a[8:11], v[168:171]\n\t"
    "ds_read2_b32 v[30:31], v31 offset0:24 offset1:28\n\t" "v_mfma_f32_16x16x32_bf16 v[172:175], a[128:131], a[12:15], v[172:175]\n\t" "s_waitcnt lgkmcnt(15)\n\t"
    "v_mfma_f32_16x16x32_bf16 v[176:179], a[132:135], a[0:3], v[176:179]\n\t" "ds_read_b64_tr_b16 a[128:129], %[vr3] offset:43008\n\t" "s_add_u32 m0, %[sl], 57344\n\t"
    "v_mfma_f32_16x16x32_bf16 v[180:183], a[132:135], a[4:7], v[180:183]\n\t" "global_load_lds_dwordx4 %[vb0], s[78:79]\n\t" "ds_read_b64_tr_b16 a[130:131], %[vr3] offset:46592\n\t"
    "v_mfma_f32_16x16x32_bf16 v[184:187], a[132:135], a[8:11], v[184:187]\n\t" "v_mfma_f32_16x16x32_bf16 v[188:191], a[132:135], a[12:15], v[188:191]\n\t"
    "v_mfma_f32_16x16x32_bf16 v[192:195], a[136:139], a[0:3], v[192:195]\n\t" "ds_read_b64_tr_b16 a[132:133], %[vr3] offset:43040\n\t"
    "v_mfma_f32_16x16x32_bf16 v[196:199], a[136:139], a[4:7], v[196:199]\n\t" "global_load_dwordx4 a[100:103], %[va], s[76:77] offset:1024 sc1\n\t" "ds_read_b64_tr_b16 a[134:135], %[vr3] offset:46624\n\t"
    "s_waitcnt lgkmcnt(4)\n\t" "v_add_f32 v24, v24, v25\n\t" "v_mfma_f32_16x16x32_bf16 v[200:203], a[136:139], a[8:11], v[200:203]\n\t" "v_add_f32 v26, v26, v27\n\t"
    "v_mfma_f32_16x16x32_bf16 v[204:207], a[136:139], a[12:15], v[204:207]\n\t" "v_add_f32 v28, v28, v29\n\t" "v_add_f32 v30, v30, v31\n\t"
    "v_mfma_f32_16x16x32_bf16 v[208:211], a[140:143], a[0:3], v[208:211]\n\t" "ds_read_b64_tr_b16 a[136:137], %[vr5] offset:43008\n\t" "v_add_f32 v24, v24, v26\n\t" "s_add_u32 m0, %[sl], 58368\n\t"
    "v_mfma_f32_16x16x32_bf16 v[212:215], a[140:143], a[4:7], v[212:215]\n\t" "global_load_lds_dwordx4 %[vb1], s[78:79]\n\t" "ds_read_b64_tr_b16 a[138:139], %[vr5] offset:46592\n\t"
    "v_add_f32 v28, v28, v30\n\t" "v_mfma_f32_16x16x32_bf16 v[216:219], a[140:143], a[8:11], v[216:219]\n\t" "v_add_f32 v24, v24, v28\n\t"
    "v_mfma_f32_16x16x32_bf16 v[220:223], a[140:143], a[12:15], v[220:223]\n\t" "v_mfma_f32_16x16x32_bf16 v[224:227], a[144:147], a[0:3], v[224:227]\n\t"
    "ds_read_b64_tr_b16 a[140:141], %[vr5] offset:43040\n\t" "v_cmp_u_f32 vcc, v24, v24\n\t" "v_mfma_f32_16x16x32_bf16 v[228:231], a[144:147], a[4:7], v[228:231]\n\t"
    "ds_read_b64_tr_b16 a[142:143], %[vr5] offset:46624\n\t" "v_mfma_f32_16x16x32_bf16 v[232:235], a[144:147], a[8:11], v[232:235]\n\t"
    "v_mfma_f32_16x16x32_bf16 v[236:239], a[144:147], a[12:15], v[236:239]\n\t" "v_mfma_f32_16x16x32_bf16 v[240:243], a[148:151], a[0:3], v[240:243]\n\t"
    "ds_read_b64_tr_b16 a[144:145], %[vr7] offset:43008\n\t" "v_mfma_f32_16x16x32_bf16 v[244:247], a[148:151], a[4:7], v[244:247]\n\t" "ds_read_b64_tr_b16 a[146:147], %[vr7] offset:46592\n\t"
    "s_nop 0\n\t" "s_cbranch_vccnz .Lslow1_%=\n\t" ".Lgo1_%=:\n\t" "v_accvgpr_read_b32 v27, %[cw]\n\t" "v_mfma_f32_16x16x32_bf16 v[248:251], a[148:151], a[8:11], v[248:251]\n\t"
    "v_and_b32 v25, 0xffff0000, v24\n\t" "v_mfma_f32_16x16x32_bf16 v[252:255], a[148:151], a[12:15], v[252:255]\n\t" "v_mfma_f32_16x16x32_bf16 v[32:35], a[152:155], a[16:19], v[32:35]\n\t"
    "ds_read_b64_tr_b16 a[148:149], %[vr1] offset:50176\n\t" "v_add_u32 v27, 0x800, v27\n\t" "v_mfma_f32_16x16x32_bf16 v[36:39], a[152:155], a[20:23], v[36:39]\n\t"
    "global_load_dwordx4 a[104:107], %[va], s[76:77] offset:2048 sc1\n\t" "ds_read_b64_tr_b16 a[150:151], %[vr1] offset:53760\n\t" "v_sub_f32 v24, v24, v25\n\t"
    "v_mfma_f32_16x16x32_bf16 v[40:43], a[152:155], a[24:27], v[40:43]\n\t" "v_cvt_pk_bf16_f32 v24, v25, v24\n\t" "v_mfma_f32_16x16x32_bf16 v[44:47], a[152:155], a[28:31], v[44:47]\n\t"
    "v_mfma_f32_16x16x32_bf16 v[48:51], a[156:159], a[16:19], v[48:51]\n\t" "ds_read_b64_tr_b16 a[152:153], %[vr1] offset:50208\n\t" "s_cmp_lg_u64 %[m01], 0\n\t" "s_cselect_b64 exec, -1, 0\n\t"
    "ds_write2_b32 v27, v24, v24 offset0:0 offset1:1\n\t" "s_mov_b64 exec, -1\n\t" "s_add_u32 m0, %[sl], 59392\n\t" "v_mfma_f32_16x16x32_bf16 v[52:55], a[156:159], a[20:23], v[52:55]\n\t"
    "global_load_lds_dwordx4 %[vb2], s[78:79]\n\t" "ds_read_b64_tr_b16 a[154:155], %[vr1] offset:53792\n\t" "v_mfma_f32_16x16x32_bf16 v[56:59], a[156:159], a[24:27], v[56:59]\n\t"
    "v_mfma_f32_16x16x32_bf16 v[60:63], a[156:159], a[28:31], v[60:63]\n\t" "s_waitcnt lgkmcnt(13)\n\t" "v_mfma_f32_16x16x32_bf16 v[64:67], a[128:131], a[16:19], v[64:67]\n\t"
    "ds_read_b64_tr_b16 a[156:157], %[vr3] offset:50176\n\t" "v_mfma_f32_16x16x32_bf16 v[68:71], a[128:131], a[20:23], v[68:71]\n\t" "global_load_dwordx4 a[108:111], %[va], s[76:77] offset:3072 sc1\n\t"
    "s_add_u32 s76, s76, 4096\n\t" "s_addc_u32 s77, s77, 0\n\t" "ds_read_b64_tr_b16 a[158:159], %[vr3] offset:53760\n\t" "v_mfma_f32_16x16x32_bf16 v[72:75], a[128:131], a[24:27], v[72:75]\n\t"
    "v_mfma_f32_16x16x32_bf16 v[76:79], a[128:131], a[28:31], v[76:79]\n\t" "s_waitcnt lgkmcnt(13)\n\t" "v_mfma_f32_16x16x32_bf16 v[80:83], a[132:135], a[16:19], v[80:83]\n\t"
    "ds_read_b64_tr_b16 a[128:129], %[vr3] offset:50208\n\t" "s_add_u32 m0, %[sl], 60416\n\t" "v_mfma_f32_16x16x32_bf16 v[84:87], a[132:135], a[20:23], v[84:87]\n\t" "s_mov_b64 exec, s[82:83]\n\t"
    "global_load_lds_dwordx4 %[vb3], s[78:79]\n\t" "s_mov_b64 exec, -1\n\t" "s_add_u32 s78, s78, 229376\n\t" "s_addc_u32 s79, s79, 0\n\t" "ds_read_b64_tr_b16 a[130:131], %[vr3] offset:53792\n\t"
    "v_mfma_f32_16x16x32_bf16 v[88:91], a[132:135], a[24:27], v[88:91]\n\t" "v_mfma_f32_16x16x32_bf16 v[92:95], a[132:135], a[28:31], v[92:95]\n\t" "s_waitcnt lgkmcnt(13)\n\t"
    "v_mfma_f32_16x16x32_bf16 v[96:99], a[136:139], a[16:19], v[96:99]\n\t" "ds_read_b64_tr_b16 a[132:133], %[vr5] offset:50176\n\t"
    "v_mfma_f32_16x16x32_bf16 v[100:103], a[136:139], a[20:23], v[100:103]\n\t" "global_load_dwordx4 a[112:115], %[va], s[76:77] offset:0 sc1\n\t" "ds_read_b64_tr_b16 a[134:135], %[vr5] offset:53760\n\t"
    "v_mfma_f32_16x16x32_bf16 v[104:107], a[136:139], a[24:27], v[104:107]\n\t" "v_mfma_f32_16x16x32_bf16 v[108:111], a[136:139], a[28:31], v[108:111]\n\t" "s_waitcnt lgkmcnt(13)\n\t"
    "v_mfma_f32_16x16x32_bf16 v[112:115], a[140:143], a[16:19], v[112:115]\n\t" "ds_read_b64_tr_b16 a[136:137], %[vr5] offset:50208\n\t" "s_add_u32 m0, %[sl], 71680\n\t"
    "v_mfma_f32_16x16x32_bf16 v[116:119], a[140:143], a[20:23], v[116:119]\n\t" "global_load_lds_dwordx4 %[vb0], s[78:79]\n\t" "ds_read_b64_tr_b16 a[138:139], %[vr5] offset:53792\n\t"
    "v_mfma_f32_16x16x32_bf16 v[120:123], a[140:143], a[24:27], v[120:123]\n\t" "v_mfma_f32_16x16x32_bf16 v[124:127], a[140:143], a[28:31], v[124:127]\n\t" "s_waitcnt lgkmcnt(13)\n\t"
    "v_mfma_f32_16x16x32_bf16 v[128:131], a[144:147], a[16:19], v[128:131]\n\t" "ds_read_b64_tr_b16 a[140:141], %[vr7] offset:50176\n\t"
    "v_mfma_f32_16x16x32_bf16 v[132:135], a[144:147], a[20:23], v[132:135]\n\t" "global_load_dwordx4 a[116:119], %[va], s[76:77] offset:1024 sc1\n\t"
    "ds_read_b64_tr_b16 a[142:143], %[vr7] offset:53760\n\t" "v_mfma_f32_16x16x32_bf16 v[136:139], a[144:147], a[24:27], v[136:139]\n\t"
    "v_mfma_f32_16x16x32_bf16 v[140:143], a[144:147], a[28:31], v[140:143]\n\t" "s_waitcnt lgkmcnt(13)\n\t" "v_mfma_f32_16x16x32_bf16 v[144:147], a[148:151], a[16:19], v[144:147]\n\t"
    "s_waitcnt lgkmcnt(11)\n\t" "s_waitcnt vmcnt(11)\n\t" "s_barrier\n\t" "v_accvgpr_read_b32 v31, %[ct]\n\t" "s_mov_b64 exec, 0xffff\n\t" "v_add_u32 v31, %[gt0], v31\n\t" "s_mov_b64 exec, -1\n\t"
    "ds_read_b128 a[144:147], v31 offset:0\n\t" "s_add_u32 m0, %[sl], 72704\n\t" "v_mfma_f32_16x16x32_bf16 v[148:151], a[148:151], a[20:23], v[148:151]\n\t" "global_load_lds_dwordx4 %[vb1], s[78:79]\n\t"
    "v_mfma_f32_16x16x32_bf16 v[152:155], a[148:151], a[24:27], v[152:155]\n\t" "v_mfma_f32_16x16x32_bf16 v[156:159], a[148:151], a[28:31], v[156:159]\n\t" "s_waitcnt lgkmcnt(11)\n\t"
    "v_mfma_f32_16x16x32_bf16 v[160:163], a[152:155], a[16:19], v[160:163]\n\t" "ds_read_b128 a[148:151], v31 offset:256\n\t" "v_mfma_f32_16x16x32_bf16 v[164:167], a[152:155], a[20:23], v[164:167]\n\t"
    "global_load_dwordx4 a[120:123], %[va], s[76:77] offset:2048 sc1\n\t" "v_mfma_f32_16x16x32_bf16 v[168:171], a[152:155], a[24:27], v[168:171]\n\t"
    "v_mfma_f32_16x16x32_bf16 v[172:175], a[152:155], a[28:31], v[172:175]\n\t" "s_waitcnt lgkmcnt(10)\n\t" "v_mfma_f32_16x16x32_bf16 v[176:179], a[156:159], a[16:19], v[176:179]\n\t"
    "ds_read_b64_tr_b16 a[152:153], %[vr0] offset:0\n\t" "s_add_u32 m0, %[sl], 73728\n\t" "v_mfma_f32_16x16x32_bf16 v[180:183], a[156:159], a[20:23], v[180:183]\n\t"
    "global_load_lds_dwordx4 %[vb2], s[78:79]\n\t" "ds_read_b64_tr_b16 a[154:155], %[vr0] offset:3584\n\t" "v_mfma_f32_16x16x32_bf16 v[184:187], a[156:159], a[24:27], v[184:187]\n\t"
    "v_mfma_f32_16x16x32_bf16 v[188:191], a[156:159], a[28:31], v[188:191]\n\t" "s_waitcnt lgkmcnt(10)\n\t" "v_mfma_f32_16x16x32_bf16 v[192:195], a[128:131], a[16:19], v[192:195]\n\t"
    "ds_read_b64_tr_b16 a[156:157], %[vr0] offset:7168\n\t" "v_mfma_f32_16x16x32_bf16 v[196:199], a[128:131], a[20:23], v[196:199]\n\t"
    "global_load_dwordx4 a[124:127], %[va], s[76:77] offset:3072 sc1\n\t" "s_add_u32 s76, s76, s86\n\t" "s_addc_u32 s77, s77, s87\n\t" "ds_read_b64_tr_b16 a[158:159], %[vr0] offset:10752\n\t"
    "v_mfma_f32_16x16x32_bf16 v[200:203], a[128:131], a[24:27], v[200:203]\n\t" "v_mfma_f32_16x16x32_bf16 v[204:207], a[128:131], a[28:31], v[204:207]\n\t" "s_waitcnt lgkmcnt(10)\n\t"
    "v_mfma_f32_16x16x32_bf16 v[208:211], a[132:135], a[16:19], v[208:211]\n\t" "ds_read_b64_tr_b16 a[128:129], %[vr0] offset:14336\n\t" "s_add_u32 m0, %[sl], 74752\n\t"
    "v_mfma_f32_16x16x32_bf16 v[212:215], a[132:135], a[20:23], v[212:215]\n\t" "s_mov_b64 exec, s[82:83]\n\t" "global_load_lds_dwordx4 %[vb3], s[78:79]\n\t" "s_mov_b64 exec, -1\n\t"
    "s_add_u32 s78, s78, s88\n\t" "s_addc_u32 s79, s79, s89\n\t" "ds_read_b64_tr_b16 a[130:131], %[vr0] offset:17920\n\t" "v_mfma_f32_16x16x32_bf16 v[216:219], a[132:135], a[24:27], v[216:219]\n\t"
    "v_mfma_f32_16x16x32_bf16 v[220:223], a[132:135], a[28:31], v[220:223]\n\t" "s_waitcnt lgkmcnt(10)\n\t" "v_mfma_f32_16x16x32_bf16 v[224:227], a[136:139], a[16:19], v[224:227]\n\t"
    "ds_read_b64_tr_b16 a[132:133], %[vr0] offset:21504\n\t" "v_mfma_f32_16x16x32_bf16 v[228:231], a[136:139], a[20:23], v[228:231]\n\t" "ds_read_b64_tr_b16 a[134:135], %[vr0] offset:25088\n\t"
    "v_mfma_f32_16x16x32_bf16 v[232:235], a[136:139], a[24:27], v[232:235]\n\t" "v_mfma_f32_16x16x32_bf16 v[236:239], a[136:139], a[28:31], v[236:239]\n\t" "s_waitcnt lgkmcnt(10)\n\t"
    "v_mfma_f32_16x16x32_bf16 v[240:243], a[140:143], a[16:19], v[240:243]\n\t" "ds_read_b64_tr_b16 a[136:137], %[vr0] offset:28672\n\t"
    "v_mfma_f32_16x16x32_bf16 v[244:247], a[140:143], a[20:23], v[244:247]\n\t" "ds_read_b64_tr_b16 a[138:139], %[vr0] offset:32256\n\t"
    "v_mfma_f32_16x16x32_bf16 v[248:251], a[140:143], a[24:27], v[248:251]\n\t" "v_mfma_f32_16x16x32_bf16 v[252:255], a[140:143], a[28:31], v[252:255]\n\t" "s_waitcnt lgkmcnt(11)\n\t"
    "v_mfma_f32_16x16x32_bf16 v[32:35], a[144:147], a[200:203], v[32:35]\n\t" "ds_read_b64_tr_b16 a[140:141], %[vr0] offset:35840\n\t"
    "v_mfma_f32_16x16x32_bf16 v[36:39], a[144:147], a[204:207], v[36:39]\n\t" "ds_read_b64_tr_b16 a[142:143], %[vr0] offset:39424\n\t"
    "v_mfma_f32_16x16x32_bf16 v[40:43], a[144:147], a[208:211], v[40:43]\n\t" "v_mfma_f32_16x16x32_bf16 v[44:47], a[144:147], a[212:215], v[44:47]\n\t" "s_waitcnt lgkmcnt(12)\n\t"
    "v_mfma_f32_16x16x32_bf16 v[144:147], a[148:151], a[200:203], v[144:147]\n\t" "ds_read_b64_tr_b16 a[144:145], %[vr0] offset:43008\n\t"
    "v_mfma_f32_16x16x32_bf16 v[148:151], a[148:151], a[204:207], v[148:151]\n\t" "ds_read_b64_tr_b16 a[146:147], %[vr0] offset:46592\n\t"
    "v_mfma_f32_16x16x32_bf16 v[152:155], a[148:151], a[208:211], v[152:155]\n\t" "v_mfma_f32_16x16x32_bf16 v[156:159], a[148:151], a[212:215], v[156:159]\n\t" "s_waitcnt lgkmcnt(12)\n\t"
    "v_mfma_f32_16x16x32_bf16 v[32:35], a[152:155], a[32:35], v[32:35]\n\t" "ds_read_b64_tr_b16 a[148:149], %[vr0] offset:50176\n\t" "v_mfma_f32_16x16x32_bf16 v[36:39], a[152:155], a[36:39], v[36:39]\n\t"
    "ds_read_b64_tr_b16 a[150:151], %[vr0] offset:53760\n\t" "v_mfma_f32_16x16x32_bf16 v[40:43], a[152:155], a[40:43], v[40:43]\n\t" "v_mfma_f32_16x16x32_bf16 v[44:47], a[152:155], a[44:47], v[44:47]\n\t"
    "s_waitcnt lgkmcnt(12)\n\t" "v_mfma_f32_16x16x32_bf16 v[144:147], a[156:159], a[32:35], v[144:147]\n\t" "s_waitcnt vmcnt(0)\n\t" "s_barrier\n\t" "ds_read_b64_tr_b16 a[152:153], %[vr1] offset:0\n\t"
    "v_accvgpr_read_b32 v30, %[vbd]\n\t" "s_mov_b64 s[78:79], %[sbd]\n\t" "v_mfma_f32_16x16x32_bf16 v[148:151], a[156:159], a[36:39], v[148:151]\n\t"
    "ds_read_b64_tr_b16 a[154:155], %[vr1] offset:3584\n\t" "s_add_u32 m0, %[sld], 24576\n\t" "s_nop 0\n\t" "global_load_lds_dwordx4 v30, s[78:79] offset:0\n\t"
    "v_mfma_f32_16x16x32_bf16 v[152:155], a[156:159], a[40:43], v[152:155]\n\t" "v_mfma_f32_16x16x32_bf16 v[156:159], a[156:159], a[44:47], v[156:159]\n\t" "s_waitcnt lgkmcnt(12)\n\t"
    "v_mfma_f32_16x16x32_bf16 v[32:35], a[128:131], a[48:51], v[32:35]\n\t" "ds_read_b64_tr_b16 a[156:157], %[vr1] offset:7168\n\t" "s_add_u32 m0, %[sld], 28544\n\t" "s_nop 0\n\t"
    "global_load_lds_dwordx4 v30, s[78:79] offset:128\n\t" "s_add_u32 s78, s78, 65536\n\t" "s_addc_u32 s79, s79, 0\n\t" "v_mfma_f32_16x16x32_bf16 v[36:39], a[128:131], a[52:55], v[36:39]\n\t"
    "ds_read_b64_tr_b16 a[158:159], %[vr1] offset:10752\n\t" "v_mfma_f32_16x16x32_bf16 v[40:43], a[128:131], a[56:59], v[40:43]\n\t" "v_mfma_f32_16x16x32_bf16 v[44:47], a[128:131], a[60:63], v[44:47]\n\t"
    "s_add_u32 m0, %[sld], 32768\n\t" "s_nop 0\n\t" "global_load_lds_dwordx4 v30, s[78:79] offset:0\n\t" "s_waitcnt lgkmcnt(12)\n\t"
    "v_mfma_f32_16x16x32_bf16 v[144:147], a[132:135], a[48:51], v[144:147]\n\t" "ds_read_b64_tr_b16 a[128:129], %[vr1] offset:14336\n\t"
    "v_mfma_f32_16x16x32_bf16 v[148:151], a[132:135], a[52:55], v[148:151]\n\t" "ds_read_b64_tr_b16 a[130:131], %[vr1] offset:17920\n\t"
    "v_mfma_f32_16x16x32_bf16 v[152:155], a[132:135], a[56:59], v[152:155]\n\t" "s_add_u32 m0, %[sld], 36736\n\t" "s_nop 0\n\t" "global_load_lds_dwordx4 v30, s[78:79] offset:128\n\t"
    "s_add_u32 s78, s78, 65536\n\t" "s_addc_u32 s79, s79, 0\n\t" "v_mfma_f32_16x16x32_bf16 v[156:159], a[132:135], a[60:63], v[156:159]\n\t" "s_waitcnt lgkmcnt(12)\n\t"
    "v_mfma_f32_16x16x32_bf16 v[32:35], a[136:139], a[64:67], v[32:35]\n\t" "ds_read_b64_tr_b16 a[132:133], %[vr1] offset:21504\n\t" "v_mfma_f32_16x16x32_bf16 v[36:39], a[136:139], a[68:71], v[36:39]\n\t"
    "ds_read_b64_tr_b16 a[134:135], %[vr1] offset:25088\n\t" "s_add_u32 m0, %[sld], 40960\n\t" "s_nop 0\n\t" "global_load_lds_dwordx4 v30, s[78:79] offset:0\n\t"
    "v_mfma_f32_16x16x32_bf16 v[40:43], a[136:139], a[72:75], v[40:43]\n\t" "v_mfma_f32_16x16x32_bf16 v[44:47], a[136:139], a[76:79], v[44:47]\n\t" "s_waitcnt lgkmcnt(12)\n\t"
    "v_mfma_f32_16x16x32_bf16 v[144:147], a[140:143], a[64:67], v[144:147]\n\t" "ds_read_b128 a[136:139], v31 offset:512\n\t" "s_add_u32 m0, %[sld], 44928\n\t" "s_nop 0\n\t"
    "global_load_lds_dwordx4 v30, s[78:79] offset:128\n\t" "s_add_u32 s78, s78, 65536\n\t" "s_addc_u32 s79, s79, 0\n\t" "v_mfma_f32_16x16x32_bf16 v[148:151], a[140:143], a[68:71], v[148:151]\n\t"
    "v_mfma_f32_16x16x32_bf16 v[152:155], a[140:143], a[72:75], v[152:155]\n\t" "v_mfma_f32_16x16x32_bf16 v[156:159], a[140:143], a[76:79], v[156:159]\n\t" "s_add_u32 m0, %[sld], 139264\n\t" "s_nop 0\n\t"
    "global_load_lds_dwordx4 v30, s[78:79] offset:0\n\t" "s_waitcnt lgkmcnt(11)\n\t" "v_mfma_f32_16x16x32_bf16 v[32:35], a[144:147], a[80:83], v[32:35]\n\t" "ds_read_b128 a[140:143], v31 offset:768\n\t"
    "v_mfma_f32_16x16x32_bf16 v[36:39], a[144:147], a[84:87], v[36:39]\n\t" "v_mfma_f32_16x16x32_bf16 v[40:43], a[144:147], a[88:91], v[40:43]\n\t" "s_add_u32 m0, %[sld], 143232\n\t" "s_nop 0\n\t"
    "global_load_lds_dwordx4 v30, s[78:79] offset:128\n\t" "s_add_u32 s78, s78, 65536\n\t" "s_addc_u32 s79, s79, 0\n\t" "v_mfma_f32_16x16x32_bf16 v[44:47], a[144:147], a[92:95], v[44:47]\n\t"
    "s_waitcnt lgkmcnt(10)\n\t" "v_mfma_f32_16x16x32_bf16 v[144:147], a[148:151], a[80:83], v[144:147]\n\t" "ds_read_b64_tr_b16 a[144:145], %[vr0] offset:32\n\t"
    "v_mfma_f32_16x16x32_bf16 v[148:151], a[148:151], a[84:87], v[148:151]\n\t" "ds_read_b64_tr_b16 a[146:147], %[vr0] offset:3616\n\t" "s_add_u32 m0, %[sld], 147456\n\t" "s_nop 0\n\t"
    "global_load_lds_dwordx4 v30, s[78:79] offset:0\n\t" "v_mfma_f32_16x16x32_bf16 v[152:155], a[148:151], a[88:91], v[152:155]\n\t"
    "v_mfma_f32_16x16x32_bf16 v[156:159], a[148:151], a[92:95], v[156:159]\n\t" "s_waitcnt lgkmcnt(10)\n\t" "v_mfma_f32_16x16x32_bf16 v[32:35], a[152:155], a[96:99], v[32:35]\n\t"
    "ds_read_b64_tr_b16 a[148:149], %[vr0] offset:7200\n\t" "s_add_u32 m0, %[sld], 151424\n\t" "s_nop 0\n\t" "global_load_lds_dwordx4 v30, s[78:79] offset:128\n\t" "s_add_u32 s78, s78, 65536\n\t"
    "s_addc_u32 s79, s79, 0\n\t" "v_mfma_f32_16x16x32_bf16 v[36:39], a[152:155], a[100:103], v[36:39]\n\t" "ds_read_b64_tr_b16 a[150:151], %[vr0] offset:10784\n\t"
    "v_mfma_f32_16x16x32_bf16 v[40:43], a[152:155], a[104:107], v[40:43]\n\t" "v_mfma_f32_16x16x32_bf16 v[44:47], a[152:155], a[108:111], v[44:47]\n\t" "s_add_u32 m0, %[sld], 155648\n\t" "s_nop 0\n\t"
    "global_load_lds_dwordx4 v30, s[78:79] offset:0\n\t" "s_waitcnt lgkmcnt(10)\n\t" "v_mfma_f32_16x16x32_bf16 v[144:147], a[156:159], a[96:99], v[144:147]\n\t"
    "ds_read_b64_tr_b16 a[152:153], %[vr0] offset:14368\n\t" "v_mfma_f32_16x16x32_bf16 v[148:151], a[156:159], a[100:103], v[148:151]\n\t" "ds_read_b64_tr_b16 a[154:155], %[vr0] offset:17952\n\t"
    "v_mfma_f32_16x16x32_bf16 v[152:155], a[156:159], a[104:107], v[152:155]\n\t" "s_add_u32 m0, %[sld], 159616\n\t" "s_nop 0\n\t" "global_load_lds_dwordx4 v30, s[78:79] offset:128\n\t"
    "s_add_u32 s78, s78, 65536\n\t" "s_addc_u32 s79, s79, 0\n\t" "v_mfma_f32_16x16x32_bf16 v[156:159], a[156:159], a[108:111], v[156:159]\n\t" "s_waitcnt lgkmcnt(10)\n\t"
    "v_mfma_f32_16x16x32_bf16 v[32:35], a[128:131], a[112:115], v[32:35]\n\t" "ds_read_b64_tr_b16 a[156:157], %[vr0] offset:21536\n\t"
    "v_mfma_f32_16x16x32_bf16 v[36:39], a[128:131], a[116:119], v[36:39]\n\t" "ds_read_b64_tr_b16 a[158:159], %[vr0] offset:25120\n\t" "s_add_u32 m0, %[sld], 0\n\t" "s_nop 0\n\t"
    "global_load_lds_dwordx4 v30, s[78:79] offset:0\n\t" "v_mfma_f32_16x16x32_bf16 v[40:43], a[128:131], a[120:123], v[40:43]\n\t" "v_mfma_f32_16x16x32_bf16 v[44:47], a[128:131], a[124:127], v[44:47]\n\t"
    "s_waitcnt lgkmcnt(10)\n\t" "v_mfma_f32_16x16x32_bf16 v[144:147], a[132:135], a[112:115], v[144:147]\n\t" "ds_read_b64_tr_b16 a[128:129], %[vr0] offset:28704\n\t" "s_add_u32 m0, %[sld], 3968\n\t"
    "s_nop 0\n\t" "global_load_lds_dwordx4 v30, s[78:79] offset:128\n\t" "s_add_u32 s78, s78, 65536\n\t" "s_addc_u32 s79, s79, 0\n\t"
    "v_mfma_f32_16x16x32_bf16 v[148:151], a[132:135], a[116:119], v[148:151]\n\t" "ds_read_b64_tr_b16 a[130:131], %[vr0] offset:32288\n\t"
    "v_mfma_f32_16x16x32_bf16 v[152:155], a[132:135], a[120:123], v[152:155]\n\t" "v_mfma_f32_16x16x32_bf16 v[156:159], a[132:135], a[124:127], v[156:159]\n\t" "s_add_u32 m0, %[sld], 8192\n\t"
    "s_nop 0\n\t" "global_load_lds_dwordx4 v30, s[78:79] offset:0\n\t" "s_waitcnt lgkmcnt(11)\n\t" "v_mfma_f32_16x16x32_bf16 v[48:51], a[136:139], a[200:203], v[48:51]\n\t"
    "ds_read_b64_tr_b16 a[132:133], %[vr0] offset:35872\n\t" "v_mfma_f32_16x16x32_bf16 v[52:55], a[136:139], a[204:207], v[52:55]\n\t" "ds_read_b64_tr_b16 a[134:135], %[vr0] offset:39456\n\t"
    "v_mfma_f32_16x16x32_bf16 v[56:59], a[136:139], a[208:211], v[56:59]\n\t" "s_add_u32 m0, %[sld], 12160\n\t" "s_nop 0\n\t" "global_load_lds_dwordx4 v30, s[78:79] offset:128\n\t"
    "s_add_u32 s78, s78, 65536\n\t" "s_addc_u32 s79, s79, 0\n\t" "v_mfma_f32_16x16x32_bf16 v[60:63], a[136:139], a[212:215], v[60:63]\n\t" "s_waitcnt lgkmcnt(12)\n\t"
    "v_mfma_f32_16x16x32_bf16 v[160:163], a[140:143], a[200:203], v[160:163]\n\t" "ds_read_b64_tr_b16 a[136:137], %[vr0] offset:43040\n\t"
    "v_mfma_f32_16x16x32_bf16 v[164:167], a[140:143], a[204:207], v[164:167]\n\t" "ds_read_b64_tr_b16 a[138:139], %[vr0] offset:46624\n\t"
    "v_mfma_f32_16x16x32_bf16 v[168:171], a[140:143], a[208:211], v[168:171]\n\t" "v_mfma_f32_16x16x32_bf16 v[172:175], a[140:143], a[212:215], v[172:175]\n\t" "s_waitcnt lgkmcnt(12)\n\t"
    "v_mfma_f32_16x16x32_bf16 v[48:51], a[144:147], a[32:35], v[48:51]\n\t" "ds_read_b64_tr_b16 a[140:141], %[vr0] offset:50208\n\t" "v_mfma_f32_16x16x32_bf16 v[52:55], a[144:147], a[36:39], v[52:55]\n\t"
    "ds_read_b64_tr_b16 a[142:143], %[vr0] offset:53792\n\t" "v_mul_f32 v32, v32, v16\n\t" "v_mul_f32 v33, v33, v16\n\t" "v_mfma_f32_16x16x32_bf16 v[56:59], a[144:147], a[40:43], v[56:59]\n\t"
    "v_mul_f32 v34, v34, v16\n\t" "v_mul_f32 v35, v35, v16\n\t" "v_mfma_f32_16x16x32_bf16 v[60:63], a[144:147], a[44:47], v[60:63]\n\t" "v_exp_f32 v32, v32\n\t" "s_waitcnt lgkmcnt(12)\n\t"
    "v_mfma_f32_16x16x32_bf16 v[160:163], a[148:151], a[32:35], v[160:163]\n\t" "ds_read_b64_tr_b16 a[144:145], %[vr1] offset:32\n\t" "v_exp_f32 v33, v33\n\t" "v_exp_f32 v34, v34\n\t"
    "v_mfma_f32_16x16x32_bf16 v[164:167], a[148:151], a[36:39], v[164:167]\n\t" "ds_read_b64_tr_b16 a[146:147], %[vr1] offset:3616\n\t" "v_exp_f32 v35, v35\n\t"
    "v_mfma_f32_16x16x32_bf16 v[168:171], a[148:151], a[40:43], v[168:171]\n\t" "v_fma_f32 v32, v32, v20, v20\n\t" "v_fma_f32 v33, v33, v20, v20\n\t"
    "v_mfma_f32_16x16x32_bf16 v[172:175], a[148:151], a[44:47], v[172:175]\n\t" "v_fma_f32 v34, v34, v20, v20\n\t" "v_fma_f32 v35, v35, v20, v20\n\t" "s_waitcnt lgkmcnt(12)\n\t"
    "v_mfma_f32_16x16x32_bf16 v[48:51], a[152:155], a[48:51], v[48:51]\n\t" "ds_read_b64_tr_b16 a[148:149], %[vr1] offset:7200\n\t" "v_rcp_f32 v32, v32\n\t" "v_rcp_f32 v33, v33\n\t"
    "v_mfma_f32_16x16x32_bf16 v[52:55], a[152:155], a[52:55], v[52:55]\n\t" "ds_read_b64_tr_b16 a[150:151], %[vr1] offset:10784\n\t" "v_rcp_f32 v34, v34\n\t"
    "v_mfma_f32_16x16x32_bf16 v[56:59], a[152:155], a[56:59], v[56:59]\n\t" "v_rcp_f32 v35, v35\n\t" "v_mfma_f32_16x16x32_bf16 v[60:63], a[152:155], a[60:63], v[60:63]\n\t" "v_mul_f32 v144, v144, v32\n\t"
    "v_mul_f32 v145, v145, v33\n\t" "s_waitcnt lgkmcnt(12)\n\t" "v_mfma_f32_16x16x32_bf16 v[160:163], a[156:159], a[48:51], v[160:163]\n\t" "ds_read_b64_tr_b16 a[152:153], %[vr1] offset:14368\n\t"
    "v_mul_f32 v146, v146, v34\n\t" "v_mul_f32 v147, v147, v35\n\t" "v_cvt_pk_bf16_f32 v32, v144, v145\n\t" "v_mfma_f32_16x16x32_bf16 v[164:167], a[156:159], a[52:55], v[164:167]\n\t"
    "ds_read_b64_tr_b16 a[154:155], %[vr1] offset:17952\n\t" "v_cvt_pk_bf16_f32 v33, v146, v147\n\t" "v_mul_f32 v36, v36, v17\n\t"
    "v_mfma_f32_16x16x32_bf16 v[168:171], a[156:159], a[56:59], v[168:171]\n\t" "v_mul_f32 v37, v37, v17\n\t" "v_mul_f32 v38, v38, v17\n\t"
    "v_mfma_f32_16x16x32_bf16 v[172:175], a[156:159], a[60:63], v[172:175]\n\t" "v_mul_f32 v39, v39, v17\n\t" "v_exp_f32 v36, v36\n\t" "s_waitcnt lgkmcnt(12)\n\t"
    "v_mfma_f32_16x16x32_bf16 v[48:51], a[128:131], a[64:67], v[48:51]\n\t" "ds_read_b64_tr_b16 a[156:157], %[vr1] offset:21536\n\t" "v_exp_f32 v37, v37\n\t" "v_exp_f32 v38, v38\n\t"
    "v_mfma_f32_16x16x32_bf16 v[52:55], a[128:131], a[68:71], v[52:55]\n\t" "ds_read_b64_tr_b16 a[158:159], %[vr1] offset:25120\n\t" "v_exp_f32 v39, v39\n\t"
    "v_mfma_f32_16x16x32_bf16 v[56:59], a[128:131], a[72:75], v[56:59]\n\t" "v_fma_f32 v36, v36, v21, v21\n\t" "v_fma_f32 v37, v37, v21, v21\n\t"
    "v_mfma_f32_16x16x32_bf16 v[60:63], a[128:131], a[76:79], v[60:63]\n\t" "v_fma_f32 v38, v38, v21, v21\n\t" "v_fma_f32 v39, v39, v21, v21\n\t" "s_waitcnt lgkmcnt(12)\n\t"
    "v_mfma_f32_16x16x32_bf16 v[160:163], a[132:135], a[64:67], v[160:163]\n\t" "v_accvgpr_read_b32 v31, %[ct]\n\t" "s_mov_b64 exec, 0xffff\n\t" "v_add_u32 v31, %[gt1], v31\n\t" "s_mov_b64 exec, -1\n\t"
    "ds_read_b128 a[128:131], v31 offset:0\n\t" "v_rcp_f32 v36, v36\n\t" "v_rcp_f32 v37, v37\n\t" "v_mfma_f32_16x16x32_bf16 v[164:167], a[132:135], a[68:71], v[164:167]\n\t" "v_rcp_f32 v38, v38\n\t"
    "v_mfma_f32_16x16x32_bf16 v[168:171], a[132:135], a[72:75], v[168:171]\n\t" "v_rcp_f32 v39, v39\n\t" "v_mfma_f32_16x16x32_bf16 v[172:175], a[132:135], a[76:79], v[172:175]\n\t"
    "v_mul_f32 v148, v148, v36\n\t" "v_mul_f32 v149, v149, v37\n\t" "s_waitcnt lgkmcnt(11)\n\t" "v_mfma_f32_16x16x32_bf16 v[48:51], a[136:139], a[80:83], v[48:51]\n\t"
    "ds_read_b128 a[132:135], v31 offset:256\n\t" "v_mul_f32 v150, v150, v38\n\t" "v_mul_f32 v151, v151, v39\n\t" "v_cvt_pk_bf16_f32 v36, v148, v149\n\t"
    "v_mfma_f32_16x16x32_bf16 v[52:55], a[136:139], a[84:87], v[52:55]\n\t" "v_cvt_pk_bf16_f32 v37, v150, v151\n\t" "v_mul_f32 v40, v40, v18\n\t"
    "v_mfma_f32_16x16x32_bf16 v[56:59], a[136:139], a[88:91], v[56:59]\n\t" "v_mul_f32 v41, v41, v18\n\t" "v_mul_f32 v42, v42, v18\n\t"
    "v_mfma_f32_16x16x32_bf16 v[60:63], a[136:139], a[92:95], v[60:63]\n\t" "v_mul_f32 v43, v43, v18\n\t" "v_exp_f32 v40, v40\n\t" "s_waitcnt lgkmcnt(10)\n\t"
    "v_mfma_f32_16x16x32_bf16 v[160:163], a[140:143], a[80:83], v[160:163]\n\t" "ds_read_b64_tr_b16 a[136:137], %[vr2] offset:0\n\t" "v_exp_f32 v41, v41\n\t" "v_exp_f32 v42, v42\n\t"
    "v_mfma_f32_16x16x32_bf16 v[164:167], a[140:143], a[84:87], v[164:167]\n\t" "ds_read_b64_tr_b16 a[138:139], %[vr2] offset:3584\n\t" "v_exp_f32 v43, v43\n\t"
    "v_mfma_f32_16x16x32_bf16 v[168:171], a[140:143], a[88:91], v[168:171]\n\t" "v_fma_f32 v40, v40, v22, v22\n\t" "v_fma_f32 v41, v41, v22, v22\n\t"
    "v_mfma_f32_16x16x32_bf16 v[172:175], a[140:143], a[92:95], v[172:175]\n\t" "v_fma_f32 v42, v42, v22, v22\n\t" "v_fma_f32 v43, v43, v22, v22\n\t" "s_waitcnt lgkmcnt(10)\n\t"
    "v_mfma_f32_16x16x32_bf16 v[48:51], a[144:147], a[96:99], v[48:51]\n\t" "ds_read_b64_tr_b16 a[140:141], %[vr2] offset:7168\n\t" "v_rcp_f32 v40, v40\n\t" "v_rcp_f32 v41, v41\n\t"
    "v_mfma_f32_16x16x32_bf16 v[52:55], a[144:147], a[100:103], v[52:55]\n\t" "ds_read_b64_tr_b16 a[142:143], %[vr2] offset:10752\n\t" "v_rcp_f32 v42, v42\n\t"
    "v_mfma_f32_16x16x32_bf16 v[56:59], a[144:147], a[104:107], v[56:59]\n\t" "v_rcp_f32 v43, v43\n\t" "v_mfma_f32_16x16x32_bf16 v[60:63], a[144:147], a[108:111], v[60:63]\n\t"
    "v_mul_f32 v152, v152, v40\n\t" "v_mul_f32 v153, v153, v41\n\t" "s_waitcnt lgkmcnt(10)\n\t" "v_mfma_f32_16x16x32_bf16 v[160:163], a[148:151], a[96:99], v[160:163]\n\t"
    "ds_read_b64_tr_b16 a[144:145], %[vr2] offset:14336\n\t" "v_mul_f32 v154, v154, v42\n\t" "v_mul_f32 v155, v155, v43\n\t" "v_cvt_pk_bf16_f32 v40, v152, v153\n\t"
    "v_mfma_f32_16x16x32_bf16 v[164:167], a[148:151], a[100:103], v[164:167]\n\t" "ds_read_b64_tr_b16 a[146:147], %[vr2] offset:17920\n\t" "v_cvt_pk_bf16_f32 v41, v154, v155\n\t"
    "v_mul_f32 v44, v44, v19\n\t" "v_mfma_f32_16x16x32_bf16 v[168:171], a[148:151], a[104:107], v[168:171]\n\t" "v_mul_f32 v45, v45, v19\n\t" "v_mul_f32 v46, v46, v19\n\t"
    "v_mfma_f32_16x16x32_bf16 v[172:175], a[148:151], a[108:111], v[172:175]\n\t" "v_mul_f32 v47, v47, v19\n\t" "v_exp_f32 v44, v44\n\t" "s_waitcnt lgkmcnt(10)\n\t"
    "v_mfma_f32_16x16x32_bf16 v[48:51], a[152:155], a[112:115], v[48:51]\n\t" "ds_read_b64_tr_b16 a[148:149], %[vr2] offset:21504\n\t" "v_exp_f32 v45, v45\n\t" "v_exp_f32 v46, v46\n\t"
    "v_mfma_f32_16x16x32_bf16 v[52:55], a[152:155], a[116:119], v[52:55]\n\t" "ds_read_b64_tr_b16 a[150:151], %[vr2] offset:25088\n\t" "v_exp_f32 v47, v47\n\t"
    "v_mfma_f32_16x16x32_bf16 v[56:59], a[152:155], a[120:123], v[56:59]\n\t" "v_fma_f32 v44, v44, v23, v23\n\t" "v_fma_f32 v45, v45, v23, v23\n\t"
    "v_mfma_f32_16x16x32_bf16 v[60:63], a[152:155], a[124:127], v[60:63]\n\t" "v_fma_f32 v46, v46, v23, v23\n\t" "v_fma_f32 v47, v47, v23, v23\n\t" "s_waitcnt lgkmcnt(10)\n\t"
    "v_mfma_f32_16x16x32_bf16 v[160:163], a[156:159], a[112:115], v[160:163]\n\t" "ds_read_b64_tr_b16 a[152:153], %[vr2] offset:28672\n\t" "v_rcp_f32 v44, v44\n\t" "v_rcp_f32 v45, v45\n\t"
    "v_mfma_f32_16x16x32_bf16 v[164:167], a[156:159], a[116:119], v[164:167]\n\t" "ds_read_b64_tr_b16 a[154:155], %[vr2] offset:32256\n\t" "v_rcp_f32 v46, v46\n\t"
    "v_mfma_f32_16x16x32_bf16 v[168:171], a[156:159], a[120:123], v[168:171]\n\t" "v_rcp_f32 v47, v47\n\t" "v_mfma_f32_16x16x32_bf16 v[172:175], a[156:159], a[124:127], v[172:175]\n\t"
    "v_mul_f32 v156, v156, v44\n\t" "v_mul_f32 v157, v157, v45\n\t" "s_waitcnt lgkmcnt(11)\n\t" "v_mfma_f32_16x16x32_bf16 v[64:67], a[128:131], a[200:203], v[64:67]\n\t"
    "ds_read_b64_tr_b16 a[156:157], %[vr2] offset:35840\n\t" "v_mul_f32 v158, v158, v46\n\t" "v_mul_f32 v159, v159, v47\n\t" "v_cvt_pk_bf16_f32 v44, v156, v157\n\t"
    "v_mfma_f32_16x16x32_bf16 v[68:71], a[128:131], a[204:207], v[68:71]\n\t" "ds_read_b64_tr_b16 a[158:159], %[vr2] offset:39424\n\t" "v_cvt_pk_bf16_f32 v45, v158, v159\n\t"
    "v_mfma_f32_16x16x32_bf16 v[72:75], a[128:131], a[208:211], v[72:75]\n\t" "v_mfma_f32_16x16x32_bf16 v[76:79], a[128:131], a[212:215], v[76:79]\n\t" "s_waitcnt lgkmcnt(12)\n\t"
    "v_mfma_f32_16x16x32_bf16 v[176:179], a[132:135], a[200:203], v[176:179]\n\t" "ds_read_b64_tr_b16 a[128:129], %[vr2] offset:43008\n\t"
    "v_mfma_f32_16x16x32_bf16 v[180:183], a[132:135], a[204:207], v[180:183]\n\t" "ds_read_b64_tr_b16 a[130:131], %[vr2] offset:46592\n\t" "s_add_u32 s90, %[shl], %[go0]\n\t"
    "s_addc_u32 s91, %[shh], 0\n\t" "s_add_u32 s92, s90, 229376\n\t" "s_addc_u32 s93, s91, 0\n\t" "v_mfma_f32_16x16x32_bf16 v[184:187], a[132:135], a[208:211], v[184:187]\n\t"
    "v_mul_f32 v48, v48, v16\n\t" "v_mul_f32 v49, v49, v16\n\t" "v_mfma_f32_16x16x32_bf16 v[188:191], a[132:135], a[212:215], v[188:191]\n\t" "v_mul_f32 v50, v50, v16\n\t" "v_mul_f32 v51, v51, v16\n\t"
    "s_waitcnt lgkmcnt(12)\n\t" "v_mfma_f32_16x16x32_bf16 v[64:67], a[136:139], a[32:35], v[64:67]\n\t" "ds_read_b64_tr_b16 a[132:133], %[vr2] offset:50176\n\t" "v_exp_f32 v48, v48\n\t"
    "v_exp_f32 v49, v49\n\t" "v_mfma_f32_16x16x32_bf16 v[68:71], a[136:139], a[36:39], v[68:71]\n\t" "ds_read_b64_tr_b16 a[134:135], %[vr2] offset:53760\n\t" "v_exp_f32 v50, v50\n\t"
    "v_mfma_f32_16x16x32_bf16 v[72:75], a[136:139], a[40:43], v[72:75]\n\t" "v_exp_f32 v51, v51\n\t" "v_mfma_f32_16x16x32_bf16 v[76:79], a[136:139], a[44:47], v[76:79]\n\t"
    "v_fma_f32 v48, v48, v20, v20\n\t" "v_fma_f32 v49, v49, v20, v20\n\t" "s_waitcnt lgkmcnt(12)\n\t" "v_mfma_f32_16x16x32_bf16 v[176:179], a[140:143], a[32:35], v[176:179]\n\t"
    "ds_read_b64_tr_b16 a[136:137], %[vr3] offset:0\n\t" "v_fma_f32 v50, v50, v20, v20\n\t" "v_fma_f32 v51, v51, v20, v20\n\t" "v_rcp_f32 v48, v48\n\t"
    "v_mfma_f32_16x16x32_bf16 v[180:183], a[140:143], a[36:39], v[180:183]\n\t" "ds_read_b64_tr_b16 a[138:139], %[vr3] offset:3584\n\t" "v_rcp_f32 v49, v49\n\t"
    "v_mfma_f32_16x16x32_bf16 v[184:187], a[140:143], a[40:43], v[184:187]\n\t" "v_rcp_f32 v50, v50\n\t" "v_mfma_f32_16x16x32_bf16 v[188:191], a[140:143], a[44:47], v[188:191]\n\t"
    "v_rcp_f32 v51, v51\n\t" "s_waitcnt lgkmcnt(12)\n\t" "v_mfma_f32_16x16x32_bf16 v[64:67], a[144:147], a[48:51], v[64:67]\n\t" "ds_read_b64_tr_b16 a[140:141], %[vr3] offset:7168\n\t"
    "v_mul_f32 v160, v160, v48\n\t" "v_mul_f32 v161, v161, v49\n\t" "v_mul_f32 v162, v162, v50\n\t" "v_mfma_f32_16x16x32_bf16 v[68:71], a[144:147], a[52:55], v[68:71]\n\t"
    "ds_read_b64_tr_b16 a[142:143], %[vr3] offset:10752\n\t" "v_mul_f32 v163, v163, v51\n\t" "v_cvt_pk_bf16_f32 v34, v160, v161\n\t" "v_mfma_f32_16x16x32_bf16 v[72:75], a[144:147], a[56:59], v[72:75]\n\t"
    "v_cvt_pk_bf16_f32 v35, v162, v163\n\t" "global_store_dwordx4 %[va], v[32:35], s[90:91] offset:0 sc1\n\t" "v_mfma_f32_16x16x32_bf16 v[76:79], a[144:147], a[60:63], v[76:79]\n\t"
    "v_mul_f32 v52, v52, v17\n\t" "v_mul_f32 v53, v53, v17\n\t" "s_waitcnt lgkmcnt(12)\n\t" "v_mfma_f32_16x16x32_bf16 v[176:179], a[148:151], a[48:51], v[176:179]\n\t"
    "ds_read_b64_tr_b16 a[144:145], %[vr3] offset:14336\n\t" "v_mul_f32 v54, v54, v17\n\t" "v_mul_f32 v55, v55, v17\n\t" "v_exp_f32 v52, v52\n\t"
    "v_mfma_f32_16x16x32_bf16 v[180:183], a[148:151], a[52:55], v[180:183]\n\t" "ds_read_b64_tr_b16 a[146:147], %[vr3] offset:17920\n\t" "v_exp_f32 v53, v53\n\t"
    "v_mfma_f32_16x16x32_bf16 v[184:187], a[148:151], a[56:59], v[184:187]\n\t" "v_exp_f32 v54, v54\n\t" "v_mfma_f32_16x16x32_bf16 v[188:191], a[148:151], a[60:63], v[188:191]\n\t"
    "v_exp_f32 v55, v55\n\t" "s_waitcnt lgkmcnt(12)\n\t" "v_mfma_f32_16x16x32_bf16 v[64:67], a[152:155], a[64:67], v[64:67]\n\t" "ds_read_b64_tr_b16 a[148:149], %[vr3] offset:21504\n\t"
    "v_fma_f32 v52, v52, v21, v21\n\t" "v_fma_f32 v53, v53, v21, v21\n\t" "v_fma_f32 v54, v54, v21, v21\n\t" "v_mfma_f32_16x16x32_bf16 v[68:71], a[152:155], a[68:71], v[68:71]\n\t"
    "ds_read_b64_tr_b16 a[150:151], %[vr3] offset:25088\n\t" "v_fma_f32 v55, v55, v21, v21\n\t" "v_rcp_f32 v52, v52\n\t" "v_mfma_f32_16x16x32_bf16 v[72:75], a[152:155], a[72:75], v[72:75]\n\t"
    "v_rcp_f32 v53, v53\n\t" "v_mfma_f32_16x16x32_bf16 v[76:79], a[152:155], a[76:79], v[76:79]\n\t" "v_rcp_f32 v54, v54\n\t" "s_waitcnt lgkmcnt(12)\n\t"
    "v_mfma_f32_16x16x32_bf16 v[176:179], a[156:159], a[64:67], v[176:179]\n\t" "ds_read_b128 a[152:155], v31 offset:512\n\t" "v_rcp_f32 v55, v55\n\t" "v_mul_f32 v164, v164, v52\n\t"
    "v_mfma_f32_16x16x32_bf16 v[180:183], a[156:159], a[68:71], v[180:183]\n\t" "v_mul_f32 v165, v165, v53\n\t" "v_mul_f32 v166, v166, v54\n\t"
    "v_mfma_f32_16x16x32_bf16 v[184:187], a[156:159], a[72:75], v[184:187]\n\t" "v_mul_f32 v167, v167, v55\n\t" "v_cvt_pk_bf16_f32 v38, v164, v165\n\t"
    "v_mfma_f32_16x16x32_bf16 v[188:191], a[156:159], a[76:79], v[188:191]\n\t" "v_cvt_pk_bf16_f32 v39, v166, v167\n\t" "global_store_dwordx4 %[va], v[36:39], s[90:91] offset:1024 sc1\n\t"
    "s_waitcnt lgkmcnt(11)\n\t" "v_mfma_f32_16x16x32_bf16 v[64:67], a[128:131], a[80:83], v[64:67]\n\t" "ds_read_b128 a[156:159], v31 offset:768\n\t" "v_mul_f32 v56, v56, v18\n\t"
    "v_mul_f32 v57, v57, v18\n\t" "v_mul_f32 v58, v58, v18\n\t" "v_mfma_f32_16x16x32_bf16 v[68:71], a[128:131], a[84:87], v[68:71]\n\t" "v_mul_f32 v59, v59, v18\n\t" "v_exp_f32 v56, v56\n\t"
    "v_mfma_f32_16x16x32_bf16 v[72:75], a[128:131], a[88:91], v[72:75]\n\t" "v_exp_f32 v57, v57\n\t" "v_mfma_f32_16x16x32_bf16 v[76:79], a[128:131], a[92:95], v[76:79]\n\t" "v_exp_f32 v58, v58\n\t"
    "s_waitcnt lgkmcnt(10)\n\t" "v_mfma_f32_16x16x32_bf16 v[176:179], a[132:135], a[80:83], v[176:179]\n\t" "ds_read_b64_tr_b16 a[128:129], %[vr2] offset:32\n\t" "v_exp_f32 v59, v59\n\t"
    "v_fma_f32 v56, v56, v22, v22\n\t" "v_mfma_f32_16x16x32_bf16 v[180:183], a[132:135], a[84:87], v[180:183]\n\t" "ds_read_b64_tr_b16 a[130:131], %[vr2] offset:3616\n\t"
    "v_fma_f32 v57, v57, v22, v22\n\t" "v_fma_f32 v58, v58, v22, v22\n\t" "v_mfma_f32_16x16x32_bf16 v[184:187], a[132:135], a[88:91], v[184:187]\n\t" "v_fma_f32 v59, v59, v22, v22\n\t"
    "v_rcp_f32 v56, v56\n\t" "v_mfma_f32_16x16x32_bf16 v[188:191], a[132:135], a[92:95], v[188:191]\n\t" "v_rcp_f32 v57, v57\n\t" "s_waitcnt lgkmcnt(10)\n\t"
    "v_mfma_f32_16x16x32_bf16 v[64:67], a[136:139], a[96:99], v[64:67]\n\t" "ds_read_b64_tr_b16 a[132:133], %[vr2] offset:7200\n\t" "v_rcp_f32 v58, v58\n\t" "v_rcp_f32 v59, v59\n\t"
    "v_mfma_f32_16x16x32_bf16 v[68:71], a[136:139], a[100:103], v[68:71]\n\t" "ds_read_b64_tr_b16 a[134:135], %[vr2] offset:10784\n\t" "v_mul_f32 v168, v168, v56\n\t" "v_mul_f32 v169, v169, v57\n\t"
    "v_mfma_f32_16x16x32_bf16 v[72:75], a[136:139], a[104:107], v[72:75]\n\t" "v_mul_f32 v170, v170, v58\n\t" "v_mul_f32 v171, v171, v59\n\t"
    "v_mfma_f32_16x16x32_bf16 v[76:79], a[136:139], a[108:111], v[76:79]\n\t" "v_cvt_pk_bf16_f32 v42, v168, v169\n\t" "v_cvt_pk_bf16_f32 v43, v170, v171\n\t" "s_waitcnt lgkmcnt(10)\n\t"
    "v_mfma_f32_16x16x32_bf16 v[176:179], a[140:143], a[96:99], v[176:179]\n\t" "ds_read_b64_tr_b16 a[136:137], %[vr2] offset:14368\n\t" "global_store_dwordx4 %[va], v[40:43], s[92:93] offset:0 sc1\n\t"
    "v_mul_f32 v60, v60, v19\n\t" "v_mul_f32 v61, v61, v19\n\t" "v_mfma_f32_16x16x32_bf16 v[180:183], a[140:143], a[100:103], v[180:183]\n\t" "ds_read_b64_tr_b16 a[138:139], %[vr2] offset:17952\n\t"
    "v_mul_f32 v62, v62, v19\n\t" "v_mul_f32 v63, v63, v19\n\t" "v_mfma_f32_16x16x32_bf16 v[184:187], a[140:143], a[104:107], v[184:187]\n\t" "v_exp_f32 v60, v60\n\t"
    "v_mfma_f32_16x16x32_bf16 v[188:191], a[140:143], a[108:111], v[188:191]\n\t" "v_exp_f32 v61, v61\n\t" "s_waitcnt lgkmcnt(10)\n\t"
    "v_mfma_f32_16x16x32_bf16 v[64:67], a[144:147], a[112:115], v[64:67]\n\t" "ds_read_b64_tr_b16 a[140:141], %[vr2] offset:21536\n\t" "v_exp_f32 v62, v62\n\t" "v_exp_f32 v63, v63\n\t"
    "v_mfma_f32_16x16x32_bf16 v[68:71], a[144:147], a[116:119], v[68:71]\n\t" "ds_read_b64_tr_b16 a[142:143], %[vr2] offset:25120\n\t" "v_fma_f32 v60, v60, v23, v23\n\t" "v_fma_f32 v61, v61, v23, v23\n\t"
    "v_mfma_f32_16x16x32_bf16 v[72:75], a[144:147], a[120:123], v[72:75]\n\t" "v_fma_f32 v62, v62, v23, v23\n\t" "v_fma_f32 v63, v63, v23, v23\n\t"
    "v_mfma_f32_16x16x32_bf16 v[76:79], a[144:147], a[124:127], v[76:79]\n\t" "v_rcp_f32 v60, v60\n\t" "s_waitcnt lgkmcnt(10)\n\t"
    "v_mfma_f32_16x16x32_bf16 v[176:179], a[148:151], a[112:115], v[176:179]\n\t" "ds_read_b64_tr_b16 a[144:145], %[vr2] offset:28704\n\t" "v_rcp_f32 v61, v61\n\t" "v_rcp_f32 v62, v62\n\t"
    "v_mfma_f32_16x16x32_bf16 v[180:183], a[148:151], a[116:119], v[180:183]\n\t" "ds_read_b64_tr_b16 a[146:147], %[vr2] offset:32288\n\t" "v_rcp_f32 v63, v63\n\t"
    "v_mfma_f32_16x16x32_bf16 v[184:187], a[148:151], a[120:123], v[184:187]\n\t" "v_mul_f32 v172, v172, v60\n\t" "v_mul_f32 v173, v173, v61\n\t"
    "v_mfma_f32_16x16x32_bf16 v[188:191], a[148:151], a[124:127], v[188:191]\n\t" "v_mul_f32 v174, v174, v62\n\t" "v_mul_f32 v175, v175, v63\n\t" "s_waitcnt lgkmcnt(11)\n\t"
    "v_mfma_f32_16x16x32_bf16 v[80:83], a[152:155], a[200:203], v[80:83]\n\t" "ds_read_b64_tr_b16 a[148:149], %[vr2] offset:35872\n\t" "v_cvt_pk_bf16_f32 v46, v172, v173\n\t"
    "v_cvt_pk_bf16_f32 v47, v174, v175\n\t" "global_store_dwordx4 %[va], v[44:47], s[92:93] offset:1024 sc1\n\t" "v_mfma_f32_16x16x32_bf16 v[84:87], a[152:155], a[204:207], v[84:87]\n\t"
    "ds_read_b64_tr_b16 a[150:151], %[vr2] offset:39456\n\t" "v_mfma_f32_16x16x32_bf16 v[88:91], a[152:155], a[208:211], v[88:91]\n\t"
    "v_mfma_f32_16x16x32_bf16 v[92:95], a[152:155], a[212:215], v[92:95]\n\t" "s_waitcnt lgkmcnt(12)\n\t" "v_mfma_f32_16x16x32_bf16 v[192:195], a[156:159], a[200:203], v[192:195]\n\t"
    "ds_read_b64_tr_b16 a[152:153], %[vr2] offset:43040\n\t" "v_mfma_f32_16x16x32_bf16 v[196:199], a[156:159], a[204:207], v[196:199]\n\t" "ds_read_b64_tr_b16 a[154:155], %[vr2] offset:46624\n\t"
    "v_mul_f32 v64, v64, v16\n\t" "v_mul_f32 v65, v65, v16\n\t" "v_mfma_f32_16x16x32_bf16 v[200:203], a[156:159], a[208:211], v[200:203]\n\t" "v_mul_f32 v66, v66, v16\n\t" "v_mul_f32 v67, v67, v16\n\t"
    "v_mfma_f32_16x16x32_bf16 v[204:207], a[156:159], a[212:215], v[204:207]\n\t" "v_exp_f32 v64, v64\n\t" "s_waitcnt lgkmcnt(12)\n\t"
    "v_mfma_f32_16x16x32_bf16 v[80:83], a[128:131], a[32:35], v[80:83]\n\t" "ds_read_b64_tr_b16 a[156:157], %[vr2] offset:50208\n\t" "v_exp_f32 v65, v65\n\t" "v_exp_f32 v66, v66\n\t"
    "v_mfma_f32_16x16x32_bf16 v[84:87], a[128:131], a[36:39], v[84:87]\n\t" "ds_read_b64_tr_b16 a[158:159], %[vr2] offset:53792\n\t" "v_exp_f32 v67, v67\n\t"
    "v_mfma_f32_16x16x32_bf16 v[88:91], a[128:131], a[40:43], v[88:91]\n\t" "v_fma_f32 v64, v64, v20, v20\n\t" "v_fma_f32 v65, v65, v20, v20\n\t"
    "v_mfma_f32_16x16x32_bf16 v[92:95], a[128:131], a[44:47], v[92:95]\n\t" "v_fma_f32 v66, v66, v20, v20\n\t" "v_fma_f32 v67, v67, v20, v20\n\t" "s_waitcnt lgkmcnt(12)\n\t"
    "v_mfma_f32_16x16x32_bf16 v[192:195], a[132:135], a[32:35], v[192:195]\n\t" "ds_read_b64_tr_b16 a[128:129], %[vr3] offset:32\n\t" "v_rcp_f32 v64, v64\n\t" "v_rcp_f32 v65, v65\n\t"
    "v_mfma_f32_16x16x32_bf16 v[196:199], a[132:135], a[36:39], v[196:199]\n\t" "ds_read_b64_tr_b16 a[130:131], %[vr3] offset:3616\n\t" "v_rcp_f32 v66, v66\n\t"
    "v_mfma_f32_16x16x32_bf16 v[200:203], a[132:135], a[40:43], v[200:203]\n\t" "v_rcp_f32 v67, v67\n\t" "v_mfma_f32_16x16x32_bf16 v[204:207], a[132:135], a[44:47], v[204:207]\n\t"
    "v_mul_f32 v176, v176, v64\n\t" "v_mul_f32 v177, v177, v65\n\t" "s_waitcnt lgkmcnt(12)\n\t" "v_mfma_f32_16x16x32_bf16 v[80:83], a[136:139], a[48:51], v[80:83]\n\t"
    "ds_read_b64_tr_b16 a[132:133], %[vr3] offset:7200\n\t" "v_mul_f32 v178, v178, v66\n\t" "v_mul_f32 v179, v179, v67\n\t" "v_cvt_pk_bf16_f32 v64, v176, v177\n\t"
    "v_mfma_f32_16x16x32_bf16 v[84:87], a[136:139], a[52:55], v[84:87]\n\t" "ds_read_b64_tr_b16 a[134:135], %[vr3] offset:10784\n\t" "v_cvt_pk_bf16_f32 v65, v178, v179\n\t" "v_mul_f32 v68, v68, v17\n\t"
    "v_mfma_f32_16x16x32_bf16 v[88:91], a[136:139], a[56:59], v[88:91]\n\t" "v_mul_f32 v69, v69, v17\n\t" "v_mul_f32 v70, v70, v17\n\t"
    "v_mfma_f32_16x16x32_bf16 v[92:95], a[136:139], a[60:63], v[92:95]\n\t" "v_mul_f32 v71, v71, v17\n\t" "v_exp_f32 v68, v68\n\t" "s_waitcnt lgkmcnt(12)\n\t"
    "v_mfma_f32_16x16x32_bf16 v[192:195], a[140:143], a[48:51], v[192:195]\n\t" "ds_read_b64_tr_b16 a[136:137], %[vr3] offset:14368\n\t" "v_exp_f32 v69, v69\n\t" "v_exp_f32 v70, v70\n\t"
    "v_mfma_f32_16x16x32_bf16 v[196:199], a[140:143], a[52:55], v[196:199]\n\t" "ds_read_b64_tr_b16 a[138:139], %[vr3] offset:17952\n\t" "v_exp_f32 v71, v71\n\t"
    "v_mfma_f32_16x16x32_bf16 v[200:203], a[140:143], a[56:59], v[200:203]\n\t" "v_fma_f32 v68, v68, v21, v21\n\t" "v_fma_f32 v69, v69, v21, v21\n\t"
    "v_mfma_f32_16x16x32_bf16 v[204:207], a[140:143], a[60:63], v[204:207]\n\t" "v_fma_f32 v70, v70, v21, v21\n\t" "v_fma_f32 v71, v71, v21, v21\n\t" "s_waitcnt lgkmcnt(12)\n\t"
    "v_mfma_f32_16x16x32_bf16 v[80:83], a[144:147], a[64:67], v[80:83]\n\t" "ds_read_b64_tr_b16 a[140:141], %[vr3] offset:21536\n\t" "v_rcp_f32 v68, v68\n\t" "v_rcp_f32 v69, v69\n\t"
    "v_mfma_f32_16x16x32_bf16 v[84:87], a[144:147], a[68:71], v[84:87]\n\t" "ds_read_b64_tr_b16 a[142:143], %[vr3] offset:25120\n\t" "v_rcp_f32 v70, v70\n\t"
    "v_mfma_f32_16x16x32_bf16 v[88:91], a[144:147], a[72:75], v[88:91]\n\t" "v_rcp_f32 v71, v71\n\t" "v_mfma_f32_16x16x32_bf16 v[92:95], a[144:147], a[76:79], v[92:95]\n\t" "v_mul_f32 v180, v180, v68\n\t"
    "v_mul_f32 v181, v181, v69\n\t" "s_waitcnt lgkmcnt(12)\n\t" "v_mfma_f32_16x16x32_bf16 v[192:195], a[148:151], a[64:67], v[192:195]\n\t" "v_accvgpr_read_b32 v31, %[ct]\n\t" "s_mov_b64 exec, 0xffff\n\t"
    "v_add_u32 v31, %[gt2], v31\n\t" "s_mov_b64 exec, -1\n\t" "ds_read_b128 a[144:147], v31 offset:0\n\t" "v_mul_f32 v182, v182, v70\n\t" "v_mul_f32 v183, v183, v71\n\t"
    "v_cvt_pk_bf16_f32 v68, v180, v181\n\t" "v_mfma_f32_16x16x32_bf16 v[196:199], a[148:151], a[68:71], v[196:199]\n\t" "v_cvt_pk_bf16_f32 v69, v182, v183\n\t" "v_mul_f32 v72, v72, v18\n\t"
    "v_mfma_f32_16x16x32_bf16 v[200:203], a[148:151], a[72:75], v[200:203]\n\t" "v_mul_f32 v73, v73, v18\n\t" "v_mul_f32 v74, v74, v18\n\t"
    "v_mfma_f32_16x16x32_bf16 v[204:207], a[148:151], a[76:79], v[204:207]\n\t" "v_mul_f32 v75, v75, v18\n\t" "v_exp_f32 v72, v72\n\t" "s_waitcnt lgkmcnt(11)\n\t"
    "v_mfma_f32_16x16x32_bf16 v[80:83], a[152:155], a[80:83], v[80:83]\n\t" "ds_read_b128 a[148:151], v31 offset:256\n\t" "v_exp_f32 v73, v73\n\t" "v_exp_f32 v74, v74\n\t"
    "v_mfma_f32_16x16x32_bf16 v[84:87], a[152:155], a[84:87], v[84:87]\n\t" "v_exp_f32 v75, v75\n\t" "v_mfma_f32_16x16x32_bf16 v[88:91], a[152:155], a[88:91], v[88:91]\n\t"
    "v_fma_f32 v72, v72, v22, v22\n\t" "v_fma_f32 v73, v73, v22, v22\n\t" "v_mfma_f32_16x16x32_bf16 v[92:95], a[152:155], a[92:95], v[92:95]\n\t" "v_fma_f32 v74, v74, v22, v22\n\t"
    "v_fma_f32 v75, v75, v22, v22\n\t" "s_waitcnt lgkmcnt(10)\n\t" "v_mfma_f32_16x16x32_bf16 v[192:195], a[156:159], a[80:83], v[192:195]\n\t" "ds_read_b64_tr_b16 a[152:153], %[vr4] offset:0\n\t"
    "v_rcp_f32 v72, v72\n\t" "v_rcp_f32 v73, v73\n\t" "v_mfma_f32_16x16x32_bf16 v[196:199], a[156:159], a[84:87], v[196:199]\n\t" "ds_read_b64_tr_b16 a[154:155], %[vr4] offset:3584\n\t"
    "v_rcp_f32 v74, v74\n\t" "v_mfma_f32_16x16x32_bf16 v[200:203], a[156:159], a[88:91], v[200:203]\n\t" "v_rcp_f32 v75, v75\n\t"
    "v_mfma_f32_16x16x32_bf16 v[204:207], a[156:159], a[92:95], v[204:207]\n\t" "v_mul_f32 v184, v184, v72\n\t" "v_mul_f32 v185, v185, v73\n\t" "s_waitcnt lgkmcnt(10)\n\t"
    "v_mfma_f32_16x16x32_bf16 v[80:83], a[128:131], a[96:99], v[80:83]\n\t" "ds_read_b64_tr_b16 a[156:157], %[vr4] offset:7168\n\t" "v_mul_f32 v186, v186, v74\n\t" "v_mul_f32 v187, v187, v75\n\t"
    "v_cvt_pk_bf16_f32 v72, v184, v185\n\t" "v_mfma_f32_16x16x32_bf16 v[84:87], a[128:131], a[100:103], v[84:87]\n\t" "ds_read_b64_tr_b16 a[158:159], %[vr4] offset:10752\n\t"
    "v_cvt_pk_bf16_f32 v73, v186, v187\n\t" "v_mul_f32 v76, v76, v19\n\t" "v_mfma_f32_16x16x32_bf16 v[88:91], a[128:131], a[104:107], v[88:91]\n\t" "v_mul_f32 v77, v77, v19\n\t"
    "v_mul_f32 v78, v78, v19\n\t" "v_mfma_f32_16x16x32_bf16 v[92:95], a[128:131], a[108:111], v[92:95]\n\t" "v_mul_f32 v79, v79, v19\n\t" "v_exp_f32 v76, v76\n\t" "s_waitcnt lgkmcnt(10)\n\t"
    "v_mfma_f32_16x16x32_bf16 v[192:195], a[132:135], a[96:99], v[192:195]\n\t" "ds_read_b64_tr_b16 a[128:129], %[vr4] offset:14336\n\t" "v_exp_f32 v77, v77\n\t" "v_exp_f32 v78, v78\n\t"
    "v_mfma_f32_16x16x32_bf16 v[196:199], a[132:135], a[100:103], v[196:199]\n\t" "ds_read_b64_tr_b16 a[130:131], %[vr4] offset:17920\n\t" "v_exp_f32 v79, v79\n\t"
    "v_mfma_f32_16x16x32_bf16 v[200:203], a[132:135], a[104:107], v[200:203]\n\t" "v_fma_f32 v76, v76, v23, v23\n\t" "v_fma_f32 v77, v77, v23, v23\n\t"
    "v_mfma_f32_16x16x32_bf16 v[204:207], a[132:135], a[108:111], v[204:207]\n\t" "v_fma_f32 v78, v78, v23, v23\n\t" "v_fma_f32 v79, v79, v23, v23\n\t" "s_waitcnt lgkmcnt(10)\n\t"
    "v_mfma_f32_16x16x32_bf16 v[80:83], a[136:139], a[112:115], v[80:83]\n\t" "ds_read_b64_tr_b16 a[132:133], %[vr4] offset:21504\n\t" "v_rcp_f32 v76, v76\n\t" "v_rcp_f32 v77, v77\n\t"
    "v_mfma_f32_16x16x32_bf16 v[84:87], a[136:139], a[116:119], v[84:87]\n\t" "ds_read_b64_tr_b16 a[134:135], %[vr4] offset:25088\n\t" "v_rcp_f32 v78, v78\n\t"
    "v_mfma_f32_16x16x32_bf16 v[88:91], a[136:139], a[120:123], v[88:91]\n\t" "v_rcp_f32 v79, v79\n\t" "v_mfma_f32_16x16x32_bf16 v[92:95], a[136:139], a[124:127], v[92:95]\n\t"
    "v_mul_f32 v188, v188, v76\n\t" "v_mul_f32 v189, v189, v77\n\t" "s_waitcnt lgkmcnt(10)\n\t" "v_mfma_f32_16x16x32_bf16 v[192:195], a[140:143], a[112:115], v[192:195]\n\t"
    "ds_read_b64_tr_b16 a[136:137], %[vr4] offset:28672\n\t" "v_mul_f32 v190, v190, v78\n\t" "v_mul_f32 v191, v191, v79\n\t" "v_cvt_pk_bf16_f32 v76, v188, v189\n\t"
    "v_mfma_f32_16x16x32_bf16 v[196:199], a[140:143], a[116:119], v[196:199]\n\t" "ds_read_b64_tr_b16 a[138:139], %[vr4] offset:32256\n\t" "v_cvt_pk_bf16_f32 v77, v190, v191\n\t"
    "v_mfma_f32_16x16x32_bf16 v[200:203], a[140:143], a[120:123], v[200:203]\n\t" "v_mfma_f32_16x16x32_bf16 v[204:207], a[140:143], a[124:127], v[204:207]\n\t" "s_waitcnt lgkmcnt(11)\n\t"
    "v_mfma_f32_16x16x32_bf16 v[96:99], a[144:147], a[200:203], v[96:99]\n\t" "ds_read_b64_tr_b16 a[140:141], %[vr4] offset:35840\n\t"
    "v_mfma_f32_16x16x32_bf16 v[100:103], a[144:147], a[204:207], v[100:103]\n\t" "ds_read_b64_tr_b16 a[142:143], %[vr4] offset:39424\n\t"
    "v_mfma_f32_16x16x32_bf16 v[104:107], a[144:147], a[208:211], v[104:107]\n\t" "v_mfma_f32_16x16x32_bf16 v[108:111], a[144:147], a[212:215], v[108:111]\n\t" "s_waitcnt lgkmcnt(12)\n\t"
    "v_mfma_f32_16x16x32_bf16 v[208:211], a[148:151], a[200:203], v[208:211]\n\t" "ds_read_b64_tr_b16 a[144:145], %[vr4] offset:43008\n\t" "s_add_u32 s90, %[shl], %[go1]\n\t"
    "s_addc_u32 s91, %[shh], 0\n\t" "s_add_u32 s92, s90, 229376\n\t" "s_addc_u32 s93, s91, 0\n\t" "v_mul_f32 v80, v80, v16\n\t"
    "v_mfma_f32_16x16x32_bf16 v[212:215], a[148:151], a[204:207], v[212:215]\n\t" "ds_read_b64_tr_b16 a[146:147], %[vr4] offset:46592\n\t" "v_mul_f32 v81, v81, v16\n\t" "v_mul_f32 v82, v82, v16\n\t"
    "v_mfma_f32_16x16x32_bf16 v[216:219], a[148:151], a[208:211], v[216:219]\n\t" "v_mul_f32 v83, v83, v16\n\t" "v_exp_f32 v80, v80\n\t"
    "v_mfma_f32_16x16x32_bf16 v[220:223], a[148:151], a[212:215], v[220:223]\n\t" "v_exp_f32 v81, v81\n\t" "s_waitcnt lgkmcnt(12)\n\t"
    "v_mfma_f32_16x16x32_bf16 v[96:99], a[152:155], a[32:35], v[96:99]\n\t" "ds_read_b64_tr_b16 a[148:149], %[vr4] offset:50176\n\t" "v_exp_f32 v82, v82\n\t" "v_exp_f32 v83, v83\n\t"
    "v_mfma_f32_16x16x32_bf16 v[100:103], a[152:155], a[36:39], v[100:103]\n\t" "ds_read_b64_tr_b16 a[150:151], %[vr4] offset:53760\n\t" "v_fma_f32 v80, v80, v20, v20\n\t"
    "v_fma_f32 v81, v81, v20, v20\n\t" "v_mfma_f32_16x16x32_bf16 v[104:107], a[152:155], a[40:43], v[104:107]\n\t" "v_fma_f32 v82, v82, v20, v20\n\t" "v_fma_f32 v83, v83, v20, v20\n\t"
    "v_mfma_f32_16x16x32_bf16 v[108:111], a[152:155], a[44:47], v[108:111]\n\t" "v_rcp_f32 v80, v80\n\t" "s_waitcnt lgkmcnt(12)\n\t"
    "v_mfma_f32_16x16x32_bf16 v[208:211], a[156:159], a[32:35], v[208:211]\n\t" "ds_read_b64_tr_b16 a[152:153], %[vr5] offset:0\n\t" "v_rcp_f32 v81, v81\n\t" "v_rcp_f32 v82, v82\n\t"
    "v_mfma_f32_16x16x32_bf16 v[212:215], a[156:159], a[36:39], v[212:215]\n\t" "ds_read_b64_tr_b16 a[154:155], %[vr5] offset:3584\n\t" "v_rcp_f32 v83, v83\n\t"
    "v_mfma_f32_16x16x32_bf16 v[216:219], a[156:159], a[40:43], v[216:219]\n\t" "v_mul_f32 v192, v192, v80\n\t" "v_mul_f32 v193, v193, v81\n\t"
    "v_mfma_f32_16x16x32_bf16 v[220:223], a[156:159], a[44:47], v[220:223]\n\t" "v_mul_f32 v194, v194, v82\n\t" "v_mul_f32 v195, v195, v83\n\t" "s_waitcnt lgkmcnt(12)\n\t"
    "v_mfma_f32_16x16x32_bf16 v[96:99], a[128:131], a[48:51], v[96:99]\n\t" "ds_read_b64_tr_b16 a[156:157], %[vr5] offset:7168\n\t" "v_cvt_pk_bf16_f32 v66, v192, v193\n\t"
    "v_cvt_pk_bf16_f32 v67, v194, v195\n\t" "global_store_dwordx4 %[va], v[64:67], s[90:91] offset:0 sc1\n\t" "v_mfma_f32_16x16x32_bf16 v[100:103], a[128:131], a[52:55], v[100:103]\n\t"
    "ds_read_b64_tr_b16 a[158:159], %[vr5] offset:10752\n\t" "v_mul_f32 v84, v84, v17\n\t" "v_mul_f32 v85, v85, v17\n\t" "v_mfma_f32_16x16x32_bf16 v[104:107], a[128:131], a[56:59], v[104:107]\n\t"
    "v_mul_f32 v86, v86, v17\n\t" "v_mul_f32 v87, v87, v17\n\t" "v_mfma_f32_16x16x32_bf16 v[108:111], a[128:131], a[60:63], v[108:111]\n\t" "v_exp_f32 v84, v84\n\t" "s_waitcnt lgkmcnt(12)\n\t"
    "v_mfma_f32_16x16x32_bf16 v[208:211], a[132:135], a[48:51], v[208:211]\n\t" "ds_read_b64_tr_b16 a[128:129], %[vr5] offset:14336\n\t" "v_exp_f32 v85, v85\n\t" "v_exp_f32 v86, v86\n\t"
    "v_mfma_f32_16x16x32_bf16 v[212:215], a[132:135], a[52:55], v[212:215]\n\t" "ds_read_b64_tr_b16 a[130:131], %[vr5] offset:17920\n\t" "v_exp_f32 v87, v87\n\t"
    "v_mfma_f32_16x16x32_bf16 v[216:219], a[132:135], a[56:59], v[216:219]\n\t" "v_fma_f32 v84, v84, v21, v21\n\t" "v_fma_f32 v85, v85, v21, v21\n\t"
    "v_mfma_f32_16x16x32_bf16 v[220:223], a[132:135], a[60:63], v[220:223]\n\t" "v_fma_f32 v86, v86, v21, v21\n\t" "v_fma_f32 v87, v87, v21, v21\n\t" "s_waitcnt lgkmcnt(12)\n\t"
    "v_mfma_f32_16x16x32_bf16 v[96:99], a[136:139], a[64:67], v[96:99]\n\t" "ds_read_b64_tr_b16 a[132:133], %[vr5] offset:21504\n\t" "v_rcp_f32 v84, v84\n\t" "v_rcp_f32 v85, v85\n\t"
    "v_mfma_f32_16x16x32_bf16 v[100:103], a[136:139], a[68:71], v[100:103]\n\t" "ds_read_b64_tr_b16 a[134:135], %[vr5] offset:25088\n\t" "v_rcp_f32 v86, v86\n\t"
    "v_mfma_f32_16x16x32_bf16 v[104:107], a[136:139], a[72:75], v[104:107]\n\t" "v_rcp_f32 v87, v87\n\t" "v_mfma_f32_16x16x32_bf16 v[108:111], a[136:139], a[76:79], v[108:111]\n\t"
    "v_mul_f32 v196, v196, v84\n\t" "v_mul_f32 v197, v197, v85\n\t" "s_waitcnt lgkmcnt(12)\n\t" "v_mfma_f32_16x16x32_bf16 v[208:211], a[140:143], a[64:67], v[208:211]\n\t"
    "ds_read_b128 a[136:139], v31 offset:512\n\t" "v_mul_f32 v198, v198, v86\n\t" "v_mul_f32 v199, v199, v87\n\t" "v_cvt_pk_bf16_f32 v70, v196, v197\n\t"
    "v_mfma_f32_16x16x32_bf16 v[212:215], a[140:143], a[68:71], v[212:215]\n\t" "v_cvt_pk_bf16_f32 v71, v198, v199\n\t" "global_store_dwordx4 %[va], v[68:71], s[90:91] offset:1024 sc1\n\t"
    "v_mfma_f32_16x16x32_bf16 v[216:219], a[140:143], a[72:75], v[216:219]\n\t" "v_mul_f32 v88, v88, v18\n\t" "v_mul_f32 v89, v89, v18\n\t"
    "v_mfma_f32_16x16x32_bf16 v[220:223], a[140:143], a[76:79], v[220:223]\n\t" "v_mul_f32 v90, v90, v18\n\t" "v_mul_f32 v91, v91, v18\n\t" "s_waitcnt lgkmcnt(11)\n\t"
    "v_mfma_f32_16x16x32_bf16 v[96:99], a[144:147], a[80:83], v[96:99]\n\t" "ds_read_b128 a[140:143], v31 offset:768\n\t" "v_exp_f32 v88, v88\n\t" "v_exp_f32 v89, v89\n\t"
    "v_mfma_f32_16x16x32_bf16 v[100:103], a[144:147], a[84:87], v[100:103]\n\t" "v_exp_f32 v90, v90\n\t" "v_mfma_f32_16x16x32_bf16 v[104:107], a[144:147], a[88:91], v[104:107]\n\t"
    "v_exp_f32 v91, v91\n\t" "v_mfma_f32_16x16x32_bf16 v[108:111], a[144:147], a[92:95], v[108:111]\n\t" "v_fma_f32 v88, v88, v22, v22\n\t" "v_fma_f32 v89, v89, v22, v22\n\t" "s_waitcnt lgkmcnt(10)\n\t"
    "v_mfma_f32_16x16x32_bf16 v[208:211], a[148:151], a[80:83], v[208:211]\n\t" "ds_read_b64_tr_b16 a[144:145], %[vr4] offset:32\n\t" "v_fma_f32 v90, v90, v22, v22\n\t" "v_fma_f32 v91, v91, v22, v22\n\t"
    "v_rcp_f32 v88, v88\n\t" "v_mfma_f32_16x16x32_bf16 v[212:215], a[148:151], a[84:87], v[212:215]\n\t" "ds_read_b64_tr_b16 a[146:147], %[vr4] offset:3616\n\t" "v_rcp_f32 v89, v89\n\t"
    "v_mfma_f32_16x16x32_bf16 v[216:219], a[148:151], a[88:91], v[216:219]\n\t" "v_rcp_f32 v90, v90\n\t" "v_mfma_f32_16x16x32_bf16 v[220:223], a[148:151], a[92:95], v[220:223]\n\t"
    "v_rcp_f32 v91, v91\n\t" "s_waitcnt lgkmcnt(10)\n\t" "v_mfma_f32_16x16x32_bf16 v[96:99], a[152:155], a[96:99], v[96:99]\n\t" "ds_read_b64_tr_b16 a[148:149], %[vr4] offset:7200\n\t"
    "v_mul_f32 v200, v200, v88\n\t" "v_mul_f32 v201, v201, v89\n\t" "v_mul_f32 v202, v202, v90\n\t" "v_mfma_f32_16x16x32_bf16 v[100:103], a[152:155], a[100:103], v[100:103]\n\t"
    "ds_read_b64_tr_b16 a[150:151], %[vr4] offset:10784\n\t" "v_mul_f32 v203, v203, v91\n\t" "v_cvt_pk_bf16_f32 v74, v200, v201\n\t"
    "v_mfma_f32_16x16x32_bf16 v[104:107], a[152:155], a[104:107], v[104:107]\n\t" "v_cvt_pk_bf16_f32 v75, v202, v203\n\t" "global_store_dwordx4 %[va], v[72:75], s[92:93] offset:0 sc1\n\t"
    "v_mfma_f32_16x16x32_bf16 v[108:111], a[152:155], a[108:111], v[108:111]\n\t" "v_mul_f32 v92, v92, v19\n\t" "v_mul_f32 v93, v93, v19\n\t" "s_waitcnt lgkmcnt(10)\n\t"
    "v_mfma_f32_16x16x32_bf16 v[208:211], a[156:159], a[96:99], v[208:211]\n\t" "ds_read_b64_tr_b16 a[152:153], %[vr4] offset:14368\n\t" "v_mul_f32 v94, v94, v19\n\t" "v_mul_f32 v95, v95, v19\n\t"
    "v_exp_f32 v92, v92\n\t" "v_mfma_f32_16x16x32_bf16 v[212:215], a[156:159], a[100:103], v[212:215]\n\t" "ds_read_b64_tr_b16 a[154:155], %[vr4] offset:17952\n\t" "v_exp_f32 v93, v93\n\t"
    "v_mfma_f32_16x16x32_bf16 v[216:219], a[156:159], a[104:107], v[216:219]\n\t" "v_exp_f32 v94, v94\n\t" "v_mfma_f32_16x16x32_bf16 v[220:223], a[156:159], a[108:111], v[220:223]\n\t"
    "v_exp_f32 v95, v95\n\t" "s_waitcnt lgkmcnt(10)\n\t" "v_mfma_f32_16x16x32_bf16 v[96:99], a[128:131], a[112:115], v[96:99]\n\t" "ds_read_b64_tr_b16 a[156:157], %[vr4] offset:21536\n\t"
    "v_fma_f32 v92, v92, v23, v23\n\t" "v_fma_f32 v93, v93, v23, v23\n\t" "v_fma_f32 v94, v94, v23, v23\n\t" "v_mfma_f32_16x16x32_bf16 v[100:103], a[128:131], a[116:119], v[100:103]\n\t"
    "ds_read_b64_tr_b16 a[158:159], %[vr4] offset:25120\n\t" "v_fma_f32 v95, v95, v23, v23\n\t" "v_rcp_f32 v92, v92\n\t" "v_mfma_f32_16x16x32_bf16 v[104:107], a[128:131], a[120:123], v[104:107]\n\t"
    "v_rcp_f32 v93, v93\n\t" "v_mfma_f32_16x16x32_bf16 v[108:111], a[128:131], a[124:127], v[108:111]\n\t" "v_rcp_f32 v94, v94\n\t" "s_waitcnt lgkmcnt(10)\n\t"
    "v_mfma_f32_16x16x32_bf16 v[208:211], a[132:135], a[112:115], v[208:211]\n\t" "ds_read_b64_tr_b16 a[128:129], %[vr4] offset:28704\n\t" "v_rcp_f32 v95, v95\n\t" "v_mul_f32 v204, v204, v92\n\t"
    "v_mfma_f32_16x16x32_bf16 v[212:215], a[132:135], a[116:119], v[212:215]\n\t" "ds_read_b64_tr_b16 a[130:131], %[vr4] offset:32288\n\t" "v_mul_f32 v205, v205, v93\n\t" "v_mul_f32 v206, v206, v94\n\t"
    "v_mfma_f32_16x16x32_bf16 v[216:219], a[132:135], a[120:123], v[216:219]\n\t" "v_mul_f32 v207, v207, v95\n\t" "v_cvt_pk_bf16_f32 v78, v204, v205\n\t"
    "v_mfma_f32_16x16x32_bf16 v[220:223], a[132:135], a[124:127], v[220:223]\n\t" "v_cvt_pk_bf16_f32 v79, v206, v207\n\t" "global_store_dwordx4 %[va], v[76:79], s[92:93] offset:1024 sc1\n\t"
    "s_waitcnt lgkmcnt(11)\n\t" "v_mfma_f32_16x16x32_bf16 v[112:115], a[136:139], a[200:203], v[112:115]\n\t" "ds_read_b64_tr_b16 a[132:133], %[vr4] offset:35872\n\t"
    "v_mfma_f32_16x16x32_bf16 v[116:119], a[136:139], a[204:207], v[116:119]\n\t" "ds_read_b64_tr_b16 a[134:135], %[vr4] offset:39456\n\t"
    "v_mfma_f32_16x16x32_bf16 v[120:123], a[136:139], a[208:211], v[120:123]\n\t" "v_mfma_f32_16x16x32_bf16 v[124:127], a[136:139], a[212:215], v[124:127]\n\t" "s_waitcnt lgkmcnt(12)\n\t"
    "v_mfma_f32_16x16x32_bf16 v[224:227], a[140:143], a[200:203], v[224:227]\n\t" "ds_read_b64_tr_b16 a[136:137], %[vr4] offset:43040\n\t" "v_mul_f32 v96, v96, v16\n\t" "v_mul_f32 v97, v97, v16\n\t"
    "v_mul_f32 v98, v98, v16\n\t" "v_mfma_f32_16x16x32_bf16 v[228:231], a[140:143], a[204:207], v[228:231]\n\t" "ds_read_b64_tr_b16 a[138:139], %[vr4] offset:46624\n\t" "v_mul_f32 v99, v99, v16\n\t"
    "v_exp_f32 v96, v96\n\t" "v_mfma_f32_16x16x32_bf16 v[232:235], a[140:143], a[208:211], v[232:235]\n\t" "v_exp_f32 v97, v97\n\t"
    "v_mfma_f32_16x16x32_bf16 v[236:239], a[140:143], a[212:215], v[236:239]\n\t" "v_exp_f32 v98, v98\n\t" "s_waitcnt lgkmcnt(12)\n\t"
    "v_mfma_f32_16x16x32_bf16 v[112:115], a[144:147], a[32:35], v[112:115]\n\t" "ds_read_b64_tr_b16 a[140:141], %[vr4] offset:50208\n\t" "v_exp_f32 v99, v99\n\t" "v_fma_f32 v96, v96, v20, v20\n\t"
    "v_mfma_f32_16x16x32_bf16 v[116:119], a[144:147], a[36:39], v[116:119]\n\t" "ds_read_b64_tr_b16 a[142:143], %[vr4] offset:53792\n\t" "v_fma_f32 v97, v97, v20, v20\n\t"
    "v_fma_f32 v98, v98, v20, v20\n\t" "v_mfma_f32_16x16x32_bf16 v[120:123], a[144:147], a[40:43], v[120:123]\n\t" "v_fma_f32 v99, v99, v20, v20\n\t" "v_rcp_f32 v96, v96\n\t"
    "v_mfma_f32_16x16x32_bf16 v[124:127], a[144:147], a[44:47], v[124:127]\n\t" "v_rcp_f32 v97, v97\n\t" "s_waitcnt lgkmcnt(12)\n\t"
    "v_mfma_f32_16x16x32_bf16 v[224:227], a[148:151], a[32:35], v[224:227]\n\t" "ds_read_b64_tr_b16 a[144:145], %[vr5] offset:32\n\t" "v_rcp_f32 v98, v98\n\t" "v_rcp_f32 v99, v99\n\t"
    "v_mfma_f32_16x16x32_bf16 v[228:231], a[148:151], a[36:39], v[228:231]\n\t" "ds_read_b64_tr_b16 a[146:147], %[vr5] offset:3616\n\t" "v_mul_f32 v208, v208, v96\n\t" "v_mul_f32 v209, v209, v97\n\t"
    "v_mfma_f32_16x16x32_bf16 v[232:235], a[148:151], a[40:43], v[232:235]\n\t" "v_mul_f32 v210, v210, v98\n\t" "v_mul_f32 v211, v211, v99\n\t"
    "v_mfma_f32_16x16x32_bf16 v[236:239], a[148:151], a[44:47], v[236:239]\n\t" "v_cvt_pk_bf16_f32 v96, v208, v209\n\t" "v_cvt_pk_bf16_f32 v97, v210, v211\n\t" "s_waitcnt lgkmcnt(12)\n\t"
    "v_mfma_f32_16x16x32_bf16 v[112:115], a[152:155], a[48:51], v[112:115]\n\t" "ds_read_b64_tr_b16 a[148:149], %[vr5] offset:7200\n\t" "v_mul_f32 v100, v100, v17\n\t" "v_mul_f32 v101, v101, v17\n\t"
    "v_mul_f32 v102, v102, v17\n\t" "v_mfma_f32_16x16x32_bf16 v[116:119], a[152:155], a[52:55], v[116:119]\n\t" "ds_read_b64_tr_b16 a[150:151], %[vr5] offset:10784\n\t" "v_mul_f32 v103, v103, v17\n\t"
    "v_exp_f32 v100, v100\n\t" "v_mfma_f32_16x16x32_bf16 v[120:123], a[152:155], a[56:59], v[120:123]\n\t" "v_exp_f32 v101, v101\n\t"
    "v_mfma_f32_16x16x32_bf16 v[124:127], a[152:155], a[60:63], v[124:127]\n\t" "v_exp_f32 v102, v102\n\t" "s_waitcnt lgkmcnt(12)\n\t"
    "v_mfma_f32_16x16x32_bf16 v[224:227], a[156:159], a[48:51], v[224:227]\n\t" "ds_read_b64_tr_b16 a[152:153], %[vr5] offset:14368\n\t" "v_exp_f32 v103, v103\n\t" "v_fma_f32 v100, v100, v21, v21\n\t"
    "v_mfma_f32_16x16x32_bf16 v[228:231], a[156:159], a[52:55], v[228:231]\n\t" "ds_read_b64_tr_b16 a[154:155], %[vr5] offset:17952\n\t" "v_fma_f32 v101, v101, v21, v21\n\t"
    "v_fma_f32 v102, v102, v21, v21\n\t" "v_mfma_f32_16x16x32_bf16 v[232:235], a[156:159], a[56:59], v[232:235]\n\t" "v_fma_f32 v103, v103, v21, v21\n\t" "v_rcp_f32 v100, v100\n\t"
    "v_mfma_f32_16x16x32_bf16 v[236:239], a[156:159], a[60:63], v[236:239]\n\t" "v_rcp_f32 v101, v101\n\t" "s_waitcnt lgkmcnt(12)\n\t"
    "v_mfma_f32_16x16x32_bf16 v[112:115], a[128:131], a[64:67], v[112:115]\n\t" "ds_read_b64_tr_b16 a[156:157], %[vr5] offset:21536\n\t" "v_rcp_f32 v102, v102\n\t" "v_rcp_f32 v103, v103\n\t"
    "v_mfma_f32_16x16x32_bf16 v[116:119], a[128:131], a[68:71], v[116:119]\n\t" "ds_read_b64_tr_b16 a[158:159], %[vr5] offset:25120\n\t" "v_mul_f32 v212, v212, v100\n\t" "v_mul_f32 v213, v213, v101\n\t"
    "v_mfma_f32_16x16x32_bf16 v[120:123], a[128:131], a[72:75], v[120:123]\n\t" "v_mul_f32 v214, v214, v102\n\t" "v_mul_f32 v215, v215, v103\n\t"
    "v_mfma_f32_16x16x32_bf16 v[124:127], a[128:131], a[76:79], v[124:127]\n\t" "v_cvt_pk_bf16_f32 v100, v212, v213\n\t" "v_cvt_pk_bf16_f32 v101, v214, v215\n\t" "s_waitcnt lgkmcnt(12)\n\t"
    "v_mfma_f32_16x16x32_bf16 v[224:227], a[132:135], a[64:67], v[224:227]\n\t" "v_accvgpr_read_b32 v31, %[ct]\n\t" "s_mov_b64 exec, 0xffff\n\t" "v_add_u32 v31, %[gt3], v31\n\t" "s_mov_b64 exec, -1\n\t"
    "ds_read_b128 a[128:131], v31 offset:0\n\t" "v_mul_f32 v104, v104, v18\n\t" "v_mul_f32 v105, v105, v18\n\t" "v_mul_f32 v106, v106, v18\n\t"
    "v_mfma_f32_16x16x32_bf16 v[228:231], a[132:135], a[68:71], v[228:231]\n\t" "v_mul_f32 v107, v107, v18\n\t" "v_exp_f32 v104, v104\n\t"
    "v_mfma_f32_16x16x32_bf16 v[232:235], a[132:135], a[72:75], v[232:235]\n\t" "v_exp_f32 v105, v105\n\t" "v_mfma_f32_16x16x32_bf16 v[236:239], a[132:135], a[76:79], v[236:239]\n\t"
    "v_exp_f32 v106, v106\n\t" "s_waitcnt lgkmcnt(11)\n\t" "v_mfma_f32_16x16x32_bf16 v[112:115], a[136:139], a[80:83], v[112:115]\n\t" "ds_read_b128 a[132:135], v31 offset:256\n\t"
    "v_exp_f32 v107, v107\n\t" "v_fma_f32 v104, v104, v22, v22\n\t" "v_mfma_f32_16x16x32_bf16 v[116:119], a[136:139], a[84:87], v[116:119]\n\t" "v_fma_f32 v105, v105, v22, v22\n\t"
    "v_fma_f32 v106, v106, v22, v22\n\t" "v_mfma_f32_16x16x32_bf16 v[120:123], a[136:139], a[88:91], v[120:123]\n\t" "v_fma_f32 v107, v107, v22, v22\n\t" "v_rcp_f32 v104, v104\n\t"
    "v_mfma_f32_16x16x32_bf16 v[124:127], a[136:139], a[92:95], v[124:127]\n\t" "v_rcp_f32 v105, v105\n\t" "s_waitcnt lgkmcnt(10)\n\t"
    "v_mfma_f32_16x16x32_bf16 v[224:227], a[140:143], a[80:83], v[224:227]\n\t" "ds_read_b64_tr_b16 a[136:137], %[vr6] offset:0\n\t" "v_rcp_f32 v106, v106\n\t" "v_rcp_f32 v107, v107\n\t"
    "v_mfma_f32_16x16x32_bf16 v[228:231], a[140:143], a[84:87], v[228:231]\n\t" "ds_read_b64_tr_b16 a[138:139], %[vr6] offset:3584\n\t" "v_mul_f32 v216, v216, v104\n\t" "v_mul_f32 v217, v217, v105\n\t"
    "v_mfma_f32_16x16x32_bf16 v[232:235], a[140:143], a[88:91], v[232:235]\n\t" "v_mul_f32 v218, v218, v106\n\t" "v_mul_f32 v219, v219, v107\n\t"
    "v_mfma_f32_16x16x32_bf16 v[236:239], a[140:143], a[92:95], v[236:239]\n\t" "v_cvt_pk_bf16_f32 v104, v216, v217\n\t" "v_cvt_pk_bf16_f32 v105, v218, v219\n\t" "s_waitcnt lgkmcnt(10)\n\t"
    "v_mfma_f32_16x16x32_bf16 v[112:115], a[144:147], a[96:99], v[112:115]\n\t" "ds_read_b64_tr_b16 a[140:141], %[vr6] offset:7168\n\t" "v_mul_f32 v108, v108, v19\n\t" "v_mul_f32 v109, v109, v19\n\t"
    "v_mul_f32 v110, v110, v19\n\t" "v_mfma_f32_16x16x32_bf16 v[116:119], a[144:147], a[100:103], v[116:119]\n\t" "ds_read_b64_tr_b16 a[142:143], %[vr6] offset:10752\n\t" "v_mul_f32 v111, v111, v19\n\t"
    "v_exp_f32 v108, v108\n\t" "v_mfma_f32_16x16x32_bf16 v[120:123], a[144:147], a[104:107], v[120:123]\n\t" "v_exp_f32 v109, v109\n\t"
    "v_mfma_f32_16x16x32_bf16 v[124:127], a[144:147], a[108:111], v[124:127]\n\t" "v_exp_f32 v110, v110\n\t" "s_waitcnt lgkmcnt(10)\n\t"
    "v_mfma_f32_16x16x32_bf16 v[224:227], a[148:151], a[96:99], v[224:227]\n\t" "ds_read_b64_tr_b16 a[144:145], %[vr6] offset:14336\n\t" "v_exp_f32 v111, v111\n\t" "v_fma_f32 v108, v108, v23, v23\n\t"
    "v_mfma_f32_16x16x32_bf16 v[228:231], a[148:151], a[100:103], v[228:231]\n\t" "ds_read_b64_tr_b16 a[146:147], %[vr6] offset:17920\n\t" "v_fma_f32 v109, v109, v23, v23\n\t"
    "v_fma_f32 v110, v110, v23, v23\n\t" "v_mfma_f32_16x16x32_bf16 v[232:235], a[148:151], a[104:107], v[232:235]\n\t" "v_fma_f32 v111, v111, v23, v23\n\t" "v_rcp_f32 v108, v108\n\t"
    "v_mfma_f32_16x16x32_bf16 v[236:239], a[148:151], a[108:111], v[236:239]\n\t" "v_rcp_f32 v109, v109\n\t" "s_waitcnt lgkmcnt(10)\n\t"
    "v_mfma_f32_16x16x32_bf16 v[112:115], a[152:155], a[112:115], v[112:115]\n\t" "ds_read_b64_tr_b16 a[148:149], %[vr6] offset:21504\n\t" "v_rcp_f32 v110, v110\n\t" "v_rcp_f32 v111, v111\n\t"
    "v_mfma_f32_16x16x32_bf16 v[116:119], a[152:155], a[116:119], v[116:119]\n\t" "ds_read_b64_tr_b16 a[150:151], %[vr6] offset:25088\n\t" "v_mul_f32 v220, v220, v108\n\t" "v_mul_f32 v221, v221, v109\n\t"
    "v_mfma_f32_16x16x32_bf16 v[120:123], a[152:155], a[120:123], v[120:123]\n\t" "v_mul_f32 v222, v222, v110\n\t" "v_mul_f32 v223, v223, v111\n\t"
    "v_mfma_f32_16x16x32_bf16 v[124:127], a[152:155], a[124:127], v[124:127]\n\t" "v_cvt_pk_bf16_f32 v108, v220, v221\n\t" "v_cvt_pk_bf16_f32 v109, v222, v223\n\t" "s_waitcnt lgkmcnt(10)\n\t"
    "v_mfma_f32_16x16x32_bf16 v[224:227], a[156:159], a[112:115], v[224:227]\n\t" "ds_read_b64_tr_b16 a[152:153], %[vr6] offset:28672\n\t"
    "v_mfma_f32_16x16x32_bf16 v[228:231], a[156:159], a[116:119], v[228:231]\n\t" "ds_read_b64_tr_b16 a[154:155], %[vr6] offset:32256\n\t"
    "v_mfma_f32_16x16x32_bf16 v[232:235], a[156:159], a[120:123], v[232:235]\n\t" "v_mfma_f32_16x16x32_bf16 v[236:239], a[156:159], a[124:127], v[236:239]\n\t" "s_waitcnt lgkmcnt(11)\n\t"
    "v_mfma_f32_16x16x32_bf16 v[128:131], a[128:131], a[200:203], v[128:131]\n\t" "ds_read_b64_tr_b16 a[156:157], %[vr6] offset:35840\n\t"
    "v_mfma_f32_16x16x32_bf16 v[132:135], a[128:131], a[204:207], v[132:135]\n\t" "ds_read_b64_tr_b16 a[158:159], %[vr6] offset:39424\n\t"
    "v_mfma_f32_16x16x32_bf16 v[136:139], a[128:131], a[208:211], v[136:139]\n\t" "v_mfma_f32_16x16x32_bf16 v[140:143], a[128:131], a[212:215], v[140:143]\n\t" "s_waitcnt lgkmcnt(12)\n\t"
    "v_mfma_f32_16x16x32_bf16 v[240:243], a[132:135], a[200:203], v[240:243]\n\t" "ds_read_b64_tr_b16 a[128:129], %[vr6] offset:43008\n\t" "s_add_u32 s90, %[shl], %[go2]\n\t"
    "s_addc_u32 s91, %[shh], 0\n\t" "s_add_u32 s92, s90, 229376\n\t" "s_addc_u32 s93, s91, 0\n\t" "v_mul_f32 v112, v112, v16\n\t"
    "v_mfma_f32_16x16x32_bf16 v[244:247], a[132:135], a[204:207], v[244:247]\n\t" "ds_read_b64_tr_b16 a[130:131], %[vr6] offset:46592\n\t" "v_mul_f32 v113, v113, v16\n\t" "v_mul_f32 v114, v114, v16\n\t"
    "v_mfma_f32_16x16x32_bf16 v[248:251], a[132:135], a[208:211], v[248:251]\n\t" "v_mul_f32 v115, v115, v16\n\t" "v_exp_f32 v112, v112\n\t"
    "v_mfma_f32_16x16x32_bf16 v[252:255], a[132:135], a[212:215], v[252:255]\n\t" "v_exp_f32 v113, v113\n\t" "s_waitcnt lgkmcnt(12)\n\t"
    "v_mfma_f32_16x16x32_bf16 v[128:131], a[136:139], a[32:35], v[128:131]\n\t" "ds_read_b64_tr_b16 a[132:133], %[vr6] offset:50176\n\t" "v_exp_f32 v114, v114\n\t" "v_exp_f32 v115, v115\n\t"
    "v_mfma_f32_16x16x32_bf16 v[132:135], a[136:139], a[36:39], v[132:135]\n\t" "ds_read_b64_tr_b16 a[134:135], %[vr6] offset:53760\n\t" "v_fma_f32 v112, v112, v20, v20\n\t"
    "v_fma_f32 v113, v113, v20, v20\n\t" "v_mfma_f32_16x16x32_bf16 v[136:139], a[136:139], a[40:43], v[136:139]\n\t" "v_fma_f32 v114, v114, v20, v20\n\t" "v_fma_f32 v115, v115, v20, v20\n\t"
    "v_mfma_f32_16x16x32_bf16 v[140:143], a[136:139], a[44:47], v[140:143]\n\t" "v_rcp_f32 v112, v112\n\t" "s_waitcnt lgkmcnt(12)\n\t"
    "v_mfma_f32_16x16x32_bf16 v[240:243], a[140:143], a[32:35], v[240:243]\n\t" "ds_read_b64_tr_b16 a[136:137], %[vr7] offset:0\n\t" "v_rcp_f32 v113, v113\n\t" "v_rcp_f32 v114, v114\n\t"
    "v_mfma_f32_16x16x32_bf16 v[244:247], a[140:143], a[36:39], v[244:247]\n\t" "ds_read_b64_tr_b16 a[138:139], %[vr7] offset:3584\n\t" "v_rcp_f32 v115, v115\n\t"
    "v_mfma_f32_16x16x32_bf16 v[248:251], a[140:143], a[40:43], v[248:251]\n\t" "v_mul_f32 v224, v224, v112\n\t" "v_mul_f32 v225, v225, v113\n\t"
    "v_mfma_f32_16x16x32_bf16 v[252:255], a[140:143], a[44:47], v[252:255]\n\t" "v_mul_f32 v226, v226, v114\n\t" "v_mul_f32 v227, v227, v115\n\t" "s_waitcnt lgkmcnt(12)\n\t"
    "v_mfma_f32_16x16x32_bf16 v[128:131], a[144:147], a[48:51], v[128:131]\n\t" "ds_read_b64_tr_b16 a[140:141], %[vr7] offset:7168\n\t" "v_cvt_pk_bf16_f32 v98, v224, v225\n\t"
    "v_cvt_pk_bf16_f32 v99, v226, v227\n\t" "global_store_dwordx4 %[va], v[96:99], s[90:91] offset:0 sc1\n\t" "v_mfma_f32_16x16x32_bf16 v[132:135], a[144:147], a[52:55], v[132:135]\n\t"
    "ds_read_b64_tr_b16 a[142:143], %[vr7] offset:10752\n\t" "v_mul_f32 v116, v116, v17\n\t" "v_mul_f32 v117, v117, v17\n\t" "v_mfma_f32_16x16x32_bf16 v[136:139], a[144:147], a[56:59], v[136:139]\n\t"
    "v_mul_f32 v118, v118, v17\n\t" "v_mul_f32 v119, v119, v17\n\t" "v_mfma_f32_16x16x32_bf16 v[140:143], a[144:147], a[60:63], v[140:143]\n\t" "v_exp_f32 v116, v116\n\t" "s_waitcnt lgkmcnt(12)\n\t"
    "v_mfma_f32_16x16x32_bf16 v[240:243], a[148:151], a[48:51], v[240:243]\n\t" "ds_read_b64_tr_b16 a[144:145], %[vr7] offset:14336\n\t" "v_exp_f32 v117, v117\n\t" "v_exp_f32 v118, v118\n\t"
    "v_mfma_f32_16x16x32_bf16 v[244:247], a[148:151], a[52:55], v[244:247]\n\t" "ds_read_b64_tr_b16 a[146:147], %[vr7] offset:17920\n\t" "v_exp_f32 v119, v119\n\t"
    "v_mfma_f32_16x16x32_bf16 v[248:251], a[148:151], a[56:59], v[248:251]\n\t" "v_fma_f32 v116, v116, v21, v21\n\t" "v_fma_f32 v117, v117, v21, v21\n\t"
    "v_mfma_f32_16x16x32_bf16 v[252:255], a[148:151], a[60:63], v[252:255]\n\t" "v_fma_f32 v118, v118, v21, v21\n\t" "v_fma_f32 v119, v119, v21, v21\n\t" "s_waitcnt lgkmcnt(12)\n\t"
    "v_mfma_f32_16x16x32_bf16 v[128:131], a[152:155], a[64:67], v[128:131]\n\t" "ds_read_b64_tr_b16 a[148:149], %[vr7] offset:21504\n\t" "v_rcp_f32 v116, v116\n\t" "v_rcp_f32 v117, v117\n\t"
    "v_mfma_f32_16x16x32_bf16 v[132:135], a[152:155], a[68:71], v[132:135]\n\t" "ds_read_b64_tr_b16 a[150:151], %[vr7] offset:25088\n\t" "v_rcp_f32 v118, v118\n\t"
    "v_mfma_f32_16x16x32_bf16 v[136:139], a[152:155], a[72:75], v[136:139]\n\t" "v_rcp_f32 v119, v119\n\t" "v_mfma_f32_16x16x32_bf16 v[140:143], a[152:155], a[76:79], v[140:143]\n\t"
    "v_mul_f32 v228, v228, v116\n\t" "v_mul_f32 v229, v229, v117\n\t" "s_waitcnt lgkmcnt(12)\n\t" "v_mfma_f32_16x16x32_bf16 v[240:243], a[156:159], a[64:67], v[240:243]\n\t"
    "v_mul_f32 v230, v230, v118\n\t" "v_mul_f32 v231, v231, v119\n\t" "v_cvt_pk_bf16_f32 v102, v228, v229\n\t" "v_mfma_f32_16x16x32_bf16 v[244:247], a[156:159], a[68:71], v[244:247]\n\t"
    "v_cvt_pk_bf16_f32 v103, v230, v231\n\t" "global_store_dwordx4 %[va], v[100:103], s[90:91] offset:1024 sc1\n\t" "v_mfma_f32_16x16x32_bf16 v[248:251], a[156:159], a[72:75], v[248:251]\n\t"
    "v_mul_f32 v120, v120, v18\n\t" "v_mul_f32 v121, v121, v18\n\t" "v_mfma_f32_16x16x32_bf16 v[252:255], a[156:159], a[76:79], v[252:255]\n\t" "v_mul_f32 v122, v122, v18\n\t"
    "v_mul_f32 v123, v123, v18\n\t" "s_waitcnt lgkmcnt(10)\n\t" "v_mfma_f32_16x16x32_bf16 v[128:131], a[128:131], a[80:83], v[128:131]\n\t" "v_exp_f32 v120, v120\n\t" "v_exp_f32 v121, v121\n\t"
    "v_mfma_f32_16x16x32_bf16 v[132:135], a[128:131], a[84:87], v[132:135]\n\t" "v_exp_f32 v122, v122\n\t" "v_mfma_f32_16x16x32_bf16 v[136:139], a[128:131], a[88:91], v[136:139]\n\t"
    "v_exp_f32 v123, v123\n\t" "v_mfma_f32_16x16x32_bf16 v[140:143], a[128:131], a[92:95], v[140:143]\n\t" "v_fma_f32 v120, v120, v22, v22\n\t" "v_fma_f32 v121, v121, v22, v22\n\t"
    "s_waitcnt lgkmcnt(8)\n\t" "v_mfma_f32_16x16x32_bf16 v[240:243], a[132:135], a[80:83], v[240:243]\n\t" "v_fma_f32 v122, v122, v22, v22\n\t" "v_fma_f32 v123, v123, v22, v22\n\t"
    "v_rcp_f32 v120, v120\n\t" "v_mfma_f32_16x16x32_bf16 v[244:247], a[132:135], a[84:87], v[244:247]\n\t" "v_rcp_f32 v121, v121\n\t"
    "v_mfma_f32_16x16x32_bf16 v[248:251], a[132:135], a[88:91], v[248:251]\n\t" "v_rcp_f32 v122, v122\n\t" "v_mfma_f32_16x16x32_bf16 v[252:255], a[132:135], a[92:95], v[252:255]\n\t"
    "v_rcp_f32 v123, v123\n\t" "s_waitcnt lgkmcnt(6)\n\t" "v_mfma_f32_16x16x32_bf16 v[128:131], a[136:139], a[96:99], v[128:131]\n\t" "v_mul_f32 v232, v232, v120\n\t" "v_mul_f32 v233, v233, v121\n\t"
    "v_mul_f32 v234, v234, v122\n\t" "v_mfma_f32_16x16x32_bf16 v[132:135], a[136:139], a[100:103], v[132:135]\n\t" "v_mul_f32 v235, v235, v123\n\t" "v_cvt_pk_bf16_f32 v106, v232, v233\n\t"
    "v_mfma_f32_16x16x32_bf16 v[136:139], a[136:139], a[104:107], v[136:139]\n\t" "v_cvt_pk_bf16_f32 v107, v234, v235\n\t" "global_store_dwordx4 %[va], v[104:107], s[92:93] offset:0 sc1\n\t"
    "v_mfma_f32_16x16x32_bf16 v[140:143], a[136:139], a[108:111], v[140:143]\n\t" "v_mul_f32 v124, v124, v19\n\t" "v_mul_f32 v125, v125, v19\n\t" "s_waitcnt lgkmcnt(4)\n\t"
    "v_mfma_f32_16x16x32_bf16 v[240:243], a[140:143], a[96:99], v[240:243]\n\t" "v_mul_f32 v126, v126, v19\n\t" "v_mul_f32 v127, v127, v19\n\t" "v_exp_f32 v124, v124\n\t"
    "v_mfma_f32_16x16x32_bf16 v[244:247], a[140:143], a[100:103], v[244:247]\n\t" "v_exp_f32 v125, v125\n\t" "v_mfma_f32_16x16x32_bf16 v[248:251], a[140:143], a[104:107], v[248:251]\n\t"
    "v_exp_f32 v126, v126\n\t" "v_mfma_f32_16x16x32_bf16 v[252:255], a[140:143], a[108:111], v[252:255]\n\t" "v_exp_f32 v127, v127\n\t" "s_waitcnt lgkmcnt(2)\n\t"
    "v_mfma_f32_16x16x32_bf16 v[128:131], a[144:147], a[112:115], v[128:131]\n\t" "v_fma_f32 v124, v124, v23, v23\n\t" "v_fma_f32 v125, v125, v23, v23\n\t" "v_fma_f32 v126, v126, v23, v23\n\t"
    "v_mfma_f32_16x16x32_bf16 v[132:135], a[144:147], a[116:119], v[132:135]\n\t" "v_fma_f32 v127, v127, v23, v23\n\t" "v_rcp_f32 v124, v124\n\t"
    "v_mfma_f32_16x16x32_bf16 v[136:139], a[144:147], a[120:123], v[136:139]\n\t" "v_rcp_f32 v125, v125\n\t" "v_mfma_f32_16x16x32_bf16 v[140:143], a[144:147], a[124:127], v[140:143]\n\t"
    "v_rcp_f32 v126, v126\n\t" "s_waitcnt lgkmcnt(0)\n\t" "v_mfma_f32_16x16x32_bf16 v[240:243], a[148:151], a[112:115], v[240:243]\n\t" "v_rcp_f32 v127, v127\n\t" "v_mul_f32 v236, v236, v124\n\t"
    "v_mfma_f32_16x16x32_bf16 v[244:247], a[148:151], a[116:119], v[244:247]\n\t" "v_mul_f32 v237, v237, v125\n\t" "v_mul_f32 v238, v238, v126\n\t"
    "v_mfma_f32_16x16x32_bf16 v[248:251], a[148:151], a[120:123], v[248:251]\n\t" "v_mul_f32 v239, v239, v127\n\t" "v_cvt_pk_bf16_f32 v110, v236, v237\n\t"
    "v_mfma_f32_16x16x32_bf16 v[252:255], a[148:151], a[124:127], v[252:255]\n\t" "v_cvt_pk_bf16_f32 v111, v238, v239\n\t" "global_store_dwordx4 %[va], v[108:111], s[92:93] offset:1024 sc1\n\t"
    "s_nop 7\n\t" "s_add_u32 s90, %[shl], %[go3]\n\t" "s_addc_u32 s91, %[shh], 0\n\t" "s_add_u32 s92, s90, 229376\n\t" "s_addc_u32 s93, s91, 0\n\t" "v_mul_f32 v128, v128, v16\n\t"
    "v_mul_f32 v129, v129, v16\n\t" "v_mul_f32 v130, v130, v16\n\t" "v_mul_f32 v131, v131, v16\n\t" "v_exp_f32 v128, v128\n\t" "v_exp_f32 v129, v129\n\t" "v_exp_f32 v130, v130\n\t"
    "v_exp_f32 v131, v131\n\t" "v_fma_f32 v128, v128, v20, v20\n\t" "v_fma_f32 v129, v129, v20, v20\n\t" "v_fma_f32 v130, v130, v20, v20\n\t" "v_fma_f32 v131, v131, v20, v20\n\t"
    "v_rcp_f32 v128, v128\n\t" "v_rcp_f32 v129, v129\n\t" "v_rcp_f32 v130, v130\n\t" "v_rcp_f32 v131, v131\n\t" "v_mul_f32 v240, v240, v128\n\t" "v_mul_f32 v241, v241, v129\n\t"
    "v_mul_f32 v242, v242, v130\n\t" "v_mul_f32 v243, v243, v131\n\t" "v_cvt_pk_bf16_f32 v128, v240, v241\n\t" "v_cvt_pk_bf16_f32 v129, v242, v243\n\t" "v_lshrrev_b32 v31, 1, %[va]\n\t"
    "global_store_dwordx2 v31, v[128:129], s[90:91] offset:0 sc1\n\t" "v_mul_f32 v132, v132, v17\n\t" "v_mul_f32 v133, v133, v17\n\t" "v_mul_f32 v134, v134, v17\n\t" "v_mul_f32 v135, v135, v17\n\t"
    "v_exp_f32 v132, v132\n\t" "v_exp_f32 v133, v133\n\t" "v_exp_f32 v134, v134\n\t" "v_exp_f32 v135, v135\n\t" "v_fma_f32 v132, v132, v21, v21\n\t" "v_fma_f32 v133, v133, v21, v21\n\t"
    "v_fma_f32 v134, v134, v21, v21\n\t" "v_fma_f32 v135, v135, v21, v21\n\t" "v_rcp_f32 v132, v132\n\t" "v_rcp_f32 v133, v133\n\t" "v_rcp_f32 v134, v134\n\t" "v_rcp_f32 v135, v135\n\t"
    "v_mul_f32 v244, v244, v132\n\t" "v_mul_f32 v245, v245, v133\n\t" "v_mul_f32 v246, v246, v134\n\t" "v_mul_f32 v247, v247, v135\n\t" "v_cvt_pk_bf16_f32 v132, v244, v245\n\t"
    "v_cvt_pk_bf16_f32 v133, v246, v247\n\t" "global_store_dwordx2 v31, v[132:133], s[90:91] offset:512 sc1\n\t" "v_mul_f32 v136, v136, v18\n\t" "v_mul_f32 v137, v137, v18\n\t"
    "v_mul_f32 v138, v138, v18\n\t" "v_mul_f32 v139, v139, v18\n\t" "v_exp_f32 v136, v136\n\t" "v_exp_f32 v137, v137\n\t" "v_exp_f32 v138, v138\n\t" "v_exp_f32 v139, v139\n\t"
    "v_fma_f32 v136, v136, v22, v22\n\t" "v_fma_f32 v137, v137, v22, v22\n\t" "v_fma_f32 v138, v138, v22, v22\n\t" "v_fma_f32 v139, v139, v22, v22\n\t" "v_rcp_f32 v136, v136\n\t"
    "v_rcp_f32 v137, v137\n\t" "v_rcp_f32 v138, v138\n\t" "v_rcp_f32 v139, v139\n\t" "v_mul_f32 v248, v248, v136\n\t" "v_mul_f32 v249, v249, v137\n\t" "v_mul_f32 v250, v250, v138\n\t"
    "v_mul_f32 v251, v251, v139\n\t" "v_cvt_pk_bf16_f32 v136, v248, v249\n\t" "v_cvt_pk_bf16_f32 v137, v250, v251\n\t" "global_store_dwordx2 v31, v[136:137], s[92:93] offset:0 sc1\n\t"
    "v_mul_f32 v140, v140, v19\n\t" "v_mul_f32 v141, v141, v19\n\t" "v_mul_f32 v142, v142, v19\n\t" "v_mul_f32 v143, v143, v19\n\t" "v_exp_f32 v140, v140\n\t" "v_exp_f32 v141, v141\n\t"
    "v_exp_f32 v142, v142\n\t" "v_exp_f32 v143, v143\n\t" "v_fma_f32 v140, v140, v23, v23\n\t" "v_fma_f32 v141, v141, v23, v23\n\t" "v_fma_f32 v142, v142, v23, v23\n\t"
    "v_fma_f32 v143, v143, v23, v23\n\t" "v_rcp_f32 v140, v140\n\t" "v_rcp_f32 v141, v141\n\t" "v_rcp_f32 v142, v142\n\t" "v_rcp_f32 v143, v143\n\t" "v_mul_f32 v252, v252, v140\n\t"
    "v_mul_f32 v253, v253, v141\n\t" "v_mul_f32 v254, v254, v142\n\t" "v_mul_f32 v255, v255, v143\n\t" "v_cvt_pk_bf16_f32 v140, v252, v253\n\t" "v_cvt_pk_bf16_f32 v141, v254, v255\n\t"
    "global_store_dwordx2 v31, v[140:141], s[92:93] offset:512 sc1\n\t" "v_mov_b32 v28, 0\n\t" "v_mov_b64 v[30:31], %[tag]\n\t" "s_waitcnt vmcnt(0)\n\t" "s_barrier\n\t"
    "s_cmp_eq_u32 %[sld], %[sl0]\n\t" "s_cselect_b64 exec, 1, 0\n\t" "global_store_dwordx2 v28, v[30:31], %[sc] sc1\n\t" "s_mov_b64 exec, -1\n\t"
    :
    : [va] "{v0}"(va), [ve] "{a248}"(ve), [vb0] "{v1}"(vb[0]), [vb1] "{v2}"(vb[1]), [vb2] "{v3}"(vb[2]), [vb3] "{v4}"(vb[3]), [vr0] "{v5}"(vr[0]), [vr1] "{v6}"(vr[1]), [vr2] "{v7}"(vr[2]), [vr3] "{v8}"(vr[3]), [vr4] "{v9}"(vr[4]), [vr5] "{v10}"(vr[5]), [vr6] "{v11}"(vr[6]), [vr7] "{v12}"(vr[7]), [gt0] "s"(gt[0]), [go0] "s"(go[0]), [gt1] "s"(gt[1]), [go1] "s"(go[1]), [gt2] "s"(gt[2]), [go2] "s"(go[2]), [gt3] "s"(gt[3]), [go3] "s"(go[3]), [ct] "{a249}"(ct), [cr0] "{a250}"(cr0), [cr1] "{a251}"(cr1), [cw] "{a252}"(cw), [cp] "{a253}"(vp), [vs] "{a254}"(vs), [vf] "{v13}"(vf), [sq] "s"(sq), [tag] "s"(tag), [sa] "s"(sa), [sb] "s"(sb + 688128), [se] "s"(se), [ss] "s"(ss), [sl] "s"(sl), [su0] "s"(su0), [su6] "s"(su6), [sr] "s"(sr), [su] "s"(su), [srl] "s"(srl), [rb] "s"(ldsb + RAW), [shl] "s"(shl), [shh] "s"(shh), [wa] "s"(wa), [wb] "s"(wb), [e0] "s"(e0), [e1] "s"(e1), [m01] "s"(m01), [m6] "s"(m6), [tp0] "s"(tp0), [vbd] "{a255}"(vbd), [sbd] "s"(sbd), [sld] "s"(sld), [sl0] "s"(ldsb), [sc] "s"(scnt)
    : "v16", "v17", "v18", "v19", "v20", "v21", "v22", "v23", "v24", "v25", "v26", "v27", "v28", "v29", "v30", "v31", "v32", "v33", "v34", "v35", "v36", "v37", "v38", "v39", "v40", "v41", "v42", "v43", "v44", "v45", "v46", "v47", "v48", "v49", "v50", "v51", "v52", "v53", "v54", "v55", "v56", "v57", "v58", "v59", "v60", "v61", "v62", "v63", "v64", "v65", "v66", "v67", "v68", "v69", "v70", "v71", "v72", "v73", "v74", "v75", "v76", "v77", "v78", "v79", "v80", "v81", "v82", "v83", "v84", "v85", "v86", "v87", "v88", "v89", "v90", "v91", "v92", "v93", "v94", "v95", "v96", "v97", "v98", "v99", "v100", "v101", "v102", "v103", "v104", "v105", "v106", "v107", "v108", "v109", "v110", "v111", "v112", "v113", "v114", "v115", "v116", "v117", "v118", "v119", "v120", "v121", "v122", "v123", "v124", "v125", "v126", "v127", "v128", "v129", "v130", "v131", "v132", "v133", "v134", "v135", "v136", "v137", "v138", "v139", "v140", "v141", "v142", "v143", "v144", "v145", "v146", "v147", "v148", "v149", "v150", "v151", "v152", "v153", "v154", "v155", "v156", "v157", "v158", "v159", "v160", "v161", "v162", "v163", "v164", "v165", "v166", "v167", "v168", "v169", "v170", "v171", "v172", "v173", "v174", "v175", "v176", "v177", "v178", "v179", "v180", "v181", "v182", "v183", "v184", "v185", "v186", "v187", "v188", "v189", "v190", "v191", "v192", "v193", "v194", "v195", "v196", "v197", "v198", "v199", "v200", "v201", "v202", "v203", "v204", "v205", "v206", "v207", "v208", "v209", "v210", "v211", "v212", "v213", "v214", "v215", "v216", "v217", "v218", "v219", "v220", "v221", "v222", "v223", "v224", "v225", "v226", "v227", "v228", "v229", "v230", "v231", "v232", "v233", "v234", "v235", "v236", "v237", "v238", "v239", "v240", "v241", "v242", "v243", "v244", "v245", "v246", "v247", "v248", "v249", "v250", "v251", "v252", "v253", "v254", "v255", "vcc", "a0", "a1", "a2", "a3", "a4", "a5", "a6", "a7", "a8", "a9", "a10", "a11", "a12", "a13", "a14", "a15", "a16", "a17", "a18", "a19", "a20", "a21", "a22", "a23", "a24", "a25", "a26", "a27", "a28", "a29", "a30", "a31", "a32", "a33", "a34", "a35", "a36", "a37", "a38", "a39", "a40", "a41", "a42", "a43", "a44", "a45", "a46", "a47", "a48", "a49", "a50", "a51", "a52", "a53", "a54", "a55", "a56", "a57", "a58", "a59", "a60", "a61", "a62", "a63", "a64", "a65", "a66", "a67", "a68", "a69", "a70", "a71", "a72", "a73", "a74", "a75", "a76", "a77", "a78", "a79", "a80", "a81", "a82", "a83", "a84", "a85", "a86", "a87", "a88", "a89", "a90", "a91", "a92", "a93", "a94", "a95", "a96", "a97", "a98", "a99", "a100", "a101", "a102", "a103", "a104", "a105", "a106", "a107", "a108", "a109", "a110", "a111", "a112", "a113", "a114", "a115", "a116", "a117", "a118", "a119", "a120", "a121", "a122", "a123", "a124", "a125", "a126", "a127", "a128", "a129", "a130", "a131", "a132", "a133", "a134", "a135", "a136", "a137", "a138", "a139", "a140", "a141", "a142", "a143", "a144", "a145", "a146", "a147", "a148", "a149", "a150", "a151", "a152", "a153", "a154", "a155", "a156", "a157", "a158", "a159", "a160", "a161", "a162", "a163", "a164", "a165", "a166", "a167", "a168", "a169", "a170", "a171", "a172", "a173", "a174", "a175", "a176", "a177", "a178", "a179", "a180", "a181", "a182", "a183", "a184", "a185", "a186", "a187", "a188", "a189", "a190", "a191", "a192", "a193", "a194", "a195", "a196", "a197", "a198", "a199", "a200", "a201", "a202", "a203", "a204", "a205", "a206", "a207", "a208", "a209", "a210", "a211", "a212", "a213", "a214", "a215", "a216", "a217", "a218", "a219", "a220", "a221", "a222", "a223", "a224", "a225", "a226", "a227", "a228", "a229", "a230", "a231", "a232", "a233", "a234", "a235", "a236", "a237", "a238", "a239", "a240", "a241", "a242", "a243", "a244", "a245", "a246", "a247", "s76", "s77", "s78", "s79", "s80", "s81", "s82", "s83", "s84", "s85", "s86", "s87", "s88", "s89", "s90", "s91", "s92", "s93", "s94", "s95", "s96", "s97", "s98", "s99", "scc", "memory");
  } }
  {
  asm volatile("" : "+s"(bx), "+s"(wid), "+s"(ws)); asm volatile("" : "+s"(tag));
  const int xcd = bx & 7, loc = bx >> 3, lane = __builtin_amdgcn_mbcnt_hi(~0u, __builtin_amdgcn_mbcnt_lo(~0u, 0u));
  const int kh = xcd & 1, rb = (xcd >> 1) * 4 + (loc & 3), cb = loc >> 2, fr = lane & 15, fq = lane >> 4;
  u32x4 bv[4], rx[4]; f32x4 gv[4], acc[8]; unsigned long long fl;
  {
    const unsigned vx = lane * 16, vo = lane * 8, vg = fq * 16, vf = fr * 64, sl0 = ldsb + wid * 1024 + (kh ^ 1) * 4096, sl1 = sl0 ^ 4096; unsigned vb0 = f_vb(wid, lane) + (kh ^ 1) * 128, vb1 = vb0 ^ 128, vr[24];
#pragma unroll
    for (int j = 0; j < 12; ++j) { vr[j] = ldsb + f_vr(lane, j & 3) + (kh ^ 1) * 4096 + (j >> 2) * 65536; vr[12 + j] = vr[j] ^ 4096; }
    const char* sw = ws + W_TAGS + (HF + ((rb >> 1) * 32 + kh * 16) * 8) * 8;
    const char* sa = ws + W_HH + ((size_t)(rb * 4 + wid) * 112 + kh * 56) * 2048; const char* sb = (const char*)(pfw + (size_t)kh * 1792 * D_ + cb * 128);
    const char* sx = ws + W_X1 + (bx * 4 + wid) * 4096; const char* sg = ws + W_GB + bx * 256;
    const char* sm = ws + W_XCH + (bx * 4 + wid) * 4096; const char* st = ws + W_XCH + ((bx ^ 1) * 4 + wid) * 4096; const char* sf = ws + W_FLAGS + (bx * 4 + wid) * 128; const char* sq = ws + W_FLAGS + ((bx ^ 1) * 4 + wid) * 128;
    asm volatile("s_mov_b64 s[40:41], %[sa]\n\t" "s_add_u32 m0, %[sl0], 49152\n\t" "s_nop 0\n\t" "global_load_lds_dwordx4 %[vb0], %[sb]\n\t" "v_add_u32 %[vb0], 0x10000, %[vb0]\n\t" "s_add_u32 m0, %[sl1], 49152\n\t"
    "s_nop 0\n\t" "global_load_lds_dwordx4 %[vb1], %[sb]\n\t" "v_add_u32 %[vb1], 0x10000, %[vb1]\n\t" "s_add_u32 m0, %[sl0], 57344\n\t" "s_nop 0\n\t" "global_load_lds_dwordx4 %[vb0], %[sb]\n\t"
    "v_add_u32 %[vb0], 0x10000, %[vb0]\n\t" "s_add_u32 m0, %[sl1], 57344\n\t" "s_nop 0\n\t" "global_load_lds_dwordx4 %[vb1], %[sb]\n\t" "v_add_u32 %[vb1], 0x10000, %[vb1]\n\t"
    "s_add_u32 m0, %[sl0], 65536\n\t" "s_nop 0\n\t" "global_load_lds_dwordx4 %[vb0], %[sb]\n\t" "v_add_u32 %[vb0], 0x10000, %[vb0]\n\t" "s_add_u32 m0, %[sl1], 65536\n\t" "s_nop 0\n\t"
    "global_load_lds_dwordx4 %[vb1], %[sb]\n\t" "v_add_u32 %[vb1], 0x10000, %[vb1]\n\t" "s_add_u32 m0, %[sl0], 73728\n\t" "s_nop 0\n\t" "global_load_lds_dwordx4 %[vb0], %[sb]\n\t"
    "v_add_u32 %[vb0], 0x10000, %[vb0]\n\t" "s_add_u32 m0, %[sl1], 73728\n\t" "s_nop 0\n\t" "global_load_lds_dwordx4 %[vb1], %[sb]\n\t" "v_add_u32 %[vb1], 0x10000, %[vb1]\n\t"
    "s_add_u32 m0, %[sl0], 81920\n\t" "s_nop 0\n\t" "global_load_lds_dwordx4 %[vb0], %[sb]\n\t" "v_add_u32 %[vb0], 0x10000, %[vb0]\n\t" "s_add_u32 m0, %[sl1], 81920\n\t" "s_nop 0\n\t"
    "global_load_lds_dwordx4 %[vb1], %[sb]\n\t" "v_add_u32 %[vb1], 0x10000, %[vb1]\n\t" "s_add_u32 m0, %[sl0], 90112\n\t" "s_nop 0\n\t" "global_load_lds_dwordx4 %[vb0], %[sb]\n\t"
    "v_add_u32 %[vb0], 0x10000, %[vb0]\n\t" "s_add_u32 m0, %[sl1], 90112\n\t" "s_nop 0\n\t" "global_load_lds_dwordx4 %[vb1], %[sb]\n\t" "v_add_u32 %[vb1], 0x10000, %[vb1]\n\t"
    "s_add_u32 m0, %[sl0], 98304\n\t" "s_nop 0\n\t" "global_load_lds_dwordx4 %[vb0], %[sb]\n\t" "v_add_u32 %[vb0], 0x10000, %[vb0]\n\t" "s_add_u32 m0, %[sl1], 98304\n\t" "s_nop 0\n\t"
    "global_load_lds_dwordx4 %[vb1], %[sb]\n\t" "v_add_u32 %[vb1], 0x10000, %[vb1]\n\t" "s_add_u32 m0, %[sl0], 106496\n\t" "s_nop 0\n\t" "global_load_lds_dwordx4 %[vb0], %[sb]\n\t"
    "v_add_u32 %[vb0], 0x10000, %[vb0]\n\t" "s_add_u32 m0, %[sl1], 106496\n\t" "s_nop 0\n\t" "global_load_lds_dwordx4 %[vb1], %[sb]\n\t" "v_add_u32 %[vb1], 0x10000, %[vb1]\n\t"
    "ds_read_b64_tr_b16 a[192:193], %[vr0] offset:24576\n\t" "ds_read_b64_tr_b16 a[194:195], %[vr0] offset:26624\n\t" "ds_read_b64_tr_b16 a[196:197], %[vr1] offset:24576\n\t"
    "ds_read_b64_tr_b16 a[198:199], %[vr1] offset:26624\n\t" "ds_read_b64_tr_b16 a[200:201], %[vr2] offset:24576\n\t" "ds_read_b64_tr_b16 a[202:203], %[vr2] offset:26624\n\t"
    "ds_read_b64_tr_b16 a[204:205], %[vr3] offset:24576\n\t" "ds_read_b64_tr_b16 a[206:207], %[vr3] offset:26624\n\t" "s_mov_b32 s44, 0x400000\n\t" ".Lwait_%=:\n\t"
    "global_load_dwordx2 %[fl], %[vf], %[sw] sc1\n\t" "s_waitcnt vmcnt(0)\n\t" "v_cmp_ne_u64 vcc, %[tag], %[fl]\n\t" "s_nop 4\n\t" "s_cbranch_vccz .Lgo_%=\n\t" "s_sleep 1\n\t" "s_sub_u32 s44, s44, 1\n\t"
    "s_cbranch_scc0 .Lwait_%=\n\t" ".Lgo_%=:\n\t" "s_nop 0\n\t" "global_load_dwordx4 a[0:3], %[vx], s[40:41] offset:0 sc1\n\t" "s_nop 0\n\t"
    "global_load_dwordx4 a[4:7], %[vx], s[40:41] offset:1024 sc1\n\t" "s_nop 0\n\t" "global_load_dwordx4 a[8:11], %[vx], s[40:41] offset:2048 sc1\n\t" "s_nop 0\n\t"
    "global_load_dwordx4 a[12:15], %[vx], s[40:41] offset:3072 sc1\n\t" "s_nop 0\n\t" "s_add_u32 s40, s40, 4096\n\t" "s_addc_u32 s41, s41, 0\n\t"
    "global_load_dwordx4 a[16:19], %[vx], s[40:41] offset:0 sc1\n\t" "s_nop 0\n\t" "global_load_dwordx4 a[20:23], %[vx], s[40:41] offset:1024 sc1\n\t" "s_nop 0\n\t"
    "global_load_dwordx2 a[24:25], %[vo], s[40:41] offset:2048 sc1\n\t" "s_nop 0\n\t" "global_load_dwordx2 a[26:27], %[vo], s[40:41] offset:3072 sc1\n\t" "s_nop 0\n\t"
    "global_load_dwordx2 a[28:29], %[vo], s[40:41] offset:2560 sc1\n\t" "s_nop 0\n\t" "global_load_dwordx2 a[30:31], %[vo], s[40:41] offset:3584 sc1\n\t" "s_waitcnt vmcnt(8)\n\t"
    "s_waitcnt lgkmcnt(4)\n\t" "v_mfma_f32_16x16x32_bf16 v[224:227], a[192:195], a[0:3], 0\n\t" "ds_read_b64_tr_b16 a[208:209], %[vr12] offset:24576\n\t"
    "v_mfma_f32_16x16x32_bf16 v[240:243], a[192:195], a[4:7], 0\n\t" "ds_read_b64_tr_b16 a[210:211], %[vr12] offset:26624\n\t" "s_add_u32 s40, s40, 4096\n\t" "s_addc_u32 s41, s41, 0\n\t"
    "global_load_dwordx4 a[32:35], %[vx], s[40:41] offset:0 sc1\n\t" "v_mfma_f32_16x16x32_bf16 v[228:231], a[196:199], a[0:3], 0\n\t" "ds_read_b64_tr_b16 a[212:213], %[vr13] offset:24576\n\t"
    "v_mfma_f32_16x16x32_bf16 v[244:247], a[196:199], a[4:7], 0\n\t" "ds_read_b64_tr_b16 a[214:215], %[vr13] offset:26624\n\t" "s_waitcnt lgkmcnt(4)\n\t"
    "v_mfma_f32_16x16x32_bf16 v[232:235], a[200:203], a[0:3], 0\n\t" "ds_read_b64_tr_b16 a[216:217], %[vr14] offset:24576\n\t" "v_mfma_f32_16x16x32_bf16 v[248:251], a[200:203], a[4:7], 0\n\t"
    "ds_read_b64_tr_b16 a[218:219], %[vr14] offset:26624\n\t" "v_mfma_f32_16x16x32_bf16 v[236:239], a[204:207], a[0:3], 0\n\t" "ds_read_b64_tr_b16 a[220:221], %[vr15] offset:24576\n\t"
    "v_mfma_f32_16x16x32_bf16 v[252:255], a[204:207], a[4:7], 0\n\t" "ds_read_b64_tr_b16 a[222:223], %[vr15] offset:26624\n\t" "s_waitcnt lgkmcnt(4)\n\t"
    "v_mfma_f32_16x16x32_bf16 %0, a[208:211], a[0:3], 0\n\t" "ds_read_b64_tr_b16 a[192:193], %[vr0] offset:32768\n\t" "global_load_dwordx4 a[36:39], %[vx], s[40:41] offset:1024 sc1\n\t"
    "v_mfma_f32_16x16x32_bf16 %1, a[208:211], a[4:7], 0\n\t" "ds_read_b64_tr_b16 a[194:195], %[vr0] offset:34816\n\t" "v_mfma_f32_16x16x32_bf16 %2, a[212:215], a[0:3], 0\n\t"
    "ds_read_b64_tr_b16 a[196:197], %[vr1] offset:32768\n\t" "v_mfma_f32_16x16x32_bf16 %3, a[212:215], a[4:7], 0\n\t" "ds_read_b64_tr_b16 a[198:199], %[vr1] offset:34816\n\t" "s_waitcnt lgkmcnt(4)\n\t"
    "v_mfma_f32_16x16x32_bf16 %4, a[216:219], a[0:3], 0\n\t" "ds_read_b64_tr_b16 a[200:201], %[vr2] offset:32768\n\t" "v_mfma_f32_16x16x32_bf16 %5, a[216:219], a[4:7], 0\n\t"
    "ds_read_b64_tr_b16 a[202:203], %[vr2] offset:34816\n\t" "v_mfma_f32_16x16x32_bf16 %6, a[220:223], a[0:3], 0\n\t" "ds_read_b64_tr_b16 a[204:205], %[vr3] offset:32768\n\t"
    "v_mfma_f32_16x16x32_bf16 %7, a[220:223], a[4:7], 0\n\t" "ds_read_b64_tr_b16 a[206:207], %[vr3] offset:34816\n\t" "s_waitcnt lgkmcnt(4)\n\t" "s_waitcnt vmcnt(8)\n\t"
    "v_mfma_f32_16x16x32_bf16 v[224:227], a[192:195], a[8:11], v[224:227]\n\t" "ds_read_b64_tr_b16 a[208:209], %[vr12] offset:32768\n\t"
    "v_mfma_f32_16x16x32_bf16 v[240:243], a[192:195], a[12:15], v[240:243]\n\t" "ds_read_b64_tr_b16 a[210:211], %[vr12] offset:34816\n\t"
    "global_load_dwordx4 a[40:43], %[vx], s[40:41] offset:2048 sc1\n\t" "v_mfma_f32_16x16x32_bf16 v[228:231], a[196:199], a[8:11], v[228:231]\n\t" "ds_read_b64_tr_b16 a[212:213], %[vr13] offset:32768\n\t"
    "v_mfma_f32_16x16x32_bf16 v[244:247], a[196:199], a[12:15], v[244:247]\n\t" "ds_read_b64_tr_b16 a[214:215], %[vr13] offset:34816\n\t" "s_waitcnt lgkmcnt(4)\n\t"
    "v_mfma_f32_16x16x32_bf16 v[232:235], a[200:203], a[8:11], v[232:235]\n\t" "ds_read_b64_tr_b16 a[216:217], %[vr14] offset:32768\n\t"
    "v_mfma_f32_16x16x32_bf16 v[248:251], a[200:203], a[12:15], v[248:251]\n\t" "ds_read_b64_tr_b16 a[218:219], %[vr14] offset:34816\n\t"
    "v_mfma_f32_16x16x32_bf16 v[236:239], a[204:207], a[8:11], v[236:239]\n\t" "ds_read_b64_tr_b16 a[220:221], %[vr15] offset:32768\n\t"
    "v_mfma_f32_16x16x32_bf16 v[252:255], a[204:207], a[12:15], v[252:255]\n\t" "ds_read_b64_tr_b16 a[222:223], %[vr15] offset:34816\n\t" "s_waitcnt lgkmcnt(4)\n\t"
    "v_mfma_f32_16x16x32_bf16 %0, a[208:211], a[8:11], %0\n\t" "s_barrier\n\t" "ds_read_b64_tr_b16 a[192:193], %[vr0] offset:40960\n\t" "global_load_dwordx4 a[44:47], %[vx], s[40:41] offset:3072 sc1\n\t"
    "v_mfma_f32_16x16x32_bf16 %1, a[208:211], a[12:15], %1\n\t" "ds_read_b64_tr_b16 a[194:195], %[vr0] offset:43008\n\t" "v_mfma_f32_16x16x32_bf16 %2, a[212:215], a[8:11], %2\n\t"
    "ds_read_b64_tr_b16 a[196:197], %[vr1] offset:40960\n\t" "v_mfma_f32_16x16x32_bf16 %3, a[212:215], a[12:15], %3\n\t" "ds_read_b64_tr_b16 a[198:199], %[vr1] offset:43008\n\t" "s_waitcnt lgkmcnt(4)\n\t"
    "v_mfma_f32_16x16x32_bf16 %4, a[216:219], a[8:11], %4\n\t" "ds_read_b64_tr_b16 a[200:201], %[vr2] offset:40960\n\t" "v_mfma_f32_16x16x32_bf16 %5, a[216:219], a[12:15], %5\n\t"
    "ds_read_b64_tr_b16 a[202:203], %[vr2] offset:43008\n\t" "v_mfma_f32_16x16x32_bf16 %6, a[220:223], a[8:11], %6\n\t" "ds_read_b64_tr_b16 a[204:205], %[vr3] offset:40960\n\t"
    "v_mfma_f32_16x16x32_bf16 %7, a[220:223], a[12:15], %7\n\t" "ds_read_b64_tr_b16 a[206:207], %[vr3] offset:43008\n\t" "s_waitcnt lgkmcnt(4)\n\t" "s_waitcnt vmcnt(8)\n\t"
    "v_mfma_f32_16x16x32_bf16 v[224:227], a[192:195], a[16:19], v[224:227]\n\t" "ds_read_b64_tr_b16 a[208:209], %[vr12] offset:40960\n\t"
    "v_mfma_f32_16x16x32_bf16 v[240:243], a[192:195], a[20:23], v[240:243]\n\t" "ds_read_b64_tr_b16 a[210:211], %[vr12] offset:43008\n\t" "s_add_u32 s40, s40, 4096\n\t" "s_addc_u32 s41, s41, 0\n\t"
    "global_load_dwordx4 a[48:51], %[vx], s[40:41] offset:0 sc1\n\t" "v_mfma_f32_16x16x32_bf16 v[228:231], a[196:199], a[16:19], v[228:231]\n\t" "ds_read_b64_tr_b16 a[212:213], %[vr13] offset:40960\n\t"
    "v_mfma_f32_16x16x32_bf16 v[244:247], a[196:199], a[20:23], v[244:247]\n\t" "ds_read_b64_tr_b16 a[214:215], %[vr13] offset:43008\n\t" "s_waitcnt lgkmcnt(4)\n\t"
    "v_mfma_f32_16x16x32_bf16 v[232:235], a[200:203], a[16:19], v[232:235]\n\t" "ds_read_b64_tr_b16 a[216:217], %[vr14] offset:40960\n\t"
    "v_mfma_f32_16x16x32_bf16 v[248:251], a[200:203], a[20:23], v[248:251]\n\t" "ds_read_b64_tr_b16 a[218:219], %[vr14] offset:43008\n\t"
    "v_mfma_f32_16x16x32_bf16 v[236:239], a[204:207], a[16:19], v[236:239]\n\t" "ds_read_b64_tr_b16 a[220:221], %[vr15] offset:40960\n\t"
    "v_mfma_f32_16x16x32_bf16 v[252:255], a[204:207], a[20:23], v[252:255]\n\t" "ds_read_b64_tr_b16 a[222:223], %[vr15] offset:43008\n\t" "s_waitcnt lgkmcnt(4)\n\t"
    "v_mfma_f32_16x16x32_bf16 %0, a[208:211], a[16:19], %0\n\t" "ds_read_b64_tr_b16 a[192:193], %[vr8] offset:8192\n\t" "global_load_dwordx4 a[52:55], %[vx], s[40:41] offset:1024 sc1\n\t"
    "v_mfma_f32_16x16x32_bf16 %1, a[208:211], a[20:23], %1\n\t" "ds_read_b64_tr_b16 a[194:195], %[vr8] offset:10240\n\t" "v_mfma_f32_16x16x32_bf16 %2, a[212:215], a[16:19], %2\n\t"
    "ds_read_b64_tr_b16 a[196:197], %[vr9] offset:8192\n\t" "v_mfma_f32_16x16x32_bf16 %3, a[212:215], a[20:23], %3\n\t" "ds_read_b64_tr_b16 a[198:199], %[vr9] offset:10240\n\t" "s_waitcnt lgkmcnt(4)\n\t"
    "v_mfma_f32_16x16x32_bf16 %4, a[216:219], a[16:19], %4\n\t" "ds_read_b64_tr_b16 a[200:201], %[vr10] offset:8192\n\t" "v_mfma_f32_16x16x32_bf16 %5, a[216:219], a[20:23], %5\n\t"
    "ds_read_b64_tr_b16 a[202:203], %[vr10] offset:10240\n\t" "v_mfma_f32_16x16x32_bf16 %6, a[220:223], a[16:19], %6\n\t" "ds_read_b64_tr_b16 a[204:205], %[vr11] offset:8192\n\t"
    "v_mfma_f32_16x16x32_bf16 %7, a[220:223], a[20:23], %7\n\t" "ds_read_b64_tr_b16 a[206:207], %[vr11] offset:10240\n\t" "s_waitcnt lgkmcnt(4)\n\t" "s_waitcnt vmcnt(6)\n\t"
    "v_mfma_f32_16x16x32_bf16 v[224:227], a[192:195], a[24:27], v[224:227]\n\t" "ds_read_b64_tr_b16 a[208:209], %[vr20] offset:8192\n\t"
    "v_mfma_f32_16x16x32_bf16 v[240:243], a[192:195], a[28:31], v[240:243]\n\t" "ds_read_b64_tr_b16 a[210:211], %[vr20] offset:10240\n\t"
    "global_load_dwordx4 a[56:59], %[vx], s[40:41] offset:2048 sc1\n\t" "v_mfma_f32_16x16x32_bf16 v[228:231], a[196:199], a[24:27], v[228:231]\n\t" "ds_read_b64_tr_b16 a[212:213], %[vr21] offset:8192\n\t"
    "v_mfma_f32_16x16x32_bf16 v[244:247], a[196:199], a[28:31], v[244:247]\n\t" "ds_read_b64_tr_b16 a[214:215], %[vr21] offset:10240\n\t" "s_waitcnt lgkmcnt(4)\n\t"
    "v_mfma_f32_16x16x32_bf16 v[232:235], a[200:203], a[24:27], v[232:235]\n\t" "ds_read_b64_tr_b16 a[216:217], %[vr22] offset:8192\n\t"
    "v_mfma_f32_16x16x32_bf16 v[248:251], a[200:203], a[28:31], v[248:251]\n\t" "ds_read_b64_tr_b16 a[218:219], %[vr22] offset:10240\n\t"
    "v_mfma_f32_16x16x32_bf16 v[236:239], a[204:207], a[24:27], v[236:239]\n\t" "ds_read_b64_tr_b16 a[220:221], %[vr23] offset:8192\n\t"
    "v_mfma_f32_16x16x32_bf16 v[252:255], a[204:207], a[28:31], v[252:255]\n\t" "ds_read_b64_tr_b16 a[222:223], %[vr23] offset:10240\n\t" "s_waitcnt lgkmcnt(4)\n\t"
    "v_mfma_f32_16x16x32_bf16 %0, a[208:211], a[24:27], %0\n\t" "s_barrier\n\t" "ds_read_b64_tr_b16 a[192:193], %[vr8] offset:16384\n\t" "global_load_dwordx4 a[60:63], %[vx], s[40:41] offset:3072 sc1\n\t"
    "v_mfma_f32_16x16x32_bf16 %1, a[208:211], a[28:31], %1\n\t" "ds_read_b64_tr_b16 a[194:195], %[vr8] offset:18432\n\t" "v_mfma_f32_16x16x32_bf16 %2, a[212:215], a[24:27], %2\n\t"
    "ds_read_b64_tr_b16 a[196:197], %[vr9] offset:16384\n\t" "v_mfma_f32_16x16x32_bf16 %3, a[212:215], a[28:31], %3\n\t" "ds_read_b64_tr_b16 a[198:199], %[vr9] offset:18432\n\t" "s_waitcnt lgkmcnt(4)\n\t"
    "v_mfma_f32_16x16x32_bf16 %4, a[216:219], a[24:27], %4\n\t" "ds_read_b64_tr_b16 a[200:201], %[vr10] offset:16384\n\t" "v_mfma_f32_16x16x32_bf16 %5, a[216:219], a[28:31], %5\n\t"
    "ds_read_b64_tr_b16 a[202:203], %[vr10] offset:18432\n\t" "v_mfma_f32_16x16x32_bf16 %6, a[220:223], a[24:27], %6\n\t" "ds_read_b64_tr_b16 a[204:205], %[vr11] offset:16384\n\t"
    "v_mfma_f32_16x16x32_bf16 %7, a[220:223], a[28:31], %7\n\t" "ds_read_b64_tr_b16 a[206:207], %[vr11] offset:18432\n\t" "s_waitcnt lgkmcnt(4)\n\t" "s_waitcnt vmcnt(6)\n\t"
    "v_mfma_f32_16x16x32_bf16 v[224:227], a[192:195], a[32:35], v[224:227]\n\t" "ds_read_b64_tr_b16 a[208:209], %[vr20] offset:16384\n\t"
    "v_mfma_f32_16x16x32_bf16 v[240:243], a[192:195], a[36:39], v[240:243]\n\t" "ds_read_b64_tr_b16 a[210:211], %[vr20] offset:18432\n\t" "s_add_u32 s40, s40, 4096\n\t" "s_addc_u32 s41, s41, 0\n\t"
    "global_load_dwordx4 a[0:3], %[vx], s[40:41] offset:0 sc1\n\t" "v_mfma_f32_16x16x32_bf16 v[228:231], a[196:199], a[32:35], v[228:231]\n\t" "ds_read_b64_tr_b16 a[212:213], %[vr21] offset:16384\n\t"
    "v_mfma_f32_16x16x32_bf16 v[244:247], a[196:199], a[36:39], v[244:247]\n\t" "ds_read_b64_tr_b16 a[214:215], %[vr21] offset:18432\n\t" "s_waitcnt lgkmcnt(4)\n\t"
    "v_mfma_f32_16x16x32_bf16 v[232:235], a[200:203], a[32:35], v[232:235]\n\t" "ds_read_b64_tr_b16 a[216:217], %[vr22] offset:16384\n\t"
    "v_mfma_f32_16x16x32_bf16 v[248:251], a[200:203], a[36:39], v[248:251]\n\t" "ds_read_b64_tr_b16 a[218:219], %[vr22] offset:18432\n\t"
    "v_mfma_f32_16x16x32_bf16 v[236:239], a[204:207], a[32:35], v[236:239]\n\t" "ds_read_b64_tr_b16 a[220:221], %[vr23] offset:16384\n\t"
    "v_mfma_f32_16x16x32_bf16 v[252:255], a[204:207], a[36:39], v[252:255]\n\t" "ds_read_b64_tr_b16 a[222:223], %[vr23] offset:18432\n\t" "s_waitcnt lgkmcnt(4)\n\t"
    "v_mfma_f32_16x16x32_bf16 %0, a[208:211], a[32:35], %0\n\t" "ds_read_b64_tr_b16 a[192:193], %[vr8] offset:24576\n\t" "global_load_dwordx4 a[4:7], %[vx], s[40:41] offset:1024 sc1\n\t"
    "v_mfma_f32_16x16x32_bf16 %1, a[208:211], a[36:39], %1\n\t" "ds_read_b64_tr_b16 a[194:195], %[vr8] offset:26624\n\t" "v_mfma_f32_16x16x32_bf16 %2, a[212:215], a[32:35], %2\n\t"
    "ds_read_b64_tr_b16 a[196:197], %[vr9] offset:24576\n\t" "v_mfma_f32_16x16x32_bf16 %3, a[212:215], a[36:39], %3\n\t" "ds_read_b64_tr_b16 a[198:199], %[vr9] offset:26624\n\t" "s_waitcnt lgkmcnt(4)\n\t"
    "v_mfma_f32_16x16x32_bf16 %4, a[216:219], a[32:35], %4\n\t" "ds_read_b64_tr_b16 a[200:201], %[vr10] offset:24576\n\t" "v_mfma_f32_16x16x32_bf16 %5, a[216:219], a[36:39], %5\n\t"
    "ds_read_b64_tr_b16 a[202:203], %[vr10] offset:26624\n\t" "v_mfma_f32_16x16x32_bf16 %6, a[220:223], a[32:35], %6\n\t" "ds_read_b64_tr_b16 a[204:205], %[vr11] offset:24576\n\t"
    "v_mfma_f32_16x16x32_bf16 %7, a[220:223], a[36:39], %7\n\t" "ds_read_b64_tr_b16 a[206:207], %[vr11] offset:26624\n\t" "s_waitcnt lgkmcnt(4)\n\t" "s_waitcnt vmcnt(6)\n\t"
    "v_mfma_f32_16x16x32_bf16 v[224:227], a[192:195], a[40:43], v[224:227]\n\t" "ds_read_b64_tr_b16 a[208:209], %[vr20] offset:24576\n\t"
    "v_mfma_f32_16x16x32_bf16 v[240:243], a[192:195], a[44:47], v[240:243]\n\t" "ds_read_b64_tr_b16 a[210:211], %[vr20] offset:26624\n\t" "global_load_dwordx4 a[8:11], %[vx], s[40:41] offset:2048 sc1\n\t"
    "v_mfma_f32_16x16x32_bf16 v[228:231], a[196:199], a[40:43], v[228:231]\n\t" "ds_read_b64_tr_b16 a[212:213], %[vr21] offset:24576\n\t"
    "v_mfma_f32_16x16x32_bf16 v[244:247], a[196:199], a[44:47], v[244:247]\n\t" "ds_read_b64_tr_b16 a[214:215], %[vr21] offset:26624\n\t" "s_waitcnt lgkmcnt(4)\n\t"
    "v_mfma_f32_16x16x32_bf16 v[232:235], a[200:203], a[40:43], v[232:235]\n\t" "ds_read_b64_tr_b16 a[216:217], %[vr22] offset:24576\n\t"
    "v_mfma_f32_16x16x32_bf16 v[248:251], a[200:203], a[44:47], v[248:251]\n\t" "ds_read_b64_tr_b16 a[218:219], %[vr22] offset:26624\n\t"
    "v_mfma_f32_16x16x32_bf16 v[236:239], a[204:207], a[40:43], v[236:239]\n\t" "ds_read_b64_tr_b16 a[220:221], %[vr23] offset:24576\n\t"
    "v_mfma_f32_16x16x32_bf16 v[252:255], a[204:207], a[44:47], v[252:255]\n\t" "ds_read_b64_tr_b16 a[222:223], %[vr23] offset:26624\n\t" "s_waitcnt lgkmcnt(4)\n\t"
    "v_mfma_f32_16x16x32_bf16 %0, a[208:211], a[40:43], %0\n\t" "s_barrier\n\t" "ds_read_b64_tr_b16 a[192:193], %[vr0] offset:0\n\t" "global_load_dwordx4 a[12:15], %[vx], s[40:41] offset:3072 sc1\n\t"
    "v_mfma_f32_16x16x32_bf16 %1, a[208:211], a[44:47], %1\n\t" "ds_read_b64_tr_b16 a[194:195], %[vr0] offset:2048\n\t" "v_mfma_f32_16x16x32_bf16 %2, a[212:215], a[40:43], %2\n\t"
    "ds_read_b64_tr_b16 a[196:197], %[vr1] offset:0\n\t" "v_mfma_f32_16x16x32_bf16 %3, a[212:215], a[44:47], %3\n\t" "ds_read_b64_tr_b16 a[198:199], %[vr1] offset:2048\n\t" "s_waitcnt lgkmcnt(4)\n\t"
    "v_mfma_f32_16x16x32_bf16 %4, a[216:219], a[40:43], %4\n\t" "ds_read_b64_tr_b16 a[200:201], %[vr2] offset:0\n\t" "v_mfma_f32_16x16x32_bf16 %5, a[216:219], a[44:47], %5\n\t"
    "ds_read_b64_tr_b16 a[202:203], %[vr2] offset:2048\n\t" "v_mfma_f32_16x16x32_bf16 %6, a[220:223], a[40:43], %6\n\t" "ds_read_b64_tr_b16 a[204:205], %[vr3] offset:0\n\t"
    "v_mfma_f32_16x16x32_bf16 %7, a[220:223], a[44:47], %7\n\t" "ds_read_b64_tr_b16 a[206:207], %[vr3] offset:2048\n\t" "s_waitcnt lgkmcnt(4)\n\t" "s_waitcnt vmcnt(6)\n\t"
    "v_mfma_f32_16x16x32_bf16 v[224:227], a[192:195], a[48:51], v[224:227]\n\t" "ds_read_b64_tr_b16 a[208:209], %[vr12] offset:0\n\t"
    "v_mfma_f32_16x16x32_bf16 v[240:243], a[192:195], a[52:55], v[240:243]\n\t" "ds_read_b64_tr_b16 a[210:211], %[vr12] offset:2048\n\t" "s_add_u32 s40, s40, 4096\n\t" "s_addc_u32 s41, s41, 0\n\t"
    "global_load_dwordx2 a[16:17], %[vo], s[40:41] offset:0 sc1\n\t" "v_mfma_f32_16x16x32_bf16 v[228:231], a[196:199], a[48:51], v[228:231]\n\t" "ds_read_b64_tr_b16 a[212:213], %[vr13] offset:0\n\t"
    "v_mfma_f32_16x16x32_bf16 v[244:247], a[196:199], a[52:55], v[244:247]\n\t" "ds_read_b64_tr_b16 a[214:215], %[vr13] offset:2048\n\t" "s_waitcnt lgkmcnt(4)\n\t"
    "v_mfma_f32_16x16x32_bf16 v[232:235], a[200:203], a[48:51], v[232:235]\n\t" "ds_read_b64_tr_b16 a[216:217], %[vr14] offset:0\n\t"
    "v_mfma_f32_16x16x32_bf16 v[248:251], a[200:203], a[52:55], v[248:251]\n\t" "ds_read_b64_tr_b16 a[218:219], %[vr14] offset:2048\n\t" "global_load_dwordx2 a[18:19], %[vo], s[40:41] offset:1024 sc1\n\t"
    "v_mfma_f32_16x16x32_bf16 v[236:239], a[204:207], a[48:51], v[236:239]\n\t" "ds_read_b64_tr_b16 a[220:221], %[vr15] offset:0\n\t"
    "v_mfma_f32_16x16x32_bf16 v[252:255], a[204:207], a[52:55], v[252:255]\n\t" "ds_read_b64_tr_b16 a[222:223], %[vr15] offset:2048\n\t" "s_waitcnt lgkmcnt(4)\n\t"
    "v_mfma_f32_16x16x32_bf16 %0, a[208:211], a[48:51], %0\n\t" "ds_read_b64_tr_b16 a[192:193], %[vr0] offset:8192\n\t" "v_mfma_f32_16x16x32_bf16 %1, a[208:211], a[52:55], %1\n\t"
    "ds_read_b64_tr_b16 a[194:195], %[vr0] offset:10240\n\t" "global_load_dwordx2 a[20:21], %[vo], s[40:41] offset:512 sc1\n\t" "v_mfma_f32_16x16x32_bf16 %2, a[212:215], a[48:51], %2\n\t"
    "ds_read_b64_tr_b16 a[196:197], %[vr1] offset:8192\n\t" "v_mfma_f32_16x16x32_bf16 %3, a[212:215], a[52:55], %3\n\t" "ds_read_b64_tr_b16 a[198:199], %[vr1] offset:10240\n\t" "s_waitcnt lgkmcnt(4)\n\t"
    "v_mfma_f32_16x16x32_bf16 %4, a[216:219], a[48:51], %4\n\t" "ds_read_b64_tr_b16 a[200:201], %[vr2] offset:8192\n\t" "v_mfma_f32_16x16x32_bf16 %5, a[216:219], a[52:55], %5\n\t"
    "ds_read_b64_tr_b16 a[202:203], %[vr2] offset:10240\n\t" "global_load_dwordx2 a[22:23], %[vo], s[40:41] offset:1536 sc1\n\t" "v_mfma_f32_16x16x32_bf16 %6, a[220:223], a[48:51], %6\n\t"
    "ds_read_b64_tr_b16 a[204:205], %[vr3] offset:8192\n\t" "v_mfma_f32_16x16x32_bf16 %7, a[220:223], a[52:55], %7\n\t" "ds_read_b64_tr_b16 a[206:207], %[vr3] offset:10240\n\t" "s_waitcnt lgkmcnt(4)\n\t"
    "s_waitcnt vmcnt(8)\n\t" "v_mfma_f32_16x16x32_bf16 v[224:227], a[192:195], a[56:59], v[224:227]\n\t" "ds_read_b64_tr_b16 a[208:209], %[vr12] offset:8192\n\t"
    "v_mfma_f32_16x16x32_bf16 v[240:243], a[192:195], a[60:63], v[240:243]\n\t" "ds_read_b64_tr_b16 a[210:211], %[vr12] offset:10240\n\t"
    "global_load_dwordx4 a[24:27], %[vx], s[40:41] offset:2048 sc1\n\t" "v_mfma_f32_16x16x32_bf16 v[228:231], a[196:199], a[56:59], v[228:231]\n\t" "ds_read_b64_tr_b16 a[212:213], %[vr13] offset:8192\n\t"
    "v_mfma_f32_16x16x32_bf16 v[244:247], a[196:199], a[60:63], v[244:247]\n\t" "ds_read_b64_tr_b16 a[214:215], %[vr13] offset:10240\n\t" "s_waitcnt lgkmcnt(4)\n\t"
    "v_mfma_f32_16x16x32_bf16 v[232:235], a[200:203], a[56:59], v[232:235]\n\t" "ds_read_b64_tr_b16 a[216:217], %[vr14] offset:8192\n\t"
    "v_mfma_f32_16x16x32_bf16 v[248:251], a[200:203], a[60:63], v[248:251]\n\t" "ds_read_b64_tr_b16 a[218:219], %[vr14] offset:10240\n\t"
    "v_mfma_f32_16x16x32_bf16 v[236:239], a[204:207], a[56:59], v[236:239]\n\t" "ds_read_b64_tr_b16 a[220:221], %[vr15] offset:8192\n\t"
    "v_mfma_f32_16x16x32_bf16 v[252:255], a[204:207], a[60:63], v[252:255]\n\t" "ds_read_b64_tr_b16 a[222:223], %[vr15] offset:10240\n\t" "s_waitcnt lgkmcnt(4)\n\t"
    "v_mfma_f32_16x16x32_bf16 %0, a[208:211], a[56:59], %0\n\t" "s_barrier\n\t" "ds_read_b64_tr_b16 a[192:193], %[vr0] offset:49152\n\t" "global_load_dwordx4 a[28:31], %[vx], s[40:41] offset:3072 sc1\n\t"
    "v_mfma_f32_16x16x32_bf16 %1, a[208:211], a[60:63], %1\n\t" "ds_read_b64_tr_b16 a[194:195], %[vr0] offset:51200\n\t" "v_mfma_f32_16x16x32_bf16 %2, a[212:215], a[56:59], %2\n\t"
    "ds_read_b64_tr_b16 a[196:197], %[vr1] offset:49152\n\t" "v_mfma_f32_16x16x32_bf16 %3, a[212:215], a[60:63], %3\n\t" "ds_read_b64_tr_b16 a[198:199], %[vr1] offset:51200\n\t" "s_waitcnt lgkmcnt(4)\n\t"
    "v_mfma_f32_16x16x32_bf16 %4, a[216:219], a[56:59], %4\n\t" "ds_read_b64_tr_b16 a[200:201], %[vr2] offset:49152\n\t" "v_mfma_f32_16x16x32_bf16 %5, a[216:219], a[60:63], %5\n\t"
    "ds_read_b64_tr_b16 a[202:203], %[vr2] offset:51200\n\t" "v_mfma_f32_16x16x32_bf16 %6, a[220:223], a[56:59], %6\n\t" "ds_read_b64_tr_b16 a[204:205], %[vr3] offset:49152\n\t"
    "v_mfma_f32_16x16x32_bf16 %7, a[220:223], a[60:63], %7\n\t" "ds_read_b64_tr_b16 a[206:207], %[vr3] offset:51200\n\t" "s_waitcnt lgkmcnt(4)\n\t" "s_waitcnt vmcnt(8)\n\t"
    "v_mfma_f32_16x16x32_bf16 v[224:227], a[192:195], a[0:3], v[224:227]\n\t" "ds_read_b64_tr_b16 a[208:209], %[vr12] offset:49152\n\t"
    "v_mfma_f32_16x16x32_bf16 v[240:243], a[192:195], a[4:7], v[240:243]\n\t" "ds_read_b64_tr_b16 a[210:211], %[vr12] offset:51200\n\t" "s_add_u32 s40, s40, 4096\n\t" "s_addc_u32 s41, s41, 0\n\t"
    "global_load_dwordx4 a[32:35], %[vx], s[40:41] offset:0 sc1\n\t" "v_mfma_f32_16x16x32_bf16 v[228:231], a[196:199], a[0:3], v[228:231]\n\t" "ds_read_b64_tr_b16 a[212:213], %[vr13] offset:49152\n\t"
    "v_mfma_f32_16x16x32_bf16 v[244:247], a[196:199], a[4:7], v[244:247]\n\t" "ds_read_b64_tr_b16 a[214:215], %[vr13] offset:51200\n\t" "s_waitcnt lgkmcnt(4)\n\t"
    "v_mfma_f32_16x16x32_bf16 v[232:235], a[200:203], a[0:3], v[232:235]\n\t" "ds_read_b64_tr_b16 a[216:217], %[vr14] offset:49152\n\t" "s_add_u32 m0, %[sl0], 24576\n\t"
    "v_mfma_f32_16x16x32_bf16 v[248:251], a[200:203], a[4:7], v[248:251]\n\t" "ds_read_b64_tr_b16 a[218:219], %[vr14] offset:51200\n\t" "global_load_lds_dwordx4 %[vb0], %[sb]\n\t"
    "v_add_u32 %[vb0], 0x10000, %[vb0]\n\t" "v_mfma_f32_16x16x32_bf16 v[236:239], a[204:207], a[0:3], v[236:239]\n\t" "ds_read_b64_tr_b16 a[220:221], %[vr15] offset:49152\n\t"
    "v_mfma_f32_16x16x32_bf16 v[252:255], a[204:207], a[4:7], v[252:255]\n\t" "ds_read_b64_tr_b16 a[222:223], %[vr15] offset:51200\n\t" "s_waitcnt lgkmcnt(4)\n\t"
    "v_mfma_f32_16x16x32_bf16 %0, a[208:211], a[0:3], %0\n\t" "ds_read_b64_tr_b16 a[192:193], %[vr0] offset:57344\n\t" "v_mfma_f32_16x16x32_bf16 %1, a[208:211], a[4:7], %1\n\t"
    "ds_read_b64_tr_b16 a[194:195], %[vr0] offset:59392\n\t" "global_load_dwordx4 a[36:39], %[vx], s[40:41] offset:1024 sc1\n\t" "v_mfma_f32_16x16x32_bf16 %2, a[212:215], a[0:3], %2\n\t"
    "ds_read_b64_tr_b16 a[196:197], %[vr1] offset:57344\n\t" "v_mfma_f32_16x16x32_bf16 %3, a[212:215], a[4:7], %3\n\t" "ds_read_b64_tr_b16 a[198:199], %[vr1] offset:59392\n\t" "s_waitcnt lgkmcnt(4)\n\t"
    "v_mfma_f32_16x16x32_bf16 %4, a[216:219], a[0:3], %4\n\t" "ds_read_b64_tr_b16 a[200:201], %[vr2] offset:57344\n\t" "s_add_u32 m0, %[sl1], 24576\n\t"
    "v_mfma_f32_16x16x32_bf16 %5, a[216:219], a[4:7], %5\n\t" "ds_read_b64_tr_b16 a[202:203], %[vr2] offset:59392\n\t" "global_load_lds_dwordx4 %[vb1], %[sb]\n\t" "v_add_u32 %[vb1], 0x10000, %[vb1]\n\t"
    "v_mfma_f32_16x16x32_bf16 %6, a[220:223], a[0:3], %6\n\t" "ds_read_b64_tr_b16 a[204:205], %[vr3] offset:57344\n\t" "v_mfma_f32_16x16x32_bf16 %7, a[220:223], a[4:7], %7\n\t"
    "ds_read_b64_tr_b16 a[206:207], %[vr3] offset:59392\n\t" "s_waitcnt lgkmcnt(4)\n\t" "s_waitcnt vmcnt(10)\n\t" "v_mfma_f32_16x16x32_bf16 v[224:227], a[192:195], a[8:11], v[224:227]\n\t"
    "ds_read_b64_tr_b16 a[208:209], %[vr12] offset:57344\n\t" "v_mfma_f32_16x16x32_bf16 v[240:243], a[192:195], a[12:15], v[240:243]\n\t" "ds_read_b64_tr_b16 a[210:211], %[vr12] offset:59392\n\t"
    "global_load_dwordx4 a[40:43], %[vx], s[40:41] offset:2048 sc1\n\t" "v_mfma_f32_16x16x32_bf16 v[228:231], a[196:199], a[8:11], v[228:231]\n\t" "ds_read_b64_tr_b16 a[212:213], %[vr13] offset:57344\n\t"
    "v_mfma_f32_16x16x32_bf16 v[244:247], a[196:199], a[12:15], v[244:247]\n\t" "ds_read_b64_tr_b16 a[214:215], %[vr13] offset:59392\n\t" "s_waitcnt lgkmcnt(4)\n\t"
    "v_mfma_f32_16x16x32_bf16 v[232:235], a[200:203], a[8:11], v[232:235]\n\t" "ds_read_b64_tr_b16 a[216:217], %[vr14] offset:57344\n\t" "s_add_u32 m0, %[sl0], 32768\n\t"
    "v_mfma_f32_16x16x32_bf16 v[248:251], a[200:203], a[12:15], v[248:251]\n\t" "ds_read_b64_tr_b16 a[218:219], %[vr14] offset:59392\n\t" "global_load_lds_dwordx4 %[vb0], %[sb]\n\t"
    "v_add_u32 %[vb0], 0x10000, %[vb0]\n\t" "v_mfma_f32_16x16x32_bf16 v[236:239], a[204:207], a[8:11], v[236:239]\n\t" "ds_read_b64_tr_b16 a[220:221], %[vr15] offset:57344\n\t"
    "v_mfma_f32_16x16x32_bf16 v[252:255], a[204:207], a[12:15], v[252:255]\n\t" "ds_read_b64_tr_b16 a[222:223], %[vr15] offset:59392\n\t" "s_waitcnt lgkmcnt(4)\n\t"
    "v_mfma_f32_16x16x32_bf16 %0, a[208:211], a[8:11], %0\n\t" "s_barrier\n\t" "ds_read_b64_tr_b16 a[192:193], %[vr4] offset:0\n\t" "v_mfma_f32_16x16x32_bf16 %1, a[208:211], a[12:15], %1\n\t"
    "ds_read_b64_tr_b16 a[194:195], %[vr4] offset:2048\n\t" "global_load_dwordx4 a[44:47], %[vx], s[40:41] offset:3072 sc1\n\t" "v_mfma_f32_16x16x32_bf16 %2, a[212:215], a[8:11], %2\n\t"
    "ds_read_b64_tr_b16 a[196:197], %[vr5] offset:0\n\t" "v_mfma_f32_16x16x32_bf16 %3, a[212:215], a[12:15], %3\n\t" "ds_read_b64_tr_b16 a[198:199], %[vr5] offset:2048\n\t" "s_waitcnt lgkmcnt(4)\n\t"
    "v_mfma_f32_16x16x32_bf16 %4, a[216:219], a[8:11], %4\n\t" "ds_read_b64_tr_b16 a[200:201], %[vr6] offset:0\n\t" "s_add_u32 m0, %[sl1], 32768\n\t"
    "v_mfma_f32_16x16x32_bf16 %5, a[216:219], a[12:15], %5\n\t" "ds_read_b64_tr_b16 a[202:203], %[vr6] offset:2048\n\t" "global_load_lds_dwordx4 %[vb1], %[sb]\n\t" "v_add_u32 %[vb1], 0x10000, %[vb1]\n\t"
    "v_mfma_f32_16x16x32_bf16 %6, a[220:223], a[8:11], %6\n\t" "ds_read_b64_tr_b16 a[204:205], %[vr7] offset:0\n\t" "v_mfma_f32_16x16x32_bf16 %7, a[220:223], a[12:15], %7\n\t"
    "ds_read_b64_tr_b16 a[206:207], %[vr7] offset:2048\n\t" "s_waitcnt lgkmcnt(4)\n\t" "s_waitcnt vmcnt(10)\n\t" "v_mfma_f32_16x16x32_bf16 v[224:227], a[192:195], a[16:19], v[224:227]\n\t"
    "ds_read_b64_tr_b16 a[208:209], %[vr16] offset:0\n\t" "v_mfma_f32_16x16x32_bf16 v[240:243], a[192:195], a[20:23], v[240:243]\n\t" "ds_read_b64_tr_b16 a[210:211], %[vr16] offset:2048\n\t"
    "s_add_u32 s40, s40, 4096\n\t" "s_addc_u32 s41, s41, 0\n\t" "global_load_dwordx4 a[48:51], %[vx], s[40:41] offset:0 sc1\n\t" "v_mfma_f32_16x16x32_bf16 v[228:231], a[196:199], a[16:19], v[228:231]\n\t"
    "ds_read_b64_tr_b16 a[212:213], %[vr17] offset:0\n\t" "v_mfma_f32_16x16x32_bf16 v[244:247], a[196:199], a[20:23], v[244:247]\n\t" "ds_read_b64_tr_b16 a[214:215], %[vr17] offset:2048\n\t"
    "s_waitcnt lgkmcnt(4)\n\t" "v_mfma_f32_16x16x32_bf16 v[232:235], a[200:203], a[16:19], v[232:235]\n\t" "ds_read_b64_tr_b16 a[216:217], %[vr18] offset:0\n\t" "s_add_u32 m0, %[sl0], 40960\n\t"
    "v_mfma_f32_16x16x32_bf16 v[248:251], a[200:203], a[20:23], v[248:251]\n\t" "ds_read_b64_tr_b16 a[218:219], %[vr18] offset:2048\n\t" "global_load_lds_dwordx4 %[vb0], %[sb]\n\t"
    "v_add_u32 %[vb0], 0x10000, %[vb0]\n\t" "v_mfma_f32_16x16x32_bf16 v[236:239], a[204:207], a[16:19], v[236:239]\n\t" "ds_read_b64_tr_b16 a[220:221], %[vr19] offset:0\n\t"
    "v_mfma_f32_16x16x32_bf16 v[252:255], a[204:207], a[20:23], v[252:255]\n\t" "ds_read_b64_tr_b16 a[222:223], %[vr19] offset:2048\n\t" "s_waitcnt lgkmcnt(4)\n\t"
    "v_mfma_f32_16x16x32_bf16 %0, a[208:211], a[16:19], %0\n\t" "ds_read_b64_tr_b16 a[192:193], %[vr4] offset:8192\n\t" "v_mfma_f32_16x16x32_bf16 %1, a[208:211], a[20:23], %1\n\t"
    "ds_read_b64_tr_b16 a[194:195], %[vr4] offset:10240\n\t" "global_load_dwordx4 a[52:55], %[vx], s[40:41] offset:1024 sc1\n\t" "v_mfma_f32_16x16x32_bf16 %2, a[212:215], a[16:19], %2\n\t"
    "ds_read_b64_tr_b16 a[196:197], %[vr5] offset:8192\n\t" "v_mfma_f32_16x16x32_bf16 %3, a[212:215], a[20:23], %3\n\t" "ds_read_b64_tr_b16 a[198:199], %[vr5] offset:10240\n\t" "s_waitcnt lgkmcnt(4)\n\t"
    "v_mfma_f32_16x16x32_bf16 %4, a[216:219], a[16:19], %4\n\t" "ds_read_b64_tr_b16 a[200:201], %[vr6] offset:8192\n\t" "s_add_u32 m0, %[sl1], 40960\n\t"
    "v_mfma_f32_16x16x32_bf16 %5, a[216:219], a[20:23], %5\n\t" "ds_read_b64_tr_b16 a[202:203], %[vr6] offset:10240\n\t" "global_load_lds_dwordx4 %[vb1], %[sb]\n\t" "v_add_u32 %[vb1], 0x10000, %[vb1]\n\t"
    "v_mfma_f32_16x16x32_bf16 %6, a[220:223], a[16:19], %6\n\t" "ds_read_b64_tr_b16 a[204:205], %[vr7] offset:8192\n\t" "v_mfma_f32_16x16x32_bf16 %7, a[220:223], a[20:23], %7\n\t"
    "ds_read_b64_tr_b16 a[206:207], %[vr7] offset:10240\n\t" "s_waitcnt lgkmcnt(4)\n\t" "s_waitcnt vmcnt(12)\n\t" "v_mfma_f32_16x16x32_bf16 v[224:227], a[192:195], a[24:27], v[224:227]\n\t"
    "ds_read_b64_tr_b16 a[208:209], %[vr16] offset:8192\n\t" "v_mfma_f32_16x16x32_bf16 v[240:243], a[192:195], a[28:31], v[240:243]\n\t" "ds_read_b64_tr_b16 a[210:211], %[vr16] offset:10240\n\t"
    "global_load_dwordx4 a[56:59], %[vx], s[40:41] offset:2048 sc1\n\t" "v_mfma_f32_16x16x32_bf16 v[228:231], a[196:199], a[24:27], v[228:231]\n\t" "ds_read_b64_tr_b16 a[212:213], %[vr17] offset:8192\n\t"
    "v_mfma_f32_16x16x32_bf16 v[244:247], a[196:199], a[28:31], v[244:247]\n\t" "ds_read_b64_tr_b16 a[214:215], %[vr17] offset:10240\n\t" "s_waitcnt lgkmcnt(4)\n\t"
    "v_mfma_f32_16x16x32_bf16 v[232:235], a[200:203], a[24:27], v[232:235]\n\t" "ds_read_b64_tr_b16 a[216:217], %[vr18] offset:8192\n\t" "s_add_u32 m0, %[sl0], 139264\n\t"
    "v_mfma_f32_16x16x32_bf16 v[248:251], a[200:203], a[28:31], v[248:251]\n\t" "ds_read_b64_tr_b16 a[218:219], %[vr18] offset:10240\n\t" "global_load_lds_dwordx4 %[vb0], %[sb]\n\t"
    "v_add_u32 %[vb0], 0x10000, %[vb0]\n\t" "v_mfma_f32_16x16x32_bf16 v[236:239], a[204:207], a[24:27], v[236:239]\n\t" "ds_read_b64_tr_b16 a[220:221], %[vr19] offset:8192\n\t"
    "v_mfma_f32_16x16x32_bf16 v[252:255], a[204:207], a[28:31], v[252:255]\n\t" "ds_read_b64_tr_b16 a[222:223], %[vr19] offset:10240\n\t" "s_waitcnt lgkmcnt(4)\n\t"
    "v_mfma_f32_16x16x32_bf16 %0, a[208:211], a[24:27], %0\n\t" "s_barrier\n\t" "ds_read_b64_tr_b16 a[192:193], %[vr4] offset:16384\n\t" "v_mfma_f32_16x16x32_bf16 %1, a[208:211], a[28:31], %1\n\t"
    "ds_read_b64_tr_b16 a[194:195], %[vr4] offset:18432\n\t" "global_load_dwordx4 a[60:63], %[vx], s[40:41] offset:3072 sc1\n\t" "v_mfma_f32_16x16x32_bf16 %2, a[212:215], a[24:27], %2\n\t"
    "ds_read_b64_tr_b16 a[196:197], %[vr5] offset:16384\n\t" "v_mfma_f32_16x16x32_bf16 %3, a[212:215], a[28:31], %3\n\t" "ds_read_b64_tr_b16 a[198:199], %[vr5] offset:18432\n\t" "s_waitcnt lgkmcnt(4)\n\t"
    "v_mfma_f32_16x16x32_bf16 %4, a[216:219], a[24:27], %4\n\t" "ds_read_b64_tr_b16 a[200:201], %[vr6] offset:16384\n\t" "s_add_u32 m0, %[sl1], 139264\n\t"
    "v_mfma_f32_16x16x32_bf16 %5, a[216:219], a[28:31], %5\n\t" "ds_read_b64_tr_b16 a[202:203], %[vr6] offset:18432\n\t" "global_load_lds_dwordx4 %[vb1], %[sb]\n\t" "v_add_u32 %[vb1], 0x10000, %[vb1]\n\t"
    "v_mfma_f32_16x16x32_bf16 %6, a[220:223], a[24:27], %6\n\t" "ds_read_b64_tr_b16 a[204:205], %[vr7] offset:16384\n\t" "v_mfma_f32_16x16x32_bf16 %7, a[220:223], a[28:31], %7\n\t"
    "ds_read_b64_tr_b16 a[206:207], %[vr7] offset:18432\n\t" "s_waitcnt lgkmcnt(4)\n\t" "s_waitcnt vmcnt(13)\n\t" "v_mfma_f32_16x16x32_bf16 v[224:227], a[192:195], a[32:35], v[224:227]\n\t"
    "ds_read_b64_tr_b16 a[208:209], %[vr16] offset:16384\n\t" "v_mfma_f32_16x16x32_bf16 v[240:243], a[192:195], a[36:39], v[240:243]\n\t" "ds_read_b64_tr_b16 a[210:211], %[vr16] offset:18432\n\t"
    "s_add_u32 s40, s40, 4096\n\t" "s_addc_u32 s41, s41, 0\n\t" "global_load_dwordx4 a[0:3], %[vx], s[40:41] offset:0 sc1\n\t" "v_mfma_f32_16x16x32_bf16 v[228:231], a[196:199], a[32:35], v[228:231]\n\t"
    "ds_read_b64_tr_b16 a[212:213], %[vr17] offset:16384\n\t" "v_mfma_f32_16x16x32_bf16 v[244:247], a[196:199], a[36:39], v[244:247]\n\t" "ds_read_b64_tr_b16 a[214:215], %[vr17] offset:18432\n\t"
    "s_waitcnt lgkmcnt(4)\n\t" "v_mfma_f32_16x16x32_bf16 v[232:235], a[200:203], a[32:35], v[232:235]\n\t" "ds_read_b64_tr_b16 a[216:217], %[vr18] offset:16384\n\t" "s_add_u32 m0, %[sl0], 147456\n\t"
    "v_mfma_f32_16x16x32_bf16 v[248:251], a[200:203], a[36:39], v[248:251]\n\t" "ds_read_b64_tr_b16 a[218:219], %[vr18] offset:18432\n\t" "global_load_lds_dwordx4 %[vb0], %[sb]\n\t"
    "v_add_u32 %[vb0], 0x10000, %[vb0]\n\t" "v_mfma_f32_16x16x32_bf16 v[236:239], a[204:207], a[32:35], v[236:239]\n\t" "ds_read_b64_tr_b16 a[220:221], %[vr19] offset:16384\n\t"
    "v_mfma_f32_16x16x32_bf16 v[252:255], a[204:207], a[36:39], v[252:255]\n\t" "ds_read_b64_tr_b16 a[222:223], %[vr19] offset:18432\n\t" "s_waitcnt lgkmcnt(4)\n\t"
    "v_mfma_f32_16x16x32_bf16 %0, a[208:211], a[32:35], %0\n\t" "ds_read_b64_tr_b16 a[192:193], %[vr4] offset:24576\n\t" "v_mfma_f32_16x16x32_bf16 %1, a[208:211], a[36:39], %1\n\t"
    "ds_read_b64_tr_b16 a[194:195], %[vr4] offset:26624\n\t" "global_load_dwordx4 a[4:7], %[vx], s[40:41] offset:1024 sc1\n\t" "v_mfma_f32_16x16x32_bf16 %2, a[212:215], a[32:35], %2\n\t"
    "ds_read_b64_tr_b16 a[196:197], %[vr5] offset:24576\n\t" "v_mfma_f32_16x16x32_bf16 %3, a[212:215], a[36:39], %3\n\t" "ds_read_b64_tr_b16 a[198:199], %[vr5] offset:26624\n\t" "s_waitcnt lgkmcnt(4)\n\t"
    "v_mfma_f32_16x16x32_bf16 %4, a[216:219], a[32:35], %4\n\t" "ds_read_b64_tr_b16 a[200:201], %[vr6] offset:24576\n\t" "s_add_u32 m0, %[sl1], 147456\n\t"
    "v_mfma_f32_16x16x32_bf16 %5, a[216:219], a[36:39], %5\n\t" "ds_read_b64_tr_b16 a[202:203], %[vr6] offset:26624\n\t" "global_load_lds_dwordx4 %[vb1], %[sb]\n\t" "v_add_u32 %[vb1], 0x10000, %[vb1]\n\t"
    "v_mfma_f32_16x16x32_bf16 %6, a[220:223], a[32:35], %6\n\t" "ds_read_b64_tr_b16 a[204:205], %[vr7] offset:24576\n\t" "v_mfma_f32_16x16x32_bf16 %7, a[220:223], a[36:39], %7\n\t"
    "ds_read_b64_tr_b16 a[206:207], %[vr7] offset:26624\n\t" "s_waitcnt lgkmcnt(4)\n\t" "s_waitcnt vmcnt(13)\n\t" "v_mfma_f32_16x16x32_bf16 v[224:227], a[192:195], a[40:43], v[224:227]\n\t"
    "ds_read_b64_tr_b16 a[208:209], %[vr16] offset:24576\n\t" "v_mfma_f32_16x16x32_bf16 v[240:243], a[192:195], a[44:47], v[240:243]\n\t" "ds_read_b64_tr_b16 a[210:211], %[vr16] offset:26624\n\t"
    "global_load_dwordx2 a[8:9], %[vo], s[40:41] offset:2048 sc1\n\t" "v_mfma_f32_16x16x32_bf16 v[228:231], a[196:199], a[40:43], v[228:231]\n\t" "ds_read_b64_tr_b16 a[212:213], %[vr17] offset:24576\n\t"
    "v_mfma_f32_16x16x32_bf16 v[244:247], a[196:199], a[44:47], v[244:247]\n\t" "ds_read_b64_tr_b16 a[214:215], %[vr17] offset:26624\n\t"
    "global_load_dwordx2 a[10:11], %[vo], s[40:41] offset:3072 sc1\n\t" "s_waitcnt lgkmcnt(4)\n\t" "v_mfma_f32_16x16x32_bf16 v[232:235], a[200:203], a[40:43], v[232:235]\n\t"
    "ds_read_b64_tr_b16 a[216:217], %[vr18] offset:24576\n\t" "v_mfma_f32_16x16x32_bf16 v[248:251], a[200:203], a[44:47], v[248:251]\n\t" "ds_read_b64_tr_b16 a[218:219], %[vr18] offset:26624\n\t"
    "s_add_u32 m0, %[sl0], 155648\n\t" "v_mfma_f32_16x16x32_bf16 v[236:239], a[204:207], a[40:43], v[236:239]\n\t" "ds_read_b64_tr_b16 a[220:221], %[vr19] offset:24576\n\t"
    "global_load_lds_dwordx4 %[vb0], %[sb]\n\t" "v_add_u32 %[vb0], 0x10000, %[vb0]\n\t" "v_mfma_f32_16x16x32_bf16 v[252:255], a[204:207], a[44:47], v[252:255]\n\t"
    "ds_read_b64_tr_b16 a[222:223], %[vr19] offset:26624\n\t" "s_waitcnt lgkmcnt(4)\n\t" "v_mfma_f32_16x16x32_bf16 %0, a[208:211], a[40:43], %0\n\t" "s_barrier\n\t"
    "ds_read_b64_tr_b16 a[192:193], %[vr4] offset:32768\n\t" "global_load_dwordx2 a[12:13], %[vo], s[40:41] offset:2560 sc1\n\t" "v_mfma_f32_16x16x32_bf16 %1, a[208:211], a[44:47], %1\n\t"
    "ds_read_b64_tr_b16 a[194:195], %[vr4] offset:34816\n\t" "v_mfma_f32_16x16x32_bf16 %2, a[212:215], a[40:43], %2\n\t" "ds_read_b64_tr_b16 a[196:197], %[vr5] offset:32768\n\t"
    "v_mfma_f32_16x16x32_bf16 %3, a[212:215], a[44:47], %3\n\t" "ds_read_b64_tr_b16 a[198:199], %[vr5] offset:34816\n\t" "global_load_dwordx2 a[14:15], %[vo], s[40:41] offset:3584 sc1\n\t"
    "s_waitcnt lgkmcnt(4)\n\t" "v_mfma_f32_16x16x32_bf16 %4, a[216:219], a[40:43], %4\n\t" "ds_read_b64_tr_b16 a[200:201], %[vr6] offset:32768\n\t" "s_add_u32 m0, %[sl1], 155648\n\t"
    "v_mfma_f32_16x16x32_bf16 %5, a[216:219], a[44:47], %5\n\t" "ds_read_b64_tr_b16 a[202:203], %[vr6] offset:34816\n\t" "global_load_lds_dwordx4 %[vb1], %[sb]\n\t" "v_add_u32 %[vb1], 0x10000, %[vb1]\n\t"
    "v_mfma_f32_16x16x32_bf16 %6, a[220:223], a[40:43], %6\n\t" "ds_read_b64_tr_b16 a[204:205], %[vr7] offset:32768\n\t" "v_mfma_f32_16x16x32_bf16 %7, a[220:223], a[44:47], %7\n\t"
    "ds_read_b64_tr_b16 a[206:207], %[vr7] offset:34816\n\t" "s_waitcnt lgkmcnt(4)\n\t" "s_waitcnt vmcnt(15)\n\t" "v_mfma_f32_16x16x32_bf16 v[224:227], a[192:195], a[48:51], v[224:227]\n\t"
    "ds_read_b64_tr_b16 a[208:209], %[vr16] offset:32768\n\t" "v_mfma_f32_16x16x32_bf16 v[240:243], a[192:195], a[52:55], v[240:243]\n\t" "ds_read_b64_tr_b16 a[210:211], %[vr16] offset:34816\n\t"
    "s_add_u32 s40, s40, 4096\n\t" "s_addc_u32 s41, s41, 0\n\t" "global_load_dwordx4 a[16:19], %[vx], s[40:41] offset:0 sc1\n\t" "v_mfma_f32_16x16x32_bf16 v[228:231], a[196:199], a[48:51], v[228:231]\n\t"
    "ds_read_b64_tr_b16 a[212:213], %[vr17] offset:32768\n\t" "v_mfma_f32_16x16x32_bf16 v[244:247], a[196:199], a[52:55], v[244:247]\n\t" "ds_read_b64_tr_b16 a[214:215], %[vr17] offset:34816\n\t"
    "s_waitcnt lgkmcnt(4)\n\t" "v_mfma_f32_16x16x32_bf16 v[232:235], a[200:203], a[48:51], v[232:235]\n\t" "ds_read_b64_tr_b16 a[216:217], %[vr18] offset:32768\n\t" "s_add_u32 m0, %[sl0], 0\n\t"
    "v_mfma_f32_16x16x32_bf16 v[248:251], a[200:203], a[52:55], v[248:251]\n\t" "ds_read_b64_tr_b16 a[218:219], %[vr18] offset:34816\n\t" "global_load_lds_dwordx4 %[vb0], %[sb]\n\t"
    "v_add_u32 %[vb0], 0x10000, %[vb0]\n\t" "v_mfma_f32_16x16x32_bf16 v[236:239], a[204:207], a[48:51], v[236:239]\n\t" "ds_read_b64_tr_b16 a[220:221], %[vr19] offset:32768\n\t"
    "v_mfma_f32_16x16x32_bf16 v[252:255], a[204:207], a[52:55], v[252:255]\n\t" "ds_read_b64_tr_b16 a[222:223], %[vr19] offset:34816\n\t" "s_waitcnt lgkmcnt(4)\n\t"
    "v_mfma_f32_16x16x32_bf16 %0, a[208:211], a[48:51], %0\n\t" "ds_read_b64_tr_b16 a[192:193], %[vr4] offset:40960\n\t" "v_mfma_f32_16x16x32_bf16 %1, a[208:211], a[52:55], %1\n\t"
    "ds_read_b64_tr_b16 a[194:195], %[vr4] offset:43008\n\t" "global_load_dwordx4 a[20:23], %[vx], s[40:41] offset:1024 sc1\n\t" "v_mfma_f32_16x16x32_bf16 %2, a[212:215], a[48:51], %2\n\t"
    "ds_read_b64_tr_b16 a[196:197], %[vr5] offset:40960\n\t" "v_mfma_f32_16x16x32_bf16 %3, a[212:215], a[52:55], %3\n\t" "ds_read_b64_tr_b16 a[198:199], %[vr5] offset:43008\n\t" "s_waitcnt lgkmcnt(4)\n\t"
    "v_mfma_f32_16x16x32_bf16 %4, a[216:219], a[48:51], %4\n\t" "ds_read_b64_tr_b16 a[200:201], %[vr6] offset:40960\n\t" "s_add_u32 m0, %[sl1], 0\n\t"
    "v_mfma_f32_16x16x32_bf16 %5, a[216:219], a[52:55], %5\n\t" "ds_read_b64_tr_b16 a[202:203], %[vr6] offset:43008\n\t" "global_load_lds_dwordx4 %[vb1], %[sb]\n\t" "v_add_u32 %[vb1], 0x10000, %[vb1]\n\t"
    "v_mfma_f32_16x16x32_bf16 %6, a[220:223], a[48:51], %6\n\t" "ds_read_b64_tr_b16 a[204:205], %[vr7] offset:40960\n\t" "v_mfma_f32_16x16x32_bf16 %7, a[220:223], a[52:55], %7\n\t"
    "ds_read_b64_tr_b16 a[206:207], %[vr7] offset:43008\n\t" "s_waitcnt lgkmcnt(4)\n\t" "s_waitcnt vmcnt(15)\n\t" "v_mfma_f32_16x16x32_bf16 v[224:227], a[192:195], a[56:59], v[224:227]\n\t"
    "ds_read_b64_tr_b16 a[208:209], %[vr16] offset:40960\n\t" "v_mfma_f32_16x16x32_bf16 v[240:243], a[192:195], a[60:63], v[240:243]\n\t" "ds_read_b64_tr_b16 a[210:211], %[vr16] offset:43008\n\t"
    "global_load_dwordx4 a[24:27], %[vx], s[40:41] offset:2048 sc1\n\t" "v_mfma_f32_16x16x32_bf16 v[228:231], a[196:199], a[56:59], v[228:231]\n\t"
    "ds_read_b64_tr_b16 a[212:213], %[vr17] offset:40960\n\t" "v_mfma_f32_16x16x32_bf16 v[244:247], a[196:199], a[60:63], v[244:247]\n\t" "ds_read_b64_tr_b16 a[214:215], %[vr17] offset:43008\n\t"
    "s_waitcnt lgkmcnt(4)\n\t" "v_mfma_f32_16x16x32_bf16 v[232:235], a[200:203], a[56:59], v[232:235]\n\t" "ds_read_b64_tr_b16 a[216:217], %[vr18] offset:40960\n\t" "s_add_u32 m0, %[sl0], 8192\n\t"
    "v_mfma_f32_16x16x32_bf16 v[248:251], a[200:203], a[60:63], v[248:251]\n\t" "ds_read_b64_tr_b16 a[218:219], %[vr18] offset:43008\n\t" "global_load_lds_dwordx4 %[vb0], %[sb]\n\t"
    "v_add_u32 %[vb0], 0x10000, %[vb0]\n\t" "v_mfma_f32_16x16x32_bf16 v[236:239], a[204:207], a[56:59], v[236:239]\n\t" "ds_read_b64_tr_b16 a[220:221], %[vr19] offset:40960\n\t"
    "v_mfma_f32_16x16x32_bf16 v[252:255], a[204:207], a[60:63], v[252:255]\n\t" "ds_read_b64_tr_b16 a[222:223], %[vr19] offset:43008\n\t" "s_waitcnt lgkmcnt(4)\n\t"
    "v_mfma_f32_16x16x32_bf16 %0, a[208:211], a[56:59], %0\n\t" "s_barrier\n\t" "ds_read_b64_tr_b16 a[192:193], %[vr0] offset:24576\n\t" "v_mfma_f32_16x16x32_bf16 %1, a[208:211], a[60:63], %1\n\t"
    "ds_read_b64_tr_b16 a[194:195], %[vr0] offset:26624\n\t" "global_load_dwordx4 a[28:31], %[vx], s[40:41] offset:3072 sc1\n\t" "v_mfma_f32_16x16x32_bf16 %2, a[212:215], a[56:59], %2\n\t"
    "ds_read_b64_tr_b16 a[196:197], %[vr1] offset:24576\n\t" "v_mfma_f32_16x16x32_bf16 %3, a[212:215], a[60:63], %3\n\t" "ds_read_b64_tr_b16 a[198:199], %[vr1] offset:26624\n\t" "s_waitcnt lgkmcnt(4)\n\t"
    "v_mfma_f32_16x16x32_bf16 %4, a[216:219], a[56:59], %4\n\t" "ds_read_b64_tr_b16 a[200:201], %[vr2] offset:24576\n\t" "s_add_u32 m0, %[sl1], 8192\n\t"
    "v_mfma_f32_16x16x32_bf16 %5, a[216:219], a[60:63], %5\n\t" "ds_read_b64_tr_b16 a[202:203], %[vr2] offset:26624\n\t" "global_load_lds_dwordx4 %[vb1], %[sb]\n\t" "v_add_u32 %[vb1], 0x10000, %[vb1]\n\t"
    "v_mfma_f32_16x16x32_bf16 %6, a[220:223], a[56:59], %6\n\t" "ds_read_b64_tr_b16 a[204:205], %[vr3] offset:24576\n\t" "v_mfma_f32_16x16x32_bf16 %7, a[220:223], a[60:63], %7\n\t"
    "ds_read_b64_tr_b16 a[206:207], %[vr3] offset:26624\n\t" "s_waitcnt lgkmcnt(4)\n\t" "s_waitcnt vmcnt(15)\n\t" "v_mfma_f32_16x16x32_bf16 v[224:227], a[192:195], a[0:3], v[224:227]\n\t"
    "ds_read_b64_tr_b16 a[208:209], %[vr12] offset:24576\n\t" "v_mfma_f32_16x16x32_bf16 v[240:243], a[192:195], a[4:7], v[240:243]\n\t" "ds_read_b64_tr_b16 a[210:211], %[vr12] offset:26624\n\t"
    "s_add_u32 s40, s40, 4096\n\t" "s_addc_u32 s41, s41, 0\n\t" "global_load_dwordx4 a[32:35], %[vx], s[40:41] offset:0 sc1\n\t" "v_mfma_f32_16x16x32_bf16 v[228:231], a[196:199], a[0:3], v[228:231]\n\t"
    "ds_read_b64_tr_b16 a[212:213], %[vr13] offset:24576\n\t" "v_mfma_f32_16x16x32_bf16 v[244:247], a[196:199], a[4:7], v[244:247]\n\t" "ds_read_b64_tr_b16 a[214:215], %[vr13] offset:26624\n\t"
    "s_waitcnt lgkmcnt(4)\n\t" "v_mfma_f32_16x16x32_bf16 v[232:235], a[200:203], a[0:3], v[232:235]\n\t" "ds_read_b64_tr_b16 a[216:217], %[vr14] offset:24576\n\t" "s_add_u32 m0, %[sl0], 49152\n\t"
    "v_mfma_f32_16x16x32_bf16 v[248:251], a[200:203], a[4:7], v[248:251]\n\t" "ds_read_b64_tr_b16 a[218:219], %[vr14] offset:26624\n\t" "global_load_lds_dwordx4 %[vb0], %[sb]\n\t"
    "v_add_u32 %[vb0], 0x10000, %[vb0]\n\t" "v_mfma_f32_16x16x32_bf16 v[236:239], a[204:207], a[0:3], v[236:239]\n\t" "ds_read_b64_tr_b16 a[220:221], %[vr15] offset:24576\n\t"
    "v_mfma_f32_16x16x32_bf16 v[252:255], a[204:207], a[4:7], v[252:255]\n\t" "ds_read_b64_tr_b16 a[222:223], %[vr15] offset:26624\n\t" "s_waitcnt lgkmcnt(4)\n\t"
    "v_mfma_f32_16x16x32_bf16 %0, a[208:211], a[0:3], %0\n\t" "ds_read_b64_tr_b16 a[192:193], %[vr0] offset:32768\n\t" "v_mfma_f32_16x16x32_bf16 %1, a[208:211], a[4:7], %1\n\t"
    "ds_read_b64_tr_b16 a[194:195], %[vr0] offset:34816\n\t" "global_load_dwordx4 a[36:39], %[vx], s[40:41] offset:1024 sc1\n\t" "v_mfma_f32_16x16x32_bf16 %2, a[212:215], a[0:3], %2\n\t"
    "ds_read_b64_tr_b16 a[196:197], %[vr1] offset:32768\n\t" "v_mfma_f32_16x16x32_bf16 %3, a[212:215], a[4:7], %3\n\t" "ds_read_b64_tr_b16 a[198:199], %[vr1] offset:34816\n\t" "s_waitcnt lgkmcnt(4)\n\t"
    "v_mfma_f32_16x16x32_bf16 %4, a[216:219], a[0:3], %4\n\t" "ds_read_b64_tr_b16 a[200:201], %[vr2] offset:32768\n\t" "s_add_u32 m0, %[sl1], 49152\n\t"
    "v_mfma_f32_16x16x32_bf16 %5, a[216:219], a[4:7], %5\n\t" "ds_read_b64_tr_b16 a[202:203], %[vr2] offset:34816\n\t" "global_load_lds_dwordx4 %[vb1], %[sb]\n\t" "v_add_u32 %[vb1], 0x10000, %[vb1]\n\t"
    "v_mfma_f32_16x16x32_bf16 %6, a[220:223], a[0:3], %6\n\t" "ds_read_b64_tr_b16 a[204:205], %[vr3] offset:32768\n\t" "v_mfma_f32_16x16x32_bf16 %7, a[220:223], a[4:7], %7\n\t"
    "ds_read_b64_tr_b16 a[206:207], %[vr3] offset:34816\n\t" "s_waitcnt lgkmcnt(4)\n\t" "s_waitcnt vmcnt(13)\n\t" "v_mfma_f32_16x16x32_bf16 v[224:227], a[192:195], a[8:11], v[224:227]\n\t"
    "ds_read_b64_tr_b16 a[208:209], %[vr12] offset:32768\n\t" "v_mfma_f32_16x16x32_bf16 v[240:243], a[192:195], a[12:15], v[240:243]\n\t" "ds_read_b64_tr_b16 a[210:211], %[vr12] offset:34816\n\t"
    "global_load_dwordx4 a[40:43], %[vx], s[40:41] offset:2048 sc1\n\t" "v_mfma_f32_16x16x32_bf16 v[228:231], a[196:199], a[8:11], v[228:231]\n\t" "ds_read_b64_tr_b16 a[212:213], %[vr13] offset:32768\n\t"
    "v_mfma_f32_16x16x32_bf16 v[244:247], a[196:199], a[12:15], v[244:247]\n\t" "ds_read_b64_tr_b16 a[214:215], %[vr13] offset:34816\n\t" "s_waitcnt lgkmcnt(4)\n\t"
    "v_mfma_f32_16x16x32_bf16 v[232:235], a[200:203], a[8:11], v[232:235]\n\t" "ds_read_b64_tr_b16 a[216:217], %[vr14] offset:32768\n\t" "s_add_u32 m0, %[sl0], 57344\n\t"
    "v_mfma_f32_16x16x32_bf16 v[248:251], a[200:203], a[12:15], v[248:251]\n\t" "ds_read_b64_tr_b16 a[218:219], %[vr14] offset:34816\n\t" "global_load_lds_dwordx4 %[vb0], %[sb]\n\t"
    "v_add_u32 %[vb0], 0x10000, %[vb0]\n\t" "v_mfma_f32_16x16x32_bf16 v[236:239], a[204:207], a[8:11], v[236:239]\n\t" "ds_read_b64_tr_b16 a[220:221], %[vr15] offset:32768\n\t"
    "v_mfma_f32_16x16x32_bf16 v[252:255], a[204:207], a[12:15], v[252:255]\n\t" "ds_read_b64_tr_b16 a[222:223], %[vr15] offset:34816\n\t" "s_waitcnt lgkmcnt(4)\n\t"
    "v_mfma_f32_16x16x32_bf16 %0, a[208:211], a[8:11], %0\n\t" "s_barrier\n\t" "ds_read_b64_tr_b16 a[192:193], %[vr0] offset:40960\n\t" "v_mfma_f32_16x16x32_bf16 %1, a[208:211], a[12:15], %1\n\t"
    "ds_read_b64_tr_b16 a[194:195], %[vr0] offset:43008\n\t" "global_load_dwordx4 a[44:47], %[vx], s[40:41] offset:3072 sc1\n\t" "v_mfma_f32_16x16x32_bf16 %2, a[212:215], a[8:11], %2\n\t"
    "ds_read_b64_tr_b16 a[196:197], %[vr1] offset:40960\n\t" "v_mfma_f32_16x16x32_bf16 %3, a[212:215], a[12:15], %3\n\t" "ds_read_b64_tr_b16 a[198:199], %[vr1] offset:43008\n\t" "s_waitcnt lgkmcnt(4)\n\t"
    "v_mfma_f32_16x16x32_bf16 %4, a[216:219], a[8:11], %4\n\t" "ds_read_b64_tr_b16 a[200:201], %[vr2] offset:40960\n\t" "s_add_u32 m0, %[sl1], 57344\n\t"
    "v_mfma_f32_16x16x32_bf16 %5, a[216:219], a[12:15], %5\n\t" "ds_read_b64_tr_b16 a[202:203], %[vr2] offset:43008\n\t" "global_load_lds_dwordx4 %[vb1], %[sb]\n\t" "v_add_u32 %[vb1], 0x10000, %[vb1]\n\t"
    "v_mfma_f32_16x16x32_bf16 %6, a[220:223], a[8:11], %6\n\t" "ds_read_b64_tr_b16 a[204:205], %[vr3] offset:40960\n\t" "v_mfma_f32_16x16x32_bf16 %7, a[220:223], a[12:15], %7\n\t"
    "ds_read_b64_tr_b16 a[206:207], %[vr3] offset:43008\n\t" "s_waitcnt lgkmcnt(4)\n\t" "s_waitcnt vmcnt(13)\n\t" "v_mfma_f32_16x16x32_bf16 v[224:227], a[192:195], a[16:19], v[224:227]\n\t"
    "ds_read_b64_tr_b16 a[208:209], %[vr12] offset:40960\n\t" "v_mfma_f32_16x16x32_bf16 v[240:243], a[192:195], a[20:23], v[240:243]\n\t" "ds_read_b64_tr_b16 a[210:211], %[vr12] offset:43008\n\t"
    "s_add_u32 s40, s40, 4096\n\t" "s_addc_u32 s41, s41, 0\n\t" "global_load_dwordx4 a[48:51], %[vx], s[40:41] offset:0 sc1\n\t" "v_mfma_f32_16x16x32_bf16 v[228:231], a[196:199], a[16:19], v[228:231]\n\t"
    "ds_read_b64_tr_b16 a[212:213], %[vr13] offset:40960\n\t" "v_mfma_f32_16x16x32_bf16 v[244:247], a[196:199], a[20:23], v[244:247]\n\t" "ds_read_b64_tr_b16 a[214:215], %[vr13] offset:43008\n\t"
    "s_waitcnt lgkmcnt(4)\n\t" "v_mfma_f32_16x16x32_bf16 v[232:235], a[200:203], a[16:19], v[232:235]\n\t" "ds_read_b64_tr_b16 a[216:217], %[vr14] offset:40960\n\t" "s_add_u32 m0, %[sl0], 65536\n\t"
    "v_mfma_f32_16x16x32_bf16 v[248:251], a[200:203], a[20:23], v[248:251]\n\t" "ds_read_b64_tr_b16 a[218:219], %[vr14] offset:43008\n\t" "global_load_lds_dwordx4 %[vb0], %[sb]\n\t"
    "v_add_u32 %[vb0], 0x10000, %[vb0]\n\t" "v_mfma_f32_16x16x32_bf16 v[236:239], a[204:207], a[16:19], v[236:239]\n\t" "ds_read_b64_tr_b16 a[220:221], %[vr15] offset:40960\n\t"
    "v_mfma_f32_16x16x32_bf16 v[252:255], a[204:207], a[20:23], v[252:255]\n\t" "ds_read_b64_tr_b16 a[222:223], %[vr15] offset:43008\n\t" "s_waitcnt lgkmcnt(4)\n\t"
    "v_mfma_f32_16x16x32_bf16 %0, a[208:211], a[16:19], %0\n\t" "ds_read_b64_tr_b16 a[192:193], %[vr8] offset:8192\n\t" "v_mfma_f32_16x16x32_bf16 %1, a[208:211], a[20:23], %1\n\t"
    "ds_read_b64_tr_b16 a[194:195], %[vr8] offset:10240\n\t" "global_load_dwordx4 a[52:55], %[vx], s[40:41] offset:1024 sc1\n\t" "v_mfma_f32_16x16x32_bf16 %2, a[212:215], a[16:19], %2\n\t"
    "ds_read_b64_tr_b16 a[196:197], %[vr9] offset:8192\n\t" "v_mfma_f32_16x16x32_bf16 %3, a[212:215], a[20:23], %3\n\t" "ds_read_b64_tr_b16 a[198:199], %[vr9] offset:10240\n\t" "s_waitcnt lgkmcnt(4)\n\t"
    "v_mfma_f32_16x16x32_bf16 %4, a[216:219], a[16:19], %4\n\t" "ds_read_b64_tr_b16 a[200:201], %[vr10] offset:8192\n\t" "s_add_u32 m0, %[sl1], 65536\n\t"
    "v_mfma_f32_16x16x32_bf16 %5, a[216:219], a[20:23], %5\n\t" "ds_read_b64_tr_b16 a[202:203], %[vr10] offset:10240\n\t" "global_load_lds_dwordx4 %[vb1], %[sb]\n\t"
    "v_add_u32 %[vb1], 0x10000, %[vb1]\n\t" "v_mfma_f32_16x16x32_bf16 %6, a[220:223], a[16:19], %6\n\t" "ds_read_b64_tr_b16 a[204:205], %[vr11] offset:8192\n\t"
    "v_mfma_f32_16x16x32_bf16 %7, a[220:223], a[20:23], %7\n\t" "ds_read_b64_tr_b16 a[206:207], %[vr11] offset:10240\n\t" "s_waitcnt lgkmcnt(4)\n\t" "s_waitcnt vmcnt(13)\n\t"
    "v_mfma_f32_16x16x32_bf16 v[224:227], a[192:195], a[24:27], v[224:227]\n\t" "ds_read_b64_tr_b16 a[208:209], %[vr20] offset:8192\n\t"
    "v_mfma_f32_16x16x32_bf16 v[240:243], a[192:195], a[28:31], v[240:243]\n\t" "ds_read_b64_tr_b16 a[210:211], %[vr20] offset:10240\n\t"
    "global_load_dwordx4 a[56:59], %[vx], s[40:41] offset:2048 sc1\n\t" "v_mfma_f32_16x16x32_bf16 v[228:231], a[196:199], a[24:27], v[228:231]\n\t" "ds_read_b64_tr_b16 a[212:213], %[vr21] offset:8192\n\t"
    "v_mfma_f32_16x16x32_bf16 v[244:247], a[196:199], a[28:31], v[244:247]\n\t" "ds_read_b64_tr_b16 a[214:215], %[vr21] offset:10240\n\t" "s_waitcnt lgkmcnt(4)\n\t"
    "v_mfma_f32_16x16x32_bf16 v[232:235], a[200:203], a[24:27], v[232:235]\n\t" "ds_read_b64_tr_b16 a[216:217], %[vr22] offset:8192\n\t" "s_add_u32 m0, %[sl0], 73728\n\t"
    "v_mfma_f32_16x16x32_bf16 v[248:251], a[200:203], a[28:31], v[248:251]\n\t" "ds_read_b64_tr_b16 a[218:219], %[vr22] offset:10240\n\t" "global_load_lds_dwordx4 %[vb0], %[sb]\n\t"
    "v_add_u32 %[vb0], 0x10000, %[vb0]\n\t" "v_mfma_f32_16x16x32_bf16 v[236:239], a[204:207], a[24:27], v[236:239]\n\t" "ds_read_b64_tr_b16 a[220:221], %[vr23] offset:8192\n\t"
    "v_mfma_f32_16x16x32_bf16 v[252:255], a[204:207], a[28:31], v[252:255]\n\t" "ds_read_b64_tr_b16 a[222:223], %[vr23] offset:10240\n\t" "s_waitcnt lgkmcnt(4)\n\t"
    "v_mfma_f32_16x16x32_bf16 %0, a[208:211], a[24:27], %0\n\t" "s_barrier\n\t" "ds_read_b64_tr_b16 a[192:193], %[vr8] offset:16384\n\t" "v_mfma_f32_16x16x32_bf16 %1, a[208:211], a[28:31], %1\n\t"
    "ds_read_b64_tr_b16 a[194:195], %[vr8] offset:18432\n\t" "global_load_dwordx4 a[60:63], %[vx], s[40:41] offset:3072 sc1\n\t" "v_mfma_f32_16x16x32_bf16 %2, a[212:215], a[24:27], %2\n\t"
    "ds_read_b64_tr_b16 a[196:197], %[vr9] offset:16384\n\t" "v_mfma_f32_16x16x32_bf16 %3, a[212:215], a[28:31], %3\n\t" "ds_read_b64_tr_b16 a[198:199], %[vr9] offset:18432\n\t" "s_waitcnt lgkmcnt(4)\n\t"
    "v_mfma_f32_16x16x32_bf16 %4, a[216:219], a[24:27], %4\n\t" "ds_read_b64_tr_b16 a[200:201], %[vr10] offset:16384\n\t" "s_add_u32 m0, %[sl1], 73728\n\t"
    "v_mfma_f32_16x16x32_bf16 %5, a[216:219], a[28:31], %5\n\t" "ds_read_b64_tr_b16 a[202:203], %[vr10] offset:18432\n\t" "global_load_lds_dwordx4 %[vb1], %[sb]\n\t"
    "v_add_u32 %[vb1], 0x10000, %[vb1]\n\t" "v_mfma_f32_16x16x32_bf16 %6, a[220:223], a[24:27], %6\n\t" "ds_read_b64_tr_b16 a[204:205], %[vr11] offset:16384\n\t"
    "v_mfma_f32_16x16x32_bf16 %7, a[220:223], a[28:31], %7\n\t" "ds_read_b64_tr_b16 a[206:207], %[vr11] offset:18432\n\t" "s_waitcnt lgkmcnt(4)\n\t" "s_waitcnt vmcnt(13)\n\t"
    "v_mfma_f32_16x16x32_bf16 v[224:227], a[192:195], a[32:35], v[224:227]\n\t" "ds_read_b64_tr_b16 a[208:209], %[vr20] offset:16384\n\t"
    "v_mfma_f32_16x16x32_bf16 v[240:243], a[192:195], a[36:39], v[240:243]\n\t" "ds_read_b64_tr_b16 a[210:211], %[vr20] offset:18432\n\t" "s_add_u32 s40, s40, 4096\n\t" "s_addc_u32 s41, s41, 0\n\t"
    "global_load_dwordx2 a[0:1], %[vo], s[40:41] offset:0 sc1\n\t" "v_mfma_f32_16x16x32_bf16 v[228:231], a[196:199], a[32:35], v[228:231]\n\t" "ds_read_b64_tr_b16 a[212:213], %[vr21] offset:16384\n\t"
    "v_mfma_f32_16x16x32_bf16 v[244:247], a[196:199], a[36:39], v[244:247]\n\t" "ds_read_b64_tr_b16 a[214:215], %[vr21] offset:18432\n\t" "global_load_dwordx2 a[2:3], %[vo], s[40:41] offset:1024 sc1\n\t"
    "s_waitcnt lgkmcnt(4)\n\t" "v_mfma_f32_16x16x32_bf16 v[232:235], a[200:203], a[32:35], v[232:235]\n\t" "ds_read_b64_tr_b16 a[216:217], %[vr22] offset:16384\n\t"
    "v_mfma_f32_16x16x32_bf16 v[248:251], a[200:203], a[36:39], v[248:251]\n\t" "ds_read_b64_tr_b16 a[218:219], %[vr22] offset:18432\n\t" "s_add_u32 m0, %[sl0], 81920\n\t"
    "v_mfma_f32_16x16x32_bf16 v[236:239], a[204:207], a[32:35], v[236:239]\n\t" "ds_read_b64_tr_b16 a[220:221], %[vr23] offset:16384\n\t" "global_load_lds_dwordx4 %[vb0], %[sb]\n\t"
    "v_add_u32 %[vb0], 0x10000, %[vb0]\n\t" "v_mfma_f32_16x16x32_bf16 v[252:255], a[204:207], a[36:39], v[252:255]\n\t" "ds_read_b64_tr_b16 a[222:223], %[vr23] offset:18432\n\t" "s_waitcnt lgkmcnt(4)\n\t"
    "v_mfma_f32_16x16x32_bf16 %0, a[208:211], a[32:35], %0\n\t" "ds_read_b64_tr_b16 a[192:193], %[vr8] offset:24576\n\t" "global_load_dwordx2 a[4:5], %[vo], s[40:41] offset:512 sc1\n\t"
    "v_mfma_f32_16x16x32_bf16 %1, a[208:211], a[36:39], %1\n\t" "ds_read_b64_tr_b16 a[194:195], %[vr8] offset:26624\n\t" "v_mfma_f32_16x16x32_bf16 %2, a[212:215], a[32:35], %2\n\t"
    "ds_read_b64_tr_b16 a[196:197], %[vr9] offset:24576\n\t" "v_mfma_f32_16x16x32_bf16 %3, a[212:215], a[36:39], %3\n\t" "ds_read_b64_tr_b16 a[198:199], %[vr9] offset:26624\n\t"
    "global_load_dwordx2 a[6:7], %[vo], s[40:41] offset:1536 sc1\n\t" "s_waitcnt lgkmcnt(4)\n\t" "v_mfma_f32_16x16x32_bf16 %4, a[216:219], a[32:35], %4\n\t"
    "ds_read_b64_tr_b16 a[200:201], %[vr10] offset:24576\n\t" "s_add_u32 m0, %[sl1], 81920\n\t" "v_mfma_f32_16x16x32_bf16 %5, a[216:219], a[36:39], %5\n\t"
    "ds_read_b64_tr_b16 a[202:203], %[vr10] offset:26624\n\t" "global_load_lds_dwordx4 %[vb1], %[sb]\n\t" "v_add_u32 %[vb1], 0x10000, %[vb1]\n\t"
    "v_mfma_f32_16x16x32_bf16 %6, a[220:223], a[32:35], %6\n\t" "ds_read_b64_tr_b16 a[204:205], %[vr11] offset:24576\n\t" "v_mfma_f32_16x16x32_bf16 %7, a[220:223], a[36:39], %7\n\t"
    "ds_read_b64_tr_b16 a[206:207], %[vr11] offset:26624\n\t" "global_load_dwordx4 %[bv0], %[vx], %[sx] offset:0\n\t" "s_waitcnt lgkmcnt(4)\n\t" "s_waitcnt vmcnt(16)\n\t"
    "v_mfma_f32_16x16x32_bf16 v[224:227], a[192:195], a[40:43], v[224:227]\n\t" "ds_read_b64_tr_b16 a[208:209], %[vr20] offset:24576\n\t"
    "v_mfma_f32_16x16x32_bf16 v[240:243], a[192:195], a[44:47], v[240:243]\n\t" "ds_read_b64_tr_b16 a[210:211], %[vr20] offset:26624\n\t" "global_load_dwordx4 a[8:11], %[vx], s[40:41] offset:2048 sc1\n\t"
    "v_mfma_f32_16x16x32_bf16 v[228:231], a[196:199], a[40:43], v[228:231]\n\t" "ds_read_b64_tr_b16 a[212:213], %[vr21] offset:24576\n\t"
    "v_mfma_f32_16x16x32_bf16 v[244:247], a[196:199], a[44:47], v[244:247]\n\t" "ds_read_b64_tr_b16 a[214:215], %[vr21] offset:26624\n\t" "s_waitcnt lgkmcnt(4)\n\t"
    "v_mfma_f32_16x16x32_bf16 v[232:235], a[200:203], a[40:43], v[232:235]\n\t" "ds_read_b64_tr_b16 a[216:217], %[vr22] offset:24576\n\t" "s_add_u32 m0, %[sl0], 90112\n\t"
    "v_mfma_f32_16x16x32_bf16 v[248:251], a[200:203], a[44:47], v[248:251]\n\t" "ds_read_b64_tr_b16 a[218:219], %[vr22] offset:26624\n\t" "global_load_lds_dwordx4 %[vb0], %[sb]\n\t"
    "v_add_u32 %[vb0], 0x10000, %[vb0]\n\t" "v_mfma_f32_16x16x32_bf16 v[236:239], a[204:207], a[40:43], v[236:239]\n\t" "ds_read_b64_tr_b16 a[220:221], %[vr23] offset:24576\n\t"
    "v_mfma_f32_16x16x32_bf16 v[252:255], a[204:207], a[44:47], v[252:255]\n\t" "ds_read_b64_tr_b16 a[222:223], %[vr23] offset:26624\n\t" "s_waitcnt lgkmcnt(4)\n\t"
    "v_mfma_f32_16x16x32_bf16 %0, a[208:211], a[40:43], %0\n\t" "s_barrier\n\t" "ds_read_b64_tr_b16 a[192:193], %[vr0] offset:0\n\t" "v_mfma_f32_16x16x32_bf16 %1, a[208:211], a[44:47], %1\n\t"
    "ds_read_b64_tr_b16 a[194:195], %[vr0] offset:2048\n\t" "global_load_dwordx4 a[12:15], %[vx], s[40:41] offset:3072 sc1\n\t" "v_mfma_f32_16x16x32_bf16 %2, a[212:215], a[40:43], %2\n\t"
    "ds_read_b64_tr_b16 a[196:197], %[vr1] offset:0\n\t" "v_mfma_f32_16x16x32_bf16 %3, a[212:215], a[44:47], %3\n\t" "ds_read_b64_tr_b16 a[198:199], %[vr1] offset:2048\n\t" "s_waitcnt lgkmcnt(4)\n\t"
    "v_mfma_f32_16x16x32_bf16 %4, a[216:219], a[40:43], %4\n\t" "ds_read_b64_tr_b16 a[200:201], %[vr2] offset:0\n\t" "s_add_u32 m0, %[sl1], 90112\n\t"
    "v_mfma_f32_16x16x32_bf16 %5, a[216:219], a[44:47], %5\n\t" "ds_read_b64_tr_b16 a[202:203], %[vr2] offset:2048\n\t" "global_load_lds_dwordx4 %[vb1], %[sb]\n\t" "v_add_u32 %[vb1], 0x10000, %[vb1]\n\t"
    "v_mfma_f32_16x16x32_bf16 %6, a[220:223], a[40:43], %6\n\t" "ds_read_b64_tr_b16 a[204:205], %[vr3] offset:0\n\t" "v_mfma_f32_16x16x32_bf16 %7, a[220:223], a[44:47], %7\n\t"
    "ds_read_b64_tr_b16 a[206:207], %[vr3] offset:2048\n\t" "global_load_dwordx4 %[bv1], %[vx], %[sx] offset:1024\n\t" "s_waitcnt lgkmcnt(4)\n\t" "s_waitcnt vmcnt(17)\n\t"
    "v_mfma_f32_16x16x32_bf16 v[224:227], a[192:195], a[48:51], v[224:227]\n\t" "ds_read_b64_tr_b16 a[208:209], %[vr12] offset:0\n\t"
    "v_mfma_f32_16x16x32_bf16 v[240:243], a[192:195], a[52:55], v[240:243]\n\t" "ds_read_b64_tr_b16 a[210:211], %[vr12] offset:2048\n\t" "s_add_u32 s40, s40, 4096\n\t" "s_addc_u32 s41, s41, 0\n\t"
    "global_load_dwordx4 a[16:19], %[vx], s[40:41] offset:0 sc1\n\t" "v_mfma_f32_16x16x32_bf16 v[228:231], a[196:199], a[48:51], v[228:231]\n\t" "ds_read_b64_tr_b16 a[212:213], %[vr13] offset:0\n\t"
    "v_mfma_f32_16x16x32_bf16 v[244:247], a[196:199], a[52:55], v[244:247]\n\t" "ds_read_b64_tr_b16 a[214:215], %[vr13] offset:2048\n\t" "s_waitcnt lgkmcnt(4)\n\t"
    "v_mfma_f32_16x16x32_bf16 v[232:235], a[200:203], a[48:51], v[232:235]\n\t" "ds_read_b64_tr_b16 a[216:217], %[vr14] offset:0\n\t" "s_add_u32 m0, %[sl0], 98304\n\t"
    "v_mfma_f32_16x16x32_bf16 v[248:251], a[200:203], a[52:55], v[248:251]\n\t" "ds_read_b64_tr_b16 a[218:219], %[vr14] offset:2048\n\t" "global_load_lds_dwordx4 %[vb0], %[sb]\n\t"
    "v_add_u32 %[vb0], 0x10000, %[vb0]\n\t" "v_mfma_f32_16x16x32_bf16 v[236:239], a[204:207], a[48:51], v[236:239]\n\t" "ds_read_b64_tr_b16 a[220:221], %[vr15] offset:0\n\t"
    "v_mfma_f32_16x16x32_bf16 v[252:255], a[204:207], a[52:55], v[252:255]\n\t" "ds_read_b64_tr_b16 a[222:223], %[vr15] offset:2048\n\t" "s_waitcnt lgkmcnt(4)\n\t"
    "v_mfma_f32_16x16x32_bf16 %0, a[208:211], a[48:51], %0\n\t" "ds_read_b64_tr_b16 a[192:193], %[vr0] offset:8192\n\t" "v_mfma_f32_16x16x32_bf16 %1, a[208:211], a[52:55], %1\n\t"
    "ds_read_b64_tr_b16 a[194:195], %[vr0] offset:10240\n\t" "global_load_dwordx4 a[20:23], %[vx], s[40:41] offset:1024 sc1\n\t" "v_mfma_f32_16x16x32_bf16 %2, a[212:215], a[48:51], %2\n\t"
    "ds_read_b64_tr_b16 a[196:197], %[vr1] offset:8192\n\t" "v_mfma_f32_16x16x32_bf16 %3, a[212:215], a[52:55], %3\n\t" "ds_read_b64_tr_b16 a[198:199], %[vr1] offset:10240\n\t" "s_waitcnt lgkmcnt(4)\n\t"
    "v_mfma_f32_16x16x32_bf16 %4, a[216:219], a[48:51], %4\n\t" "ds_read_b64_tr_b16 a[200:201], %[vr2] offset:8192\n\t" "s_add_u32 m0, %[sl1], 98304\n\t"
    "v_mfma_f32_16x16x32_bf16 %5, a[216:219], a[52:55], %5\n\t" "ds_read_b64_tr_b16 a[202:203], %[vr2] offset:10240\n\t" "global_load_lds_dwordx4 %[vb1], %[sb]\n\t" "v_add_u32 %[vb1], 0x10000, %[vb1]\n\t"
    "v_mfma_f32_16x16x32_bf16 %6, a[220:223], a[48:51], %6\n\t" "ds_read_b64_tr_b16 a[204:205], %[vr3] offset:8192\n\t" "v_mfma_f32_16x16x32_bf16 %7, a[220:223], a[52:55], %7\n\t"
    "ds_read_b64_tr_b16 a[206:207], %[vr3] offset:10240\n\t" "global_load_dwordx4 %[bv2], %[vx], %[sx] offset:2048\n\t" "s_waitcnt lgkmcnt(4)\n\t" "s_waitcnt vmcnt(18)\n\t"
    "v_mfma_f32_16x16x32_bf16 v[224:227], a[192:195], a[56:59], v[224:227]\n\t" "ds_read_b64_tr_b16 a[208:209], %[vr12] offset:8192\n\t"
    "v_mfma_f32_16x16x32_bf16 v[240:243], a[192:195], a[60:63], v[240:243]\n\t" "ds_read_b64_tr_b16 a[210:211], %[vr12] offset:10240\n\t"
    "global_load_dwordx4 a[24:27], %[vx], s[40:41] offset:2048 sc1\n\t" "v_mfma_f32_16x16x32_bf16 v[228:231], a[196:199], a[56:59], v[228:231]\n\t" "ds_read_b64_tr_b16 a[212:213], %[vr13] offset:8192\n\t"
    "v_mfma_f32_16x16x32_bf16 v[244:247], a[196:199], a[60:63], v[244:247]\n\t" "ds_read_b64_tr_b16 a[214:215], %[vr13] offset:10240\n\t" "s_waitcnt lgkmcnt(4)\n\t"
    "v_mfma_f32_16x16x32_bf16 v[232:235], a[200:203], a[56:59], v[232:235]\n\t" "ds_read_b64_tr_b16 a[216:217], %[vr14] offset:8192\n\t" "s_add_u32 m0, %[sl0], 106496\n\t"
    "v_mfma_f32_16x16x32_bf16 v[248:251], a[200:203], a[60:63], v[248:251]\n\t" "ds_read_b64_tr_b16 a[218:219], %[vr14] offset:10240\n\t" "global_load_lds_dwordx4 %[vb0], %[sb]\n\t"
    "v_add_u32 %[vb0], 0x10000, %[vb0]\n\t" "v_mfma_f32_16x16x32_bf16 v[236:239], a[204:207], a[56:59], v[236:239]\n\t" "ds_read_b64_tr_b16 a[220:221], %[vr15] offset:8192\n\t"
    "v_mfma_f32_16x16x32_bf16 v[252:255], a[204:207], a[60:63], v[252:255]\n\t" "ds_read_b64_tr_b16 a[222:223], %[vr15] offset:10240\n\t" "s_waitcnt lgkmcnt(4)\n\t"
    "v_mfma_f32_16x16x32_bf16 %0, a[208:211], a[56:59], %0\n\t" "s_barrier\n\t" "ds_read_b64_tr_b16 a[192:193], %[vr0] offset:49152\n\t" "v_mfma_f32_16x16x32_bf16 %1, a[208:211], a[60:63], %1\n\t"
    "ds_read_b64_tr_b16 a[194:195], %[vr0] offset:51200\n\t" "global_load_dwordx4 a[28:31], %[vx], s[40:41] offset:3072 sc1\n\t" "v_mfma_f32_16x16x32_bf16 %2, a[212:215], a[56:59], %2\n\t"
    "ds_read_b64_tr_b16 a[196:197], %[vr1] offset:49152\n\t" "v_mfma_f32_16x16x32_bf16 %3, a[212:215], a[60:63], %3\n\t" "ds_read_b64_tr_b16 a[198:199], %[vr1] offset:51200\n\t" "s_waitcnt lgkmcnt(4)\n\t"
    "v_mfma_f32_16x16x32_bf16 %4, a[216:219], a[56:59], %4\n\t" "ds_read_b64_tr_b16 a[200:201], %[vr2] offset:49152\n\t" "s_add_u32 m0, %[sl1], 106496\n\t"
    "v_mfma_f32_16x16x32_bf16 %5, a[216:219], a[60:63], %5\n\t" "ds_read_b64_tr_b16 a[202:203], %[vr2] offset:51200\n\t" "global_load_lds_dwordx4 %[vb1], %[sb]\n\t" "v_add_u32 %[vb1], 0x10000, %[vb1]\n\t"
    "v_mfma_f32_16x16x32_bf16 %6, a[220:223], a[56:59], %6\n\t" "ds_read_b64_tr_b16 a[204:205], %[vr3] offset:49152\n\t" "v_mfma_f32_16x16x32_bf16 %7, a[220:223], a[60:63], %7\n\t"
    "ds_read_b64_tr_b16 a[206:207], %[vr3] offset:51200\n\t" "global_load_dwordx4 %[bv3], %[vx], %[sx] offset:3072\n\t" "s_waitcnt lgkmcnt(4)\n\t" "s_waitcnt vmcnt(17)\n\t"
    "v_mfma_f32_16x16x32_bf16 v[224:227], a[192:195], a[0:3], v[224:227]\n\t" "ds_read_b64_tr_b16 a[208:209], %[vr12] offset:49152\n\t"
    "v_mfma_f32_16x16x32_bf16 v[240:243], a[192:195], a[4:7], v[240:243]\n\t" "ds_read_b64_tr_b16 a[210:211], %[vr12] offset:51200\n\t" "s_add_u32 s40, s40, 4096\n\t" "s_addc_u32 s41, s41, 0\n\t"
    "global_load_dwordx4 a[32:35], %[vx], s[40:41] offset:0 sc1\n\t" "v_mfma_f32_16x16x32_bf16 v[228:231], a[196:199], a[0:3], v[228:231]\n\t" "ds_read_b64_tr_b16 a[212:213], %[vr13] offset:49152\n\t"
    "v_mfma_f32_16x16x32_bf16 v[244:247], a[196:199], a[4:7], v[244:247]\n\t" "ds_read_b64_tr_b16 a[214:215], %[vr13] offset:51200\n\t" "s_waitcnt lgkmcnt(4)\n\t"
    "v_mfma_f32_16x16x32_bf16 v[232:235], a[200:203], a[0:3], v[232:235]\n\t" "ds_read_b64_tr_b16 a[216:217], %[vr14] offset:49152\n\t" "s_add_u32 m0, %[sl0], 24576\n\t"
    "v_mfma_f32_16x16x32_bf16 v[248:251], a[200:203], a[4:7], v[248:251]\n\t" "ds_read_b64_tr_b16 a[218:219], %[vr14] offset:51200\n\t" "global_load_lds_dwordx4 %[vb0], %[sb]\n\t"
    "v_add_u32 %[vb0], 0x10000, %[vb0]\n\t" "v_mfma_f32_16x16x32_bf16 v[236:239], a[204:207], a[0:3], v[236:239]\n\t" "ds_read_b64_tr_b16 a[220:221], %[vr15] offset:49152\n\t"
    "v_mfma_f32_16x16x32_bf16 v[252:255], a[204:207], a[4:7], v[252:255]\n\t" "ds_read_b64_tr_b16 a[222:223], %[vr15] offset:51200\n\t" "s_waitcnt lgkmcnt(4)\n\t"
    "v_mfma_f32_16x16x32_bf16 %0, a[208:211], a[0:3], %0\n\t" "ds_read_b64_tr_b16 a[192:193], %[vr0] offset:57344\n\t" "v_mfma_f32_16x16x32_bf16 %1, a[208:211], a[4:7], %1\n\t"
    "ds_read_b64_tr_b16 a[194:195], %[vr0] offset:59392\n\t" "global_load_dwordx4 a[36:39], %[vx], s[40:41] offset:1024 sc1\n\t" "v_mfma_f32_16x16x32_bf16 %2, a[212:215], a[0:3], %2\n\t"
    "ds_read_b64_tr_b16 a[196:197], %[vr1] offset:57344\n\t" "v_mfma_f32_16x16x32_bf16 %3, a[212:215], a[4:7], %3\n\t" "ds_read_b64_tr_b16 a[198:199], %[vr1] offset:59392\n\t" "s_waitcnt lgkmcnt(4)\n\t"
    "v_mfma_f32_16x16x32_bf16 %4, a[216:219], a[0:3], %4\n\t" "ds_read_b64_tr_b16 a[200:201], %[vr2] offset:57344\n\t" "s_add_u32 m0, %[sl1], 24576\n\t"
    "v_mfma_f32_16x16x32_bf16 %5, a[216:219], a[4:7], %5\n\t" "ds_read_b64_tr_b16 a[202:203], %[vr2] offset:59392\n\t" "global_load_lds_dwordx4 %[vb1], %[sb]\n\t" "v_add_u32 %[vb1], 0x10000, %[vb1]\n\t"
    "v_mfma_f32_16x16x32_bf16 %6, a[220:223], a[0:3], %6\n\t" "ds_read_b64_tr_b16 a[204:205], %[vr3] offset:57344\n\t" "v_mfma_f32_16x16x32_bf16 %7, a[220:223], a[4:7], %7\n\t"
    "ds_read_b64_tr_b16 a[206:207], %[vr3] offset:59392\n\t" "global_load_dwordx4 %[gv0], %[vg], %[sg] offset:0 sc1\n\t" "s_waitcnt lgkmcnt(4)\n\t" "s_waitcnt vmcnt(17)\n\t"
    "v_mfma_f32_16x16x32_bf16 v[224:227], a[192:195], a[8:11], v[224:227]\n\t" "ds_read_b64_tr_b16 a[208:209], %[vr12] offset:57344\n\t"
    "v_mfma_f32_16x16x32_bf16 v[240:243], a[192:195], a[12:15], v[240:243]\n\t" "ds_read_b64_tr_b16 a[210:211], %[vr12] offset:59392\n\t"
    "global_load_dwordx4 a[40:43], %[vx], s[40:41] offset:2048 sc1\n\t" "v_mfma_f32_16x16x32_bf16 v[228:231], a[196:199], a[8:11], v[228:231]\n\t" "ds_read_b64_tr_b16 a[212:213], %[vr13] offset:57344\n\t"
    "v_mfma_f32_16x16x32_bf16 v[244:247], a[196:199], a[12:15], v[244:247]\n\t" "ds_read_b64_tr_b16 a[214:215], %[vr13] offset:59392\n\t" "s_waitcnt lgkmcnt(4)\n\t"
    "v_mfma_f32_16x16x32_bf16 v[232:235], a[200:203], a[8:11], v[232:235]\n\t" "ds_read_b64_tr_b16 a[216:217], %[vr14] offset:57344\n\t" "s_add_u32 m0, %[sl0], 32768\n\t"
    "v_mfma_f32_16x16x32_bf16 v[248:251], a[200:203], a[12:15], v[248:251]\n\t" "ds_read_b64_tr_b16 a[218:219], %[vr14] offset:59392\n\t" "global_load_lds_dwordx4 %[vb0], %[sb]\n\t"
    "v_add_u32 %[vb0], 0x10000, %[vb0]\n\t" "v_mfma_f32_16x16x32_bf16 v[236:239], a[204:207], a[8:11], v[236:239]\n\t" "ds_read_b64_tr_b16 a[220:221], %[vr15] offset:57344\n\t"
    "v_mfma_f32_16x16x32_bf16 v[252:255], a[204:207], a[12:15], v[252:255]\n\t" "ds_read_b64_tr_b16 a[222:223], %[vr15] offset:59392\n\t" "s_waitcnt lgkmcnt(4)\n\t"
    "v_mfma_f32_16x16x32_bf16 %0, a[208:211], a[8:11], %0\n\t" "s_barrier\n\t" "ds_read_b64_tr_b16 a[192:193], %[vr4] offset:0\n\t" "v_mfma_f32_16x16x32_bf16 %1, a[208:211], a[12:15], %1\n\t"
    "ds_read_b64_tr_b16 a[194:195], %[vr4] offset:2048\n\t" "global_load_dwordx4 a[44:47], %[vx], s[40:41] offset:3072 sc1\n\t" "v_mfma_f32_16x16x32_bf16 %2, a[212:215], a[8:11], %2\n\t"
    "ds_read_b64_tr_b16 a[196:197], %[vr5] offset:0\n\t" "v_mfma_f32_16x16x32_bf16 %3, a[212:215], a[12:15], %3\n\t" "ds_read_b64_tr_b16 a[198:199], %[vr5] offset:2048\n\t" "s_waitcnt lgkmcnt(4)\n\t"
    "v_mfma_f32_16x16x32_bf16 %4, a[216:219], a[8:11], %4\n\t" "ds_read_b64_tr_b16 a[200:201], %[vr6] offset:0\n\t" "s_add_u32 m0, %[sl1], 32768\n\t"
    "v_mfma_f32_16x16x32_bf16 %5, a[216:219], a[12:15], %5\n\t" "ds_read_b64_tr_b16 a[202:203], %[vr6] offset:2048\n\t" "global_load_lds_dwordx4 %[vb1], %[sb]\n\t" "v_add_u32 %[vb1], 0x10000, %[vb1]\n\t"
    "v_mfma_f32_16x16x32_bf16 %6, a[220:223], a[8:11], %6\n\t" "ds_read_b64_tr_b16 a[204:205], %[vr7] offset:0\n\t" "v_mfma_f32_16x16x32_bf16 %7, a[220:223], a[12:15], %7\n\t"
    "ds_read_b64_tr_b16 a[206:207], %[vr7] offset:2048\n\t" "global_load_dwordx4 %[gv1], %[vg], %[sg] offset:64 sc1\n\t" "s_waitcnt lgkmcnt(4)\n\t" "s_waitcnt vmcnt(17)\n\t"
    "v_mfma_f32_16x16x32_bf16 v[224:227], a[192:195], a[16:19], v[224:227]\n\t" "ds_read_b64_tr_b16 a[208:209], %[vr16] offset:0\n\t"
    "v_mfma_f32_16x16x32_bf16 v[240:243], a[192:195], a[20:23], v[240:243]\n\t" "ds_read_b64_tr_b16 a[210:211], %[vr16] offset:2048\n\t" "s_add_u32 s40, s40, 4096\n\t" "s_addc_u32 s41, s41, 0\n\t"
    "global_load_dwordx4 a[48:51], %[vx], s[40:41] offset:0 sc1\n\t" "v_mfma_f32_16x16x32_bf16 v[228:231], a[196:199], a[16:19], v[228:231]\n\t" "ds_read_b64_tr_b16 a[212:213], %[vr17] offset:0\n\t"
    "v_mfma_f32_16x16x32_bf16 v[244:247], a[196:199], a[20:23], v[244:247]\n\t" "ds_read_b64_tr_b16 a[214:215], %[vr17] offset:2048\n\t" "s_waitcnt lgkmcnt(4)\n\t"
    "v_mfma_f32_16x16x32_bf16 v[232:235], a[200:203], a[16:19], v[232:235]\n\t" "ds_read_b64_tr_b16 a[216:217], %[vr18] offset:0\n\t" "s_add_u32 m0, %[sl0], 40960\n\t"
    "v_mfma_f32_16x16x32_bf16 v[248:251], a[200:203], a[20:23], v[248:251]\n\t" "ds_read_b64_tr_b16 a[218:219], %[vr18] offset:2048\n\t" "global_load_lds_dwordx4 %[vb0], %[sb]\n\t"
    "v_add_u32 %[vb0], 0x10000, %[vb0]\n\t" "v_mfma_f32_16x16x32_bf16 v[236:239], a[204:207], a[16:19], v[236:239]\n\t" "ds_read_b64_tr_b16 a[220:221], %[vr19] offset:0\n\t"
    "v_mfma_f32_16x16x32_bf16 v[252:255], a[204:207], a[20:23], v[252:255]\n\t" "ds_read_b64_tr_b16 a[222:223], %[vr19] offset:2048\n\t" "s_waitcnt lgkmcnt(4)\n\t"
    "v_mfma_f32_16x16x32_bf16 %0, a[208:211], a[16:19], %0\n\t" "ds_read_b64_tr_b16 a[192:193], %[vr4] offset:8192\n\t" "v_mfma_f32_16x16x32_bf16 %1, a[208:211], a[20:23], %1\n\t"
    "ds_read_b64_tr_b16 a[194:195], %[vr4] offset:10240\n\t" "global_load_dwordx4 a[52:55], %[vx], s[40:41] offset:1024 sc1\n\t" "v_mfma_f32_16x16x32_bf16 %2, a[212:215], a[16:19], %2\n\t"
    "ds_read_b64_tr_b16 a[196:197], %[vr5] offset:8192\n\t" "v_mfma_f32_16x16x32_bf16 %3, a[212:215], a[20:23], %3\n\t" "ds_read_b64_tr_b16 a[198:199], %[vr5] offset:10240\n\t" "s_waitcnt lgkmcnt(4)\n\t"
    "v_mfma_f32_16x16x32_bf16 %4, a[216:219], a[16:19], %4\n\t" "ds_read_b64_tr_b16 a[200:201], %[vr6] offset:8192\n\t" "s_add_u32 m0, %[sl1], 40960\n\t"
    "v_mfma_f32_16x16x32_bf16 %5, a[216:219], a[20:23], %5\n\t" "ds_read_b64_tr_b16 a[202:203], %[vr6] offset:10240\n\t" "global_load_lds_dwordx4 %[vb1], %[sb]\n\t" "v_add_u32 %[vb1], 0x10000, %[vb1]\n\t"
    "v_mfma_f32_16x16x32_bf16 %6, a[220:223], a[16:19], %6\n\t" "ds_read_b64_tr_b16 a[204:205], %[vr7] offset:8192\n\t" "v_mfma_f32_16x16x32_bf16 %7, a[220:223], a[20:23], %7\n\t"
    "ds_read_b64_tr_b16 a[206:207], %[vr7] offset:10240\n\t" "global_load_dwordx4 %[gv2], %[vg], %[sg] offset:128 sc1\n\t" "s_waitcnt lgkmcnt(4)\n\t" "s_waitcnt vmcnt(17)\n\t"
    "v_mfma_f32_16x16x32_bf16 v[224:227], a[192:195], a[24:27], v[224:227]\n\t" "ds_read_b64_tr_b16 a[208:209], %[vr16] offset:8192\n\t"
    "v_mfma_f32_16x16x32_bf16 v[240:243], a[192:195], a[28:31], v[240:243]\n\t" "ds_read_b64_tr_b16 a[210:211], %[vr16] offset:10240\n\t"
    "global_load_dwordx2 a[56:57], %[vo], s[40:41] offset:2048 sc1\n\t" "v_mfma_f32_16x16x32_bf16 v[228:231], a[196:199], a[24:27], v[228:231]\n\t" "ds_read_b64_tr_b16 a[212:213], %[vr17] offset:8192\n\t"
    "v_mfma_f32_16x16x32_bf16 v[244:247], a[196:199], a[28:31], v[244:247]\n\t" "ds_read_b64_tr_b16 a[214:215], %[vr17] offset:10240\n\t"
    "global_load_dwordx2 a[58:59], %[vo], s[40:41] offset:3072 sc1\n\t" "s_waitcnt lgkmcnt(4)\n\t" "v_mfma_f32_16x16x32_bf16 v[232:235], a[200:203], a[24:27], v[232:235]\n\t"
    "ds_read_b64_tr_b16 a[216:217], %[vr18] offset:8192\n\t" "v_mfma_f32_16x16x32_bf16 v[248:251], a[200:203], a[28:31], v[248:251]\n\t" "ds_read_b64_tr_b16 a[218:219], %[vr18] offset:10240\n\t"
    "s_add_u32 m0, %[sl0], 139264\n\t" "v_mfma_f32_16x16x32_bf16 v[236:239], a[204:207], a[24:27], v[236:239]\n\t" "ds_read_b64_tr_b16 a[220:221], %[vr19] offset:8192\n\t"
    "global_load_lds_dwordx4 %[vb0], %[sb]\n\t" "v_add_u32 %[vb0], 0x10000, %[vb0]\n\t" "v_mfma_f32_16x16x32_bf16 v[252:255], a[204:207], a[28:31], v[252:255]\n\t"
    "ds_read_b64_tr_b16 a[222:223], %[vr19] offset:10240\n\t" "s_waitcnt lgkmcnt(4)\n\t" "v_mfma_f32_16x16x32_bf16 %0, a[208:211], a[24:27], %0\n\t" "s_barrier\n\t"
    "ds_read_b64_tr_b16 a[192:193], %[vr4] offset:16384\n\t" "global_load_dwordx2 a[60:61], %[vo], s[40:41] offset:2560 sc1\n\t" "v_mfma_f32_16x16x32_bf16 %1, a[208:211], a[28:31], %1\n\t"
    "ds_read_b64_tr_b16 a[194:195], %[vr4] offset:18432\n\t" "v_mfma_f32_16x16x32_bf16 %2, a[212:215], a[24:27], %2\n\t" "ds_read_b64_tr_b16 a[196:197], %[vr5] offset:16384\n\t"
    "v_mfma_f32_16x16x32_bf16 %3, a[212:215], a[28:31], %3\n\t" "ds_read_b64_tr_b16 a[198:199], %[vr5] offset:18432\n\t" "global_load_dwordx2 a[62:63], %[vo], s[40:41] offset:3584 sc1\n\t"
    "s_waitcnt lgkmcnt(4)\n\t" "v_mfma_f32_16x16x32_bf16 %4, a[216:219], a[24:27], %4\n\t" "ds_read_b64_tr_b16 a[200:201], %[vr6] offset:16384\n\t" "s_add_u32 m0, %[sl1], 139264\n\t"
    "v_mfma_f32_16x16x32_bf16 %5, a[216:219], a[28:31], %5\n\t" "ds_read_b64_tr_b16 a[202:203], %[vr6] offset:18432\n\t" "global_load_lds_dwordx4 %[vb1], %[sb]\n\t" "v_add_u32 %[vb1], 0x10000, %[vb1]\n\t"
    "v_mfma_f32_16x16x32_bf16 %6, a[220:223], a[24:27], %6\n\t" "ds_read_b64_tr_b16 a[204:205], %[vr7] offset:16384\n\t" "v_mfma_f32_16x16x32_bf16 %7, a[220:223], a[28:31], %7\n\t"
    "ds_read_b64_tr_b16 a[206:207], %[vr7] offset:18432\n\t" "global_load_dwordx4 %[gv3], %[vg], %[sg] offset:192 sc1\n\t" "s_waitcnt lgkmcnt(4)\n\t" "s_waitcnt vmcnt(19)\n\t"
    "v_mfma_f32_16x16x32_bf16 v[224:227], a[192:195], a[32:35], v[224:227]\n\t" "ds_read_b64_tr_b16 a[208:209], %[vr16] offset:16384\n\t"
    "v_mfma_f32_16x16x32_bf16 v[240:243], a[192:195], a[36:39], v[240:243]\n\t" "ds_read_b64_tr_b16 a[210:211], %[vr16] offset:18432\n\t" "s_add_u32 s40, s40, 4096\n\t" "s_addc_u32 s41, s41, 0\n\t"
    "global_load_dwordx4 a[0:3], %[vx], s[40:41] offset:0 sc1\n\t" "v_mfma_f32_16x16x32_bf16 v[228:231], a[196:199], a[32:35], v[228:231]\n\t" "ds_read_b64_tr_b16 a[212:213], %[vr17] offset:16384\n\t"
    "v_mfma_f32_16x16x32_bf16 v[244:247], a[196:199], a[36:39], v[244:247]\n\t" "ds_read_b64_tr_b16 a[214:215], %[vr17] offset:18432\n\t" "s_waitcnt lgkmcnt(4)\n\t"
    "v_mfma_f32_16x16x32_bf16 v[232:235], a[200:203], a[32:35], v[232:235]\n\t" "ds_read_b64_tr_b16 a[216:217], %[vr18] offset:16384\n\t" "s_add_u32 m0, %[sl0], 147456\n\t"
    "v_mfma_f32_16x16x32_bf16 v[248:251], a[200:203], a[36:39], v[248:251]\n\t" "ds_read_b64_tr_b16 a[218:219], %[vr18] offset:18432\n\t" "global_load_lds_dwordx4 %[vb0], %[sb]\n\t"
    "v_add_u32 %[vb0], 0x10000, %[vb0]\n\t" "v_mfma_f32_16x16x32_bf16 v[236:239], a[204:207], a[32:35], v[236:239]\n\t" "ds_read_b64_tr_b16 a[220:221], %[vr19] offset:16384\n\t"
    "v_mfma_f32_16x16x32_bf16 v[252:255], a[204:207], a[36:39], v[252:255]\n\t" "ds_read_b64_tr_b16 a[222:223], %[vr19] offset:18432\n\t" "s_waitcnt lgkmcnt(4)\n\t"
    "v_mfma_f32_16x16x32_bf16 %0, a[208:211], a[32:35], %0\n\t" "ds_read_b64_tr_b16 a[192:193], %[vr4] offset:24576\n\t" "v_mfma_f32_16x16x32_bf16 %1, a[208:211], a[36:39], %1\n\t"
    "ds_read_b64_tr_b16 a[194:195], %[vr4] offset:26624\n\t" "global_load_dwordx4 a[4:7], %[vx], s[40:41] offset:1024 sc1\n\t" "v_mfma_f32_16x16x32_bf16 %2, a[212:215], a[32:35], %2\n\t"
    "ds_read_b64_tr_b16 a[196:197], %[vr5] offset:24576\n\t" "v_mfma_f32_16x16x32_bf16 %3, a[212:215], a[36:39], %3\n\t" "ds_read_b64_tr_b16 a[198:199], %[vr5] offset:26624\n\t" "s_waitcnt lgkmcnt(4)\n\t"
    "v_mfma_f32_16x16x32_bf16 %4, a[216:219], a[32:35], %4\n\t" "ds_read_b64_tr_b16 a[200:201], %[vr6] offset:24576\n\t" "s_add_u32 m0, %[sl1], 147456\n\t"
    "v_mfma_f32_16x16x32_bf16 %5, a[216:219], a[36:39], %5\n\t" "ds_read_b64_tr_b16 a[202:203], %[vr6] offset:26624\n\t" "global_load_lds_dwordx4 %[vb1], %[sb]\n\t" "v_add_u32 %[vb1], 0x10000, %[vb1]\n\t"
    "v_mfma_f32_16x16x32_bf16 %6, a[220:223], a[32:35], %6\n\t" "ds_read_b64_tr_b16 a[204:205], %[vr7] offset:24576\n\t" "v_mfma_f32_16x16x32_bf16 %7, a[220:223], a[36:39], %7\n\t"
    "ds_read_b64_tr_b16 a[206:207], %[vr7] offset:26624\n\t" "s_waitcnt lgkmcnt(4)\n\t" "s_waitcnt vmcnt(18)\n\t" "v_mfma_f32_16x16x32_bf16 v[224:227], a[192:195], a[40:43], v[224:227]\n\t"
    "ds_read_b64_tr_b16 a[208:209], %[vr16] offset:24576\n\t" "v_mfma_f32_16x16x32_bf16 v[240:243], a[192:195], a[44:47], v[240:243]\n\t" "ds_read_b64_tr_b16 a[210:211], %[vr16] offset:26624\n\t"
    "global_load_dwordx4 a[8:11], %[vx], s[40:41] offset:2048 sc1\n\t" "v_mfma_f32_16x16x32_bf16 v[228:231], a[196:199], a[40:43], v[228:231]\n\t" "ds_read_b64_tr_b16 a[212:213], %[vr17] offset:24576\n\t"
    "v_mfma_f32_16x16x32_bf16 v[244:247], a[196:199], a[44:47], v[244:247]\n\t" "ds_read_b64_tr_b16 a[214:215], %[vr17] offset:26624\n\t" "s_waitcnt lgkmcnt(4)\n\t"
    "v_mfma_f32_16x16x32_bf16 v[232:235], a[200:203], a[40:43], v[232:235]\n\t" "ds_read_b64_tr_b16 a[216:217], %[vr18] offset:24576\n\t" "s_add_u32 m0, %[sl0], 155648\n\t"
    "v_mfma_f32_16x16x32_bf16 v[248:251], a[200:203], a[44:47], v[248:251]\n\t" "ds_read_b64_tr_b16 a[218:219], %[vr18] offset:26624\n\t" "global_load_lds_dwordx4 %[vb0], %[sb]\n\t"
    "v_add_u32 %[vb0], 0x10000, %[vb0]\n\t" "v_mfma_f32_16x16x32_bf16 v[236:239], a[204:207], a[40:43], v[236:239]\n\t" "ds_read_b64_tr_b16 a[220:221], %[vr19] offset:24576\n\t"
    "v_mfma_f32_16x16x32_bf16 v[252:255], a[204:207], a[44:47], v[252:255]\n\t" "ds_read_b64_tr_b16 a[222:223], %[vr19] offset:26624\n\t" "s_waitcnt lgkmcnt(4)\n\t"
    "v_mfma_f32_16x16x32_bf16 %0, a[208:211], a[40:43], %0\n\t" "s_barrier\n\t" "ds_read_b64_tr_b16 a[192:193], %[vr4] offset:32768\n\t" "v_mfma_f32_16x16x32_bf16 %1, a[208:211], a[44:47], %1\n\t"
    "ds_read_b64_tr_b16 a[194:195], %[vr4] offset:34816\n\t" "global_load_dwordx4 a[12:15], %[vx], s[40:41] offset:3072 sc1\n\t" "v_mfma_f32_16x16x32_bf16 %2, a[212:215], a[40:43], %2\n\t"
    "ds_read_b64_tr_b16 a[196:197], %[vr5] offset:32768\n\t" "v_mfma_f32_16x16x32_bf16 %3, a[212:215], a[44:47], %3\n\t" "ds_read_b64_tr_b16 a[198:199], %[vr5] offset:34816\n\t" "s_waitcnt lgkmcnt(4)\n\t"
    "v_mfma_f32_16x16x32_bf16 %4, a[216:219], a[40:43], %4\n\t" "ds_read_b64_tr_b16 a[200:201], %[vr6] offset:32768\n\t" "s_add_u32 m0, %[sl1], 155648\n\t"
    "v_mfma_f32_16x16x32_bf16 %5, a[216:219], a[44:47], %5\n\t" "ds_read_b64_tr_b16 a[202:203], %[vr6] offset:34816\n\t" "global_load_lds_dwordx4 %[vb1], %[sb]\n\t" "v_add_u32 %[vb1], 0x10000, %[vb1]\n\t"
    "v_mfma_f32_16x16x32_bf16 %6, a[220:223], a[40:43], %6\n\t" "ds_read_b64_tr_b16 a[204:205], %[vr7] offset:32768\n\t" "v_mfma_f32_16x16x32_bf16 %7, a[220:223], a[44:47], %7\n\t"
    "ds_read_b64_tr_b16 a[206:207], %[vr7] offset:34816\n\t" "s_waitcnt lgkmcnt(4)\n\t" "s_waitcnt vmcnt(17)\n\t" "v_mfma_f32_16x16x32_bf16 v[224:227], a[192:195], a[48:51], v[224:227]\n\t"
    "ds_read_b64_tr_b16 a[208:209], %[vr16] offset:32768\n\t" "v_mfma_f32_16x16x32_bf16 v[240:243], a[192:195], a[52:55], v[240:243]\n\t" "ds_read_b64_tr_b16 a[210:211], %[vr16] offset:34816\n\t"
    "s_add_u32 s40, s40, 4096\n\t" "s_addc_u32 s41, s41, 0\n\t" "global_load_dwordx4 a[16:19], %[vx], s[40:41] offset:0 sc1\n\t" "v_mfma_f32_16x16x32_bf16 v[228:231], a[196:199], a[48:51], v[228:231]\n\t"
    "ds_read_b64_tr_b16 a[212:213], %[vr17] offset:32768\n\t" "v_mfma_f32_16x16x32_bf16 v[244:247], a[196:199], a[52:55], v[244:247]\n\t" "ds_read_b64_tr_b16 a[214:215], %[vr17] offset:34816\n\t"
    "s_waitcnt lgkmcnt(4)\n\t" "v_mfma_f32_16x16x32_bf16 v[232:235], a[200:203], a[48:51], v[232:235]\n\t" "ds_read_b64_tr_b16 a[216:217], %[vr18] offset:32768\n\t" "s_add_u32 m0, %[sl0], 0\n\t"
    "v_mfma_f32_16x16x32_bf16 v[248:251], a[200:203], a[52:55], v[248:251]\n\t" "ds_read_b64_tr_b16 a[218:219], %[vr18] offset:34816\n\t" "global_load_lds_dwordx4 %[vb0], %[sb]\n\t"
    "v_add_u32 %[vb0], 0x10000, %[vb0]\n\t" "v_mfma_f32_16x16x32_bf16 v[236:239], a[204:207], a[48:51], v[236:239]\n\t" "ds_read_b64_tr_b16 a[220:221], %[vr19] offset:32768\n\t"
    "v_mfma_f32_16x16x32_bf16 v[252:255], a[204:207], a[52:55], v[252:255]\n\t" "ds_read_b64_tr_b16 a[222:223], %[vr19] offset:34816\n\t" "s_waitcnt lgkmcnt(4)\n\t"
    "v_mfma_f32_16x16x32_bf16 %0, a[208:211], a[48:51], %0\n\t" "ds_read_b64_tr_b16 a[192:193], %[vr4] offset:40960\n\t" "v_mfma_f32_16x16x32_bf16 %1, a[208:211], a[52:55], %1\n\t"
    "ds_read_b64_tr_b16 a[194:195], %[vr4] offset:43008\n\t" "global_load_dwordx4 a[20:23], %[vx], s[40:41] offset:1024 sc1\n\t" "v_mfma_f32_16x16x32_bf16 %2, a[212:215], a[48:51], %2\n\t"
    "ds_read_b64_tr_b16 a[196:197], %[vr5] offset:40960\n\t" "v_mfma_f32_16x16x32_bf16 %3, a[212:215], a[52:55], %3\n\t" "ds_read_b64_tr_b16 a[198:199], %[vr5] offset:43008\n\t" "s_waitcnt lgkmcnt(4)\n\t"
    "v_mfma_f32_16x16x32_bf16 %4, a[216:219], a[48:51], %4\n\t" "ds_read_b64_tr_b16 a[200:201], %[vr6] offset:40960\n\t" "s_add_u32 m0, %[sl1], 0\n\t"
    "v_mfma_f32_16x16x32_bf16 %5, a[216:219], a[52:55], %5\n\t" "ds_read_b64_tr_b16 a[202:203], %[vr6] offset:43008\n\t" "global_load_lds_dwordx4 %[vb1], %[sb]\n\t" "v_add_u32 %[vb1], 0x10000, %[vb1]\n\t"
    "v_mfma_f32_16x16x32_bf16 %6, a[220:223], a[48:51], %6\n\t" "ds_read_b64_tr_b16 a[204:205], %[vr7] offset:40960\n\t" "v_mfma_f32_16x16x32_bf16 %7, a[220:223], a[52:55], %7\n\t"
    "ds_read_b64_tr_b16 a[206:207], %[vr7] offset:43008\n\t" "s_waitcnt lgkmcnt(4)\n\t" "s_waitcnt vmcnt(14)\n\t" "v_mfma_f32_16x16x32_bf16 v[224:227], a[192:195], a[56:59], v[224:227]\n\t"
    "ds_read_b64_tr_b16 a[208:209], %[vr16] offset:40960\n\t" "v_mfma_f32_16x16x32_bf16 v[240:243], a[192:195], a[60:63], v[240:243]\n\t" "ds_read_b64_tr_b16 a[210:211], %[vr16] offset:43008\n\t"
    "global_load_dwordx4 a[24:27], %[vx], s[40:41] offset:2048 sc1\n\t" "v_mfma_f32_16x16x32_bf16 v[228:231], a[196:199], a[56:59], v[228:231]\n\t"
    "ds_read_b64_tr_b16 a[212:213], %[vr17] offset:40960\n\t" "v_mfma_f32_16x16x32_bf16 v[244:247], a[196:199], a[60:63], v[244:247]\n\t" "ds_read_b64_tr_b16 a[214:215], %[vr17] offset:43008\n\t"
    "s_waitcnt lgkmcnt(4)\n\t" "v_mfma_f32_16x16x32_bf16 v[232:235], a[200:203], a[56:59], v[232:235]\n\t" "ds_read_b64_tr_b16 a[216:217], %[vr18] offset:40960\n\t" "s_add_u32 m0, %[sl0], 8192\n\t"
    "v_mfma_f32_16x16x32_bf16 v[248:251], a[200:203], a[60:63], v[248:251]\n\t" "ds_read_b64_tr_b16 a[218:219], %[vr18] offset:43008\n\t" "global_load_lds_dwordx4 %[vb0], %[sb]\n\t"
    "v_add_u32 %[vb0], 0x10000, %[vb0]\n\t" "v_mfma_f32_16x16x32_bf16 v[236:239], a[204:207], a[56:59], v[236:239]\n\t" "ds_read_b64_tr_b16 a[220:221], %[vr19] offset:40960\n\t"
    "v_mfma_f32_16x16x32_bf16 v[252:255], a[204:207], a[60:63], v[252:255]\n\t" "ds_read_b64_tr_b16 a[222:223], %[vr19] offset:43008\n\t" "s_waitcnt lgkmcnt(4)\n\t"
    "v_mfma_f32_16x16x32_bf16 %0, a[208:211], a[56:59], %0\n\t" "s_barrier\n\t" "ds_read_b64_tr_b16 a[192:193], %[vr0] offset:24576\n\t" "v_mfma_f32_16x16x32_bf16 %1, a[208:211], a[60:63], %1\n\t"
    "ds_read_b64_tr_b16 a[194:195], %[vr0] offset:26624\n\t" "global_load_dwordx4 a[28:31], %[vx], s[40:41] offset:3072 sc1\n\t" "v_mfma_f32_16x16x32_bf16 %2, a[212:215], a[56:59], %2\n\t"
    "ds_read_b64_tr_b16 a[196:197], %[vr1] offset:24576\n\t" "v_mfma_f32_16x16x32_bf16 %3, a[212:215], a[60:63], %3\n\t" "ds_read_b64_tr_b16 a[198:199], %[vr1] offset:26624\n\t" "s_waitcnt lgkmcnt(4)\n\t"
    "v_mfma_f32_16x16x32_bf16 %4, a[216:219], a[56:59], %4\n\t" "ds_read_b64_tr_b16 a[200:201], %[vr2] offset:24576\n\t" "s_add_u32 m0, %[sl1], 8192\n\t"
    "v_mfma_f32_16x16x32_bf16 %5, a[216:219], a[60:63], %5\n\t" "ds_read_b64_tr_b16 a[202:203], %[vr2] offset:26624\n\t" "global_load_lds_dwordx4 %[vb1], %[sb]\n\t" "v_add_u32 %[vb1], 0x10000, %[vb1]\n\t"
    "v_mfma_f32_16x16x32_bf16 %6, a[220:223], a[56:59], %6\n\t" "ds_read_b64_tr_b16 a[204:205], %[vr3] offset:24576\n\t" "v_mfma_f32_16x16x32_bf16 %7, a[220:223], a[60:63], %7\n\t"
    "ds_read_b64_tr_b16 a[206:207], %[vr3] offset:26624\n\t" "s_waitcnt lgkmcnt(4)\n\t" "s_waitcnt vmcnt(13)\n\t" "v_mfma_f32_16x16x32_bf16 v[224:227], a[192:195], a[0:3], v[224:227]\n\t"
    "ds_read_b64_tr_b16 a[208:209], %[vr12] offset:24576\n\t" "v_mfma_f32_16x16x32_bf16 v[240:243], a[192:195], a[4:7], v[240:243]\n\t" "ds_read_b64_tr_b16 a[210:211], %[vr12] offset:26624\n\t"
    "s_add_u32 s40, s40, 4096\n\t" "s_addc_u32 s41, s41, 0\n\t" "global_load_dwordx4 a[32:35], %[vx], s[40:41] offset:0 sc1\n\t" "v_mfma_f32_16x16x32_bf16 v[228:231], a[196:199], a[0:3], v[228:231]\n\t"
    "ds_read_b64_tr_b16 a[212:213], %[vr13] offset:24576\n\t" "v_mfma_f32_16x16x32_bf16 v[244:247], a[196:199], a[4:7], v[244:247]\n\t" "ds_read_b64_tr_b16 a[214:215], %[vr13] offset:26624\n\t"
    "s_waitcnt lgkmcnt(4)\n\t" "v_mfma_f32_16x16x32_bf16 v[232:235], a[200:203], a[0:3], v[232:235]\n\t" "ds_read_b64_tr_b16 a[216:217], %[vr14] offset:24576\n\t" "s_add_u32 m0, %[sl0], 49152\n\t"
    "v_mfma_f32_16x16x32_bf16 v[248:251], a[200:203], a[4:7], v[248:251]\n\t" "ds_read_b64_tr_b16 a[218:219], %[vr14] offset:26624\n\t" "global_load_lds_dwordx4 %[vb0], %[sb]\n\t"
    "v_add_u32 %[vb0], 0x10000, %[vb0]\n\t" "v_mfma_f32_16x16x32_bf16 v[236:239], a[204:207], a[0:3], v[236:239]\n\t" "ds_read_b64_tr_b16 a[220:221], %[vr15] offset:24576\n\t"
    "v_mfma_f32_16x16x32_bf16 v[252:255], a[204:207], a[4:7], v[252:255]\n\t" "ds_read_b64_tr_b16 a[222:223], %[vr15] offset:26624\n\t" "s_waitcnt lgkmcnt(4)\n\t"
    "v_mfma_f32_16x16x32_bf16 %0, a[208:211], a[0:3], %0\n\t" "ds_read_b64_tr_b16 a[192:193], %[vr0] offset:32768\n\t" "v_mfma_f32_16x16x32_bf16 %1, a[208:211], a[4:7], %1\n\t"
    "ds_read_b64_tr_b16 a[194:195], %[vr0] offset:34816\n\t" "global_load_dwordx4 a[36:39], %[vx], s[40:41] offset:1024 sc1\n\t" "v_mfma_f32_16x16x32_bf16 %2, a[212:215], a[0:3], %2\n\t"
    "ds_read_b64_tr_b16 a[196:197], %[vr1] offset:32768\n\t" "v_mfma_f32_16x16x32_bf16 %3, a[212:215], a[4:7], %3\n\t" "ds_read_b64_tr_b16 a[198:199], %[vr1] offset:34816\n\t" "s_waitcnt lgkmcnt(4)\n\t"
    "v_mfma_f32_16x16x32_bf16 %4, a[216:219], a[0:3], %4\n\t" "ds_read_b64_tr_b16 a[200:201], %[vr2] offset:32768\n\t" "s_add_u32 m0, %[sl1], 49152\n\t"
    "v_mfma_f32_16x16x32_bf16 %5, a[216:219], a[4:7], %5\n\t" "ds_read_b64_tr_b16 a[202:203], %[vr2] offset:34816\n\t" "global_load_lds_dwordx4 %[vb1], %[sb]\n\t" "v_add_u32 %[vb1], 0x10000, %[vb1]\n\t"
    "v_mfma_f32_16x16x32_bf16 %6, a[220:223], a[0:3], %6\n\t" "ds_read_b64_tr_b16 a[204:205], %[vr3] offset:32768\n\t" "v_mfma_f32_16x16x32_bf16 %7, a[220:223], a[4:7], %7\n\t"
    "ds_read_b64_tr_b16 a[206:207], %[vr3] offset:34816\n\t" "s_waitcnt lgkmcnt(4)\n\t" "s_waitcnt vmcnt(13)\n\t" "v_mfma_f32_16x16x32_bf16 v[224:227], a[192:195], a[8:11], v[224:227]\n\t"
    "ds_read_b64_tr_b16 a[208:209], %[vr12] offset:32768\n\t" "v_mfma_f32_16x16x32_bf16 v[240:243], a[192:195], a[12:15], v[240:243]\n\t" "ds_read_b64_tr_b16 a[210:211], %[vr12] offset:34816\n\t"
    "global_load_dwordx4 a[40:43], %[vx], s[40:41] offset:2048 sc1\n\t" "v_mfma_f32_16x16x32_bf16 v[228:231], a[196:199], a[8:11], v[228:231]\n\t" "ds_read_b64_tr_b16 a[212:213], %[vr13] offset:32768\n\t"
    "v_mfma_f32_16x16x32_bf16 v[244:247], a[196:199], a[12:15], v[244:247]\n\t" "ds_read_b64_tr_b16 a[214:215], %[vr13] offset:34816\n\t" "s_waitcnt lgkmcnt(4)\n\t"
    "v_mfma_f32_16x16x32_bf16 v[232:235], a[200:203], a[8:11], v[232:235]\n\t" "ds_read_b64_tr_b16 a[216:217], %[vr14] offset:32768\n\t" "s_add_u32 m0, %[sl0], 57344\n\t"
    "v_mfma_f32_16x16x32_bf16 v[248:251], a[200:203], a[12:15], v[248:251]\n\t" "ds_read_b64_tr_b16 a[218:219], %[vr14] offset:34816\n\t" "global_load_lds_dwordx4 %[vb0], %[sb]\n\t"
    "v_add_u32 %[vb0], 0x10000, %[vb0]\n\t" "v_mfma_f32_16x16x32_bf16 v[236:239], a[204:207], a[8:11], v[236:239]\n\t" "ds_read_b64_tr_b16 a[220:221], %[vr15] offset:32768\n\t"
    "v_mfma_f32_16x16x32_bf16 v[252:255], a[204:207], a[12:15], v[252:255]\n\t" "ds_read_b64_tr_b16 a[222:223], %[vr15] offset:34816\n\t" "s_waitcnt lgkmcnt(4)\n\t"
    "v_mfma_f32_16x16x32_bf16 %0, a[208:211], a[8:11], %0\n\t" "s_barrier\n\t" "ds_read_b64_tr_b16 a[192:193], %[vr0] offset:40960\n\t" "v_mfma_f32_16x16x32_bf16 %1, a[208:211], a[12:15], %1\n\t"
    "ds_read_b64_tr_b16 a[194:195], %[vr0] offset:43008\n\t" "global_load_dwordx4 a[44:47], %[vx], s[40:41] offset:3072 sc1\n\t" "v_mfma_f32_16x16x32_bf16 %2, a[212:215], a[8:11], %2\n\t"
    "ds_read_b64_tr_b16 a[196:197], %[vr1] offset:40960\n\t" "v_mfma_f32_16x16x32_bf16 %3, a[212:215], a[12:15], %3\n\t" "ds_read_b64_tr_b16 a[198:199], %[vr1] offset:43008\n\t" "s_waitcnt lgkmcnt(4)\n\t"
    "v_mfma_f32_16x16x32_bf16 %4, a[216:219], a[8:11], %4\n\t" "ds_read_b64_tr_b16 a[200:201], %[vr2] offset:40960\n\t" "s_add_u32 m0, %[sl1], 57344\n\t"
    "v_mfma_f32_16x16x32_bf16 %5, a[216:219], a[12:15], %5\n\t" "ds_read_b64_tr_b16 a[202:203], %[vr2] offset:43008\n\t" "global_load_lds_dwordx4 %[vb1], %[sb]\n\t" "v_add_u32 %[vb1], 0x10000, %[vb1]\n\t"
    "v_mfma_f32_16x16x32_bf16 %6, a[220:223], a[8:11], %6\n\t" "ds_read_b64_tr_b16 a[204:205], %[vr3] offset:40960\n\t" "v_mfma_f32_16x16x32_bf16 %7, a[220:223], a[12:15], %7\n\t"
    "ds_read_b64_tr_b16 a[206:207], %[vr3] offset:43008\n\t" "s_waitcnt lgkmcnt(4)\n\t" "s_waitcnt vmcnt(13)\n\t" "v_mfma_f32_16x16x32_bf16 v[224:227], a[192:195], a[16:19], v[224:227]\n\t"
    "ds_read_b64_tr_b16 a[208:209], %[vr12] offset:40960\n\t" "v_mfma_f32_16x16x32_bf16 v[240:243], a[192:195], a[20:23], v[240:243]\n\t" "ds_read_b64_tr_b16 a[210:211], %[vr12] offset:43008\n\t"
    "s_add_u32 s40, s40, 4096\n\t" "s_addc_u32 s41, s41, 0\n\t" "global_load_dwordx2 a[48:49], %[vo], s[40:41] offset:0 sc1\n\t" "v_mfma_f32_16x16x32_bf16 v[228:231], a[196:199], a[16:19], v[228:231]\n\t"
    "ds_read_b64_tr_b16 a[212:213], %[vr13] offset:40960\n\t" "v_mfma_f32_16x16x32_bf16 v[244:247], a[196:199], a[20:23], v[244:247]\n\t" "ds_read_b64_tr_b16 a[214:215], %[vr13] offset:43008\n\t"
    "global_load_dwordx2 a[50:51], %[vo], s[40:41] offset:1024 sc1\n\t" "s_waitcnt lgkmcnt(4)\n\t" "v_mfma_f32_16x16x32_bf16 v[232:235], a[200:203], a[16:19], v[232:235]\n\t"
    "ds_read_b64_tr_b16 a[216:217], %[vr14] offset:40960\n\t" "v_mfma_f32_16x16x32_bf16 v[248:251], a[200:203], a[20:23], v[248:251]\n\t" "ds_read_b64_tr_b16 a[218:219], %[vr14] offset:43008\n\t"
    "s_add_u32 m0, %[sl0], 65536\n\t" "v_mfma_f32_16x16x32_bf16 v[236:239], a[204:207], a[16:19], v[236:239]\n\t" "ds_read_b64_tr_b16 a[220:221], %[vr15] offset:40960\n\t"
    "global_load_lds_dwordx4 %[vb0], %[sb]\n\t" "v_add_u32 %[vb0], 0x10000, %[vb0]\n\t" "v_mfma_f32_16x16x32_bf16 v[252:255], a[204:207], a[20:23], v[252:255]\n\t"
    "ds_read_b64_tr_b16 a[222:223], %[vr15] offset:43008\n\t" "s_waitcnt lgkmcnt(4)\n\t" "v_mfma_f32_16x16x32_bf16 %0, a[208:211], a[16:19], %0\n\t" "ds_read_b64_tr_b16 a[192:193], %[vr8] offset:8192\n\t"
    "global_load_dwordx2 a[52:53], %[vo], s[40:41] offset:512 sc1\n\t" "v_mfma_f32_16x16x32_bf16 %1, a[208:211], a[20:23], %1\n\t" "ds_read_b64_tr_b16 a[194:195], %[vr8] offset:10240\n\t"
    "v_mfma_f32_16x16x32_bf16 %2, a[212:215], a[16:19], %2\n\t" "ds_read_b64_tr_b16 a[196:197], %[vr9] offset:8192\n\t" "v_mfma_f32_16x16x32_bf16 %3, a[212:215], a[20:23], %3\n\t"
    "ds_read_b64_tr_b16 a[198:199], %[vr9] offset:10240\n\t" "global_load_dwordx2 a[54:55], %[vo], s[40:41] offset:1536 sc1\n\t" "s_waitcnt lgkmcnt(4)\n\t"
    "v_mfma_f32_16x16x32_bf16 %4, a[216:219], a[16:19], %4\n\t" "ds_read_b64_tr_b16 a[200:201], %[vr10] offset:8192\n\t" "s_add_u32 m0, %[sl1], 65536\n\t"
    "v_mfma_f32_16x16x32_bf16 %5, a[216:219], a[20:23], %5\n\t" "ds_read_b64_tr_b16 a[202:203], %[vr10] offset:10240\n\t" "global_load_lds_dwordx4 %[vb1], %[sb]\n\t"
    "v_add_u32 %[vb1], 0x10000, %[vb1]\n\t" "v_mfma_f32_16x16x32_bf16 %6, a[220:223], a[16:19], %6\n\t" "ds_read_b64_tr_b16 a[204:205], %[vr11] offset:8192\n\t"
    "v_mfma_f32_16x16x32_bf16 %7, a[220:223], a[20:23], %7\n\t" "ds_read_b64_tr_b16 a[206:207], %[vr11] offset:10240\n\t" "s_waitcnt lgkmcnt(4)\n\t" "s_waitcnt vmcnt(15)\n\t"
    "v_mfma_f32_16x16x32_bf16 v[224:227], a[192:195], a[24:27], v[224:227]\n\t" "ds_read_b64_tr_b16 a[208:209], %[vr20] offset:8192\n\t"
    "v_mfma_f32_16x16x32_bf16 v[240:243], a[192:195], a[28:31], v[240:243]\n\t" "ds_read_b64_tr_b16 a[210:211], %[vr20] offset:10240\n\t"
    "global_load_dwordx4 a[56:59], %[vx], s[40:41] offset:2048 sc1\n\t" "v_mfma_f32_16x16x32_bf16 v[228:231], a[196:199], a[24:27], v[228:231]\n\t" "ds_read_b64_tr_b16 a[212:213], %[vr21] offset:8192\n\t"
    "v_mfma_f32_16x16x32_bf16 v[244:247], a[196:199], a[28:31], v[244:247]\n\t" "ds_read_b64_tr_b16 a[214:215], %[vr21] offset:10240\n\t" "s_waitcnt lgkmcnt(4)\n\t"
    "v_mfma_f32_16x16x32_bf16 v[232:235], a[200:203], a[24:27], v[232:235]\n\t" "ds_read_b64_tr_b16 a[216:217], %[vr22] offset:8192\n\t"
    "v_mfma_f32_16x16x32_bf16 v[248:251], a[200:203], a[28:31], v[248:251]\n\t" "ds_read_b64_tr_b16 a[218:219], %[vr22] offset:10240\n\t" "s_add_u32 m0, %[sl0], 73728\n\t"
    "v_mfma_f32_16x16x32_bf16 v[236:239], a[204:207], a[24:27], v[236:239]\n\t" "ds_read_b64_tr_b16 a[220:221], %[vr23] offset:8192\n\t" "global_load_lds_dwordx4 %[vb0], %[sb]\n\t"
    "v_add_u32 %[vb0], 0x10000, %[vb0]\n\t" "v_mfma_f32_16x16x32_bf16 v[252:255], a[204:207], a[28:31], v[252:255]\n\t" "ds_read_b64_tr_b16 a[222:223], %[vr23] offset:10240\n\t" "s_waitcnt lgkmcnt(4)\n\t"
    "v_mfma_f32_16x16x32_bf16 %0, a[208:211], a[24:27], %0\n\t" "s_barrier\n\t" "ds_read_b64_tr_b16 a[192:193], %[vr8] offset:16384\n\t" "v_mfma_f32_16x16x32_bf16 %1, a[208:211], a[28:31], %1\n\t"
    "ds_read_b64_tr_b16 a[194:195], %[vr8] offset:18432\n\t" "v_mfma_f32_16x16x32_bf16 %2, a[212:215], a[24:27], %2\n\t" "ds_read_b64_tr_b16 a[196:197], %[vr9] offset:16384\n\t"
    "v_mfma_f32_16x16x32_bf16 %3, a[212:215], a[28:31], %3\n\t" "ds_read_b64_tr_b16 a[198:199], %[vr9] offset:18432\n\t" "global_load_dwordx4 a[60:63], %[vx], s[40:41] offset:3072 sc1\n\t"
    "s_waitcnt lgkmcnt(4)\n\t" "v_mfma_f32_16x16x32_bf16 %4, a[216:219], a[24:27], %4\n\t" "ds_read_b64_tr_b16 a[200:201], %[vr10] offset:16384\n\t"
    "v_mfma_f32_16x16x32_bf16 %5, a[216:219], a[28:31], %5\n\t" "ds_read_b64_tr_b16 a[202:203], %[vr10] offset:18432\n\t" "v_mfma_f32_16x16x32_bf16 %6, a[220:223], a[24:27], %6\n\t"
    "ds_read_b64_tr_b16 a[204:205], %[vr11] offset:16384\n\t" "v_mfma_f32_16x16x32_bf16 %7, a[220:223], a[28:31], %7\n\t" "ds_read_b64_tr_b16 a[206:207], %[vr11] offset:18432\n\t"
    "s_waitcnt lgkmcnt(4)\n\t" "s_waitcnt vmcnt(14)\n\t" "v_mfma_f32_16x16x32_bf16 v[224:227], a[192:195], a[32:35], v[224:227]\n\t" "ds_read_b64_tr_b16 a[208:209], %[vr20] offset:16384\n\t"
    "v_mfma_f32_16x16x32_bf16 v[240:243], a[192:195], a[36:39], v[240:243]\n\t" "ds_read_b64_tr_b16 a[210:211], %[vr20] offset:18432\n\t" "s_add_u32 s40, s40, 4096\n\t" "s_addc_u32 s41, s41, 0\n\t"
    "global_load_dwordx4 a[0:3], %[vx], s[40:41] offset:0 sc1\n\t" "v_mfma_f32_16x16x32_bf16 v[228:231], a[196:199], a[32:35], v[228:231]\n\t" "ds_read_b64_tr_b16 a[212:213], %[vr21] offset:16384\n\t"
    "v_mfma_f32_16x16x32_bf16 v[244:247], a[196:199], a[36:39], v[244:247]\n\t" "ds_read_b64_tr_b16 a[214:215], %[vr21] offset:18432\n\t" "s_waitcnt lgkmcnt(4)\n\t"
    "v_mfma_f32_16x16x32_bf16 v[232:235], a[200:203], a[32:35], v[232:235]\n\t" "ds_read_b64_tr_b16 a[216:217], %[vr22] offset:16384\n\t"
    "v_mfma_f32_16x16x32_bf16 v[248:251], a[200:203], a[36:39], v[248:251]\n\t" "ds_read_b64_tr_b16 a[218:219], %[vr22] offset:18432\n\t" "s_add_u32 m0, %[sl0], 81920\n\t"
    "v_mfma_f32_16x16x32_bf16 v[236:239], a[204:207], a[32:35], v[236:239]\n\t" "ds_read_b64_tr_b16 a[220:221], %[vr23] offset:16384\n\t" "global_load_lds_dwordx4 %[vb0], %[sb]\n\t"
    "v_add_u32 %[vb0], 0x10000, %[vb0]\n\t" "v_mfma_f32_16x16x32_bf16 v[252:255], a[204:207], a[36:39], v[252:255]\n\t" "ds_read_b64_tr_b16 a[222:223], %[vr23] offset:18432\n\t" "s_waitcnt lgkmcnt(4)\n\t"
    "v_mfma_f32_16x16x32_bf16 %0, a[208:211], a[32:35], %0\n\t" "ds_read_b64_tr_b16 a[192:193], %[vr8] offset:24576\n\t" "v_mfma_f32_16x16x32_bf16 %1, a[208:211], a[36:39], %1\n\t"
    "ds_read_b64_tr_b16 a[194:195], %[vr8] offset:26624\n\t" "v_mfma_f32_16x16x32_bf16 %2, a[212:215], a[32:35], %2\n\t" "ds_read_b64_tr_b16 a[196:197], %[vr9] offset:24576\n\t"
    "v_mfma_f32_16x16x32_bf16 %3, a[212:215], a[36:39], %3\n\t" "ds_read_b64_tr_b16 a[198:199], %[vr9] offset:26624\n\t" "global_load_dwordx4 a[4:7], %[vx], s[40:41] offset:1024 sc1\n\t"
    "s_waitcnt lgkmcnt(4)\n\t" "v_mfma_f32_16x16x32_bf16 %4, a[216:219], a[32:35], %4\n\t" "ds_read_b64_tr_b16 a[200:201], %[vr10] offset:24576\n\t"
    "v_mfma_f32_16x16x32_bf16 %5, a[216:219], a[36:39], %5\n\t" "ds_read_b64_tr_b16 a[202:203], %[vr10] offset:26624\n\t" "v_mfma_f32_16x16x32_bf16 %6, a[220:223], a[32:35], %6\n\t"
    "ds_read_b64_tr_b16 a[204:205], %[vr11] offset:24576\n\t" "v_mfma_f32_16x16x32_bf16 %7, a[220:223], a[36:39], %7\n\t" "ds_read_b64_tr_b16 a[206:207], %[vr11] offset:26624\n\t"
    "s_waitcnt lgkmcnt(4)\n\t" "s_waitcnt vmcnt(13)\n\t" "v_mfma_f32_16x16x32_bf16 v[224:227], a[192:195], a[40:43], v[224:227]\n\t" "ds_read_b64_tr_b16 a[208:209], %[vr20] offset:24576\n\t"
    "v_mfma_f32_16x16x32_bf16 v[240:243], a[192:195], a[44:47], v[240:243]\n\t" "ds_read_b64_tr_b16 a[210:211], %[vr20] offset:26624\n\t" "global_load_dwordx4 a[8:11], %[vx], s[40:41] offset:2048 sc1\n\t"
    "v_mfma_f32_16x16x32_bf16 v[228:231], a[196:199], a[40:43], v[228:231]\n\t" "ds_read_b64_tr_b16 a[212:213], %[vr21] offset:24576\n\t"
    "v_mfma_f32_16x16x32_bf16 v[244:247], a[196:199], a[44:47], v[244:247]\n\t" "ds_read_b64_tr_b16 a[214:215], %[vr21] offset:26624\n\t" "s_waitcnt lgkmcnt(4)\n\t"
    "v_mfma_f32_16x16x32_bf16 v[232:235], a[200:203], a[40:43], v[232:235]\n\t" "ds_read_b64_tr_b16 a[216:217], %[vr22] offset:24576\n\t"
    "v_mfma_f32_16x16x32_bf16 v[248:251], a[200:203], a[44:47], v[248:251]\n\t" "ds_read_b64_tr_b16 a[218:219], %[vr22] offset:26624\n\t" "s_add_u32 m0, %[sl0], 90112\n\t"
    "v_mfma_f32_16x16x32_bf16 v[236:239], a[204:207], a[40:43], v[236:239]\n\t" "ds_read_b64_tr_b16 a[220:221], %[vr23] offset:24576\n\t" "global_load_lds_dwordx4 %[vb0], %[sb]\n\t"
    "v_add_u32 %[vb0], 0x10000, %[vb0]\n\t" "v_mfma_f32_16x16x32_bf16 v[252:255], a[204:207], a[44:47], v[252:255]\n\t" "ds_read_b64_tr_b16 a[222:223], %[vr23] offset:26624\n\t" "s_waitcnt lgkmcnt(4)\n\t"
    "v_mfma_f32_16x16x32_bf16 %0, a[208:211], a[40:43], %0\n\t" "s_barrier\n\t" "ds_read_b64_tr_b16 a[192:193], %[vr0] offset:0\n\t" "v_mfma_f32_16x16x32_bf16 %1, a[208:211], a[44:47], %1\n\t"
    "ds_read_b64_tr_b16 a[194:195], %[vr0] offset:2048\n\t" "v_mfma_f32_16x16x32_bf16 %2, a[212:215], a[40:43], %2\n\t" "ds_read_b64_tr_b16 a[196:197], %[vr1] offset:0\n\t"
    "v_mfma_f32_16x16x32_bf16 %3, a[212:215], a[44:47], %3\n\t" "ds_read_b64_tr_b16 a[198:199], %[vr1] offset:2048\n\t" "global_load_dwordx4 a[12:15], %[vx], s[40:41] offset:3072 sc1\n\t"
    "s_waitcnt lgkmcnt(4)\n\t" "v_mfma_f32_16x16x32_bf16 %4, a[216:219], a[40:43], %4\n\t" "ds_read_b64_tr_b16 a[200:201], %[vr2] offset:0\n\t" "v_mfma_f32_16x16x32_bf16 %5, a[216:219], a[44:47], %5\n\t"
    "ds_read_b64_tr_b16 a[202:203], %[vr2] offset:2048\n\t" "v_mfma_f32_16x16x32_bf16 %6, a[220:223], a[40:43], %6\n\t" "ds_read_b64_tr_b16 a[204:205], %[vr3] offset:0\n\t"
    "v_mfma_f32_16x16x32_bf16 %7, a[220:223], a[44:47], %7\n\t" "ds_read_b64_tr_b16 a[206:207], %[vr3] offset:2048\n\t" "s_waitcnt lgkmcnt(4)\n\t" "s_waitcnt vmcnt(10)\n\t"
    "v_mfma_f32_16x16x32_bf16 v[224:227], a[192:195], a[48:51], v[224:227]\n\t" "ds_read_b64_tr_b16 a[208:209], %[vr12] offset:0\n\t"
    "v_mfma_f32_16x16x32_bf16 v[240:243], a[192:195], a[52:55], v[240:243]\n\t" "ds_read_b64_tr_b16 a[210:211], %[vr12] offset:2048\n\t" "s_add_u32 s40, s40, 4096\n\t" "s_addc_u32 s41, s41, 0\n\t"
    "global_load_dwordx4 a[16:19], %[vx], s[40:41] offset:0 sc1\n\t" "v_mfma_f32_16x16x32_bf16 v[228:231], a[196:199], a[48:51], v[228:231]\n\t" "ds_read_b64_tr_b16 a[212:213], %[vr13] offset:0\n\t"
    "v_mfma_f32_16x16x32_bf16 v[244:247], a[196:199], a[52:55], v[244:247]\n\t" "ds_read_b64_tr_b16 a[214:215], %[vr13] offset:2048\n\t" "s_waitcnt lgkmcnt(4)\n\t"
    "v_mfma_f32_16x16x32_bf16 v[232:235], a[200:203], a[48:51], v[232:235]\n\t" "ds_read_b64_tr_b16 a[216:217], %[vr14] offset:0\n\t"
    "v_mfma_f32_16x16x32_bf16 v[248:251], a[200:203], a[52:55], v[248:251]\n\t" "ds_read_b64_tr_b16 a[218:219], %[vr14] offset:2048\n\t" "s_add_u32 m0, %[sl0], 98304\n\t"
    "v_mfma_f32_16x16x32_bf16 v[236:239], a[204:207], a[48:51], v[236:239]\n\t" "ds_read_b64_tr_b16 a[220:221], %[vr15] offset:0\n\t" "global_load_lds_dwordx4 %[vb0], %[sb]\n\t"
    "v_add_u32 %[vb0], 0x10000, %[vb0]\n\t" "v_mfma_f32_16x16x32_bf16 v[252:255], a[204:207], a[52:55], v[252:255]\n\t" "ds_read_b64_tr_b16 a[222:223], %[vr15] offset:2048\n\t" "s_waitcnt lgkmcnt(4)\n\t"
    "v_mfma_f32_16x16x32_bf16 %0, a[208:211], a[48:51], %0\n\t" "ds_read_b64_tr_b16 a[192:193], %[vr0] offset:8192\n\t" "v_mfma_f32_16x16x32_bf16 %1, a[208:211], a[52:55], %1\n\t"
    "ds_read_b64_tr_b16 a[194:195], %[vr0] offset:10240\n\t" "v_mfma_f32_16x16x32_bf16 %2, a[212:215], a[48:51], %2\n\t" "ds_read_b64_tr_b16 a[196:197], %[vr1] offset:8192\n\t"
    "v_mfma_f32_16x16x32_bf16 %3, a[212:215], a[52:55], %3\n\t" "ds_read_b64_tr_b16 a[198:199], %[vr1] offset:10240\n\t" "global_load_dwordx4 a[20:23], %[vx], s[40:41] offset:1024 sc1\n\t"
    "s_waitcnt lgkmcnt(4)\n\t" "v_mfma_f32_16x16x32_bf16 %4, a[216:219], a[48:51], %4\n\t" "ds_read_b64_tr_b16 a[200:201], %[vr2] offset:8192\n\t"
    "v_mfma_f32_16x16x32_bf16 %5, a[216:219], a[52:55], %5\n\t" "ds_read_b64_tr_b16 a[202:203], %[vr2] offset:10240\n\t" "v_mfma_f32_16x16x32_bf16 %6, a[220:223], a[48:51], %6\n\t"
    "ds_read_b64_tr_b16 a[204:205], %[vr3] offset:8192\n\t" "v_mfma_f32_16x16x32_bf16 %7, a[220:223], a[52:55], %7\n\t" "ds_read_b64_tr_b16 a[206:207], %[vr3] offset:10240\n\t" "s_waitcnt lgkmcnt(4)\n\t"
    "s_waitcnt vmcnt(9)\n\t" "v_mfma_f32_16x16x32_bf16 v[224:227], a[192:195], a[56:59], v[224:227]\n\t" "ds_read_b64_tr_b16 a[208:209], %[vr12] offset:8192\n\t"
    "v_mfma_f32_16x16x32_bf16 v[240:243], a[192:195], a[60:63], v[240:243]\n\t" "ds_read_b64_tr_b16 a[210:211], %[vr12] offset:10240\n\t"
    "global_load_dwordx4 a[64:67], %[vx], s[40:41] offset:2048 sc1\n\t" "v_mfma_f32_16x16x32_bf16 v[228:231], a[196:199], a[56:59], v[228:231]\n\t" "ds_read_b64_tr_b16 a[212:213], %[vr13] offset:8192\n\t"
    "v_mfma_f32_16x16x32_bf16 v[244:247], a[196:199], a[60:63], v[244:247]\n\t" "ds_read_b64_tr_b16 a[214:215], %[vr13] offset:10240\n\t" "s_waitcnt lgkmcnt(4)\n\t"
    "v_mfma_f32_16x16x32_bf16 v[232:235], a[200:203], a[56:59], v[232:235]\n\t" "ds_read_b64_tr_b16 a[216:217], %[vr14] offset:8192\n\t"
    "v_mfma_f32_16x16x32_bf16 v[248:251], a[200:203], a[60:63], v[248:251]\n\t" "ds_read_b64_tr_b16 a[218:219], %[vr14] offset:10240\n\t" "s_add_u32 m0, %[sl0], 106496\n\t"
    "v_mfma_f32_16x16x32_bf16 v[236:239], a[204:207], a[56:59], v[236:239]\n\t" "ds_read_b64_tr_b16 a[220:221], %[vr15] offset:8192\n\t" "global_load_lds_dwordx4 %[vb0], %[sb]\n\t"
    "v_add_u32 %[vb0], 0x10000, %[vb0]\n\t" "v_mfma_f32_16x16x32_bf16 v[252:255], a[204:207], a[60:63], v[252:255]\n\t" "ds_read_b64_tr_b16 a[222:223], %[vr15] offset:10240\n\t" "s_waitcnt lgkmcnt(4)\n\t"
    "v_mfma_f32_16x16x32_bf16 %0, a[208:211], a[56:59], %0\n\t" "s_barrier\n\t" "ds_read_b64_tr_b16 a[192:193], %[vr0] offset:49152\n\t" "v_mfma_f32_16x16x32_bf16 %1, a[208:211], a[60:63], %1\n\t"
    "ds_read_b64_tr_b16 a[194:195], %[vr0] offset:51200\n\t" "v_mfma_f32_16x16x32_bf16 %2, a[212:215], a[56:59], %2\n\t" "ds_read_b64_tr_b16 a[196:197], %[vr1] offset:49152\n\t"
    "v_mfma_f32_16x16x32_bf16 %3, a[212:215], a[60:63], %3\n\t" "ds_read_b64_tr_b16 a[198:199], %[vr1] offset:51200\n\t" "global_load_dwordx4 a[68:71], %[vx], s[40:41] offset:3072 sc1\n\t"
    "s_waitcnt lgkmcnt(4)\n\t" "v_mfma_f32_16x16x32_bf16 %4, a[216:219], a[56:59], %4\n\t" "ds_read_b64_tr_b16 a[200:201], %[vr2] offset:49152\n\t"
    "v_mfma_f32_16x16x32_bf16 %5, a[216:219], a[60:63], %5\n\t" "ds_read_b64_tr_b16 a[202:203], %[vr2] offset:51200\n\t" "v_mfma_f32_16x16x32_bf16 %6, a[220:223], a[56:59], %6\n\t"
    "ds_read_b64_tr_b16 a[204:205], %[vr3] offset:49152\n\t" "v_mfma_f32_16x16x32_bf16 %7, a[220:223], a[60:63], %7\n\t" "ds_read_b64_tr_b16 a[206:207], %[vr3] offset:51200\n\t" "s_waitcnt lgkmcnt(4)\n\t"
    "s_waitcnt vmcnt(9)\n\t" "v_mfma_f32_16x16x32_bf16 v[224:227], a[192:195], a[0:3], v[224:227]\n\t" "ds_read_b64_tr_b16 a[208:209], %[vr12] offset:49152\n\t"
    "v_mfma_f32_16x16x32_bf16 v[240:243], a[192:195], a[4:7], v[240:243]\n\t" "ds_read_b64_tr_b16 a[210:211], %[vr12] offset:51200\n\t" "s_add_u32 s40, s40, 4096\n\t" "s_addc_u32 s41, s41, 0\n\t"
    "global_load_dwordx4 a[72:75], %[vx], s[40:41] offset:0 sc1\n\t" "v_mfma_f32_16x16x32_bf16 v[228:231], a[196:199], a[0:3], v[228:231]\n\t" "ds_read_b64_tr_b16 a[212:213], %[vr13] offset:49152\n\t"
    "v_mfma_f32_16x16x32_bf16 v[244:247], a[196:199], a[4:7], v[244:247]\n\t" "ds_read_b64_tr_b16 a[214:215], %[vr13] offset:51200\n\t" "s_waitcnt lgkmcnt(4)\n\t"
    "v_mfma_f32_16x16x32_bf16 v[232:235], a[200:203], a[0:3], v[232:235]\n\t" "ds_read_b64_tr_b16 a[216:217], %[vr14] offset:49152\n\t"
    "v_mfma_f32_16x16x32_bf16 v[248:251], a[200:203], a[4:7], v[248:251]\n\t" "ds_read_b64_tr_b16 a[218:219], %[vr14] offset:51200\n\t" "s_add_u32 m0, %[sl0], 24576\n\t"
    "v_mfma_f32_16x16x32_bf16 v[236:239], a[204:207], a[0:3], v[236:239]\n\t" "ds_read_b64_tr_b16 a[220:221], %[vr15] offset:49152\n\t" "global_load_lds_dwordx4 %[vb0], %[sb]\n\t"
    "v_add_u32 %[vb0], 0x10000, %[vb0]\n\t" "v_mfma_f32_16x16x32_bf16 v[252:255], a[204:207], a[4:7], v[252:255]\n\t" "ds_read_b64_tr_b16 a[222:223], %[vr15] offset:51200\n\t" "s_waitcnt lgkmcnt(4)\n\t"
    "v_mfma_f32_16x16x32_bf16 %0, a[208:211], a[0:3], %0\n\t" "ds_read_b64_tr_b16 a[192:193], %[vr0] offset:57344\n\t" "v_mfma_f32_16x16x32_bf16 %1, a[208:211], a[4:7], %1\n\t"
    "ds_read_b64_tr_b16 a[194:195], %[vr0] offset:59392\n\t" "v_mfma_f32_16x16x32_bf16 %2, a[212:215], a[0:3], %2\n\t" "ds_read_b64_tr_b16 a[196:197], %[vr1] offset:57344\n\t"
    "v_mfma_f32_16x16x32_bf16 %3, a[212:215], a[4:7], %3\n\t" "ds_read_b64_tr_b16 a[198:199], %[vr1] offset:59392\n\t" "global_load_dwordx4 a[76:79], %[vx], s[40:41] offset:1024 sc1\n\t"
    "s_waitcnt lgkmcnt(4)\n\t" "v_mfma_f32_16x16x32_bf16 %4, a[216:219], a[0:3], %4\n\t" "ds_read_b64_tr_b16 a[200:201], %[vr2] offset:57344\n\t" "v_mfma_f32_16x16x32_bf16 %5, a[216:219], a[4:7], %5\n\t"
    "ds_read_b64_tr_b16 a[202:203], %[vr2] offset:59392\n\t" "v_mfma_f32_16x16x32_bf16 %6, a[220:223], a[0:3], %6\n\t" "ds_read_b64_tr_b16 a[204:205], %[vr3] offset:57344\n\t"
    "v_mfma_f32_16x16x32_bf16 %7, a[220:223], a[4:7], %7\n\t" "ds_read_b64_tr_b16 a[206:207], %[vr3] offset:59392\n\t" "s_waitcnt lgkmcnt(4)\n\t" "s_waitcnt vmcnt(9)\n\t"
    "v_mfma_f32_16x16x32_bf16 v[224:227], a[192:195], a[8:11], v[224:227]\n\t" "ds_read_b64_tr_b16 a[208:209], %[vr12] offset:57344\n\t"
    "v_mfma_f32_16x16x32_bf16 v[240:243], a[192:195], a[12:15], v[240:243]\n\t" "ds_read_b64_tr_b16 a[210:211], %[vr12] offset:59392\n\t"
    "global_load_dwordx2 a[80:81], %[vo], s[40:41] offset:2048 sc1\n\t" "v_mfma_f32_16x16x32_bf16 v[228:231], a[196:199], a[8:11], v[228:231]\n\t" "ds_read_b64_tr_b16 a[212:213], %[vr13] offset:57344\n\t"
    "v_mfma_f32_16x16x32_bf16 v[244:247], a[196:199], a[12:15], v[244:247]\n\t" "ds_read_b64_tr_b16 a[214:215], %[vr13] offset:59392\n\t" "s_waitcnt lgkmcnt(4)\n\t"
    "v_mfma_f32_16x16x32_bf16 v[232:235], a[200:203], a[8:11], v[232:235]\n\t" "ds_read_b64_tr_b16 a[216:217], %[vr14] offset:57344\n\t" "global_load_dwordx2 a[82:83], %[vo], s[40:41] offset:3072 sc1\n\t"
    "v_mfma_f32_16x16x32_bf16 v[248:251], a[200:203], a[12:15], v[248:251]\n\t" "ds_read_b64_tr_b16 a[218:219], %[vr14] offset:59392\n\t"
    "v_mfma_f32_16x16x32_bf16 v[236:239], a[204:207], a[8:11], v[236:239]\n\t" "ds_read_b64_tr_b16 a[220:221], %[vr15] offset:57344\n\t" "s_add_u32 m0, %[sl0], 32768\n\t"
    "v_mfma_f32_16x16x32_bf16 v[252:255], a[204:207], a[12:15], v[252:255]\n\t" "ds_read_b64_tr_b16 a[222:223], %[vr15] offset:59392\n\t" "global_load_lds_dwordx4 %[vb0], %[sb]\n\t"
    "v_add_u32 %[vb0], 0x10000, %[vb0]\n\t" "s_waitcnt lgkmcnt(4)\n\t" "v_mfma_f32_16x16x32_bf16 %0, a[208:211], a[8:11], %0\n\t" "s_barrier\n\t" "ds_read_b64_tr_b16 a[192:193], %[vr4] offset:0\n\t"
    "v_mfma_f32_16x16x32_bf16 %1, a[208:211], a[12:15], %1\n\t" "ds_read_b64_tr_b16 a[194:195], %[vr4] offset:2048\n\t" "v_mfma_f32_16x16x32_bf16 %2, a[212:215], a[8:11], %2\n\t"
    "ds_read_b64_tr_b16 a[196:197], %[vr5] offset:0\n\t" "global_load_dwordx2 a[84:85], %[vo], s[40:41] offset:2560 sc1\n\t" "v_mfma_f32_16x16x32_bf16 %3, a[212:215], a[12:15], %3\n\t"
    "ds_read_b64_tr_b16 a[198:199], %[vr5] offset:2048\n\t" "s_waitcnt lgkmcnt(4)\n\t" "v_mfma_f32_16x16x32_bf16 %4, a[216:219], a[8:11], %4\n\t" "ds_read_b64_tr_b16 a[200:201], %[vr6] offset:0\n\t"
    "v_mfma_f32_16x16x32_bf16 %5, a[216:219], a[12:15], %5\n\t" "ds_read_b64_tr_b16 a[202:203], %[vr6] offset:2048\n\t" "global_load_dwordx2 a[86:87], %[vo], s[40:41] offset:3584 sc1\n\t"
    "v_mfma_f32_16x16x32_bf16 %6, a[220:223], a[8:11], %6\n\t" "ds_read_b64_tr_b16 a[204:205], %[vr7] offset:0\n\t" "v_mfma_f32_16x16x32_bf16 %7, a[220:223], a[12:15], %7\n\t"
    "ds_read_b64_tr_b16 a[206:207], %[vr7] offset:2048\n\t" "s_waitcnt lgkmcnt(4)\n\t" "s_waitcnt vmcnt(11)\n\t" "v_mfma_f32_16x16x32_bf16 v[224:227], a[192:195], a[16:19], v[224:227]\n\t"
    "ds_read_b64_tr_b16 a[208:209], %[vr16] offset:0\n\t" "v_mfma_f32_16x16x32_bf16 v[240:243], a[192:195], a[20:23], v[240:243]\n\t" "ds_read_b64_tr_b16 a[210:211], %[vr16] offset:2048\n\t"
    "s_add_u32 s40, s40, 4096\n\t" "s_addc_u32 s41, s41, 0\n\t" "global_load_dwordx4 a[88:91], %[vx], s[40:41] offset:0 sc1\n\t" "v_mfma_f32_16x16x32_bf16 v[228:231], a[196:199], a[16:19], v[228:231]\n\t"
    "ds_read_b64_tr_b16 a[212:213], %[vr17] offset:0\n\t" "s_add_u32 m0, %[sl0], 40960\n\t" "v_mfma_f32_16x16x32_bf16 v[244:247], a[196:199], a[20:23], v[244:247]\n\t"
    "ds_read_b64_tr_b16 a[214:215], %[vr17] offset:2048\n\t" "global_load_lds_dwordx4 %[vb0], %[sb]\n\t" "v_add_u32 %[vb0], 0x10000, %[vb0]\n\t" "s_waitcnt lgkmcnt(4)\n\t"
    "v_mfma_f32_16x16x32_bf16 v[232:235], a[200:203], a[16:19], v[232:235]\n\t" "ds_read_b64_tr_b16 a[216:217], %[vr18] offset:0\n\t"
    "v_mfma_f32_16x16x32_bf16 v[248:251], a[200:203], a[20:23], v[248:251]\n\t" "ds_read_b64_tr_b16 a[218:219], %[vr18] offset:2048\n\t"
    "v_mfma_f32_16x16x32_bf16 v[236:239], a[204:207], a[16:19], v[236:239]\n\t" "ds_read_b64_tr_b16 a[220:221], %[vr19] offset:0\n\t" "global_load_dwordx4 a[92:95], %[vx], s[40:41] offset:1024 sc1\n\t"
    "v_mfma_f32_16x16x32_bf16 v[252:255], a[204:207], a[20:23], v[252:255]\n\t" "ds_read_b64_tr_b16 a[222:223], %[vr19] offset:2048\n\t" "s_waitcnt lgkmcnt(4)\n\t"
    "v_mfma_f32_16x16x32_bf16 %0, a[208:211], a[16:19], %0\n\t" "s_barrier\n\t" "ds_read_b64_tr_b16 a[192:193], %[vr4] offset:8192\n\t" "global_load_dwordx4 a[96:99], %[vx], s[40:41] offset:2048 sc1\n\t"
    "v_mfma_f32_16x16x32_bf16 %1, a[208:211], a[20:23], %1\n\t" "ds_read_b64_tr_b16 a[194:195], %[vr4] offset:10240\n\t" "v_mfma_f32_16x16x32_bf16 %2, a[212:215], a[16:19], %2\n\t"
    "ds_read_b64_tr_b16 a[196:197], %[vr5] offset:8192\n\t" "s_add_u32 m0, %[sl0], 139264\n\t" "v_mfma_f32_16x16x32_bf16 %3, a[212:215], a[20:23], %3\n\t"
    "ds_read_b64_tr_b16 a[198:199], %[vr5] offset:10240\n\t" "global_load_lds_dwordx4 %[vb0], %[sb]\n\t" "v_add_u32 %[vb0], 0x10000, %[vb0]\n\t" "s_waitcnt lgkmcnt(4)\n\t"
    "v_mfma_f32_16x16x32_bf16 %4, a[216:219], a[16:19], %4\n\t" "ds_read_b64_tr_b16 a[200:201], %[vr6] offset:8192\n\t" "v_mfma_f32_16x16x32_bf16 %5, a[216:219], a[20:23], %5\n\t"
    "ds_read_b64_tr_b16 a[202:203], %[vr6] offset:10240\n\t" "global_load_dwordx4 a[100:103], %[vx], s[40:41] offset:3072 sc1\n\t" "v_mfma_f32_16x16x32_bf16 %6, a[220:223], a[16:19], %6\n\t"
    "ds_read_b64_tr_b16 a[204:205], %[vr7] offset:8192\n\t" "v_mfma_f32_16x16x32_bf16 %7, a[220:223], a[20:23], %7\n\t" "ds_read_b64_tr_b16 a[206:207], %[vr7] offset:10240\n\t" "s_waitcnt lgkmcnt(4)\n\t"
    "s_waitcnt vmcnt(14)\n\t" "v_mfma_f32_16x16x32_bf16 v[224:227], a[192:195], a[64:67], v[224:227]\n\t" "s_barrier\n\t" "ds_read_b64_tr_b16 a[208:209], %[vr4] offset:16384\n\t"
    "v_mfma_f32_16x16x32_bf16 v[240:243], a[192:195], a[68:71], v[240:243]\n\t" "ds_read_b64_tr_b16 a[210:211], %[vr4] offset:18432\n\t" "s_add_u32 s40, s40, 4096\n\t" "s_addc_u32 s41, s41, 0\n\t"
    "global_load_dwordx4 a[104:107], %[vx], s[40:41] offset:0 sc1\n\t" "v_mfma_f32_16x16x32_bf16 v[228:231], a[196:199], a[64:67], v[228:231]\n\t" "ds_read_b64_tr_b16 a[212:213], %[vr5] offset:16384\n\t"
    "s_add_u32 m0, %[sl0], 147456\n\t" "v_mfma_f32_16x16x32_bf16 v[244:247], a[196:199], a[68:71], v[244:247]\n\t" "ds_read_b64_tr_b16 a[214:215], %[vr5] offset:18432\n\t"
    "global_load_lds_dwordx4 %[vb0], %[sb]\n\t" "v_add_u32 %[vb0], 0x10000, %[vb0]\n\t" "s_waitcnt lgkmcnt(4)\n\t" "v_mfma_f32_16x16x32_bf16 v[232:235], a[200:203], a[64:67], v[232:235]\n\t"
    "ds_read_b64_tr_b16 a[216:217], %[vr6] offset:16384\n\t" "v_mfma_f32_16x16x32_bf16 v[248:251], a[200:203], a[68:71], v[248:251]\n\t" "ds_read_b64_tr_b16 a[218:219], %[vr6] offset:18432\n\t"
    "global_load_dwordx4 a[108:111], %[vx], s[40:41] offset:1024 sc1\n\t" "v_mfma_f32_16x16x32_bf16 v[236:239], a[204:207], a[64:67], v[236:239]\n\t"
    "ds_read_b64_tr_b16 a[220:221], %[vr7] offset:16384\n\t" "v_mfma_f32_16x16x32_bf16 v[252:255], a[204:207], a[68:71], v[252:255]\n\t" "ds_read_b64_tr_b16 a[222:223], %[vr7] offset:18432\n\t"
    "s_waitcnt lgkmcnt(4)\n\t" "s_waitcnt vmcnt(14)\n\t" "v_mfma_f32_16x16x32_bf16 v[224:227], a[208:211], a[72:75], v[224:227]\n\t" "s_barrier\n\t"
    "ds_read_b64_tr_b16 a[192:193], %[vr4] offset:24576\n\t" "v_mfma_f32_16x16x32_bf16 v[240:243], a[208:211], a[76:79], v[240:243]\n\t" "ds_read_b64_tr_b16 a[194:195], %[vr4] offset:26624\n\t"
    "global_load_dwordx4 a[112:115], %[vx], s[40:41] offset:2048 sc1\n\t" "v_mfma_f32_16x16x32_bf16 v[228:231], a[212:215], a[72:75], v[228:231]\n\t"
    "ds_read_b64_tr_b16 a[196:197], %[vr5] offset:24576\n\t" "s_add_u32 m0, %[sl0], 155648\n\t" "v_mfma_f32_16x16x32_bf16 v[244:247], a[212:215], a[76:79], v[244:247]\n\t"
    "ds_read_b64_tr_b16 a[198:199], %[vr5] offset:26624\n\t" "global_load_lds_dwordx4 %[vb0], %[sb]\n\t" "v_add_u32 %[vb0], 0x10000, %[vb0]\n\t" "s_waitcnt lgkmcnt(4)\n\t"
    "v_mfma_f32_16x16x32_bf16 v[232:235], a[216:219], a[72:75], v[232:235]\n\t" "ds_read_b64_tr_b16 a[200:201], %[vr6] offset:24576\n\t"
    "v_mfma_f32_16x16x32_bf16 v[248:251], a[216:219], a[76:79], v[248:251]\n\t" "ds_read_b64_tr_b16 a[202:203], %[vr6] offset:26624\n\t"
    "global_load_dwordx4 a[116:119], %[vx], s[40:41] offset:3072 sc1\n\t" "v_mfma_f32_16x16x32_bf16 v[236:239], a[220:223], a[72:75], v[236:239]\n\t"
    "ds_read_b64_tr_b16 a[204:205], %[vr7] offset:24576\n\t" "v_mfma_f32_16x16x32_bf16 v[252:255], a[220:223], a[76:79], v[252:255]\n\t" "ds_read_b64_tr_b16 a[206:207], %[vr7] offset:26624\n\t"
    "s_waitcnt lgkmcnt(4)\n\t" "s_waitcnt vmcnt(12)\n\t" "v_mfma_f32_16x16x32_bf16 v[224:227], a[192:195], a[80:83], v[224:227]\n\t" "s_barrier\n\t"
    "ds_read_b64_tr_b16 a[208:209], %[vr4] offset:32768\n\t" "v_mfma_f32_16x16x32_bf16 v[240:243], a[192:195], a[84:87], v[240:243]\n\t" "ds_read_b64_tr_b16 a[210:211], %[vr4] offset:34816\n\t"
    "s_add_u32 s40, s40, 4096\n\t" "s_addc_u32 s41, s41, 0\n\t" "global_load_dwordx4 a[120:123], %[vx], s[40:41] offset:0 sc1\n\t"
    "v_mfma_f32_16x16x32_bf16 v[228:231], a[196:199], a[80:83], v[228:231]\n\t" "ds_read_b64_tr_b16 a[212:213], %[vr5] offset:32768\n\t" "s_add_u32 m0, %[sl0], 0\n\t"
    "v_mfma_f32_16x16x32_bf16 v[244:247], a[196:199], a[84:87], v[244:247]\n\t" "ds_read_b64_tr_b16 a[214:215], %[vr5] offset:34816\n\t" "global_load_lds_dwordx4 %[vb0], %[sb]\n\t"
    "v_add_u32 %[vb0], 0x10000, %[vb0]\n\t" "s_waitcnt lgkmcnt(4)\n\t" "v_mfma_f32_16x16x32_bf16 v[232:235], a[200:203], a[80:83], v[232:235]\n\t" "ds_read_b64_tr_b16 a[216:217], %[vr6] offset:32768\n\t"
    "v_mfma_f32_16x16x32_bf16 v[248:251], a[200:203], a[84:87], v[248:251]\n\t" "ds_read_b64_tr_b16 a[218:219], %[vr6] offset:34816\n\t"
    "global_load_dwordx4 a[124:127], %[vx], s[40:41] offset:1024 sc1\n\t" "v_mfma_f32_16x16x32_bf16 v[236:239], a[204:207], a[80:83], v[236:239]\n\t"
    "ds_read_b64_tr_b16 a[220:221], %[vr7] offset:32768\n\t" "v_mfma_f32_16x16x32_bf16 v[252:255], a[204:207], a[84:87], v[252:255]\n\t" "ds_read_b64_tr_b16 a[222:223], %[vr7] offset:34816\n\t"
    "s_waitcnt lgkmcnt(4)\n\t" "s_waitcnt vmcnt(12)\n\t" "v_mfma_f32_16x16x32_bf16 v[224:227], a[208:211], a[88:91], v[224:227]\n\t" "s_barrier\n\t"
    "ds_read_b64_tr_b16 a[192:193], %[vr4] offset:40960\n\t" "v_mfma_f32_16x16x32_bf16 v[240:243], a[208:211], a[92:95], v[240:243]\n\t" "ds_read_b64_tr_b16 a[194:195], %[vr4] offset:43008\n\t"
    "global_load_dwordx4 a[128:131], %[vx], s[40:41] offset:2048 sc1\n\t" "v_mfma_f32_16x16x32_bf16 v[228:231], a[212:215], a[88:91], v[228:231]\n\t"
    "ds_read_b64_tr_b16 a[196:197], %[vr5] offset:40960\n\t" "s_add_u32 m0, %[sl0], 8192\n\t" "v_mfma_f32_16x16x32_bf16 v[244:247], a[212:215], a[92:95], v[244:247]\n\t"
    "ds_read_b64_tr_b16 a[198:199], %[vr5] offset:43008\n\t" "global_load_lds_dwordx4 %[vb0], %[sb]\n\t" "v_add_u32 %[vb0], 0x10000, %[vb0]\n\t" "s_waitcnt lgkmcnt(4)\n\t"
    "v_mfma_f32_16x16x32_bf16 v[232:235], a[216:219], a[88:91], v[232:235]\n\t" "ds_read_b64_tr_b16 a[200:201], %[vr6] offset:40960\n\t"
    "v_mfma_f32_16x16x32_bf16 v[248:251], a[216:219], a[92:95], v[248:251]\n\t" "ds_read_b64_tr_b16 a[202:203], %[vr6] offset:43008\n\t"
    "global_load_dwordx4 a[132:135], %[vx], s[40:41] offset:3072 sc1\n\t" "v_mfma_f32_16x16x32_bf16 v[236:239], a[220:223], a[88:91], v[236:239]\n\t"
    "ds_read_b64_tr_b16 a[204:205], %[vr7] offset:40960\n\t" "v_mfma_f32_16x16x32_bf16 v[252:255], a[220:223], a[92:95], v[252:255]\n\t" "ds_read_b64_tr_b16 a[206:207], %[vr7] offset:43008\n\t"
    "s_waitcnt lgkmcnt(4)\n\t" "s_waitcnt vmcnt(12)\n\t" "v_mfma_f32_16x16x32_bf16 v[224:227], a[192:195], a[96:99], v[224:227]\n\t" "s_barrier\n\t"
    "ds_read_b64_tr_b16 a[208:209], %[vr0] offset:24576\n\t" "v_mfma_f32_16x16x32_bf16 v[240:243], a[192:195], a[100:103], v[240:243]\n\t" "ds_read_b64_tr_b16 a[210:211], %[vr0] offset:26624\n\t"
    "s_add_u32 s40, s40, 4096\n\t" "s_addc_u32 s41, s41, 0\n\t" "global_load_dwordx2 a[136:137], %[vo], s[40:41] offset:0 sc1\n\t"
    "v_mfma_f32_16x16x32_bf16 v[228:231], a[196:199], a[96:99], v[228:231]\n\t" "ds_read_b64_tr_b16 a[212:213], %[vr1] offset:24576\n\t"
    "global_load_dwordx2 a[138:139], %[vo], s[40:41] offset:1024 sc1\n\t" "v_mfma_f32_16x16x32_bf16 v[244:247], a[196:199], a[100:103], v[244:247]\n\t"
    "ds_read_b64_tr_b16 a[214:215], %[vr1] offset:26624\n\t" "s_waitcnt lgkmcnt(4)\n\t" "v_mfma_f32_16x16x32_bf16 v[232:235], a[200:203], a[96:99], v[232:235]\n\t"
    "ds_read_b64_tr_b16 a[216:217], %[vr2] offset:24576\n\t" "global_load_dwordx2 a[140:141], %[vo], s[40:41] offset:512 sc1\n\t"
    "v_mfma_f32_16x16x32_bf16 v[248:251], a[200:203], a[100:103], v[248:251]\n\t" "ds_read_b64_tr_b16 a[218:219], %[vr2] offset:26624\n\t"
    "v_mfma_f32_16x16x32_bf16 v[236:239], a[204:207], a[96:99], v[236:239]\n\t" "ds_read_b64_tr_b16 a[220:221], %[vr3] offset:24576\n\t"
    "global_load_dwordx2 a[142:143], %[vo], s[40:41] offset:1536 sc1\n\t" "v_mfma_f32_16x16x32_bf16 v[252:255], a[204:207], a[100:103], v[252:255]\n\t"
    "ds_read_b64_tr_b16 a[222:223], %[vr3] offset:26624\n\t" "s_waitcnt lgkmcnt(4)\n\t" "s_waitcnt vmcnt(13)\n\t" "v_mfma_f32_16x16x32_bf16 v[224:227], a[208:211], a[104:107], v[224:227]\n\t"
    "s_barrier\n\t" "ds_read_b64_tr_b16 a[192:193], %[vr0] offset:32768\n\t" "v_mfma_f32_16x16x32_bf16 v[240:243], a[208:211], a[108:111], v[240:243]\n\t"
    "ds_read_b64_tr_b16 a[194:195], %[vr0] offset:34816\n\t" "global_load_dwordx4 a[144:147], %[vx], s[40:41] offset:2048 sc1\n\t"
    "v_mfma_f32_16x16x32_bf16 v[228:231], a[212:215], a[104:107], v[228:231]\n\t" "ds_read_b64_tr_b16 a[196:197], %[vr1] offset:32768\n\t"
    "v_mfma_f32_16x16x32_bf16 v[244:247], a[212:215], a[108:111], v[244:247]\n\t" "ds_read_b64_tr_b16 a[198:199], %[vr1] offset:34816\n\t" "s_waitcnt lgkmcnt(4)\n\t"
    "v_mfma_f32_16x16x32_bf16 v[232:235], a[216:219], a[104:107], v[232:235]\n\t" "ds_read_b64_tr_b16 a[200:201], %[vr2] offset:32768\n\t"
    "global_load_dwordx4 a[148:151], %[vx], s[40:41] offset:3072 sc1\n\t" "v_mfma_f32_16x16x32_bf16 v[248:251], a[216:219], a[108:111], v[248:251]\n\t"
    "ds_read_b64_tr_b16 a[202:203], %[vr2] offset:34816\n\t" "v_mfma_f32_16x16x32_bf16 v[236:239], a[220:223], a[104:107], v[236:239]\n\t" "ds_read_b64_tr_b16 a[204:205], %[vr3] offset:32768\n\t"
    "v_mfma_f32_16x16x32_bf16 v[252:255], a[220:223], a[108:111], v[252:255]\n\t" "ds_read_b64_tr_b16 a[206:207], %[vr3] offset:34816\n\t" "s_waitcnt lgkmcnt(4)\n\t" "s_waitcnt vmcnt(12)\n\t"
    "v_mfma_f32_16x16x32_bf16 v[224:227], a[192:195], a[112:115], v[224:227]\n\t" "s_barrier\n\t" "ds_read_b64_tr_b16 a[208:209], %[vr0] offset:40960\n\t"
    "v_mfma_f32_16x16x32_bf16 v[240:243], a[192:195], a[116:119], v[240:243]\n\t" "ds_read_b64_tr_b16 a[210:211], %[vr0] offset:43008\n\t" "s_add_u32 s40, s40, 4096\n\t" "s_addc_u32 s41, s41, 0\n\t"
    "global_load_dwordx4 a[152:155], %[vx], s[40:41] offset:0 sc1\n\t" "v_mfma_f32_16x16x32_bf16 v[228:231], a[196:199], a[112:115], v[228:231]\n\t"
    "ds_read_b64_tr_b16 a[212:213], %[vr1] offset:40960\n\t" "v_mfma_f32_16x16x32_bf16 v[244:247], a[196:199], a[116:119], v[244:247]\n\t" "ds_read_b64_tr_b16 a[214:215], %[vr1] offset:43008\n\t"
    "s_waitcnt lgkmcnt(4)\n\t" "v_mfma_f32_16x16x32_bf16 v[232:235], a[200:203], a[112:115], v[232:235]\n\t" "ds_read_b64_tr_b16 a[216:217], %[vr2] offset:40960\n\t"
    "global_load_dwordx4 a[156:159], %[vx], s[40:41] offset:1024 sc1\n\t" "v_mfma_f32_16x16x32_bf16 v[248:251], a[200:203], a[116:119], v[248:251]\n\t"
    "ds_read_b64_tr_b16 a[218:219], %[vr2] offset:43008\n\t" "v_mfma_f32_16x16x32_bf16 v[236:239], a[204:207], a[112:115], v[236:239]\n\t" "ds_read_b64_tr_b16 a[220:221], %[vr3] offset:40960\n\t"
    "v_mfma_f32_16x16x32_bf16 v[252:255], a[204:207], a[116:119], v[252:255]\n\t" "ds_read_b64_tr_b16 a[222:223], %[vr3] offset:43008\n\t" "s_waitcnt lgkmcnt(4)\n\t" "s_waitcnt vmcnt(11)\n\t"
    "v_mfma_f32_16x16x32_bf16 v[224:227], a[208:211], a[120:123], v[224:227]\n\t" "s_barrier\n\t" "ds_read_b64_tr_b16 a[192:193], %[vr8] offset:8192\n\t"
    "v_mfma_f32_16x16x32_bf16 v[240:243], a[208:211], a[124:127], v[240:243]\n\t" "ds_read_b64_tr_b16 a[194:195], %[vr8] offset:10240\n\t"
    "global_load_dwordx4 a[160:163], %[vx], s[40:41] offset:2048 sc1\n\t" "v_mfma_f32_16x16x32_bf16 v[228:231], a[212:215], a[120:123], v[228:231]\n\t"
    "ds_read_b64_tr_b16 a[196:197], %[vr9] offset:8192\n\t" "global_load_dwordx4 a[164:167], %[vx], s[40:41] offset:3072 sc1\n\t"
    "v_mfma_f32_16x16x32_bf16 v[244:247], a[212:215], a[124:127], v[244:247]\n\t" "ds_read_b64_tr_b16 a[198:199], %[vr9] offset:10240\n\t" "s_waitcnt lgkmcnt(4)\n\t" "s_add_u32 m0, %[sl1], 73728\n\t"
    "v_mfma_f32_16x16x32_bf16 v[232:235], a[216:219], a[120:123], v[232:235]\n\t" "ds_read_b64_tr_b16 a[200:201], %[vr10] offset:8192\n\t" "global_load_lds_dwordx4 %[vb1], %[sb]\n\t"
    "v_add_u32 %[vb1], 0x10000, %[vb1]\n\t" "v_mfma_f32_16x16x32_bf16 v[248:251], a[216:219], a[124:127], v[248:251]\n\t" "ds_read_b64_tr_b16 a[202:203], %[vr10] offset:10240\n\t"
    "s_add_u32 m0, %[sl1], 81920\n\t" "v_mfma_f32_16x16x32_bf16 v[236:239], a[220:223], a[120:123], v[236:239]\n\t" "ds_read_b64_tr_b16 a[204:205], %[vr11] offset:8192\n\t"
    "global_load_lds_dwordx4 %[vb1], %[sb]\n\t" "v_add_u32 %[vb1], 0x10000, %[vb1]\n\t" "v_mfma_f32_16x16x32_bf16 v[252:255], a[220:223], a[124:127], v[252:255]\n\t"
    "ds_read_b64_tr_b16 a[206:207], %[vr11] offset:10240\n\t" "s_waitcnt lgkmcnt(4)\n\t" "s_waitcnt vmcnt(12)\n\t" "v_mfma_f32_16x16x32_bf16 v[224:227], a[192:195], a[128:131], v[224:227]\n\t"
    "s_barrier\n\t" "ds_read_b64_tr_b16 a[208:209], %[vr8] offset:16384\n\t" "s_add_u32 m0, %[sl1], 90112\n\t" "v_mfma_f32_16x16x32_bf16 v[240:243], a[192:195], a[132:135], v[240:243]\n\t"
    "ds_read_b64_tr_b16 a[210:211], %[vr8] offset:18432\n\t" "global_load_lds_dwordx4 %[vb1], %[sb]\n\t" "v_add_u32 %[vb1], 0x10000, %[vb1]\n\t" "s_add_u32 m0, %[sl1], 98304\n\t"
    "v_mfma_f32_16x16x32_bf16 v[228:231], a[196:199], a[128:131], v[228:231]\n\t" "ds_read_b64_tr_b16 a[212:213], %[vr9] offset:16384\n\t" "global_load_lds_dwordx4 %[vb1], %[sb]\n\t"
    "v_add_u32 %[vb1], 0x10000, %[vb1]\n\t" "v_mfma_f32_16x16x32_bf16 v[244:247], a[196:199], a[132:135], v[244:247]\n\t" "ds_read_b64_tr_b16 a[214:215], %[vr9] offset:18432\n\t"
    "s_waitcnt lgkmcnt(4)\n\t" "s_add_u32 m0, %[sl1], 106496\n\t" "v_mfma_f32_16x16x32_bf16 v[232:235], a[200:203], a[128:131], v[232:235]\n\t" "ds_read_b64_tr_b16 a[216:217], %[vr10] offset:16384\n\t"
    "global_load_lds_dwordx4 %[vb1], %[sb]\n\t" "v_add_u32 %[vb1], 0x10000, %[vb1]\n\t" "v_mfma_f32_16x16x32_bf16 v[248:251], a[200:203], a[132:135], v[248:251]\n\t"
    "ds_read_b64_tr_b16 a[218:219], %[vr10] offset:18432\n\t" "s_add_u32 m0, %[sl1], 24576\n\t" "v_mfma_f32_16x16x32_bf16 v[236:239], a[204:207], a[128:131], v[236:239]\n\t"
    "ds_read_b64_tr_b16 a[220:221], %[vr11] offset:16384\n\t" "global_load_lds_dwordx4 %[vb1], %[sb]\n\t" "v_add_u32 %[vb1], 0x10000, %[vb1]\n\t"
    "v_mfma_f32_16x16x32_bf16 v[252:255], a[204:207], a[132:135], v[252:255]\n\t" "ds_read_b64_tr_b16 a[222:223], %[vr11] offset:18432\n\t" "s_waitcnt lgkmcnt(4)\n\t" "s_waitcnt vmcnt(12)\n\t"
    "v_mfma_f32_16x16x32_bf16 v[224:227], a[208:211], a[136:139], v[224:227]\n\t" "s_barrier\n\t" "ds_read_b64_tr_b16 a[192:193], %[vr8] offset:24576\n\t" "s_add_u32 m0, %[sl1], 32768\n\t"
    "v_mfma_f32_16x16x32_bf16 v[240:243], a[208:211], a[140:143], v[240:243]\n\t" "ds_read_b64_tr_b16 a[194:195], %[vr8] offset:26624\n\t" "global_load_lds_dwordx4 %[vb1], %[sb]\n\t"
    "v_add_u32 %[vb1], 0x10000, %[vb1]\n\t" "s_add_u32 m0, %[sl1], 40960\n\t" "v_mfma_f32_16x16x32_bf16 v[228:231], a[212:215], a[136:139], v[228:231]\n\t"
    "ds_read_b64_tr_b16 a[196:197], %[vr9] offset:24576\n\t" "global_load_lds_dwordx4 %[vb1], %[sb]\n\t" "v_add_u32 %[vb1], 0x10000, %[vb1]\n\t"
    "v_mfma_f32_16x16x32_bf16 v[244:247], a[212:215], a[140:143], v[244:247]\n\t" "ds_read_b64_tr_b16 a[198:199], %[vr9] offset:26624\n\t" "s_waitcnt lgkmcnt(4)\n\t" "s_add_u32 m0, %[sl1], 139264\n\t"
    "v_mfma_f32_16x16x32_bf16 v[232:235], a[216:219], a[136:139], v[232:235]\n\t" "ds_read_b64_tr_b16 a[200:201], %[vr10] offset:24576\n\t" "global_load_lds_dwordx4 %[vb1], %[sb]\n\t"
    "v_add_u32 %[vb1], 0x10000, %[vb1]\n\t" "v_mfma_f32_16x16x32_bf16 v[248:251], a[216:219], a[140:143], v[248:251]\n\t" "ds_read_b64_tr_b16 a[202:203], %[vr10] offset:26624\n\t"
    "s_add_u32 m0, %[sl1], 147456\n\t" "v_mfma_f32_16x16x32_bf16 v[236:239], a[220:223], a[136:139], v[236:239]\n\t" "ds_read_b64_tr_b16 a[204:205], %[vr11] offset:24576\n\t"
    "global_load_lds_dwordx4 %[vb1], %[sb]\n\t" "v_add_u32 %[vb1], 0x10000, %[vb1]\n\t" "v_mfma_f32_16x16x32_bf16 v[252:255], a[220:223], a[140:143], v[252:255]\n\t"
    "ds_read_b64_tr_b16 a[206:207], %[vr11] offset:26624\n\t" "s_waitcnt lgkmcnt(4)\n\t" "s_waitcnt vmcnt(14)\n\t" "v_mfma_f32_16x16x32_bf16 v[224:227], a[192:195], a[144:147], v[224:227]\n\t"
    "s_barrier\n\t" "ds_read_b64_tr_b16 a[208:209], %[vr0] offset:0\n\t" "s_add_u32 m0, %[sl1], 155648\n\t" "v_mfma_f32_16x16x32_bf16 v[240:243], a[192:195], a[148:151], v[240:243]\n\t"
    "ds_read_b64_tr_b16 a[210:211], %[vr0] offset:2048\n\t" "global_load_lds_dwordx4 %[vb1], %[sb]\n\t" "v_add_u32 %[vb1], 0x10000, %[vb1]\n\t"
    "v_mfma_f32_16x16x32_bf16 v[228:231], a[196:199], a[144:147], v[228:231]\n\t" "ds_read_b64_tr_b16 a[212:213], %[vr1] offset:0\n\t" "s_add_u32 m0, %[sl1], 0\n\t"
    "v_mfma_f32_16x16x32_bf16 v[244:247], a[196:199], a[148:151], v[244:247]\n\t" "ds_read_b64_tr_b16 a[214:215], %[vr1] offset:2048\n\t" "global_load_lds_dwordx4 %[vb1], %[sb]\n\t"
    "v_add_u32 %[vb1], 0x10000, %[vb1]\n\t" "s_waitcnt lgkmcnt(4)\n\t" "v_mfma_f32_16x16x32_bf16 v[232:235], a[200:203], a[144:147], v[232:235]\n\t" "ds_read_b64_tr_b16 a[216:217], %[vr2] offset:0\n\t"
    "s_add_u32 m0, %[sl1], 8192\n\t" "v_mfma_f32_16x16x32_bf16 v[248:251], a[200:203], a[148:151], v[248:251]\n\t" "ds_read_b64_tr_b16 a[218:219], %[vr2] offset:2048\n\t"
    "global_load_lds_dwordx4 %[vb1], %[sb]\n\t" "v_add_u32 %[vb1], 0x10000, %[vb1]\n\t" "v_mfma_f32_16x16x32_bf16 v[236:239], a[204:207], a[144:147], v[236:239]\n\t"
    "ds_read_b64_tr_b16 a[220:221], %[vr3] offset:0\n\t" "v_mfma_f32_16x16x32_bf16 v[252:255], a[204:207], a[148:151], v[252:255]\n\t" "ds_read_b64_tr_b16 a[222:223], %[vr3] offset:2048\n\t"
    "s_waitcnt lgkmcnt(4)\n\t" "s_waitcnt vmcnt(15)\n\t" "v_mfma_f32_16x16x32_bf16 v[224:227], a[208:211], a[152:155], v[224:227]\n\t" "s_barrier\n\t"
    "ds_read_b64_tr_b16 a[192:193], %[vr0] offset:8192\n\t" "v_mfma_f32_16x16x32_bf16 v[240:243], a[208:211], a[156:159], v[240:243]\n\t" "ds_read_b64_tr_b16 a[194:195], %[vr0] offset:10240\n\t"
    "v_mfma_f32_16x16x32_bf16 v[228:231], a[212:215], a[152:155], v[228:231]\n\t" "ds_read_b64_tr_b16 a[196:197], %[vr1] offset:8192\n\t"
    "v_mfma_f32_16x16x32_bf16 v[244:247], a[212:215], a[156:159], v[244:247]\n\t" "ds_read_b64_tr_b16 a[198:199], %[vr1] offset:10240\n\t" "s_waitcnt lgkmcnt(4)\n\t"
    "v_mfma_f32_16x16x32_bf16 v[232:235], a[216:219], a[152:155], v[232:235]\n\t" "ds_read_b64_tr_b16 a[200:201], %[vr2] offset:8192\n\t"
    "v_mfma_f32_16x16x32_bf16 v[248:251], a[216:219], a[156:159], v[248:251]\n\t" "ds_read_b64_tr_b16 a[202:203], %[vr2] offset:10240\n\t"
    "v_mfma_f32_16x16x32_bf16 v[236:239], a[220:223], a[152:155], v[236:239]\n\t" "ds_read_b64_tr_b16 a[204:205], %[vr3] offset:8192\n\t"
    "v_mfma_f32_16x16x32_bf16 v[252:255], a[220:223], a[156:159], v[252:255]\n\t" "ds_read_b64_tr_b16 a[206:207], %[vr3] offset:10240\n\t" "s_waitcnt lgkmcnt(4)\n\t" "s_waitcnt vmcnt(13)\n\t"
    "v_mfma_f32_16x16x32_bf16 v[224:227], a[192:195], a[160:163], v[224:227]\n\t" "s_waitcnt vmcnt(9)\n\t" "s_barrier\n\t" "ds_read_b64_tr_b16 a[208:209], %[vr16] offset:8192\n\t"
    "v_mfma_f32_16x16x32_bf16 v[240:243], a[192:195], a[164:167], v[240:243]\n\t" "ds_read_b64_tr_b16 a[210:211], %[vr16] offset:10240\n\t"
    "v_mfma_f32_16x16x32_bf16 v[228:231], a[196:199], a[160:163], v[228:231]\n\t" "ds_read_b64_tr_b16 a[212:213], %[vr17] offset:8192\n\t"
    "v_mfma_f32_16x16x32_bf16 v[244:247], a[196:199], a[164:167], v[244:247]\n\t" "ds_read_b64_tr_b16 a[214:215], %[vr17] offset:10240\n\t" "s_waitcnt lgkmcnt(4)\n\t"
    "v_mfma_f32_16x16x32_bf16 v[232:235], a[200:203], a[160:163], v[232:235]\n\t" "ds_read_b64_tr_b16 a[216:217], %[vr18] offset:8192\n\t"
    "v_mfma_f32_16x16x32_bf16 v[248:251], a[200:203], a[164:167], v[248:251]\n\t" "ds_read_b64_tr_b16 a[218:219], %[vr18] offset:10240\n\t"
    "v_mfma_f32_16x16x32_bf16 v[236:239], a[204:207], a[160:163], v[236:239]\n\t" "ds_read_b64_tr_b16 a[220:221], %[vr19] offset:8192\n\t"
    "v_mfma_f32_16x16x32_bf16 v[252:255], a[204:207], a[164:167], v[252:255]\n\t" "ds_read_b64_tr_b16 a[222:223], %[vr19] offset:10240\n\t" "s_waitcnt lgkmcnt(4)\n\t"
    "v_mfma_f32_16x16x32_bf16 %0, a[208:211], a[64:67], %0\n\t" "ds_read_b64_tr_b16 a[192:193], %[vr16] offset:16384\n\t" "v_mfma_f32_16x16x32_bf16 %1, a[208:211], a[68:71], %1\n\t"
    "ds_read_b64_tr_b16 a[194:195], %[vr16] offset:18432\n\t" "v_mfma_f32_16x16x32_bf16 %2, a[212:215], a[64:67], %2\n\t" "ds_read_b64_tr_b16 a[196:197], %[vr17] offset:16384\n\t"
    "v_mfma_f32_16x16x32_bf16 %3, a[212:215], a[68:71], %3\n\t" "ds_read_b64_tr_b16 a[198:199], %[vr17] offset:18432\n\t" "s_waitcnt lgkmcnt(4)\n\t"
    "v_mfma_f32_16x16x32_bf16 %4, a[216:219], a[64:67], %4\n\t" "ds_read_b64_tr_b16 a[200:201], %[vr18] offset:16384\n\t" "v_mfma_f32_16x16x32_bf16 %5, a[216:219], a[68:71], %5\n\t"
    "ds_read_b64_tr_b16 a[202:203], %[vr18] offset:18432\n\t" "v_mfma_f32_16x16x32_bf16 %6, a[220:223], a[64:67], %6\n\t" "ds_read_b64_tr_b16 a[204:205], %[vr19] offset:16384\n\t"
    "v_mfma_f32_16x16x32_bf16 %7, a[220:223], a[68:71], %7\n\t" "ds_read_b64_tr_b16 a[206:207], %[vr19] offset:18432\n\t" "s_waitcnt lgkmcnt(4)\n\t"
    "v_mfma_f32_16x16x32_bf16 %0, a[192:195], a[72:75], %0\n\t" "ds_read_b64_tr_b16 a[208:209], %[vr16] offset:24576\n\t" "v_cvt_pk_bf16_f32 v224, v224, v225\n\t" "v_cvt_pk_bf16_f32 v225, v226, v227\n\t"
    "v_mfma_f32_16x16x32_bf16 %1, a[192:195], a[76:79], %1\n\t" "ds_read_b64_tr_b16 a[210:211], %[vr16] offset:26624\n\t" "v_cvt_pk_bf16_f32 v226, v228, v229\n\t" "v_cvt_pk_bf16_f32 v227, v230, v231\n\t"
    "global_store_dwordx4 %[vx], v[224:227], %[sm] offset:0 sc1\n\t" "v_mfma_f32_16x16x32_bf16 %2, a[196:199], a[72:75], %2\n\t" "ds_read_b64_tr_b16 a[212:213], %[vr17] offset:24576\n\t"
    "v_cvt_pk_bf16_f32 v232, v232, v233\n\t" "v_cvt_pk_bf16_f32 v233, v234, v235\n\t" "v_mfma_f32_16x16x32_bf16 %3, a[196:199], a[76:79], %3\n\t" "ds_read_b64_tr_b16 a[214:215], %[vr17] offset:26624\n\t"
    "v_cvt_pk_bf16_f32 v234, v236, v237\n\t" "v_cvt_pk_bf16_f32 v235, v238, v239\n\t" "global_store_dwordx4 %[vx], v[232:235], %[sm] offset:1024 sc1\n\t" "s_waitcnt lgkmcnt(4)\n\t"
    "v_mfma_f32_16x16x32_bf16 %4, a[200:203], a[72:75], %4\n\t" "ds_read_b64_tr_b16 a[216:217], %[vr18] offset:24576\n\t" "v_cvt_pk_bf16_f32 v240, v240, v241\n\t" "v_cvt_pk_bf16_f32 v241, v242, v243\n\t"
    "v_mfma_f32_16x16x32_bf16 %5, a[200:203], a[76:79], %5\n\t" "ds_read_b64_tr_b16 a[218:219], %[vr18] offset:26624\n\t" "v_cvt_pk_bf16_f32 v242, v244, v245\n\t" "v_cvt_pk_bf16_f32 v243, v246, v247\n\t"
    "global_store_dwordx4 %[vx], v[240:243], %[sm] offset:2048 sc1\n\t" "v_mfma_f32_16x16x32_bf16 %6, a[204:207], a[72:75], %6\n\t" "ds_read_b64_tr_b16 a[220:221], %[vr19] offset:24576\n\t"
    "v_cvt_pk_bf16_f32 v248, v248, v249\n\t" "v_cvt_pk_bf16_f32 v249, v250, v251\n\t" "v_mfma_f32_16x16x32_bf16 %7, a[204:207], a[76:79], %7\n\t" "ds_read_b64_tr_b16 a[222:223], %[vr19] offset:26624\n\t"
    "v_cvt_pk_bf16_f32 v250, v252, v253\n\t" "v_cvt_pk_bf16_f32 v251, v254, v255\n\t" "global_store_dwordx4 %[vx], v[248:251], %[sm] offset:3072 sc1\n\t" "s_waitcnt lgkmcnt(4)\n\t"
    "v_mfma_f32_16x16x32_bf16 %0, a[208:211], a[80:83], %0\n\t" "ds_read_b64_tr_b16 a[192:193], %[vr16] offset:32768\n\t" "v_mfma_f32_16x16x32_bf16 %1, a[208:211], a[84:87], %1\n\t"
    "ds_read_b64_tr_b16 a[194:195], %[vr16] offset:34816\n\t" "v_mfma_f32_16x16x32_bf16 %2, a[212:215], a[80:83], %2\n\t" "ds_read_b64_tr_b16 a[196:197], %[vr17] offset:32768\n\t"
    "v_mfma_f32_16x16x32_bf16 %3, a[212:215], a[84:87], %3\n\t" "ds_read_b64_tr_b16 a[198:199], %[vr17] offset:34816\n\t" "s_waitcnt lgkmcnt(4)\n\t"
    "v_mfma_f32_16x16x32_bf16 %4, a[216:219], a[80:83], %4\n\t" "ds_read_b64_tr_b16 a[200:201], %[vr18] offset:32768\n\t" "v_mfma_f32_16x16x32_bf16 %5, a[216:219], a[84:87], %5\n\t"
    "ds_read_b64_tr_b16 a[202:203], %[vr18] offset:34816\n\t" "v_mfma_f32_16x16x32_bf16 %6, a[220:223], a[80:83], %6\n\t" "ds_read_b64_tr_b16 a[204:205], %[vr19] offset:32768\n\t"
    "v_mfma_f32_16x16x32_bf16 %7, a[220:223], a[84:87], %7\n\t" "ds_read_b64_tr_b16 a[206:207], %[vr19] offset:34816\n\t" "s_waitcnt lgkmcnt(4)\n\t"
    "v_mfma_f32_16x16x32_bf16 %0, a[192:195], a[88:91], %0\n\t" "s_waitcnt vmcnt(9)\n\t" "s_barrier\n\t" "ds_read_b64_tr_b16 a[208:209], %[vr16] offset:40960\n\t"
    "v_mfma_f32_16x16x32_bf16 %1, a[192:195], a[92:95], %1\n\t" "ds_read_b64_tr_b16 a[210:211], %[vr16] offset:43008\n\t" "v_mfma_f32_16x16x32_bf16 %2, a[196:199], a[88:91], %2\n\t"
    "ds_read_b64_tr_b16 a[212:213], %[vr17] offset:40960\n\t" "v_mfma_f32_16x16x32_bf16 %3, a[196:199], a[92:95], %3\n\t" "ds_read_b64_tr_b16 a[214:215], %[vr17] offset:43008\n\t"
    "s_waitcnt lgkmcnt(4)\n\t" "v_mfma_f32_16x16x32_bf16 %4, a[200:203], a[88:91], %4\n\t" "ds_read_b64_tr_b16 a[216:217], %[vr18] offset:40960\n\t"
    "v_mfma_f32_16x16x32_bf16 %5, a[200:203], a[92:95], %5\n\t" "ds_read_b64_tr_b16 a[218:219], %[vr18] offset:43008\n\t" "v_mfma_f32_16x16x32_bf16 %6, a[204:207], a[88:91], %6\n\t"
    "ds_read_b64_tr_b16 a[220:221], %[vr19] offset:40960\n\t" "v_mfma_f32_16x16x32_bf16 %7, a[204:207], a[92:95], %7\n\t" "ds_read_b64_tr_b16 a[222:223], %[vr19] offset:43008\n\t"
    "s_waitcnt lgkmcnt(4)\n\t" "v_mfma_f32_16x16x32_bf16 %0, a[208:211], a[96:99], %0\n\t" "ds_read_b64_tr_b16 a[192:193], %[vr12] offset:24576\n\t"
    "v_mfma_f32_16x16x32_bf16 %1, a[208:211], a[100:103], %1\n\t" "ds_read_b64_tr_b16 a[194:195], %[vr12] offset:26624\n\t" "v_mfma_f32_16x16x32_bf16 %2, a[212:215], a[96:99], %2\n\t"
    "ds_read_b64_tr_b16 a[196:197], %[vr13] offset:24576\n\t" "v_mfma_f32_16x16x32_bf16 %3, a[212:215], a[100:103], %3\n\t" "ds_read_b64_tr_b16 a[198:199], %[vr13] offset:26624\n\t"
    "s_waitcnt lgkmcnt(4)\n\t" "v_mfma_f32_16x16x32_bf16 %4, a[216:219], a[96:99], %4\n\t" "ds_read_b64_tr_b16 a[200:201], %[vr14] offset:24576\n\t"
    "v_mfma_f32_16x16x32_bf16 %5, a[216:219], a[100:103], %5\n\t" "ds_read_b64_tr_b16 a[202:203], %[vr14] offset:26624\n\t" "v_mfma_f32_16x16x32_bf16 %6, a[220:223], a[96:99], %6\n\t"
    "ds_read_b64_tr_b16 a[204:205], %[vr15] offset:24576\n\t" "v_mfma_f32_16x16x32_bf16 %7, a[220:223], a[100:103], %7\n\t" "ds_read_b64_tr_b16 a[206:207], %[vr15] offset:26624\n\t"
    "s_waitcnt lgkmcnt(4)\n\t" "v_mfma_f32_16x16x32_bf16 %0, a[192:195], a[104:107], %0\n\t" "ds_read_b64_tr_b16 a[208:209], %[vr12] offset:32768\n\t"
    "v_mfma_f32_16x16x32_bf16 %1, a[192:195], a[108:111], %1\n\t" "ds_read_b64_tr_b16 a[210:211], %[vr12] offset:34816\n\t" "v_mfma_f32_16x16x32_bf16 %2, a[196:199], a[104:107], %2\n\t"
    "ds_read_b64_tr_b16 a[212:213], %[vr13] offset:32768\n\t" "v_mfma_f32_16x16x32_bf16 %3, a[196:199], a[108:111], %3\n\t" "ds_read_b64_tr_b16 a[214:215], %[vr13] offset:34816\n\t"
    "s_waitcnt lgkmcnt(4)\n\t" "v_mfma_f32_16x16x32_bf16 %4, a[200:203], a[104:107], %4\n\t" "ds_read_b64_tr_b16 a[216:217], %[vr14] offset:32768\n\t"
    "v_mfma_f32_16x16x32_bf16 %5, a[200:203], a[108:111], %5\n\t" "ds_read_b64_tr_b16 a[218:219], %[vr14] offset:34816\n\t" "v_mfma_f32_16x16x32_bf16 %6, a[204:207], a[104:107], %6\n\t"
    "ds_read_b64_tr_b16 a[220:221], %[vr15] offset:32768\n\t" "v_mfma_f32_16x16x32_bf16 %7, a[204:207], a[108:111], %7\n\t" "ds_read_b64_tr_b16 a[222:223], %[vr15] offset:34816\n\t"
    "s_waitcnt lgkmcnt(4)\n\t" "v_mfma_f32_16x16x32_bf16 %0, a[208:211], a[112:115], %0\n\t" "ds_read_b64_tr_b16 a[192:193], %[vr12] offset:40960\n\t"
    "v_mfma_f32_16x16x32_bf16 %1, a[208:211], a[116:119], %1\n\t" "ds_read_b64_tr_b16 a[194:195], %[vr12] offset:43008\n\t" "v_mfma_f32_16x16x32_bf16 %2, a[212:215], a[112:115], %2\n\t"
    "ds_read_b64_tr_b16 a[196:197], %[vr13] offset:40960\n\t" "v_mfma_f32_16x16x32_bf16 %3, a[212:215], a[116:119], %3\n\t" "ds_read_b64_tr_b16 a[198:199], %[vr13] offset:43008\n\t"
    "s_waitcnt lgkmcnt(4)\n\t" "v_mfma_f32_16x16x32_bf16 %4, a[216:219], a[112:115], %4\n\t" "ds_read_b64_tr_b16 a[200:201], %[vr14] offset:40960\n\t"
    "v_mfma_f32_16x16x32_bf16 %5, a[216:219], a[116:119], %5\n\t" "ds_read_b64_tr_b16 a[202:203], %[vr14] offset:43008\n\t" "v_mfma_f32_16x16x32_bf16 %6, a[220:223], a[112:115], %6\n\t"
    "ds_read_b64_tr_b16 a[204:205], %[vr15] offset:40960\n\t" "v_mfma_f32_16x16x32_bf16 %7, a[220:223], a[116:119], %7\n\t" "ds_read_b64_tr_b16 a[206:207], %[vr15] offset:43008\n\t"
    "s_waitcnt lgkmcnt(4)\n\t" "v_mfma_f32_16x16x32_bf16 %0, a[192:195], a[120:123], %0\n\t" "s_waitcnt vmcnt(5)\n\t" "s_barrier\n\t" "ds_read_b64_tr_b16 a[208:209], %[vr20] offset:8192\n\t"
    "v_mfma_f32_16x16x32_bf16 %1, a[192:195], a[124:127], %1\n\t" "ds_read_b64_tr_b16 a[210:211], %[vr20] offset:10240\n\t" "v_mfma_f32_16x16x32_bf16 %2, a[196:199], a[120:123], %2\n\t"
    "ds_read_b64_tr_b16 a[212:213], %[vr21] offset:8192\n\t" "v_mfma_f32_16x16x32_bf16 %3, a[196:199], a[124:127], %3\n\t" "ds_read_b64_tr_b16 a[214:215], %[vr21] offset:10240\n\t"
    "s_waitcnt lgkmcnt(4)\n\t" "v_mfma_f32_16x16x32_bf16 %4, a[200:203], a[120:123], %4\n\t" "ds_read_b64_tr_b16 a[216:217], %[vr22] offset:8192\n\t"
    "v_mfma_f32_16x16x32_bf16 %5, a[200:203], a[124:127], %5\n\t" "ds_read_b64_tr_b16 a[218:219], %[vr22] offset:10240\n\t" "v_mfma_f32_16x16x32_bf16 %6, a[204:207], a[120:123], %6\n\t"
    "ds_read_b64_tr_b16 a[220:221], %[vr23] offset:8192\n\t" "v_mfma_f32_16x16x32_bf16 %7, a[204:207], a[124:127], %7\n\t" "ds_read_b64_tr_b16 a[222:223], %[vr23] offset:10240\n\t"
    "s_waitcnt vmcnt(0)\n\t" "s_mov_b64 exec, 1\n\t" "global_store_dwordx2 %[vz], %[tv], %[sf] sc1\n\t" "s_mov_b64 exec, -1\n\t" "s_waitcnt lgkmcnt(4)\n\t"
    "v_mfma_f32_16x16x32_bf16 %0, a[208:211], a[128:131], %0\n\t" "ds_read_b64_tr_b16 a[192:193], %[vr20] offset:16384\n\t" "v_mfma_f32_16x16x32_bf16 %1, a[208:211], a[132:135], %1\n\t"
    "ds_read_b64_tr_b16 a[194:195], %[vr20] offset:18432\n\t" "v_mfma_f32_16x16x32_bf16 %2, a[212:215], a[128:131], %2\n\t" "ds_read_b64_tr_b16 a[196:197], %[vr21] offset:16384\n\t"
    "v_mfma_f32_16x16x32_bf16 %3, a[212:215], a[132:135], %3\n\t" "ds_read_b64_tr_b16 a[198:199], %[vr21] offset:18432\n\t" "s_waitcnt lgkmcnt(4)\n\t"
    "v_mfma_f32_16x16x32_bf16 %4, a[216:219], a[128:131], %4\n\t" "ds_read_b64_tr_b16 a[200:201], %[vr22] offset:16384\n\t" "v_mfma_f32_16x16x32_bf16 %5, a[216:219], a[132:135], %5\n\t"
    "ds_read_b64_tr_b16 a[202:203], %[vr22] offset:18432\n\t" "v_mfma_f32_16x16x32_bf16 %6, a[220:223], a[128:131], %6\n\t" "ds_read_b64_tr_b16 a[204:205], %[vr23] offset:16384\n\t"
    "v_mfma_f32_16x16x32_bf16 %7, a[220:223], a[132:135], %7\n\t" "ds_read_b64_tr_b16 a[206:207], %[vr23] offset:18432\n\t" "s_waitcnt lgkmcnt(4)\n\t"
    "v_mfma_f32_16x16x32_bf16 %0, a[192:195], a[136:139], %0\n\t" "ds_read_b64_tr_b16 a[208:209], %[vr20] offset:24576\n\t" "v_mfma_f32_16x16x32_bf16 %1, a[192:195], a[140:143], %1\n\t"
    "ds_read_b64_tr_b16 a[210:211], %[vr20] offset:26624\n\t" "v_mfma_f32_16x16x32_bf16 %2, a[196:199], a[136:139], %2\n\t" "ds_read_b64_tr_b16 a[212:213], %[vr21] offset:24576\n\t"
    "v_mfma_f32_16x16x32_bf16 %3, a[196:199], a[140:143], %3\n\t" "ds_read_b64_tr_b16 a[214:215], %[vr21] offset:26624\n\t" "s_waitcnt lgkmcnt(4)\n\t"
    "v_mfma_f32_16x16x32_bf16 %4, a[200:203], a[136:139], %4\n\t" "ds_read_b64_tr_b16 a[216:217], %[vr22] offset:24576\n\t" "v_mfma_f32_16x16x32_bf16 %5, a[200:203], a[140:143], %5\n\t"
    "ds_read_b64_tr_b16 a[218:219], %[vr22] offset:26624\n\t" "v_mfma_f32_16x16x32_bf16 %6, a[204:207], a[136:139], %6\n\t" "ds_read_b64_tr_b16 a[220:221], %[vr23] offset:24576\n\t"
    "v_mfma_f32_16x16x32_bf16 %7, a[204:207], a[140:143], %7\n\t" "ds_read_b64_tr_b16 a[222:223], %[vr23] offset:26624\n\t" "s_waitcnt lgkmcnt(4)\n\t"
    "v_mfma_f32_16x16x32_bf16 %0, a[208:211], a[144:147], %0\n\t" "ds_read_b64_tr_b16 a[192:193], %[vr12] offset:0\n\t" "global_load_dwordx2 %[fl], %[vz], %[sq] sc1\n\t"
    "v_mfma_f32_16x16x32_bf16 %1, a[208:211], a[148:151], %1\n\t" "ds_read_b64_tr_b16 a[194:195], %[vr12] offset:2048\n\t" "v_mfma_f32_16x16x32_bf16 %2, a[212:215], a[144:147], %2\n\t"
    "ds_read_b64_tr_b16 a[196:197], %[vr13] offset:0\n\t" "v_mfma_f32_16x16x32_bf16 %3, a[212:215], a[148:151], %3\n\t" "ds_read_b64_tr_b16 a[198:199], %[vr13] offset:2048\n\t" "s_waitcnt lgkmcnt(4)\n\t"
    "v_mfma_f32_16x16x32_bf16 %4, a[216:219], a[144:147], %4\n\t" "ds_read_b64_tr_b16 a[200:201], %[vr14] offset:0\n\t" "v_mfma_f32_16x16x32_bf16 %5, a[216:219], a[148:151], %5\n\t"
    "ds_read_b64_tr_b16 a[202:203], %[vr14] offset:2048\n\t" "v_mfma_f32_16x16x32_bf16 %6, a[220:223], a[144:147], %6\n\t" "ds_read_b64_tr_b16 a[204:205], %[vr15] offset:0\n\t"
    "v_mfma_f32_16x16x32_bf16 %7, a[220:223], a[148:151], %7\n\t" "ds_read_b64_tr_b16 a[206:207], %[vr15] offset:2048\n\t" "s_waitcnt lgkmcnt(4)\n\t"
    "v_mfma_f32_16x16x32_bf16 %0, a[192:195], a[152:155], %0\n\t" "s_barrier\n\t" "ds_read_b64_tr_b16 a[208:209], %[vr12] offset:8192\n\t" "v_mfma_f32_16x16x32_bf16 %1, a[192:195], a[156:159], %1\n\t"
    "ds_read_b64_tr_b16 a[210:211], %[vr12] offset:10240\n\t" "v_mfma_f32_16x16x32_bf16 %2, a[196:199], a[152:155], %2\n\t" "ds_read_b64_tr_b16 a[212:213], %[vr13] offset:8192\n\t"
    "v_mfma_f32_16x16x32_bf16 %3, a[196:199], a[156:159], %3\n\t" "ds_read_b64_tr_b16 a[214:215], %[vr13] offset:10240\n\t" "s_waitcnt lgkmcnt(4)\n\t"
    "v_mfma_f32_16x16x32_bf16 %4, a[200:203], a[152:155], %4\n\t" "ds_read_b64_tr_b16 a[216:217], %[vr14] offset:8192\n\t" "v_mfma_f32_16x16x32_bf16 %5, a[200:203], a[156:159], %5\n\t"
    "ds_read_b64_tr_b16 a[218:219], %[vr14] offset:10240\n\t" "v_mfma_f32_16x16x32_bf16 %6, a[204:207], a[152:155], %6\n\t" "ds_read_b64_tr_b16 a[220:221], %[vr15] offset:8192\n\t"
    "v_mfma_f32_16x16x32_bf16 %7, a[204:207], a[156:159], %7\n\t" "ds_read_b64_tr_b16 a[222:223], %[vr15] offset:10240\n\t" "s_waitcnt lgkmcnt(4)\n\t"
    "v_mfma_f32_16x16x32_bf16 %0, a[208:211], a[160:163], %0\n\t" "v_mfma_f32_16x16x32_bf16 %1, a[208:211], a[164:167], %1\n\t" "v_mfma_f32_16x16x32_bf16 %2, a[212:215], a[160:163], %2\n\t"
    "v_mfma_f32_16x16x32_bf16 %3, a[212:215], a[164:167], %3\n\t" "s_waitcnt lgkmcnt(0)\n\t" "v_mfma_f32_16x16x32_bf16 %4, a[216:219], a[160:163], %4\n\t"
    "v_mfma_f32_16x16x32_bf16 %5, a[216:219], a[164:167], %5\n\t" "v_mfma_f32_16x16x32_bf16 %6, a[220:223], a[160:163], %6\n\t" "v_mfma_f32_16x16x32_bf16 %7, a[220:223], a[164:167], %7\n\t"
    "s_mov_b32 s44, 0x400000\n\t" ".Lpoll_%=:\n\t" "s_waitcnt vmcnt(0)\n\t" "v_cmp_ne_u64 vcc, %[tag], %[fl]\n\t" "s_nop 4\n\t" "s_cbranch_vccz .Lrdy_%=\n\t"
    "global_load_dwordx2 %[fl], %[vz], %[sq] sc1\n\t" "s_sub_u32 s44, s44, 1\n\t" "s_cbranch_scc0 .Lpoll_%=\n\t" ".Lrdy_%=:\n\t" "global_load_dwordx4 %[rx0], %[vx], %[st] offset:0 sc1\n\t"
    "global_load_dwordx4 %[rx1], %[vx], %[st] offset:1024 sc1\n\t" "global_load_dwordx4 %[rx2], %[vx], %[st] offset:2048 sc1\n\t" "global_load_dwordx4 %[rx3], %[vx], %[st] offset:3072 sc1\n\t"
    "s_waitcnt vmcnt(0)\n\t"
    : "=&v"(acc[0]), "=&v"(acc[1]), "=&v"(acc[2]), "=&v"(acc[3]), "=&v"(acc[4]), "=&v"(acc[5]), "=&v"(acc[6]), "=&v"(acc[7]), [rx0] "=&v"(rx[0]), [rx1] "=&v"(rx[1]), [rx2] "=&v"(rx[2]), [rx3] "=&v"(rx[3]), [bv0] "=&v"(bv[0]), [bv1] "=&v"(bv[1]), [bv2] "=&v"(bv[2]), [bv3] "=&v"(bv[3]), [gv0] "=&v"(gv[0]), [gv1] "=&v"(gv[1]), [gv2] "=&v"(gv[2]), [gv3] "=&v"(gv[3]), [fl] "=&v"(fl), [vb0] "+v"(vb0), [vb1] "+v"(vb1)
    : [vr0] "v"(vr[0]), [vr1] "v"(vr[1]), [vr2] "v"(vr[2]), [vr3] "v"(vr[3]), [vr4] "v"(vr[4]), [vr5] "v"(vr[5]), [vr6] "v"(vr[6]), [vr7] "v"(vr[7]), [vr8] "v"(vr[8]), [vr9] "v"(vr[9]), [vr10] "v"(vr[10]), [vr11] "v"(vr[11]), [vr12] "v"(vr[12]), [vr13] "v"(vr[13]), [vr14] "v"(vr[14]), [vr15] "v"(vr[15]), [vr16] "v"(vr[16]), [vr17] "v"(vr[17]), [vr18] "v"(vr[18]), [vr19] "v"(vr[19]), [vr20] "v"(vr[20]), [vr21] "v"(vr[21]), [vr22] "v"(vr[22]), [vr23] "v"(vr[23]), [vo] "v"(vo), [vx] "v"(vx), [vg] "v"(vg), [vf] "v"(vf), [vz] "v"(0), [tv] "v"(tag), [sb] "s"(sb + 524288), [tag] "s"(tag), [sa] "s"(sa), [sl0] "s"(sl0), [sl1] "s"(sl1), [sx] "s"(sx), [sg] "s"(sg), [sm] "s"(sm), [st] "s"(st), [sf] "s"(sf), [sq] "s"(sq), [sw] "s"(sw)
    : "v224", "v225", "v226", "v227", "v228", "v229", "v230", "v231", "v232", "v233", "v234", "v235", "v236", "v237", "v238", "v239", "v240", "v241", "v242", "v243", "v244", "v245", "v246", "v247", "v248", "v249", "v250", "v251", "v252", "v253", "v254", "v255", "vcc", "a0", "a1", "a2", "a3", "a4", "a5", "a6", "a7", "a8", "a9", "a10", "a11", "a12", "a13", "a14", "a15", "a16", "a17", "a18", "a19", "a20", "a21", "a22", "a23", "a24", "a25", "a26", "a27", "a28", "a29", "a30", "a31", "a32", "a33", "a34", "a35", "a36", "a37", "a38", "a39", "a40", "a41", "a42", "a43", "a44", "a45", "a46", "a47", "a48", "a49", "a50", "a51", "a52", "a53", "a54", "a55", "a56", "a57", "a58", "a59", "a60", "a61", "a62", "a63", "a64", "a65", "a66", "a67", "a68", "a69", "a70", "a71", "a72", "a73", "a74", "a75", "a76", "a77", "a78", "a79", "a80", "a81", "a82", "a83", "a84", "a85", "a86", "a87", "a88", "a89", "a90", "a91", "a92", "a93", "a94", "a95", "a96", "a97", "a98", "a99", "a100", "a101", "a102", "a103", "a104", "a105", "a106", "a107", "a108", "a109", "a110", "a111", "a112", "a113", "a114", "a115", "a116", "a117", "a118", "a119", "a120", "a121", "a122", "a123", "a124", "a125", "a126", "a127", "a128", "a129", "a130", "a131", "a132", "a133", "a134", "a135", "a136", "a137", "a138", "a139", "a140", "a141", "a142", "a143", "a144", "a145", "a146", "a147", "a148", "a149", "a150", "a151", "a152", "a153", "a154", "a155", "a156", "a157", "a158", "a159", "a160", "a161", "a162", "a163", "a164", "a165", "a166", "a167", "a168", "a169", "a170", "a171", "a172", "a173", "a174", "a175", "a176", "a177", "a178", "a179", "a180", "a181", "a182", "a183", "a184", "a185", "a186", "a187", "a188", "a189", "a190", "a191", "a192", "a193", "a194", "a195", "a196", "a197", "a198", "a199", "a200", "a201", "a202", "a203", "a204", "a205", "a206", "a207", "a208", "a209", "a210", "a211", "a212", "a213", "a214", "a215", "a216", "a217", "a218", "a219", "a220", "a221", "a222", "a223", "s40", "s41", "s42", "s43", "s44", "s45", "scc", "memory");
  }
  u16* op = out + (size_t)(rb * 128 + wid * 32 + fr) * D_ + cb * 128 + kh * 64 + (fq & 1) * 16 + (fq >> 1) * 8;
#pragma unroll
  for (int i = 0; i < 4; ++i) {
    u32x2 h2[2];
#pragma unroll
    for (int j = 0; j < 2; ++j) { const int n = 2 * (i & 1) + j; const f32x4 t = acc[n * 2 + (i >> 1)] + (f32x4){bf_lo(rx[i][2 * j]), bf_hi(rx[i][2 * j]), bf_lo(rx[i][2 * j + 1]), bf_hi(rx[i][2 * j + 1])};
      h2[j] = (u32x2){pk_bf16(bf_lo(bv[i][2 * j]) + gv[n][0] * t[0], bf_hi(bv[i][2 * j]) + gv[n][1] * t[1]), pk_bf16(bf_lo(bv[i][2 * j + 1]) + gv[n][2] * t[2], bf_hi(bv[i][2 * j + 1]) + gv[n][3] * t[3])}; }
    *(u32x4*)(op + (i >> 1) * 16 * D_ + (i & 1) * 32) = widen(h2[0], h2[1]);
  }
  }
  asm volatile("k_down_end:");
}

extern "C" void kernel_launch(void* const* d_in, const int* in_sizes, int n_in, void* d_out, int out_size, void* d_ws, size_t ws_size, hipStream_t stream) {
  const u16* x = (const u16*)d_in[0]; const u16* wqkv = (const u16*)d_in[1]; const u16* wo = (const u16*)d_in[2];
  const u16* wg = (const u16*)d_in[4]; const u16* wu = (const u16*)d_in[5]; const u16* wd = (const u16*)d_in[6];
  const u16* c = (const u16*)d_in[7]; const u16* wmod = (const u16*)d_in[8];
  k_main<<<256, 256, 0, stream>>>(c, wmod, x, wqkv, wo, wg, wu, wd, (u16*)d_out, (char*)d_ws);
}
```

```cpp
#include <hip/hip_runtime.h>
typedef unsigned short u16;
typedef float f32x4 __attribute__((ext_vector_type(4)));
typedef float f32x2 __attribute__((ext_vector_type(2)));
typedef unsigned u32x4 __attribute__((ext_vector_type(4)));
typedef unsigned u32x2 __attribute__((ext_vector_type(2)));
#define LAS __attribute__((address_space(3)))
#define DI __device__ __forceinline__

constexpr int S_ = 2048, D_ = 1024, F_ = 3584;

constexpr unsigned W_GB = 0, W_EF = 1 << 16, W_TAGS = W_EF + (1 << 15), W_PART = W_TAGS + (1 << 18), W_MODP = W_PART + (1 << 15), W_UVP = W_MODP + (1 << 16),
                 W_STATS = W_UVP + (1 << 19), W_O = W_STATS + 16 * S_ * 8, W_Y = W_O + S_ * D_ * 2, W_HH = W_Y + S_ * D_ * 2,
                 W_XCH = W_HH + S_ * F_ * 2;

DI unsigned f_vb(int wid, int lane) { const int r = wid * 8 + (lane >> 3), c = lane & 7; return (unsigned)(r * 2048 + (((((c >> 1) ^ ((r >> 1) & 3)) << 1) | (c & 1)) << 4)); }
DI unsigned f_vr(int lane, int j) { const int row = lane >> 2; return (unsigned)(row * 128 + ((j ^ ((row >> 1) & 3)) << 5) + (lane & 3) * 8); }

#define FS 256
#define FA(i) ((i) * FS)
constexpr int OF = 0, YF = 32 * FS, HF = 64 * FS, EP = 96 * FS;
extern "C" __global__ __launch_bounds__(256, 1) void k_main(const u16* __restrict__ c, const u16* __restrict__ wmod, const u16* __restrict__ x, const u16* __restrict__ w, const u16* __restrict__ wo,
                                                            const u16* __restrict__ wg, const u16* __restrict__ wu, const u16* __restrict__ pfw, u16* __restrict__ out, char* ws) {
  const char* code; int len;
  asm volatile("s_getpc_b64 %0\n.Lpc%=:\n\ts_mov_b32 %1, k_down_end-.Lpc%=\n\ts_mov_b64 exec, 3\n\tglobal_load_dword a255, %2, %0\n\ts_mov_b64 exec, -1"
               : "=&s"(code), "=s"(len) : "v"(((blockIdx.x & ~7u) + (threadIdx.x >> 5) + (threadIdx.x & 1)) * 128) : "a255");
  asm volatile("" :: "s"(c), "s"(wmod), "s"(x), "s"(w), "s"(wo), "s"(wg), "s"(wu), "s"(pfw), "s"(out), "s"(ws));
  constexpr int SCT = 16384, PH = 18944, RING = 20480, HALO = 131072;
  __shared__ __attribute__((aligned(1024))) char smem[163840];
  LAS char* lds = (LAS char*)smem;
  const unsigned ldsb = (unsigned)(size_t)lds;
  int wid = __builtin_amdgcn_readfirstlane(threadIdx.x >> 6);
  int bx = blockIdx.x;
#define ROT (((loc >> 2) + 1 * (xcd & 6)) & 7)
#define KD ((loc >> 2) * 56 % 56)
#define IDX() const int xcd = bx & 7, loc = bx >> 3, gk = loc >> 3, gc = xcd * 8 + (loc & 7), rb = (xcd >> 1) * 4 + (loc & 3), cb = (xcd & 1) * 8 + (loc >> 2); const unsigned sl1 = ldsb + wid * 1024; const int rot = ROT, co = rot * 2 + (xcd & 1); const char* so = (const char*)(wo + co * 64)
  unsigned long long tag; unsigned cv; float mu[2]; unsigned vbo; const char* cd; int tid = threadIdx.x;
  { IDX();
  { const int lane = threadIdx.x & 63;
    const unsigned vc = threadIdx.x * 2, vs = (lane >> 2) * 12288 + (((lane & 3) ^ ((lane >> 5) << 1)) << 4), sl4 = ldsb + wid * 4096;
    const u16* sc = c + gk * 256; const u16* sw = wmod + (size_t)(gk * 256 + wid * 64) * 6144 + gc * 32; const char* st = ws + W_TAGS + EP * 8;
    asm volatile( "global_load_ushort v204, %[vc], %[sc]\n\t" "s_mov_b64 s[40:41], %[sw]\n\t" "s_add_u32 m0, %[sl4], 0\n\t" "s_nop 0\n\t" "global_load_lds_dwordx4 %[vs], s[40:41] nt\n\t"
    "s_add_u32 s40, s40, 196608\n\t" "s_addc_u32 s41, s41, 0\n\t" "s_add_u32 m0, %[sl4], 1024\n\t" "s_nop 0\n\t" "global_load_lds_dwordx4 %[vs], s[40:41] nt\n\t" "s_add_u32 s40, s40, 196608\n\t"
    "s_addc_u32 s41, s41, 0\n\t" "s_add_u32 m0, %[sl4], 2048\n\t" "s_nop 0\n\t" "global_load_lds_dwordx4 %[vs], s[40:41] nt\n\t" "s_add_u32 s40, s40, 196608\n\t" "s_addc_u32 s41, s41, 0\n\t"
    "s_add_u32 m0, %[sl4], 3072\n\t" "s_nop 0\n\t" "global_load_lds_dwordx4 %[vs], s[40:41] nt\n\t" "s_load_dwordx2 s[62:63], %[st], 0x0\n\t"
    : "=&{v204}"(cv), "=&{s[62:63]}"(tag)
    : [vc] "v"(vc), [vs] "v"(vs), [sc] "s"(sc), [sw] "s"(sw), [sl4] "s"(sl4), [st] "s"(st)
    : "s40", "s41", "scc", "memory");
  }
  { asm volatile("" : "+v"(tid));
    int lane = tid & 63;
    const unsigned sl = ldsb + (wid & 1) * 1024 + (wid >> 1) * 6144;
    const unsigned dx = (lane >> 3) * 2048 + (((lane & 7) ^ (lane >> 3)) << 4); unsigned d0 = dx + ((rb ? 16 : 0) + wid * 32) * 2048, d1 = d0 + 16384, d2 = d0 + 32768, d3 = d0 + 49152;
    const char* xb0 = (const char*)x + (size_t)(rb ? rb * 128 - 16 : 0) * 2048;
    const unsigned xl = (unsigned)(size_t)xb0, xh_ = (unsigned)((size_t)xb0 >> 32), k0 = rot * 256;
    asm volatile("s_add_u32 s45, %[k0], 0\n\t" "s_and_b32 s45, s45, 0x7ff\n\t" "s_add_u32 s64, %[xl], s45\n\t" "s_addc_u32 s65, %[xh_], 0\n\t" "s_add_u32 m0, %[sl], 20480\n\t" "s_nop 0\n\t"
    "global_load_lds_dwordx4 v196, s[64:65] offset:0 nt\n\t" "s_add_u32 m0, %[sl], 32768\n\t" "s_nop 0\n\t" "global_load_lds_dwordx4 v197, s[64:65] offset:0 nt\n\t" "s_add_u32 m0, %[sl], 22528\n\t"
    "s_nop 0\n\t" "global_load_lds_dwordx4 v198, s[64:65] offset:0 nt\n\t" "s_add_u32 m0, %[sl], 34816\n\t" "s_nop 0\n\t" "global_load_lds_dwordx4 v199, s[64:65] offset:0 nt\n\t"
    :: "{v196}"(d0), "{v197}"(d1), "{v198}"(d2), "{v199}"(d3), [sl] "s"(sl), [xl] "s"(xl), [xh_] "s"(xh_), [k0] "s"(k0)
    : "s45", "s64", "s65", "scc", "memory");
    asm volatile("" : "+v"(tid)); lane = tid & 63;
    const int ww = loc * 4 + wid; unsigned tv;
    { const int g = lane >> 4;
      const unsigned vsr = ldsb + (8 * g + ((lane >> 2) & 3)) * 64 + (((wid ^ g) & 1) << 5) + (lane & 3) * 8, vsc = ldsb + g * 16, vcw = ldsb + SCT + tid * 2;
      const char* su = ws + W_PART + (gk * 512 + gc * 8 + wid * 4) * 16 - ldsb; const u16* sq = w;
      const unsigned t0 = ((ww * 2 + (lane & 1)) * 4 + (xcd >> 1)) * 6144 + (xcd & 1) * 1024 + ((384 - (unsigned)(size_t)w) & 896) + (lane >> 1) * 2048;
      asm volatile( "s_mov_b32 s54, 0x07060503\n\t" "s_mov_b32 s55, 0x07060502\n\t" "s_mov_b32 s56, 0x07060501\n\t" "s_mov_b32 s60, 0x00010001\n\t" "s_mov_b32 s61, s60\n\t" "s_add_u32 s45, %[k0], 0\n\t"
    "s_and_b32 s45, s45, 0x7ff\n\t" "s_add_u32 s64, %[xl], s45\n\t" "s_addc_u32 s65, %[xh_], 0\n\t" "s_add_u32 m0, %[sl], 24448\n\t" "s_nop 0\n\t"
    "global_load_lds_dwordx4 v196, s[64:65] offset:128 nt\n\t" "s_add_u32 m0, %[sl], 36736\n\t" "s_nop 0\n\t" "global_load_lds_dwordx4 v197, s[64:65] offset:128 nt\n\t" "s_add_u32 m0, %[sl], 44928\n\t"
    "s_nop 0\n\t" "global_load_lds_dwordx4 v198, s[64:65] offset:128 nt\n\t" "s_add_u32 m0, %[sl], 57216\n\t" "s_nop 0\n\t" "global_load_lds_dwordx4 v199, s[64:65] offset:128 nt\n\t"
    "s_mov_b64 exec, 63\n\t" "global_load_dword v205, %[t0], %[sq]\n\t" "s_mov_b64 exec, -1\n\t" "s_waitcnt vmcnt(13)\n\t" "v_lshlrev_b32 v204, 16, v204\n\t"
    "v_mul_f32 v206, 0xbfb8aa3b, v204\n\t" "v_exp_f32 v206, v206\n\t" "s_nop 0\n\t" "v_add_f32 v206, 1.0, v206\n\t" "v_rcp_f32 v206, v206\n\t" "s_nop 0\n\t" "v_mul_f32 v204, v204, v206\n\t"
    "v_cvt_pk_bf16_f32 v204, v204, v204\n\t" "ds_write_b16 v233, v204\n\t" "s_waitcnt vmcnt(9)\n\t" "s_waitcnt lgkmcnt(0)\n\t" "s_barrier\n\t" "s_add_u32 s62, s62, 1\n\t"
    "s_addc_u32 s63, s63, 0\n\t" "s_cmp_gt_u32 %[wid], 1\n\t" "s_cbranch_scc1 .Lns_%=\n\t" "ds_read_b64_tr_b16 v[48:49], v228 offset:0\n\t" "ds_read_b64_tr_b16 v[50:51], v228 offset:256\n\t"
    "ds_read_b128 v[80:83], v229 offset:16384\n\t" "ds_read_b64_tr_b16 v[52:53], v228 offset:2048\n\t" "ds_read_b64_tr_b16 v[54:55], v228 offset:2304\n\t" "ds_read_b128 v[84:87], v229 offset:16448\n\t"
    "ds_read_b64_tr_b16 v[56:57], v228 offset:4096\n\t" "ds_read_b64_tr_b16 v[58:59], v228 offset:4352\n\t" "ds_read_b128 v[88:91], v229 offset:16512\n\t"
    "ds_read_b64_tr_b16 v[60:61], v228 offset:6144\n\t" "ds_read_b64_tr_b16 v[62:63], v228 offset:6400\n\t" "ds_read_b128 v[92:95], v229 offset:16576\n\t" "s_waitcnt lgkmcnt(9)\n\t"
    "v_mfma_f32_16x16x32_bf16 v[112:115], v[48:51], v[80:83], 0\n\t" "ds_read_b64_tr_b16 v[64:65], v228 offset:8192\n\t" "ds_read_b64_tr_b16 v[66:67], v228 offset:8448\n\t"
    "ds_read_b128 v[96:99], v229 offset:16640\n\t" "s_waitcnt lgkmcnt(9)\n\t" "v_mfma_f32_16x16x32_bf16 v[112:115], v[52:55], v[84:87], v[112:115]\n\t" "ds_read_b64_tr_b16 v[68:69], v228 offset:10240\n\t"
    "ds_read_b64_tr_b16 v[70:71], v228 offset:10496\n\t" "ds_read_b128 v[100:103], v229 offset:16704\n\t" "s_waitcnt lgkmcnt(9)\n\t"
    "v_mfma_f32_16x16x32_bf16 v[112:115], v[56:59], v[88:91], v[112:115]\n\t" "ds_read_b64_tr_b16 v[72:73], v228 offset:12288\n\t" "ds_read_b64_tr_b16 v[74:75], v228 offset:12544\n\t"
    "ds_read_b128 v[104:107], v229 offset:16768\n\t" "s_waitcnt lgkmcnt(9)\n\t" "v_mfma_f32_16x16x32_bf16 v[112:115], v[60:63], v[92:95], v[112:115]\n\t"
    "ds_read_b64_tr_b16 v[76:77], v228 offset:14336\n\t" "ds_read_b64_tr_b16 v[78:79], v228 offset:14592\n\t" "ds_read_b128 v[108:111], v229 offset:16832\n\t" "s_waitcnt lgkmcnt(9)\n\t"
    "v_mfma_f32_16x16x32_bf16 v[112:115], v[64:67], v[96:99], v[112:115]\n\t" "s_waitcnt lgkmcnt(6)\n\t" "v_mfma_f32_16x16x32_bf16 v[112:115], v[68:71], v[100:103], v[112:115]\n\t"
    "s_waitcnt lgkmcnt(3)\n\t" "v_mfma_f32_16x16x32_bf16 v[112:115], v[72:75], v[104:107], v[112:115]\n\t" "s_waitcnt lgkmcnt(0)\n\t"
    "v_mfma_f32_16x16x32_bf16 v[112:115], v[76:79], v[108:111], v[112:115]\n\t" "s_nop 7\n\t" "v_perm_b32 v116, v112, v115, s54\n\t" "v_perm_b32 v117, v113, v115, s55\n\t"
    "v_perm_b32 v118, v114, v115, s56\n\t" "v_xad_u32 v119, v116, v117, s62\n\t" "s_mov_b64 exec, s[60:61]\n\t" "v_xad_u32 v119, v118, v117, v119\n\t"
    "global_store_dwordx4 v229, v[116:119], %[su] sc1\n\t" "s_mov_b64 exec, -1\n\t" ".Lns_%=:\n\t"
    : "+{v204}"(cv), "=&{v205}"(tv), "+{s[62:63]}"(tag)
    : "{v196}"(d0), "{v197}"(d1), "{v198}"(d2), "{v199}"(d3), [sl] "s"(sl), [xl] "s"(xl), [xh_] "s"(xh_), [k0] "s"(k0), "{v233}"(vcw), "{v228}"(vsr), "{v229}"(vsc), [t0] "v"(t0), [sq] "s"(sq), [su] "s"(su), [wid] "s"(wid)
    : "v48", "v49", "v50", "v51", "v52", "v53", "v54", "v55", "v56", "v57", "v58", "v59", "v60", "v61", "v62", "v63", "v64", "v65", "v66", "v67", "v68", "v69", "v70", "v71", "v72", "v73", "v74", "v75", "v76", "v77", "v78", "v79", "v80", "v81", "v82", "v83", "v84", "v85", "v86", "v87", "v88", "v89", "v90", "v91", "v92", "v93", "v94", "v95", "v96", "v97", "v98", "v99", "v100", "v101", "v102", "v103", "v104", "v105", "v106", "v107", "v108", "v109", "v110", "v111", "v112", "v113", "v114", "v115", "v116", "v117", "v118", "v119", "v206", "s45", "s54", "s55", "s56", "s60", "s61", "s64", "s65", "scc", "memory");
    }
    asm volatile("" : "+v"(tid)); lane = tid & 63;
    const int fr = lane & 15, g = lane >> 4, q = (lane >> 2) & 3, ls = ((tid >> 3) - 4 * rot) & 31;
    unsigned vb = (unsigned)((rot * 128 + wid * 8 + (lane >> 3)) * 6144 + (((lane & 7) ^ ((((lane >> 4) & 1) | ((wid & 1) << 1)) << 1)) << 4));
    unsigned dh0 = dx, dh1 = dh0 + 16384, xr0 = sl + RING + ((lane >> 3) & 1) * 12288 + (lane & 7) * 128 + ((g ^ (lane & 7)) << 4), xr1 = xr0 ^ 64;
    unsigned vh = ldsb + HALO + (g * 16 + (fr ^ g)) * 16, vdt = ldsb + (lane & 3) * 64 + g * 16, vhd = vdt + wid * 512, vhx = vh + wid * 2048;
    unsigned vg = tid * 16 + W_PART + 4096, vtw = ldsb + ls * 256 + (tid & 7) * 8, vhw = ldsb + HALO + ls * 1024 + ((tid >> 1) & 3) * 272 + (tid & 1) * 8, vs8 = ldsb + SCT + (tid >> 3) * 16;
    unsigned vpr = ldsb + PH + fr * 8, vph = vpr + wid * 128, vm = (tid >> 3) * 98304 + (tid & 7) * 16; vbo = f_vb(wid, lane);
    unsigned vr0 = ldsb + RING + (g & 1) * 1024 + (g >> 1) * 6144 + q * 128 + ((wid ^ ((q >> 1) | ((g & 1) << 1))) << 5) + (lane & 3) * 8, vr1 = vr0 ^ 32, vr2 = vr0 ^ 64, vr3 = vr0 ^ 96;
    unsigned vr4 = vr0 + 65536, vr5 = vr1 + 65536, vr6 = vr2 + 65536, vr7 = vr3 + 65536;
    const unsigned l0 = ldsb, lh = ldsb + HALO, lt = lh + 24576 + wid * 128, lr = lh + 22528 + wid * 256, w128 = wid * 128, w4k = wid * 4096, xw = w4k + 4096 + (wid == 1 ? 2048 : 0), xh = (wid >> 1) * 2048 + (wid & 1) * 8, ox = (wid >> 1) * 2048 + W_O + (rb * 4 + wid) * 65536 + cb * 4096, kh = k0 + w128;
    const int rw = rb * 4 + wid;
    const char* sb = (const char*)(w + 1024 + cb * 64); const u16* sm = wmod + (size_t)gk * 256 * 6144 + 2048 + gc * 64;
    const unsigned sf = W_TAGS + OF * 8 + FA(rb * 16 + cb);
    unsigned o0 = min(32768 + (ww * 8 + lane) * 128, len - 4), o1 = sf + (lane & 1) * YF * 8, o2 = W_TAGS + HF * 8 + FA(((xcd & 1) * 4 + (loc & 3)) * 32 + (xcd >> 1) * 8 + (loc >> 2));
    unsigned o3 = ((ww * 2 + (lane & 1)) * 4 + (xcd >> 1)) * 2048 + (xcd & 1) * 128 + (lane & 14) * 128;
    asm volatile("" : "+v"(o0), "+v"(o1), "+v"(o2), "+v"(o3));
    const char* t2 = (lane < 8 ? code : lane < 12 ? ws : (const char*)wo) + (lane < 8 ? o0 : lane < 10 ? o1 : lane < 12 ? o2 : o3);
    const int c3 = (3 - ((unsigned)(size_t)w >> 7)) & 7, w2 = ww - ((loc >> 2) > c3 ? 16 : 0), ix = (w2 >= 56 ? w2 - 56 : w2) * 16 * 1 + lane, r14 = (ix * 4682) >> 16;
    unsigned t3 = (xcd & 1) * 512 * 7168 + r14 * (7168 - 1792) + (xcd >> 1) * 1792 + ix * 128; const u16* pf = w2 >= 56 ? wu : wg;
    unsigned long long tm = (loc >> 2) == c3 ? 0ull : ~0ull >> (64 - 16 * 1);
    cd = code + min(ww * 1024, len - 1024);
    { const char* cd1 = cd; const char* cd = cd1 - wid * 1024;
    asm volatile(".p2align 3\n\t" "s_mov_b32 s43, 0xffa30000\n\t" "s_mov_b32 s46, 0x00100001\n\t" "s_mov_b32 s47, 0x10000100\n\t" "s_lshl_b64 s[48:49], s[46:47], 1\n\t" "s_lshl_b64 s[50:51], s[46:47], 2\n\t"
    "s_lshl_b64 s[52:53], s[46:47], 3\n\t" "s_mov_b32 s57, 0x04000c0c\n\t" "s_mov_b32 s58, 0x0706000c\n\t" "s_add_u32 s45, %[k0], 256\n\t" "s_and_b32 s45, s45, 0x7ff\n\t" "s_add_u32 s64, %[xl], s45\n\t"
    "s_addc_u32 s65, %[xh_], 0\n\t" "s_add_u32 m0, %[sl], 47104\n\t" "s_nop 0\n\t" "global_load_lds_dwordx4 v196, s[64:65] offset:0\n\t" "s_add_u32 m0, %[sl], 59392\n\t" "s_nop 0\n\t"
    "global_load_lds_dwordx4 v197, s[64:65] offset:0\n\t" "s_add_u32 m0, %[sl], 49152\n\t" "s_nop 0\n\t" "global_load_lds_dwordx4 v198, s[64:65] offset:0\n\t" "s_add_u32 m0, %[sl], 61440\n\t"
    "s_nop 0\n\t" "global_load_lds_dwordx4 v199, s[64:65] offset:0\n\t" "s_add_u32 m0, %[sl], 69504\n\t" "s_nop 0\n\t" "global_load_lds_dwordx4 v196, s[64:65] offset:128\n\t"
    "s_add_u32 m0, %[sl], 81792\n\t" "s_nop 0\n\t" "global_load_lds_dwordx4 v197, s[64:65] offset:128\n\t" "s_add_u32 m0, %[sl], 71552\n\t" "s_nop 0\n\t"
    "global_load_lds_dwordx4 v198, s[64:65] offset:128\n\t" "s_add_u32 m0, %[sl], 83840\n\t" "s_nop 0\n\t" "global_load_lds_dwordx4 v199, s[64:65] offset:128\n\t" "ds_read_b128 v[252:255], v234\n\t"
    "v_mov_b32 v112, 0x3f803f80\n\t" "v_mov_b32 v113, 0x3f803f80\n\t" "v_mov_b32 v114, 0x3f803f80\n\t" "v_mov_b32 v115, 0x3f803f80\n\t" "s_mov_b32 s60, 0xfffcfffc\n\t" "s_mov_b32 s61, s60\n\t"
    "s_mov_b32 s44, 0x100000\n\t" "s_barrier\n\t" "s_waitcnt vmcnt(13)\n\t" "ds_read_b128 a[0:3], v202 offset:0\n\t" "ds_read_b128 a[4:7], v202 offset:2048\n\t"
    "ds_read_b128 a[8:11], v203 offset:0\n\t" "ds_read_b128 a[12:15], v203 offset:2048\n\t" "s_waitcnt lgkmcnt(0)\n\t" "s_add_u32 s45, %[kh], 0\n\t" "s_and_b32 s45, s45, 0x7ff\n\t"
    "s_add_u32 s40, %[xl], s45\n\t" "s_addc_u32 s41, %[xh_], 0\n\t" "s_add_u32 m0, %[sl], 20480\n\t" "s_nop 0\n\t" "global_load_lds_dwordx4 v200, s[40:41]\n\t" "s_add_u32 m0, %[sl], 32768\n\t"
    "s_nop 0\n\t" "global_load_lds_dwordx4 v201, s[40:41]\n\t" "s_add_u32 s45, %[kh], 512\n\t" "s_and_b32 s45, s45, 0x7ff\n\t" "s_add_u32 s40, %[xl], s45\n\t" "s_addc_u32 s41, %[xh_], 0\n\t"
    "s_add_u32 m0, %[sl], 22528\n\t" "s_nop 0\n\t" "global_load_lds_dwordx4 v200, s[40:41]\n\t" "s_add_u32 m0, %[sl], 34816\n\t" "s_nop 0\n\t" "global_load_lds_dwordx4 v201, s[40:41]\n\t"
    "s_add_u32 m0, %[sl], 96256\n\t" "s_nop 0\n\t" "global_load_lds_dwordx4 v0, %[sb] offset:-2048\n\t" "global_load_lds_dwordx4 v0, %[sb] offset:0\n\t" "global_load_lds_dwordx4 v0, %[sb] offset:2048\n\t"
    "v_add_u32 v0, 0x30000, v0\n\t" "s_add_u32 m0, %[sl], 108544\n\t" "s_nop 0\n\t" "global_load_lds_dwordx4 v0, %[sb] offset:-2048\n\t" "global_load_lds_dwordx4 v0, %[sb] offset:0\n\t"
    "global_load_lds_dwordx4 v0, %[sb] offset:2048\n\t" "v_add_u32 v0, 0x30000, v0\n\t" "s_waitcnt vmcnt(19)\n\t" "v_mfma_f32_16x16x32_bf16 v[116:119], v[112:115], a[0:3], 0\n\t"
    "ds_read_b128 a[16:19], v202 offset:4096\n\t" "ds_read_b128 a[20:23], v202 offset:24576\n\t" "v_mfma_f32_16x16x32_bf16 v[128:131], a[0:3], a[0:3], 0\n\t" "ds_read_b128 a[24:27], v203 offset:4096\n\t"
    "ds_read_b128 a[28:31], v203 offset:24576\n\t" "v_mfma_f32_16x16x32_bf16 v[120:123], v[112:115], a[4:7], 0\n\t" "s_add_u32 s45, %[kh], 1024\n\t" "s_and_b32 s45, s45, 0x7ff\n\t"
    "s_add_u32 s40, %[xl], s45\n\t" "s_addc_u32 s41, %[xh_], 0\n\t" "v_mfma_f32_16x16x32_bf16 v[132:135], a[4:7], a[4:7], 0\n\t" "v_mfma_f32_16x16x32_bf16 v[116:119], v[112:115], a[8:11], v[116:119]\n\t"
    "v_mfma_f32_16x16x32_bf16 v[128:131], a[8:11], a[8:11], v[128:131]\n\t" "s_waitcnt lgkmcnt(0)\n\t" "s_add_u32 m0, %[sl], 24576\n\t"
    "v_mfma_f32_16x16x32_bf16 v[120:123], v[112:115], a[12:15], v[120:123]\n\t" "global_load_lds_dwordx4 v200, s[40:41]\n\t" "s_add_u32 m0, %[sl], 36864\n\t"
    "v_mfma_f32_16x16x32_bf16 v[132:135], a[12:15], a[12:15], v[132:135]\n\t" "global_load_lds_dwordx4 v201, s[40:41]\n\t" "s_add_u32 s45, %[kh], 1536\n\t" "s_and_b32 s45, s45, 0x7ff\n\t"
    "s_add_u32 s40, %[xl], s45\n\t" "s_addc_u32 s41, %[xh_], 0\n\t" "s_add_u32 m0, %[sl], 45056\n\t" "s_nop 0\n\t" "global_load_lds_dwordx4 v200, s[40:41]\n\t" "s_add_u32 m0, %[sl], 57344\n\t"
    "s_nop 0\n\t" "global_load_lds_dwordx4 v201, s[40:41]\n\t" "s_waitcnt vmcnt(18)\n\t" "v_mfma_f32_16x16x32_bf16 v[116:119], v[112:115], a[16:19], v[116:119]\n\t"
    "ds_read_b128 a[32:35], v202 offset:26624\n\t" "ds_read_b128 a[36:39], v202 offset:28672\n\t" "v_mfma_f32_16x16x32_bf16 v[128:131], a[16:19], a[16:19], v[128:131]\n\t"
    "ds_read_b128 a[40:43], v203 offset:26624\n\t" "ds_read_b128 a[44:47], v203 offset:28672\n\t" "v_mfma_f32_16x16x32_bf16 v[120:123], v[112:115], a[20:23], v[120:123]\n\t"
    "s_add_u32 s45, %[k0], 512\n\t" "s_and_b32 s45, s45, 0x7ff\n\t" "s_add_u32 s64, %[xl], s45\n\t" "s_addc_u32 s65, %[xh_], 0\n\t"
    "v_mfma_f32_16x16x32_bf16 v[132:135], a[20:23], a[20:23], v[132:135]\n\t" "v_mfma_f32_16x16x32_bf16 v[116:119], v[112:115], a[24:27], v[116:119]\n\t"
    "v_mfma_f32_16x16x32_bf16 v[128:131], a[24:27], a[24:27], v[128:131]\n\t" "s_waitcnt lgkmcnt(0)\n\t" "s_add_u32 m0, %[sl], 47104\n\t"
    "v_mfma_f32_16x16x32_bf16 v[120:123], v[112:115], a[28:31], v[120:123]\n\t" "global_load_lds_dwordx4 v196, s[64:65] offset:0\n\t" "s_add_u32 m0, %[sl], 59392\n\t"
    "v_mfma_f32_16x16x32_bf16 v[132:135], a[28:31], a[28:31], v[132:135]\n\t" "global_load_lds_dwordx4 v197, s[64:65] offset:0\n\t" "s_add_u32 m0, %[sl], 49152\n\t" "s_nop 0\n\t"
    "global_load_lds_dwordx4 v198, s[64:65] offset:0\n\t" "s_add_u32 m0, %[sl], 61440\n\t" "s_nop 0\n\t" "global_load_lds_dwordx4 v199, s[64:65] offset:0\n\t" "s_waitcnt vmcnt(18)\n\t"
    "v_mfma_f32_16x16x32_bf16 v[116:119], v[112:115], a[32:35], v[116:119]\n\t" "ds_read_b128 a[48:51], v202 offset:49152\n\t" "ds_read_b128 a[52:55], v202 offset:51200\n\t"
    "v_mfma_f32_16x16x32_bf16 v[128:131], a[32:35], a[32:35], v[128:131]\n\t" "ds_read_b128 a[56:59], v203 offset:49152\n\t" "ds_read_b128 a[60:63], v203 offset:51200\n\t"
    "v_mfma_f32_16x16x32_bf16 v[120:123], v[112:115], a[36:39], v[120:123]\n\t" "v_mfma_f32_16x16x32_bf16 v[132:135], a[36:39], a[36:39], v[132:135]\n\t"
    "v_mfma_f32_16x16x32_bf16 v[116:119], v[112:115], a[40:43], v[116:119]\n\t" "v_mfma_f32_16x16x32_bf16 v[128:131], a[40:43], a[40:43], v[128:131]\n\t" "s_waitcnt lgkmcnt(0)\n\t"
    "s_add_u32 m0, %[sl], 69504\n\t" "v_mfma_f32_16x16x32_bf16 v[120:123], v[112:115], a[44:47], v[120:123]\n\t" "global_load_lds_dwordx4 v196, s[64:65] offset:128\n\t" "s_add_u32 m0, %[sl], 81792\n\t"
    "v_mfma_f32_16x16x32_bf16 v[132:135], a[44:47], a[44:47], v[132:135]\n\t" "global_load_lds_dwordx4 v197, s[64:65] offset:128\n\t" "s_add_u32 m0, %[sl], 71552\n\t" "s_nop 0\n\t"
    "global_load_lds_dwordx4 v198, s[64:65] offset:128\n\t" "s_add_u32 m0, %[sl], 83840\n\t" "s_nop 0\n\t" "global_load_lds_dwordx4 v199, s[64:65] offset:128\n\t" "s_waitcnt vmcnt(18)\n\t"
    "v_mfma_f32_16x16x32_bf16 v[116:119], v[112:115], a[48:51], v[116:119]\n\t" "ds_read_b128 v[48:51], v202 offset:0\n\t" "ds_read_b128 v[20:23], v202 offset:2048\n\t"
    "v_mfma_f32_16x16x32_bf16 v[128:131], a[48:51], a[48:51], v[128:131]\n\t" "ds_read_b128 v[52:55], v203 offset:0\n\t" "ds_read_b128 v[24:27], v203 offset:2048\n\t"
    "v_mfma_f32_16x16x32_bf16 v[120:123], v[112:115], a[52:55], v[120:123]\n\t" "s_add_u32 s45, %[k0], 768\n\t" "s_and_b32 s45, s45, 0x7ff\n\t" "s_add_u32 s64, %[xl], s45\n\t"
    "s_addc_u32 s65, %[xh_], 0\n\t" "v_mfma_f32_16x16x32_bf16 v[132:135], a[52:55], a[52:55], v[132:135]\n\t" "v_mfma_f32_16x16x32_bf16 v[116:119], v[112:115], a[56:59], v[116:119]\n\t"
    "v_mfma_f32_16x16x32_bf16 v[128:131], a[56:59], a[56:59], v[128:131]\n\t" "s_waitcnt lgkmcnt(0)\n\t" "s_add_u32 m0, %[sl], 20480\n\t"
    "v_mfma_f32_16x16x32_bf16 v[120:123], v[112:115], a[60:63], v[120:123]\n\t" "global_load_lds_dwordx4 v196, s[64:65] offset:0\n\t" "s_add_u32 m0, %[sl], 32768\n\t"
    "v_mfma_f32_16x16x32_bf16 v[132:135], a[60:63], a[60:63], v[132:135]\n\t" "global_load_lds_dwordx4 v197, s[64:65] offset:0\n\t" "s_add_u32 m0, %[sl], 22528\n\t" "s_nop 0\n\t"
    "global_load_lds_dwordx4 v198, s[64:65] offset:0\n\t" "s_add_u32 m0, %[sl], 34816\n\t" "s_nop 0\n\t" "global_load_lds_dwordx4 v199, s[64:65] offset:0\n\t" "s_waitcnt vmcnt(12)\n\t"
    "v_mfma_f32_16x16x32_bf16 v[124:127], v[112:115], v[48:51], 0\n\t" "ds_read_b128 v[28:31], v202 offset:4096\n\t" "ds_read_b128 v[36:39], v202 offset:24576\n\t"
    "v_mfma_f32_16x16x32_bf16 v[136:139], v[48:51], v[48:51], 0\n\t" "ds_read_b128 v[32:35], v203 offset:4096\n\t" "ds_read_b128 v[40:43], v203 offset:24576\n\t"
    "v_mfma_f32_16x16x32_bf16 v[124:127], v[112:115], v[20:23], v[124:127]\n\t" "v_mfma_f32_16x16x32_bf16 v[136:139], v[20:23], v[20:23], v[136:139]\n\t"
    "v_mfma_f32_16x16x32_bf16 v[124:127], v[112:115], v[52:55], v[124:127]\n\t" "v_mfma_f32_16x16x32_bf16 v[136:139], v[52:55], v[52:55], v[136:139]\n\t" "s_waitcnt lgkmcnt(0)\n\t"
    "s_add_u32 m0, %[sl], 24448\n\t" "v_mfma_f32_16x16x32_bf16 v[124:127], v[112:115], v[24:27], v[124:127]\n\t" "global_load_lds_dwordx4 v196, s[64:65] offset:128\n\t" "s_add_u32 m0, %[sl], 36736\n\t"
    "v_mfma_f32_16x16x32_bf16 v[136:139], v[24:27], v[24:27], v[136:139]\n\t" "global_load_lds_dwordx4 v197, s[64:65] offset:128\n\t" "s_add_u32 m0, %[sl], 44928\n\t" "s_nop 0\n\t"
    "global_load_lds_dwordx4 v198, s[64:65] offset:128\n\t" "s_add_u32 m0, %[sl], 57216\n\t" "s_nop 0\n\t" "global_load_lds_dwordx4 v199, s[64:65] offset:128\n\t" "s_mov_b64 s[40:41], %[ws]\n\t"
    "global_load_dwordx4 v[80:83], v230, s[40:41] offset:-4096 sc1\n\t" "global_load_dwordx4 v[96:99], v230, s[40:41] offset:0 sc1\n\t" "s_add_u32 s40, s40, 8192\n\t" "s_addc_u32 s41, s41, 0\n\t"
    "global_load_dwordx4 v[84:87], v230, s[40:41] offset:-4096 sc1\n\t" "global_load_dwordx4 v[100:103], v230, s[40:41] offset:0 sc1\n\t" "s_add_u32 s40, s40, 8192\n\t" "s_addc_u32 s41, s41, 0\n\t"
    "global_load_dwordx4 v[88:91], v230, s[40:41] offset:-4096 sc1\n\t" "global_load_dwordx4 v[104:107], v230, s[40:41] offset:0 sc1\n\t" "s_add_u32 s40, s40, 8192\n\t" "s_addc_u32 s41, s41, 0\n\t"
    "global_load_dwordx4 v[92:95], v230, s[40:41] offset:-4096 sc1\n\t" "global_load_dwordx4 v[108:111], v230, s[40:41] offset:0 sc1\n\t" "s_waitcnt vmcnt(20)\n\t"
    "v_mfma_f32_16x16x32_bf16 v[124:127], v[112:115], v[28:31], v[124:127]\n\t" "ds_read_b128 a[64:67], v202 offset:26624\n\t" "ds_read_b128 a[68:71], v202 offset:28672\n\t"
    "v_mfma_f32_16x16x32_bf16 v[136:139], v[28:31], v[28:31], v[136:139]\n\t" "ds_read_b128 a[72:75], v203 offset:26624\n\t" "ds_read_b128 a[76:79], v203 offset:28672\n\t"
    "v_mfma_f32_16x16x32_bf16 v[124:127], v[112:115], v[36:39], v[124:127]\n\t" "s_add_u32 s45, %[k0], 1024\n\t" "s_and_b32 s45, s45, 0x7ff\n\t" "s_add_u32 s64, %[xl], s45\n\t"
    "s_addc_u32 s65, %[xh_], 0\n\t" "v_mfma_f32_16x16x32_bf16 v[136:139], v[36:39], v[36:39], v[136:139]\n\t" "v_mfma_f32_16x16x32_bf16 v[124:127], v[112:115], v[32:35], v[124:127]\n\t"
    "v_mfma_f32_16x16x32_bf16 v[136:139], v[32:35], v[32:35], v[136:139]\n\t" "s_waitcnt lgkmcnt(0)\n\t" "s_add_u32 m0, %[sl], 47104\n\t"
    "v_mfma_f32_16x16x32_bf16 v[124:127], v[112:115], v[40:43], v[124:127]\n\t" "global_load_lds_dwordx4 v196, s[64:65] offset:0\n\t" "s_add_u32 m0, %[sl], 59392\n\t"
    "v_mfma_f32_16x16x32_bf16 v[136:139], v[40:43], v[40:43], v[136:139]\n\t" "global_load_lds_dwordx4 v197, s[64:65] offset:0\n\t" "s_add_u32 m0, %[sl], 49152\n\t" "s_nop 0\n\t"
    "global_load_lds_dwordx4 v198, s[64:65] offset:0\n\t" "s_add_u32 m0, %[sl], 61440\n\t" "s_nop 0\n\t" "global_load_lds_dwordx4 v199, s[64:65] offset:0\n\t" "s_waitcnt vmcnt(20)\n\t"
    "v_mfma_f32_16x16x32_bf16 v[116:119], v[112:115], a[64:67], v[116:119]\n\t" "ds_read_b128 a[80:83], v202 offset:49152\n\t" "ds_read_b128 a[84:87], v202 offset:51200\n\t"
    "v_mfma_f32_16x16x32_bf16 v[128:131], a[64:67], a[64:67], v[128:131]\n\t" "ds_read_b128 a[88:91], v203 offset:49152\n\t" "ds_read_b128 a[92:95], v203 offset:51200\n\t"
    "v_mfma_f32_16x16x32_bf16 v[120:123], v[112:115], a[68:71], v[120:123]\n\t" "v_cndmask_b32 v142, 0, v136, s[46:47]\n\t" "v_cndmask_b32 v142, v142, v137, s[48:49]\n\t"
    "v_cndmask_b32 v142, v142, v138, s[50:51]\n\t" "v_cndmask_b32 v142, v142, v139, s[52:53]\n\t" "v_mfma_f32_16x16x32_bf16 v[132:135], a[68:71], a[68:71], v[132:135]\n\t" "v_mov_b32 v143, v142\n\t"
    "s_nop 1\n\t" "v_permlane16_swap_b32 v142, v143\n\t" "v_add_f32_e64 v142, v142, v143\n\t" "v_mfma_f32_16x16x32_bf16 v[116:119], v[112:115], a[72:75], v[116:119]\n\t" "v_mov_b32 v143, v142\n\t"
    "s_nop 1\n\t" "v_permlane32_swap_b32 v142, v143\n\t" "v_add_f32 v142, v142, v143\n\t" "v_mfma_f32_16x16x32_bf16 v[128:131], a[72:75], a[72:75], v[128:131]\n\t" "s_waitcnt lgkmcnt(0)\n\t"
    "s_add_u32 m0, %[sl], 69504\n\t" "v_mfma_f32_16x16x32_bf16 v[120:123], v[112:115], a[76:79], v[120:123]\n\t" "global_load_lds_dwordx4 v196, s[64:65] offset:128\n\t" "s_add_u32 m0, %[sl], 81792\n\t"
    "v_mfma_f32_16x16x32_bf16 v[132:135], a[76:79], a[76:79], v[132:135]\n\t" "global_load_lds_dwordx4 v197, s[64:65] offset:128\n\t" "s_add_u32 m0, %[sl], 71552\n\t" "s_nop 0\n\t"
    "global_load_lds_dwordx4 v198, s[64:65] offset:128\n\t" "s_add_u32 m0, %[sl], 83840\n\t" "s_nop 0\n\t" "global_load_lds_dwordx4 v199, s[64:65] offset:128\n\t" "s_waitcnt vmcnt(20)\n\t"
    "v_mfma_f32_16x16x32_bf16 v[116:119], v[112:115], a[80:83], v[116:119]\n\t" "ds_read_b128 a[96:99], v202 offset:0\n\t" "ds_read_b128 a[100:103], v202 offset:2048\n\t"
    "v_mfma_f32_16x16x32_bf16 v[128:131], a[80:83], a[80:83], v[128:131]\n\t" "ds_read_b128 a[104:107], v203 offset:0\n\t" "ds_read_b128 a[108:111], v203 offset:2048\n\t"
    "v_mfma_f32_16x16x32_bf16 v[120:123], v[112:115], a[84:87], v[120:123]\n\t" "s_add_u32 s45, %[k0], 1280\n\t" "s_and_b32 s45, s45, 0x7ff\n\t" "s_add_u32 s64, %[xl], s45\n\t"
    "s_addc_u32 s65, %[xh_], 0\n\t" "v_mfma_f32_16x16x32_bf16 v[132:135], a[84:87], a[84:87], v[132:135]\n\t" "v_mfma_f32_16x16x32_bf16 v[116:119], v[112:115], a[88:91], v[116:119]\n\t"
    "v_mfma_f32_16x16x32_bf16 v[128:131], a[88:91], a[88:91], v[128:131]\n\t" "s_waitcnt lgkmcnt(0)\n\t" "s_add_u32 m0, %[sl], 20480\n\t"
    "v_mfma_f32_16x16x32_bf16 v[120:123], v[112:115], a[92:95], v[120:123]\n\t" "global_load_lds_dwordx4 v196, s[64:65] offset:0\n\t" "s_add_u32 m0, %[sl], 32768\n\t"
    "v_mfma_f32_16x16x32_bf16 v[132:135], a[92:95], a[92:95], v[132:135]\n\t" "global_load_lds_dwordx4 v197, s[64:65] offset:0\n\t" "s_add_u32 m0, %[sl], 22528\n\t" "s_nop 0\n\t"
    "global_load_lds_dwordx4 v198, s[64:65] offset:0\n\t" "s_add_u32 m0, %[sl], 34816\n\t" "s_nop 0\n\t" "global_load_lds_dwordx4 v199, s[64:65] offset:0\n\t" "s_waitcnt vmcnt(20)\n\t"
    "v_mfma_f32_16x16x32_bf16 v[116:119], v[112:115], a[96:99], v[116:119]\n\t" "ds_read_b128 a[112:115], v202 offset:4096\n\t" "ds_read_b128 a[116:119], v202 offset:24576\n\t"
    "v_mfma_f32_16x16x32_bf16 v[128:131], a[96:99], a[96:99], v[128:131]\n\t" "ds_read_b128 a[120:123], v203 offset:4096\n\t" "ds_read_b128 a[124:127], v203 offset:24576\n\t"
    "v_mfma_f32_16x16x32_bf16 v[120:123], v[112:115], a[100:103], v[120:123]\n\t" "v_mfma_f32_16x16x32_bf16 v[132:135], a[100:103], a[100:103], v[132:135]\n\t"
    "v_mfma_f32_16x16x32_bf16 v[116:119], v[112:115], a[104:107], v[116:119]\n\t" "v_mfma_f32_16x16x32_bf16 v[128:131], a[104:107], a[104:107], v[128:131]\n\t" "s_waitcnt lgkmcnt(0)\n\t"
    "s_add_u32 m0, %[sl], 24448\n\t" "v_mfma_f32_16x16x32_bf16 v[120:123], v[112:115], a[108:111], v[120:123]\n\t" "global_load_lds_dwordx4 v196, s[64:65] offset:128\n\t" "s_add_u32 m0, %[sl], 36736\n\t"
    "v_mfma_f32_16x16x32_bf16 v[132:135], a[108:111], a[108:111], v[132:135]\n\t" "global_load_lds_dwordx4 v197, s[64:65] offset:128\n\t" "s_add_u32 m0, %[sl], 44928\n\t" "s_nop 0\n\t"
    "global_load_lds_dwordx4 v198, s[64:65] offset:128\n\t" "s_add_u32 m0, %[sl], 57216\n\t" "s_nop 0\n\t" "global_load_lds_dwordx4 v199, s[64:65] offset:128\n\t" "s_waitcnt vmcnt(12)\n\t"
    "v_mfma_f32_16x16x32_bf16 v[116:119], v[112:115], a[112:115], v[116:119]\n\t" "ds_read_b128 a[128:131], v202 offset:26624\n\t" "ds_read_b128 a[132:135], v202 offset:28672\n\t"
    "v_mfma_f32_16x16x32_bf16 v[128:131], a[112:115], a[112:115], v[128:131]\n\t" "ds_read_b128 a[136:139], v203 offset:26624\n\t" "ds_read_b128 a[140:143], v203 offset:28672\n\t"
    "v_mfma_f32_16x16x32_bf16 v[120:123], v[112:115], a[116:119], v[120:123]\n\t" "s_add_u32 s45, %[k0], 1536\n\t" "s_and_b32 s45, s45, 0x7ff\n\t" "s_add_u32 s64, %[xl], s45\n\t"
    "s_addc_u32 s65, %[xh_], 0\n\t" "v_xad_u32 v144, v80, v81, s62\n\t" "v_xad_u32 v145, v84, v85, s62\n\t" "v_xad_u32 v146, v88, v89, s62\n\t"
    "v_mfma_f32_16x16x32_bf16 v[132:135], a[116:119], a[116:119], v[132:135]\n\t" "v_xad_u32 v147, v92, v93, s62\n\t" "v_xad_u32 v148, v96, v97, s62\n\t" "v_xad_u32 v149, v100, v101, s62\n\t"
    "v_xad_u32 v150, v104, v105, s62\n\t" "v_mfma_f32_16x16x32_bf16 v[116:119], v[112:115], a[120:123], v[116:119]\n\t" "v_xad_u32 v151, v108, v109, s62\n\t" "v_xad_u32 v144, v82, v81, v144\n\t"
    "v_xad_u32 v145, v86, v85, v145\n\t" "v_xad_u32 v146, v90, v89, v146\n\t" "v_mfma_f32_16x16x32_bf16 v[128:131], a[120:123], a[120:123], v[128:131]\n\t" "v_xad_u32 v147, v94, v93, v147\n\t"
    "v_xad_u32 v148, v98, v97, v148\n\t" "v_xad_u32 v149, v102, v101, v149\n\t" "v_xad_u32 v150, v106, v105, v150\n\t" "s_waitcnt lgkmcnt(0)\n\t" "s_add_u32 m0, %[sl], 47104\n\t"
    "v_mfma_f32_16x16x32_bf16 v[120:123], v[112:115], a[124:127], v[120:123]\n\t" "global_load_lds_dwordx4 v196, s[64:65] offset:0\n\t" "s_add_u32 m0, %[sl], 59392\n\t"
    "v_mfma_f32_16x16x32_bf16 v[132:135], a[124:127], a[124:127], v[132:135]\n\t" "global_load_lds_dwordx4 v197, s[64:65] offset:0\n\t" "s_add_u32 m0, %[sl], 49152\n\t" "s_nop 0\n\t"
    "global_load_lds_dwordx4 v198, s[64:65] offset:0\n\t" "s_add_u32 m0, %[sl], 61440\n\t" "s_nop 0\n\t" "global_load_lds_dwordx4 v199, s[64:65] offset:0\n\t" "s_waitcnt vmcnt(12)\n\t"
    "v_mfma_f32_16x16x32_bf16 v[116:119], v[112:115], a[128:131], v[116:119]\n\t" "ds_read_b128 a[144:147], v202 offset:49152\n\t" "ds_read_b128 a[148:151], v202 offset:51200\n\t"
    "v_mfma_f32_16x16x32_bf16 v[128:131], a[128:131], a[128:131], v[128:131]\n\t" "ds_read_b128 a[152:155], v203 offset:49152\n\t" "ds_read_b128 a[156:159], v203 offset:51200\n\t"
    "v_mfma_f32_16x16x32_bf16 v[120:123], v[112:115], a[132:135], v[120:123]\n\t" "v_xad_u32 v151, v110, v109, v151\n\t" "v_xor_b32_e64 v140, v144, v83\n\t"
    "v_bitop3_b32 v140, v140, v145, v87 bitop3:0xf6\n\t" "v_bitop3_b32 v140, v140, v146, v91 bitop3:0xf6\n\t" "v_mfma_f32_16x16x32_bf16 v[132:135], a[132:135], a[132:135], v[132:135]\n\t"
    "v_bitop3_b32 v140, v140, v147, v95 bitop3:0xf6\n\t" "v_bitop3_b32 v140, v140, v148, v99 bitop3:0xf6\n\t" "v_bitop3_b32 v140, v140, v149, v103 bitop3:0xf6\n\t"
    "v_bitop3_b32 v140, v140, v150, v107 bitop3:0xf6\n\t" "v_mfma_f32_16x16x32_bf16 v[116:119], v[112:115], a[136:139], v[116:119]\n\t" "v_bitop3_b32 v140, v140, v151, v111 bitop3:0xf6\n\t"
    "v_cmp_ne_u32_e64 vcc, 0, v140\n\t" "v_perm_b32 v83, v80, v81, s57\n\t" "v_perm_b32 v87, v84, v85, s57\n\t" "v_mfma_f32_16x16x32_bf16 v[128:131], a[136:139], a[136:139], v[128:131]\n\t"
    "v_perm_b32 v91, v88, v89, s57\n\t" "v_perm_b32 v95, v92, v93, s57\n\t" "v_perm_b32 v99, v96, v97, s57\n\t" "v_perm_b32 v103, v100, v101, s57\n\t" "s_waitcnt lgkmcnt(0)\n\t"
    "s_add_u32 m0, %[sl], 69504\n\t" "v_mfma_f32_16x16x32_bf16 v[120:123], v[112:115], a[140:143], v[120:123]\n\t" "global_load_lds_dwordx4 v196, s[64:65] offset:128\n\t" "s_add_u32 m0, %[sl], 81792\n\t"
    "v_mfma_f32_16x16x32_bf16 v[132:135], a[140:143], a[140:143], v[132:135]\n\t" "global_load_lds_dwordx4 v197, s[64:65] offset:128\n\t" "s_add_u32 m0, %[sl], 71552\n\t" "s_nop 0\n\t"
    "global_load_lds_dwordx4 v198, s[64:65] offset:128\n\t" "s_add_u32 m0, %[sl], 83840\n\t" "s_nop 0\n\t" "global_load_lds_dwordx4 v199, s[64:65] offset:128\n\t" "s_waitcnt vmcnt(12)\n\t"
    "v_mfma_f32_16x16x32_bf16 v[116:119], v[112:115], a[144:147], v[116:119]\n\t" "ds_read_b128 a[160:163], v202 offset:0\n\t" "ds_read_b128 a[164:167], v202 offset:2048\n\t"
    "v_mfma_f32_16x16x32_bf16 v[128:131], a[144:147], a[144:147], v[128:131]\n\t" "ds_read_b128 a[168:171], v203 offset:0\n\t" "ds_read_b128 a[172:175], v203 offset:2048\n\t"
    "v_mfma_f32_16x16x32_bf16 v[120:123], v[112:115], a[148:151], v[120:123]\n\t" "s_add_u32 s45, %[k0], 1792\n\t" "s_and_b32 s45, s45, 0x7ff\n\t" "s_add_u32 s64, %[xl], s45\n\t"
    "s_addc_u32 s65, %[xh_], 0\n\t" "v_perm_b32 v107, v104, v105, s57\n\t" "v_perm_b32 v111, v108, v109, s57\n\t" "s_cbranch_vccnz .Lslow_%=\n\t" ".Lgo_%=:\n\t" "v_perm_b32 v83, v83, v82, s58\n\t"
    "v_mfma_f32_16x16x32_bf16 v[132:135], a[148:151], a[148:151], v[132:135]\n\t" "v_perm_b32 v87, v87, v86, s58\n\t" "v_perm_b32 v91, v91, v90, s58\n\t" "v_perm_b32 v95, v95, v94, s58\n\t"
    "v_perm_b32 v99, v99, v98, s58\n\t" "v_mfma_f32_16x16x32_bf16 v[116:119], v[112:115], a[152:155], v[116:119]\n\t" "v_perm_b32 v103, v103, v102, s58\n\t" "v_perm_b32 v107, v107, v106, s58\n\t"
    "v_perm_b32 v111, v111, v110, s58\n\t" "v_add_f32 v96, v96, v100\n\t" "v_mfma_f32_16x16x32_bf16 v[128:131], a[152:155], a[152:155], v[128:131]\n\t" "v_add_f32 v97, v97, v101\n\t"
    "v_add_f32 v98, v98, v102\n\t" "v_add_f32 v99, v99, v103\n\t" "v_add_f32_e64 v80, v80, v84\n\t" "s_waitcnt lgkmcnt(0)\n\t" "s_add_u32 m0, %[sl], 20480\n\t"
    "v_mfma_f32_16x16x32_bf16 v[120:123], v[112:115], a[156:159], v[120:123]\n\t" "global_load_lds_dwordx4 v196, s[64:65] offset:0\n\t" "s_add_u32 m0, %[sl], 32768\n\t"
    "v_mfma_f32_16x16x32_bf16 v[132:135], a[156:159], a[156:159], v[132:135]\n\t" "global_load_lds_dwordx4 v197, s[64:65] offset:0\n\t" "s_add_u32 m0, %[sl], 22528\n\t" "s_nop 0\n\t"
    "global_load_lds_dwordx4 v198, s[64:65] offset:0\n\t" "s_add_u32 m0, %[sl], 34816\n\t" "s_nop 0\n\t" "global_load_lds_dwordx4 v199, s[64:65] offset:0\n\t" "s_waitcnt vmcnt(12)\n\t"
    "v_mfma_f32_16x16x32_bf16 v[116:119], v[112:115], a[160:163], v[116:119]\n\t" "ds_read_b128 a[176:179], v202 offset:4096\n\t" "ds_read_b128 a[180:183], v202 offset:24576\n\t"
    "v_mfma_f32_16x16x32_bf16 v[128:131], a[160:163], a[160:163], v[128:131]\n\t" "ds_read_b128 a[184:187], v203 offset:4096\n\t" "ds_read_b128 a[188:191], v203 offset:24576\n\t"
    "v_mfma_f32_16x16x32_bf16 v[120:123], v[112:115], a[164:167], v[120:123]\n\t" "v_add_f32 v81, v81, v85\n\t" "v_add_f32 v82, v82, v86\n\t" "v_add_f32 v83, v83, v87\n\t"
    "v_add_f32_e64 v104, v104, v108\n\t" "v_mfma_f32_16x16x32_bf16 v[132:135], a[164:167], a[164:167], v[132:135]\n\t" "v_add_f32 v105, v105, v109\n\t" "v_add_f32 v106, v106, v110\n\t"
    "v_add_f32 v107, v107, v111\n\t" "v_add_f32 v88, v88, v92\n\t" "v_mfma_f32_16x16x32_bf16 v[116:119], v[112:115], a[168:171], v[116:119]\n\t" "v_add_f32 v89, v89, v93\n\t" "v_add_f32 v90, v90, v94\n\t"
    "v_add_f32 v91, v91, v95\n\t" "v_add_f32 v96, v96, v104\n\t" "v_mfma_f32_16x16x32_bf16 v[128:131], a[168:171], a[168:171], v[128:131]\n\t" "v_add_f32 v97, v97, v105\n\t" "v_add_f32 v98, v98, v106\n\t"
    "v_add_f32 v99, v99, v107\n\t" "v_add_f32_e64 v80, v80, v88\n\t" "s_waitcnt lgkmcnt(0)\n\t" "s_add_u32 m0, %[sl], 24448\n\t"
    "v_mfma_f32_16x16x32_bf16 v[120:123], v[112:115], a[172:175], v[120:123]\n\t" "global_load_lds_dwordx4 v196, s[64:65] offset:128\n\t" "s_add_u32 m0, %[sl], 36736\n\t"
    "v_mfma_f32_16x16x32_bf16 v[132:135], a[172:175], a[172:175], v[132:135]\n\t" "global_load_lds_dwordx4 v197, s[64:65] offset:128\n\t" "s_add_u32 m0, %[sl], 44928\n\t" "s_nop 0\n\t"
    "global_load_lds_dwordx4 v198, s[64:65] offset:128\n\t" "s_add_u32 m0, %[sl], 57216\n\t" "s_nop 0\n\t" "global_load_lds_dwordx4 v199, s[64:65] offset:128\n\t" "s_waitcnt vmcnt(12)\n\t"
    "v_mfma_f32_16x16x32_bf16 v[116:119], v[112:115], a[176:179], v[116:119]\n\t" "ds_read_b128 a[192:195], v202 offset:26624\n\t" "ds_read_b128 a[196:199], v202 offset:28672\n\t"
    "v_add_f32 v81, v81, v89\n\t" "v_add_f32 v82, v82, v90\n\t" "v_add_f32 v83, v83, v91\n\t" "v_cvt_pk_bf16_f32 v96, v96, v97\n\t"
    "v_mfma_f32_16x16x32_bf16 v[128:131], a[176:179], a[176:179], v[128:131]\n\t" "ds_read_b128 a[200:203], v203 offset:26624\n\t" "ds_read_b128 a[204:207], v203 offset:28672\n\t"
    "v_cvt_pk_bf16_f32 v98, v98, v99\n\t" "v_cvt_pk_bf16_f32 v80, v80, v81\n\t" "v_cvt_pk_bf16_f32 v81, v82, v83\n\t" "v_xor_b32_e64 v82, 16, v232\n\t"
    "v_mfma_f32_16x16x32_bf16 v[120:123], v[112:115], a[180:183], v[120:123]\n\t" "v_and_b32 v97, 0xffff0000, v96\n\t" "v_lshlrev_b32_e64 v96, 16, v96\n\t" "v_and_b32 v99, 0xffff0000, v98\n\t"
    "v_lshlrev_b32_e64 v98, 16, v98\n\t" "v_mfma_f32_16x16x32_bf16 v[132:135], a[180:183], a[180:183], v[132:135]\n\t" "v_add_f32 v96, 1.0, v96\n\t" "v_add_f32 v97, 1.0, v97\n\t"
    "v_add_f32 v98, 1.0, v98\n\t" "v_add_f32 v99, 1.0, v99\n\t" "v_mfma_f32_16x16x32_bf16 v[116:119], v[112:115], a[184:187], v[116:119]\n\t" "v_cvt_pk_bf16_f32 v96, v96, v97\n\t"
    "v_cvt_pk_bf16_f32 v97, v98, v99\n\t" "v_mov_b32 v101, 0\n\t" "v_mov_b32 v103, 0\n\t" "v_mfma_f32_16x16x32_bf16 v[128:131], a[184:187], a[184:187], v[128:131]\n\t" "v_mov_b32 v104, 0\n\t"
    "v_mov_b32 v106, 0\n\t" "v_and_b32 v100, 0xffff, v96\n\t" "v_and_b32 v102, 0xffff0000, v96\n\t" "s_waitcnt lgkmcnt(0)\n\t" "v_mfma_f32_16x16x32_bf16 v[120:123], v[112:115], a[188:191], v[120:123]\n\t"
    "v_and_b32 v105, 0xffff, v97\n\t" "v_and_b32 v107, 0xffff0000, v97\n\t" "v_mfma_f32_16x16x32_bf16 v[132:135], a[188:191], a[188:191], v[132:135]\n\t" "s_waitcnt vmcnt(8)\n\t"
    "v_mfma_f32_16x16x32_bf16 v[116:119], v[112:115], a[192:195], v[116:119]\n\t" "ds_read_b128 a[208:211], v202 offset:49152\n\t" "ds_read_b128 a[212:215], v202 offset:51200\n\t"
    "v_mfma_f32_16x16x32_bf16 v[128:131], a[192:195], a[192:195], v[128:131]\n\t" "ds_read_b128 a[216:219], v203 offset:49152\n\t" "ds_read_b128 a[220:223], v203 offset:51200\n\t"
    "v_mfma_f32_16x16x32_bf16 v[120:123], v[112:115], a[196:199], v[120:123]\n\t" "v_mfma_f32_16x16x32_bf16 v[132:135], a[196:199], a[196:199], v[132:135]\n\t"
    "v_mfma_f32_16x16x32_bf16 v[116:119], v[112:115], a[200:203], v[116:119]\n\t" "v_mfma_f32_16x16x32_bf16 v[128:131], a[200:203], a[200:203], v[128:131]\n\t" "s_waitcnt lgkmcnt(0)\n\t"
    "v_mfma_f32_16x16x32_bf16 v[120:123], v[112:115], a[204:207], v[120:123]\n\t" "v_mfma_f32_16x16x32_bf16 v[132:135], a[204:207], a[204:207], v[132:135]\n\t" "s_waitcnt vmcnt(4)\n\t"
    "v_mfma_f32_16x16x32_bf16 v[116:119], v[112:115], a[208:211], v[116:119]\n\t" "ds_read_b128 a[224:227], v202 offset:0\n\t" "ds_read_b128 a[228:231], v202 offset:2048\n\t"
    "v_mfma_f32_16x16x32_bf16 v[128:131], a[208:211], a[208:211], v[128:131]\n\t" "ds_read_b128 a[232:235], v203 offset:0\n\t" "ds_read_b128 a[236:239], v203 offset:2048\n\t"
    "v_mfma_f32_16x16x32_bf16 v[120:123], v[112:115], a[212:215], v[120:123]\n\t" "v_mfma_f32_16x16x32_bf16 v[132:135], a[212:215], a[212:215], v[132:135]\n\t"
    "v_mfma_f32_16x16x32_bf16 v[116:119], v[112:115], a[216:219], v[116:119]\n\t" "v_mfma_f32_16x16x32_bf16 v[128:131], a[216:219], a[216:219], v[128:131]\n\t" "s_waitcnt lgkmcnt(0)\n\t"
    "v_mfma_f32_16x16x32_bf16 v[120:123], v[112:115], a[220:223], v[120:123]\n\t" "v_mfma_f32_16x16x32_bf16 v[132:135], a[220:223], a[220:223], v[132:135]\n\t" "s_waitcnt vmcnt(0)\n\t"
    "v_mfma_f32_16x16x32_bf16 v[116:119], v[112:115], a[224:227], v[116:119]\n\t" "ds_read_b128 a[240:243], v202 offset:4096\n\t" "ds_read_b128 a[244:247], v202 offset:24576\n\t"
    "v_mfma_f32_16x16x32_bf16 v[128:131], a[224:227], a[224:227], v[128:131]\n\t" "ds_read_b128 a[248:251], v203 offset:4096\n\t" "ds_read_b128 a[252:255], v203 offset:24576\n\t"
    "v_mfma_f32_16x16x32_bf16 v[120:123], v[112:115], a[228:231], v[120:123]\n\t" "ds_write2_b32 v235, v142, v124 offset1:1\n\t"
    "v_mfma_f32_16x16x32_bf16 v[132:135], a[228:231], a[228:231], v[132:135]\n\t" "ds_write2_b64 v231, v[100:101], v[102:103] offset0:0 offset1:8\n\t"
    "v_mfma_f32_16x16x32_bf16 v[116:119], v[112:115], a[232:235], v[116:119]\n\t" "ds_write2_b64 v231, v[104:105], v[106:107] offset0:16 offset1:24\n\t"
    "v_mfma_f32_16x16x32_bf16 v[128:131], a[232:235], a[232:235], v[128:131]\n\t" "ds_write_b64 v82, v[80:81]\n\t" "s_waitcnt lgkmcnt(4)\n\t"
    "v_mfma_f32_16x16x32_bf16 v[120:123], v[112:115], a[236:239], v[120:123]\n\t" "ds_write_b64 v232, v[96:97]\n\t" "v_mfma_f32_16x16x32_bf16 v[132:135], a[236:239], a[236:239], v[132:135]\n\t"
    "s_add_u32 m0, %[sl], 120832\n\t" "s_nop 0\n\t" "global_load_lds_dwordx4 v0, %[sb] offset:-2048\n\t" "global_load_lds_dwordx4 v0, %[sb] offset:0\n\t"
    "global_load_lds_dwordx4 v0, %[sb] offset:2048\n\t" "v_add_u32 v0, 0x30000, v0\n\t" "s_waitcnt lgkmcnt(0)\n\t" "s_barrier\n\t" "ds_read_b128 v[220:223], v2 offset:0\n\t"
    "ds_read_b128 v[44:47], v18 offset:0\n\t" "v_mfma_f32_16x16x32_bf16 v[116:119], v[112:115], a[240:243], v[116:119]\n\t" "ds_read_b128 v[242:245], v18 offset:256\n\t"
    "ds_read_b128 v[224:227], v2 offset:256\n\t" "v_mfma_f32_16x16x32_bf16 v[128:131], a[240:243], a[240:243], v[128:131]\n\t" "ds_read2_b64 v[148:151], v246 offset0:0 offset1:16\n\t"
    "ds_read2_b64 v[152:155], v246 offset0:32 offset1:48\n\t" "v_mfma_f32_16x16x32_bf16 v[120:123], v[112:115], a[244:247], v[120:123]\n\t" "ds_read_b64_tr_b16 v[156:157], v8 offset:8192\n\t"
    "ds_read_b64_tr_b16 v[158:159], v8 offset:8704\n\t" "v_mfma_f32_16x16x32_bf16 v[132:135], a[244:247], a[244:247], v[132:135]\n\t" "ds_read_b64_tr_b16 v[160:161], v9 offset:8192\n\t"
    "ds_read_b64_tr_b16 v[162:163], v9 offset:8704\n\t" "v_mfma_f32_16x16x32_bf16 v[116:119], v[112:115], a[248:251], v[116:119]\n\t" "ds_read_b64_tr_b16 v[164:165], v10 offset:8192\n\t"
    "ds_read_b64_tr_b16 v[166:167], v10 offset:8704\n\t" "v_mfma_f32_16x16x32_bf16 v[128:131], a[248:251], a[248:251], v[128:131]\n\t" "ds_read_b64_tr_b16 v[168:169], v8 offset:10240\n\t"
    "ds_read_b64_tr_b16 v[170:171], v8 offset:10752\n\t" "v_mfma_f32_16x16x32_bf16 v[120:123], v[112:115], a[252:255], v[120:123]\n\t" "ds_read_b64_tr_b16 v[172:173], v9 offset:10240\n\t"
    "s_waitcnt lgkmcnt(11)\n\t" "ds_read_b64_tr_b16 v[174:175], v9 offset:10752\n\t" "v_mfma_f32_16x16x32_bf16 v[132:135], a[252:255], a[252:255], v[132:135]\n\t"
    "ds_read_b64_tr_b16 v[176:177], v10 offset:10240\n\t" "ds_read_b64_tr_b16 v[178:179], v10 offset:10752\n\t" "v_mfma_f32_4x4x4_16b_bf16 v[204:207], v[220:221], a[0:1], 0\n\t"
    "v_mfma_f32_4x4x4_16b_bf16 v[208:211], v[222:223], a[2:3], 0\n\t" "v_mfma_f32_4x4x4_16b_bf16 v[212:215], v[220:221], a[4:5], 0\n\t" "v_mfma_f32_4x4x4_16b_bf16 v[216:219], v[222:223], a[6:7], 0\n\t"
    "v_mfma_f32_4x4x4_16b_bf16 v[56:59], v[44:45], v[48:49], 0\n\t" "v_mfma_f32_4x4x4_16b_bf16 v[60:63], v[46:47], v[50:51], 0\n\t" "v_mfma_f32_4x4x4_16b_bf16 v[64:67], v[242:243], v[52:53], 0\n\t"
    "v_mfma_f32_4x4x4_16b_bf16 v[68:71], v[244:245], v[54:55], 0\n\t" "s_waitcnt lgkmcnt(12)\n\t" "v_add_f32 v148, v148, v150\n\t" "v_add_f32 v152, v152, v154\n\t" "v_add_f32 v149, v149, v151\n\t"
    "v_add_f32 v153, v153, v155\n\t" "v_add_f32 v241, v148, v152\n\t" "v_add_f32 v238, v149, v153\n\t" "v_cvt_pk_bf16_f32 v48, v56, v57\n\t" "v_cvt_pk_bf16_f32 v49, v58, v59\n\t"
    "v_cvt_pk_bf16_f32 v50, v60, v61\n\t" "v_cvt_pk_bf16_f32 v51, v62, v63\n\t" "v_cvt_pk_bf16_f32 v52, v64, v65\n\t" "v_cvt_pk_bf16_f32 v53, v66, v67\n\t" "v_cvt_pk_bf16_f32 v54, v68, v69\n\t"
    "v_cvt_pk_bf16_f32 v55, v70, v71\n\t" "s_mov_b64 exec, s[60:61]\n\t" "ds_write_b128 v19, v[48:51] offset:0\n\t" "ds_write_b128 v19, v[52:55] offset:1024\n\t" "s_mov_b64 exec, -1\n\t"
    "v_cvt_pk_bf16_f32 v188, v204, v205\n\t" "v_cvt_pk_bf16_f32 v189, v206, v207\n\t" "v_cvt_pk_bf16_f32 v190, v208, v209\n\t" "v_cvt_pk_bf16_f32 v191, v210, v211\n\t"
    "v_cvt_pk_bf16_f32 v192, v212, v213\n\t" "v_cvt_pk_bf16_f32 v193, v214, v215\n\t" "v_cvt_pk_bf16_f32 v194, v216, v217\n\t" "v_cvt_pk_bf16_f32 v195, v218, v219\n\t" "s_nop 1\n\t"
    "s_waitcnt lgkmcnt(6)\n\t" "v_mfma_f32_16x16x32_bf16 v[56:59], v[156:159], v[188:191], 0\n\t" "ds_read_b64_tr_b16 v[180:181], v7 offset:8192\n\t" "s_add_u32 m0, %[sl], 71680\n\t"
    "v_mfma_f32_16x16x32_bf16 v[60:63], v[156:159], v[192:195], 0\n\t" "ds_read_b64_tr_b16 v[182:183], v7 offset:8704\n\t" "v_mfma_f32_16x16x32_bf16 v[64:67], v[160:163], v[188:191], 0\n\t"
    "ds_read_b64_tr_b16 v[184:185], v7 offset:10240\n\t" "global_load_lds_dwordx4 v0, %[sb] offset:-2048\n\t" "v_mfma_f32_16x16x32_bf16 v[68:71], v[160:163], v[192:195], 0\n\t"
    "ds_read_b64_tr_b16 v[186:187], v7 offset:10752\n\t" "v_mfma_f32_4x4x4_16b_bf16 v[204:207], v[224:225], a[8:9], 0\n\t" "v_mfma_f32_4x4x4_16b_bf16 v[208:211], v[226:227], a[10:11], 0\n\t"
    "v_mfma_f32_4x4x4_16b_bf16 v[212:215], v[224:225], a[12:13], 0\n\t" "v_mfma_f32_4x4x4_16b_bf16 v[216:219], v[226:227], a[14:15], 0\n\t" "v_cndmask_b32 v239, 0, v128, s[46:47]\n\t"
    "v_mfma_f32_16x16x32_bf16 v[72:75], v[164:167], v[188:191], 0\n\t" "ds_read_b64_tr_b16 v[156:157], v7 offset:12288\n\t" "v_cndmask_b32 v239, v239, v129, s[48:49]\n\t"
    "v_mfma_f32_16x16x32_bf16 v[76:79], v[164:167], v[192:195], 0\n\t" "ds_read_b64_tr_b16 v[158:159], v7 offset:12800\n\t" "global_load_lds_dwordx4 v0, %[sb] offset:0\n\t"
    "v_mfma_f32_16x16x32_bf16 v[88:91], v[168:171], v[188:191], 0\n\t" "s_waitcnt lgkmcnt(6)\n\t" "s_barrier\n\t" "ds_read_b128 v[228:231], v1 offset:0\n\t"
    "ds_read_b64_tr_b16 v[160:161], v8 offset:12288\n\t" "v_cvt_pk_bf16_f32 v196, v204, v205\n\t" "v_mfma_f32_16x16x32_bf16 v[92:95], v[168:171], v[192:195], 0\n\t"
    "ds_read_b64_tr_b16 v[162:163], v8 offset:12800\n\t" "v_cvt_pk_bf16_f32 v197, v206, v207\n\t" "s_waitcnt lgkmcnt(5)\n\t" "v_mfma_f32_16x16x32_bf16 v[96:99], v[172:175], v[188:191], 0\n\t"
    "ds_read_b64_tr_b16 v[164:165], v9 offset:12288\n\t" "v_cndmask_b32 v239, v239, v130, s[50:51]\n\t" "v_mfma_f32_16x16x32_bf16 v[100:103], v[172:175], v[192:195], 0\n\t"
    "ds_read_b64_tr_b16 v[166:167], v9 offset:12800\n\t" "v_cvt_pk_bf16_f32 v198, v208, v209\n\t" "v_mfma_f32_16x16x32_bf16 v[104:107], v[176:179], v[188:191], 0\n\t"
    "ds_read_b64_tr_b16 v[168:169], v10 offset:12288\n\t" "v_cvt_pk_bf16_f32 v199, v210, v211\n\t" "v_mfma_f32_16x16x32_bf16 v[108:111], v[176:179], v[192:195], 0\n\t"
    "ds_read_b64_tr_b16 v[170:171], v10 offset:12800\n\t" "ds_read_b128 v[220:223], v2 offset:512\n\t" "v_mfma_f32_16x16x32_bf16 v[48:51], v[180:183], v[188:191], 0\n\t"
    "ds_read_b64_tr_b16 v[172:173], v7 offset:20480\n\t" "v_cvt_pk_bf16_f32 v200, v212, v213\n\t" "v_mfma_f32_16x16x32_bf16 v[52:55], v[180:183], v[192:195], 0\n\t"
    "ds_read_b64_tr_b16 v[174:175], v7 offset:20992\n\t" "v_cvt_pk_bf16_f32 v201, v214, v215\n\t" "s_waitcnt lgkmcnt(9)\n\t" "v_mfma_f32_16x16x32_bf16 v[152:155], v[228:231], v[180:183], 0\n\t"
    "global_load_lds_dwordx4 v0, %[sb] offset:2048\n\t" "s_cmp_eq_u32 %[rot], 7\n\t" "s_cselect_b32 s42, s43, 0x30000\n\t" "v_add_u32 v0, s42, v0\n\t"
    "v_mfma_f32_16x16x32_bf16 v[80:83], v[184:187], v[188:191], 0\n\t" "ds_read_b64_tr_b16 v[176:177], v8 offset:20480\n\t" "v_cvt_pk_bf16_f32 v202, v216, v217\n\t"
    "v_mfma_f32_16x16x32_bf16 v[84:87], v[184:187], v[192:195], 0\n\t" "ds_read_b64_tr_b16 v[178:179], v8 offset:20992\n\t" "v_cvt_pk_bf16_f32 v203, v218, v219\n\t"
    "v_mfma_f32_16x16x32_bf16 v[144:147], v[184:187], v[228:231], 0\n\t" "v_cndmask_b32 v239, v239, v131, s[52:53]\n\t" "v_mov_b32_e64 v236, v116\n\t" "v_cndmask_b32 v240, 0, v132, s[46:47]\n\t"
    "v_cndmask_b32 v240, v240, v133, s[48:49]\n\t" "v_cndmask_b32 v240, v240, v134, s[50:51]\n\t" "v_cndmask_b32 v240, v240, v135, s[52:53]\n\t" "v_mov_b32 v237, v120\n\t" "s_waitcnt lgkmcnt(5)\n\t"
    "v_mfma_f32_16x16x32_bf16 v[112:115], v[188:191], v[156:159], 0\n\t" "ds_read_b64_tr_b16 v[180:181], v9 offset:20480\n\t" "s_add_u32 m0, %[sl], 83968\n\t"
    "v_mfma_f32_16x16x32_bf16 v[116:119], v[192:195], v[156:159], 0\n\t" "ds_read_b64_tr_b16 v[182:183], v9 offset:20992\n\t" "global_load_lds_dwordx4 v0, %[sb] offset:-2048\n\t"
    "v_mfma_f32_16x16x32_bf16 v[148:151], v[228:231], v[156:159], 0\n\t" "ds_read_b128 v[232:235], v1 offset:1024\n\t" "global_load_lds_dwordx4 v0, %[sb] offset:0\n\t"
    "v_mfma_f32_16x16x32_bf16 v[120:123], v[188:191], v[160:163], 0\n\t" "ds_read_b64_tr_b16 v[184:185], v10 offset:20480\n\t" "v_mfma_f32_16x16x32_bf16 v[124:127], v[192:195], v[160:163], 0\n\t"
    "ds_read_b64_tr_b16 v[186:187], v10 offset:20992\n\t" "v_mfma_f32_16x16x32_bf16 v[128:131], v[188:191], v[164:167], 0\n\t" "ds_read_b64_tr_b16 v[156:157], v7 offset:22528\n\t"
    "v_mfma_f32_16x16x32_bf16 v[132:135], v[192:195], v[164:167], 0\n\t" "ds_read_b64_tr_b16 v[158:159], v7 offset:23040\n\t" "global_load_lds_dwordx4 v0, %[sb] offset:2048\n\t"
    "v_mfma_f32_16x16x32_bf16 v[136:139], v[188:191], v[168:171], 0\n\t" "ds_read_b64_tr_b16 v[160:161], v8 offset:22528\n\t" "v_add_u32 v0, 0x30000, v0\n\t"
    "v_mfma_f32_16x16x32_bf16 v[140:143], v[192:195], v[168:171], 0\n\t" "ds_read_b64_tr_b16 v[162:163], v8 offset:23040\n\t" "s_waitcnt lgkmcnt(4)\n\t"
    "v_mfma_f32_16x16x32_bf16 v[48:51], v[172:175], v[196:199], v[48:51]\n\t" "ds_read_b64_tr_b16 v[164:165], v9 offset:22528\n\t" "s_add_u32 m0, %[sl], 22528\n\t"
    "v_mfma_f32_16x16x32_bf16 v[52:55], v[172:175], v[200:203], v[52:55]\n\t" "ds_read_b64_tr_b16 v[166:167], v9 offset:23040\n\t" "global_load_lds_dwordx4 v0, %[sb] offset:-2048\n\t"
    "v_mfma_f32_16x16x32_bf16 v[152:155], v[232:235], v[172:175], v[152:155]\n\t" "global_load_lds_dwordx4 v0, %[sb] offset:0\n\t" "v_mfma_f32_16x16x32_bf16 v[56:59], v[176:179], v[196:199], v[56:59]\n\t"
    "ds_read_b64_tr_b16 v[168:169], v10 offset:22528\n\t" "v_mfma_f32_4x4x4_16b_bf16 v[204:207], v[220:221], a[16:17], 0\n\t" "v_mfma_f32_4x4x4_16b_bf16 v[208:211], v[222:223], a[18:19], 0\n\t"
    "v_mfma_f32_4x4x4_16b_bf16 v[212:215], v[220:221], a[20:21], 0\n\t" "v_mfma_f32_4x4x4_16b_bf16 v[216:219], v[222:223], a[22:23], 0\n\t"
    "v_mfma_f32_16x16x32_bf16 v[60:63], v[176:179], v[200:203], v[60:63]\n\t" "ds_read_b64_tr_b16 v[170:171], v10 offset:23040\n\t"
    "v_mfma_f32_16x16x32_bf16 v[64:67], v[180:183], v[196:199], v[64:67]\n\t" "ds_read_b64_tr_b16 v[172:173], v7 offset:24576\n\t" "global_load_lds_dwordx4 v0, %[sb] offset:2048\n\t"
    "v_mfma_f32_16x16x32_bf16 v[68:71], v[180:183], v[200:203], v[68:71]\n\t" "ds_read_b64_tr_b16 v[174:175], v7 offset:25088\n\t" "v_cvt_pk_bf16_f32 v188, v204, v205\n\t"
    "v_mfma_f32_16x16x32_bf16 v[72:75], v[184:187], v[196:199], v[72:75]\n\t" "ds_read_b64_tr_b16 v[176:177], v8 offset:24576\n\t" "v_cvt_pk_bf16_f32 v189, v206, v207\n\t"
    "v_mfma_f32_16x16x32_bf16 v[76:79], v[184:187], v[200:203], v[76:79]\n\t" "ds_read_b64_tr_b16 v[178:179], v8 offset:25088\n\t" "s_waitcnt lgkmcnt(4)\n\t"
    "v_mfma_f32_16x16x32_bf16 v[80:83], v[156:159], v[196:199], v[80:83]\n\t" "ds_read_b64_tr_b16 v[180:181], v9 offset:24576\n\t" "v_cvt_pk_bf16_f32 v190, v208, v209\n\t"
    "v_mfma_f32_16x16x32_bf16 v[84:87], v[156:159], v[200:203], v[84:87]\n\t" "ds_read_b64_tr_b16 v[182:183], v9 offset:25088\n\t" "v_cvt_pk_bf16_f32 v191, v210, v211\n\t"
    "v_mfma_f32_16x16x32_bf16 v[144:147], v[156:159], v[232:235], v[144:147]\n\t" "ds_read_b128 v[224:227], v2 offset:768\n\t" "v_add_u32 v0, 0x30000, v0\n\t"
    "v_mfma_f32_16x16x32_bf16 v[88:91], v[160:163], v[196:199], v[88:91]\n\t" "ds_read_b64_tr_b16 v[184:185], v10 offset:24576\n\t" "v_cvt_pk_bf16_f32 v192, v212, v213\n\t"
    "v_mfma_f32_16x16x32_bf16 v[92:95], v[160:163], v[200:203], v[92:95]\n\t" "ds_read_b64_tr_b16 v[186:187], v10 offset:25088\n\t" "v_cvt_pk_bf16_f32 v193, v214, v215\n\t"
    "v_mfma_f32_16x16x32_bf16 v[96:99], v[164:167], v[196:199], v[96:99]\n\t" "s_waitcnt vmcnt(9)\n\t" "s_barrier\n\t" "ds_read_b64_tr_b16 v[156:157], v7 offset:32768\n\t"
    "v_mfma_f32_16x16x32_bf16 v[100:103], v[164:167], v[200:203], v[100:103]\n\t" "ds_read_b64_tr_b16 v[158:159], v7 offset:33280\n\t" "v_cvt_pk_bf16_f32 v194, v216, v217\n\t"
    "v_mfma_f32_16x16x32_bf16 v[104:107], v[168:171], v[196:199], v[104:107]\n\t" "ds_read_b64_tr_b16 v[160:161], v8 offset:32768\n\t" "v_cvt_pk_bf16_f32 v195, v218, v219\n\t"
    "v_mfma_f32_16x16x32_bf16 v[108:111], v[168:171], v[200:203], v[108:111]\n\t" "ds_read_b64_tr_b16 v[162:163], v8 offset:33280\n\t" "s_waitcnt lgkmcnt(4)\n\t"
    "v_mfma_f32_16x16x32_bf16 v[112:115], v[196:199], v[172:175], v[112:115]\n\t" "ds_read_b64_tr_b16 v[164:165], v9 offset:32768\n\t" "s_add_u32 m0, %[sl], 34816\n\t"
    "v_mfma_f32_16x16x32_bf16 v[116:119], v[200:203], v[172:175], v[116:119]\n\t" "ds_read_b64_tr_b16 v[166:167], v9 offset:33280\n\t" "global_load_lds_dwordx4 v0, %[sb] offset:-2048\n\t"
    "v_mfma_f32_16x16x32_bf16 v[148:151], v[232:235], v[172:175], v[148:151]\n\t" "ds_read_b128 v[228:231], v1 offset:2048\n\t" "global_load_lds_dwordx4 v0, %[sb] offset:0\n\t"
    "v_mfma_f32_16x16x32_bf16 v[120:123], v[196:199], v[176:179], v[120:123]\n\t" "ds_read_b64_tr_b16 v[168:169], v10 offset:32768\n\t" "global_load_lds_dwordx4 v0, %[sb] offset:2048\n\t"
    "v_mfma_f32_16x16x32_bf16 v[124:127], v[200:203], v[176:179], v[124:127]\n\t" "ds_read_b64_tr_b16 v[170:171], v10 offset:33280\n\t" "v_add_u32 v0, 0x30000, v0\n\t"
    "v_mfma_f32_16x16x32_bf16 v[128:131], v[196:199], v[180:183], v[128:131]\n\t" "ds_read_b64_tr_b16 v[172:173], v7 offset:34816\n\t"
    "v_mfma_f32_16x16x32_bf16 v[132:135], v[200:203], v[180:183], v[132:135]\n\t" "ds_read_b64_tr_b16 v[174:175], v7 offset:35328\n\t" "s_mov_b64 exec, %[tm]\n\t" "global_load_dword v17, v16, %[pf]\n\t"
    "s_mov_b64 exec, -1\n\t" "v_mfma_f32_16x16x32_bf16 v[136:139], v[196:199], v[184:187], v[136:139]\n\t" "ds_read_b64_tr_b16 v[176:177], v8 offset:34816\n\t"
    "v_mfma_f32_16x16x32_bf16 v[140:143], v[200:203], v[184:187], v[140:143]\n\t" "ds_read_b64_tr_b16 v[178:179], v8 offset:35328\n\t" "s_waitcnt lgkmcnt(4)\n\t"
    "v_mfma_f32_16x16x32_bf16 v[48:51], v[156:159], v[188:191], v[48:51]\n\t" "ds_read_b64_tr_b16 v[180:181], v9 offset:34816\n\t" "s_add_u32 m0, %[sl], 47104\n\t"
    "v_mfma_f32_16x16x32_bf16 v[52:55], v[156:159], v[192:195], v[52:55]\n\t" "ds_read_b64_tr_b16 v[182:183], v9 offset:35328\n\t"
    "v_mfma_f32_16x16x32_bf16 v[152:155], v[228:231], v[156:159], v[152:155]\n\t" "global_load_lds_dwordx4 v0, %[sb] offset:-2048\n\t"
    "v_mfma_f32_16x16x32_bf16 v[56:59], v[160:163], v[188:191], v[56:59]\n\t" "ds_read_b64_tr_b16 v[184:185], v10 offset:34816\n\t" "v_mfma_f32_4x4x4_16b_bf16 v[204:207], v[224:225], a[24:25], 0\n\t"
    "v_mfma_f32_4x4x4_16b_bf16 v[208:211], v[226:227], a[26:27], 0\n\t" "v_mfma_f32_4x4x4_16b_bf16 v[212:215], v[224:225], a[28:29], 0\n\t"
    "v_mfma_f32_4x4x4_16b_bf16 v[216:219], v[226:227], a[30:31], 0\n\t" "v_mfma_f32_16x16x32_bf16 v[60:63], v[160:163], v[192:195], v[60:63]\n\t" "ds_read_b64_tr_b16 v[186:187], v10 offset:35328\n\t"
    "v_mfma_f32_16x16x32_bf16 v[64:67], v[164:167], v[188:191], v[64:67]\n\t" "ds_read_b64_tr_b16 v[156:157], v7 offset:36864\n\t" "global_load_lds_dwordx4 v0, %[sb] offset:0\n\t"
    "v_mfma_f32_16x16x32_bf16 v[68:71], v[164:167], v[192:195], v[68:71]\n\t" "ds_read_b64_tr_b16 v[158:159], v7 offset:37376\n\t" "v_cvt_pk_bf16_f32 v196, v204, v205\n\t"
    "v_mfma_f32_16x16x32_bf16 v[72:75], v[168:171], v[188:191], v[72:75]\n\t" "ds_read_b64_tr_b16 v[160:161], v8 offset:36864\n\t" "v_cvt_pk_bf16_f32 v197, v206, v207\n\t"
    "v_mfma_f32_16x16x32_bf16 v[76:79], v[168:171], v[192:195], v[76:79]\n\t" "ds_read_b64_tr_b16 v[162:163], v8 offset:37376\n\t" "s_waitcnt lgkmcnt(4)\n\t"
    "v_mfma_f32_16x16x32_bf16 v[80:83], v[172:175], v[188:191], v[80:83]\n\t" "ds_read_b64_tr_b16 v[164:165], v9 offset:36864\n\t" "v_cvt_pk_bf16_f32 v198, v208, v209\n\t"
    "v_mfma_f32_16x16x32_bf16 v[84:87], v[172:175], v[192:195], v[84:87]\n\t" "ds_read_b64_tr_b16 v[166:167], v9 offset:37376\n\t" "v_cvt_pk_bf16_f32 v199, v210, v211\n\t"
    "v_mfma_f32_16x16x32_bf16 v[144:147], v[172:175], v[228:231], v[144:147]\n\t" "ds_read_b128 v[220:223], v2 offset:1024\n\t" "global_load_lds_dwordx4 v0, %[sb] offset:2048\n\t"
    "v_mfma_f32_16x16x32_bf16 v[88:91], v[176:179], v[188:191], v[88:91]\n\t" "ds_read_b64_tr_b16 v[168:169], v10 offset:36864\n\t" "v_cvt_pk_bf16_f32 v200, v212, v213\n\t"
    "v_mfma_f32_16x16x32_bf16 v[92:95], v[176:179], v[192:195], v[92:95]\n\t" "ds_read_b64_tr_b16 v[170:171], v10 offset:37376\n\t" "v_cvt_pk_bf16_f32 v201, v214, v215\n\t"
    "v_mfma_f32_16x16x32_bf16 v[96:99], v[180:183], v[188:191], v[96:99]\n\t" "s_waitcnt vmcnt(13)\n\t" "s_barrier\n\t" "ds_read_b64_tr_b16 v[172:173], v3 offset:49152\n\t"
    "v_mfma_f32_16x16x32_bf16 v[100:103], v[180:183], v[192:195], v[100:103]\n\t" "ds_read_b64_tr_b16 v[174:175], v3 offset:49664\n\t" "v_cvt_pk_bf16_f32 v202, v216, v217\n\t"
    "v_mfma_f32_16x16x32_bf16 v[104:107], v[184:187], v[188:191], v[104:107]\n\t" "ds_read_b64_tr_b16 v[176:177], v4 offset:49152\n\t" "v_cvt_pk_bf16_f32 v203, v218, v219\n\t"
    "v_mfma_f32_16x16x32_bf16 v[108:111], v[184:187], v[192:195], v[108:111]\n\t" "ds_read_b64_tr_b16 v[178:179], v4 offset:49664\n\t" "s_waitcnt lgkmcnt(4)\n\t"
    "v_mfma_f32_16x16x32_bf16 v[112:115], v[188:191], v[156:159], v[112:115]\n\t" "ds_read_b64_tr_b16 v[180:181], v5 offset:49152\n\t" "s_cmp_eq_u32 %[rot], 6\n\t" "s_cselect_b32 s42, s43, 0x30000\n\t"
    "v_add_u32 v0, s42, v0\n\t" "v_mfma_f32_16x16x32_bf16 v[116:119], v[192:195], v[156:159], v[116:119]\n\t" "ds_read_b64_tr_b16 v[182:183], v5 offset:49664\n\t" "s_add_u32 m0, %[sl], 59392\n\t"
    "v_mfma_f32_16x16x32_bf16 v[148:151], v[228:231], v[156:159], v[148:151]\n\t" "ds_read_b128 v[232:235], v1 offset:3072\n\t" "global_load_lds_dwordx4 v0, %[sb] offset:-2048\n\t"
    "v_mfma_f32_16x16x32_bf16 v[120:123], v[188:191], v[160:163], v[120:123]\n\t" "ds_read_b64_tr_b16 v[184:185], v6 offset:49152\n\t" "global_load_lds_dwordx4 v0, %[sb] offset:0\n\t"
    "v_mfma_f32_16x16x32_bf16 v[124:127], v[192:195], v[160:163], v[124:127]\n\t" "ds_read_b64_tr_b16 v[186:187], v6 offset:49664\n\t"
    "v_mfma_f32_16x16x32_bf16 v[128:131], v[188:191], v[164:167], v[128:131]\n\t" "ds_read_b64_tr_b16 v[156:157], v3 offset:51200\n\t"
    "v_mfma_f32_16x16x32_bf16 v[132:135], v[192:195], v[164:167], v[132:135]\n\t" "ds_read_b64_tr_b16 v[158:159], v3 offset:51712\n\t" "global_load_lds_dwordx4 v0, %[sb] offset:2048\n\t"
    "v_mfma_f32_16x16x32_bf16 v[136:139], v[188:191], v[168:171], v[136:139]\n\t" "ds_read_b64_tr_b16 v[160:161], v4 offset:51200\n\t" "v_add_u32 v0, 0x30000, v0\n\t"
    "v_mfma_f32_16x16x32_bf16 v[140:143], v[192:195], v[168:171], v[140:143]\n\t" "ds_read_b64_tr_b16 v[162:163], v4 offset:51712\n\t" "s_waitcnt lgkmcnt(4)\n\t"
    "v_mfma_f32_16x16x32_bf16 v[48:51], v[172:175], v[196:199], v[48:51]\n\t" "ds_read_b64_tr_b16 v[164:165], v5 offset:51200\n\t" "s_add_u32 m0, %[sl], 96256\n\t"
    "v_mfma_f32_16x16x32_bf16 v[52:55], v[172:175], v[200:203], v[52:55]\n\t" "ds_read_b64_tr_b16 v[166:167], v5 offset:51712\n\t" "ds_read_b128 v[44:47], v18 offset:2048\n\t"
    "v_mfma_f32_16x16x32_bf16 v[152:155], v[232:235], v[172:175], v[152:155]\n\t" "global_load_lds_dwordx4 v0, %[sb] offset:-2048\n\t" "ds_read_b128 v[242:245], v18 offset:2304\n\t"
    "v_mfma_f32_16x16x32_bf16 v[56:59], v[176:179], v[196:199], v[56:59]\n\t" "ds_read_b64_tr_b16 v[168:169], v6 offset:51200\n\t" "v_mfma_f32_4x4x4_16b_bf16 v[204:207], v[220:221], a[32:33], 0\n\t"
    "v_mfma_f32_4x4x4_16b_bf16 v[208:211], v[222:223], a[34:35], 0\n\t" "v_mfma_f32_4x4x4_16b_bf16 v[212:215], v[220:221], a[36:37], 0\n\t"
    "v_mfma_f32_4x4x4_16b_bf16 v[216:219], v[222:223], a[38:39], 0\n\t" "v_mfma_f32_16x16x32_bf16 v[60:63], v[176:179], v[200:203], v[60:63]\n\t" "ds_read_b64_tr_b16 v[170:171], v6 offset:51712\n\t"
    "v_mfma_f32_16x16x32_bf16 v[64:67], v[180:183], v[196:199], v[64:67]\n\t" "ds_read_b64_tr_b16 v[172:173], v3 offset:53248\n\t" "v_mfma_f32_16x16x32_bf16 v[68:71], v[180:183], v[200:203], v[68:71]\n\t"
    "ds_read_b64_tr_b16 v[174:175], v3 offset:53760\n\t" "v_cvt_pk_bf16_f32 v188, v204, v205\n\t" "v_mfma_f32_16x16x32_bf16 v[72:75], v[184:187], v[196:199], v[72:75]\n\t"
    "ds_read_b64_tr_b16 v[176:177], v4 offset:53248\n\t" "v_cvt_pk_bf16_f32 v189, v206, v207\n\t" "v_mfma_f32_16x16x32_bf16 v[76:79], v[184:187], v[200:203], v[76:79]\n\t"
    "ds_read_b64_tr_b16 v[178:179], v4 offset:53760\n\t" "s_waitcnt lgkmcnt(4)\n\t" "v_mfma_f32_16x16x32_bf16 v[80:83], v[156:159], v[196:199], v[80:83]\n\t"
    "ds_read_b64_tr_b16 v[180:181], v5 offset:53248\n\t" "v_cvt_pk_bf16_f32 v190, v208, v209\n\t" "v_mfma_f32_16x16x32_bf16 v[84:87], v[156:159], v[200:203], v[84:87]\n\t"
    "ds_read_b64_tr_b16 v[182:183], v5 offset:53760\n\t" "v_cvt_pk_bf16_f32 v191, v210, v211\n\t" "v_mfma_f32_16x16x32_bf16 v[144:147], v[156:159], v[232:235], v[144:147]\n\t"
    "ds_read_b128 v[224:227], v2 offset:1280\n\t" "global_load_lds_dwordx4 v0, %[sb] offset:0\n\t" "v_mfma_f32_16x16x32_bf16 v[88:91], v[160:163], v[196:199], v[88:91]\n\t"
    "ds_read_b64_tr_b16 v[184:185], v6 offset:53248\n\t" "v_cvt_pk_bf16_f32 v192, v212, v213\n\t" "v_mfma_f32_16x16x32_bf16 v[92:95], v[160:163], v[200:203], v[92:95]\n\t"
    "ds_read_b64_tr_b16 v[186:187], v6 offset:53760\n\t" "v_cvt_pk_bf16_f32 v193, v214, v215\n\t" "v_mfma_f32_16x16x32_bf16 v[96:99], v[164:167], v[196:199], v[96:99]\n\t" "s_waitcnt vmcnt(12)\n\t"
    "s_barrier\n\t" "ds_read_b64_tr_b16 v[156:157], v3 offset:61440\n\t" "v_mfma_f32_16x16x32_bf16 v[100:103], v[164:167], v[200:203], v[100:103]\n\t" "ds_read_b64_tr_b16 v[158:159], v3 offset:61952\n\t"
    "v_cvt_pk_bf16_f32 v194, v216, v217\n\t" "v_mfma_f32_16x16x32_bf16 v[104:107], v[168:171], v[196:199], v[104:107]\n\t" "ds_read_b64_tr_b16 v[160:161], v4 offset:61440\n\t"
    "v_cvt_pk_bf16_f32 v195, v218, v219\n\t" "v_mfma_f32_16x16x32_bf16 v[108:111], v[168:171], v[200:203], v[108:111]\n\t" "ds_read_b64_tr_b16 v[162:163], v4 offset:61952\n\t"
    "v_mfma_f32_4x4x4_16b_bf16 v[204:207], v[44:45], v[20:21], 0\n\t" "v_mfma_f32_4x4x4_16b_bf16 v[208:211], v[46:47], v[22:23], 0\n\t" "v_mfma_f32_4x4x4_16b_bf16 v[212:215], v[242:243], v[24:25], 0\n\t"
    "v_mfma_f32_4x4x4_16b_bf16 v[216:219], v[244:245], v[26:27], 0\n\t" "s_waitcnt lgkmcnt(4)\n\t" "v_mfma_f32_16x16x32_bf16 v[112:115], v[196:199], v[172:175], v[112:115]\n\t"
    "ds_read_b64_tr_b16 v[164:165], v5 offset:61440\n\t" "v_mfma_f32_16x16x32_bf16 v[116:119], v[200:203], v[172:175], v[116:119]\n\t" "ds_read_b64_tr_b16 v[166:167], v5 offset:61952\n\t"
    "v_mfma_f32_16x16x32_bf16 v[148:151], v[232:235], v[172:175], v[148:151]\n\t" "ds_read_b128 v[228:231], v1 offset:4096\n\t" "global_load_lds_dwordx4 v0, %[sb] offset:2048\n\t"
    "v_mfma_f32_16x16x32_bf16 v[120:123], v[196:199], v[176:179], v[120:123]\n\t" "ds_read_b64_tr_b16 v[168:169], v6 offset:61440\n\t" "v_add_u32 v0, 0x30000, v0\n\t"
    "v_mfma_f32_16x16x32_bf16 v[124:127], v[200:203], v[176:179], v[124:127]\n\t" "ds_read_b64_tr_b16 v[170:171], v6 offset:61952\n\t" "v_cvt_pk_bf16_f32 v20, v204, v205\n\t"
    "v_mfma_f32_16x16x32_bf16 v[128:131], v[196:199], v[180:183], v[128:131]\n\t" "ds_read_b64_tr_b16 v[172:173], v3 offset:63488\n\t" "v_cvt_pk_bf16_f32 v21, v206, v207\n\t"
    "v_mfma_f32_16x16x32_bf16 v[132:135], v[200:203], v[180:183], v[132:135]\n\t" "ds_read_b64_tr_b16 v[174:175], v3 offset:64000\n\t" "v_cvt_pk_bf16_f32 v22, v208, v209\n\t"
    "v_mfma_f32_16x16x32_bf16 v[136:139], v[196:199], v[184:187], v[136:139]\n\t" "ds_read_b64_tr_b16 v[176:177], v4 offset:63488\n\t" "v_cvt_pk_bf16_f32 v23, v210, v211\n\t"
    "v_mfma_f32_16x16x32_bf16 v[140:143], v[200:203], v[184:187], v[140:143]\n\t" "ds_read_b64_tr_b16 v[178:179], v4 offset:64000\n\t" "s_waitcnt lgkmcnt(4)\n\t"
    "v_mfma_f32_16x16x32_bf16 v[48:51], v[156:159], v[188:191], v[48:51]\n\t" "ds_read_b64_tr_b16 v[180:181], v5 offset:63488\n\t" "s_add_u32 m0, %[sl], 108544\n\t"
    "v_mfma_f32_16x16x32_bf16 v[52:55], v[156:159], v[192:195], v[52:55]\n\t" "ds_read_b64_tr_b16 v[182:183], v5 offset:64000\n\t" "v_cvt_pk_bf16_f32 v24, v212, v213\n\t"
    "v_mfma_f32_16x16x32_bf16 v[152:155], v[228:231], v[156:159], v[152:155]\n\t" "v_cvt_pk_bf16_f32 v25, v214, v215\n\t" "v_cvt_pk_bf16_f32 v26, v216, v217\n\t" "v_cvt_pk_bf16_f32 v27, v218, v219\n\t"
    "s_mov_b64 exec, s[60:61]\n\t" "ds_write_b128 v19, v[20:23] offset:8192\n\t" "ds_write_b128 v19, v[24:27] offset:9216\n\t" "s_mov_b64 exec, -1\n\t"
    "v_mfma_f32_16x16x32_bf16 v[56:59], v[160:163], v[188:191], v[56:59]\n\t" "ds_read_b64_tr_b16 v[184:185], v6 offset:63488\n\t" "global_load_lds_dwordx4 v0, %[sb] offset:-2048\n\t"
    "v_mfma_f32_4x4x4_16b_bf16 v[204:207], v[224:225], a[40:41], 0\n\t" "v_mfma_f32_4x4x4_16b_bf16 v[208:211], v[226:227], a[42:43], 0\n\t"
    "v_mfma_f32_4x4x4_16b_bf16 v[212:215], v[224:225], a[44:45], 0\n\t" "v_mfma_f32_4x4x4_16b_bf16 v[216:219], v[226:227], a[46:47], 0\n\t"
    "v_mfma_f32_16x16x32_bf16 v[60:63], v[160:163], v[192:195], v[60:63]\n\t" "ds_read_b64_tr_b16 v[186:187], v6 offset:64000\n\t" "v_mfma_f32_16x16x32_bf16 v[64:67], v[164:167], v[188:191], v[64:67]\n\t"
    "ds_read_b64_tr_b16 v[156:157], v7 offset:0\n\t" "v_mfma_f32_16x16x32_bf16 v[68:71], v[164:167], v[192:195], v[68:71]\n\t" "ds_read_b64_tr_b16 v[158:159], v7 offset:512\n\t"
    "v_cvt_pk_bf16_f32 v196, v204, v205\n\t" "v_mfma_f32_16x16x32_bf16 v[72:75], v[168:171], v[188:191], v[72:75]\n\t" "ds_read_b64_tr_b16 v[160:161], v8 offset:0\n\t"
    "v_cvt_pk_bf16_f32 v197, v206, v207\n\t" "v_mfma_f32_16x16x32_bf16 v[76:79], v[168:171], v[192:195], v[76:79]\n\t" "ds_read_b64_tr_b16 v[162:163], v8 offset:512\n\t" "s_waitcnt lgkmcnt(4)\n\t"
    "v_mfma_f32_16x16x32_bf16 v[80:83], v[172:175], v[188:191], v[80:83]\n\t" "ds_read_b64_tr_b16 v[164:165], v9 offset:0\n\t" "v_cvt_pk_bf16_f32 v198, v208, v209\n\t"
    "v_mfma_f32_16x16x32_bf16 v[84:87], v[172:175], v[192:195], v[84:87]\n\t" "ds_read_b64_tr_b16 v[166:167], v9 offset:512\n\t" "v_cvt_pk_bf16_f32 v199, v210, v211\n\t"
    "v_mfma_f32_16x16x32_bf16 v[144:147], v[172:175], v[228:231], v[144:147]\n\t" "ds_read_b128 v[220:223], v2 offset:1536\n\t" "global_load_lds_dwordx4 v0, %[sb] offset:0\n\t"
    "v_mfma_f32_16x16x32_bf16 v[88:91], v[176:179], v[188:191], v[88:91]\n\t" "ds_read_b64_tr_b16 v[168:169], v10 offset:0\n\t" "v_cvt_pk_bf16_f32 v200, v212, v213\n\t"
    "v_mfma_f32_16x16x32_bf16 v[92:95], v[176:179], v[192:195], v[92:95]\n\t" "ds_read_b64_tr_b16 v[170:171], v10 offset:512\n\t" "v_cvt_pk_bf16_f32 v201, v214, v215\n\t"
    "v_mfma_f32_16x16x32_bf16 v[96:99], v[180:183], v[188:191], v[96:99]\n\t" "ds_read_b64_tr_b16 v[172:173], v3 offset:0\n\t" "v_mfma_f32_16x16x32_bf16 v[100:103], v[180:183], v[192:195], v[100:103]\n\t"
    "ds_read_b64_tr_b16 v[174:175], v3 offset:512\n\t" "v_cvt_pk_bf16_f32 v202, v216, v217\n\t" "v_mfma_f32_16x16x32_bf16 v[104:107], v[184:187], v[188:191], v[104:107]\n\t"
    "ds_read_b64_tr_b16 v[176:177], v4 offset:0\n\t" "v_cvt_pk_bf16_f32 v203, v218, v219\n\t" "v_mfma_f32_16x16x32_bf16 v[108:111], v[184:187], v[192:195], v[108:111]\n\t"
    "ds_read_b64_tr_b16 v[178:179], v4 offset:512\n\t" "s_waitcnt lgkmcnt(4)\n\t" "v_mfma_f32_16x16x32_bf16 v[112:115], v[188:191], v[156:159], v[112:115]\n\t"
    "ds_read_b64_tr_b16 v[180:181], v5 offset:0\n\t" "v_mfma_f32_16x16x32_bf16 v[116:119], v[192:195], v[156:159], v[116:119]\n\t" "ds_read_b64_tr_b16 v[182:183], v5 offset:512\n\t"
    "v_mfma_f32_16x16x32_bf16 v[148:151], v[228:231], v[156:159], v[148:151]\n\t" "ds_read_b128 v[232:235], v1 offset:5120\n\t" "global_load_lds_dwordx4 v0, %[sb] offset:2048\n\t"
    "v_mfma_f32_16x16x32_bf16 v[120:123], v[188:191], v[160:163], v[120:123]\n\t" "ds_read_b64_tr_b16 v[184:185], v6 offset:0\n\t" "v_add_u32 v0, 0x30000, v0\n\t"
    "v_mfma_f32_16x16x32_bf16 v[124:127], v[192:195], v[160:163], v[124:127]\n\t" "ds_read_b64_tr_b16 v[186:187], v6 offset:512\n\t"
    "v_mfma_f32_16x16x32_bf16 v[128:131], v[188:191], v[164:167], v[128:131]\n\t" "ds_read_b64_tr_b16 v[156:157], v3 offset:2048\n\t"
    "v_mfma_f32_16x16x32_bf16 v[132:135], v[192:195], v[164:167], v[132:135]\n\t" "ds_read_b64_tr_b16 v[158:159], v3 offset:2560\n\t" "s_mov_b64 exec, 0xfffffff\n\t"
    "global_load_dword v17, v[14:15], off\n\t" "s_mov_b64 exec, -1\n\t" "v_mfma_f32_16x16x32_bf16 v[136:139], v[188:191], v[168:171], v[136:139]\n\t" "ds_read_b64_tr_b16 v[160:161], v4 offset:2048\n\t"
    "v_mfma_f32_16x16x32_bf16 v[140:143], v[192:195], v[168:171], v[140:143]\n\t" "ds_read_b64_tr_b16 v[162:163], v4 offset:2560\n\t" "s_waitcnt lgkmcnt(4)\n\t"
    "v_mfma_f32_16x16x32_bf16 v[48:51], v[172:175], v[196:199], v[48:51]\n\t" "ds_read_b64_tr_b16 v[164:165], v5 offset:2048\n\t" "s_add_u32 m0, %[sl], 120832\n\t"
    "v_mfma_f32_16x16x32_bf16 v[52:55], v[172:175], v[200:203], v[52:55]\n\t" "ds_read_b64_tr_b16 v[166:167], v5 offset:2560\n\t" "ds_read_b128 v[44:47], v18 offset:4096\n\t"
    "v_mfma_f32_16x16x32_bf16 v[152:155], v[232:235], v[172:175], v[152:155]\n\t" "global_load_lds_dwordx4 v0, %[sb] offset:-2048\n\t" "ds_read_b128 v[242:245], v18 offset:4352\n\t"
    "v_mfma_f32_16x16x32_bf16 v[56:59], v[176:179], v[196:199], v[56:59]\n\t" "ds_read_b64_tr_b16 v[168:169], v6 offset:2048\n\t" "v_mov_b32_e64 v20, v239\n\t"
    "v_mfma_f32_4x4x4_16b_bf16 v[204:207], v[220:221], a[48:49], 0\n\t" "v_mfma_f32_4x4x4_16b_bf16 v[208:211], v[222:223], a[50:51], 0\n\t"
    "v_mfma_f32_4x4x4_16b_bf16 v[212:215], v[220:221], a[52:53], 0\n\t" "v_mfma_f32_4x4x4_16b_bf16 v[216:219], v[222:223], a[54:55], 0\n\t" "v_mov_b32_e64 v21, v240\n\t"
    "v_mfma_f32_16x16x32_bf16 v[60:63], v[176:179], v[200:203], v[60:63]\n\t" "ds_read_b64_tr_b16 v[170:171], v6 offset:2560\n\t" "v_mfma_f32_16x16x32_bf16 v[64:67], v[180:183], v[196:199], v[64:67]\n\t"
    "ds_read_b64_tr_b16 v[172:173], v3 offset:4096\n\t" "v_mfma_f32_16x16x32_bf16 v[68:71], v[180:183], v[200:203], v[68:71]\n\t" "ds_read_b64_tr_b16 v[174:175], v3 offset:4608\n\t"
    "v_cvt_pk_bf16_f32 v188, v204, v205\n\t" "v_mfma_f32_16x16x32_bf16 v[72:75], v[184:187], v[196:199], v[72:75]\n\t" "ds_read_b64_tr_b16 v[176:177], v4 offset:4096\n\t"
    "v_cvt_pk_bf16_f32 v189, v206, v207\n\t" "v_mfma_f32_16x16x32_bf16 v[76:79], v[184:187], v[200:203], v[76:79]\n\t" "ds_read_b64_tr_b16 v[178:179], v4 offset:4608\n\t" "s_waitcnt lgkmcnt(4)\n\t"
    "v_mfma_f32_16x16x32_bf16 v[80:83], v[156:159], v[196:199], v[80:83]\n\t" "ds_read_b64_tr_b16 v[180:181], v5 offset:4096\n\t" "v_cvt_pk_bf16_f32 v190, v208, v209\n\t"
    "v_mfma_f32_16x16x32_bf16 v[84:87], v[156:159], v[200:203], v[84:87]\n\t" "ds_read_b64_tr_b16 v[182:183], v5 offset:4608\n\t" "v_cvt_pk_bf16_f32 v191, v210, v211\n\t"
    "v_mfma_f32_16x16x32_bf16 v[144:147], v[156:159], v[232:235], v[144:147]\n\t" "ds_read_b128 v[224:227], v2 offset:1792\n\t" "global_load_lds_dwordx4 v0, %[sb] offset:0\n\t"
    "v_mfma_f32_16x16x32_bf16 v[88:91], v[160:163], v[196:199], v[88:91]\n\t" "ds_read_b64_tr_b16 v[184:185], v6 offset:4096\n\t" "v_cvt_pk_bf16_f32 v192, v212, v213\n\t"
    "v_mfma_f32_16x16x32_bf16 v[92:95], v[160:163], v[200:203], v[92:95]\n\t" "ds_read_b64_tr_b16 v[186:187], v6 offset:4608\n\t" "v_cvt_pk_bf16_f32 v193, v214, v215\n\t"
    "v_mfma_f32_16x16x32_bf16 v[96:99], v[164:167], v[196:199], v[96:99]\n\t" "s_waitcnt vmcnt(12)\n\t" "s_barrier\n\t" "ds_read_b64_tr_b16 v[156:157], v3 offset:12288\n\t"
    "v_mfma_f32_16x16x32_bf16 v[100:103], v[164:167], v[200:203], v[100:103]\n\t" "ds_read_b64_tr_b16 v[158:159], v3 offset:12800\n\t" "v_cvt_pk_bf16_f32 v194, v216, v217\n\t"
    "v_mfma_f32_16x16x32_bf16 v[104:107], v[168:171], v[196:199], v[104:107]\n\t" "ds_read_b64_tr_b16 v[160:161], v4 offset:12288\n\t" "v_cvt_pk_bf16_f32 v195, v218, v219\n\t"
    "v_mfma_f32_16x16x32_bf16 v[108:111], v[168:171], v[200:203], v[108:111]\n\t" "ds_read_b64_tr_b16 v[162:163], v4 offset:12800\n\t" "v_mfma_f32_4x4x4_16b_bf16 v[204:207], v[44:45], v[28:29], 0\n\t"
    "v_mfma_f32_4x4x4_16b_bf16 v[208:211], v[46:47], v[30:31], 0\n\t" "v_mfma_f32_4x4x4_16b_bf16 v[212:215], v[242:243], v[32:33], 0\n\t"
    "v_mfma_f32_4x4x4_16b_bf16 v[216:219], v[244:245], v[34:35], 0\n\t" "s_waitcnt lgkmcnt(4)\n\t" "v_mfma_f32_16x16x32_bf16 v[112:115], v[196:199], v[172:175], v[112:115]\n\t"
    "ds_read_b64_tr_b16 v[164:165], v5 offset:12288\n\t" "v_mfma_f32_16x16x32_bf16 v[116:119], v[200:203], v[172:175], v[116:119]\n\t" "ds_read_b64_tr_b16 v[166:167], v5 offset:12800\n\t"
    "v_mfma_f32_16x16x32_bf16 v[148:151], v[232:235], v[172:175], v[148:151]\n\t" "ds_read_b128 v[228:231], v1 offset:6144\n\t" "global_load_lds_dwordx4 v0, %[sb] offset:2048\n\t"
    "v_mfma_f32_16x16x32_bf16 v[120:123], v[196:199], v[176:179], v[120:123]\n\t" "ds_read_b64_tr_b16 v[168:169], v6 offset:12288\n\t" "s_cmp_eq_u32 %[rot], 5\n\t" "s_cselect_b32 s42, s43, 0x30000\n\t"
    "v_add_u32 v0, s42, v0\n\t" "v_mfma_f32_16x16x32_bf16 v[124:127], v[200:203], v[176:179], v[124:127]\n\t" "ds_read_b64_tr_b16 v[170:171], v6 offset:12800\n\t" "v_cvt_pk_bf16_f32 v28, v204, v205\n\t"
    "v_mfma_f32_16x16x32_bf16 v[128:131], v[196:199], v[180:183], v[128:131]\n\t" "ds_read_b64_tr_b16 v[172:173], v3 offset:14336\n\t" "v_cvt_pk_bf16_f32 v29, v206, v207\n\t"
    "v_mfma_f32_16x16x32_bf16 v[132:135], v[200:203], v[180:183], v[132:135]\n\t" "ds_read_b64_tr_b16 v[174:175], v3 offset:14848\n\t" "v_cvt_pk_bf16_f32 v30, v208, v209\n\t"
    "v_mfma_f32_16x16x32_bf16 v[136:139], v[196:199], v[184:187], v[136:139]\n\t" "ds_read_b64_tr_b16 v[176:177], v4 offset:14336\n\t" "v_cvt_pk_bf16_f32 v31, v210, v211\n\t"
    "v_mfma_f32_16x16x32_bf16 v[140:143], v[200:203], v[184:187], v[140:143]\n\t" "ds_read_b64_tr_b16 v[178:179], v4 offset:14848\n\t" "s_waitcnt lgkmcnt(4)\n\t"
    "v_mfma_f32_16x16x32_bf16 v[48:51], v[156:159], v[188:191], v[48:51]\n\t" "ds_read_b64_tr_b16 v[180:181], v5 offset:14336\n\t" "s_add_u32 m0, %[sl], 71680\n\t"
    "v_mfma_f32_16x16x32_bf16 v[52:55], v[156:159], v[192:195], v[52:55]\n\t" "ds_read_b64_tr_b16 v[182:183], v5 offset:14848\n\t" "v_cvt_pk_bf16_f32 v32, v212, v213\n\t"
    "v_mfma_f32_16x16x32_bf16 v[152:155], v[228:231], v[156:159], v[152:155]\n\t" "v_cvt_pk_bf16_f32 v33, v214, v215\n\t" "v_cvt_pk_bf16_f32 v34, v216, v217\n\t" "v_cvt_pk_bf16_f32 v35, v218, v219\n\t"
    "s_mov_b64 exec, s[60:61]\n\t" "ds_write_b128 v19, v[28:31] offset:16384\n\t" "ds_write_b128 v19, v[32:35] offset:17408\n\t" "s_mov_b64 exec, -1\n\t"
    "v_mfma_f32_16x16x32_bf16 v[56:59], v[160:163], v[188:191], v[56:59]\n\t" "ds_read_b64_tr_b16 v[184:185], v6 offset:14336\n\t" "global_load_lds_dwordx4 v0, %[sb] offset:-2048\n\t"
    "v_mfma_f32_4x4x4_16b_bf16 v[204:207], v[224:225], a[56:57], 0\n\t" "v_mfma_f32_4x4x4_16b_bf16 v[208:211], v[226:227], a[58:59], 0\n\t"
    "v_mfma_f32_4x4x4_16b_bf16 v[212:215], v[224:225], a[60:61], 0\n\t" "v_mfma_f32_4x4x4_16b_bf16 v[216:219], v[226:227], a[62:63], 0\n\t" "v_permlane16_swap_b32 v239, v20\n\t"
    "v_mfma_f32_16x16x32_bf16 v[60:63], v[160:163], v[192:195], v[60:63]\n\t" "ds_read_b64_tr_b16 v[186:187], v6 offset:14848\n\t" "v_permlane16_swap_b32 v240, v21\n\t"
    "v_mfma_f32_16x16x32_bf16 v[64:67], v[164:167], v[188:191], v[64:67]\n\t" "ds_read_b64_tr_b16 v[156:157], v3 offset:16384\n\t" "v_add_f32 v239, v239, v20\n\t"
    "v_mfma_f32_16x16x32_bf16 v[68:71], v[164:167], v[192:195], v[68:71]\n\t" "ds_read_b64_tr_b16 v[158:159], v3 offset:16896\n\t" "v_cvt_pk_bf16_f32 v196, v204, v205\n\t"
    "v_mfma_f32_16x16x32_bf16 v[72:75], v[168:171], v[188:191], v[72:75]\n\t" "ds_read_b64_tr_b16 v[160:161], v4 offset:16384\n\t" "v_cvt_pk_bf16_f32 v197, v206, v207\n\t"
    "v_mfma_f32_16x16x32_bf16 v[76:79], v[168:171], v[192:195], v[76:79]\n\t" "ds_read_b64_tr_b16 v[162:163], v4 offset:16896\n\t" "s_waitcnt lgkmcnt(4)\n\t"
    "v_mfma_f32_16x16x32_bf16 v[80:83], v[172:175], v[188:191], v[80:83]\n\t" "ds_read_b64_tr_b16 v[164:165], v5 offset:16384\n\t" "v_cvt_pk_bf16_f32 v198, v208, v209\n\t"
    "v_mfma_f32_16x16x32_bf16 v[84:87], v[172:175], v[192:195], v[84:87]\n\t" "ds_read_b64_tr_b16 v[166:167], v5 offset:16896\n\t" "v_cvt_pk_bf16_f32 v199, v210, v211\n\t"
    "v_mfma_f32_16x16x32_bf16 v[144:147], v[172:175], v[228:231], v[144:147]\n\t" "ds_read_b128 v[220:223], v2 offset:2048\n\t" "global_load_lds_dwordx4 v0, %[sb] offset:0\n\t"
    "v_mfma_f32_16x16x32_bf16 v[88:91], v[176:179], v[188:191], v[88:91]\n\t" "ds_read_b64_tr_b16 v[168:169], v6 offset:16384\n\t" "v_cvt_pk_bf16_f32 v200, v212, v213\n\t"
    "v_mfma_f32_16x16x32_bf16 v[92:95], v[176:179], v[192:195], v[92:95]\n\t" "ds_read_b64_tr_b16 v[170:171], v6 offset:16896\n\t" "v_cvt_pk_bf16_f32 v201, v214, v215\n\t"
    "v_mfma_f32_16x16x32_bf16 v[96:99], v[180:183], v[188:191], v[96:99]\n\t" "ds_read_b64_tr_b16 v[172:173], v3 offset:24576\n\t" "v_add_f32 v240, v240, v21\n\t"
    "v_mfma_f32_16x16x32_bf16 v[100:103], v[180:183], v[192:195], v[100:103]\n\t" "ds_read_b64_tr_b16 v[174:175], v3 offset:25088\n\t" "v_cvt_pk_bf16_f32 v202, v216, v217\n\t"
    "v_mfma_f32_16x16x32_bf16 v[104:107], v[184:187], v[188:191], v[104:107]\n\t" "ds_read_b64_tr_b16 v[176:177], v4 offset:24576\n\t" "v_cvt_pk_bf16_f32 v203, v218, v219\n\t"
    "v_mfma_f32_16x16x32_bf16 v[108:111], v[184:187], v[192:195], v[108:111]\n\t" "ds_read_b64_tr_b16 v[178:179], v4 offset:25088\n\t" "s_waitcnt lgkmcnt(4)\n\t"
    "v_mfma_f32_16x16x32_bf16 v[112:115], v[188:191], v[156:159], v[112:115]\n\t" "ds_read_b64_tr_b16 v[180:181], v5 offset:24576\n\t" "v_mov_b32 v20, v239\n\t"
    "v_mfma_f32_16x16x32_bf16 v[116:119], v[192:195], v[156:159], v[116:119]\n\t" "ds_read_b64_tr_b16 v[182:183], v5 offset:25088\n\t" "v_mov_b32_e64 v21, v240\n\t"
    "v_mfma_f32_16x16x32_bf16 v[148:151], v[228:231], v[156:159], v[148:151]\n\t" "ds_read_b128 v[232:235], v1 offset:7168\n\t" "global_load_lds_dwordx4 v0, %[sb] offset:2048\n\t"
    "v_mfma_f32_16x16x32_bf16 v[120:123], v[188:191], v[160:163], v[120:123]\n\t" "ds_read_b64_tr_b16 v[184:185], v6 offset:24576\n\t" "v_add_u32 v0, 0x30000, v0\n\t"
    "v_mfma_f32_16x16x32_bf16 v[124:127], v[192:195], v[160:163], v[124:127]\n\t" "ds_read_b64_tr_b16 v[186:187], v6 offset:25088\n\t"
    "v_mfma_f32_16x16x32_bf16 v[128:131], v[188:191], v[164:167], v[128:131]\n\t" "ds_read_b64_tr_b16 v[156:157], v3 offset:26624\n\t"
    "v_mfma_f32_16x16x32_bf16 v[132:135], v[192:195], v[164:167], v[132:135]\n\t" "ds_read_b64_tr_b16 v[158:159], v3 offset:27136\n\t" "v_permlane32_swap_b32 v239, v20\n\t"
    "v_mfma_f32_16x16x32_bf16 v[136:139], v[188:191], v[168:171], v[136:139]\n\t" "ds_read_b64_tr_b16 v[160:161], v4 offset:26624\n\t" "v_permlane32_swap_b32 v240, v21\n\t"
    "v_mfma_f32_16x16x32_bf16 v[140:143], v[192:195], v[168:171], v[140:143]\n\t" "ds_read_b64_tr_b16 v[162:163], v4 offset:27136\n\t" "s_waitcnt lgkmcnt(4)\n\t"
    "v_mfma_f32_16x16x32_bf16 v[48:51], v[172:175], v[196:199], v[48:51]\n\t" "ds_read_b64_tr_b16 v[164:165], v5 offset:26624\n\t" "s_add_u32 m0, %[sl], 83968\n\t"
    "v_mfma_f32_16x16x32_bf16 v[52:55], v[172:175], v[200:203], v[52:55]\n\t" "ds_read_b64_tr_b16 v[166:167], v5 offset:27136\n\t" "ds_read_b128 v[44:47], v18 offset:6144\n\t"
    "v_mfma_f32_16x16x32_bf16 v[152:155], v[232:235], v[172:175], v[152:155]\n\t" "global_load_lds_dwordx4 v0, %[sb] offset:-2048\n\t" "ds_read_b128 v[242:245], v18 offset:6400\n\t"
    "v_mfma_f32_16x16x32_bf16 v[56:59], v[176:179], v[196:199], v[56:59]\n\t" "ds_read_b64_tr_b16 v[168:169], v6 offset:26624\n\t" "v_add_f32 v239, v239, v20\n\t"
    "v_mfma_f32_4x4x4_16b_bf16 v[204:207], v[220:221], a[64:65], 0\n\t" "v_mfma_f32_4x4x4_16b_bf16 v[208:211], v[222:223], a[66:67], 0\n\t"
    "v_mfma_f32_4x4x4_16b_bf16 v[212:215], v[220:221], a[68:69], 0\n\t" "v_mfma_f32_4x4x4_16b_bf16 v[216:219], v[222:223], a[70:71], 0\n\t" "v_add_f32_e64 v240, v240, v21\n\t"
    "v_mfma_f32_16x16x32_bf16 v[60:63], v[176:179], v[200:203], v[60:63]\n\t" "ds_read_b64_tr_b16 v[170:171], v6 offset:27136\n\t" "v_mul_f32 v236, 0x3a800000, v236\n\t"
    "v_mfma_f32_16x16x32_bf16 v[64:67], v[180:183], v[196:199], v[64:67]\n\t" "ds_read_b64_tr_b16 v[172:173], v3 offset:28672\n\t" "v_mul_f32 v237, 0x3a800000, v237\n\t"
    "v_mfma_f32_16x16x32_bf16 v[68:71], v[180:183], v[200:203], v[68:71]\n\t" "ds_read_b64_tr_b16 v[174:175], v3 offset:29184\n\t" "v_cvt_pk_bf16_f32 v188, v204, v205\n\t"
    "v_mfma_f32_16x16x32_bf16 v[72:75], v[184:187], v[196:199], v[72:75]\n\t" "ds_read_b64_tr_b16 v[176:177], v4 offset:28672\n\t" "v_cvt_pk_bf16_f32 v189, v206, v207\n\t"
    "v_mfma_f32_16x16x32_bf16 v[76:79], v[184:187], v[200:203], v[76:79]\n\t" "ds_read_b64_tr_b16 v[178:179], v4 offset:29184\n\t" "s_waitcnt lgkmcnt(4)\n\t"
    "v_mfma_f32_16x16x32_bf16 v[80:83], v[156:159], v[196:199], v[80:83]\n\t" "ds_read_b64_tr_b16 v[180:181], v5 offset:28672\n\t" "v_cvt_pk_bf16_f32 v190, v208, v209\n\t"
    "v_mfma_f32_16x16x32_bf16 v[84:87], v[156:159], v[200:203], v[84:87]\n\t" "ds_read_b64_tr_b16 v[182:183], v5 offset:29184\n\t" "v_cvt_pk_bf16_f32 v191, v210, v211\n\t"
    "v_mfma_f32_16x16x32_bf16 v[144:147], v[156:159], v[232:235], v[144:147]\n\t" "ds_read_b128 v[224:227], v2 offset:2304\n\t" "global_load_lds_dwordx4 v0, %[sb] offset:0\n\t"
    "v_mfma_f32_16x16x32_bf16 v[88:91], v[160:163], v[196:199], v[88:91]\n\t" "ds_read_b64_tr_b16 v[184:185], v6 offset:28672\n\t" "v_cvt_pk_bf16_f32 v192, v212, v213\n\t"
    "v_mfma_f32_16x16x32_bf16 v[92:95], v[160:163], v[200:203], v[92:95]\n\t" "ds_read_b64_tr_b16 v[186:187], v6 offset:29184\n\t" "v_cvt_pk_bf16_f32 v193, v214, v215\n\t"
    "v_mfma_f32_16x16x32_bf16 v[96:99], v[164:167], v[196:199], v[96:99]\n\t" "s_waitcnt vmcnt(12)\n\t" "s_barrier\n\t" "ds_read_b64_tr_b16 v[156:157], v3 offset:36864\n\t"
    "v_mfma_f32_16x16x32_bf16 v[100:103], v[164:167], v[200:203], v[100:103]\n\t" "ds_read_b64_tr_b16 v[158:159], v3 offset:37376\n\t" "v_cvt_pk_bf16_f32 v194, v216, v217\n\t"
    "v_mfma_f32_16x16x32_bf16 v[104:107], v[168:171], v[196:199], v[104:107]\n\t" "ds_read_b64_tr_b16 v[160:161], v4 offset:36864\n\t" "v_cvt_pk_bf16_f32 v195, v218, v219\n\t"
    "v_mfma_f32_16x16x32_bf16 v[108:111], v[168:171], v[200:203], v[108:111]\n\t" "ds_read_b64_tr_b16 v[162:163], v4 offset:37376\n\t" "v_mfma_f32_4x4x4_16b_bf16 v[204:207], v[44:45], v[36:37], 0\n\t"
    "v_mfma_f32_4x4x4_16b_bf16 v[208:211], v[46:47], v[38:39], 0\n\t" "v_mfma_f32_4x4x4_16b_bf16 v[212:215], v[242:243], v[40:41], 0\n\t"
    "v_mfma_f32_4x4x4_16b_bf16 v[216:219], v[244:245], v[42:43], 0\n\t" "s_waitcnt lgkmcnt(4)\n\t" "v_mfma_f32_16x16x32_bf16 v[112:115], v[196:199], v[172:175], v[112:115]\n\t"
    "ds_read_b64_tr_b16 v[164:165], v5 offset:36864\n\t" "v_mfma_f32_16x16x32_bf16 v[116:119], v[200:203], v[172:175], v[116:119]\n\t" "ds_read_b64_tr_b16 v[166:167], v5 offset:37376\n\t"
    "v_mfma_f32_16x16x32_bf16 v[148:151], v[232:235], v[172:175], v[148:151]\n\t" "ds_read_b128 v[228:231], v1 offset:8192\n\t" "global_load_lds_dwordx4 v0, %[sb] offset:2048\n\t"
    "v_mfma_f32_16x16x32_bf16 v[120:123], v[196:199], v[176:179], v[120:123]\n\t" "ds_read_b64_tr_b16 v[168:169], v6 offset:36864\n\t" "v_add_u32 v0, 0x30000, v0\n\t"
    "v_mfma_f32_16x16x32_bf16 v[124:127], v[200:203], v[176:179], v[124:127]\n\t" "ds_read_b64_tr_b16 v[170:171], v6 offset:37376\n\t" "v_add_u32 v16, 0x70000, v16\n\t"
    "v_mfma_f32_16x16x32_bf16 v[128:131], v[196:199], v[180:183], v[128:131]\n\t" "ds_read_b64_tr_b16 v[172:173], v3 offset:38912\n\t" "v_cvt_pk_bf16_f32 v36, v204, v205\n\t"
    "v_mfma_f32_16x16x32_bf16 v[132:135], v[200:203], v[180:183], v[132:135]\n\t" "ds_read_b64_tr_b16 v[174:175], v3 offset:39424\n\t" "s_mov_b64 exec, %[tm]\n\t" "global_load_dword v17, v16, %[pf]\n\t"
    "s_mov_b64 exec, -1\n\t" "v_mfma_f32_16x16x32_bf16 v[136:139], v[196:199], v[184:187], v[136:139]\n\t" "ds_read_b64_tr_b16 v[176:177], v4 offset:38912\n\t" "v_cvt_pk_bf16_f32 v37, v206, v207\n\t"
    "v_mfma_f32_16x16x32_bf16 v[140:143], v[200:203], v[184:187], v[140:143]\n\t" "ds_read_b64_tr_b16 v[178:179], v4 offset:39424\n\t" "s_waitcnt lgkmcnt(4)\n\t"
    "v_mfma_f32_16x16x32_bf16 v[48:51], v[156:159], v[188:191], v[48:51]\n\t" "ds_read_b64_tr_b16 v[180:181], v5 offset:38912\n\t" "s_add_u32 m0, %[sl], 22528\n\t"
    "v_mfma_f32_16x16x32_bf16 v[52:55], v[156:159], v[192:195], v[52:55]\n\t" "ds_read_b64_tr_b16 v[182:183], v5 offset:39424\n\t" "v_cvt_pk_bf16_f32 v38, v208, v209\n\t"
    "v_mfma_f32_16x16x32_bf16 v[152:155], v[228:231], v[156:159], v[152:155]\n\t" "v_cvt_pk_bf16_f32 v39, v210, v211\n\t" "v_cvt_pk_bf16_f32 v40, v212, v213\n\t" "v_cvt_pk_bf16_f32 v41, v214, v215\n\t"
    "v_cvt_pk_bf16_f32 v42, v216, v217\n\t" "v_cvt_pk_bf16_f32 v43, v218, v219\n\t" "s_mov_b64 exec, s[60:61]\n\t" "ds_write_b128 v19, v[36:39] offset:24576\n\t"
    "ds_write_b128 v19, v[40:43] offset:25600\n\t" "s_mov_b64 exec, -1\n\t" "v_mfma_f32_16x16x32_bf16 v[56:59], v[160:163], v[188:191], v[56:59]\n\t" "ds_read_b64_tr_b16 v[184:185], v6 offset:38912\n\t"
    "global_load_lds_dwordx4 v0, %[sb] offset:-2048\n\t" "v_mfma_f32_4x4x4_16b_bf16 v[204:207], v[224:225], a[72:73], 0\n\t" "v_mfma_f32_4x4x4_16b_bf16 v[208:211], v[226:227], a[74:75], 0\n\t"
    "v_mfma_f32_4x4x4_16b_bf16 v[212:215], v[224:225], a[76:77], 0\n\t" "v_mfma_f32_4x4x4_16b_bf16 v[216:219], v[226:227], a[78:79], 0\n\t" "v_mul_f32 v238, 0x3a800000, v238\n\t"
    "v_mfma_f32_16x16x32_bf16 v[60:63], v[160:163], v[192:195], v[60:63]\n\t" "ds_read_b64_tr_b16 v[186:187], v6 offset:39424\n\t" "v_mul_f32 v239, 0x3a800000, v239\n\t"
    "v_mfma_f32_16x16x32_bf16 v[64:67], v[164:167], v[188:191], v[64:67]\n\t" "ds_read_b64_tr_b16 v[156:157], v3 offset:40960\n\t" "global_load_lds_dwordx4 v0, %[sb] offset:0\n\t"
    "v_mfma_f32_16x16x32_bf16 v[68:71], v[164:167], v[192:195], v[68:71]\n\t" "ds_read_b64_tr_b16 v[158:159], v3 offset:41472\n\t" "v_cvt_pk_bf16_f32 v196, v204, v205\n\t"
    "v_mfma_f32_16x16x32_bf16 v[72:75], v[168:171], v[188:191], v[72:75]\n\t" "ds_read_b64_tr_b16 v[160:161], v4 offset:40960\n\t" "v_cvt_pk_bf16_f32 v197, v206, v207\n\t"
    "v_mfma_f32_16x16x32_bf16 v[76:79], v[168:171], v[192:195], v[76:79]\n\t" "ds_read_b64_tr_b16 v[162:163], v4 offset:41472\n\t" "s_waitcnt lgkmcnt(4)\n\t"
    "v_mfma_f32_16x16x32_bf16 v[80:83], v[172:175], v[188:191], v[80:83]\n\t" "ds_read_b64_tr_b16 v[164:165], v5 offset:40960\n\t" "v_cvt_pk_bf16_f32 v198, v208, v209\n\t"
    "v_mfma_f32_16x16x32_bf16 v[84:87], v[172:175], v[192:195], v[84:87]\n\t" "ds_read_b64_tr_b16 v[166:167], v5 offset:41472\n\t" "v_cvt_pk_bf16_f32 v199, v210, v211\n\t"
    "v_mfma_f32_16x16x32_bf16 v[144:147], v[172:175], v[228:231], v[144:147]\n\t" "ds_read_b128 v[220:223], v2 offset:2560\n\t" "global_load_lds_dwordx4 v0, %[sb] offset:2048\n\t"
    "v_mfma_f32_16x16x32_bf16 v[88:91], v[176:179], v[188:191], v[88:91]\n\t" "ds_read_b64_tr_b16 v[168:169], v6 offset:40960\n\t" "v_cvt_pk_bf16_f32 v200, v212, v213\n\t"
    "v_mfma_f32_16x16x32_bf16 v[92:95], v[176:179], v[192:195], v[92:95]\n\t" "ds_read_b64_tr_b16 v[170:171], v6 offset:41472\n\t" "v_cvt_pk_bf16_f32 v201, v214, v215\n\t"
    "v_mfma_f32_16x16x32_bf16 v[96:99], v[180:183], v[188:191], v[96:99]\n\t" "ds_read_b64_tr_b16 v[172:173], v7 offset:8192\n\t" "v_add_u32 v0, 0x30000, v0\n\t"
    "v_mfma_f32_16x16x32_bf16 v[100:103], v[180:183], v[192:195], v[100:103]\n\t" "ds_read_b64_tr_b16 v[174:175], v7 offset:8704\n\t" "v_cvt_pk_bf16_f32 v202, v216, v217\n\t"
    "v_mfma_f32_16x16x32_bf16 v[104:107], v[184:187], v[188:191], v[104:107]\n\t" "ds_read_b64_tr_b16 v[176:177], v8 offset:8192\n\t" "v_cvt_pk_bf16_f32 v203, v218, v219\n\t"
    "v_mfma_f32_16x16x32_bf16 v[108:111], v[184:187], v[192:195], v[108:111]\n\t" "ds_read_b64_tr_b16 v[178:179], v8 offset:8704\n\t" "s_waitcnt lgkmcnt(4)\n\t"
    "v_mfma_f32_16x16x32_bf16 v[112:115], v[188:191], v[156:159], v[112:115]\n\t" "ds_read_b64_tr_b16 v[180:181], v9 offset:8192\n\t" "v_mul_f32 v240, 0x3a800000, v240\n\t"
    "v_mfma_f32_16x16x32_bf16 v[116:119], v[192:195], v[156:159], v[116:119]\n\t" "ds_read_b64_tr_b16 v[182:183], v9 offset:8704\n\t" "v_mul_f32 v241, 0x3a800000, v241\n\t"
    "v_mfma_f32_16x16x32_bf16 v[148:151], v[228:231], v[156:159], v[148:151]\n\t" "ds_read_b128 v[232:235], v1 offset:9216\n\t" "global_load_dwordx4 a[32:35], v11, %[sm] nt\n\t"
    "v_mfma_f32_16x16x32_bf16 v[120:123], v[188:191], v[160:163], v[120:123]\n\t" "ds_read_b64_tr_b16 v[184:185], v10 offset:8192\n\t" "v_add_u32 v11, 0x3000, v11\n\t"
    "v_mfma_f32_16x16x32_bf16 v[124:127], v[192:195], v[160:163], v[124:127]\n\t" "ds_read_b64_tr_b16 v[186:187], v10 offset:8704\n\t" "v_fma_f32 v239, -v236, v236, v239\n\t"
    "v_mfma_f32_16x16x32_bf16 v[128:131], v[188:191], v[164:167], v[128:131]\n\t" "ds_read_b64_tr_b16 v[156:157], v7 offset:10240\n\t" "v_fma_f32 v240, -v237, v237, v240\n\t"
    "v_mfma_f32_16x16x32_bf16 v[132:135], v[192:195], v[164:167], v[132:135]\n\t" "ds_read_b64_tr_b16 v[158:159], v7 offset:10752\n\t" "global_load_dwordx4 a[36:39], v11, %[sm] nt\n\t"
    "v_mfma_f32_16x16x32_bf16 v[136:139], v[188:191], v[168:171], v[136:139]\n\t" "ds_read_b64_tr_b16 v[160:161], v8 offset:10240\n\t" "v_fma_f32 v241, -v238, v238, v241\n\t"
    "v_mfma_f32_16x16x32_bf16 v[140:143], v[192:195], v[168:171], v[140:143]\n\t" "ds_read_b64_tr_b16 v[162:163], v8 offset:10752\n\t" "s_waitcnt lgkmcnt(4)\n\t"
    "v_mfma_f32_16x16x32_bf16 v[48:51], v[172:175], v[196:199], v[48:51]\n\t" "ds_read_b64_tr_b16 v[164:165], v9 offset:10240\n\t" "s_add_u32 m0, %[sl], 34816\n\t"
    "v_mfma_f32_16x16x32_bf16 v[52:55], v[172:175], v[200:203], v[52:55]\n\t" "ds_read_b64_tr_b16 v[166:167], v9 offset:10752\n\t" "v_max_f32_e64 v239, 0, v239\n\t"
    "v_mfma_f32_16x16x32_bf16 v[152:155], v[232:235], v[172:175], v[152:155]\n\t" "global_load_lds_dwordx4 v0, %[sb] offset:-2048\n\t" "v_max_f32_e64 v240, 0, v240\n\t"
    "v_mfma_f32_16x16x32_bf16 v[56:59], v[176:179], v[196:199], v[56:59]\n\t" "ds_read_b64_tr_b16 v[168:169], v10 offset:10240\n\t" "v_max_f32_e64 v241, 0, v241\n\t"
    "v_mfma_f32_4x4x4_16b_bf16 v[204:207], v[220:221], a[80:81], 0\n\t" "v_mfma_f32_4x4x4_16b_bf16 v[208:211], v[222:223], a[82:83], 0\n\t"
    "v_mfma_f32_4x4x4_16b_bf16 v[212:215], v[220:221], a[84:85], 0\n\t" "v_mfma_f32_4x4x4_16b_bf16 v[216:219], v[222:223], a[86:87], 0\n\t" "v_add_f32 v239, 0x3727c5ac, v239\n\t"
    "v_mfma_f32_16x16x32_bf16 v[60:63], v[176:179], v[200:203], v[60:63]\n\t" "ds_read_b64_tr_b16 v[170:171], v10 offset:10752\n\t" "v_add_f32 v240, 0x3727c5ac, v240\n\t"
    "v_mfma_f32_16x16x32_bf16 v[64:67], v[180:183], v[196:199], v[64:67]\n\t" "ds_read_b64_tr_b16 v[172:173], v7 offset:12288\n\t" "global_load_lds_dwordx4 v0, %[sb] offset:0\n\t"
    "v_mfma_f32_16x16x32_bf16 v[68:71], v[180:183], v[200:203], v[68:71]\n\t" "ds_read_b64_tr_b16 v[174:175], v7 offset:12800\n\t" "v_cvt_pk_bf16_f32 v188, v204, v205\n\t"
    "v_mfma_f32_16x16x32_bf16 v[72:75], v[184:187], v[196:199], v[72:75]\n\t" "ds_read_b64_tr_b16 v[176:177], v8 offset:12288\n\t" "v_cvt_pk_bf16_f32 v189, v206, v207\n\t"
    "v_mfma_f32_16x16x32_bf16 v[76:79], v[184:187], v[200:203], v[76:79]\n\t" "ds_read_b64_tr_b16 v[178:179], v8 offset:12800\n\t" "s_waitcnt lgkmcnt(4)\n\t"
    "v_mfma_f32_16x16x32_bf16 v[80:83], v[156:159], v[196:199], v[80:83]\n\t" "ds_read_b64_tr_b16 v[180:181], v9 offset:12288\n\t" "v_cvt_pk_bf16_f32 v190, v208, v209\n\t"
    "v_mfma_f32_16x16x32_bf16 v[84:87], v[156:159], v[200:203], v[84:87]\n\t" "ds_read_b64_tr_b16 v[182:183], v9 offset:12800\n\t" "v_cvt_pk_bf16_f32 v191, v210, v211\n\t"
    "v_mfma_f32_16x16x32_bf16 v[144:147], v[156:159], v[232:235], v[144:147]\n\t" "ds_read_b128 v[224:227], v2 offset:2816\n\t" "global_load_lds_dwordx4 v0, %[sb] offset:2048\n\t"
    "v_mfma_f32_16x16x32_bf16 v[88:91], v[160:163], v[196:199], v[88:91]\n\t" "ds_read_b64_tr_b16 v[184:185], v10 offset:12288\n\t" "v_cvt_pk_bf16_f32 v192, v212, v213\n\t"
    "v_mfma_f32_16x16x32_bf16 v[92:95], v[160:163], v[200:203], v[92:95]\n\t" "ds_read_b64_tr_b16 v[186:187], v10 offset:12800\n\t" "v_cvt_pk_bf16_f32 v193, v214, v215\n\t"
    "v_mfma_f32_16x16x32_bf16 v[96:99], v[164:167], v[196:199], v[96:99]\n\t" "s_waitcnt vmcnt(15)\n\t" "s_barrier\n\t" "ds_read_b64_tr_b16 v[156:157], v7 offset:20480\n\t"
    "v_mfma_f32_16x16x32_bf16 v[100:103], v[164:167], v[200:203], v[100:103]\n\t" "ds_read_b64_tr_b16 v[158:159], v7 offset:20992\n\t" "v_cvt_pk_bf16_f32 v194, v216, v217\n\t"
    "v_mfma_f32_16x16x32_bf16 v[104:107], v[168:171], v[196:199], v[104:107]\n\t" "ds_read_b64_tr_b16 v[160:161], v8 offset:20480\n\t" "v_cvt_pk_bf16_f32 v195, v218, v219\n\t"
    "v_mfma_f32_16x16x32_bf16 v[108:111], v[168:171], v[200:203], v[108:111]\n\t" "ds_read_b64_tr_b16 v[162:163], v8 offset:20992\n\t" "s_waitcnt lgkmcnt(4)\n\t"
    "v_mfma_f32_16x16x32_bf16 v[112:115], v[196:199], v[172:175], v[112:115]\n\t" "ds_read_b64_tr_b16 v[164:165], v9 offset:20480\n\t" "s_cmp_eq_u32 %[rot], 4\n\t" "s_cselect_b32 s42, s43, 0x30000\n\t"
    "v_add_u32 v0, s42, v0\n\t" "v_mfma_f32_16x16x32_bf16 v[116:119], v[200:203], v[172:175], v[116:119]\n\t" "ds_read_b64_tr_b16 v[166:167], v9 offset:20992\n\t" "v_add_u32 v11, 0x3000, v11\n\t"
    "v_mfma_f32_16x16x32_bf16 v[148:151], v[232:235], v[172:175], v[148:151]\n\t" "ds_read_b128 v[228:231], v1 offset:10240\n\t" "global_load_dwordx4 a[40:43], v11, %[sm] nt\n\t"
    "v_mfma_f32_16x16x32_bf16 v[120:123], v[196:199], v[176:179], v[120:123]\n\t" "ds_read_b64_tr_b16 v[168:169], v10 offset:20480\n\t" "v_add_u32 v11, 0x3000, v11\n\t"
    "v_mfma_f32_16x16x32_bf16 v[124:127], v[200:203], v[176:179], v[124:127]\n\t" "ds_read_b64_tr_b16 v[170:171], v10 offset:20992\n\t" "v_add_f32 v241, 0x3727c5ac, v241\n\t"
    "v_mfma_f32_16x16x32_bf16 v[128:131], v[196:199], v[180:183], v[128:131]\n\t" "ds_read_b64_tr_b16 v[172:173], v7 offset:22528\n\t" "v_rsq_f32_e64 v20, v239\n\t"
    "v_mfma_f32_16x16x32_bf16 v[132:135], v[200:203], v[180:183], v[132:135]\n\t" "ds_read_b64_tr_b16 v[174:175], v7 offset:23040\n\t" "global_load_dwordx4 a[44:47], v11, %[sm] nt\n\t"
    "v_mfma_f32_16x16x32_bf16 v[136:139], v[196:199], v[184:187], v[136:139]\n\t" "ds_read_b64_tr_b16 v[176:177], v8 offset:22528\n\t" "v_rsq_f32_e64 v21, v240\n\t"
    "v_mfma_f32_16x16x32_bf16 v[140:143], v[200:203], v[184:187], v[140:143]\n\t" "ds_read_b64_tr_b16 v[178:179], v8 offset:23040\n\t" "s_waitcnt lgkmcnt(4)\n\t"
    "v_mfma_f32_16x16x32_bf16 v[48:51], v[156:159], v[188:191], v[48:51]\n\t" "ds_read_b64_tr_b16 v[180:181], v9 offset:22528\n\t" "s_add_u32 m0, %[sl], 47104\n\t"
    "v_mfma_f32_16x16x32_bf16 v[52:55], v[156:159], v[192:195], v[52:55]\n\t" "ds_read_b64_tr_b16 v[182:183], v9 offset:23040\n\t" "v_rsq_f32 v22, v241\n\t"
    "v_mfma_f32_16x16x32_bf16 v[152:155], v[228:231], v[156:159], v[152:155]\n\t" "global_load_lds_dwordx4 v0, %[sb] offset:-2048\n\t" "v_and_b32_e64 v26, 15, v13\n\t"
    "v_mfma_f32_16x16x32_bf16 v[56:59], v[160:163], v[188:191], v[56:59]\n\t" "ds_read_b64_tr_b16 v[184:185], v10 offset:22528\n\t" "v_lshl_add_u32 v26, v26, 2, %[l0]\n\t"
    "v_mfma_f32_4x4x4_16b_bf16 v[204:207], v[224:225], a[88:89], 0\n\t" "v_mfma_f32_4x4x4_16b_bf16 v[208:211], v[226:227], a[90:91], 0\n\t"
    "v_mfma_f32_4x4x4_16b_bf16 v[212:215], v[224:225], a[92:93], 0\n\t" "v_mfma_f32_4x4x4_16b_bf16 v[216:219], v[226:227], a[94:95], 0\n\t" "v_add_u32_e64 v27, %[w128], v26\n\t"
    "v_mfma_f32_16x16x32_bf16 v[60:63], v[160:163], v[192:195], v[60:63]\n\t" "ds_read_b64_tr_b16 v[186:187], v10 offset:23040\n\t" "v_mul_f32_e64 v239, v239, v20\n\t"
    "v_mfma_f32_16x16x32_bf16 v[64:67], v[164:167], v[188:191], v[64:67]\n\t" "ds_read_b64_tr_b16 v[156:157], v7 offset:24576\n\t" "global_load_lds_dwordx4 v0, %[sb] offset:0\n\t"
    "v_mfma_f32_16x16x32_bf16 v[68:71], v[164:167], v[192:195], v[68:71]\n\t" "ds_read_b64_tr_b16 v[158:159], v7 offset:25088\n\t" "v_cvt_pk_bf16_f32 v196, v204, v205\n\t"
    "v_mfma_f32_16x16x32_bf16 v[72:75], v[168:171], v[188:191], v[72:75]\n\t" "ds_read_b64_tr_b16 v[160:161], v8 offset:24576\n\t" "v_cvt_pk_bf16_f32 v197, v206, v207\n\t"
    "v_mfma_f32_16x16x32_bf16 v[76:79], v[168:171], v[192:195], v[76:79]\n\t" "ds_read_b64_tr_b16 v[162:163], v8 offset:25088\n\t" "s_waitcnt lgkmcnt(4)\n\t"
    "v_mfma_f32_16x16x32_bf16 v[80:83], v[172:175], v[188:191], v[80:83]\n\t" "ds_read_b64_tr_b16 v[164:165], v9 offset:24576\n\t" "v_cvt_pk_bf16_f32 v198, v208, v209\n\t"
    "v_mfma_f32_16x16x32_bf16 v[84:87], v[172:175], v[192:195], v[84:87]\n\t" "ds_read_b64_tr_b16 v[166:167], v9 offset:25088\n\t" "v_cvt_pk_bf16_f32 v199, v210, v211\n\t"
    "v_mfma_f32_16x16x32_bf16 v[144:147], v[172:175], v[228:231], v[144:147]\n\t" "ds_read_b128 v[220:223], v2 offset:3072\n\t" "global_load_lds_dwordx4 v0, %[sb] offset:2048\n\t"
    "v_mfma_f32_16x16x32_bf16 v[88:91], v[176:179], v[188:191], v[88:91]\n\t" "ds_read_b64_tr_b16 v[168:169], v10 offset:24576\n\t" "v_cvt_pk_bf16_f32 v200, v212, v213\n\t"
    "v_mfma_f32_16x16x32_bf16 v[92:95], v[176:179], v[192:195], v[92:95]\n\t" "ds_read_b64_tr_b16 v[170:171], v10 offset:25088\n\t" "v_cvt_pk_bf16_f32 v201, v214, v215\n\t"
    "v_mfma_f32_16x16x32_bf16 v[96:99], v[180:183], v[188:191], v[96:99]\n\t" "ds_read_b64_tr_b16 v[172:173], v7 offset:32768\n\t" "v_add_u32 v0, 0x30000, v0\n\t"
    "v_mfma_f32_16x16x32_bf16 v[100:103], v[180:183], v[192:195], v[100:103]\n\t" "ds_read_b64_tr_b16 v[174:175], v7 offset:33280\n\t" "v_cvt_pk_bf16_f32 v202, v216, v217\n\t"
    "v_mfma_f32_16x16x32_bf16 v[104:107], v[184:187], v[188:191], v[104:107]\n\t" "ds_read_b64_tr_b16 v[176:177], v8 offset:32768\n\t" "v_cvt_pk_bf16_f32 v203, v218, v219\n\t"
    "v_mfma_f32_16x16x32_bf16 v[108:111], v[184:187], v[192:195], v[108:111]\n\t" "ds_read_b64_tr_b16 v[178:179], v8 offset:33280\n\t" "s_waitcnt lgkmcnt(4)\n\t"
    "v_mfma_f32_16x16x32_bf16 v[112:115], v[188:191], v[156:159], v[112:115]\n\t" "ds_read_b64_tr_b16 v[180:181], v9 offset:32768\n\t" "v_add_u32 v11, 0x3000, v11\n\t"
    "v_mfma_f32_16x16x32_bf16 v[116:119], v[192:195], v[156:159], v[116:119]\n\t" "ds_read_b64_tr_b16 v[182:183], v9 offset:33280\n\t" "v_mul_f32_e64 v240, v240, v21\n\t"
    "v_mfma_f32_16x16x32_bf16 v[148:151], v[228:231], v[156:159], v[148:151]\n\t" "ds_read_b128 v[232:235], v1 offset:11264\n\t" "global_load_dwordx4 a[48:51], v11, %[sm] nt\n\t"
    "v_mfma_f32_16x16x32_bf16 v[120:123], v[188:191], v[160:163], v[120:123]\n\t" "ds_read_b64_tr_b16 v[184:185], v10 offset:32768\n\t" "v_add_u32 v11, 0x3000, v11\n\t"
    "v_mfma_f32_16x16x32_bf16 v[124:127], v[192:195], v[160:163], v[124:127]\n\t" "ds_read_b64_tr_b16 v[186:187], v10 offset:33280\n\t" "v_mul_f32_e64 v241, v241, v22\n\t"
    "v_mfma_f32_16x16x32_bf16 v[128:131], v[188:191], v[164:167], v[128:131]\n\t" "ds_read_b64_tr_b16 v[156:157], v7 offset:34816\n\t" "ds_write_b32 v27, v20 offset:17472\n\t"
    "v_mfma_f32_16x16x32_bf16 v[132:135], v[192:195], v[164:167], v[132:135]\n\t" "ds_read_b64_tr_b16 v[158:159], v7 offset:35328\n\t" "global_load_dwordx4 a[52:55], v11, %[sm] nt\n\t"
    "v_mfma_f32_16x16x32_bf16 v[136:139], v[188:191], v[168:171], v[136:139]\n\t" "ds_read_b64_tr_b16 v[160:161], v8 offset:34816\n\t" "ds_write_b32 v27, v21 offset:17536\n\t"
    "v_mfma_f32_16x16x32_bf16 v[140:143], v[192:195], v[168:171], v[140:143]\n\t" "ds_read_b64_tr_b16 v[162:163], v8 offset:35328\n\t" "s_waitcnt lgkmcnt(6)\n\t"
    "v_mfma_f32_16x16x32_bf16 v[48:51], v[172:175], v[196:199], v[48:51]\n\t" "ds_read_b64_tr_b16 v[164:165], v9 offset:34816\n\t" "s_add_u32 m0, %[sl], 59392\n\t"
    "v_mfma_f32_16x16x32_bf16 v[52:55], v[172:175], v[200:203], v[52:55]\n\t" "ds_read_b64_tr_b16 v[166:167], v9 offset:35328\n\t" "ds_write_b32 v26, v22 offset:17408\n\t"
    "v_mfma_f32_16x16x32_bf16 v[152:155], v[232:235], v[172:175], v[152:155]\n\t" "global_load_lds_dwordx4 v0, %[sb] offset:-2048\n\t" "s_mov_b64 s[46:47], 0xffff\n\t"
    "v_mfma_f32_16x16x32_bf16 v[56:59], v[176:179], v[196:199], v[56:59]\n\t" "ds_read_b64_tr_b16 v[168:169], v10 offset:34816\n\t" "s_mov_b32 s48, -1\n\t"
    "v_mfma_f32_4x4x4_16b_bf16 v[204:207], v[220:221], a[96:97], 0\n\t" "v_mfma_f32_4x4x4_16b_bf16 v[208:211], v[222:223], a[98:99], 0\n\t"
    "v_mfma_f32_4x4x4_16b_bf16 v[212:215], v[220:221], a[100:101], 0\n\t" "v_mfma_f32_4x4x4_16b_bf16 v[216:219], v[222:223], a[102:103], 0\n\t" "s_mov_b32 s49, 0\n\t"
    "v_mfma_f32_16x16x32_bf16 v[60:63], v[176:179], v[200:203], v[60:63]\n\t" "ds_read_b64_tr_b16 v[170:171], v10 offset:35328\n\t" "v_and_b32 v23, 0xffff0000, v236\n\t"
    "v_mfma_f32_16x16x32_bf16 v[64:67], v[180:183], v[196:199], v[64:67]\n\t" "ds_read_b64_tr_b16 v[172:173], v7 offset:36864\n\t" "global_load_lds_dwordx4 v0, %[sb] offset:0\n\t"
    "v_mfma_f32_16x16x32_bf16 v[68:71], v[180:183], v[200:203], v[68:71]\n\t" "ds_read_b64_tr_b16 v[174:175], v7 offset:37376\n\t" "v_cvt_pk_bf16_f32 v188, v204, v205\n\t"
    "v_mfma_f32_16x16x32_bf16 v[72:75], v[184:187], v[196:199], v[72:75]\n\t" "ds_read_b64_tr_b16 v[176:177], v8 offset:36864\n\t" "v_cvt_pk_bf16_f32 v189, v206, v207\n\t"
    "v_mfma_f32_16x16x32_bf16 v[76:79], v[184:187], v[200:203], v[76:79]\n\t" "ds_read_b64_tr_b16 v[178:179], v8 offset:37376\n\t" "s_waitcnt lgkmcnt(4)\n\t"
    "v_mfma_f32_16x16x32_bf16 v[80:83], v[156:159], v[196:199], v[80:83]\n\t" "ds_read_b64_tr_b16 v[180:181], v9 offset:36864\n\t" "v_cvt_pk_bf16_f32 v190, v208, v209\n\t"
    "v_mfma_f32_16x16x32_bf16 v[84:87], v[156:159], v[200:203], v[84:87]\n\t" "ds_read_b64_tr_b16 v[182:183], v9 offset:37376\n\t" "v_cvt_pk_bf16_f32 v191, v210, v211\n\t"
    "v_mfma_f32_16x16x32_bf16 v[144:147], v[156:159], v[232:235], v[144:147]\n\t" "ds_read_b128 v[224:227], v2 offset:3328\n\t" "global_load_lds_dwordx4 v0, %[sb] offset:2048\n\t"
    "v_mfma_f32_16x16x32_bf16 v[88:91], v[160:163], v[196:199], v[88:91]\n\t" "ds_read_b64_tr_b16 v[184:185], v10 offset:36864\n\t" "v_cvt_pk_bf16_f32 v192, v212, v213\n\t"
    "v_mfma_f32_16x16x32_bf16 v[92:95], v[160:163], v[200:203], v[92:95]\n\t" "ds_read_b64_tr_b16 v[186:187], v10 offset:37376\n\t" "v_cvt_pk_bf16_f32 v193, v214, v215\n\t"
    "v_mfma_f32_16x16x32_bf16 v[96:99], v[164:167], v[196:199], v[96:99]\n\t" "s_waitcnt vmcnt(19)\n\t" "s_barrier\n\t" "ds_read_b64_tr_b16 v[156:157], v3 offset:49152\n\t"
    "v_mfma_f32_16x16x32_bf16 v[100:103], v[164:167], v[200:203], v[100:103]\n\t" "ds_read_b64_tr_b16 v[158:159], v3 offset:49664\n\t" "v_cvt_pk_bf16_f32 v194, v216, v217\n\t"
    "v_mfma_f32_16x16x32_bf16 v[104:107], v[168:171], v[196:199], v[104:107]\n\t" "ds_read_b64_tr_b16 v[160:161], v4 offset:49152\n\t" "v_cvt_pk_bf16_f32 v195, v218, v219\n\t"
    "v_mfma_f32_16x16x32_bf16 v[108:111], v[168:171], v[200:203], v[108:111]\n\t" "ds_read_b64_tr_b16 v[162:163], v4 offset:49664\n\t" "s_waitcnt lgkmcnt(4)\n\t"
    "v_mfma_f32_16x16x32_bf16 v[112:115], v[196:199], v[172:175], v[112:115]\n\t" "ds_read_b64_tr_b16 v[164:165], v5 offset:49152\n\t" "v_add_u32 v0, 0x30000, v0\n\t"
    "v_mfma_f32_16x16x32_bf16 v[116:119], v[200:203], v[172:175], v[116:119]\n\t" "ds_read_b64_tr_b16 v[166:167], v5 offset:49664\n\t" "v_add_u32 v11, 0x3000, v11\n\t"
    "v_mfma_f32_16x16x32_bf16 v[148:151], v[232:235], v[172:175], v[148:151]\n\t" "ds_read_b128 v[228:231], v1 offset:12288\n\t" "global_load_dwordx4 a[56:59], v11, %[sm] nt\n\t"
    "v_mfma_f32_16x16x32_bf16 v[120:123], v[196:199], v[176:179], v[120:123]\n\t" "ds_read_b64_tr_b16 v[168:169], v6 offset:49152\n\t" "v_add_u32 v11, 0x3000, v11\n\t"
    "v_mfma_f32_16x16x32_bf16 v[124:127], v[200:203], v[176:179], v[124:127]\n\t" "ds_read_b64_tr_b16 v[170:171], v6 offset:49664\n\t" "v_sub_f32 v24, v236, v23\n\t"
    "v_mfma_f32_16x16x32_bf16 v[128:131], v[196:199], v[180:183], v[128:131]\n\t" "ds_read_b64_tr_b16 v[172:173], v3 offset:51200\n\t" "v_cndmask_b32 v23, v24, v23, s[46:47]\n\t"
    "v_mfma_f32_16x16x32_bf16 v[132:135], v[200:203], v[180:183], v[132:135]\n\t" "ds_read_b64_tr_b16 v[174:175], v3 offset:51712\n\t" "global_load_dwordx4 a[60:63], v11, %[sm] nt\n\t"
    "v_mfma_f32_16x16x32_bf16 v[136:139], v[196:199], v[184:187], v[136:139]\n\t" "ds_read_b64_tr_b16 v[176:177], v4 offset:51200\n\t" "v_cvt_pk_bf16_f32 v23, -v23, -v23\n\t"
    "v_mfma_f32_16x16x32_bf16 v[140:143], v[200:203], v[184:187], v[140:143]\n\t" "ds_read_b64_tr_b16 v[178:179], v4 offset:51712\n\t" "s_waitcnt lgkmcnt(4)\n\t"
    "v_mfma_f32_16x16x32_bf16 v[48:51], v[156:159], v[188:191], v[48:51]\n\t" "ds_read_b64_tr_b16 v[180:181], v5 offset:51200\n\t" "s_add_u32 m0, %[sl], 96256\n\t"
    "v_mfma_f32_16x16x32_bf16 v[52:55], v[156:159], v[192:195], v[52:55]\n\t" "ds_read_b64_tr_b16 v[182:183], v5 offset:51712\n\t" "v_cndmask_b32 v246, 0, v23, s[48:49]\n\t"
    "v_mfma_f32_16x16x32_bf16 v[152:155], v[228:231], v[156:159], v[152:155]\n\t" "global_load_lds_dwordx4 v0, %[sb] offset:-2048\n\t" "v_and_b32 v23, 0xffff0000, v239\n\t"
    "v_mfma_f32_16x16x32_bf16 v[56:59], v[160:163], v[188:191], v[56:59]\n\t" "ds_read_b64_tr_b16 v[184:185], v6 offset:51200\n\t" "v_sub_f32 v24, v239, v23\n\t"
    "v_mfma_f32_4x4x4_16b_bf16 v[204:207], v[224:225], a[104:105], 0\n\t" "v_mfma_f32_4x4x4_16b_bf16 v[208:211], v[226:227], a[106:107], 0\n\t"
    "v_mfma_f32_4x4x4_16b_bf16 v[212:215], v[224:225], a[108:109], 0\n\t" "v_mfma_f32_4x4x4_16b_bf16 v[216:219], v[226:227], a[110:111], 0\n\t" "v_cndmask_b32 v23, v24, v23, s[46:47]\n\t"
    "v_mfma_f32_16x16x32_bf16 v[60:63], v[160:163], v[192:195], v[60:63]\n\t" "ds_read_b64_tr_b16 v[186:187], v6 offset:51712\n\t" "v_cvt_pk_bf16_f32 v23, v23, v23\n\t"
    "v_mfma_f32_16x16x32_bf16 v[64:67], v[164:167], v[188:191], v[64:67]\n\t" "ds_read_b64_tr_b16 v[156:157], v3 offset:53248\n\t" "v_cndmask_b32 v247, 0, v23, s[48:49]\n\t"
    "v_mfma_f32_16x16x32_bf16 v[68:71], v[164:167], v[192:195], v[68:71]\n\t" "ds_read_b64_tr_b16 v[158:159], v3 offset:53760\n\t" "v_cvt_pk_bf16_f32 v196, v204, v205\n\t"
    "v_mfma_f32_16x16x32_bf16 v[72:75], v[168:171], v[188:191], v[72:75]\n\t" "ds_read_b64_tr_b16 v[160:161], v4 offset:53248\n\t" "v_cvt_pk_bf16_f32 v197, v206, v207\n\t"
    "v_mfma_f32_16x16x32_bf16 v[76:79], v[168:171], v[192:195], v[76:79]\n\t" "ds_read_b64_tr_b16 v[162:163], v4 offset:53760\n\t" "s_waitcnt lgkmcnt(4)\n\t"
    "v_mfma_f32_16x16x32_bf16 v[80:83], v[172:175], v[188:191], v[80:83]\n\t" "ds_read_b64_tr_b16 v[164:165], v5 offset:53248\n\t" "v_cvt_pk_bf16_f32 v198, v208, v209\n\t"
    "v_mfma_f32_16x16x32_bf16 v[84:87], v[172:175], v[192:195], v[84:87]\n\t" "ds_read_b64_tr_b16 v[166:167], v5 offset:53760\n\t" "v_cvt_pk_bf16_f32 v199, v210, v211\n\t"
    "v_mfma_f32_16x16x32_bf16 v[144:147], v[172:175], v[228:231], v[144:147]\n\t" "ds_read_b128 v[220:223], v2 offset:3584\n\t" "global_load_lds_dwordx4 v0, %[sb] offset:0\n\t"
    "v_mfma_f32_16x16x32_bf16 v[88:91], v[176:179], v[188:191], v[88:91]\n\t" "ds_read_b64_tr_b16 v[168:169], v6 offset:53248\n\t" "v_cvt_pk_bf16_f32 v200, v212, v213\n\t"
    "v_mfma_f32_16x16x32_bf16 v[92:95], v[176:179], v[192:195], v[92:95]\n\t" "ds_read_b64_tr_b16 v[170:171], v6 offset:53760\n\t" "v_cvt_pk_bf16_f32 v201, v214, v215\n\t"
    "v_mfma_f32_16x16x32_bf16 v[96:99], v[180:183], v[188:191], v[96:99]\n\t" "ds_read_b64_tr_b16 v[172:173], v3 offset:61440\n\t" "v_and_b32 v23, 0xffff0000, v237\n\t"
    "v_mfma_f32_16x16x32_bf16 v[100:103], v[180:183], v[192:195], v[100:103]\n\t" "ds_read_b64_tr_b16 v[174:175], v3 offset:61952\n\t" "v_cvt_pk_bf16_f32 v202, v216, v217\n\t"
    "v_mfma_f32_16x16x32_bf16 v[104:107], v[184:187], v[188:191], v[104:107]\n\t" "ds_read_b64_tr_b16 v[176:177], v4 offset:61440\n\t" "v_cvt_pk_bf16_f32 v203, v218, v219\n\t"
    "v_mfma_f32_16x16x32_bf16 v[108:111], v[184:187], v[192:195], v[108:111]\n\t" "ds_read_b64_tr_b16 v[178:179], v4 offset:61952\n\t" "s_waitcnt lgkmcnt(4)\n\t"
    "v_mfma_f32_16x16x32_bf16 v[112:115], v[188:191], v[156:159], v[112:115]\n\t" "ds_read_b64_tr_b16 v[180:181], v5 offset:61440\n\t" "v_sub_f32_e64 v24, v237, v23\n\t"
    "v_mfma_f32_16x16x32_bf16 v[116:119], v[192:195], v[156:159], v[116:119]\n\t" "ds_read_b64_tr_b16 v[182:183], v5 offset:61952\n\t" "v_cndmask_b32 v23, v24, v23, s[46:47]\n\t"
    "v_mfma_f32_16x16x32_bf16 v[148:151], v[228:231], v[156:159], v[148:151]\n\t" "ds_read_b128 v[232:235], v1 offset:13312\n\t" "global_load_lds_dwordx4 v0, %[sb] offset:2048\n\t"
    "v_mfma_f32_16x16x32_bf16 v[120:123], v[188:191], v[160:163], v[120:123]\n\t" "ds_read_b64_tr_b16 v[184:185], v6 offset:61440\n\t" "v_add_u32 v0, 0x30000, v0\n\t"
    "v_mfma_f32_16x16x32_bf16 v[124:127], v[192:195], v[160:163], v[124:127]\n\t" "ds_read_b64_tr_b16 v[186:187], v6 offset:61952\n\t" "v_cvt_pk_bf16_f32 v23, -v23, -v23\n\t"
    "v_mfma_f32_16x16x32_bf16 v[128:131], v[188:191], v[164:167], v[128:131]\n\t" "ds_read_b64_tr_b16 v[156:157], v3 offset:63488\n\t" "v_cndmask_b32 v248, 0, v23, s[48:49]\n\t"
    "v_mfma_f32_16x16x32_bf16 v[132:135], v[192:195], v[164:167], v[132:135]\n\t" "ds_read_b64_tr_b16 v[158:159], v3 offset:64000\n\t" "v_and_b32 v23, 0xffff0000, v240\n\t"
    "v_mfma_f32_16x16x32_bf16 v[136:139], v[188:191], v[168:171], v[136:139]\n\t" "ds_read_b64_tr_b16 v[160:161], v4 offset:63488\n\t" "v_sub_f32_e64 v24, v240, v23\n\t"
    "v_mfma_f32_16x16x32_bf16 v[140:143], v[192:195], v[168:171], v[140:143]\n\t" "ds_read_b64_tr_b16 v[162:163], v4 offset:64000\n\t" "s_waitcnt lgkmcnt(4)\n\t"
    "v_mfma_f32_16x16x32_bf16 v[48:51], v[172:175], v[196:199], v[48:51]\n\t" "ds_read_b64_tr_b16 v[164:165], v5 offset:63488\n\t" "s_add_u32 m0, %[sl], 108544\n\t"
    "v_mfma_f32_16x16x32_bf16 v[52:55], v[172:175], v[200:203], v[52:55]\n\t" "ds_read_b64_tr_b16 v[166:167], v5 offset:64000\n\t" "v_cndmask_b32 v23, v24, v23, s[46:47]\n\t"
    "v_mfma_f32_16x16x32_bf16 v[152:155], v[232:235], v[172:175], v[152:155]\n\t" "global_load_lds_dwordx4 v0, %[sb] offset:-2048\n\t" "v_cvt_pk_bf16_f32 v23, v23, v23\n\t"
    "v_mfma_f32_16x16x32_bf16 v[56:59], v[176:179], v[196:199], v[56:59]\n\t" "ds_read_b64_tr_b16 v[168:169], v6 offset:63488\n\t" "v_cndmask_b32 v249, 0, v23, s[48:49]\n\t"
    "v_mfma_f32_4x4x4_16b_bf16 v[204:207], v[220:221], a[112:113], 0\n\t" "v_mfma_f32_4x4x4_16b_bf16 v[208:211], v[222:223], a[114:115], 0\n\t"
    "v_mfma_f32_4x4x4_16b_bf16 v[212:215], v[220:221], a[116:117], 0\n\t" "v_mfma_f32_4x4x4_16b_bf16 v[216:219], v[222:223], a[118:119], 0\n\t" "v_and_b32 v23, 0xffff0000, v238\n\t"
    "v_mfma_f32_16x16x32_bf16 v[60:63], v[176:179], v[200:203], v[60:63]\n\t" "ds_read_b64_tr_b16 v[170:171], v6 offset:64000\n\t" "v_sub_f32 v24, v238, v23\n\t"
    "v_mfma_f32_16x16x32_bf16 v[64:67], v[180:183], v[196:199], v[64:67]\n\t" "ds_read_b64_tr_b16 v[172:173], v7 offset:0\n\t" "v_cndmask_b32 v23, v24, v23, s[46:47]\n\t"
    "v_mfma_f32_16x16x32_bf16 v[68:71], v[180:183], v[200:203], v[68:71]\n\t" "ds_read_b64_tr_b16 v[174:175], v7 offset:512\n\t" "v_cvt_pk_bf16_f32 v188, v204, v205\n\t"
    "v_mfma_f32_16x16x32_bf16 v[72:75], v[184:187], v[196:199], v[72:75]\n\t" "ds_read_b64_tr_b16 v[176:177], v8 offset:0\n\t" "v_cvt_pk_bf16_f32 v189, v206, v207\n\t"
    "v_mfma_f32_16x16x32_bf16 v[76:79], v[184:187], v[200:203], v[76:79]\n\t" "ds_read_b64_tr_b16 v[178:179], v8 offset:512\n\t" "s_waitcnt lgkmcnt(4)\n\t"
    "v_mfma_f32_16x16x32_bf16 v[80:83], v[156:159], v[196:199], v[80:83]\n\t" "ds_read_b64_tr_b16 v[180:181], v9 offset:0\n\t" "v_cvt_pk_bf16_f32 v190, v208, v209\n\t"
    "v_mfma_f32_16x16x32_bf16 v[84:87], v[156:159], v[200:203], v[84:87]\n\t" "ds_read_b64_tr_b16 v[182:183], v9 offset:512\n\t" "v_cvt_pk_bf16_f32 v191, v210, v211\n\t"
    "v_mfma_f32_16x16x32_bf16 v[144:147], v[156:159], v[232:235], v[144:147]\n\t" "ds_read_b128 v[224:227], v2 offset:3840\n\t" "global_load_lds_dwordx4 v0, %[sb] offset:0\n\t"
    "v_mfma_f32_16x16x32_bf16 v[88:91], v[160:163], v[196:199], v[88:91]\n\t" "ds_read_b64_tr_b16 v[184:185], v10 offset:0\n\t" "v_cvt_pk_bf16_f32 v192, v212, v213\n\t"
    "v_mfma_f32_16x16x32_bf16 v[92:95], v[160:163], v[200:203], v[92:95]\n\t" "ds_read_b64_tr_b16 v[186:187], v10 offset:512\n\t" "v_cvt_pk_bf16_f32 v193, v214, v215\n\t"
    "v_mfma_f32_16x16x32_bf16 v[96:99], v[164:167], v[196:199], v[96:99]\n\t" "s_waitcnt vmcnt(17)\n\t" "s_barrier\n\t" "ds_read_b64_tr_b16 v[156:157], v3 offset:0\n\t"
    "v_mfma_f32_16x16x32_bf16 v[100:103], v[164:167], v[200:203], v[100:103]\n\t" "ds_read_b64_tr_b16 v[158:159], v3 offset:512\n\t" "v_cvt_pk_bf16_f32 v194, v216, v217\n\t"
    "v_mfma_f32_16x16x32_bf16 v[104:107], v[168:171], v[196:199], v[104:107]\n\t" "ds_read_b64_tr_b16 v[160:161], v4 offset:0\n\t" "v_cvt_pk_bf16_f32 v195, v218, v219\n\t"
    "v_mfma_f32_16x16x32_bf16 v[108:111], v[168:171], v[200:203], v[108:111]\n\t" "ds_read_b64_tr_b16 v[162:163], v4 offset:512\n\t" "s_waitcnt lgkmcnt(4)\n\t"
    "v_mfma_f32_16x16x32_bf16 v[112:115], v[196:199], v[172:175], v[112:115]\n\t" "ds_read_b64_tr_b16 v[164:165], v5 offset:0\n\t" "v_cvt_pk_bf16_f32 v23, -v23, -v23\n\t"
    "v_mfma_f32_16x16x32_bf16 v[116:119], v[200:203], v[172:175], v[116:119]\n\t" "ds_read_b64_tr_b16 v[166:167], v5 offset:512\n\t" "v_cndmask_b32 v250, 0, v23, s[48:49]\n\t"
    "v_mfma_f32_16x16x32_bf16 v[148:151], v[232:235], v[172:175], v[148:151]\n\t" "ds_read_b128 v[228:231], v1 offset:14336\n\t" "global_load_lds_dwordx4 v0, %[sb] offset:2048\n\t"
    "v_mfma_f32_16x16x32_bf16 v[120:123], v[196:199], v[176:179], v[120:123]\n\t" "ds_read_b64_tr_b16 v[168:169], v6 offset:0\n\t" "s_cmp_eq_u32 %[rot], 3\n\t" "s_cselect_b32 s42, s43, 0x30000\n\t"
    "v_add_u32 v0, s42, v0\n\t" "v_mfma_f32_16x16x32_bf16 v[124:127], v[200:203], v[176:179], v[124:127]\n\t" "ds_read_b64_tr_b16 v[170:171], v6 offset:512\n\t" "v_add_u32 v16, 0x70000, v16\n\t"
    "v_mfma_f32_16x16x32_bf16 v[128:131], v[196:199], v[180:183], v[128:131]\n\t" "ds_read_b64_tr_b16 v[172:173], v3 offset:2048\n\t" "v_and_b32 v23, 0xffff0000, v241\n\t"
    "v_mfma_f32_16x16x32_bf16 v[132:135], v[200:203], v[180:183], v[132:135]\n\t" "ds_read_b64_tr_b16 v[174:175], v3 offset:2560\n\t" "s_mov_b64 exec, %[tm]\n\t" "global_load_dword a64, v16, %[pf]\n\t"
    "s_mov_b64 exec, -1\n\t" "v_mfma_f32_16x16x32_bf16 v[136:139], v[196:199], v[184:187], v[136:139]\n\t" "ds_read_b64_tr_b16 v[176:177], v4 offset:2048\n\t" "v_sub_f32_e64 v24, v241, v23\n\t"
    "v_mfma_f32_16x16x32_bf16 v[140:143], v[200:203], v[184:187], v[140:143]\n\t" "ds_read_b64_tr_b16 v[178:179], v4 offset:2560\n\t" "s_waitcnt lgkmcnt(4)\n\t"
    "v_mfma_f32_16x16x32_bf16 v[48:51], v[156:159], v[188:191], v[48:51]\n\t" "ds_read_b64_tr_b16 v[180:181], v5 offset:2048\n\t" "s_add_u32 m0, %[sl], 120832\n\t"
    "v_mfma_f32_16x16x32_bf16 v[52:55], v[156:159], v[192:195], v[52:55]\n\t" "ds_read_b64_tr_b16 v[182:183], v5 offset:2560\n\t" "v_cndmask_b32 v23, v24, v23, s[46:47]\n\t"
    "v_mfma_f32_16x16x32_bf16 v[152:155], v[228:231], v[156:159], v[152:155]\n\t" "global_load_lds_dwordx4 v0, %[sb] offset:-2048\n\t" "v_cvt_pk_bf16_f32 v23, v23, v23\n\t"
    "v_mfma_f32_16x16x32_bf16 v[56:59], v[160:163], v[188:191], v[56:59]\n\t" "ds_read_b64_tr_b16 v[184:185], v6 offset:2048\n\t" "v_cndmask_b32 v251, 0, v23, s[48:49]\n\t"
    "v_mfma_f32_4x4x4_16b_bf16 v[204:207], v[224:225], a[120:121], 0\n\t" "v_mfma_f32_4x4x4_16b_bf16 v[208:211], v[226:227], a[122:123], 0\n\t"
    "v_mfma_f32_4x4x4_16b_bf16 v[212:215], v[224:225], a[124:125], 0\n\t" "v_mfma_f32_4x4x4_16b_bf16 v[216:219], v[226:227], a[126:127], 0\n\t" "v_mul_f32 v240, 0x3e8293ee, v20\n\t"
    "v_mfma_f32_16x16x32_bf16 v[60:63], v[160:163], v[192:195], v[60:63]\n\t" "ds_read_b64_tr_b16 v[186:187], v6 offset:2560\n\t" "v_mul_f32 v241, 0x3e8293ee, v21\n\t"
    "v_mfma_f32_16x16x32_bf16 v[64:67], v[164:167], v[188:191], v[64:67]\n\t" "ds_read_b64_tr_b16 v[156:157], v3 offset:4096\n\t" "v_mfma_f32_16x16x32_bf16 v[68:71], v[164:167], v[192:195], v[68:71]\n\t"
    "ds_read_b64_tr_b16 v[158:159], v3 offset:4608\n\t" "v_cvt_pk_bf16_f32 v196, v204, v205\n\t" "v_mfma_f32_16x16x32_bf16 v[72:75], v[168:171], v[188:191], v[72:75]\n\t"
    "ds_read_b64_tr_b16 v[160:161], v4 offset:4096\n\t" "v_cvt_pk_bf16_f32 v197, v206, v207\n\t" "v_mfma_f32_16x16x32_bf16 v[76:79], v[168:171], v[192:195], v[76:79]\n\t"
    "ds_read_b64_tr_b16 v[162:163], v4 offset:4608\n\t" "s_waitcnt lgkmcnt(4)\n\t" "v_mfma_f32_16x16x32_bf16 v[80:83], v[172:175], v[188:191], v[80:83]\n\t"
    "ds_read_b64_tr_b16 v[164:165], v5 offset:4096\n\t" "v_cvt_pk_bf16_f32 v198, v208, v209\n\t" "v_mfma_f32_16x16x32_bf16 v[84:87], v[172:175], v[192:195], v[84:87]\n\t"
    "ds_read_b64_tr_b16 v[166:167], v5 offset:4608\n\t" "v_cvt_pk_bf16_f32 v199, v210, v211\n\t" "v_mfma_f32_16x16x32_bf16 v[144:147], v[172:175], v[228:231], v[144:147]\n\t"
    "ds_read_b128 v[220:223], v2 offset:4096\n\t" "global_load_lds_dwordx4 v0, %[sb] offset:0\n\t" "v_mfma_f32_16x16x32_bf16 v[88:91], v[176:179], v[188:191], v[88:91]\n\t"
    "ds_read_b64_tr_b16 v[168:169], v6 offset:4096\n\t" "v_cvt_pk_bf16_f32 v200, v212, v213\n\t" "v_mfma_f32_16x16x32_bf16 v[92:95], v[176:179], v[192:195], v[92:95]\n\t"
    "ds_read_b64_tr_b16 v[170:171], v6 offset:4608\n\t" "v_cvt_pk_bf16_f32 v201, v214, v215\n\t" "v_mfma_f32_16x16x32_bf16 v[96:99], v[180:183], v[188:191], v[96:99]\n\t"
    "ds_read_b64_tr_b16 v[172:173], v3 offset:12288\n\t" "v_mfma_f32_16x16x32_bf16 v[100:103], v[180:183], v[192:195], v[100:103]\n\t" "ds_read_b64_tr_b16 v[174:175], v3 offset:12800\n\t"
    "v_cvt_pk_bf16_f32 v202, v216, v217\n\t" "v_mfma_f32_16x16x32_bf16 v[104:107], v[184:187], v[188:191], v[104:107]\n\t" "ds_read_b64_tr_b16 v[176:177], v4 offset:12288\n\t"
    "v_cvt_pk_bf16_f32 v203, v218, v219\n\t" "v_mfma_f32_16x16x32_bf16 v[108:111], v[184:187], v[192:195], v[108:111]\n\t" "ds_read_b64_tr_b16 v[178:179], v4 offset:12800\n\t" "s_waitcnt lgkmcnt(4)\n\t"
    "v_mfma_f32_16x16x32_bf16 v[112:115], v[188:191], v[156:159], v[112:115]\n\t" "ds_read_b64_tr_b16 v[180:181], v5 offset:12288\n\t"
    "v_mfma_f32_16x16x32_bf16 v[116:119], v[192:195], v[156:159], v[116:119]\n\t" "ds_read_b64_tr_b16 v[182:183], v5 offset:12800\n\t"
    "v_mfma_f32_16x16x32_bf16 v[148:151], v[228:231], v[156:159], v[148:151]\n\t" "ds_read_b128 v[232:235], v1 offset:15360\n\t" "global_load_lds_dwordx4 v0, %[sb] offset:2048\n\t"
    "v_mfma_f32_16x16x32_bf16 v[120:123], v[188:191], v[160:163], v[120:123]\n\t" "ds_read_b64_tr_b16 v[184:185], v6 offset:12288\n\t" "v_add_u32 v0, 0x30000, v0\n\t"
    "v_mfma_f32_16x16x32_bf16 v[124:127], v[192:195], v[160:163], v[124:127]\n\t" "ds_read_b64_tr_b16 v[186:187], v6 offset:12800\n\t"
    "v_mfma_f32_16x16x32_bf16 v[128:131], v[188:191], v[164:167], v[128:131]\n\t" "ds_read_b64_tr_b16 v[156:157], v3 offset:14336\n\t"
    "v_mfma_f32_16x16x32_bf16 v[132:135], v[192:195], v[164:167], v[132:135]\n\t" "ds_read_b64_tr_b16 v[158:159], v3 offset:14848\n\t"
    "v_mfma_f32_16x16x32_bf16 v[136:139], v[188:191], v[168:171], v[136:139]\n\t" "ds_read_b64_tr_b16 v[160:161], v4 offset:14336\n\t"
    "v_mfma_f32_16x16x32_bf16 v[140:143], v[192:195], v[168:171], v[140:143]\n\t" "ds_read_b64_tr_b16 v[162:163], v4 offset:14848\n\t" "s_waitcnt lgkmcnt(4)\n\t"
    "v_mfma_f32_16x16x32_bf16 v[48:51], v[172:175], v[196:199], v[48:51]\n\t" "ds_read_b64_tr_b16 v[164:165], v5 offset:14336\n\t" "s_add_u32 m0, %[sl], 71680\n\t"
    "v_mfma_f32_16x16x32_bf16 v[52:55], v[172:175], v[200:203], v[52:55]\n\t" "ds_read_b64_tr_b16 v[166:167], v5 offset:14848\n\t"
    "v_mfma_f32_16x16x32_bf16 v[152:155], v[232:235], v[172:175], v[152:155]\n\t" "global_load_lds_dwordx4 v0, %[sb] offset:-2048\n\t"
    "v_mfma_f32_16x16x32_bf16 v[56:59], v[176:179], v[196:199], v[56:59]\n\t" "ds_read_b64_tr_b16 v[168:169], v6 offset:14336\n\t" "v_mfma_f32_4x4x4_16b_bf16 v[204:207], v[220:221], a[128:129], 0\n\t"
    "v_mfma_f32_4x4x4_16b_bf16 v[208:211], v[222:223], a[130:131], 0\n\t" "v_mfma_f32_4x4x4_16b_bf16 v[212:215], v[220:221], a[132:133], 0\n\t"
    "v_mfma_f32_4x4x4_16b_bf16 v[216:219], v[222:223], a[134:135], 0\n\t" "v_mfma_f32_16x16x32_bf16 v[60:63], v[176:179], v[200:203], v[60:63]\n\t" "ds_read_b64_tr_b16 v[170:171], v6 offset:14848\n\t"
    "v_mfma_f32_16x16x32_bf16 v[64:67], v[180:183], v[196:199], v[64:67]\n\t" "ds_read_b64_tr_b16 v[172:173], v3 offset:16384\n\t" "v_mfma_f32_16x16x32_bf16 v[68:71], v[180:183], v[200:203], v[68:71]\n\t"
    "ds_read_b64_tr_b16 v[174:175], v3 offset:16896\n\t" "v_cvt_pk_bf16_f32 v188, v204, v205\n\t" "v_mfma_f32_16x16x32_bf16 v[72:75], v[184:187], v[196:199], v[72:75]\n\t"
    "ds_read_b64_tr_b16 v[176:177], v4 offset:16384\n\t" "v_cvt_pk_bf16_f32 v189, v206, v207\n\t" "v_mfma_f32_16x16x32_bf16 v[76:79], v[184:187], v[200:203], v[76:79]\n\t"
    "ds_read_b64_tr_b16 v[178:179], v4 offset:16896\n\t" "s_waitcnt lgkmcnt(4)\n\t" "v_mfma_f32_16x16x32_bf16 v[80:83], v[156:159], v[196:199], v[80:83]\n\t"
    "ds_read_b64_tr_b16 v[180:181], v5 offset:16384\n\t" "v_cvt_pk_bf16_f32 v190, v208, v209\n\t" "v_mfma_f32_16x16x32_bf16 v[84:87], v[156:159], v[200:203], v[84:87]\n\t"
    "ds_read_b64_tr_b16 v[182:183], v5 offset:16896\n\t" "v_cvt_pk_bf16_f32 v191, v210, v211\n\t" "v_mfma_f32_16x16x32_bf16 v[144:147], v[156:159], v[232:235], v[144:147]\n\t"
    "ds_read_b128 v[224:227], v2 offset:4352\n\t" "global_load_lds_dwordx4 v0, %[sb] offset:0\n\t" "v_mfma_f32_16x16x32_bf16 v[88:91], v[160:163], v[196:199], v[88:91]\n\t"
    "ds_read_b64_tr_b16 v[184:185], v6 offset:16384\n\t" "v_cvt_pk_bf16_f32 v192, v212, v213\n\t" "v_mfma_f32_16x16x32_bf16 v[92:95], v[160:163], v[200:203], v[92:95]\n\t"
    "ds_read_b64_tr_b16 v[186:187], v6 offset:16896\n\t" "v_cvt_pk_bf16_f32 v193, v214, v215\n\t" "v_mfma_f32_16x16x32_bf16 v[96:99], v[164:167], v[196:199], v[96:99]\n\t" "s_waitcnt vmcnt(14)\n\t"
    "s_barrier\n\t" "ds_read_b64_tr_b16 v[156:157], v3 offset:24576\n\t" "v_mfma_f32_16x16x32_bf16 v[100:103], v[164:167], v[200:203], v[100:103]\n\t" "ds_read_b64_tr_b16 v[158:159], v3 offset:25088\n\t"
    "v_cvt_pk_bf16_f32 v194, v216, v217\n\t" "v_mfma_f32_16x16x32_bf16 v[104:107], v[168:171], v[196:199], v[104:107]\n\t" "ds_read_b64_tr_b16 v[160:161], v4 offset:24576\n\t"
    "v_cvt_pk_bf16_f32 v195, v218, v219\n\t" "v_mfma_f32_16x16x32_bf16 v[108:111], v[168:171], v[200:203], v[108:111]\n\t" "ds_read_b64_tr_b16 v[162:163], v4 offset:25088\n\t" "s_waitcnt lgkmcnt(4)\n\t"
    "v_mfma_f32_16x16x32_bf16 v[112:115], v[196:199], v[172:175], v[112:115]\n\t" "ds_read_b64_tr_b16 v[164:165], v5 offset:24576\n\t"
    "v_mfma_f32_16x16x32_bf16 v[116:119], v[200:203], v[172:175], v[116:119]\n\t" "ds_read_b64_tr_b16 v[166:167], v5 offset:25088\n\t"
    "v_mfma_f32_16x16x32_bf16 v[148:151], v[232:235], v[172:175], v[148:151]\n\t" "ds_read_b128 v[228:231], v1 offset:16384\n\t" "global_load_lds_dwordx4 v0, %[sb] offset:2048\n\t"
    "v_mfma_f32_16x16x32_bf16 v[120:123], v[196:199], v[176:179], v[120:123]\n\t" "ds_read_b64_tr_b16 v[168:169], v6 offset:24576\n\t" "v_add_u32 v0, 0x30000, v0\n\t"
    "v_mfma_f32_16x16x32_bf16 v[124:127], v[200:203], v[176:179], v[124:127]\n\t" "ds_read_b64_tr_b16 v[170:171], v6 offset:25088\n\t"
    "v_mfma_f32_16x16x32_bf16 v[128:131], v[196:199], v[180:183], v[128:131]\n\t" "ds_read_b64_tr_b16 v[172:173], v3 offset:26624\n\t"
    "v_mfma_f32_16x16x32_bf16 v[132:135], v[200:203], v[180:183], v[132:135]\n\t" "ds_read_b64_tr_b16 v[174:175], v3 offset:27136\n\t"
    "v_mfma_f32_16x16x32_bf16 v[136:139], v[196:199], v[184:187], v[136:139]\n\t" "ds_read_b64_tr_b16 v[176:177], v4 offset:26624\n\t"
    "v_mfma_f32_16x16x32_bf16 v[140:143], v[200:203], v[184:187], v[140:143]\n\t" "ds_read_b64_tr_b16 v[178:179], v4 offset:27136\n\t" "s_waitcnt lgkmcnt(4)\n\t"
    "v_mfma_f32_16x16x32_bf16 v[48:51], v[156:159], v[188:191], v[48:51]\n\t" "ds_read_b64_tr_b16 v[180:181], v5 offset:26624\n\t" "s_add_u32 m0, %[sl], 83968\n\t"
    "v_mfma_f32_16x16x32_bf16 v[52:55], v[156:159], v[192:195], v[52:55]\n\t" "ds_read_b64_tr_b16 v[182:183], v5 offset:27136\n\t"
    "v_mfma_f32_16x16x32_bf16 v[152:155], v[228:231], v[156:159], v[152:155]\n\t" "global_load_lds_dwordx4 v0, %[sb] offset:-2048\n\t"
    "v_mfma_f32_16x16x32_bf16 v[56:59], v[160:163], v[188:191], v[56:59]\n\t" "ds_read_b64_tr_b16 v[184:185], v6 offset:26624\n\t" "v_mfma_f32_4x4x4_16b_bf16 v[204:207], v[224:225], a[136:137], 0\n\t"
    "v_mfma_f32_4x4x4_16b_bf16 v[208:211], v[226:227], a[138:139], 0\n\t" "v_mfma_f32_4x4x4_16b_bf16 v[212:215], v[224:225], a[140:141], 0\n\t"
    "v_mfma_f32_4x4x4_16b_bf16 v[216:219], v[226:227], a[142:143], 0\n\t" "v_mfma_f32_16x16x32_bf16 v[60:63], v[160:163], v[192:195], v[60:63]\n\t" "ds_read_b64_tr_b16 v[186:187], v6 offset:27136\n\t"
    "v_mfma_f32_16x16x32_bf16 v[64:67], v[164:167], v[188:191], v[64:67]\n\t" "ds_read_b64_tr_b16 v[156:157], v3 offset:28672\n\t" "v_mfma_f32_16x16x32_bf16 v[68:71], v[164:167], v[192:195], v[68:71]\n\t"
    "ds_read_b64_tr_b16 v[158:159], v3 offset:29184\n\t" "v_cvt_pk_bf16_f32 v196, v204, v205\n\t" "v_mfma_f32_16x16x32_bf16 v[72:75], v[168:171], v[188:191], v[72:75]\n\t"
    "ds_read_b64_tr_b16 v[160:161], v4 offset:28672\n\t" "v_cvt_pk_bf16_f32 v197, v206, v207\n\t" "v_mfma_f32_16x16x32_bf16 v[76:79], v[168:171], v[192:195], v[76:79]\n\t"
    "ds_read_b64_tr_b16 v[162:163], v4 offset:29184\n\t" "s_waitcnt lgkmcnt(4)\n\t" "v_mfma_f32_16x16x32_bf16 v[80:83], v[172:175], v[188:191], v[80:83]\n\t"
    "ds_read_b64_tr_b16 v[164:165], v5 offset:28672\n\t" "v_cvt_pk_bf16_f32 v198, v208, v209\n\t" "v_mfma_f32_16x16x32_bf16 v[84:87], v[172:175], v[192:195], v[84:87]\n\t"
    "ds_read_b64_tr_b16 v[166:167], v5 offset:29184\n\t" "v_cvt_pk_bf16_f32 v199, v210, v211\n\t" "v_mfma_f32_16x16x32_bf16 v[144:147], v[172:175], v[228:231], v[144:147]\n\t"
    "ds_read_b128 v[220:223], v2 offset:4608\n\t" "global_load_lds_dwordx4 v0, %[sb] offset:0\n\t" "v_mfma_f32_16x16x32_bf16 v[88:91], v[176:179], v[188:191], v[88:91]\n\t"
    "ds_read_b64_tr_b16 v[168:169], v6 offset:28672\n\t" "v_cvt_pk_bf16_f32 v200, v212, v213\n\t" "v_mfma_f32_16x16x32_bf16 v[92:95], v[176:179], v[192:195], v[92:95]\n\t"
    "ds_read_b64_tr_b16 v[170:171], v6 offset:29184\n\t" "v_cvt_pk_bf16_f32 v201, v214, v215\n\t" "v_mfma_f32_16x16x32_bf16 v[96:99], v[180:183], v[188:191], v[96:99]\n\t"
    "ds_read_b64_tr_b16 v[172:173], v3 offset:36864\n\t" "v_mfma_f32_16x16x32_bf16 v[100:103], v[180:183], v[192:195], v[100:103]\n\t" "ds_read_b64_tr_b16 v[174:175], v3 offset:37376\n\t"
    "v_cvt_pk_bf16_f32 v202, v216, v217\n\t" "v_mfma_f32_16x16x32_bf16 v[104:107], v[184:187], v[188:191], v[104:107]\n\t" "ds_read_b64_tr_b16 v[176:177], v4 offset:36864\n\t"
    "v_cvt_pk_bf16_f32 v203, v218, v219\n\t" "v_mfma_f32_16x16x32_bf16 v[108:111], v[184:187], v[192:195], v[108:111]\n\t" "ds_read_b64_tr_b16 v[178:179], v4 offset:37376\n\t" "s_waitcnt lgkmcnt(4)\n\t"
    "v_mfma_f32_16x16x32_bf16 v[112:115], v[188:191], v[156:159], v[112:115]\n\t" "ds_read_b64_tr_b16 v[180:181], v5 offset:36864\n\t"
    "v_mfma_f32_16x16x32_bf16 v[116:119], v[192:195], v[156:159], v[116:119]\n\t" "ds_read_b64_tr_b16 v[182:183], v5 offset:37376\n\t"
    "v_mfma_f32_16x16x32_bf16 v[148:151], v[228:231], v[156:159], v[148:151]\n\t" "ds_read_b128 v[232:235], v1 offset:17408\n\t" "global_load_lds_dwordx4 v0, %[sb] offset:2048\n\t"
    "v_mfma_f32_16x16x32_bf16 v[120:123], v[188:191], v[160:163], v[120:123]\n\t" "ds_read_b64_tr_b16 v[184:185], v6 offset:36864\n\t" "v_add_u32 v0, 0x30000, v0\n\t"
    "v_mfma_f32_16x16x32_bf16 v[124:127], v[192:195], v[160:163], v[124:127]\n\t" "ds_read_b64_tr_b16 v[186:187], v6 offset:37376\n\t" "v_add_u32 v16, 0x70000, v16\n\t"
    "v_mfma_f32_16x16x32_bf16 v[128:131], v[188:191], v[164:167], v[128:131]\n\t" "ds_read_b64_tr_b16 v[156:157], v3 offset:38912\n\t"
    "v_mfma_f32_16x16x32_bf16 v[132:135], v[192:195], v[164:167], v[132:135]\n\t" "ds_read_b64_tr_b16 v[158:159], v3 offset:39424\n\t" "s_mov_b64 exec, %[tm]\n\t" "global_load_dword a64, v16, %[pf]\n\t"
    "s_mov_b64 exec, -1\n\t" "v_mfma_f32_16x16x32_bf16 v[136:139], v[188:191], v[168:171], v[136:139]\n\t" "ds_read_b64_tr_b16 v[160:161], v4 offset:38912\n\t"
    "v_mfma_f32_16x16x32_bf16 v[140:143], v[192:195], v[168:171], v[140:143]\n\t" "ds_read_b64_tr_b16 v[162:163], v4 offset:39424\n\t" "s_waitcnt lgkmcnt(4)\n\t"
    "v_mfma_f32_16x16x32_bf16 v[48:51], v[172:175], v[196:199], v[48:51]\n\t" "ds_read_b64_tr_b16 v[164:165], v5 offset:38912\n\t" "s_add_u32 m0, %[sl], 22528\n\t"
    "v_mfma_f32_16x16x32_bf16 v[52:55], v[172:175], v[200:203], v[52:55]\n\t" "ds_read_b64_tr_b16 v[166:167], v5 offset:39424\n\t"
    "v_mfma_f32_16x16x32_bf16 v[152:155], v[232:235], v[172:175], v[152:155]\n\t" "global_load_lds_dwordx4 v0, %[sb] offset:-2048\n\t"
    "v_mfma_f32_16x16x32_bf16 v[56:59], v[176:179], v[196:199], v[56:59]\n\t" "ds_read_b64_tr_b16 v[168:169], v6 offset:38912\n\t" "v_mfma_f32_4x4x4_16b_bf16 v[204:207], v[220:221], a[144:145], 0\n\t"
    "v_mfma_f32_4x4x4_16b_bf16 v[208:211], v[222:223], a[146:147], 0\n\t" "v_mfma_f32_4x4x4_16b_bf16 v[212:215], v[220:221], a[148:149], 0\n\t"
    "v_mfma_f32_4x4x4_16b_bf16 v[216:219], v[222:223], a[150:151], 0\n\t" "v_mfma_f32_16x16x32_bf16 v[60:63], v[176:179], v[200:203], v[60:63]\n\t" "ds_read_b64_tr_b16 v[170:171], v6 offset:39424\n\t"
    "v_mfma_f32_16x16x32_bf16 v[64:67], v[180:183], v[196:199], v[64:67]\n\t" "ds_read_b64_tr_b16 v[172:173], v3 offset:40960\n\t" "v_mfma_f32_16x16x32_bf16 v[68:71], v[180:183], v[200:203], v[68:71]\n\t"
    "ds_read_b64_tr_b16 v[174:175], v3 offset:41472\n\t" "v_cvt_pk_bf16_f32 v188, v204, v205\n\t" "v_mfma_f32_16x16x32_bf16 v[72:75], v[184:187], v[196:199], v[72:75]\n\t"
    "ds_read_b64_tr_b16 v[176:177], v4 offset:40960\n\t" "v_cvt_pk_bf16_f32 v189, v206, v207\n\t" "v_mfma_f32_16x16x32_bf16 v[76:79], v[184:187], v[200:203], v[76:79]\n\t"
    "ds_read_b64_tr_b16 v[178:179], v4 offset:41472\n\t" "s_waitcnt lgkmcnt(4)\n\t" "v_mfma_f32_16x16x32_bf16 v[80:83], v[156:159], v[196:199], v[80:83]\n\t"
    "ds_read_b64_tr_b16 v[180:181], v5 offset:40960\n\t" "v_cvt_pk_bf16_f32 v190, v208, v209\n\t" "v_mfma_f32_16x16x32_bf16 v[84:87], v[156:159], v[200:203], v[84:87]\n\t"
    "ds_read_b64_tr_b16 v[182:183], v5 offset:41472\n\t" "v_cvt_pk_bf16_f32 v191, v210, v211\n\t" "v_mfma_f32_16x16x32_bf16 v[144:147], v[156:159], v[232:235], v[144:147]\n\t"
    "ds_read_b128 v[224:227], v2 offset:4864\n\t" "global_load_lds_dwordx4 v0, %[sb] offset:0\n\t" "v_mfma_f32_16x16x32_bf16 v[88:91], v[160:163], v[196:199], v[88:91]\n\t"
    "ds_read_b64_tr_b16 v[184:185], v6 offset:40960\n\t" "v_cvt_pk_bf16_f32 v192, v212, v213\n\t" "v_mfma_f32_16x16x32_bf16 v[92:95], v[160:163], v[200:203], v[92:95]\n\t"
    "ds_read_b64_tr_b16 v[186:187], v6 offset:41472\n\t" "v_cvt_pk_bf16_f32 v193, v214, v215\n\t" "v_mfma_f32_16x16x32_bf16 v[96:99], v[164:167], v[196:199], v[96:99]\n\t" "s_waitcnt vmcnt(13)\n\t"
    "s_barrier\n\t" "ds_read_b64_tr_b16 v[156:157], v7 offset:8192\n\t" "v_mfma_f32_16x16x32_bf16 v[100:103], v[164:167], v[200:203], v[100:103]\n\t" "ds_read_b64_tr_b16 v[158:159], v7 offset:8704\n\t"
    "v_cvt_pk_bf16_f32 v194, v216, v217\n\t" "v_mfma_f32_16x16x32_bf16 v[104:107], v[168:171], v[196:199], v[104:107]\n\t" "ds_read_b64_tr_b16 v[160:161], v8 offset:8192\n\t"
    "v_cvt_pk_bf16_f32 v195, v218, v219\n\t" "v_mfma_f32_16x16x32_bf16 v[108:111], v[168:171], v[200:203], v[108:111]\n\t" "ds_read_b64_tr_b16 v[162:163], v8 offset:8704\n\t" "s_waitcnt lgkmcnt(4)\n\t"
    "v_mfma_f32_16x16x32_bf16 v[112:115], v[196:199], v[172:175], v[112:115]\n\t" "ds_read_b64_tr_b16 v[164:165], v9 offset:8192\n\t"
    "v_mfma_f32_16x16x32_bf16 v[116:119], v[200:203], v[172:175], v[116:119]\n\t" "ds_read_b64_tr_b16 v[166:167], v9 offset:8704\n\t"
    "v_mfma_f32_16x16x32_bf16 v[148:151], v[232:235], v[172:175], v[148:151]\n\t" "ds_read_b128 v[228:231], v1 offset:18432\n\t" "global_load_lds_dwordx4 v0, %[sb] offset:2048\n\t"
    "v_mfma_f32_16x16x32_bf16 v[120:123], v[196:199], v[176:179], v[120:123]\n\t" "ds_read_b64_tr_b16 v[168:169], v10 offset:8192\n\t" "s_cmp_eq_u32 %[rot], 2\n\t" "s_cselect_b32 s42, s43, 0x30000\n\t"
    "v_add_u32 v0, s42, v0\n\t" "v_mfma_f32_16x16x32_bf16 v[124:127], v[200:203], v[176:179], v[124:127]\n\t" "ds_read_b64_tr_b16 v[170:171], v10 offset:8704\n\t" "v_lshlrev_b32_e64 v17, 4, v13\n\t"
    "v_mfma_f32_16x16x32_bf16 v[128:131], v[196:199], v[180:183], v[128:131]\n\t" "ds_read_b64_tr_b16 v[172:173], v7 offset:10240\n\t"
    "v_mfma_f32_16x16x32_bf16 v[132:135], v[200:203], v[180:183], v[132:135]\n\t" "ds_read_b64_tr_b16 v[174:175], v7 offset:10752\n\t" "global_load_dword a64, v17, %[cd]\n\t"
    "v_mfma_f32_16x16x32_bf16 v[136:139], v[196:199], v[184:187], v[136:139]\n\t" "ds_read_b64_tr_b16 v[176:177], v8 offset:10240\n\t"
    "v_mfma_f32_16x16x32_bf16 v[140:143], v[200:203], v[184:187], v[140:143]\n\t" "ds_read_b64_tr_b16 v[178:179], v8 offset:10752\n\t" "s_waitcnt lgkmcnt(4)\n\t"
    "v_mfma_f32_16x16x32_bf16 v[48:51], v[156:159], v[188:191], v[48:51]\n\t" "ds_read_b64_tr_b16 v[180:181], v9 offset:10240\n\t" "s_add_u32 m0, %[sl], 34816\n\t"
    "v_mfma_f32_16x16x32_bf16 v[52:55], v[156:159], v[192:195], v[52:55]\n\t" "ds_read_b64_tr_b16 v[182:183], v9 offset:10752\n\t" "s_mov_b32 s50, 0xaaaaaaaa\n\t"
    "v_mfma_f32_16x16x32_bf16 v[152:155], v[228:231], v[156:159], v[152:155]\n\t" "global_load_lds_dwordx4 v0, %[sb] offset:-2048\n\t" "s_mov_b32 s51, s50\n\t"
    "v_mfma_f32_16x16x32_bf16 v[56:59], v[160:163], v[188:191], v[56:59]\n\t" "ds_read_b64_tr_b16 v[184:185], v10 offset:10240\n\t" "s_mov_b32 s52, 0xfff0ffff\n\t"
    "v_mfma_f32_4x4x4_16b_bf16 v[204:207], v[224:225], a[152:153], 0\n\t" "v_mfma_f32_4x4x4_16b_bf16 v[208:211], v[226:227], a[154:155], 0\n\t"
    "v_mfma_f32_4x4x4_16b_bf16 v[212:215], v[224:225], a[156:157], 0\n\t" "v_mfma_f32_4x4x4_16b_bf16 v[216:219], v[226:227], a[158:159], 0\n\t" "s_mov_b32 s53, 0xf000ff00\n\t"
    "v_mfma_f32_16x16x32_bf16 v[60:63], v[160:163], v[192:195], v[60:63]\n\t" "ds_read_b64_tr_b16 v[186:187], v10 offset:10752\n\t" "s_mov_b32 s54, 0xffc0fffc\n\t"
    "v_mfma_f32_16x16x32_bf16 v[64:67], v[164:167], v[188:191], v[64:67]\n\t" "ds_read_b64_tr_b16 v[156:157], v7 offset:12288\n\t" "s_mov_b32 s55, 0xc000fc00\n\t"
    "v_mfma_f32_16x16x32_bf16 v[68:71], v[164:167], v[192:195], v[68:71]\n\t" "ds_read_b64_tr_b16 v[158:159], v7 offset:12800\n\t" "v_cvt_pk_bf16_f32 v196, v204, v205\n\t"
    "v_mfma_f32_16x16x32_bf16 v[72:75], v[168:171], v[188:191], v[72:75]\n\t" "ds_read_b64_tr_b16 v[160:161], v8 offset:12288\n\t" "v_cvt_pk_bf16_f32 v197, v206, v207\n\t"
    "v_mfma_f32_16x16x32_bf16 v[76:79], v[168:171], v[192:195], v[76:79]\n\t" "ds_read_b64_tr_b16 v[162:163], v8 offset:12800\n\t" "s_waitcnt lgkmcnt(4)\n\t"
    "v_mfma_f32_16x16x32_bf16 v[80:83], v[172:175], v[188:191], v[80:83]\n\t" "ds_read_b64_tr_b16 v[164:165], v9 offset:12288\n\t" "v_cvt_pk_bf16_f32 v198, v208, v209\n\t"
    "v_mfma_f32_16x16x32_bf16 v[84:87], v[172:175], v[192:195], v[84:87]\n\t" "ds_read_b64_tr_b16 v[166:167], v9 offset:12800\n\t" "v_cvt_pk_bf16_f32 v199, v210, v211\n\t"
    "v_mfma_f32_16x16x32_bf16 v[144:147], v[172:175], v[228:231], v[144:147]\n\t" "ds_read_b128 v[220:223], v2 offset:5120\n\t" "global_load_lds_dwordx4 v0, %[sb] offset:0\n\t"
    "v_mfma_f32_16x16x32_bf16 v[88:91], v[176:179], v[188:191], v[88:91]\n\t" "ds_read_b64_tr_b16 v[168:169], v10 offset:12288\n\t" "v_cvt_pk_bf16_f32 v200, v212, v213\n\t"
    "v_mfma_f32_16x16x32_bf16 v[92:95], v[176:179], v[192:195], v[92:95]\n\t" "ds_read_b64_tr_b16 v[170:171], v10 offset:12800\n\t" "v_cvt_pk_bf16_f32 v201, v214, v215\n\t"
    "v_mfma_f32_16x16x32_bf16 v[96:99], v[180:183], v[188:191], v[96:99]\n\t" "ds_read_b64_tr_b16 v[172:173], v7 offset:20480\n\t" "s_cmp_eq_u32 %[rw], 0\n\t" "s_cselect_b64 s[58:59], -1, 0\n\t"
    "v_mfma_f32_16x16x32_bf16 v[100:103], v[180:183], v[192:195], v[100:103]\n\t" "ds_read_b64_tr_b16 v[174:175], v7 offset:20992\n\t" "v_cvt_pk_bf16_f32 v202, v216, v217\n\t"
    "v_mfma_f32_16x16x32_bf16 v[104:107], v[184:187], v[188:191], v[104:107]\n\t" "ds_read_b64_tr_b16 v[176:177], v8 offset:20480\n\t" "v_cvt_pk_bf16_f32 v203, v218, v219\n\t"
    "v_mfma_f32_16x16x32_bf16 v[108:111], v[184:187], v[192:195], v[108:111]\n\t" "ds_read_b64_tr_b16 v[178:179], v8 offset:20992\n\t" "s_waitcnt lgkmcnt(4)\n\t"
    "v_mfma_f32_16x16x32_bf16 v[112:115], v[188:191], v[156:159], v[112:115]\n\t" "ds_read_b64_tr_b16 v[180:181], v9 offset:20480\n\t" "s_andn2_b64 s[56:57], s[58:59], s[52:53]\n\t"
    "v_mfma_f32_16x16x32_bf16 v[116:119], v[192:195], v[156:159], v[116:119]\n\t" "ds_read_b64_tr_b16 v[182:183], v9 offset:20992\n\t" "s_andn2_b64 s[58:59], s[58:59], s[54:55]\n\t"
    "v_mfma_f32_16x16x32_bf16 v[148:151], v[228:231], v[156:159], v[148:151]\n\t" "ds_read_b128 v[232:235], v1 offset:19456\n\t" "global_load_lds_dwordx4 v0, %[sb] offset:2048\n\t"
    "v_mfma_f32_16x16x32_bf16 v[120:123], v[188:191], v[160:163], v[120:123]\n\t" "ds_read_b64_tr_b16 v[184:185], v10 offset:20480\n\t" "v_add_u32 v0, 0x30000, v0\n\t"
    "v_mfma_f32_16x16x32_bf16 v[124:127], v[192:195], v[160:163], v[124:127]\n\t" "ds_read_b64_tr_b16 v[186:187], v10 offset:20992\n\t" "s_mov_b32 s48, 0x00030003\n\t"
    "v_mfma_f32_16x16x32_bf16 v[128:131], v[188:191], v[164:167], v[128:131]\n\t" "ds_read_b64_tr_b16 v[156:157], v7 offset:22528\n\t" "s_mov_b32 s49, s48\n\t"
    "v_mfma_f32_16x16x32_bf16 v[132:135], v[192:195], v[164:167], v[132:135]\n\t" "ds_read_b64_tr_b16 v[158:159], v7 offset:23040\n\t" "v_and_b32 v46, 63, v13\n\t"
    "v_mfma_f32_16x16x32_bf16 v[136:139], v[188:191], v[168:171], v[136:139]\n\t" "ds_read_b64_tr_b16 v[160:161], v8 offset:22528\n\t" "v_and_b32_e64 v47, 15, v13\n\t"
    "v_mfma_f32_16x16x32_bf16 v[140:143], v[192:195], v[168:171], v[140:143]\n\t" "ds_read_b64_tr_b16 v[162:163], v8 offset:23040\n\t" "s_waitcnt lgkmcnt(4)\n\t"
    "v_mfma_f32_16x16x32_bf16 v[48:51], v[172:175], v[196:199], v[48:51]\n\t" "ds_read_b64_tr_b16 v[164:165], v9 offset:22528\n\t" "s_add_u32 m0, %[sl], 47104\n\t"
    "v_mfma_f32_16x16x32_bf16 v[52:55], v[172:175], v[200:203], v[52:55]\n\t" "ds_read_b64_tr_b16 v[166:167], v9 offset:23040\n\t" "v_and_b32 v45, 48, v13\n\t"
    "v_mfma_f32_16x16x32_bf16 v[152:155], v[232:235], v[172:175], v[152:155]\n\t" "global_load_lds_dwordx4 v0, %[sb] offset:-2048\n\t" "v_and_b32_e64 v44, 1, v13\n\t"
    "v_mfma_f32_16x16x32_bf16 v[56:59], v[176:179], v[196:199], v[56:59]\n\t" "ds_read_b64_tr_b16 v[168:169], v10 offset:22528\n\t" "v_lshl_add_u32 v44, v44, 2, v45\n\t"
    "v_mfma_f32_4x4x4_16b_bf16 v[204:207], v[220:221], a[160:161], 0\n\t" "v_mfma_f32_4x4x4_16b_bf16 v[208:211], v[222:223], a[162:163], 0\n\t"
    "v_mfma_f32_4x4x4_16b_bf16 v[212:215], v[220:221], a[164:165], 0\n\t" "v_mfma_f32_4x4x4_16b_bf16 v[216:219], v[222:223], a[166:167], 0\n\t" "v_add_u32_e64 v44, %[w128], v44\n\t"
    "v_mfma_f32_16x16x32_bf16 v[60:63], v[176:179], v[200:203], v[60:63]\n\t" "ds_read_b64_tr_b16 v[170:171], v10 offset:23040\n\t" "v_add_u32_e64 v44, %[l0], v44\n\t"
    "v_mfma_f32_16x16x32_bf16 v[64:67], v[180:183], v[196:199], v[64:67]\n\t" "ds_read_b64_tr_b16 v[172:173], v7 offset:24576\n\t" "v_add_u32_e64 v43, 64, v44\n\t"
    "v_mfma_f32_16x16x32_bf16 v[68:71], v[180:183], v[200:203], v[68:71]\n\t" "ds_read_b64_tr_b16 v[174:175], v7 offset:25088\n\t" "v_cvt_pk_bf16_f32 v188, v204, v205\n\t"
    "v_mfma_f32_16x16x32_bf16 v[72:75], v[184:187], v[196:199], v[72:75]\n\t" "ds_read_b64_tr_b16 v[176:177], v8 offset:24576\n\t" "v_cvt_pk_bf16_f32 v189, v206, v207\n\t"
    "v_mfma_f32_16x16x32_bf16 v[76:79], v[184:187], v[200:203], v[76:79]\n\t" "ds_read_b64_tr_b16 v[178:179], v8 offset:25088\n\t" "s_waitcnt lgkmcnt(4)\n\t"
    "v_mfma_f32_16x16x32_bf16 v[80:83], v[156:159], v[196:199], v[80:83]\n\t" "ds_read_b64_tr_b16 v[180:181], v9 offset:24576\n\t" "v_cvt_pk_bf16_f32 v190, v208, v209\n\t"
    "v_mfma_f32_16x16x32_bf16 v[84:87], v[156:159], v[200:203], v[84:87]\n\t" "ds_read_b64_tr_b16 v[182:183], v9 offset:25088\n\t" "v_cvt_pk_bf16_f32 v191, v210, v211\n\t"
    "v_mfma_f32_16x16x32_bf16 v[144:147], v[156:159], v[232:235], v[144:147]\n\t" "ds_read_b128 v[224:227], v2 offset:5376\n\t" "global_load_lds_dwordx4 v0, %[sb] offset:0\n\t"
    "v_mfma_f32_16x16x32_bf16 v[88:91], v[160:163], v[196:199], v[88:91]\n\t" "ds_read_b64_tr_b16 v[184:185], v10 offset:24576\n\t" "v_cvt_pk_bf16_f32 v192, v212, v213\n\t"
    "v_mfma_f32_16x16x32_bf16 v[92:95], v[160:163], v[200:203], v[92:95]\n\t" "ds_read_b64_tr_b16 v[186:187], v10 offset:25088\n\t" "v_cvt_pk_bf16_f32 v193, v214, v215\n\t"
    "v_mfma_f32_16x16x32_bf16 v[96:99], v[164:167], v[196:199], v[96:99]\n\t" "s_waitcnt vmcnt(13)\n\t" "s_barrier\n\t" "ds_read_b64_tr_b16 v[156:157], v7 offset:32768\n\t"
    "v_mfma_f32_16x16x32_bf16 v[100:103], v[164:167], v[200:203], v[100:103]\n\t" "ds_read_b64_tr_b16 v[158:159], v7 offset:33280\n\t" "v_cvt_pk_bf16_f32 v194, v216, v217\n\t"
    "v_mfma_f32_16x16x32_bf16 v[104:107], v[168:171], v[196:199], v[104:107]\n\t" "ds_read_b64_tr_b16 v[160:161], v8 offset:32768\n\t" "v_cvt_pk_bf16_f32 v195, v218, v219\n\t"
    "v_mfma_f32_16x16x32_bf16 v[108:111], v[168:171], v[200:203], v[108:111]\n\t" "ds_read_b64_tr_b16 v[162:163], v8 offset:33280\n\t" "s_waitcnt lgkmcnt(4)\n\t"
    "v_mfma_f32_16x16x32_bf16 v[112:115], v[196:199], v[172:175], v[112:115]\n\t" "ds_read_b64_tr_b16 v[164:165], v9 offset:32768\n\t" "v_cndmask_b32 v42, v44, v43, s[52:53]\n\t"
    "v_mfma_f32_16x16x32_bf16 v[116:119], v[200:203], v[172:175], v[116:119]\n\t" "ds_read_b64_tr_b16 v[166:167], v9 offset:33280\n\t" "v_cndmask_b32 v43, v44, v43, s[54:55]\n\t"
    "v_mfma_f32_16x16x32_bf16 v[148:151], v[232:235], v[172:175], v[148:151]\n\t" "ds_read_b128 v[228:231], v1 offset:20480\n\t" "global_load_lds_dwordx4 v0, %[sb] offset:2048\n\t"
    "v_mfma_f32_16x16x32_bf16 v[120:123], v[196:199], v[176:179], v[120:123]\n\t" "ds_read_b64_tr_b16 v[168:169], v10 offset:32768\n\t" "v_add_u32 v0, 0x30000, v0\n\t"
    "v_mfma_f32_16x16x32_bf16 v[124:127], v[200:203], v[176:179], v[124:127]\n\t" "ds_read_b64_tr_b16 v[170:171], v10 offset:33280\n\t" "v_add_u32 v16, 0x70000, v16\n\t"
    "v_mfma_f32_16x16x32_bf16 v[128:131], v[196:199], v[180:183], v[128:131]\n\t" "ds_read_b64_tr_b16 v[172:173], v7 offset:34816\n\t" "ds_read_b32 v24, v42 offset:17408\n\t"
    "v_mfma_f32_16x16x32_bf16 v[132:135], v[200:203], v[180:183], v[132:135]\n\t" "ds_read_b64_tr_b16 v[174:175], v7 offset:35328\n\t" "s_mov_b64 exec, %[tm]\n\t" "global_load_dword a64, v16, %[pf]\n\t"
    "s_mov_b64 exec, -1\n\t" "v_mfma_f32_16x16x32_bf16 v[136:139], v[196:199], v[184:187], v[136:139]\n\t" "ds_read_b64_tr_b16 v[176:177], v8 offset:34816\n\t" "ds_read_b32 v25, v43 offset:17416\n\t"
    "v_mfma_f32_16x16x32_bf16 v[140:143], v[200:203], v[184:187], v[140:143]\n\t" "ds_read_b64_tr_b16 v[178:179], v8 offset:35328\n\t" "s_waitcnt lgkmcnt(6)\n\t"
    "v_mfma_f32_16x16x32_bf16 v[48:51], v[156:159], v[188:191], v[48:51]\n\t" "ds_read_b64_tr_b16 v[180:181], v9 offset:34816\n\t" "s_add_u32 m0, %[sl], 59392\n\t"
    "v_mfma_f32_16x16x32_bf16 v[52:55], v[156:159], v[192:195], v[52:55]\n\t" "ds_read_b64_tr_b16 v[182:183], v9 offset:35328\n\t" "ds_read_b32 v26, v42 offset:17472\n\t"
    "v_mfma_f32_16x16x32_bf16 v[152:155], v[228:231], v[156:159], v[152:155]\n\t" "global_load_lds_dwordx4 v0, %[sb] offset:-2048\n\t" "ds_read_b32 v27, v43 offset:17480\n\t"
    "v_mfma_f32_16x16x32_bf16 v[56:59], v[160:163], v[188:191], v[56:59]\n\t" "ds_read_b64_tr_b16 v[184:185], v10 offset:34816\n\t" "v_mov_b32 v40, 0x0c0c0100\n\t"
    "v_mfma_f32_4x4x4_16b_bf16 v[204:207], v[224:225], a[168:169], 0\n\t" "v_mfma_f32_4x4x4_16b_bf16 v[208:211], v[226:227], a[170:171], 0\n\t"
    "v_mfma_f32_4x4x4_16b_bf16 v[212:215], v[224:225], a[172:173], 0\n\t" "v_mfma_f32_4x4x4_16b_bf16 v[216:219], v[226:227], a[174:175], 0\n\t" "v_mov_b32 v41, 0x01000c0c\n\t"
    "v_mfma_f32_16x16x32_bf16 v[60:63], v[160:163], v[192:195], v[60:63]\n\t" "ds_read_b64_tr_b16 v[186:187], v10 offset:35328\n\t" "v_cndmask_b32 v40, v40, v41, s[50:51]\n\t"
    "v_mfma_f32_16x16x32_bf16 v[64:67], v[164:167], v[188:191], v[64:67]\n\t" "ds_read_b64_tr_b16 v[156:157], v7 offset:36864\n\t" "v_mov_b32 v39, 0x0c0c0302\n\t"
    "v_mfma_f32_16x16x32_bf16 v[68:71], v[164:167], v[192:195], v[68:71]\n\t" "ds_read_b64_tr_b16 v[158:159], v7 offset:37376\n\t" "v_cvt_pk_bf16_f32 v196, v204, v205\n\t"
    "v_mfma_f32_16x16x32_bf16 v[72:75], v[168:171], v[188:191], v[72:75]\n\t" "ds_read_b64_tr_b16 v[160:161], v8 offset:36864\n\t" "v_cvt_pk_bf16_f32 v197, v206, v207\n\t"
    "v_mfma_f32_16x16x32_bf16 v[76:79], v[168:171], v[192:195], v[76:79]\n\t" "s_waitcnt lgkmcnt(11)\n\t" "ds_read_b64_tr_b16 v[162:163], v8 offset:37376\n\t" "s_waitcnt lgkmcnt(4)\n\t"
    "v_mfma_f32_16x16x32_bf16 v[80:83], v[172:175], v[188:191], v[80:83]\n\t" "ds_read_b64_tr_b16 v[164:165], v9 offset:36864\n\t" "v_cvt_pk_bf16_f32 v198, v208, v209\n\t"
    "v_mfma_f32_16x16x32_bf16 v[84:87], v[172:175], v[192:195], v[84:87]\n\t" "ds_read_b64_tr_b16 v[166:167], v9 offset:37376\n\t" "v_cvt_pk_bf16_f32 v199, v210, v211\n\t"
    "v_mfma_f32_16x16x32_bf16 v[144:147], v[172:175], v[228:231], v[144:147]\n\t" "ds_read_b128 v[220:223], v2 offset:5632\n\t" "global_load_lds_dwordx4 v0, %[sb] offset:0\n\t"
    "v_mfma_f32_16x16x32_bf16 v[88:91], v[176:179], v[188:191], v[88:91]\n\t" "ds_read_b64_tr_b16 v[168:169], v10 offset:36864\n\t" "v_cvt_pk_bf16_f32 v200, v212, v213\n\t"
    "v_mfma_f32_16x16x32_bf16 v[92:95], v[176:179], v[192:195], v[92:95]\n\t" "ds_read_b64_tr_b16 v[170:171], v10 offset:37376\n\t" "v_cvt_pk_bf16_f32 v201, v214, v215\n\t"
    "v_mfma_f32_16x16x32_bf16 v[96:99], v[180:183], v[188:191], v[96:99]\n\t" "ds_read_b64_tr_b16 v[172:173], v3 offset:49152\n\t" "v_mov_b32 v41, 0x03020c0c\n\t"
    "v_mfma_f32_16x16x32_bf16 v[100:103], v[180:183], v[192:195], v[100:103]\n\t" "ds_read_b64_tr_b16 v[174:175], v3 offset:49664\n\t" "v_cvt_pk_bf16_f32 v202, v216, v217\n\t"
    "v_mfma_f32_16x16x32_bf16 v[104:107], v[184:187], v[188:191], v[104:107]\n\t" "ds_read_b64_tr_b16 v[176:177], v4 offset:49152\n\t" "v_cvt_pk_bf16_f32 v203, v218, v219\n\t"
    "v_mfma_f32_16x16x32_bf16 v[108:111], v[184:187], v[192:195], v[108:111]\n\t" "ds_read_b64_tr_b16 v[178:179], v4 offset:49664\n\t" "s_waitcnt lgkmcnt(4)\n\t"
    "v_mfma_f32_16x16x32_bf16 v[112:115], v[188:191], v[156:159], v[112:115]\n\t" "ds_read_b64_tr_b16 v[180:181], v5 offset:49152\n\t" "v_cndmask_b32 v41, v39, v41, s[50:51]\n\t"
    "v_mfma_f32_16x16x32_bf16 v[116:119], v[192:195], v[156:159], v[116:119]\n\t" "ds_read_b64_tr_b16 v[182:183], v5 offset:49664\n\t" "v_mov_b32 v39, 0x0c0c0c0c\n\t"
    "v_mfma_f32_16x16x32_bf16 v[148:151], v[228:231], v[156:159], v[148:151]\n\t" "ds_read_b128 v[232:235], v1 offset:21504\n\t" "global_load_lds_dwordx4 v0, %[sb] offset:2048\n\t"
    "v_mfma_f32_16x16x32_bf16 v[120:123], v[188:191], v[160:163], v[120:123]\n\t" "ds_read_b64_tr_b16 v[184:185], v6 offset:49152\n\t" "v_add_u32 v0, 0x30000, v0\n\t"
    "v_mfma_f32_16x16x32_bf16 v[124:127], v[192:195], v[160:163], v[124:127]\n\t" "ds_read_b64_tr_b16 v[186:187], v6 offset:49664\n\t" "s_add_u32 m0, %[sl1], 0\n\t"
    "v_mfma_f32_16x16x32_bf16 v[128:131], v[188:191], v[164:167], v[128:131]\n\t" "ds_read_b64_tr_b16 v[156:157], v3 offset:51200\n\t" "v_cndmask_b32 v20, v40, v39, s[52:53]\n\t"
    "v_mfma_f32_16x16x32_bf16 v[132:135], v[192:195], v[164:167], v[132:135]\n\t" "ds_read_b64_tr_b16 v[158:159], v3 offset:51712\n\t" "global_load_lds_dwordx4 v12, %[so]\n\t"
    "v_mfma_f32_16x16x32_bf16 v[136:139], v[188:191], v[168:171], v[136:139]\n\t" "ds_read_b64_tr_b16 v[160:161], v4 offset:51200\n\t" "v_cndmask_b32 v21, v41, v39, s[54:55]\n\t"
    "v_mfma_f32_16x16x32_bf16 v[140:143], v[192:195], v[168:171], v[140:143]\n\t" "ds_read_b64_tr_b16 v[162:163], v4 offset:51712\n\t" "s_waitcnt lgkmcnt(4)\n\t"
    "v_mfma_f32_16x16x32_bf16 v[48:51], v[172:175], v[196:199], v[48:51]\n\t" "ds_read_b64_tr_b16 v[164:165], v5 offset:51200\n\t" "s_add_u32 m0, %[sl], 96256\n\t"
    "v_mfma_f32_16x16x32_bf16 v[52:55], v[172:175], v[200:203], v[52:55]\n\t" "ds_read_b64_tr_b16 v[166:167], v5 offset:51712\n\t" "v_cndmask_b32 v22, v39, v40, s[52:53]\n\t"
    "v_mfma_f32_16x16x32_bf16 v[152:155], v[232:235], v[172:175], v[152:155]\n\t" "global_load_lds_dwordx4 v0, %[sb] offset:-2048\n\t" "v_cndmask_b32 v23, v39, v41, s[54:55]\n\t"
    "v_mfma_f32_16x16x32_bf16 v[56:59], v[176:179], v[196:199], v[56:59]\n\t" "ds_read_b64_tr_b16 v[168:169], v6 offset:51200\n\t" "s_mov_b32 s60, 0xcccccccc\n\t"
    "v_mfma_f32_4x4x4_16b_bf16 v[204:207], v[220:221], a[176:177], 0\n\t" "v_mfma_f32_4x4x4_16b_bf16 v[208:211], v[222:223], a[178:179], 0\n\t"
    "v_mfma_f32_4x4x4_16b_bf16 v[212:215], v[220:221], a[180:181], 0\n\t" "v_mfma_f32_4x4x4_16b_bf16 v[216:219], v[222:223], a[182:183], 0\n\t" "s_mov_b32 s61, s60\n\t"
    "v_mfma_f32_16x16x32_bf16 v[60:63], v[176:179], v[200:203], v[60:63]\n\t" "ds_read_b64_tr_b16 v[170:171], v6 offset:51712\n\t" "v_cndmask_b32 v14, v40, v39, s[60:61]\n\t"
    "v_mfma_f32_16x16x32_bf16 v[64:67], v[180:183], v[196:199], v[64:67]\n\t" "ds_read_b64_tr_b16 v[172:173], v3 offset:53248\n\t" "v_cndmask_b32 v15, v41, v39, s[60:61]\n\t"
    "v_mfma_f32_16x16x32_bf16 v[68:71], v[180:183], v[200:203], v[68:71]\n\t" "ds_read_b64_tr_b16 v[174:175], v3 offset:53760\n\t" "v_cvt_pk_bf16_f32 v188, v204, v205\n\t"
    "v_mfma_f32_16x16x32_bf16 v[72:75], v[184:187], v[196:199], v[72:75]\n\t" "ds_read_b64_tr_b16 v[176:177], v4 offset:53248\n\t" "v_cvt_pk_bf16_f32 v189, v206, v207\n\t"
    "v_mfma_f32_16x16x32_bf16 v[76:79], v[184:187], v[200:203], v[76:79]\n\t" "ds_read_b64_tr_b16 v[178:179], v4 offset:53760\n\t" "s_waitcnt lgkmcnt(4)\n\t"
    "v_mfma_f32_16x16x32_bf16 v[80:83], v[156:159], v[196:199], v[80:83]\n\t" "ds_read_b64_tr_b16 v[180:181], v5 offset:53248\n\t" "v_cvt_pk_bf16_f32 v190, v208, v209\n\t"
    "v_mfma_f32_16x16x32_bf16 v[84:87], v[156:159], v[200:203], v[84:87]\n\t" "ds_read_b64_tr_b16 v[182:183], v5 offset:53760\n\t" "v_cvt_pk_bf16_f32 v191, v210, v211\n\t"
    "v_mfma_f32_16x16x32_bf16 v[144:147], v[156:159], v[232:235], v[144:147]\n\t" "ds_read_b128 v[224:227], v2 offset:5888\n\t" "global_load_lds_dwordx4 v0, %[sb] offset:0\n\t"
    "v_mfma_f32_16x16x32_bf16 v[88:91], v[160:163], v[196:199], v[88:91]\n\t" "ds_read_b64_tr_b16 v[184:185], v6 offset:53248\n\t" "v_cvt_pk_bf16_f32 v192, v212, v213\n\t"
    "v_mfma_f32_16x16x32_bf16 v[92:95], v[160:163], v[200:203], v[92:95]\n\t" "ds_read_b64_tr_b16 v[186:187], v6 offset:53760\n\t" "v_cvt_pk_bf16_f32 v193, v214, v215\n\t"
    "v_mfma_f32_16x16x32_bf16 v[96:99], v[164:167], v[196:199], v[96:99]\n\t" "s_waitcnt vmcnt(14)\n\t" "s_barrier\n\t" "ds_read_b64_tr_b16 v[156:157], v3 offset:61440\n\t"
    "v_mfma_f32_16x16x32_bf16 v[100:103], v[164:167], v[200:203], v[100:103]\n\t" "ds_read_b64_tr_b16 v[158:159], v3 offset:61952\n\t" "v_cvt_pk_bf16_f32 v194, v216, v217\n\t"
    "v_mfma_f32_16x16x32_bf16 v[104:107], v[168:171], v[196:199], v[104:107]\n\t" "ds_read_b64_tr_b16 v[160:161], v4 offset:61440\n\t" "v_cvt_pk_bf16_f32 v195, v218, v219\n\t"
    "v_mfma_f32_16x16x32_bf16 v[108:111], v[168:171], v[200:203], v[108:111]\n\t" "ds_read_b64_tr_b16 v[162:163], v4 offset:61952\n\t" "s_waitcnt lgkmcnt(4)\n\t"
    "v_mfma_f32_16x16x32_bf16 v[112:115], v[196:199], v[172:175], v[112:115]\n\t" "ds_read_b64_tr_b16 v[164:165], v5 offset:61440\n\t" "v_cndmask_b32 v17, v39, v40, s[60:61]\n\t"
    "v_mfma_f32_16x16x32_bf16 v[116:119], v[200:203], v[172:175], v[116:119]\n\t" "ds_read_b64_tr_b16 v[166:167], v5 offset:61952\n\t" "v_cndmask_b32 v19, v39, v41, s[60:61]\n\t"
    "v_mfma_f32_16x16x32_bf16 v[148:151], v[232:235], v[172:175], v[148:151]\n\t" "ds_read_b128 v[228:231], v1 offset:22528\n\t" "global_load_lds_dwordx4 v0, %[sb] offset:2048\n\t"
    "v_mfma_f32_16x16x32_bf16 v[120:123], v[196:199], v[176:179], v[120:123]\n\t" "ds_read_b64_tr_b16 v[168:169], v6 offset:61440\n\t" "s_cmp_eq_u32 %[rot], 1\n\t" "s_cselect_b32 s42, s43, 0x30000\n\t"
    "v_add_u32_e64 v0, s42, v0\n\t" "v_mfma_f32_16x16x32_bf16 v[124:127], v[200:203], v[176:179], v[124:127]\n\t" "ds_read_b64_tr_b16 v[170:171], v6 offset:61952\n\t" "v_add_u32 v12, 0x20000, v12\n\t"
    "v_mfma_f32_16x16x32_bf16 v[128:131], v[196:199], v[180:183], v[128:131]\n\t" "ds_read_b64_tr_b16 v[172:173], v3 offset:63488\n\t" "s_add_u32 m0, %[sl1], 8192\n\t"
    "v_mfma_f32_16x16x32_bf16 v[132:135], v[200:203], v[180:183], v[132:135]\n\t" "ds_read_b64_tr_b16 v[174:175], v3 offset:64000\n\t" "global_load_lds_dwordx4 v12, %[so]\n\t"
    "v_mfma_f32_16x16x32_bf16 v[136:139], v[196:199], v[184:187], v[136:139]\n\t" "ds_read_b64_tr_b16 v[176:177], v4 offset:63488\n\t" "v_and_b32_e64 v11, 7, v13\n\t"
    "v_mfma_f32_16x16x32_bf16 v[140:143], v[200:203], v[184:187], v[140:143]\n\t" "ds_read_b64_tr_b16 v[178:179], v4 offset:64000\n\t" "s_waitcnt lgkmcnt(4)\n\t"
    "v_mfma_f32_16x16x32_bf16 v[48:51], v[156:159], v[188:191], v[48:51]\n\t" "ds_read_b64_tr_b16 v[180:181], v5 offset:63488\n\t" "s_add_u32 m0, %[sl], 108544\n\t"
    "v_mfma_f32_16x16x32_bf16 v[52:55], v[156:159], v[192:195], v[52:55]\n\t" "ds_read_b64_tr_b16 v[182:183], v5 offset:64000\n\t" "v_lshrrev_b32 v18, 1, v45\n\t"
    "v_mfma_f32_16x16x32_bf16 v[152:155], v[228:231], v[156:159], v[152:155]\n\t" "global_load_lds_dwordx4 v0, %[sb] offset:-2048\n\t" "v_lshl_add_u32 v11, v11, 5, v18\n\t"
    "v_mfma_f32_16x16x32_bf16 v[56:59], v[160:163], v[188:191], v[56:59]\n\t" "ds_read_b64_tr_b16 v[184:185], v6 offset:63488\n\t" "v_add_u32_e64 v11, %[lr], v11\n\t"
    "v_mfma_f32_4x4x4_16b_bf16 v[204:207], v[224:225], a[184:185], 0\n\t" "v_mfma_f32_4x4x4_16b_bf16 v[208:211], v[226:227], a[186:187], 0\n\t"
    "v_mfma_f32_4x4x4_16b_bf16 v[212:215], v[224:225], a[188:189], 0\n\t" "v_mfma_f32_4x4x4_16b_bf16 v[216:219], v[226:227], a[190:191], 0\n\t" "v_mul_f32_e64 v28, v240, v24\n\t"
    "v_mfma_f32_16x16x32_bf16 v[60:63], v[160:163], v[192:195], v[60:63]\n\t" "ds_read_b64_tr_b16 v[186:187], v6 offset:64000\n\t" "v_mul_f32_e64 v29, v240, v25\n\t"
    "v_mfma_f32_16x16x32_bf16 v[64:67], v[164:167], v[188:191], v[64:67]\n\t" "ds_read_b64_tr_b16 v[156:157], v7 offset:0\n\t" "v_mul_f32_e64 v30, v241, v26\n\t"
    "v_mfma_f32_16x16x32_bf16 v[68:71], v[164:167], v[192:195], v[68:71]\n\t" "ds_read_b64_tr_b16 v[158:159], v7 offset:512\n\t" "v_cvt_pk_bf16_f32 v196, v204, v205\n\t"
    "v_mfma_f32_16x16x32_bf16 v[72:75], v[168:171], v[188:191], v[72:75]\n\t" "ds_read_b64_tr_b16 v[160:161], v8 offset:0\n\t" "v_cvt_pk_bf16_f32 v197, v206, v207\n\t"
    "v_mfma_f32_16x16x32_bf16 v[76:79], v[168:171], v[192:195], v[76:79]\n\t" "ds_read_b64_tr_b16 v[162:163], v8 offset:512\n\t" "s_waitcnt lgkmcnt(4)\n\t"
    "v_mfma_f32_16x16x32_bf16 v[80:83], v[172:175], v[188:191], v[80:83]\n\t" "ds_read_b64_tr_b16 v[164:165], v9 offset:0\n\t" "v_cvt_pk_bf16_f32 v198, v208, v209\n\t"
    "v_mfma_f32_16x16x32_bf16 v[84:87], v[172:175], v[192:195], v[84:87]\n\t" "ds_read_b64_tr_b16 v[166:167], v9 offset:512\n\t" "v_cvt_pk_bf16_f32 v199, v210, v211\n\t"
    "v_mfma_f32_16x16x32_bf16 v[144:147], v[172:175], v[228:231], v[144:147]\n\t" "ds_read_b128 v[220:223], v2 offset:6144\n\t" "global_load_lds_dwordx4 v0, %[sb] offset:0\n\t"
    "v_mfma_f32_16x16x32_bf16 v[88:91], v[176:179], v[188:191], v[88:91]\n\t" "ds_read_b64_tr_b16 v[168:169], v10 offset:0\n\t" "v_cvt_pk_bf16_f32 v200, v212, v213\n\t"
    "v_mfma_f32_16x16x32_bf16 v[92:95], v[176:179], v[192:195], v[92:95]\n\t" "ds_read_b64_tr_b16 v[170:171], v10 offset:512\n\t" "v_cvt_pk_bf16_f32 v201, v214, v215\n\t"
    "v_mfma_f32_16x16x32_bf16 v[96:99], v[180:183], v[188:191], v[96:99]\n\t" "ds_read_b64_tr_b16 v[172:173], v3 offset:0\n\t" "v_mul_f32 v31, v241, v27\n\t"
    "v_mfma_f32_16x16x32_bf16 v[100:103], v[180:183], v[192:195], v[100:103]\n\t" "ds_read_b64_tr_b16 v[174:175], v3 offset:512\n\t" "v_cvt_pk_bf16_f32 v202, v216, v217\n\t"
    "v_mfma_f32_16x16x32_bf16 v[104:107], v[184:187], v[188:191], v[104:107]\n\t" "ds_read_b64_tr_b16 v[176:177], v4 offset:0\n\t" "v_cvt_pk_bf16_f32 v203, v218, v219\n\t"
    "v_mfma_f32_16x16x32_bf16 v[108:111], v[184:187], v[192:195], v[108:111]\n\t" "ds_read_b64_tr_b16 v[178:179], v4 offset:512\n\t" "s_waitcnt lgkmcnt(4)\n\t"
    "v_mfma_f32_16x16x32_bf16 v[112:115], v[188:191], v[156:159], v[112:115]\n\t" "ds_read_b64_tr_b16 v[180:181], v5 offset:0\n\t" "v_mov_b32 v39, 0xceabfb70\n\t"
    "v_mfma_f32_16x16x32_bf16 v[116:119], v[192:195], v[156:159], v[116:119]\n\t" "ds_read_b64_tr_b16 v[182:183], v5 offset:512\n\t" "v_cndmask_b32 v28, v28, 0, s[56:57]\n\t"
    "v_mfma_f32_16x16x32_bf16 v[148:151], v[228:231], v[156:159], v[148:151]\n\t" "ds_read_b128 v[232:235], v1 offset:23552\n\t" "global_load_lds_dwordx4 v0, %[sb] offset:2048\n\t"
    "v_mfma_f32_16x16x32_bf16 v[120:123], v[188:191], v[160:163], v[120:123]\n\t" "ds_read_b64_tr_b16 v[184:185], v6 offset:0\n\t" "v_add_u32 v0, 0x30000, v0\n\t"
    "v_mfma_f32_16x16x32_bf16 v[124:127], v[192:195], v[160:163], v[124:127]\n\t" "ds_read_b64_tr_b16 v[186:187], v6 offset:512\n\t" "v_add_u32 v16, 0x70000, v16\n\t"
    "v_mfma_f32_16x16x32_bf16 v[128:131], v[188:191], v[164:167], v[128:131]\n\t" "ds_read_b64_tr_b16 v[156:157], v3 offset:2048\n\t" "v_cndmask_b32 v29, v29, 0, s[58:59]\n\t"
    "v_mfma_f32_16x16x32_bf16 v[132:135], v[192:195], v[164:167], v[132:135]\n\t" "ds_read_b64_tr_b16 v[158:159], v3 offset:2560\n\t" "s_mov_b64 exec, %[tm]\n\t" "global_load_dword a64, v16, %[pf]\n\t"
    "s_mov_b64 exec, -1\n\t" "v_mfma_f32_16x16x32_bf16 v[136:139], v[188:191], v[168:171], v[136:139]\n\t" "ds_read_b64_tr_b16 v[160:161], v4 offset:2048\n\t" "v_cndmask_b32 v32, 0, v39, s[56:57]\n\t"
    "v_mfma_f32_16x16x32_bf16 v[140:143], v[192:195], v[168:171], v[140:143]\n\t" "ds_read_b64_tr_b16 v[162:163], v4 offset:2560\n\t" "s_waitcnt lgkmcnt(4)\n\t"
    "v_mfma_f32_16x16x32_bf16 v[48:51], v[172:175], v[196:199], v[48:51]\n\t" "ds_read_b64_tr_b16 v[164:165], v5 offset:2048\n\t" "s_add_u32 m0, %[sl], 120832\n\t"
    "v_mfma_f32_16x16x32_bf16 v[52:55], v[172:175], v[200:203], v[52:55]\n\t" "ds_read_b64_tr_b16 v[166:167], v5 offset:2560\n\t" "v_cndmask_b32 v33, 0, v39, s[58:59]\n\t"
    "v_mfma_f32_16x16x32_bf16 v[152:155], v[232:235], v[172:175], v[152:155]\n\t" "global_load_lds_dwordx4 v0, %[sb] offset:-2048\n\t" "v_lshl_add_u32 v43, v46, 5, %[lh]\n\t"
    "v_mfma_f32_16x16x32_bf16 v[56:59], v[176:179], v[196:199], v[56:59]\n\t" "ds_read_b64_tr_b16 v[168:169], v6 offset:2048\n\t" "v_lshl_add_u32 v36, v47, 3, %[lt]\n\t"
    "v_mfma_f32_4x4x4_16b_bf16 v[204:207], v[220:221], a[192:193], 0\n\t" "v_mfma_f32_4x4x4_16b_bf16 v[208:211], v[222:223], a[194:195], 0\n\t"
    "v_mfma_f32_4x4x4_16b_bf16 v[212:215], v[220:221], a[196:197], 0\n\t" "v_mfma_f32_4x4x4_16b_bf16 v[216:219], v[222:223], a[198:199], 0\n\t" "v_lshl_add_u32 v35, v46, 3, %[lh]\n\t"
    "v_mfma_f32_16x16x32_bf16 v[60:63], v[176:179], v[200:203], v[60:63]\n\t" "ds_read_b64_tr_b16 v[170:171], v6 offset:2560\n\t" "v_add_u32 v35, 0x6800, v35\n\t"
    "v_mfma_f32_16x16x32_bf16 v[64:67], v[180:183], v[196:199], v[64:67]\n\t" "ds_read_b64_tr_b16 v[172:173], v3 offset:4096\n\t" "v_cndmask_b32 v35, v35, v36, s[46:47]\n\t"
    "v_mfma_f32_16x16x32_bf16 v[68:71], v[180:183], v[200:203], v[68:71]\n\t" "ds_read_b64_tr_b16 v[174:175], v3 offset:4608\n\t" "v_cvt_pk_bf16_f32 v188, v204, v205\n\t"
    "v_mfma_f32_16x16x32_bf16 v[72:75], v[184:187], v[196:199], v[72:75]\n\t" "ds_read_b64_tr_b16 v[176:177], v4 offset:4096\n\t" "v_cvt_pk_bf16_f32 v189, v206, v207\n\t"
    "v_mfma_f32_16x16x32_bf16 v[76:79], v[184:187], v[200:203], v[76:79]\n\t" "ds_read_b64_tr_b16 v[178:179], v4 offset:4608\n\t" "s_waitcnt lgkmcnt(4)\n\t"
    "v_mfma_f32_16x16x32_bf16 v[80:83], v[156:159], v[196:199], v[80:83]\n\t" "ds_read_b64_tr_b16 v[180:181], v5 offset:4096\n\t" "v_cvt_pk_bf16_f32 v190, v208, v209\n\t"
    "v_mfma_f32_16x16x32_bf16 v[84:87], v[156:159], v[200:203], v[84:87]\n\t" "ds_read_b64_tr_b16 v[182:183], v5 offset:4608\n\t" "v_cvt_pk_bf16_f32 v191, v210, v211\n\t"
    "v_mfma_f32_16x16x32_bf16 v[144:147], v[156:159], v[232:235], v[144:147]\n\t" "ds_read_b128 v[224:227], v2 offset:6400\n\t" "global_load_lds_dwordx4 v0, %[sb] offset:0\n\t"
    "v_mfma_f32_16x16x32_bf16 v[88:91], v[160:163], v[196:199], v[88:91]\n\t" "ds_read_b64_tr_b16 v[184:185], v6 offset:4096\n\t" "v_cvt_pk_bf16_f32 v192, v212, v213\n\t"
    "v_mfma_f32_16x16x32_bf16 v[92:95], v[160:163], v[200:203], v[92:95]\n\t" "ds_read_b64_tr_b16 v[186:187], v6 offset:4608\n\t" "v_cvt_pk_bf16_f32 v193, v214, v215\n\t"
    "v_mfma_f32_16x16x32_bf16 v[96:99], v[164:167], v[196:199], v[96:99]\n\t" "s_waitcnt vmcnt(15)\n\t" "s_barrier\n\t" "ds_read_b64_tr_b16 v[156:157], v3 offset:12288\n\t"
    "v_mfma_f32_16x16x32_bf16 v[100:103], v[164:167], v[200:203], v[100:103]\n\t" "ds_read_b64_tr_b16 v[158:159], v3 offset:12800\n\t" "v_cvt_pk_bf16_f32 v194, v216, v217\n\t"
    "v_mfma_f32_16x16x32_bf16 v[104:107], v[168:171], v[196:199], v[104:107]\n\t" "ds_read_b64_tr_b16 v[160:161], v4 offset:12288\n\t" "v_cvt_pk_bf16_f32 v195, v218, v219\n\t"
    "v_mfma_f32_16x16x32_bf16 v[108:111], v[168:171], v[200:203], v[108:111]\n\t" "ds_read_b64_tr_b16 v[162:163], v4 offset:12800\n\t" "s_waitcnt lgkmcnt(4)\n\t"
    "v_mfma_f32_16x16x32_bf16 v[112:115], v[196:199], v[172:175], v[112:115]\n\t" "ds_read_b64_tr_b16 v[164:165], v5 offset:12288\n\t" "v_lshl_add_u32 v34, v45, 1, %[lt]\n\t"
    "v_mfma_f32_16x16x32_bf16 v[116:119], v[200:203], v[172:175], v[116:119]\n\t" "ds_read_b64_tr_b16 v[166:167], v5 offset:12800\n\t" "v_lshl_add_u32 v34, v47, 2, v34\n\t"
    "v_mfma_f32_16x16x32_bf16 v[148:151], v[232:235], v[172:175], v[148:151]\n\t" "ds_read_b128 v[228:231], v1 offset:24576\n\t" "global_load_lds_dwordx4 v0, %[sb] offset:2048\n\t"
    "v_mfma_f32_16x16x32_bf16 v[120:123], v[196:199], v[176:179], v[120:123]\n\t" "ds_read_b64_tr_b16 v[168:169], v6 offset:12288\n\t" "v_add_u32 v0, 0x30000, v0\n\t"
    "v_mfma_f32_16x16x32_bf16 v[124:127], v[200:203], v[176:179], v[124:127]\n\t" "ds_read_b64_tr_b16 v[170:171], v6 offset:12800\n\t" "v_add_u32 v12, 0x10000, v12\n\t"
    "v_mfma_f32_16x16x32_bf16 v[128:131], v[196:199], v[180:183], v[128:131]\n\t" "ds_read_b64_tr_b16 v[172:173], v3 offset:14336\n\t" "s_add_u32 m0, %[sl1], 12288\n\t"
    "v_mfma_f32_16x16x32_bf16 v[132:135], v[200:203], v[180:183], v[132:135]\n\t" "ds_read_b64_tr_b16 v[174:175], v3 offset:14848\n\t" "global_load_lds_dwordx4 v12, %[so]\n\t"
    "v_mfma_f32_16x16x32_bf16 v[136:139], v[196:199], v[184:187], v[136:139]\n\t" "ds_read_b64_tr_b16 v[176:177], v4 offset:14336\n\t" "v_add_u32 v34, 0x200, v34\n\t"
    "v_mfma_f32_16x16x32_bf16 v[140:143], v[200:203], v[184:187], v[140:143]\n\t" "ds_read_b64_tr_b16 v[178:179], v4 offset:14848\n\t" "s_waitcnt lgkmcnt(4)\n\t"
    "v_mfma_f32_16x16x32_bf16 v[48:51], v[156:159], v[188:191], v[48:51]\n\t" "ds_read_b64_tr_b16 v[180:181], v5 offset:14336\n\t" "s_add_u32 m0, %[sl], 71680\n\t"
    "v_mfma_f32_16x16x32_bf16 v[52:55], v[156:159], v[192:195], v[52:55]\n\t" "ds_read_b64_tr_b16 v[182:183], v5 offset:14848\n\t" "v_add_u32 v37, 0x6800, v43\n\t"
    "v_mfma_f32_16x16x32_bf16 v[152:155], v[228:231], v[156:159], v[152:155]\n\t" "global_load_lds_dwordx4 v0, %[sb] offset:-2048\n\t" "v_cndmask_b32 v34, v37, v34, s[48:49]\n\t"
    "v_mfma_f32_16x16x32_bf16 v[56:59], v[160:163], v[188:191], v[56:59]\n\t" "ds_read_b64_tr_b16 v[184:185], v6 offset:14336\n\t" "v_xor_b32 v37, 0x80, v36\n\t"
    "v_mfma_f32_4x4x4_16b_bf16 v[204:207], v[224:225], a[200:201], 0\n\t" "v_mfma_f32_4x4x4_16b_bf16 v[208:211], v[226:227], a[202:203], 0\n\t"
    "v_mfma_f32_4x4x4_16b_bf16 v[212:215], v[224:225], a[204:205], 0\n\t" "v_mfma_f32_4x4x4_16b_bf16 v[216:219], v[226:227], a[206:207], 0\n\t" "v_xor_b32 v38, 0x100, v36\n\t"
    "v_mfma_f32_16x16x32_bf16 v[60:63], v[160:163], v[192:195], v[60:63]\n\t" "ds_read_b64_tr_b16 v[186:187], v6 offset:14848\n\t" "v_xor_b32 v39, 0x180, v36\n\t"
    "v_mfma_f32_16x16x32_bf16 v[64:67], v[164:167], v[188:191], v[64:67]\n\t" "ds_read_b64_tr_b16 v[156:157], v3 offset:16384\n\t" "v_add_u32 v40, %[xw], v43\n\t"
    "v_mfma_f32_16x16x32_bf16 v[68:71], v[164:167], v[192:195], v[68:71]\n\t" "ds_read_b64_tr_b16 v[158:159], v3 offset:16896\n\t" "v_cvt_pk_bf16_f32 v196, v204, v205\n\t"
    "v_mfma_f32_16x16x32_bf16 v[72:75], v[168:171], v[188:191], v[72:75]\n\t" "ds_read_b64_tr_b16 v[160:161], v4 offset:16384\n\t" "v_cvt_pk_bf16_f32 v197, v206, v207\n\t"
    "v_mfma_f32_16x16x32_bf16 v[76:79], v[168:171], v[192:195], v[76:79]\n\t" "ds_read_b64_tr_b16 v[162:163], v4 offset:16896\n\t" "s_waitcnt lgkmcnt(4)\n\t"
    "v_mfma_f32_16x16x32_bf16 v[80:83], v[172:175], v[188:191], v[80:83]\n\t" "ds_read_b64_tr_b16 v[164:165], v5 offset:16384\n\t" "v_cvt_pk_bf16_f32 v198, v208, v209\n\t"
    "v_mfma_f32_16x16x32_bf16 v[84:87], v[172:175], v[192:195], v[84:87]\n\t" "ds_read_b64_tr_b16 v[166:167], v5 offset:16896\n\t" "v_cvt_pk_bf16_f32 v199, v210, v211\n\t"
    "v_mfma_f32_16x16x32_bf16 v[144:147], v[172:175], v[228:231], v[144:147]\n\t" "ds_read_b128 v[220:223], v2 offset:6656\n\t" "global_load_lds_dwordx4 v0, %[sb] offset:0\n\t"
    "v_mfma_f32_16x16x32_bf16 v[88:91], v[176:179], v[188:191], v[88:91]\n\t" "ds_read_b64_tr_b16 v[168:169], v6 offset:16384\n\t" "v_cvt_pk_bf16_f32 v200, v212, v213\n\t"
    "v_mfma_f32_16x16x32_bf16 v[92:95], v[176:179], v[192:195], v[92:95]\n\t" "ds_read_b64_tr_b16 v[170:171], v6 offset:16896\n\t" "v_cvt_pk_bf16_f32 v201, v214, v215\n\t"
    "v_mfma_f32_16x16x32_bf16 v[96:99], v[180:183], v[188:191], v[96:99]\n\t" "ds_read_b64_tr_b16 v[172:173], v3 offset:24576\n\t" "v_xor_b32 v41, 0x800, v40\n\t"
    "v_mfma_f32_16x16x32_bf16 v[100:103], v[180:183], v[192:195], v[100:103]\n\t" "ds_read_b64_tr_b16 v[174:175], v3 offset:25088\n\t" "v_cvt_pk_bf16_f32 v202, v216, v217\n\t"
    "v_mfma_f32_16x16x32_bf16 v[104:107], v[184:187], v[188:191], v[104:107]\n\t" "ds_read_b64_tr_b16 v[176:177], v4 offset:24576\n\t" "v_cvt_pk_bf16_f32 v203, v218, v219\n\t"
    "v_mfma_f32_16x16x32_bf16 v[108:111], v[184:187], v[192:195], v[108:111]\n\t" "ds_read_b64_tr_b16 v[178:179], v4 offset:25088\n\t" "s_waitcnt lgkmcnt(4)\n\t"
    "v_mfma_f32_16x16x32_bf16 v[112:115], v[188:191], v[156:159], v[112:115]\n\t" "ds_read_b64_tr_b16 v[180:181], v5 offset:24576\n\t" "v_add_u32_e64 v42, %[xh], v43\n\t"
    "v_mfma_f32_16x16x32_bf16 v[116:119], v[192:195], v[156:159], v[116:119]\n\t" "ds_read_b64_tr_b16 v[182:183], v5 offset:25088\n\t" "v_add_u32_e64 v43, %[w4k], v43\n\t"
    "v_mfma_f32_16x16x32_bf16 v[148:151], v[228:231], v[156:159], v[148:151]\n\t" "ds_read_b128 v[232:235], v1 offset:25600\n\t" "global_load_lds_dwordx4 v0, %[sb] offset:2048\n\t"
    "v_mfma_f32_16x16x32_bf16 v[120:123], v[188:191], v[160:163], v[120:123]\n\t" "ds_read_b64_tr_b16 v[184:185], v6 offset:24576\n\t" "v_add_u32 v0, 0x30000, v0\n\t"
    "v_mfma_f32_16x16x32_bf16 v[124:127], v[192:195], v[160:163], v[124:127]\n\t" "ds_read_b64_tr_b16 v[186:187], v6 offset:25088\n\t" "v_lshl_add_u32 v44, v46, 4, %[ox]\n\t"
    "v_mfma_f32_16x16x32_bf16 v[128:131], v[188:191], v[164:167], v[128:131]\n\t" "ds_read_b64_tr_b16 v[156:157], v3 offset:26624\n\t" "v_xor_b32 v45, 0x800, v44\n\t"
    "v_mfma_f32_16x16x32_bf16 v[132:135], v[192:195], v[164:167], v[132:135]\n\t" "ds_read_b64_tr_b16 v[158:159], v3 offset:27136\n\t"
    "v_mfma_f32_16x16x32_bf16 v[136:139], v[188:191], v[168:171], v[136:139]\n\t" "ds_read_b64_tr_b16 v[160:161], v4 offset:26624\n\t"
    "v_mfma_f32_16x16x32_bf16 v[140:143], v[192:195], v[168:171], v[140:143]\n\t" "ds_read_b64_tr_b16 v[162:163], v4 offset:27136\n\t" "s_waitcnt lgkmcnt(4)\n\t"
    "v_mfma_f32_16x16x32_bf16 v[48:51], v[172:175], v[196:199], v[48:51]\n\t" "ds_read_b64_tr_b16 v[164:165], v5 offset:26624\n\t" "s_add_u32 m0, %[sl], 83968\n\t"
    "v_mfma_f32_16x16x32_bf16 v[52:55], v[172:175], v[200:203], v[52:55]\n\t" "ds_read_b64_tr_b16 v[166:167], v5 offset:27136\n\t"
    "v_mfma_f32_16x16x32_bf16 v[152:155], v[232:235], v[172:175], v[152:155]\n\t" "global_load_lds_dwordx4 v0, %[sb] offset:-2048\n\t"
    "v_mfma_f32_16x16x32_bf16 v[56:59], v[176:179], v[196:199], v[56:59]\n\t" "ds_read_b64_tr_b16 v[168:169], v6 offset:26624\n\t" "v_mfma_f32_4x4x4_16b_bf16 v[204:207], v[220:221], a[208:209], 0\n\t"
    "v_mfma_f32_4x4x4_16b_bf16 v[208:211], v[222:223], a[210:211], 0\n\t" "v_mfma_f32_4x4x4_16b_bf16 v[212:215], v[220:221], a[212:213], 0\n\t"
    "v_mfma_f32_4x4x4_16b_bf16 v[216:219], v[222:223], a[214:215], 0\n\t" "v_mfma_f32_16x16x32_bf16 v[60:63], v[176:179], v[200:203], v[60:63]\n\t" "ds_read_b64_tr_b16 v[170:171], v6 offset:27136\n\t"
    "v_mfma_f32_16x16x32_bf16 v[64:67], v[180:183], v[196:199], v[64:67]\n\t" "ds_read_b64_tr_b16 v[172:173], v3 offset:28672\n\t" "v_mfma_f32_16x16x32_bf16 v[68:71], v[180:183], v[200:203], v[68:71]\n\t"
    "ds_read_b64_tr_b16 v[174:175], v3 offset:29184\n\t" "v_cvt_pk_bf16_f32 v188, v204, v205\n\t" "v_mfma_f32_16x16x32_bf16 v[72:75], v[184:187], v[196:199], v[72:75]\n\t"
    "ds_read_b64_tr_b16 v[176:177], v4 offset:28672\n\t" "v_cvt_pk_bf16_f32 v189, v206, v207\n\t" "v_mfma_f32_16x16x32_bf16 v[76:79], v[184:187], v[200:203], v[76:79]\n\t"
    "ds_read_b64_tr_b16 v[178:179], v4 offset:29184\n\t" "s_waitcnt lgkmcnt(4)\n\t" "v_mfma_f32_16x16x32_bf16 v[80:83], v[156:159], v[196:199], v[80:83]\n\t"
    "ds_read_b64_tr_b16 v[180:181], v5 offset:28672\n\t" "v_cvt_pk_bf16_f32 v190, v208, v209\n\t" "v_mfma_f32_16x16x32_bf16 v[84:87], v[156:159], v[200:203], v[84:87]\n\t"
    "ds_read_b64_tr_b16 v[182:183], v5 offset:29184\n\t" "v_cvt_pk_bf16_f32 v191, v210, v211\n\t" "v_mfma_f32_16x16x32_bf16 v[144:147], v[156:159], v[232:235], v[144:147]\n\t"
    "ds_read_b128 v[224:227], v2 offset:6912\n\t" "global_load_lds_dwordx4 v0, %[sb] offset:0\n\t" "v_mfma_f32_16x16x32_bf16 v[88:91], v[160:163], v[196:199], v[88:91]\n\t"
    "ds_read_b64_tr_b16 v[184:185], v6 offset:28672\n\t" "v_cvt_pk_bf16_f32 v192, v212, v213\n\t" "v_mfma_f32_16x16x32_bf16 v[92:95], v[160:163], v[200:203], v[92:95]\n\t"
    "ds_read_b64_tr_b16 v[186:187], v6 offset:29184\n\t" "v_cvt_pk_bf16_f32 v193, v214, v215\n\t" "v_mfma_f32_16x16x32_bf16 v[96:99], v[164:167], v[196:199], v[96:99]\n\t" "s_waitcnt vmcnt(14)\n\t"
    "s_barrier\n\t" "ds_read_b64_tr_b16 v[156:157], v3 offset:36864\n\t" "v_mfma_f32_16x16x32_bf16 v[100:103], v[164:167], v[200:203], v[100:103]\n\t" "ds_read_b64_tr_b16 v[158:159], v3 offset:37376\n\t"
    "v_cvt_pk_bf16_f32 v194, v216, v217\n\t" "v_mfma_f32_16x16x32_bf16 v[104:107], v[168:171], v[196:199], v[104:107]\n\t" "ds_read_b64_tr_b16 v[160:161], v4 offset:36864\n\t"
    "v_cvt_pk_bf16_f32 v195, v218, v219\n\t" "v_mfma_f32_16x16x32_bf16 v[108:111], v[168:171], v[200:203], v[108:111]\n\t" "ds_read_b64_tr_b16 v[162:163], v4 offset:37376\n\t" "s_waitcnt lgkmcnt(4)\n\t"
    "v_mfma_f32_16x16x32_bf16 v[112:115], v[196:199], v[172:175], v[112:115]\n\t" "ds_read_b64_tr_b16 v[164:165], v5 offset:36864\n\t"
    "v_mfma_f32_16x16x32_bf16 v[116:119], v[200:203], v[172:175], v[116:119]\n\t" "ds_read_b64_tr_b16 v[166:167], v5 offset:37376\n\t"
    "v_mfma_f32_16x16x32_bf16 v[148:151], v[232:235], v[172:175], v[148:151]\n\t" "ds_read_b128 v[228:231], v1 offset:26624\n\t" "global_load_lds_dwordx4 v0, %[sb] offset:2048\n\t"
    "v_mfma_f32_16x16x32_bf16 v[120:123], v[196:199], v[176:179], v[120:123]\n\t" "ds_read_b64_tr_b16 v[168:169], v6 offset:36864\n\t" "v_add_u32 v16, 0x70000, v16\n\t"
    "v_mfma_f32_16x16x32_bf16 v[124:127], v[200:203], v[176:179], v[124:127]\n\t" "ds_read_b64_tr_b16 v[170:171], v6 offset:37376\n\t"
    "v_mfma_f32_16x16x32_bf16 v[128:131], v[196:199], v[180:183], v[128:131]\n\t" "ds_read_b64_tr_b16 v[172:173], v3 offset:38912\n\t"
    "v_mfma_f32_16x16x32_bf16 v[132:135], v[200:203], v[180:183], v[132:135]\n\t" "ds_read_b64_tr_b16 v[174:175], v3 offset:39424\n\t" "s_mov_b64 exec, %[tm]\n\t" "global_load_dword a64, v16, %[pf]\n\t"
    "s_mov_b64 exec, -1\n\t" "v_mfma_f32_16x16x32_bf16 v[136:139], v[196:199], v[184:187], v[136:139]\n\t" "ds_read_b64_tr_b16 v[176:177], v4 offset:38912\n\t"
    "v_mfma_f32_16x16x32_bf16 v[140:143], v[200:203], v[184:187], v[140:143]\n\t" "ds_read_b64_tr_b16 v[178:179], v4 offset:39424\n\t" "s_waitcnt lgkmcnt(4)\n\t"
    "v_mfma_f32_16x16x32_bf16 v[48:51], v[156:159], v[188:191], v[48:51]\n\t" "ds_read_b64_tr_b16 v[180:181], v5 offset:38912\n\t" "v_add_u32 v12, 0x20000, v12\n\t"
    "v_mfma_f32_16x16x32_bf16 v[52:55], v[156:159], v[192:195], v[52:55]\n\t" "ds_read_b64_tr_b16 v[182:183], v5 offset:39424\n\t" "s_add_u32 m0, %[sl1], 20480\n\t"
    "v_mfma_f32_16x16x32_bf16 v[152:155], v[228:231], v[156:159], v[152:155]\n\t" "global_load_lds_dwordx4 v12, %[so]\n\t" "v_add_u32 v12, 0x10000, v12\n\t"
    "v_mfma_f32_16x16x32_bf16 v[56:59], v[160:163], v[188:191], v[56:59]\n\t" "ds_read_b64_tr_b16 v[184:185], v6 offset:38912\n\t" "v_mfma_f32_4x4x4_16b_bf16 v[204:207], v[224:225], a[216:217], 0\n\t"
    "v_mfma_f32_4x4x4_16b_bf16 v[208:211], v[226:227], a[218:219], 0\n\t" "v_mfma_f32_4x4x4_16b_bf16 v[212:215], v[224:225], a[220:221], 0\n\t"
    "v_mfma_f32_4x4x4_16b_bf16 v[216:219], v[226:227], a[222:223], 0\n\t" "v_mfma_f32_16x16x32_bf16 v[60:63], v[160:163], v[192:195], v[60:63]\n\t" "ds_read_b64_tr_b16 v[186:187], v6 offset:39424\n\t"
    "v_mfma_f32_16x16x32_bf16 v[64:67], v[164:167], v[188:191], v[64:67]\n\t" "ds_read_b64_tr_b16 v[156:157], v3 offset:40960\n\t" "v_mfma_f32_16x16x32_bf16 v[68:71], v[164:167], v[192:195], v[68:71]\n\t"
    "ds_read_b64_tr_b16 v[158:159], v3 offset:41472\n\t" "v_cvt_pk_bf16_f32 v196, v204, v205\n\t" "v_mfma_f32_16x16x32_bf16 v[72:75], v[168:171], v[188:191], v[72:75]\n\t"
    "ds_read_b64_tr_b16 v[160:161], v4 offset:40960\n\t" "v_cvt_pk_bf16_f32 v197, v206, v207\n\t" "v_mfma_f32_16x16x32_bf16 v[76:79], v[168:171], v[192:195], v[76:79]\n\t"
    "ds_read_b64_tr_b16 v[162:163], v4 offset:41472\n\t" "s_waitcnt lgkmcnt(4)\n\t" "v_mfma_f32_16x16x32_bf16 v[80:83], v[172:175], v[188:191], v[80:83]\n\t"
    "ds_read_b64_tr_b16 v[164:165], v5 offset:40960\n\t" "v_cvt_pk_bf16_f32 v198, v208, v209\n\t" "v_mfma_f32_16x16x32_bf16 v[84:87], v[172:175], v[192:195], v[84:87]\n\t"
    "ds_read_b64_tr_b16 v[166:167], v5 offset:41472\n\t" "v_cvt_pk_bf16_f32 v199, v210, v211\n\t" "v_mfma_f32_16x16x32_bf16 v[144:147], v[172:175], v[228:231], v[144:147]\n\t"
    "ds_read_b128 v[220:223], v2 offset:7168\n\t" "s_add_u32 m0, %[sl1], 24576\n\t" "v_mfma_f32_16x16x32_bf16 v[88:91], v[176:179], v[188:191], v[88:91]\n\t"
    "ds_read_b64_tr_b16 v[168:169], v6 offset:40960\n\t" "v_cvt_pk_bf16_f32 v200, v212, v213\n\t" "v_mfma_f32_16x16x32_bf16 v[92:95], v[176:179], v[192:195], v[92:95]\n\t"
    "ds_read_b64_tr_b16 v[170:171], v6 offset:41472\n\t" "v_cvt_pk_bf16_f32 v201, v214, v215\n\t" "v_mfma_f32_16x16x32_bf16 v[96:99], v[180:183], v[188:191], v[96:99]\n\t"
    "ds_read_b64_tr_b16 v[172:173], v7 offset:8192\n\t" "global_load_lds_dwordx4 v12, %[so]\n\t" "v_mfma_f32_16x16x32_bf16 v[100:103], v[180:183], v[192:195], v[100:103]\n\t"
    "ds_read_b64_tr_b16 v[174:175], v7 offset:8704\n\t" "v_cvt_pk_bf16_f32 v202, v216, v217\n\t" "v_mfma_f32_16x16x32_bf16 v[104:107], v[184:187], v[188:191], v[104:107]\n\t"
    "ds_read_b64_tr_b16 v[176:177], v8 offset:8192\n\t" "v_cvt_pk_bf16_f32 v203, v218, v219\n\t" "v_mfma_f32_16x16x32_bf16 v[108:111], v[184:187], v[192:195], v[108:111]\n\t"
    "ds_read_b64_tr_b16 v[178:179], v8 offset:8704\n\t" "s_waitcnt lgkmcnt(4)\n\t" "v_mfma_f32_16x16x32_bf16 v[112:115], v[188:191], v[156:159], v[112:115]\n\t"
    "ds_read_b64_tr_b16 v[180:181], v9 offset:8192\n\t" "v_add_u32 v12, 0x10000, v12\n\t" "v_mfma_f32_16x16x32_bf16 v[116:119], v[192:195], v[156:159], v[116:119]\n\t"
    "ds_read_b64_tr_b16 v[182:183], v9 offset:8704\n\t" "s_add_u32 m0, %[sl1], 28672\n\t" "v_mfma_f32_16x16x32_bf16 v[148:151], v[228:231], v[156:159], v[148:151]\n\t"
    "ds_read_b128 v[232:235], v1 offset:27648\n\t" "global_load_lds_dwordx4 v12, %[so]\n\t" "v_mfma_f32_16x16x32_bf16 v[120:123], v[188:191], v[160:163], v[120:123]\n\t"
    "ds_read_b64_tr_b16 v[184:185], v10 offset:8192\n\t" "v_mfma_f32_16x16x32_bf16 v[124:127], v[192:195], v[160:163], v[124:127]\n\t" "ds_read_b64_tr_b16 v[186:187], v10 offset:8704\n\t"
    "v_mfma_f32_16x16x32_bf16 v[128:131], v[188:191], v[164:167], v[128:131]\n\t" "ds_read_b64_tr_b16 v[156:157], v7 offset:10240\n\t"
    "v_mfma_f32_16x16x32_bf16 v[132:135], v[192:195], v[164:167], v[132:135]\n\t" "ds_read_b64_tr_b16 v[158:159], v7 offset:10752\n\t"
    "v_mfma_f32_16x16x32_bf16 v[136:139], v[188:191], v[168:171], v[136:139]\n\t" "ds_read_b64_tr_b16 v[160:161], v8 offset:10240\n\t"
    "v_mfma_f32_16x16x32_bf16 v[140:143], v[192:195], v[168:171], v[140:143]\n\t" "ds_read_b64_tr_b16 v[162:163], v8 offset:10752\n\t" "s_waitcnt lgkmcnt(4)\n\t"
    "v_mfma_f32_16x16x32_bf16 v[48:51], v[172:175], v[196:199], v[48:51]\n\t" "ds_read_b64_tr_b16 v[164:165], v9 offset:10240\n\t" "v_add_u32 v12, 0x10000, v12\n\t"
    "v_mfma_f32_16x16x32_bf16 v[52:55], v[172:175], v[200:203], v[52:55]\n\t" "ds_read_b64_tr_b16 v[166:167], v9 offset:10752\n\t" "s_add_u32 m0, %[sl1], 32768\n\t"
    "v_mfma_f32_16x16x32_bf16 v[152:155], v[232:235], v[172:175], v[152:155]\n\t" "global_load_lds_dwordx4 v12, %[so]\n\t" "v_add_u32 v12, 0x10000, v12\n\t"
    "v_mfma_f32_16x16x32_bf16 v[56:59], v[176:179], v[196:199], v[56:59]\n\t" "ds_read_b64_tr_b16 v[168:169], v10 offset:10240\n\t" "v_mfma_f32_4x4x4_16b_bf16 v[204:207], v[220:221], a[224:225], 0\n\t"
    "v_mfma_f32_4x4x4_16b_bf16 v[208:211], v[222:223], a[226:227], 0\n\t" "v_mfma_f32_4x4x4_16b_bf16 v[212:215], v[220:221], a[228:229], 0\n\t"
    "v_mfma_f32_4x4x4_16b_bf16 v[216:219], v[222:223], a[230:231], 0\n\t" "v_mfma_f32_16x16x32_bf16 v[60:63], v[176:179], v[200:203], v[60:63]\n\t" "ds_read_b64_tr_b16 v[170:171], v10 offset:10752\n\t"
    "v_mfma_f32_16x16x32_bf16 v[64:67], v[180:183], v[196:199], v[64:67]\n\t" "ds_read_b64_tr_b16 v[172:173], v7 offset:12288\n\t" "v_mfma_f32_16x16x32_bf16 v[68:71], v[180:183], v[200:203], v[68:71]\n\t"
    "ds_read_b64_tr_b16 v[174:175], v7 offset:12800\n\t" "v_cvt_pk_bf16_f32 v188, v204, v205\n\t" "v_mfma_f32_16x16x32_bf16 v[72:75], v[184:187], v[196:199], v[72:75]\n\t"
    "ds_read_b64_tr_b16 v[176:177], v8 offset:12288\n\t" "v_cvt_pk_bf16_f32 v189, v206, v207\n\t" "v_mfma_f32_16x16x32_bf16 v[76:79], v[184:187], v[200:203], v[76:79]\n\t"
    "ds_read_b64_tr_b16 v[178:179], v8 offset:12800\n\t" "s_waitcnt lgkmcnt(4)\n\t" "v_mfma_f32_16x16x32_bf16 v[80:83], v[156:159], v[196:199], v[80:83]\n\t"
    "ds_read_b64_tr_b16 v[180:181], v9 offset:12288\n\t" "v_cvt_pk_bf16_f32 v190, v208, v209\n\t" "v_mfma_f32_16x16x32_bf16 v[84:87], v[156:159], v[200:203], v[84:87]\n\t"
    "ds_read_b64_tr_b16 v[182:183], v9 offset:12800\n\t" "v_cvt_pk_bf16_f32 v191, v210, v211\n\t" "v_mfma_f32_16x16x32_bf16 v[144:147], v[156:159], v[232:235], v[144:147]\n\t"
    "ds_read_b128 v[224:227], v2 offset:7424\n\t" "s_add_u32 m0, %[sl1], 36864\n\t" "v_mfma_f32_16x16x32_bf16 v[88:91], v[160:163], v[196:199], v[88:91]\n\t"
    "ds_read_b64_tr_b16 v[184:185], v10 offset:12288\n\t" "v_cvt_pk_bf16_f32 v192, v212, v213\n\t" "v_mfma_f32_16x16x32_bf16 v[92:95], v[160:163], v[200:203], v[92:95]\n\t"
    "ds_read_b64_tr_b16 v[186:187], v10 offset:12800\n\t" "v_cvt_pk_bf16_f32 v193, v214, v215\n\t" "v_mfma_f32_16x16x32_bf16 v[96:99], v[164:167], v[196:199], v[96:99]\n\t" "s_waitcnt vmcnt(12)\n\t"
    "s_barrier\n\t" "ds_read_b64_tr_b16 v[156:157], v7 offset:20480\n\t" "v_mfma_f32_16x16x32_bf16 v[100:103], v[164:167], v[200:203], v[100:103]\n\t" "ds_read_b64_tr_b16 v[158:159], v7 offset:20992\n\t"
    "v_cvt_pk_bf16_f32 v194, v216, v217\n\t" "v_mfma_f32_16x16x32_bf16 v[104:107], v[168:171], v[196:199], v[104:107]\n\t" "ds_read_b64_tr_b16 v[160:161], v8 offset:20480\n\t"
    "v_cvt_pk_bf16_f32 v195, v218, v219\n\t" "v_mfma_f32_16x16x32_bf16 v[108:111], v[168:171], v[200:203], v[108:111]\n\t" "ds_read_b64_tr_b16 v[162:163], v8 offset:20992\n\t" "s_waitcnt lgkmcnt(4)\n\t"
    "v_mfma_f32_16x16x32_bf16 v[112:115], v[196:199], v[172:175], v[112:115]\n\t" "ds_read_b64_tr_b16 v[164:165], v9 offset:20480\n\t" "global_load_lds_dwordx4 v12, %[so]\n\t"
    "v_mfma_f32_16x16x32_bf16 v[116:119], v[200:203], v[172:175], v[116:119]\n\t" "ds_read_b64_tr_b16 v[166:167], v9 offset:20992\n\t" "v_add_u32 v12, 0x10000, v12\n\t"
    "v_mfma_f32_16x16x32_bf16 v[148:151], v[232:235], v[172:175], v[148:151]\n\t" "ds_read_b128 v[228:231], v1 offset:28672\n\t" "s_add_u32 m0, %[sl1], 40960\n\t"
    "v_mfma_f32_16x16x32_bf16 v[120:123], v[196:199], v[176:179], v[120:123]\n\t" "ds_read_b64_tr_b16 v[168:169], v10 offset:20480\n\t" "global_load_lds_dwordx4 v12, %[so]\n\t"
    "v_mfma_f32_16x16x32_bf16 v[124:127], v[200:203], v[176:179], v[124:127]\n\t" "ds_read_b64_tr_b16 v[170:171], v10 offset:20992\n\t"
    "v_mfma_f32_16x16x32_bf16 v[128:131], v[196:199], v[180:183], v[128:131]\n\t" "ds_read_b64_tr_b16 v[172:173], v7 offset:22528\n\t"
    "v_mfma_f32_16x16x32_bf16 v[132:135], v[200:203], v[180:183], v[132:135]\n\t" "ds_read_b64_tr_b16 v[174:175], v7 offset:23040\n\t"
    "v_mfma_f32_16x16x32_bf16 v[136:139], v[196:199], v[184:187], v[136:139]\n\t" "ds_read_b64_tr_b16 v[176:177], v8 offset:22528\n\t"
    "v_mfma_f32_16x16x32_bf16 v[140:143], v[200:203], v[184:187], v[140:143]\n\t" "ds_read_b64_tr_b16 v[178:179], v8 offset:23040\n\t" "s_waitcnt lgkmcnt(4)\n\t"
    "v_mfma_f32_16x16x32_bf16 v[48:51], v[156:159], v[188:191], v[48:51]\n\t" "ds_read_b64_tr_b16 v[180:181], v9 offset:22528\n\t" "v_add_u32 v12, 0xfffa0000, v12\n\t"
    "v_mfma_f32_16x16x32_bf16 v[52:55], v[156:159], v[192:195], v[52:55]\n\t" "ds_read_b64_tr_b16 v[182:183], v9 offset:23040\n\t" "s_add_u32 m0, %[sl1], 16384\n\t"
    "v_mfma_f32_16x16x32_bf16 v[152:155], v[228:231], v[156:159], v[152:155]\n\t" "global_load_lds_dwordx4 v12, %[so]\n\t" "v_add_u32 v12, 0x70000, v12\n\t"
    "v_mfma_f32_16x16x32_bf16 v[56:59], v[160:163], v[188:191], v[56:59]\n\t" "ds_read_b64_tr_b16 v[184:185], v10 offset:22528\n\t" "s_add_u32 m0, %[sl1], 45056\n\t"
    "v_mfma_f32_4x4x4_16b_bf16 v[204:207], v[224:225], a[232:233], 0\n\t" "v_mfma_f32_4x4x4_16b_bf16 v[208:211], v[226:227], a[234:235], 0\n\t"
    "v_mfma_f32_4x4x4_16b_bf16 v[212:215], v[224:225], a[236:237], 0\n\t" "v_mfma_f32_4x4x4_16b_bf16 v[216:219], v[226:227], a[238:239], 0\n\t"
    "v_mfma_f32_16x16x32_bf16 v[60:63], v[160:163], v[192:195], v[60:63]\n\t" "ds_read_b64_tr_b16 v[186:187], v10 offset:23040\n\t"
    "v_mfma_f32_16x16x32_bf16 v[64:67], v[164:167], v[188:191], v[64:67]\n\t" "ds_read_b64_tr_b16 v[156:157], v7 offset:24576\n\t" "global_load_lds_dwordx4 v12, %[so]\n\t"
    "v_mfma_f32_16x16x32_bf16 v[68:71], v[164:167], v[192:195], v[68:71]\n\t" "ds_read_b64_tr_b16 v[158:159], v7 offset:25088\n\t" "v_cvt_pk_bf16_f32 v196, v204, v205\n\t"
    "v_mfma_f32_16x16x32_bf16 v[72:75], v[168:171], v[188:191], v[72:75]\n\t" "ds_read_b64_tr_b16 v[160:161], v8 offset:24576\n\t" "v_cvt_pk_bf16_f32 v197, v206, v207\n\t"
    "v_mfma_f32_16x16x32_bf16 v[76:79], v[168:171], v[192:195], v[76:79]\n\t" "ds_read_b64_tr_b16 v[162:163], v8 offset:25088\n\t" "s_waitcnt lgkmcnt(4)\n\t"
    "v_mfma_f32_16x16x32_bf16 v[80:83], v[172:175], v[188:191], v[80:83]\n\t" "ds_read_b64_tr_b16 v[164:165], v9 offset:24576\n\t" "v_cvt_pk_bf16_f32 v198, v208, v209\n\t"
    "v_mfma_f32_16x16x32_bf16 v[84:87], v[172:175], v[192:195], v[84:87]\n\t" "ds_read_b64_tr_b16 v[166:167], v9 offset:25088\n\t" "v_cvt_pk_bf16_f32 v199, v210, v211\n\t"
    "v_mfma_f32_16x16x32_bf16 v[144:147], v[172:175], v[228:231], v[144:147]\n\t" "ds_read_b128 v[220:223], v2 offset:7680\n\t" "v_add_u32 v12, 0x10000, v12\n\t"
    "v_mfma_f32_16x16x32_bf16 v[88:91], v[176:179], v[188:191], v[88:91]\n\t" "ds_read_b64_tr_b16 v[168:169], v10 offset:24576\n\t" "v_cvt_pk_bf16_f32 v200, v212, v213\n\t"
    "v_mfma_f32_16x16x32_bf16 v[92:95], v[176:179], v[192:195], v[92:95]\n\t" "ds_read_b64_tr_b16 v[170:171], v10 offset:25088\n\t" "v_cvt_pk_bf16_f32 v201, v214, v215\n\t"
    "v_mfma_f32_16x16x32_bf16 v[96:99], v[180:183], v[188:191], v[96:99]\n\t" "ds_read_b64_tr_b16 v[172:173], v7 offset:32768\n\t" "s_add_u32 m0, %[sl1], 49152\n\t"
    "v_mfma_f32_16x16x32_bf16 v[100:103], v[180:183], v[192:195], v[100:103]\n\t" "ds_read_b64_tr_b16 v[174:175], v7 offset:33280\n\t" "v_cvt_pk_bf16_f32 v202, v216, v217\n\t"
    "v_mfma_f32_16x16x32_bf16 v[104:107], v[184:187], v[188:191], v[104:107]\n\t" "ds_read_b64_tr_b16 v[176:177], v8 offset:32768\n\t" "v_cvt_pk_bf16_f32 v203, v218, v219\n\t"
    "v_mfma_f32_16x16x32_bf16 v[108:111], v[184:187], v[192:195], v[108:111]\n\t" "ds_read_b64_tr_b16 v[178:179], v8 offset:33280\n\t" "s_waitcnt lgkmcnt(4)\n\t"
    "v_mfma_f32_16x16x32_bf16 v[112:115], v[188:191], v[156:159], v[112:115]\n\t" "ds_read_b64_tr_b16 v[180:181], v9 offset:32768\n\t" "global_load_lds_dwordx4 v12, %[so]\n\t"
    "v_mfma_f32_16x16x32_bf16 v[116:119], v[192:195], v[156:159], v[116:119]\n\t" "ds_read_b64_tr_b16 v[182:183], v9 offset:33280\n\t" "v_add_u32 v12, 0x10000, v12\n\t"
    "v_mfma_f32_16x16x32_bf16 v[148:151], v[228:231], v[156:159], v[148:151]\n\t" "ds_read_b128 v[232:235], v1 offset:29696\n\t" "s_add_u32 m0, %[sl1], 53248\n\t"
    "v_mfma_f32_16x16x32_bf16 v[120:123], v[188:191], v[160:163], v[120:123]\n\t" "ds_read_b64_tr_b16 v[184:185], v10 offset:32768\n\t" "global_load_lds_dwordx4 v12, %[so]\n\t"
    "v_mfma_f32_16x16x32_bf16 v[124:127], v[192:195], v[160:163], v[124:127]\n\t" "ds_read_b64_tr_b16 v[186:187], v10 offset:33280\n\t" "v_add_u32 v16, 0x70000, v16\n\t"
    "v_mfma_f32_16x16x32_bf16 v[128:131], v[188:191], v[164:167], v[128:131]\n\t" "ds_read_b64_tr_b16 v[156:157], v7 offset:34816\n\t"
    "v_mfma_f32_16x16x32_bf16 v[132:135], v[192:195], v[164:167], v[132:135]\n\t" "ds_read_b64_tr_b16 v[158:159], v7 offset:35328\n\t" "s_mov_b64 exec, %[tm]\n\t" "global_load_dword a64, v16, %[pf]\n\t"
    "s_mov_b64 exec, -1\n\t" "v_mfma_f32_16x16x32_bf16 v[136:139], v[188:191], v[168:171], v[136:139]\n\t" "ds_read_b64_tr_b16 v[160:161], v8 offset:34816\n\t"
    "v_mfma_f32_16x16x32_bf16 v[140:143], v[192:195], v[168:171], v[140:143]\n\t" "ds_read_b64_tr_b16 v[162:163], v8 offset:35328\n\t" "s_waitcnt lgkmcnt(4)\n\t"
    "v_mfma_f32_16x16x32_bf16 v[48:51], v[172:175], v[196:199], v[48:51]\n\t" "ds_read_b64_tr_b16 v[164:165], v9 offset:34816\n\t" "v_add_u32 v12, 0x10000, v12\n\t"
    "v_mfma_f32_16x16x32_bf16 v[52:55], v[172:175], v[200:203], v[52:55]\n\t" "ds_read_b64_tr_b16 v[166:167], v9 offset:35328\n\t" "s_add_u32 m0, %[sl1], 57344\n\t"
    "v_mfma_f32_16x16x32_bf16 v[152:155], v[232:235], v[172:175], v[152:155]\n\t" "global_load_lds_dwordx4 v12, %[so]\n\t" "v_add_u32 v12, 0x10000, v12\n\t"
    "v_mfma_f32_16x16x32_bf16 v[56:59], v[176:179], v[196:199], v[56:59]\n\t" "ds_read_b64_tr_b16 v[168:169], v10 offset:34816\n\t" "v_mfma_f32_4x4x4_16b_bf16 v[204:207], v[220:221], a[240:241], 0\n\t"
    "v_mfma_f32_4x4x4_16b_bf16 v[208:211], v[222:223], a[242:243], 0\n\t" "v_mfma_f32_4x4x4_16b_bf16 v[212:215], v[220:221], a[244:245], 0\n\t"
    "v_mfma_f32_4x4x4_16b_bf16 v[216:219], v[222:223], a[246:247], 0\n\t" "v_mfma_f32_16x16x32_bf16 v[60:63], v[176:179], v[200:203], v[60:63]\n\t" "ds_read_b64_tr_b16 v[170:171], v10 offset:35328\n\t"
    "v_mfma_f32_16x16x32_bf16 v[64:67], v[180:183], v[196:199], v[64:67]\n\t" "ds_read_b64_tr_b16 v[172:173], v7 offset:36864\n\t" "v_mfma_f32_16x16x32_bf16 v[68:71], v[180:183], v[200:203], v[68:71]\n\t"
    "ds_read_b64_tr_b16 v[174:175], v7 offset:37376\n\t" "v_cvt_pk_bf16_f32 v188, v204, v205\n\t" "v_mfma_f32_16x16x32_bf16 v[72:75], v[184:187], v[196:199], v[72:75]\n\t"
    "ds_read_b64_tr_b16 v[176:177], v8 offset:36864\n\t" "v_cvt_pk_bf16_f32 v189, v206, v207\n\t" "v_mfma_f32_16x16x32_bf16 v[76:79], v[184:187], v[200:203], v[76:79]\n\t"
    "ds_read_b64_tr_b16 v[178:179], v8 offset:37376\n\t" "s_waitcnt lgkmcnt(4)\n\t" "v_mfma_f32_16x16x32_bf16 v[80:83], v[156:159], v[196:199], v[80:83]\n\t"
    "ds_read_b64_tr_b16 v[180:181], v9 offset:36864\n\t" "v_cvt_pk_bf16_f32 v190, v208, v209\n\t" "v_mfma_f32_16x16x32_bf16 v[84:87], v[156:159], v[200:203], v[84:87]\n\t"
    "ds_read_b64_tr_b16 v[182:183], v9 offset:37376\n\t" "v_cvt_pk_bf16_f32 v191, v210, v211\n\t" "v_mfma_f32_16x16x32_bf16 v[144:147], v[156:159], v[232:235], v[144:147]\n\t"
    "ds_read_b128 v[224:227], v2 offset:7936\n\t" "s_add_u32 m0, %[sl1], 61440\n\t" "v_mfma_f32_16x16x32_bf16 v[88:91], v[160:163], v[196:199], v[88:91]\n\t"
    "ds_read_b64_tr_b16 v[184:185], v10 offset:36864\n\t" "v_cvt_pk_bf16_f32 v192, v212, v213\n\t" "v_mfma_f32_16x16x32_bf16 v[92:95], v[160:163], v[200:203], v[92:95]\n\t"
    "ds_read_b64_tr_b16 v[186:187], v10 offset:37376\n\t" "v_cvt_pk_bf16_f32 v193, v214, v215\n\t" "v_mfma_f32_16x16x32_bf16 v[96:99], v[164:167], v[196:199], v[96:99]\n\t" "s_waitcnt vmcnt(13)\n\t"
    "s_barrier\n\t" "ds_read_b64_tr_b16 v[156:157], v3 offset:51200\n\t" "v_mfma_f32_16x16x32_bf16 v[100:103], v[164:167], v[200:203], v[100:103]\n\t" "ds_read_b64_tr_b16 v[158:159], v3 offset:51712\n\t"
    "v_cvt_pk_bf16_f32 v194, v216, v217\n\t" "v_mfma_f32_16x16x32_bf16 v[104:107], v[168:171], v[196:199], v[104:107]\n\t" "ds_read_b64_tr_b16 v[160:161], v3 offset:53248\n\t"
    "v_cvt_pk_bf16_f32 v195, v218, v219\n\t" "v_mfma_f32_16x16x32_bf16 v[108:111], v[168:171], v[200:203], v[108:111]\n\t" "ds_read_b64_tr_b16 v[162:163], v3 offset:53760\n\t" "s_waitcnt lgkmcnt(4)\n\t"
    "v_mfma_f32_16x16x32_bf16 v[112:115], v[196:199], v[172:175], v[112:115]\n\t" "ds_read_b64_tr_b16 v[164:165], v3 offset:49152\n\t" "global_load_lds_dwordx4 v12, %[so]\n\t"
    "v_mfma_f32_16x16x32_bf16 v[116:119], v[200:203], v[172:175], v[116:119]\n\t" "ds_read_b64_tr_b16 v[166:167], v3 offset:49664\n\t" "v_add_u32 v12, 0x10000, v12\n\t"
    "v_mfma_f32_16x16x32_bf16 v[148:151], v[232:235], v[172:175], v[148:151]\n\t" "ds_read_b128 v[228:231], v1 offset:30720\n\t" "s_add_u32 m0, %[sl1], 65536\n\t"
    "v_mfma_f32_16x16x32_bf16 v[120:123], v[196:199], v[176:179], v[120:123]\n\t" "ds_read_b64_tr_b16 v[168:169], v3 offset:63488\n\t" "ds_read_b128 v[232:235], v1 offset:31744\n\t"
    "v_mfma_f32_16x16x32_bf16 v[124:127], v[200:203], v[176:179], v[124:127]\n\t" "ds_read_b64_tr_b16 v[170:171], v3 offset:64000\n\t" "global_load_lds_dwordx4 v12, %[so]\n\t"
    "v_mfma_f32_16x16x32_bf16 v[128:131], v[196:199], v[180:183], v[128:131]\n\t" "ds_read_b64_tr_b16 v[172:173], v7 offset:0\n\t" "v_lshlrev_b32 v18, 4, v13\n\t"
    "v_mfma_f32_16x16x32_bf16 v[132:135], v[200:203], v[180:183], v[132:135]\n\t" "ds_read_b64_tr_b16 v[174:175], v7 offset:512\n\t" "global_load_dword a64, v18, %[cd]\n\t"
    "v_mfma_f32_16x16x32_bf16 v[136:139], v[196:199], v[184:187], v[136:139]\n\t" "ds_read_b64_tr_b16 v[176:177], v3 offset:61440\n\t"
    "v_mfma_f32_16x16x32_bf16 v[140:143], v[200:203], v[184:187], v[140:143]\n\t" "ds_read_b64_tr_b16 v[178:179], v3 offset:61952\n\t" "s_waitcnt lgkmcnt(4)\n\t"
    "v_mfma_f32_16x16x32_bf16 v[80:83], v[156:159], v[188:191], v[80:83]\n\t" "ds_read_b64_tr_b16 v[180:181], v4 offset:49152\n\t" "v_add_u32 v12, 0x70000, v12\n\t"
    "v_mfma_f32_16x16x32_bf16 v[84:87], v[156:159], v[192:195], v[84:87]\n\t" "ds_read_b64_tr_b16 v[182:183], v4 offset:49664\n\t" "s_add_u32 m0, %[sl1], 94208\n\t"
    "v_mfma_f32_16x16x32_bf16 v[144:147], v[156:159], v[228:231], v[144:147]\n\t" "global_load_lds_dwordx4 v12, %[so]\n\t" "v_add_u32 v12, 0x10000, v12\n\t"
    "v_mfma_f32_16x16x32_bf16 v[112:115], v[188:191], v[160:163], v[112:115]\n\t" "ds_read_b64_tr_b16 v[184:185], v5 offset:49152\n\t" "v_mfma_f32_4x4x4_16b_bf16 v[204:207], v[224:225], a[248:249], 0\n\t"
    "v_mfma_f32_4x4x4_16b_bf16 v[208:211], v[226:227], a[250:251], 0\n\t" "v_mfma_f32_4x4x4_16b_bf16 v[212:215], v[224:225], a[252:253], 0\n\t"
    "v_mfma_f32_4x4x4_16b_bf16 v[216:219], v[226:227], a[254:255], 0\n\t" "v_mfma_f32_16x16x32_bf16 v[116:119], v[192:195], v[160:163], v[116:119]\n\t" "ds_read_b64_tr_b16 v[186:187], v5 offset:49664\n\t"
    "v_mfma_f32_16x16x32_bf16 v[148:151], v[228:231], v[160:163], v[148:151]\n\t" "v_mfma_f32_16x16x32_bf16 v[48:51], v[164:167], v[188:191], v[48:51]\n\t"
    "ds_read_b64_tr_b16 v[156:157], v6 offset:49152\n\t" "v_cvt_pk_bf16_f32 v196, v204, v205\n\t" "v_mfma_f32_16x16x32_bf16 v[52:55], v[164:167], v[192:195], v[52:55]\n\t"
    "ds_read_b64_tr_b16 v[158:159], v6 offset:49664\n\t" "v_cvt_pk_bf16_f32 v197, v206, v207\n\t" "v_mfma_f32_16x16x32_bf16 v[152:155], v[228:231], v[164:167], v[152:155]\n\t"
    "s_add_u32 m0, %[sl1], 98304\n\t" "v_mfma_f32_16x16x32_bf16 v[144:147], v[168:171], v[232:235], v[144:147]\n\t" "ds_read_b64_tr_b16 v[160:161], v4 offset:51200\n\t"
    "ds_read_b64_tr_b16 v[162:163], v4 offset:51712\n\t" "v_cvt_pk_bf16_f32 v198, v208, v209\n\t" "s_waitcnt lgkmcnt(4)\n\t" "v_mfma_f32_16x16x32_bf16 v[148:151], v[232:235], v[172:175], v[148:151]\n\t"
    "ds_read_b64_tr_b16 v[164:165], v5 offset:51200\n\t" "ds_read_b64_tr_b16 v[166:167], v5 offset:51712\n\t" "v_cvt_pk_bf16_f32 v199, v210, v211\n\t"
    "v_mfma_f32_16x16x32_bf16 v[152:155], v[232:235], v[176:179], v[152:155]\n\t" "ds_read_b64_tr_b16 v[168:169], v6 offset:51200\n\t" "ds_read_b64_tr_b16 v[170:171], v6 offset:51712\n\t"
    "v_mfma_f32_16x16x32_bf16 v[56:59], v[180:183], v[188:191], v[56:59]\n\t" "ds_read_b64_tr_b16 v[172:173], v4 offset:53248\n\t" "v_cvt_pk_bf16_f32 v200, v212, v213\n\t"
    "v_mfma_f32_16x16x32_bf16 v[60:63], v[180:183], v[192:195], v[60:63]\n\t" "ds_read_b64_tr_b16 v[174:175], v4 offset:53760\n\t" "v_cvt_pk_bf16_f32 v201, v214, v215\n\t"
    "v_mfma_f32_16x16x32_bf16 v[64:67], v[184:187], v[188:191], v[64:67]\n\t" "ds_read_b64_tr_b16 v[176:177], v5 offset:53248\n\t" "global_load_lds_dwordx4 v12, %[so]\n\t"
    "v_mfma_f32_16x16x32_bf16 v[68:71], v[184:187], v[192:195], v[68:71]\n\t" "ds_read_b64_tr_b16 v[178:179], v5 offset:53760\n\t" "v_cvt_pk_bf16_f32 v202, v216, v217\n\t" "s_waitcnt lgkmcnt(4)\n\t"
    "v_mfma_f32_16x16x32_bf16 v[72:75], v[156:159], v[188:191], v[72:75]\n\t" "ds_read_b64_tr_b16 v[180:181], v6 offset:53248\n\t" "v_cvt_pk_bf16_f32 v203, v218, v219\n\t"
    "v_mfma_f32_16x16x32_bf16 v[76:79], v[156:159], v[192:195], v[76:79]\n\t" "ds_read_b64_tr_b16 v[182:183], v6 offset:53760\n\t" "v_add_u32 v12, 0x10000, v12\n\t"
    "v_mfma_f32_16x16x32_bf16 v[88:91], v[160:163], v[188:191], v[88:91]\n\t" "ds_read_b64_tr_b16 v[184:185], v3 offset:63488\n\t" "s_add_u32 m0, %[sl1], 102400\n\t"
    "v_mfma_f32_16x16x32_bf16 v[92:95], v[160:163], v[192:195], v[92:95]\n\t" "ds_read_b64_tr_b16 v[186:187], v3 offset:64000\n\t" "v_and_b32 v242, 0xffff0000, v144\n\t"
    "v_mfma_f32_16x16x32_bf16 v[96:99], v[164:167], v[188:191], v[96:99]\n\t" "ds_read_b64_tr_b16 v[156:157], v4 offset:63488\n\t" "global_load_lds_dwordx4 v12, %[so]\n\t"
    "v_mfma_f32_16x16x32_bf16 v[100:103], v[164:167], v[192:195], v[100:103]\n\t" "ds_read_b64_tr_b16 v[158:159], v4 offset:64000\n\t" "v_sub_f32 v18, v144, v242\n\t"
    "v_mfma_f32_16x16x32_bf16 v[104:107], v[168:171], v[188:191], v[104:107]\n\t" "ds_read_b64_tr_b16 v[160:161], v5 offset:63488\n\t" "v_cvt_pk_bf16_f32 v242, v242, v18\n\t"
    "v_mfma_f32_16x16x32_bf16 v[108:111], v[168:171], v[192:195], v[108:111]\n\t" "ds_read_b64_tr_b16 v[162:163], v5 offset:64000\n\t" "s_waitcnt lgkmcnt(4)\n\t"
    "v_mfma_f32_16x16x32_bf16 v[120:123], v[188:191], v[172:175], v[120:123]\n\t" "ds_read_b64_tr_b16 v[164:165], v6 offset:63488\n\t" "v_and_b32 v243, 0xffff0000, v145\n\t"
    "v_mfma_f32_16x16x32_bf16 v[124:127], v[192:195], v[172:175], v[124:127]\n\t" "ds_read_b64_tr_b16 v[166:167], v6 offset:64000\n\t" "v_sub_f32 v18, v145, v243\n\t"
    "v_mfma_f32_16x16x32_bf16 v[128:131], v[188:191], v[176:179], v[128:131]\n\t" "ds_read_b64_tr_b16 v[168:169], v7 offset:0\n\t" "v_cvt_pk_bf16_f32 v243, v243, v18\n\t"
    "v_mfma_f32_16x16x32_bf16 v[132:135], v[192:195], v[176:179], v[132:135]\n\t" "ds_read_b64_tr_b16 v[170:171], v7 offset:512\n\t" "ds_write2_b32 v34, v242, v243 offset0:0 offset1:2\n\t"
    "v_mfma_f32_16x16x32_bf16 v[136:139], v[188:191], v[180:183], v[136:139]\n\t" "ds_read_b64_tr_b16 v[172:173], v8 offset:0\n\t" "v_and_b32 v244, 0xffff0000, v146\n\t"
    "v_mfma_f32_16x16x32_bf16 v[140:143], v[192:195], v[180:183], v[140:143]\n\t" "ds_read_b64_tr_b16 v[174:175], v8 offset:512\n\t" "v_sub_f32_e64 v18, v146, v244\n\t"
    "v_mfma_f32_16x16x32_bf16 v[80:83], v[184:187], v[196:199], v[80:83]\n\t" "ds_read_b64_tr_b16 v[176:177], v9 offset:0\n\t" "v_cvt_pk_bf16_f32 v244, v244, v18\n\t"
    "v_mfma_f32_16x16x32_bf16 v[84:87], v[184:187], v[200:203], v[84:87]\n\t" "ds_read_b64_tr_b16 v[178:179], v9 offset:512\n\t" "s_waitcnt lgkmcnt(5)\n\t"
    "v_mfma_f32_16x16x32_bf16 v[88:91], v[156:159], v[196:199], v[88:91]\n\t" "ds_read_b64_tr_b16 v[180:181], v10 offset:0\n\t" "v_and_b32 v245, 0xffff0000, v147\n\t"
    "v_mfma_f32_16x16x32_bf16 v[92:95], v[156:159], v[200:203], v[92:95]\n\t" "ds_read_b64_tr_b16 v[182:183], v10 offset:512\n\t" "v_sub_f32 v18, v147, v245\n\t"
    "v_mfma_f32_16x16x32_bf16 v[96:99], v[160:163], v[196:199], v[96:99]\n\t" "ds_read_b64_tr_b16 v[184:185], v3 offset:61440\n\t" "v_cvt_pk_bf16_f32 v245, v245, v18\n\t"
    "v_mfma_f32_16x16x32_bf16 v[100:103], v[160:163], v[200:203], v[100:103]\n\t" "ds_read_b64_tr_b16 v[186:187], v3 offset:61952\n\t" "ds_write2_b32 v34, v244, v245 offset0:4 offset1:6\n\t"
    "v_mfma_f32_16x16x32_bf16 v[104:107], v[164:167], v[196:199], v[104:107]\n\t" "ds_read_b64_tr_b16 v[156:157], v4 offset:61440\n\t" "v_and_b32 v238, 0xffff0000, v148\n\t"
    "v_mfma_f32_16x16x32_bf16 v[108:111], v[164:167], v[200:203], v[108:111]\n\t" "ds_read_b64_tr_b16 v[158:159], v4 offset:61952\n\t" "v_sub_f32_e64 v18, v148, v238\n\t"
    "v_mfma_f32_16x16x32_bf16 v[112:115], v[196:199], v[168:171], v[112:115]\n\t" "ds_read_b64_tr_b16 v[160:161], v5 offset:61440\n\t" "v_cvt_pk_bf16_f32 v238, v238, v18\n\t"
    "v_mfma_f32_16x16x32_bf16 v[116:119], v[200:203], v[168:171], v[116:119]\n\t" "ds_read_b64_tr_b16 v[162:163], v5 offset:61952\n\t" "s_waitcnt lgkmcnt(5)\n\t"
    "v_mfma_f32_16x16x32_bf16 v[120:123], v[196:199], v[172:175], v[120:123]\n\t" "ds_read_b64_tr_b16 v[164:165], v6 offset:61440\n\t" "v_and_b32 v239, 0xffff0000, v149\n\t"
    "v_mfma_f32_16x16x32_bf16 v[124:127], v[200:203], v[172:175], v[124:127]\n\t" "ds_read_b64_tr_b16 v[166:167], v6 offset:61952\n\t" "v_sub_f32 v18, v149, v239\n\t"
    "v_mfma_f32_16x16x32_bf16 v[128:131], v[196:199], v[176:179], v[128:131]\n\t" "v_cvt_pk_bf16_f32 v239, v239, v18\n\t" "v_and_b32 v240, 0xffff0000, v152\n\t"
    "v_mfma_f32_16x16x32_bf16 v[132:135], v[200:203], v[176:179], v[132:135]\n\t" "v_sub_f32_e64 v18, v152, v240\n\t" "v_cvt_pk_bf16_f32 v240, v240, v18\n\t"
    "v_mfma_f32_16x16x32_bf16 v[136:139], v[196:199], v[180:183], v[136:139]\n\t" "v_and_b32 v241, 0xffff0000, v153\n\t" "v_sub_f32_e64 v18, v153, v241\n\t"
    "v_mfma_f32_16x16x32_bf16 v[140:143], v[200:203], v[180:183], v[140:143]\n\t" "v_cvt_pk_bf16_f32 v241, v241, v18\n\t" "v_mfma_f32_16x16x32_bf16 v[48:51], v[184:187], v[196:199], v[48:51]\n\t"
    "ds_write2st64_b64 v35, v[238:239], v[240:241] offset0:2 offset1:0\n\t" "v_mfma_f32_16x16x32_bf16 v[52:55], v[184:187], v[200:203], v[52:55]\n\t" "s_waitcnt lgkmcnt(1)\n\t"
    "v_mfma_f32_16x16x32_bf16 v[56:59], v[156:159], v[196:199], v[56:59]\n\t" "s_waitcnt lgkmcnt(0)\n\t" "s_barrier\n\t" "v_mfma_f32_16x16x32_bf16 v[60:63], v[156:159], v[200:203], v[60:63]\n\t"
    "ds_read2st64_b64 v[188:191], v36 offset0:1 offset1:2\n\t" "ds_read2st64_b64 v[192:195], v37 offset0:1 offset1:2\n\t" "v_mfma_f32_16x16x32_bf16 v[64:67], v[160:163], v[196:199], v[64:67]\n\t"
    "ds_read2st64_b64 v[220:223], v38 offset0:1 offset1:2\n\t" "ds_read2st64_b64 v[224:227], v39 offset0:1 offset1:2\n\t" "v_mfma_f32_16x16x32_bf16 v[68:71], v[160:163], v[200:203], v[68:71]\n\t"
    "ds_read_b64 v[228:229], v36\n\t" "ds_read_b64 v[230:231], v37\n\t" "v_mfma_f32_16x16x32_bf16 v[72:75], v[164:167], v[196:199], v[72:75]\n\t" "ds_read_b64 v[232:233], v38\n\t"
    "ds_read_b64 v[234:235], v39\n\t" "v_mfma_f32_16x16x32_bf16 v[76:79], v[164:167], v[200:203], v[76:79]\n\t" "s_waitcnt lgkmcnt(4)\n\t"
    "v_mfma_f32_16x16x16_bf16 v[84:87], v[188:189], v[248:249], v[84:87]\n\t" "s_nop 1\n\t" "v_mfma_f32_16x16x16_bf16 v[92:95], v[192:193], v[248:249], v[92:95]\n\t" "s_nop 1\n\t"
    "v_mfma_f32_16x16x16_bf16 v[100:103], v[220:221], v[248:249], v[100:103]\n\t" "s_nop 1\n\t" "v_mfma_f32_16x16x16_bf16 v[108:111], v[224:225], v[248:249], v[108:111]\n\t"
    "v_cvt_pk_bf16_f32 v164, v84, v85\n\t" "v_cvt_pk_bf16_f32 v165, v86, v87\n\t" "v_mfma_f32_16x16x16_bf16 v[144:147], v[188:189], v[250:251], v[144:147]\n\t" "v_cvt_pk_bf16_f32 v166, v92, v93\n\t"
    "v_cvt_pk_bf16_f32 v167, v94, v95\n\t" "v_mfma_f32_16x16x16_bf16 v[116:119], v[248:249], v[190:191], v[116:119]\n\t" "ds_write2_b64 v40, v[164:165], v[166:167] offset0:1 offset1:0\n\t"
    "v_cvt_pk_bf16_f32 v168, v100, v101\n\t" "v_mfma_f32_16x16x16_bf16 v[124:127], v[248:249], v[194:195], v[124:127]\n\t" "v_cvt_pk_bf16_f32 v169, v102, v103\n\t" "v_cvt_pk_bf16_f32 v170, v108, v109\n\t"
    "v_mfma_f32_16x16x16_bf16 v[132:135], v[248:249], v[222:223], v[132:135]\n\t" "v_cvt_pk_bf16_f32 v171, v110, v111\n\t" "ds_write2_b64 v41, v[168:169], v[170:171] offset0:1 offset1:0\n\t"
    "v_mfma_f32_16x16x16_bf16 v[140:143], v[248:249], v[226:227], v[140:143]\n\t" "v_cvt_pk_bf16_f32 v144, v144, v145\n\t" "v_cvt_pk_bf16_f32 v145, v146, v147\n\t"
    "v_mfma_f32_16x16x16_bf16 v[148:151], v[250:251], v[190:191], v[148:151]\n\t" "v_cvt_pk_bf16_f32 v200, v116, v117\n\t" "v_cvt_pk_bf16_f32 v201, v118, v119\n\t"
    "v_mfma_f32_16x16x16_bf16 v[80:83], v[188:189], v[246:247], v[80:83]\n\t" "v_cvt_pk_bf16_f32 v206, v124, v125\n\t" "v_cvt_pk_bf16_f32 v207, v126, v127\n\t"
    "v_mfma_f32_16x16x16_bf16 v[88:91], v[192:193], v[246:247], v[88:91]\n\t" "ds_write2_b64 v40, v[200:201], v[206:207] offset0:3 offset1:2\n\t" "v_cvt_pk_bf16_f32 v212, v132, v133\n\t"
    "v_mfma_f32_16x16x16_bf16 v[96:99], v[220:221], v[246:247], v[96:99]\n\t" "v_cvt_pk_bf16_f32 v213, v134, v135\n\t" "v_cvt_pk_bf16_f32 v218, v140, v141\n\t"
    "v_mfma_f32_16x16x16_bf16 v[104:107], v[224:225], v[246:247], v[104:107]\n\t" "v_cvt_pk_bf16_f32 v219, v142, v143\n\t" "ds_write2_b64 v41, v[212:213], v[218:219] offset0:3 offset1:2\n\t"
    "s_waitcnt lgkmcnt(4)\n\t" "v_mfma_f32_16x16x16_bf16 v[52:55], v[228:229], v[248:249], v[52:55]\n\t" "v_cvt_pk_bf16_f32 v148, v148, v149\n\t" "v_cvt_pk_bf16_f32 v149, v150, v151\n\t"
    "v_mfma_f32_16x16x16_bf16 v[60:63], v[230:231], v[248:249], v[60:63]\n\t" "ds_write2_b64 v42, v[144:145], v[148:149] offset0:0 offset1:2\n\t" "v_cvt_pk_bf16_f32 v156, v80, v81\n\t"
    "v_mfma_f32_16x16x16_bf16 v[68:71], v[232:233], v[248:249], v[68:71]\n\t" "v_cvt_pk_bf16_f32 v157, v82, v83\n\t" "v_cvt_pk_bf16_f32 v158, v88, v89\n\t"
    "v_mfma_f32_16x16x16_bf16 v[76:79], v[234:235], v[248:249], v[76:79]\n\t" "v_cvt_pk_bf16_f32 v159, v90, v91\n\t" "v_cvt_pk_bf16_f32 v160, v96, v97\n\t"
    "v_mfma_f32_16x16x16_bf16 v[48:51], v[228:229], v[246:247], v[48:51]\n\t" "v_cvt_pk_bf16_f32 v161, v98, v99\n\t" "v_cvt_pk_bf16_f32 v162, v104, v105\n\t"
    "v_mfma_f32_16x16x16_bf16 v[56:59], v[230:231], v[246:247], v[56:59]\n\t" "v_cvt_pk_bf16_f32 v163, v106, v107\n\t" "s_waitcnt lgkmcnt(0)\n\t" "s_barrier\n\t"
    "v_mfma_f32_16x16x16_bf16 v[64:67], v[232:233], v[246:247], v[64:67]\n\t" "ds_read_b128 v[144:147], v43 offset:0\n\t" "ds_read_b128 v[148:151], v43 offset:2048\n\t"
    "v_mfma_f32_16x16x16_bf16 v[72:75], v[234:235], v[246:247], v[72:75]\n\t" "ds_read_b64 v[196:197], v43 offset:16\n\t" "ds_read_b64 v[202:203], v43 offset:24\n\t"
    "v_mfma_f32_16x16x16_bf16 v[112:115], v[246:247], v[190:191], v[112:115]\n\t" "ds_read_b64 v[208:209], v43 offset:2064\n\t" "ds_read_b64 v[214:215], v43 offset:2072\n\t"
    "v_mfma_f32_16x16x16_bf16 v[120:123], v[246:247], v[194:195], v[120:123]\n\t" "v_cvt_pk_bf16_f32 v180, v52, v53\n\t" "v_cvt_pk_bf16_f32 v181, v54, v55\n\t"
    "v_mfma_f32_16x16x16_bf16 v[128:131], v[246:247], v[222:223], v[128:131]\n\t" "v_cvt_pk_bf16_f32 v182, v60, v61\n\t" "v_cvt_pk_bf16_f32 v183, v62, v63\n\t"
    "v_mfma_f32_16x16x16_bf16 v[136:139], v[246:247], v[226:227], v[136:139]\n\t" "v_cvt_pk_bf16_f32 v184, v68, v69\n\t" "v_cvt_pk_bf16_f32 v185, v70, v71\n\t"
    "v_mfma_f32_16x16x32_bf16 v[80:83], v[156:159], v[180:183], 0\n\t" "v_cvt_pk_bf16_f32 v186, v76, v77\n\t" "v_cvt_pk_bf16_f32 v187, v78, v79\n\t"
    "v_mfma_f32_16x16x32_bf16 v[84:87], v[164:167], v[180:183], 0\n\t" "v_cvt_pk_bf16_f32 v172, v48, v49\n\t" "v_cvt_pk_bf16_f32 v173, v50, v51\n\t"
    "v_mfma_f32_16x16x32_bf16 v[88:91], v[160:163], v[184:187], 0\n\t" "v_cvt_pk_bf16_f32 v174, v56, v57\n\t" "v_cvt_pk_bf16_f32 v175, v58, v59\n\t"
    "v_mfma_f32_16x16x32_bf16 v[92:95], v[168:171], v[184:187], 0\n\t" "v_cvt_pk_bf16_f32 v176, v64, v65\n\t" "v_cvt_pk_bf16_f32 v177, v66, v67\n\t" "s_waitcnt lgkmcnt(4)\n\t"
    "v_mfma_f32_16x16x32_bf16 v[96:99], v[144:147], v[172:175], 0\n\t" "v_cvt_pk_bf16_f32 v178, v72, v73\n\t" "v_cvt_pk_bf16_f32 v179, v74, v75\n\t"
    "v_mfma_f32_16x16x32_bf16 v[100:103], v[156:159], v[172:175], 0\n\t" "v_cvt_pk_bf16_f32 v198, v112, v113\n\t" "v_cvt_pk_bf16_f32 v199, v114, v115\n\t"
    "v_mfma_f32_16x16x32_bf16 v[104:107], v[148:151], v[176:179], 0\n\t" "v_cvt_pk_bf16_f32 v204, v120, v121\n\t" "v_cvt_pk_bf16_f32 v205, v122, v123\n\t"
    "v_mfma_f32_16x16x32_bf16 v[108:111], v[160:163], v[176:179], 0\n\t" "v_cvt_pk_bf16_f32 v210, v128, v129\n\t" "v_cvt_pk_bf16_f32 v211, v130, v131\n\t" "v_cvt_pk_bf16_f32 v216, v136, v137\n\t"
    "v_cvt_pk_bf16_f32 v217, v138, v139\n\t" "v_cndmask_b32 v48, v80, v81, s[50:51]\n\t" "v_cndmask_b32 v64, v84, v85, s[50:51]\n\t" "v_cndmask_b32 v49, v82, v83, s[50:51]\n\t"
    "v_cndmask_b32 v65, v86, v87, s[50:51]\n\t" "v_cndmask_b32 v48, v48, v64, s[52:53]\n\t" "v_cndmask_b32 v49, v49, v65, s[54:55]\n\t" "v_mul_f32 v48, v48, v30\n\t" "v_mul_f32 v49, v49, v31\n\t"
    "v_max_f32 v56, v48, v49\n\t" "v_cndmask_b32 v50, v88, v89, s[50:51]\n\t" "v_cndmask_b32 v64, v92, v93, s[50:51]\n\t" "v_cndmask_b32 v51, v90, v91, s[50:51]\n\t"
    "v_cndmask_b32 v65, v94, v95, s[50:51]\n\t" "v_cndmask_b32 v50, v50, v64, s[52:53]\n\t" "v_cndmask_b32 v51, v51, v65, s[54:55]\n\t" "v_mul_f32 v50, v50, v30\n\t" "v_mul_f32 v51, v51, v31\n\t"
    "v_max_f32_e64 v57, v50, v51\n\t" "v_cndmask_b32 v52, v96, v97, s[50:51]\n\t" "v_cndmask_b32 v64, v100, v101, s[50:51]\n\t" "v_cndmask_b32 v53, v98, v99, s[50:51]\n\t"
    "v_cndmask_b32 v65, v102, v103, s[50:51]\n\t" "v_cndmask_b32 v52, v52, v64, s[52:53]\n\t" "v_cndmask_b32 v53, v53, v65, s[54:55]\n\t" "v_fma_f32 v52, v52, v28, v32\n\t"
    "v_fma_f32 v53, v53, v29, v33\n\t" "v_max_f32_e64 v58, v52, v53\n\t" "v_cndmask_b32 v54, v104, v105, s[50:51]\n\t" "v_cndmask_b32 v64, v108, v109, s[50:51]\n\t"
    "v_cndmask_b32 v55, v106, v107, s[50:51]\n\t" "v_cndmask_b32 v65, v110, v111, s[50:51]\n\t" "v_cndmask_b32 v54, v54, v64, s[52:53]\n\t" "v_cndmask_b32 v55, v55, v65, s[54:55]\n\t"
    "v_fma_f32 v54, v54, v28, v32\n\t" "v_fma_f32 v55, v55, v29, v33\n\t" "v_max_f32 v59, v54, v55\n\t" "v_permlane16_swap_b32 v56, v57\n\t" "s_nop 0\n\t" "v_permlane16_swap_b32 v58, v59\n\t"
    "v_max_f32 v56, v56, v57\n\t" "v_max_f32 v58, v58, v59\n\t" "s_nop 0\n\t" "s_nop 0\n\t" "v_permlane32_swap_b32 v56, v58\n\t" "v_max_f32 v56, v56, v58\n\t" "v_mov_b32 v58, v56\n\t" "s_nop 0\n\t"
    "s_nop 0\n\t" "v_permlane32_swap_b32 v56, v58\n\t" "v_mov_b32 v57, v56\n\t" "v_mov_b32 v59, v58\n\t" "s_nop 0\n\t" "v_permlane16_swap_b32 v56, v57\n\t" "v_permlane16_swap_b32 v58, v59\n\t"
    "v_sub_f32 v48, v48, v56\n\t" "v_sub_f32 v49, v49, v56\n\t" "v_sub_f32 v50, v50, v57\n\t" "v_sub_f32 v51, v51, v57\n\t" "v_sub_f32 v52, v52, v58\n\t" "v_sub_f32 v53, v53, v58\n\t"
    "v_sub_f32 v54, v54, v59\n\t" "v_sub_f32 v55, v55, v59\n\t" "v_exp_f32 v48, v48\n\t" "v_exp_f32 v49, v49\n\t" "v_exp_f32 v50, v50\n\t" "v_exp_f32 v51, v51\n\t" "v_exp_f32 v52, v52\n\t"
    "v_exp_f32 v53, v53\n\t" "v_exp_f32 v54, v54\n\t" "v_exp_f32 v55, v55\n\t" "v_add_f32 v56, v48, v49\n\t" "v_add_f32 v57, v50, v51\n\t" "v_add_f32 v58, v52, v53\n\t" "v_add_f32 v59, v54, v55\n\t"
    "v_permlane16_swap_b32 v56, v57\n\t" "v_mul_f32 v48, v48, v26\n\t" "v_permlane16_swap_b32 v58, v59\n\t" "v_add_f32 v56, v56, v57\n\t" "v_add_f32 v58, v58, v59\n\t" "v_mul_f32 v49, v49, v27\n\t"
    "v_mul_f32 v50, v50, v26\n\t" "v_permlane32_swap_b32 v56, v58\n\t" "v_add_f32 v56, v56, v58\n\t" "v_rcp_f32 v56, v56\n\t" "v_mul_f32 v51, v51, v27\n\t" "v_mov_b32 v58, v56\n\t"
    "v_mul_f32 v52, v52, v24\n\t" "v_mul_f32 v53, v53, v25\n\t" "v_permlane32_swap_b32 v56, v58\n\t" "v_mov_b32 v57, v56\n\t" "v_mov_b32 v59, v58\n\t" "v_mul_f32 v54, v54, v24\n\t"
    "v_permlane16_swap_b32 v56, v57\n\t" "v_permlane16_swap_b32 v58, v59\n\t" "v_mul_f32 v55, v55, v25\n\t" "s_waitcnt lgkmcnt(0)\n\t" "v_mul_f32 v48, v48, v56\n\t" "v_mul_f32_e64 v49, v49, v56\n\t"
    "v_cvt_pk_bf16_f32 v48, v48, v49\n\t" "v_perm_b32 v112, v48, v48, v20\n\t" "v_perm_b32 v113, v48, v48, v21\n\t" "v_perm_b32 v114, v48, v48, v22\n\t" "v_perm_b32 v115, v48, v48, v23\n\t"
    "v_mul_f32 v50, v50, v57\n\t" "v_mul_f32 v51, v51, v57\n\t" "v_cvt_pk_bf16_f32 v50, v50, v51\n\t" "v_perm_b32 v116, v50, v50, v20\n\t" "v_perm_b32 v117, v50, v50, v21\n\t"
    "v_perm_b32 v118, v50, v50, v22\n\t" "v_perm_b32 v119, v50, v50, v23\n\t" "v_mfma_f32_16x16x32_bf16 v[80:83], v[198:201], v[112:115], 0\n\t"
    "v_mfma_f32_16x16x32_bf16 v[84:87], v[204:207], v[112:115], 0\n\t" "v_mul_f32 v52, v52, v58\n\t" "v_mul_f32 v53, v53, v58\n\t" "v_cvt_pk_bf16_f32 v52, v52, v53\n\t"
    "v_perm_b32 v120, v52, v52, v20\n\t" "v_perm_b32 v121, v52, v52, v21\n\t" "v_perm_b32 v122, v52, v52, v22\n\t" "v_perm_b32 v123, v52, v52, v23\n\t"
    "v_mfma_f32_16x16x32_bf16 v[88:91], v[210:213], v[116:119], 0\n\t" "v_mfma_f32_16x16x32_bf16 v[92:95], v[216:219], v[116:119], 0\n\t" "v_mul_f32 v54, v54, v59\n\t" "v_mul_f32 v55, v55, v59\n\t"
    "v_cvt_pk_bf16_f32 v54, v54, v55\n\t" "v_perm_b32 v124, v54, v54, v20\n\t" "v_perm_b32 v125, v54, v54, v21\n\t" "v_perm_b32 v126, v54, v54, v22\n\t" "v_perm_b32 v127, v54, v54, v23\n\t"
    "v_mfma_f32_16x16x32_bf16 v[96:99], v[196:199], v[120:123], 0\n\t" "v_mfma_f32_16x16x32_bf16 v[100:103], v[202:205], v[120:123], 0\n\t" "v_cvt_pk_bf16_f32 v128, v80, v81\n\t"
    "v_cvt_pk_bf16_f32 v129, v82, v83\n\t" "v_cvt_pk_bf16_f32 v130, v84, v85\n\t" "v_cvt_pk_bf16_f32 v131, v86, v87\n\t" "global_store_dwordx4 v44, v[128:131], %[ws] offset:1024 sc1\n\t"
    "v_mfma_f32_16x16x32_bf16 v[104:107], v[208:211], v[124:127], 0\n\t" "v_mfma_f32_16x16x32_bf16 v[108:111], v[214:217], v[124:127], 0\n\t" "v_cvt_pk_bf16_f32 v132, v88, v89\n\t"
    "v_cvt_pk_bf16_f32 v133, v90, v91\n\t" "v_cvt_pk_bf16_f32 v134, v92, v93\n\t" "v_cvt_pk_bf16_f32 v135, v94, v95\n\t" "global_store_dwordx4 v45, v[132:135], %[ws] offset:1024 sc1\n\t"
    "v_cvt_pk_bf16_f32 v136, v96, v97\n\t" "v_cvt_pk_bf16_f32 v137, v98, v99\n\t" "v_cvt_pk_bf16_f32 v138, v100, v101\n\t" "v_cvt_pk_bf16_f32 v139, v102, v103\n\t"
    "global_store_dwordx4 v44, v[136:139], %[ws] offset:0 sc1\n\t" "v_cvt_pk_bf16_f32 v140, v104, v105\n\t" "v_cvt_pk_bf16_f32 v141, v106, v107\n\t" "v_cvt_pk_bf16_f32 v142, v108, v109\n\t"
    "v_cvt_pk_bf16_f32 v143, v110, v111\n\t" "global_store_dwordx4 v45, v[140:143], %[ws] offset:0 sc1\n\t" "v_perm_b32 v156, v252, v252, v14\n\t" "v_perm_b32 v157, v252, v252, v17\n\t"
    "v_perm_b32 v158, v252, v252, v15\n\t" "v_perm_b32 v159, v252, v252, v19\n\t" "v_mfma_f32_4x4x4_16b_bf16 v[176:179], v[156:157], a[32:33], 0\n\t"
    "v_mfma_f32_4x4x4_16b_bf16 v[180:183], v[156:157], a[34:35], 0\n\t" "v_perm_b32 v160, v253, v253, v14\n\t" "v_perm_b32 v161, v253, v253, v17\n\t"
    "v_mfma_f32_4x4x4_16b_bf16 v[176:179], v[158:159], a[36:37], v[176:179]\n\t" "v_mfma_f32_4x4x4_16b_bf16 v[180:183], v[158:159], a[38:39], v[180:183]\n\t" "v_perm_b32 v162, v253, v253, v15\n\t"
    "v_perm_b32 v163, v253, v253, v19\n\t" "v_mfma_f32_4x4x4_16b_bf16 v[176:179], v[160:161], a[40:41], v[176:179]\n\t" "v_mfma_f32_4x4x4_16b_bf16 v[180:183], v[160:161], a[42:43], v[180:183]\n\t"
    "v_perm_b32 v164, v254, v254, v14\n\t" "v_perm_b32 v165, v254, v254, v17\n\t" "v_mfma_f32_4x4x4_16b_bf16 v[176:179], v[162:163], a[44:45], v[176:179]\n\t"
    "v_mfma_f32_4x4x4_16b_bf16 v[180:183], v[162:163], a[46:47], v[180:183]\n\t" "v_perm_b32 v166, v254, v254, v15\n\t" "v_perm_b32 v167, v254, v254, v19\n\t"
    "v_mfma_f32_4x4x4_16b_bf16 v[176:179], v[164:165], a[48:49], v[176:179]\n\t" "v_mfma_f32_4x4x4_16b_bf16 v[180:183], v[164:165], a[50:51], v[180:183]\n\t" "v_perm_b32 v168, v255, v255, v14\n\t"
    "v_perm_b32 v169, v255, v255, v17\n\t" "v_mfma_f32_4x4x4_16b_bf16 v[176:179], v[166:167], a[52:53], v[176:179]\n\t" "v_mfma_f32_4x4x4_16b_bf16 v[180:183], v[166:167], a[54:55], v[180:183]\n\t"
    "v_perm_b32 v170, v255, v255, v15\n\t" "v_perm_b32 v171, v255, v255, v19\n\t" "v_mfma_f32_4x4x4_16b_bf16 v[176:179], v[168:169], a[56:57], v[176:179]\n\t"
    "v_mfma_f32_4x4x4_16b_bf16 v[180:183], v[168:169], a[58:59], v[180:183]\n\t" "s_nop 0\n\t" "v_mfma_f32_4x4x4_16b_bf16 v[176:179], v[170:171], a[60:61], v[176:179]\n\t"
    "v_mfma_f32_4x4x4_16b_bf16 v[180:183], v[170:171], a[62:63], v[180:183]\n\t" "s_nop 0\n\t" "s_nop 3\n\t" "v_permlane32_swap_b32 v176, v180\n\t" "v_permlane32_swap_b32 v177, v181\n\t"
    "v_permlane32_swap_b32 v178, v182\n\t" "v_permlane32_swap_b32 v179, v183\n\t" "v_add_f32 v176, v176, v180\n\t" "v_add_f32 v178, v178, v182\n\t" "v_add_f32 v177, v177, v181\n\t"
    "v_add_f32 v179, v179, v183\n\t" "v_permlane16_swap_b32 v176, v178\n\t" "s_nop 0\n\t" "v_permlane16_swap_b32 v177, v179\n\t" "v_add_f32 v176, v176, v178\n\t" "v_add_f32_e64 v177, v177, v179\n\t"
    "s_nop 0\n\t" "v_add_f32_dpp v176, v176, v176 row_ror:8 row_mask:0xf bank_mask:0xf\n\t" "v_add_f32_dpp v177, v177, v177 row_ror:8 row_mask:0xf bank_mask:0xf\n\t" "ds_write_b64 v11, v[176:177]\n\t"
    "v_mov_b64 v[178:179], s[62:63]\n\t" "v_mov_b32 v180, %[sf]\n\t" "s_waitcnt vmcnt(0) lgkmcnt(0)\n\t" "s_barrier\n\t" "s_cmp_eq_u32 %[w128], 0\n\t" "s_cselect_b64 exec, 1, 0\n\t"
    "global_store_dwordx2 v180, v[178:179], %[ws] sc1\n\t" "s_mov_b64 exec, -1\n\t" "v_add_u32 v12, 0xffe80000, v12\n\t" "s_add_u32 m0, %[sl1], 4096\n\t" "s_nop 0\n\t"
    "global_load_lds_dwordx4 v12, %[so]\n\t" "v_add_u32 v12, 0x100000, v12\n\t" "s_add_u32 m0, %[sl1], 69632\n\t" "s_nop 0\n\t" "global_load_lds_dwordx4 v12, %[so]\n\t" "v_add_u32 v12, 0x10000, v12\n\t"
    "s_add_u32 m0, %[sl1], 73728\n\t" "s_nop 0\n\t" "global_load_lds_dwordx4 v12, %[so]\n\t" "v_add_u32 v12, 0x10000, v12\n\t" "s_add_u32 m0, %[sl1], 77824\n\t" "s_nop 0\n\t"
    "global_load_lds_dwordx4 v12, %[so]\n\t" "v_add_u32 v12, 0x10000, v12\n\t" "s_add_u32 m0, %[sl1], 81920\n\t" "s_nop 0\n\t" "global_load_lds_dwordx4 v12, %[so]\n\t" "v_add_u32 v12, 0x10000, v12\n\t"
    "s_add_u32 m0, %[sl1], 86016\n\t" "s_nop 0\n\t" "global_load_lds_dwordx4 v12, %[so]\n\t" "v_add_u32 v12, 0x10000, v12\n\t" "s_add_u32 m0, %[sl1], 90112\n\t" "s_nop 0\n\t"
    "global_load_lds_dwordx4 v12, %[so]\n\t" "v_add_u32 v12, 0x40000, v12\n\t" "s_add_u32 m0, %[sl1], 106496\n\t" "s_nop 0\n\t" "global_load_lds_dwordx4 v12, %[so]\n\t" "s_branch .Lend_%=\n\t"
    ".Lslow_%=:\n\t" "s_sleep 2\n\t" "s_mov_b64 s[40:41], %[ws]\n\t" "global_load_dwordx4 v[80:83], v230, s[40:41] offset:-4096 sc1\n\t"
    "global_load_dwordx4 v[96:99], v230, s[40:41] offset:0 sc1\n\t" "s_add_u32 s40, s40, 8192\n\t" "s_addc_u32 s41, s41, 0\n\t" "global_load_dwordx4 v[84:87], v230, s[40:41] offset:-4096 sc1\n\t"
    "global_load_dwordx4 v[100:103], v230, s[40:41] offset:0 sc1\n\t" "s_add_u32 s40, s40, 8192\n\t" "s_addc_u32 s41, s41, 0\n\t" "global_load_dwordx4 v[88:91], v230, s[40:41] offset:-4096 sc1\n\t"
    "global_load_dwordx4 v[104:107], v230, s[40:41] offset:0 sc1\n\t" "s_add_u32 s40, s40, 8192\n\t" "s_addc_u32 s41, s41, 0\n\t" "global_load_dwordx4 v[92:95], v230, s[40:41] offset:-4096 sc1\n\t"
    "global_load_dwordx4 v[108:111], v230, s[40:41] offset:0 sc1\n\t" "s_waitcnt vmcnt(0)\n\t" "v_xad_u32 v144, v80, v81, s62\n\t" "v_xad_u32 v145, v84, v85, s62\n\t" "v_xad_u32 v146, v88, v89, s62\n\t"
    "v_xad_u32 v147, v92, v93, s62\n\t" "v_xad_u32 v148, v96, v97, s62\n\t" "v_xad_u32 v149, v100, v101, s62\n\t" "v_xad_u32 v150, v104, v105, s62\n\t" "v_xad_u32 v151, v108, v109, s62\n\t"
    "v_xad_u32 v144, v82, v81, v144\n\t" "v_xad_u32 v145, v86, v85, v145\n\t" "v_xad_u32 v146, v90, v89, v146\n\t" "v_xad_u32 v147, v94, v93, v147\n\t" "v_xad_u32 v148, v98, v97, v148\n\t"
    "v_xad_u32 v149, v102, v101, v149\n\t" "v_xad_u32 v150, v106, v105, v150\n\t" "v_xad_u32 v151, v110, v109, v151\n\t" "v_xor_b32_e64 v140, v144, v83\n\t"
    "v_bitop3_b32 v140, v140, v145, v87 bitop3:0xf6\n\t" "v_bitop3_b32 v140, v140, v146, v91 bitop3:0xf6\n\t" "v_bitop3_b32 v140, v140, v147, v95 bitop3:0xf6\n\t"
    "v_bitop3_b32 v140, v140, v148, v99 bitop3:0xf6\n\t" "v_bitop3_b32 v140, v140, v149, v103 bitop3:0xf6\n\t" "v_bitop3_b32 v140, v140, v150, v107 bitop3:0xf6\n\t"
    "v_bitop3_b32 v140, v140, v151, v111 bitop3:0xf6\n\t" "v_cmp_ne_u32_e64 vcc, 0, v140\n\t" "v_perm_b32 v83, v80, v81, s57\n\t" "v_perm_b32 v87, v84, v85, s57\n\t" "v_perm_b32 v91, v88, v89, s57\n\t"
    "v_perm_b32 v95, v92, v93, s57\n\t" "v_perm_b32 v99, v96, v97, s57\n\t" "v_perm_b32 v103, v100, v101, s57\n\t" "v_perm_b32 v107, v104, v105, s57\n\t" "v_perm_b32 v111, v108, v109, s57\n\t"
    "s_cbranch_vccz .Lgo_%=\n\t" "s_sub_u32 s44, s44, 1\n\t" "s_cbranch_scc0 .Lslow_%=\n\t" "s_branch .Lgo_%=\n\t" ".Lend_%=:\n\t"
    : "+{v0}"(vb), "+{v1}"(vh), "+{v2}"(vdt), "+{v11}"(vm), "+{v12}"(vbo), "+{v13}"(tid), "+{v[14:15]}"(t2), "+{v16}"(t3), "+{v18}"(vhd), "+{v19}"(vhx), "+{v196}"(d0), "+{v197}"(d1), "+{v198}"(d2), "+{v199}"(d3), "+{v200}"(dh0), "+{v201}"(dh1), "+{v202}"(xr0), "+{v203}"(xr1), "+{v230}"(vg), "+{v231}"(vtw), "+{v232}"(vhw), "+{v234}"(vs8), "+{v235}"(vph), "+{v246}"(vpr), "+{v3}"(vr0), "+{v4}"(vr1), "+{v5}"(vr2), "+{v6}"(vr3), "+{v7}"(vr4), "+{v8}"(vr5), "+{v9}"(vr6), "+{v10}"(vr7), "+{v205}"(tv), "+{s[62:63]}"(tag), "={v236}"(mu[0]), "={v237}"(mu[1])
    : [l0] "s"(l0), [lh] "s"(lh), [lt] "s"(lt), [lr] "s"(lr), [sl] "s"(sl), [sl1] "s"(sl1), [sb] "s"(sb), [rot] "s"(rot), [xl] "s"(xl), [xh_] "s"(xh_), [k0] "s"(k0), [kh] "s"(kh), [ws] "s"(ws), [sm] "s"(sm), [so] "s"(so), [sf] "s"(sf), [pf] "s"(pf), [tm] "s"(tm), [cd] "s"(cd), [w128] "s"(w128), [w4k] "s"(w4k), [xw] "s"(xw), [xh] "s"(xh), [ox] "s"(ox), [rw] "s"(rw)
    : "v17", "v20", "v21", "v22", "v23", "v24", "v25", "v26", "v27", "v28", "v29", "v30", "v31", "v32", "v33", "v34", "v35", "v36", "v37", "v38", "v39", "v40", "v41", "v42", "v43", "v44", "v45", "v46", "v47", "v48", "v49", "v50", "v51", "v52", "v53", "v54", "v55", "v56", "v57", "v58", "v59", "v60", "v61", "v62", "v63", "v64", "v65", "v66", "v67", "v68", "v69", "v70", "v71", "v72", "v73", "v74", "v75", "v76", "v77", "v78", "v79", "v80", "v81", "v82", "v83", "v84", "v85", "v86", "v87", "v88", "v89", "v90", "v91", "v92", "v93", "v94", "v95", "v96", "v97", "v98", "v99", "v100", "v101", "v102", "v103", "v104", "v105", "v106", "v107", "v108", "v109", "v110", "v111", "v112", "v113", "v114", "v115", "v116", "v117", "v118", "v119", "v120", "v121", "v122", "v123", "v124", "v125", "v126", "v127", "v128", "v129", "v130", "v131", "v132", "v133", "v134", "v135", "v136", "v137", "v138", "v139", "v140", "v141", "v142", "v143", "v144", "v145", "v146", "v147", "v148", "v149", "v150", "v151", "v152", "v153", "v154", "v155", "v156", "v157", "v158", "v159", "v160", "v161", "v162", "v163", "v164", "v165", "v166", "v167", "v168", "v169", "v170", "v171", "v172", "v173", "v174", "v175", "v176", "v177", "v178", "v179", "v180", "v181", "v182", "v183", "v184", "v185", "v186", "v187", "v188", "v189", "v190", "v191", "v192", "v193", "v194", "v195", "v204", "v206", "v207", "v208", "v209", "v210", "v211", "v212", "v213", "v214", "v215", "v216", "v217", "v218", "v219", "v220", "v221", "v222", "v223", "v224", "v225", "v226", "v227", "v228", "v229", "v233", "v238", "v239", "v240", "v241", "v242", "v243", "v244", "v245", "v247", "v248", "v249", "v250", "v251", "v252", "v253", "v254", "v255", "a0", "a1", "a2", "a3", "a4", "a5", "a6", "a7", "a8", "a9", "a10", "a11", "a12", "a13", "a14", "a15", "a16", "a17", "a18", "a19", "a20", "a21", "a22", "a23", "a24", "a25", "a26", "a27", "a28", "a29", "a30", "a31", "a32", "a33", "a34", "a35", "a36", "a37", "a38", "a39", "a40", "a41", "a42", "a43", "a44", "a45", "a46", "a47", "a48", "a49", "a50", "a51", "a52", "a53", "a54", "a55", "a56", "a57", "a58", "a59", "a60", "a61", "a62", "a63", "a64", "a65", "a66", "a67", "a68", "a69", "a70", "a71", "a72", "a73", "a74", "a75", "a76", "a77", "a78", "a79", "a80", "a81", "a82", "a83", "a84", "a85", "a86", "a87", "a88", "a89", "a90", "a91", "a92", "a93", "a94", "a95", "a96", "a97", "a98", "a99", "a100", "a101", "a102", "a103", "a104", "a105", "a106", "a107", "a108", "a109", "a110", "a111", "a112", "a113", "a114", "a115", "a116", "a117", "a118", "a119", "a120", "a121", "a122", "a123", "a124", "a125", "a126", "a127", "a128", "a129", "a130", "a131", "a132", "a133", "a134", "a135", "a136", "a137", "a138", "a139", "a140", "a141", "a142", "a143", "a144", "a145", "a146", "a147", "a148", "a149", "a150", "a151", "a152", "a153", "a154", "a155", "a156", "a157", "a158", "a159", "a160", "a161", "a162", "a163", "a164", "a165", "a166", "a167", "a168", "a169", "a170", "a171", "a172", "a173", "a174", "a175", "a176", "a177", "a178", "a179", "a180", "a181", "a182", "a183", "a184", "a185", "a186", "a187", "a188", "a189", "a190", "a191", "a192", "a193", "a194", "a195", "a196", "a197", "a198", "a199", "a200", "a201", "a202", "a203", "a204", "a205", "a206", "a207", "a208", "a209", "a210", "a211", "a212", "a213", "a214", "a215", "a216", "a217", "a218", "a219", "a220", "a221", "a222", "a223", "a224", "a225", "a226", "a227", "a228", "a229", "a230", "a231", "a232", "a233", "a234", "a235", "a236", "a237", "a238", "a239", "a240", "a241", "a242", "a243", "a244", "a245", "a246", "a247", "a248", "a249", "a250", "a251", "a252", "a253", "a254", "a255", "s40", "s41", "s42", "s43", "s44", "s45", "s46", "s47", "s48", "s49", "s50", "s51", "s52", "s53", "s54", "s55", "s56", "s57", "s58", "s59", "s60", "s61", "s64", "s65", "vcc", "scc", "memory"); }
  } }
  asm volatile("" : "+s"(tag)); asm volatile("" : "+s"(bx), "+s"(wid), "+s"(ws));
  constexpr int YS = 16384, YW = YS == 4096 ? 131072 : 4096;
  constexpr int GB = 49152, RAW = 0, TAB = 16384, ZB = TAB + 3584;
#define GW() const unsigned sl = ldsb + GB + wid * 3584; const char* sb = (const char*)(((wid >> 1) ? wu : wg) + ((xcd >> 1) * 8 + (loc >> 2)) * 112) + (size_t)(xcd & 1) * 16 * 32 * 7168
  unsigned vb[4];
  { unsigned vr[16]; f32x4 xr[8]; const unsigned va = (tid & 63) * 16; const char* sa;
    { IDX();
    const unsigned vf = (tid & 15) * FS, rr = ldsb + HALO + 22528 + va;
    const char* sq = ws + (unsigned)(W_TAGS + OF * 8 + FA(rb * 16)); sa = ws + (unsigned)(W_O + (rb * 4 + wid) * 65536 + 4096);
    const char* su = ws + (unsigned)(wid == 3 ? W_MODP + gc * 1024 + gk * 256 : W_UVP + ((xcd >> 1) * 8 + (loc >> 2)) * 16384 + ((xcd & 1) * 4 + (loc & 3)) * 2048 + (wid & 1) * 1024);
    const int lane = tid & 63, fr = lane & 15, par = bx & 1; u32x4 id[2];
#pragma unroll
    for (int j = 0; j < 4; ++j) { vr[j] = ldsb + f_vr(lane, j) + (wid & 1) * 2048; vr[4 + j] = vr[j] + 65536; vr[8 + j] = vr[j] ^ 2048; vr[12 + j] = vr[8 + j] + 65536; }
#pragma unroll
    for (int j = 0; j < 8; ++j) id[j >> 2][j & 3] = (lane >> 1) - (fr >> 3) * 12 == (j >> 2) * 16 + (j & 3) ? 0x3f80u << (fr & 1) * 16 : 0u;
    asm volatile( "s_cmp_eq_u32 %[wid], 0\n\t" "s_cselect_b64 s[46:47], 0xffff, 0\n\t" "s_mov_b64 exec, s[46:47]\n\t" "global_load_dwordx2 v[190:191], v14, %[sq] sc1\n\t" "s_mov_b64 exec, -1\n\t"
    "v_sub_f32 v156, 0, v236\n\t" "v_sub_f32 v157, 0, v236\n\t" "v_sub_f32 v158, 0, v236\n\t" "v_sub_f32 v159, 0, v236\n\t" "v_sub_f32 v160, 0, v237\n\t" "v_sub_f32 v161, 0, v237\n\t"
    "v_sub_f32 v162, 0, v237\n\t" "v_sub_f32 v163, 0, v237\n\t" "s_cmp_eq_u32 %[par], 0\n\t" "s_cbranch_scc0 .Lx1_%=\n\t" "s_add_u32 m0, %[sl1], 110592\n\t" "v_add_u32 v12, 0x10000, v12\n\t"
    "v_mfma_f32_16x16x32_bf16 v[80:83], v[40:43], a[0:3], v[156:159]\n\t" "global_load_lds_dwordx4 v12, %[so]\n\t" "s_add_u32 m0, %[sl1], 114688\n\t" "v_add_u32 v12, 0x10000, v12\n\t"
    "v_mfma_f32_16x16x32_bf16 v[84:87], v[40:43], a[4:7], v[160:163]\n\t" "global_load_lds_dwordx4 v12, %[so]\n\t" "s_add_u32 m0, %[sl1], 118784\n\t" "v_add_u32 v12, 0x10000, v12\n\t"
    "v_mfma_f32_16x16x32_bf16 v[88:91], v[44:47], a[0:3], v[156:159]\n\t" "global_load_lds_dwordx4 v12, %[so]\n\t" "s_add_u32 m0, %[sl1], 122880\n\t" "v_add_u32 v12, 0x10000, v12\n\t"
    "v_mfma_f32_16x16x32_bf16 v[92:95], v[44:47], a[4:7], v[160:163]\n\t" "global_load_lds_dwordx4 v12, %[so]\n\t" "s_add_u32 m0, %[sl1], 126976\n\t" "v_add_u32 v12, 0x10000, v12\n\t"
    "v_mfma_f32_16x16x32_bf16 v[96:99], v[40:43], a[8:11], v[156:159]\n\t" "global_load_lds_dwordx4 v12, %[so]\n\t" "v_mfma_f32_16x16x32_bf16 v[100:103], v[40:43], a[12:15], v[160:163]\n\t"
    "v_mfma_f32_16x16x32_bf16 v[104:107], v[44:47], a[8:11], v[156:159]\n\t" "v_mfma_f32_16x16x32_bf16 v[108:111], v[44:47], a[12:15], v[160:163]\n\t" "s_branch .Lx2_%=\n\t" ".Lx1_%=:\n\t"
    "s_add_u32 m0, %[sl1], 110592\n\t" "v_add_u32 v12, 0x10000, v12\n\t" "v_mfma_f32_16x16x32_bf16 v[80:83], v[40:43], a[16:19], v[156:159]\n\t" "global_load_lds_dwordx4 v12, %[so]\n\t"
    "s_add_u32 m0, %[sl1], 114688\n\t" "v_add_u32 v12, 0x10000, v12\n\t" "v_mfma_f32_16x16x32_bf16 v[84:87], v[40:43], a[20:23], v[160:163]\n\t" "global_load_lds_dwordx4 v12, %[so]\n\t"
    "s_add_u32 m0, %[sl1], 118784\n\t" "v_add_u32 v12, 0x10000, v12\n\t" "v_mfma_f32_16x16x32_bf16 v[88:91], v[44:47], a[16:19], v[156:159]\n\t" "global_load_lds_dwordx4 v12, %[so]\n\t"
    "s_add_u32 m0, %[sl1], 122880\n\t" "v_add_u32 v12, 0x10000, v12\n\t" "v_mfma_f32_16x16x32_bf16 v[92:95], v[44:47], a[20:23], v[160:163]\n\t" "global_load_lds_dwordx4 v12, %[so]\n\t"
    "s_add_u32 m0, %[sl1], 126976\n\t" "v_add_u32 v12, 0x10000, v12\n\t" "v_mfma_f32_16x16x32_bf16 v[96:99], v[40:43], a[24:27], v[156:159]\n\t" "global_load_lds_dwordx4 v12, %[so]\n\t"
    "v_mfma_f32_16x16x32_bf16 v[100:103], v[40:43], a[28:31], v[160:163]\n\t" "v_mfma_f32_16x16x32_bf16 v[104:107], v[44:47], a[24:27], v[156:159]\n\t"
    "v_mfma_f32_16x16x32_bf16 v[108:111], v[44:47], a[28:31], v[160:163]\n\t" ".Lx2_%=:\n\t" "v_mov_b64 v[164:165], %[tag]\n\t" "s_cmp_eq_u32 %[wid], 0\n\t" "s_cselect_b64 s[42:43], 0, -1\n\t"
    "v_mov_b32 v168, -1\n\t" "v_mov_b32 v169, -1\n\t" "v_mov_b32 v170, -1\n\t" "v_mov_b32 v171, -1\n\t" "s_cmp_eq_u32 %[wid], 3\n\t" "s_cbranch_scc0 .Lnu_%=\n\t"
    "ds_read_b128 v[172:175], v35 offset:0\n\t" "ds_read_b128 v[176:179], v35 offset:256\n\t" "ds_read_b128 v[180:183], v35 offset:512\n\t" "ds_read_b128 v[184:187], v35 offset:768\n\t"
    "s_mov_b32 s48, 0x07060503\n\t" "s_mov_b32 s49, 0x07060502\n\t" "s_mov_b32 s50, 0x07060501\n\t" "s_waitcnt lgkmcnt(0)\n\t" "v_pk_add_f32 v[172:173], v[172:173], v[176:177]\n\t"
    "v_pk_add_f32 v[174:175], v[174:175], v[178:179]\n\t" "v_pk_add_f32 v[180:181], v[180:181], v[184:185]\n\t" "v_pk_add_f32 v[182:183], v[182:183], v[186:187]\n\t"
    "v_pk_add_f32 v[172:173], v[172:173], v[180:181]\n\t" "v_pk_add_f32 v[174:175], v[174:175], v[182:183]\n\t" "v_perm_b32 v168, v172, v175, s48\n\t" "v_perm_b32 v169, v173, v175, s49\n\t"
    "v_perm_b32 v170, v174, v175, s50\n\t" "v_xad_u32 v171, v168, v169, v164\n\t" "s_mov_b64 s[42:43], 0xffff\n\t" "v_xad_u32 v171, v170, v169, v171\n\t" ".Lnu_%=:\n\t" "s_mov_b64 s[40:41], %[sa]\n\t"
    "s_mov_b32 s44, 0x400000\n\t" "s_mov_b64 exec, s[46:47]\n\t" "s_waitcnt vmcnt(5)\n\t" ".Lpo_%=:\n\t" "v_cmp_ne_u64 vcc, %[tag], v[190:191]\n\t" "s_nop 4\n\t" "s_cbranch_vccz .Lro_%=\n\t"
    "s_mov_b64 exec, vcc\n\t" "s_sleep 1\n\t" "global_load_dwordx2 v[190:191], v14, %[sq] sc1\n\t" "s_sub_u32 s44, s44, 1\n\t" "s_waitcnt vmcnt(0)\n\t" "s_cbranch_scc0 .Lpo_%=\n\t" ".Lro_%=:\n\t"
    "s_mov_b64 exec, -1\n\t" "s_barrier\n\t" "global_load_dwordx4 a[64:67], v0, s[40:41] offset:-4096 sc1\n\t" "global_load_dwordx4 a[68:71], v0, s[40:41] offset:-3072 sc1\n\t"
    "global_load_dwordx4 a[72:75], v0, s[40:41] offset:-2048 sc1\n\t" "global_load_dwordx4 a[76:79], v0, s[40:41] offset:-1024 sc1\n\t" "ds_read_b64_tr_b16 a[160:161], v16 offset:0\n\t"
    "ds_read_b64_tr_b16 a[162:163], v24 offset:0\n\t" "ds_read_b64_tr_b16 a[164:165], v17 offset:0\n\t" "ds_read_b64_tr_b16 a[166:167], v25 offset:0\n\t" "ds_read_b64_tr_b16 a[168:169], v18 offset:0\n\t"
    "ds_read_b64_tr_b16 a[170:171], v26 offset:0\n\t" "ds_read_b64_tr_b16 a[172:173], v19 offset:0\n\t" "ds_read_b64_tr_b16 a[174:175], v27 offset:0\n\t"
    "ds_read_b64_tr_b16 a[176:177], v16 offset:4096\n\t" "ds_read_b64_tr_b16 a[178:179], v24 offset:4096\n\t" "ds_read_b64_tr_b16 a[180:181], v17 offset:4096\n\t"
    "ds_read_b64_tr_b16 a[182:183], v25 offset:4096\n\t" "global_load_dwordx4 a[80:83], v0, s[40:41] offset:0 sc1\n\t" "global_load_dwordx4 a[84:87], v0, s[40:41] offset:1024 sc1\n\t"
    "global_load_dwordx4 a[88:91], v0, s[40:41] offset:2048 sc1\n\t" "global_load_dwordx4 a[92:95], v0, s[40:41] offset:3072 sc1\n\t" "s_add_u32 s40, s40, 8192\n\t" "s_addc_u32 s41, s41, 0\n\t"
    "global_load_dwordx4 a[96:99], v0, s[40:41] offset:-4096 sc1\n\t" "global_load_dwordx4 a[100:103], v0, s[40:41] offset:-3072 sc1\n\t"
    "global_load_dwordx4 a[104:107], v0, s[40:41] offset:-2048 sc1\n\t" "global_load_dwordx4 a[108:111], v0, s[40:41] offset:-1024 sc1\n\t" "s_mov_b64 exec, s[42:43]\n\t"
    "global_store_dwordx4 v0, v[168:171], %[su] sc1\n\t" "s_mov_b64 exec, -1\n\t" "s_nop 1\n\t"
    : "+{v12}"(vbo), "=&{v[80:83]}"(xr[0]), "=&{v[84:87]}"(xr[1]), "=&{v[88:91]}"(xr[2]), "=&{v[92:95]}"(xr[3]), "=&{v[96:99]}"(xr[4]), "=&{v[100:103]}"(xr[5]), "=&{v[104:107]}"(xr[6]), "=&{v[108:111]}"(xr[7])
    : "{v0}"(va), "{v14}"(vf), "{v35}"(rr), "{v236}"(mu[0]), "{v237}"(mu[1]), "{v16}"(vr[0]), "{v17}"(vr[1]), "{v18}"(vr[2]), "{v19}"(vr[3]), "{v20}"(vr[4]), "{v21}"(vr[5]), "{v22}"(vr[6]), "{v23}"(vr[7]), "{v24}"(vr[8]), "{v25}"(vr[9]), "{v26}"(vr[10]), "{v27}"(vr[11]), "{v28}"(vr[12]), "{v29}"(vr[13]), "{v30}"(vr[14]), "{v31}"(vr[15]), "{v[40:43]}"(id[0]), "{v[44:47]}"(id[1]), [tag] "s"(tag), [par] "s"(par), [wid] "s"(wid), [sl1] "s"(sl1), [so] "s"(so), [su] "s"(su), [sa] "s"(sa), [sq] "s"(sq)
    : "v156", "v157", "v158", "v159", "v160", "v161", "v162", "v163", "v164", "v165", "v166", "v167", "v168", "v169", "v170", "v171", "v172", "v173", "v174", "v175", "v176", "v177", "v178", "v179", "v180", "v181", "v182", "v183", "v184", "v185", "v186", "v187", "v188", "v189", "v190", "v191", "v192", "v193", "v194", "v195", "a0", "a1", "a2", "a3", "a4", "a5", "a6", "a7", "a8", "a9", "a10", "a11", "a12", "a13", "a14", "a15", "a16", "a17", "a18", "a19", "a20", "a21", "a22", "a23", "a24", "a25", "a26", "a27", "a28", "a29", "a30", "a31", "a32", "a33", "a34", "a35", "a36", "a37", "a38", "a39", "a40", "a41", "a42", "a43", "a44", "a45", "a46", "a47", "a48", "a49", "a50", "a51", "a52", "a53", "a54", "a55", "a56", "a57", "a58", "a59", "a60", "a61", "a62", "a63", "a64", "a65", "a66", "a67", "a68", "a69", "a70", "a71", "a72", "a73", "a74", "a75", "a76", "a77", "a78", "a79", "a80", "a81", "a82", "a83", "a84", "a85", "a86", "a87", "a88", "a89", "a90", "a91", "a92", "a93", "a94", "a95", "a96", "a97", "a98", "a99", "a100", "a101", "a102", "a103", "a104", "a105", "a106", "a107", "a108", "a109", "a110", "a111", "a112", "a113", "a114", "a115", "a116", "a117", "a118", "a119", "a120", "a121", "a122", "a123", "a124", "a125", "a126", "a127", "a128", "a129", "a130", "a131", "a132", "a133", "a134", "a135", "a136", "a137", "a138", "a139", "a140", "a141", "a142", "a143", "a144", "a145", "a146", "a147", "a148", "a149", "a150", "a151", "a152", "a153", "a154", "a155", "a156", "a157", "a158", "a159", "a160", "a161", "a162", "a163", "a164", "a165", "a166", "a167", "a168", "a169", "a170", "a171", "a172", "a173", "a174", "a175", "a176", "a177", "a178", "a179", "a180", "a181", "a182", "a183", "a184", "a185", "a186", "a187", "a188", "a189", "a190", "a191", "s40", "s41", "s42", "s43", "s44", "s45", "s46", "s47", "s48", "s49", "s50", "s78", "s79", "s82", "s83", "vcc", "scc", "memory");
    }
    asm volatile("" : "+s"(bx), "+s"(wid), "+s"(ws)); asm volatile("" : "+v"(tid));
    IDX(); GW();
    const int lane = tid & 63, fr = lane & 15, fq = lane >> 4, col = fr * 4 + fq, role = wid == 1 ? 1 : (rb & 1) ? 3 : wid + (wid == 3 && bx == 0);
#pragma unroll
    for (int i = 0; i < 4; ++i) { const int idx = min(i * 64 + lane, 223), r = (idx * 293) >> 12; vb[i] = (unsigned)((wid & 1) * 16 * 7168 + r * (7168 - 224) + idx * 16); }
    const unsigned tw = ldsb + HALO + 1024 + wid * 512 + col * 4, tr = ldsb + HALO + 1024 + wid * 512 + fq * 16, vs = (lane & 31) * 8, zb = ldsb + ZB + tid * 4, xw = ldsb + 24576 + tid * 16;
    const unsigned ve = wid == 1 ? col * 4 : (fr >> 3) * 128 + (fr & 3) * 16 + (fr & 4) * 2 + fq * 2;
    const char* sg0 = ws + (unsigned)(W_MODP + co * 1024); const char* sg1 = sg0 + 32768; const char* sg2 = sg0 + (unsigned)(16384 + (wid & 1) * 32768); const char* sf = ws + (unsigned)(W_TAGS + YF * 8 + FA(rb * 16 + cb));
    const char* se = ws + (unsigned)(wid == 1 ? W_GB + bx * 256 : wid == 3 ? W_TAGS + EP * 8 : W_EF + rb * 2048 + co * 256 + (wid ? 0 : 64));
    const char* sy0 = ws + (unsigned)(W_Y + (rb >> 1) * 524288 + ((rb & 1) * 2 + (wid >> 1)) * YW + 2 * co * YS + (wid & 1) * 2048); const char* sy1 = sy0 + YS; const char* ss = ws + (unsigned)(W_STATS + (cb * S_ + rb * 128 + wid * 32) * 8);
    asm volatile(".p2align 3\n\t" "s_mov_b64 s[40:41], %[sa]\n\t" "s_mov_b32 s49, 0x04000c0c\n\t" "s_mov_b32 s50, 0x0706000c\n\t" "s_mov_b32 s44, 0x400000\n\t" "v_mov_b64 v[164:165], %[tag]\n\t"
    "s_waitcnt vmcnt(10)\n\t" "s_waitcnt lgkmcnt(4)\n\t" "v_mfma_f32_16x16x32_bf16 v[48:51], a[160:163], a[64:67], 0\n\t" "ds_read_b64_tr_b16 a[184:185], v18 offset:4096\n\t"
    "global_load_dwordx4 a[112:115], v0, s[40:41] offset:0 sc1\n\t" "v_mfma_f32_16x16x32_bf16 v[52:55], a[160:163], a[68:71], 0\n\t" "ds_read_b64_tr_b16 a[186:187], v26 offset:4096\n\t"
    "v_mfma_f32_16x16x32_bf16 v[60:63], a[164:167], a[68:71], 0\n\t" "ds_read_b64_tr_b16 a[188:189], v19 offset:4096\n\t" "global_load_dwordx4 a[116:119], v0, s[40:41] offset:1024 sc1\n\t"
    "v_mfma_f32_16x16x32_bf16 v[56:59], a[164:167], a[64:67], 0\n\t" "ds_read_b64_tr_b16 a[190:191], v27 offset:4096\n\t" "v_mfma_f32_16x16x32_bf16 v[64:67], a[168:171], a[64:67], 0\n\t"
    "ds_read_b64_tr_b16 a[160:161], v16 offset:8192\n\t" "global_load_dwordx4 a[120:123], v0, s[40:41] offset:2048 sc1\n\t" "v_mfma_f32_16x16x32_bf16 v[68:71], a[168:171], a[68:71], 0\n\t"
    "ds_read_b64_tr_b16 a[162:163], v24 offset:8192\n\t" "v_mfma_f32_16x16x32_bf16 v[76:79], a[172:175], a[68:71], 0\n\t" "ds_read_b64_tr_b16 a[164:165], v17 offset:8192\n\t"
    "v_mfma_f32_16x16x32_bf16 v[72:75], a[172:175], a[64:67], 0\n\t" "ds_read_b64_tr_b16 a[166:167], v25 offset:8192\n\t" "s_waitcnt lgkmcnt(4) vmcnt(11)\n\t"
    "v_mfma_f32_16x16x32_bf16 v[48:51], a[176:179], a[72:75], v[48:51]\n\t" "s_barrier\n\t" "ds_read_b64_tr_b16 a[168:169], v18 offset:8192\n\t"
    "global_load_dwordx4 a[124:127], v0, s[40:41] offset:3072 sc1\n\t" "v_mfma_f32_16x16x32_bf16 v[52:55], a[176:179], a[76:79], v[52:55]\n\t" "ds_read_b64_tr_b16 a[170:171], v26 offset:8192\n\t"
    "s_add_u32 s40, s40, 8192\n\t" "s_addc_u32 s41, s41, 0\n\t" "v_mfma_f32_16x16x32_bf16 v[60:63], a[180:183], a[76:79], v[60:63]\n\t" "ds_read_b64_tr_b16 a[172:173], v19 offset:8192\n\t"
    "v_mfma_f32_16x16x32_bf16 v[56:59], a[180:183], a[72:75], v[56:59]\n\t" "ds_read_b64_tr_b16 a[174:175], v27 offset:8192\n\t" "v_mfma_f32_16x16x32_bf16 v[64:67], a[184:187], a[72:75], v[64:67]\n\t"
    "ds_read_b64_tr_b16 a[176:177], v16 offset:12288\n\t" "global_load_dwordx4 a[128:131], v0, s[40:41] offset:-4096 sc1\n\t" "v_mfma_f32_16x16x32_bf16 v[68:71], a[184:187], a[76:79], v[68:71]\n\t"
    "ds_read_b64_tr_b16 a[178:179], v24 offset:12288\n\t" "v_mfma_f32_16x16x32_bf16 v[76:79], a[188:191], a[76:79], v[76:79]\n\t" "ds_read_b64_tr_b16 a[180:181], v17 offset:12288\n\t"
    "v_mfma_f32_16x16x32_bf16 v[72:75], a[188:191], a[72:75], v[72:75]\n\t" "ds_read_b64_tr_b16 a[182:183], v25 offset:12288\n\t" "s_waitcnt lgkmcnt(4) vmcnt(11)\n\t"
    "v_mfma_f32_16x16x32_bf16 v[48:51], a[160:163], a[80:83], v[48:51]\n\t" "ds_read_b64_tr_b16 a[184:185], v18 offset:12288\n\t" "global_load_dwordx4 a[132:135], v0, s[40:41] offset:-3072 sc1\n\t"
    "v_mfma_f32_16x16x32_bf16 v[52:55], a[160:163], a[84:87], v[52:55]\n\t" "ds_read_b64_tr_b16 a[186:187], v26 offset:12288\n\t" "v_mfma_f32_16x16x32_bf16 v[60:63], a[164:167], a[84:87], v[60:63]\n\t"
    "ds_read_b64_tr_b16 a[188:189], v19 offset:12288\n\t" "v_mfma_f32_16x16x32_bf16 v[56:59], a[164:167], a[80:83], v[56:59]\n\t" "ds_read_b64_tr_b16 a[190:191], v27 offset:12288\n\t"
    "v_mfma_f32_16x16x32_bf16 v[64:67], a[168:171], a[80:83], v[64:67]\n\t" "ds_read_b64_tr_b16 a[160:161], v16 offset:16384\n\t" "global_load_dwordx4 a[136:139], v0, s[40:41] offset:-2048 sc1\n\t"
    "v_mfma_f32_16x16x32_bf16 v[68:71], a[168:171], a[84:87], v[68:71]\n\t" "ds_read_b64_tr_b16 a[162:163], v24 offset:16384\n\t" "v_mfma_f32_16x16x32_bf16 v[76:79], a[172:175], a[84:87], v[76:79]\n\t"
    "ds_read_b64_tr_b16 a[164:165], v17 offset:16384\n\t" "v_mfma_f32_16x16x32_bf16 v[72:75], a[172:175], a[80:83], v[72:75]\n\t" "ds_read_b64_tr_b16 a[166:167], v25 offset:16384\n\t"
    "s_waitcnt lgkmcnt(4) vmcnt(11)\n\t" "v_mfma_f32_16x16x32_bf16 v[48:51], a[176:179], a[88:91], v[48:51]\n\t" "ds_read_b64_tr_b16 a[168:169], v18 offset:16384\n\t"
    "global_load_dwordx4 a[140:143], v0, s[40:41] offset:-1024 sc1\n\t" "v_mfma_f32_16x16x32_bf16 v[52:55], a[176:179], a[92:95], v[52:55]\n\t" "ds_read_b64_tr_b16 a[170:171], v26 offset:16384\n\t"
    "v_mfma_f32_16x16x32_bf16 v[60:63], a[180:183], a[92:95], v[60:63]\n\t" "ds_read_b64_tr_b16 a[172:173], v19 offset:16384\n\t" "v_mfma_f32_16x16x32_bf16 v[56:59], a[180:183], a[88:91], v[56:59]\n\t"
    "ds_read_b64_tr_b16 a[174:175], v27 offset:16384\n\t" "v_mfma_f32_16x16x32_bf16 v[64:67], a[184:187], a[88:91], v[64:67]\n\t" "ds_read_b64_tr_b16 a[176:177], v16 offset:20480\n\t"
    "global_load_dwordx4 a[144:147], v0, s[40:41] offset:0 sc1\n\t" "v_mfma_f32_16x16x32_bf16 v[68:71], a[184:187], a[92:95], v[68:71]\n\t" "ds_read_b64_tr_b16 a[178:179], v24 offset:20480\n\t"
    "v_mfma_f32_16x16x32_bf16 v[76:79], a[188:191], a[92:95], v[76:79]\n\t" "ds_read_b64_tr_b16 a[180:181], v17 offset:20480\n\t" "v_mfma_f32_16x16x32_bf16 v[72:75], a[188:191], a[88:91], v[72:75]\n\t"
    "ds_read_b64_tr_b16 a[182:183], v25 offset:20480\n\t" "s_waitcnt lgkmcnt(4) vmcnt(11)\n\t" "v_mfma_f32_16x16x32_bf16 v[48:51], a[160:163], a[96:99], v[48:51]\n\t"
    "ds_read_b64_tr_b16 a[184:185], v18 offset:20480\n\t" "global_load_dwordx4 a[148:151], v0, s[40:41] offset:1024 sc1\n\t" "v_mfma_f32_16x16x32_bf16 v[52:55], a[160:163], a[100:103], v[52:55]\n\t"
    "ds_read_b64_tr_b16 a[186:187], v26 offset:20480\n\t" "v_mfma_f32_16x16x32_bf16 v[60:63], a[164:167], a[100:103], v[60:63]\n\t" "ds_read_b64_tr_b16 a[188:189], v19 offset:20480\n\t"
    "v_mfma_f32_16x16x32_bf16 v[56:59], a[164:167], a[96:99], v[56:59]\n\t" "ds_read_b64_tr_b16 a[190:191], v27 offset:20480\n\t" "v_mfma_f32_16x16x32_bf16 v[64:67], a[168:171], a[96:99], v[64:67]\n\t"
    "ds_read_b64_tr_b16 a[160:161], v16 offset:24576\n\t" "global_load_dwordx4 a[152:155], v0, s[40:41] offset:2048 sc1\n\t" "v_mfma_f32_16x16x32_bf16 v[68:71], a[168:171], a[100:103], v[68:71]\n\t"
    "ds_read_b64_tr_b16 a[162:163], v24 offset:24576\n\t" "v_mfma_f32_16x16x32_bf16 v[76:79], a[172:175], a[100:103], v[76:79]\n\t" "ds_read_b64_tr_b16 a[164:165], v17 offset:24576\n\t"
    "v_mfma_f32_16x16x32_bf16 v[72:75], a[172:175], a[96:99], v[72:75]\n\t" "ds_read_b64_tr_b16 a[166:167], v25 offset:24576\n\t" "s_waitcnt lgkmcnt(4) vmcnt(11)\n\t"
    "v_mfma_f32_16x16x32_bf16 v[48:51], a[176:179], a[104:107], v[48:51]\n\t" "ds_read_b64_tr_b16 a[168:169], v18 offset:24576\n\t" "global_load_dwordx4 a[156:159], v0, s[40:41] offset:3072 sc1\n\t"
    "v_mfma_f32_16x16x32_bf16 v[52:55], a[176:179], a[108:111], v[52:55]\n\t" "ds_read_b64_tr_b16 a[170:171], v26 offset:24576\n\t" "s_add_u32 s40, s40, 8192\n\t" "s_addc_u32 s41, s41, 0\n\t"
    "v_mfma_f32_16x16x32_bf16 v[60:63], a[180:183], a[108:111], v[60:63]\n\t" "ds_read_b64_tr_b16 a[172:173], v19 offset:24576\n\t"
    "v_mfma_f32_16x16x32_bf16 v[56:59], a[180:183], a[104:107], v[56:59]\n\t" "ds_read_b64_tr_b16 a[174:175], v27 offset:24576\n\t"
    "v_mfma_f32_16x16x32_bf16 v[64:67], a[184:187], a[104:107], v[64:67]\n\t" "ds_read_b64_tr_b16 a[176:177], v16 offset:28672\n\t" "global_load_dwordx4 a[64:67], v0, s[40:41] offset:-4096 sc1\n\t"
    "v_mfma_f32_16x16x32_bf16 v[68:71], a[184:187], a[108:111], v[68:71]\n\t" "ds_read_b64_tr_b16 a[178:179], v24 offset:28672\n\t"
    "v_mfma_f32_16x16x32_bf16 v[76:79], a[188:191], a[108:111], v[76:79]\n\t" "ds_read_b64_tr_b16 a[180:181], v17 offset:28672\n\t"
    "v_mfma_f32_16x16x32_bf16 v[72:75], a[188:191], a[104:107], v[72:75]\n\t" "ds_read_b64_tr_b16 a[182:183], v25 offset:28672\n\t" "s_waitcnt lgkmcnt(4) vmcnt(11)\n\t"
    "v_mfma_f32_16x16x32_bf16 v[48:51], a[160:163], a[112:115], v[48:51]\n\t" "ds_read_b64_tr_b16 a[184:185], v18 offset:28672\n\t" "global_load_dwordx4 a[68:71], v0, s[40:41] offset:-3072 sc1\n\t"
    "v_mfma_f32_16x16x32_bf16 v[52:55], a[160:163], a[116:119], v[52:55]\n\t" "ds_read_b64_tr_b16 a[186:187], v26 offset:28672\n\t"
    "v_mfma_f32_16x16x32_bf16 v[60:63], a[164:167], a[116:119], v[60:63]\n\t" "ds_read_b64_tr_b16 a[188:189], v19 offset:28672\n\t"
    "v_mfma_f32_16x16x32_bf16 v[56:59], a[164:167], a[112:115], v[56:59]\n\t" "ds_read_b64_tr_b16 a[190:191], v27 offset:28672\n\t"
    "v_mfma_f32_16x16x32_bf16 v[64:67], a[168:171], a[112:115], v[64:67]\n\t" "ds_read_b64_tr_b16 a[160:161], v16 offset:32768\n\t" "global_load_dwordx4 a[72:75], v0, s[40:41] offset:-2048 sc1\n\t"
    "v_mfma_f32_16x16x32_bf16 v[68:71], a[168:171], a[116:119], v[68:71]\n\t" "ds_read_b64_tr_b16 a[162:163], v24 offset:32768\n\t"
    "v_mfma_f32_16x16x32_bf16 v[76:79], a[172:175], a[116:119], v[76:79]\n\t" "ds_read_b64_tr_b16 a[164:165], v17 offset:32768\n\t"
    "v_mfma_f32_16x16x32_bf16 v[72:75], a[172:175], a[112:115], v[72:75]\n\t" "ds_read_b64_tr_b16 a[166:167], v25 offset:32768\n\t" "s_waitcnt lgkmcnt(4) vmcnt(11)\n\t"
    "v_mfma_f32_16x16x32_bf16 v[48:51], a[176:179], a[120:123], v[48:51]\n\t" "ds_read_b64_tr_b16 a[168:169], v18 offset:32768\n\t" "global_load_dwordx4 a[76:79], v0, s[40:41] offset:-1024 sc1\n\t"
    "v_mfma_f32_16x16x32_bf16 v[52:55], a[176:179], a[124:127], v[52:55]\n\t" "ds_read_b64_tr_b16 a[170:171], v26 offset:32768\n\t"
    "v_mfma_f32_16x16x32_bf16 v[60:63], a[180:183], a[124:127], v[60:63]\n\t" "ds_read_b64_tr_b16 a[172:173], v19 offset:32768\n\t"
    "v_mfma_f32_16x16x32_bf16 v[56:59], a[180:183], a[120:123], v[56:59]\n\t" "ds_read_b64_tr_b16 a[174:175], v27 offset:32768\n\t"
    "v_mfma_f32_16x16x32_bf16 v[64:67], a[184:187], a[120:123], v[64:67]\n\t" "ds_read_b64_tr_b16 a[176:177], v16 offset:36864\n\t" "global_load_dwordx4 a[80:83], v0, s[40:41] offset:0 sc1\n\t"
    "v_mfma_f32_16x16x32_bf16 v[68:71], a[184:187], a[124:127], v[68:71]\n\t" "ds_read_b64_tr_b16 a[178:179], v24 offset:36864\n\t"
    "v_mfma_f32_16x16x32_bf16 v[76:79], a[188:191], a[124:127], v[76:79]\n\t" "ds_read_b64_tr_b16 a[180:181], v17 offset:36864\n\t"
    "v_mfma_f32_16x16x32_bf16 v[72:75], a[188:191], a[120:123], v[72:75]\n\t" "ds_read_b64_tr_b16 a[182:183], v25 offset:36864\n\t" "s_waitcnt lgkmcnt(4) vmcnt(11)\n\t"
    "v_mfma_f32_16x16x32_bf16 v[48:51], a[160:163], a[128:131], v[48:51]\n\t" "ds_read_b64_tr_b16 a[184:185], v18 offset:36864\n\t" "global_load_dwordx4 a[84:87], v0, s[40:41] offset:1024 sc1\n\t"
    "v_mfma_f32_16x16x32_bf16 v[52:55], a[160:163], a[132:135], v[52:55]\n\t" "ds_read_b64_tr_b16 a[186:187], v26 offset:36864\n\t"
    "v_mfma_f32_16x16x32_bf16 v[60:63], a[164:167], a[132:135], v[60:63]\n\t" "ds_read_b64_tr_b16 a[188:189], v19 offset:36864\n\t"
    "v_mfma_f32_16x16x32_bf16 v[56:59], a[164:167], a[128:131], v[56:59]\n\t" "ds_read_b64_tr_b16 a[190:191], v27 offset:36864\n\t"
    "v_mfma_f32_16x16x32_bf16 v[64:67], a[168:171], a[128:131], v[64:67]\n\t" "ds_read_b64_tr_b16 a[160:161], v16 offset:40960\n\t" "global_load_dwordx4 a[88:91], v0, s[40:41] offset:2048 sc1\n\t"
    "v_mfma_f32_16x16x32_bf16 v[68:71], a[168:171], a[132:135], v[68:71]\n\t" "ds_read_b64_tr_b16 a[162:163], v24 offset:40960\n\t"
    "v_mfma_f32_16x16x32_bf16 v[76:79], a[172:175], a[132:135], v[76:79]\n\t" "ds_read_b64_tr_b16 a[164:165], v17 offset:40960\n\t"
    "v_mfma_f32_16x16x32_bf16 v[72:75], a[172:175], a[128:131], v[72:75]\n\t" "ds_read_b64_tr_b16 a[166:167], v25 offset:40960\n\t" "s_waitcnt lgkmcnt(4) vmcnt(11)\n\t"
    "v_mfma_f32_16x16x32_bf16 v[48:51], a[176:179], a[136:139], v[48:51]\n\t" "ds_read_b64_tr_b16 a[168:169], v18 offset:40960\n\t" "global_load_dwordx4 a[92:95], v0, s[40:41] offset:3072 sc1\n\t"
    "v_mfma_f32_16x16x32_bf16 v[52:55], a[176:179], a[140:143], v[52:55]\n\t" "ds_read_b64_tr_b16 a[170:171], v26 offset:40960\n\t" "s_add_u32 s40, s40, 8192\n\t" "s_addc_u32 s41, s41, 0\n\t"
    "v_mfma_f32_16x16x32_bf16 v[60:63], a[180:183], a[140:143], v[60:63]\n\t" "ds_read_b64_tr_b16 a[172:173], v19 offset:40960\n\t"
    "v_mfma_f32_16x16x32_bf16 v[56:59], a[180:183], a[136:139], v[56:59]\n\t" "ds_read_b64_tr_b16 a[174:175], v27 offset:40960\n\t"
    "v_mfma_f32_16x16x32_bf16 v[64:67], a[184:187], a[136:139], v[64:67]\n\t" "ds_read_b64_tr_b16 a[176:177], v16 offset:45056\n\t" "global_load_dwordx4 a[96:99], v0, s[40:41] offset:-4096 sc1\n\t"
    "v_mfma_f32_16x16x32_bf16 v[68:71], a[184:187], a[140:143], v[68:71]\n\t" "ds_read_b64_tr_b16 a[178:179], v24 offset:45056\n\t"
    "v_mfma_f32_16x16x32_bf16 v[76:79], a[188:191], a[140:143], v[76:79]\n\t" "ds_read_b64_tr_b16 a[180:181], v17 offset:45056\n\t"
    "v_mfma_f32_16x16x32_bf16 v[72:75], a[188:191], a[136:139], v[72:75]\n\t" "ds_read_b64_tr_b16 a[182:183], v25 offset:45056\n\t" "s_waitcnt lgkmcnt(4) vmcnt(11)\n\t"
    "v_mfma_f32_16x16x32_bf16 v[48:51], a[160:163], a[144:147], v[48:51]\n\t" "ds_read_b64_tr_b16 a[184:185], v18 offset:45056\n\t" "global_load_dwordx4 a[100:103], v0, s[40:41] offset:-3072 sc1\n\t"
    "v_mfma_f32_16x16x32_bf16 v[52:55], a[160:163], a[148:151], v[52:55]\n\t" "ds_read_b64_tr_b16 a[186:187], v26 offset:45056\n\t" "global_load_dwordx4 v[112:115], v0, %[sg0] sc1\n\t"
    "v_mfma_f32_16x16x32_bf16 v[60:63], a[164:167], a[148:151], v[60:63]\n\t" "ds_read_b64_tr_b16 a[188:189], v19 offset:45056\n\t"
    "v_mfma_f32_16x16x32_bf16 v[56:59], a[164:167], a[144:147], v[56:59]\n\t" "ds_read_b64_tr_b16 a[190:191], v27 offset:45056\n\t" "global_load_dwordx4 v[116:119], v0, %[sg1] sc1\n\t"
    "v_mfma_f32_16x16x32_bf16 v[64:67], a[168:171], a[144:147], v[64:67]\n\t" "ds_read_b64_tr_b16 a[160:161], v16 offset:49152\n\t" "global_load_dwordx4 a[104:107], v0, s[40:41] offset:-2048 sc1\n\t"
    "v_mfma_f32_16x16x32_bf16 v[68:71], a[168:171], a[148:151], v[68:71]\n\t" "ds_read_b64_tr_b16 a[162:163], v24 offset:49152\n\t" "global_load_dwordx4 v[120:123], v0, %[sg2] sc1\n\t"
    "v_mfma_f32_16x16x32_bf16 v[76:79], a[172:175], a[148:151], v[76:79]\n\t" "ds_read_b64_tr_b16 a[164:165], v17 offset:49152\n\t"
    "v_mfma_f32_16x16x32_bf16 v[72:75], a[172:175], a[144:147], v[72:75]\n\t" "ds_read_b64_tr_b16 a[166:167], v25 offset:49152\n\t" "global_load_dword a0, v0, %[cd]\n\t"
    "s_waitcnt lgkmcnt(4) vmcnt(15)\n\t" "v_mfma_f32_16x16x32_bf16 v[48:51], a[176:179], a[152:155], v[48:51]\n\t" "ds_read_b64_tr_b16 a[168:169], v18 offset:49152\n\t"
    "global_load_dwordx4 a[108:111], v0, s[40:41] offset:-1024 sc1\n\t" "v_mfma_f32_16x16x32_bf16 v[52:55], a[176:179], a[156:159], v[52:55]\n\t" "ds_read_b64_tr_b16 a[170:171], v26 offset:49152\n\t"
    "v_mfma_f32_16x16x32_bf16 v[60:63], a[180:183], a[156:159], v[60:63]\n\t" "ds_read_b64_tr_b16 a[172:173], v19 offset:49152\n\t"
    "v_mfma_f32_16x16x32_bf16 v[56:59], a[180:183], a[152:155], v[56:59]\n\t" "ds_read_b64_tr_b16 a[174:175], v27 offset:49152\n\t"
    "v_mfma_f32_16x16x32_bf16 v[64:67], a[184:187], a[152:155], v[64:67]\n\t" "ds_read_b64_tr_b16 a[176:177], v16 offset:53248\n\t" "global_load_dwordx4 a[112:115], v0, s[40:41] offset:0 sc1\n\t"
    "v_mfma_f32_16x16x32_bf16 v[68:71], a[184:187], a[156:159], v[68:71]\n\t" "ds_read_b64_tr_b16 a[178:179], v24 offset:53248\n\t"
    "v_mfma_f32_16x16x32_bf16 v[76:79], a[188:191], a[156:159], v[76:79]\n\t" "ds_read_b64_tr_b16 a[180:181], v17 offset:53248\n\t"
    "v_mfma_f32_16x16x32_bf16 v[72:75], a[188:191], a[152:155], v[72:75]\n\t" "ds_read_b64_tr_b16 a[182:183], v25 offset:53248\n\t" "s_waitcnt lgkmcnt(4) vmcnt(15)\n\t"
    "v_mfma_f32_16x16x32_bf16 v[48:51], a[160:163], a[64:67], v[48:51]\n\t" "ds_read_b64_tr_b16 a[184:185], v18 offset:53248\n\t" "global_load_dwordx4 a[116:119], v0, s[40:41] offset:1024 sc1\n\t"
    "v_mfma_f32_16x16x32_bf16 v[52:55], a[160:163], a[68:71], v[52:55]\n\t" "ds_read_b64_tr_b16 a[186:187], v26 offset:53248\n\t" "v_mfma_f32_16x16x32_bf16 v[60:63], a[164:167], a[68:71], v[60:63]\n\t"
    "ds_read_b64_tr_b16 a[188:189], v19 offset:53248\n\t" "v_mfma_f32_16x16x32_bf16 v[56:59], a[164:167], a[64:67], v[56:59]\n\t" "ds_read_b64_tr_b16 a[190:191], v27 offset:53248\n\t"
    "v_mfma_f32_16x16x32_bf16 v[64:67], a[168:171], a[64:67], v[64:67]\n\t" "ds_read_b64_tr_b16 a[160:161], v16 offset:57344\n\t" "global_load_dwordx4 a[120:123], v0, s[40:41] offset:2048 sc1\n\t"
    "v_mfma_f32_16x16x32_bf16 v[68:71], a[168:171], a[68:71], v[68:71]\n\t" "ds_read_b64_tr_b16 a[162:163], v24 offset:57344\n\t" "v_mfma_f32_16x16x32_bf16 v[76:79], a[172:175], a[68:71], v[76:79]\n\t"
    "ds_read_b64_tr_b16 a[164:165], v17 offset:57344\n\t" "v_mfma_f32_16x16x32_bf16 v[72:75], a[172:175], a[64:67], v[72:75]\n\t" "ds_read_b64_tr_b16 a[166:167], v25 offset:57344\n\t"
    "s_waitcnt lgkmcnt(4) vmcnt(15)\n\t" "v_mfma_f32_16x16x32_bf16 v[48:51], a[176:179], a[72:75], v[48:51]\n\t" "ds_read_b64_tr_b16 a[168:169], v18 offset:57344\n\t"
    "global_load_dwordx4 a[124:127], v0, s[40:41] offset:3072 sc1\n\t" "v_mfma_f32_16x16x32_bf16 v[52:55], a[176:179], a[76:79], v[52:55]\n\t" "ds_read_b64_tr_b16 a[170:171], v26 offset:57344\n\t"
    "s_add_u32 s40, s40, 8192\n\t" "s_addc_u32 s41, s41, 0\n\t" "v_mfma_f32_16x16x32_bf16 v[60:63], a[180:183], a[76:79], v[60:63]\n\t" "ds_read_b64_tr_b16 a[172:173], v19 offset:57344\n\t"
    "v_mfma_f32_16x16x32_bf16 v[56:59], a[180:183], a[72:75], v[56:59]\n\t" "ds_read_b64_tr_b16 a[174:175], v27 offset:57344\n\t" "v_mfma_f32_16x16x32_bf16 v[64:67], a[184:187], a[72:75], v[64:67]\n\t"
    "ds_read_b64_tr_b16 a[176:177], v16 offset:61440\n\t" "global_load_dwordx4 a[128:131], v0, s[40:41] offset:-4096 sc1\n\t" "v_mfma_f32_16x16x32_bf16 v[68:71], a[184:187], a[76:79], v[68:71]\n\t"
    "ds_read_b64_tr_b16 a[178:179], v24 offset:61440\n\t" "v_mfma_f32_16x16x32_bf16 v[76:79], a[188:191], a[76:79], v[76:79]\n\t" "ds_read_b64_tr_b16 a[180:181], v17 offset:61440\n\t"
    "v_mfma_f32_16x16x32_bf16 v[72:75], a[188:191], a[72:75], v[72:75]\n\t" "ds_read_b64_tr_b16 a[182:183], v25 offset:61440\n\t" "s_waitcnt lgkmcnt(4) vmcnt(15)\n\t"
    "v_mfma_f32_16x16x32_bf16 v[48:51], a[160:163], a[80:83], v[48:51]\n\t" "ds_read_b64_tr_b16 a[184:185], v18 offset:61440\n\t" "global_load_dwordx4 a[132:135], v0, s[40:41] offset:-3072 sc1\n\t"
    "v_mfma_f32_16x16x32_bf16 v[52:55], a[160:163], a[84:87], v[52:55]\n\t" "ds_read_b64_tr_b16 a[186:187], v26 offset:61440\n\t" "v_mfma_f32_16x16x32_bf16 v[60:63], a[164:167], a[84:87], v[60:63]\n\t"
    "ds_read_b64_tr_b16 a[188:189], v19 offset:61440\n\t" "v_mfma_f32_16x16x32_bf16 v[56:59], a[164:167], a[80:83], v[56:59]\n\t" "ds_read_b64_tr_b16 a[190:191], v27 offset:61440\n\t"
    "v_mfma_f32_16x16x32_bf16 v[64:67], a[168:171], a[80:83], v[64:67]\n\t" "ds_read_b64_tr_b16 a[160:161], v20 offset:0\n\t" "global_load_dwordx4 a[136:139], v0, s[40:41] offset:-2048 sc1\n\t"
    "v_mfma_f32_16x16x32_bf16 v[68:71], a[168:171], a[84:87], v[68:71]\n\t" "ds_read_b64_tr_b16 a[162:163], v28 offset:0\n\t" "v_mfma_f32_16x16x32_bf16 v[76:79], a[172:175], a[84:87], v[76:79]\n\t"
    "ds_read_b64_tr_b16 a[164:165], v21 offset:0\n\t" "v_mfma_f32_16x16x32_bf16 v[72:75], a[172:175], a[80:83], v[72:75]\n\t" "ds_read_b64_tr_b16 a[166:167], v29 offset:0\n\t"
    "s_waitcnt lgkmcnt(4) vmcnt(15)\n\t" "v_mfma_f32_16x16x32_bf16 v[48:51], a[176:179], a[88:91], v[48:51]\n\t" "ds_read_b64_tr_b16 a[168:169], v22 offset:0\n\t"
    "global_load_dwordx4 a[140:143], v0, s[40:41] offset:-1024 sc1\n\t" "v_mfma_f32_16x16x32_bf16 v[52:55], a[176:179], a[92:95], v[52:55]\n\t" "ds_read_b64_tr_b16 a[170:171], v30 offset:0\n\t"
    "v_mfma_f32_16x16x32_bf16 v[60:63], a[180:183], a[92:95], v[60:63]\n\t" "ds_read_b64_tr_b16 a[172:173], v23 offset:0\n\t" "v_mfma_f32_16x16x32_bf16 v[56:59], a[180:183], a[88:91], v[56:59]\n\t"
    "ds_read_b64_tr_b16 a[174:175], v31 offset:0\n\t" "v_mfma_f32_16x16x32_bf16 v[64:67], a[184:187], a[88:91], v[64:67]\n\t" "ds_read_b64_tr_b16 a[176:177], v20 offset:4096\n\t"
    "global_load_dwordx4 a[144:147], v0, s[40:41] offset:0 sc1\n\t" "v_mfma_f32_16x16x32_bf16 v[68:71], a[184:187], a[92:95], v[68:71]\n\t" "ds_read_b64_tr_b16 a[178:179], v28 offset:4096\n\t"
    "v_mfma_f32_16x16x32_bf16 v[76:79], a[188:191], a[92:95], v[76:79]\n\t" "ds_read_b64_tr_b16 a[180:181], v21 offset:4096\n\t" "v_mfma_f32_16x16x32_bf16 v[72:75], a[188:191], a[88:91], v[72:75]\n\t"
    "ds_read_b64_tr_b16 a[182:183], v29 offset:4096\n\t" "s_waitcnt lgkmcnt(4) vmcnt(15)\n\t" "v_mfma_f32_16x16x32_bf16 v[48:51], a[160:163], a[96:99], v[48:51]\n\t"
    "ds_read_b64_tr_b16 a[184:185], v22 offset:4096\n\t" "global_load_dwordx4 a[148:151], v0, s[40:41] offset:1024 sc1\n\t" "s_waitcnt vmcnt(12)\n\t"
    "v_mfma_f32_16x16x32_bf16 v[52:55], a[160:163], a[100:103], v[52:55]\n\t" "ds_read_b64_tr_b16 a[186:187], v30 offset:4096\n\t" "v_xad_u32 v156, v112, v113, v164\n\t"
    "v_xad_u32 v157, v116, v117, v164\n\t" "v_mfma_f32_16x16x32_bf16 v[60:63], a[164:167], a[100:103], v[60:63]\n\t" "ds_read_b64_tr_b16 a[188:189], v23 offset:4096\n\t"
    "v_xad_u32 v158, v120, v121, v164\n\t" "v_xad_u32 v156, v114, v113, v156\n\t" "v_mfma_f32_16x16x32_bf16 v[56:59], a[164:167], a[96:99], v[56:59]\n\t"
    "ds_read_b64_tr_b16 a[190:191], v31 offset:4096\n\t" "v_xad_u32 v157, v118, v117, v157\n\t" "v_xad_u32 v158, v122, v121, v158\n\t"
    "v_mfma_f32_16x16x32_bf16 v[64:67], a[168:171], a[96:99], v[64:67]\n\t" "ds_read_b64_tr_b16 a[160:161], v20 offset:8192\n\t" "global_load_dwordx4 a[152:155], v0, s[40:41] offset:2048 sc1\n\t"
    "v_xor_b32_e64 v156, v156, v115\n\t" "v_mfma_f32_16x16x32_bf16 v[68:71], a[168:171], a[100:103], v[68:71]\n\t" "ds_read_b64_tr_b16 a[162:163], v28 offset:8192\n\t"
    "v_bitop3_b32 v156, v156, v157, v119 bitop3:0xf6\n\t" "v_bitop3_b32 v156, v156, v158, v123 bitop3:0xf6\n\t" "v_mfma_f32_16x16x32_bf16 v[76:79], a[172:175], a[100:103], v[76:79]\n\t"
    "ds_read_b64_tr_b16 a[164:165], v21 offset:8192\n\t" "v_cmp_ne_u32_e64 vcc, 0, v156\n\t" "v_perm_b32 v115, v112, v113, s49\n\t" "v_mfma_f32_16x16x32_bf16 v[72:75], a[172:175], a[96:99], v[72:75]\n\t"
    "ds_read_b64_tr_b16 a[166:167], v29 offset:8192\n\t" "v_perm_b32 v119, v116, v117, s49\n\t" "v_perm_b32 v123, v120, v121, s49\n\t" "s_waitcnt lgkmcnt(4) vmcnt(11)\n\t"
    "v_mfma_f32_16x16x32_bf16 v[48:51], a[176:179], a[104:107], v[48:51]\n\t" "ds_read_b64_tr_b16 a[168:169], v22 offset:8192\n\t" "global_load_dwordx4 a[156:159], v0, s[40:41] offset:3072 sc1\n\t"
    "s_nop 1\n\t" "v_mfma_f32_16x16x32_bf16 v[52:55], a[176:179], a[108:111], v[52:55]\n\t" "ds_read_b64_tr_b16 a[170:171], v30 offset:8192\n\t" "s_add_u32 s40, s40, 8192\n\t" "s_addc_u32 s41, s41, 0\n\t"
    "v_mfma_f32_16x16x32_bf16 v[60:63], a[180:183], a[108:111], v[60:63]\n\t" "ds_read_b64_tr_b16 a[172:173], v23 offset:8192\n\t" "s_cbranch_vccnz .Lslow_%=\n\t" ".Lgo_%=:\n\t"
    "v_mfma_f32_16x16x32_bf16 v[56:59], a[180:183], a[104:107], v[56:59]\n\t" "ds_read_b64_tr_b16 a[174:175], v31 offset:8192\n\t" "v_perm_b32 v115, v115, v114, s50\n\t"
    "v_perm_b32 v119, v119, v118, s50\n\t" "v_mfma_f32_16x16x32_bf16 v[64:67], a[184:187], a[104:107], v[64:67]\n\t" "ds_read_b64_tr_b16 a[176:177], v20 offset:12288\n\t"
    "global_load_dwordx4 a[64:67], v0, s[40:41] offset:-4096 sc1\n\t" "v_perm_b32 v123, v123, v122, s50\n\t" "v_mfma_f32_16x16x32_bf16 v[68:71], a[184:187], a[108:111], v[68:71]\n\t"
    "ds_read_b64_tr_b16 a[178:179], v28 offset:12288\n\t" "v_permlane32_swap_b32 v112, v114\n\t" "v_permlane32_swap_b32 v116, v118\n\t"
    "v_mfma_f32_16x16x32_bf16 v[76:79], a[188:191], a[108:111], v[76:79]\n\t" "ds_read_b64_tr_b16 a[180:181], v21 offset:12288\n\t" "v_permlane32_swap_b32 v120, v122\n\t"
    "v_permlane32_swap_b32 v113, v115\n\t" "v_mfma_f32_16x16x32_bf16 v[72:75], a[188:191], a[104:107], v[72:75]\n\t" "ds_read_b64_tr_b16 a[182:183], v29 offset:12288\n\t"
    "v_permlane32_swap_b32 v117, v119\n\t" "v_permlane32_swap_b32 v121, v123\n\t" "s_waitcnt lgkmcnt(4) vmcnt(11)\n\t" "v_mfma_f32_16x16x32_bf16 v[48:51], a[160:163], a[112:115], v[48:51]\n\t"
    "ds_read_b64_tr_b16 a[184:185], v22 offset:12288\n\t" "global_load_dwordx4 a[68:71], v0, s[40:41] offset:-3072 sc1\n\t" "v_add_f32 v112, v112, v114\n\t"
    "v_mfma_f32_16x16x32_bf16 v[52:55], a[160:163], a[116:119], v[52:55]\n\t" "ds_read_b64_tr_b16 a[186:187], v30 offset:12288\n\t" "v_add_f32 v116, v116, v118\n\t" "v_add_f32 v120, v120, v122\n\t"
    "v_mfma_f32_16x16x32_bf16 v[60:63], a[164:167], a[116:119], v[60:63]\n\t" "ds_read_b64_tr_b16 a[188:189], v23 offset:12288\n\t" "v_add_f32 v113, v113, v115\n\t" "v_add_f32 v117, v117, v119\n\t"
    "v_mfma_f32_16x16x32_bf16 v[56:59], a[164:167], a[112:115], v[56:59]\n\t" "ds_read_b64_tr_b16 a[190:191], v31 offset:12288\n\t" "v_add_f32 v121, v121, v123\n\t" "v_permlane16_swap_b32 v112, v113\n\t"
    "v_mfma_f32_16x16x32_bf16 v[64:67], a[168:171], a[112:115], v[64:67]\n\t" "ds_read_b64_tr_b16 a[160:161], v20 offset:16384\n\t" "global_load_dwordx4 a[72:75], v0, s[40:41] offset:-2048 sc1\n\t"
    "v_permlane16_swap_b32 v116, v117\n\t" "v_mfma_f32_16x16x32_bf16 v[68:71], a[168:171], a[116:119], v[68:71]\n\t" "ds_read_b64_tr_b16 a[162:163], v28 offset:16384\n\t"
    "v_permlane16_swap_b32 v120, v121\n\t" "v_add_f32_e64 v112, v112, v113\n\t" "v_mfma_f32_16x16x32_bf16 v[76:79], a[172:175], a[116:119], v[76:79]\n\t"
    "ds_read_b64_tr_b16 a[164:165], v21 offset:16384\n\t" "v_add_f32 v116, v116, v117\n\t" "v_add_f32 v120, v120, v121\n\t" "v_mfma_f32_16x16x32_bf16 v[72:75], a[172:175], a[112:115], v[72:75]\n\t"
    "ds_read_b64_tr_b16 a[166:167], v29 offset:16384\n\t" "v_cvt_pk_bf16_f32 v112, v112, v112\n\t" "v_cvt_pk_bf16_f32 v116, v116, v116\n\t" "s_waitcnt lgkmcnt(4) vmcnt(11)\n\t"
    "v_mfma_f32_16x16x32_bf16 v[48:51], a[176:179], a[120:123], v[48:51]\n\t" "ds_read_b64_tr_b16 a[168:169], v22 offset:16384\n\t" "global_load_dwordx4 a[76:79], v0, s[40:41] offset:-1024 sc1\n\t"
    "v_cvt_pk_bf16_f32 v120, v120, v120\n\t" "v_mfma_f32_16x16x32_bf16 v[52:55], a[176:179], a[124:127], v[52:55]\n\t" "ds_read_b64_tr_b16 a[170:171], v30 offset:16384\n\t"
    "v_and_b32 v112, 0xffff0000, v112\n\t" "v_and_b32 v116, 0xffff0000, v116\n\t" "v_mfma_f32_16x16x32_bf16 v[60:63], a[180:183], a[124:127], v[60:63]\n\t"
    "ds_read_b64_tr_b16 a[172:173], v23 offset:16384\n\t" "v_and_b32 v120, 0xffff0000, v120\n\t" "v_add_f32 v116, 1.0, v116\n\t" "v_mfma_f32_16x16x32_bf16 v[56:59], a[180:183], a[120:123], v[56:59]\n\t"
    "ds_read_b64_tr_b16 a[174:175], v31 offset:16384\n\t" "v_cvt_pk_bf16_f32 v116, v116, v116\n\t" "v_and_b32 v116, 0xffff0000, v116\n\t"
    "v_mfma_f32_16x16x32_bf16 v[64:67], a[184:187], a[120:123], v[64:67]\n\t" "ds_read_b64_tr_b16 a[176:177], v20 offset:20480\n\t" "global_load_dwordx4 a[80:83], v0, s[40:41] offset:0 sc1\n\t"
    "ds_write2st64_b32 v15, v112, v116 offset1:1\n\t" "v_mfma_f32_16x16x32_bf16 v[68:71], a[184:187], a[124:127], v[68:71]\n\t" "ds_read_b64_tr_b16 a[178:179], v28 offset:20480\n\t"
    "s_cmp_eq_u32 %[role], 0\n\t" "s_cselect_b64 exec, -1, 0\n\t" "global_store_short_d16_hi v33, v120, %[se] sc1\n\t" "s_mov_b64 exec, -1\n\t"
    "v_mfma_f32_16x16x32_bf16 v[76:79], a[188:191], a[124:127], v[76:79]\n\t" "ds_read_b64_tr_b16 a[180:181], v21 offset:20480\n\t" "s_cmp_eq_u32 %[role], 1\n\t" "s_cselect_b64 exec, -1, 0\n\t"
    "global_store_dword v33, v120, %[se] sc1\n\t" "s_mov_b64 exec, -1\n\t" "v_mfma_f32_16x16x32_bf16 v[72:75], a[188:191], a[120:123], v[72:75]\n\t" "ds_read_b64_tr_b16 a[182:183], v29 offset:20480\n\t"
    "s_cmp_eq_u32 %[role], 2\n\t" "s_cselect_b64 exec, -1, 0\n\t" "global_store_short_d16_hi v33, v116, %[se] sc1\n\t" "s_mov_b64 exec, -1\n\t" "s_waitcnt lgkmcnt(5) vmcnt(11)\n\t"
    "v_mfma_f32_16x16x32_bf16 v[48:51], a[160:163], a[128:131], v[48:51]\n\t" "ds_read_b64_tr_b16 a[184:185], v22 offset:20480\n\t" "global_load_dwordx4 a[84:87], v0, s[40:41] offset:1024 sc1\n\t"
    "s_cmp_eq_u32 %[role], 4\n\t" "s_cselect_b64 exec, 1, 0\n\t" "global_store_dwordx2 v0, v[164:165], %[se]\n\t" "s_mov_b64 exec, -1\n\t"
    "v_mfma_f32_16x16x32_bf16 v[52:55], a[160:163], a[132:135], v[52:55]\n\t" "ds_read_b64_tr_b16 a[186:187], v30 offset:20480\n\t" "ds_read_b128 v[124:127], v32 offset:0\n\t"
    "ds_read_b128 v[128:131], v32 offset:64\n\t" "v_mfma_f32_16x16x32_bf16 v[60:63], a[164:167], a[132:135], v[60:63]\n\t" "ds_read_b64_tr_b16 a[188:189], v23 offset:20480\n\t"
    "ds_read_b128 v[132:135], v32 offset:128\n\t" "ds_read_b128 v[136:139], v32 offset:192\n\t" "v_mfma_f32_16x16x32_bf16 v[56:59], a[164:167], a[128:131], v[56:59]\n\t"
    "ds_read_b64_tr_b16 a[190:191], v31 offset:20480\n\t" "ds_read_b128 v[140:143], v32 offset:256\n\t" "ds_read_b128 v[144:147], v32 offset:320\n\t"
    "v_mfma_f32_16x16x32_bf16 v[64:67], a[168:171], a[128:131], v[64:67]\n\t" "s_waitcnt lgkmcnt(11)\n\t" "ds_read_b64_tr_b16 a[160:161], v20 offset:24576\n\t"
    "global_load_dwordx4 a[88:91], v0, s[40:41] offset:2048 sc1\n\t" "v_mfma_f32_16x16x32_bf16 v[68:71], a[168:171], a[132:135], v[68:71]\n\t" "ds_read_b64_tr_b16 a[162:163], v28 offset:24576\n\t"
    "ds_read_b128 v[148:151], v32 offset:384\n\t" "ds_read_b128 v[152:155], v32 offset:448\n\t" "v_mfma_f32_16x16x32_bf16 v[76:79], a[172:175], a[132:135], v[76:79]\n\t" "s_waitcnt lgkmcnt(11)\n\t"
    "ds_read_b64_tr_b16 a[164:165], v21 offset:24576\n\t" "v_mfma_f32_16x16x32_bf16 v[72:75], a[172:175], a[128:131], v[72:75]\n\t" "ds_read_b64_tr_b16 a[166:167], v29 offset:24576\n\t"
    "s_waitcnt lgkmcnt(8) vmcnt(11)\n\t" "v_mfma_f32_16x16x32_bf16 v[48:51], a[176:179], a[136:139], v[48:51]\n\t" "ds_read_b64_tr_b16 a[168:169], v22 offset:24576\n\t"
    "global_load_dwordx4 a[92:95], v0, s[40:41] offset:3072 sc1\n\t" "v_mfma_f32_16x16x32_bf16 v[52:55], a[176:179], a[140:143], v[52:55]\n\t" "ds_read_b64_tr_b16 a[170:171], v30 offset:24576\n\t"
    "s_add_u32 s40, s40, 8192\n\t" "s_addc_u32 s41, s41, 0\n\t" "v_mfma_f32_16x16x32_bf16 v[60:63], a[180:183], a[140:143], v[60:63]\n\t" "ds_read_b64_tr_b16 a[172:173], v23 offset:24576\n\t"
    "v_mfma_f32_16x16x32_bf16 v[56:59], a[180:183], a[136:139], v[56:59]\n\t" "ds_read_b64_tr_b16 a[174:175], v31 offset:24576\n\t"
    "v_mfma_f32_16x16x32_bf16 v[64:67], a[184:187], a[136:139], v[64:67]\n\t" "ds_read_b64_tr_b16 a[176:177], v20 offset:28672\n\t" "global_load_dwordx4 a[96:99], v0, s[40:41] offset:-4096 sc1\n\t"
    "v_mfma_f32_16x16x32_bf16 v[68:71], a[184:187], a[140:143], v[68:71]\n\t" "ds_read_b64_tr_b16 a[178:179], v28 offset:28672\n\t"
    "v_mfma_f32_16x16x32_bf16 v[76:79], a[188:191], a[140:143], v[76:79]\n\t" "ds_read_b64_tr_b16 a[180:181], v21 offset:28672\n\t"
    "v_mfma_f32_16x16x32_bf16 v[72:75], a[188:191], a[136:139], v[72:75]\n\t" "s_waitcnt lgkmcnt(11)\n\t" "ds_read_b64_tr_b16 a[182:183], v29 offset:28672\n\t" "s_waitcnt lgkmcnt(4) vmcnt(11)\n\t"
    "v_mfma_f32_16x16x32_bf16 v[48:51], a[160:163], a[144:147], v[48:51]\n\t" "ds_read_b64_tr_b16 a[184:185], v22 offset:28672\n\t" "global_load_dwordx4 a[100:103], v0, s[40:41] offset:-3072 sc1\n\t"
    "v_mfma_f32_16x16x32_bf16 v[52:55], a[160:163], a[148:151], v[52:55]\n\t" "ds_read_b64_tr_b16 a[186:187], v30 offset:28672\n\t"
    "v_mfma_f32_16x16x32_bf16 v[60:63], a[164:167], a[148:151], v[60:63]\n\t" "ds_read_b64_tr_b16 a[188:189], v23 offset:28672\n\t"
    "v_mfma_f32_16x16x32_bf16 v[56:59], a[164:167], a[144:147], v[56:59]\n\t" "ds_read_b64_tr_b16 a[190:191], v31 offset:28672\n\t"
    "v_mfma_f32_16x16x32_bf16 v[64:67], a[168:171], a[144:147], v[64:67]\n\t" "ds_read_b64_tr_b16 a[160:161], v20 offset:32768\n\t" "global_load_dwordx4 a[104:107], v0, s[40:41] offset:-2048 sc1\n\t"
    "v_mfma_f32_16x16x32_bf16 v[68:71], a[168:171], a[148:151], v[68:71]\n\t" "ds_read_b64_tr_b16 a[162:163], v28 offset:32768\n\t"
    "v_mfma_f32_16x16x32_bf16 v[76:79], a[172:175], a[148:151], v[76:79]\n\t" "ds_read_b64_tr_b16 a[164:165], v21 offset:32768\n\t"
    "v_mfma_f32_16x16x32_bf16 v[72:75], a[172:175], a[144:147], v[72:75]\n\t" "ds_read_b64_tr_b16 a[166:167], v29 offset:32768\n\t" "s_waitcnt lgkmcnt(4) vmcnt(11)\n\t"
    "v_mfma_f32_16x16x32_bf16 v[48:51], a[176:179], a[152:155], v[48:51]\n\t" "ds_read_b64_tr_b16 a[168:169], v22 offset:32768\n\t" "global_load_dwordx4 a[108:111], v0, s[40:41] offset:-1024 sc1\n\t"
    "v_mfma_f32_16x16x32_bf16 v[52:55], a[176:179], a[156:159], v[52:55]\n\t" "ds_read_b64_tr_b16 a[170:171], v30 offset:32768\n\t"
    "v_mfma_f32_16x16x32_bf16 v[60:63], a[180:183], a[156:159], v[60:63]\n\t" "ds_read_b64_tr_b16 a[172:173], v23 offset:32768\n\t"
    "v_mfma_f32_16x16x32_bf16 v[56:59], a[180:183], a[152:155], v[56:59]\n\t" "ds_read_b64_tr_b16 a[174:175], v31 offset:32768\n\t"
    "v_mfma_f32_16x16x32_bf16 v[64:67], a[184:187], a[152:155], v[64:67]\n\t" "ds_read_b64_tr_b16 a[176:177], v20 offset:36864\n\t" "global_load_dwordx4 a[112:115], v0, s[40:41] offset:0 sc1\n\t"
    "v_mfma_f32_16x16x32_bf16 v[68:71], a[184:187], a[156:159], v[68:71]\n\t" "ds_read_b64_tr_b16 a[178:179], v28 offset:36864\n\t"
    "v_mfma_f32_16x16x32_bf16 v[76:79], a[188:191], a[156:159], v[76:79]\n\t" "ds_read_b64_tr_b16 a[180:181], v21 offset:36864\n\t"
    "v_mfma_f32_16x16x32_bf16 v[72:75], a[188:191], a[152:155], v[72:75]\n\t" "ds_read_b64_tr_b16 a[182:183], v29 offset:36864\n\t" "s_waitcnt lgkmcnt(4) vmcnt(11)\n\t"
    "v_mfma_f32_16x16x32_bf16 v[48:51], a[160:163], a[64:67], v[48:51]\n\t" "ds_read_b64_tr_b16 a[184:185], v22 offset:36864\n\t" "global_load_dwordx4 a[116:119], v0, s[40:41] offset:1024 sc1\n\t"
    "v_mfma_f32_16x16x32_bf16 v[52:55], a[160:163], a[68:71], v[52:55]\n\t" "ds_read_b64_tr_b16 a[186:187], v30 offset:36864\n\t" "v_mfma_f32_16x16x32_bf16 v[60:63], a[164:167], a[68:71], v[60:63]\n\t"
    "ds_read_b64_tr_b16 a[188:189], v23 offset:36864\n\t" "v_mfma_f32_16x16x32_bf16 v[56:59], a[164:167], a[64:67], v[56:59]\n\t" "ds_read_b64_tr_b16 a[190:191], v31 offset:36864\n\t"
    "v_mfma_f32_16x16x32_bf16 v[64:67], a[168:171], a[64:67], v[64:67]\n\t" "ds_read_b64_tr_b16 a[160:161], v20 offset:40960\n\t" "global_load_dwordx4 a[120:123], v0, s[40:41] offset:2048 sc1\n\t"
    "v_mfma_f32_16x16x32_bf16 v[68:71], a[168:171], a[68:71], v[68:71]\n\t" "ds_read_b64_tr_b16 a[162:163], v28 offset:40960\n\t" "v_mfma_f32_16x16x32_bf16 v[76:79], a[172:175], a[68:71], v[76:79]\n\t"
    "ds_read_b64_tr_b16 a[164:165], v21 offset:40960\n\t" "v_mfma_f32_16x16x32_bf16 v[72:75], a[172:175], a[64:67], v[72:75]\n\t" "ds_read_b64_tr_b16 a[166:167], v29 offset:40960\n\t"
    "s_waitcnt lgkmcnt(4) vmcnt(11)\n\t" "v_mfma_f32_16x16x32_bf16 v[48:51], a[176:179], a[72:75], v[48:51]\n\t" "ds_read_b64_tr_b16 a[168:169], v22 offset:40960\n\t"
    "global_load_dwordx4 a[124:127], v0, s[40:41] offset:3072 sc1\n\t" "v_mfma_f32_16x16x32_bf16 v[52:55], a[176:179], a[76:79], v[52:55]\n\t" "ds_read_b64_tr_b16 a[170:171], v30 offset:40960\n\t"
    "s_add_u32 s40, s40, 8192\n\t" "s_addc_u32 s41, s41, 0\n\t" "v_mfma_f32_16x16x32_bf16 v[60:63], a[180:183], a[76:79], v[60:63]\n\t" "ds_read_b64_tr_b16 a[172:173], v23 offset:40960\n\t"
    "s_mov_b64 s[78:79], %[sb]\n\t" "s_mov_b32 s82, -1\n\t" "s_mov_b32 s83, 0\n\t" "s_add_u32 m0, %[sl], 86016\n\t" "s_nop 0\n\t" "global_load_lds_dwordx4 v1, s[78:79]\n\t"
    "v_mfma_f32_16x16x32_bf16 v[56:59], a[180:183], a[72:75], v[56:59]\n\t" "ds_read_b64_tr_b16 a[174:175], v31 offset:40960\n\t" "v_mfma_f32_16x16x32_bf16 v[64:67], a[184:187], a[72:75], v[64:67]\n\t"
    "ds_read_b64_tr_b16 a[176:177], v20 offset:45056\n\t" "v_mfma_f32_16x16x32_bf16 v[68:71], a[184:187], a[76:79], v[68:71]\n\t" "ds_read_b64_tr_b16 a[178:179], v28 offset:45056\n\t"
    "v_mfma_f32_16x16x32_bf16 v[76:79], a[188:191], a[76:79], v[76:79]\n\t" "ds_read_b64_tr_b16 a[180:181], v21 offset:45056\n\t" "s_add_u32 m0, %[sl], 87040\n\t" "s_nop 0\n\t"
    "global_load_lds_dwordx4 v2, s[78:79]\n\t" "v_mfma_f32_16x16x32_bf16 v[72:75], a[188:191], a[72:75], v[72:75]\n\t" "ds_read_b64_tr_b16 a[182:183], v29 offset:45056\n\t"
    "s_waitcnt lgkmcnt(4) vmcnt(12)\n\t" "v_mfma_f32_16x16x32_bf16 v[48:51], a[160:163], a[80:83], v[48:51]\n\t" "ds_read_b64_tr_b16 a[184:185], v22 offset:45056\n\t"
    "v_mfma_f32_16x16x32_bf16 v[52:55], a[160:163], a[84:87], v[52:55]\n\t" "ds_read_b64_tr_b16 a[186:187], v30 offset:45056\n\t" "v_mfma_f32_16x16x32_bf16 v[60:63], a[164:167], a[84:87], v[60:63]\n\t"
    "ds_read_b64_tr_b16 a[188:189], v23 offset:45056\n\t" "s_add_u32 m0, %[sl], 88064\n\t" "s_nop 0\n\t" "global_load_lds_dwordx4 v3, s[78:79]\n\t"
    "v_mfma_f32_16x16x32_bf16 v[56:59], a[164:167], a[80:83], v[56:59]\n\t" "ds_read_b64_tr_b16 a[190:191], v31 offset:45056\n\t" "v_mfma_f32_16x16x32_bf16 v[64:67], a[168:171], a[80:83], v[64:67]\n\t"
    "ds_read_b64_tr_b16 a[160:161], v20 offset:49152\n\t" "v_mfma_f32_16x16x32_bf16 v[68:71], a[168:171], a[84:87], v[68:71]\n\t" "ds_read_b64_tr_b16 a[162:163], v28 offset:49152\n\t"
    "v_mfma_f32_16x16x32_bf16 v[76:79], a[172:175], a[84:87], v[76:79]\n\t" "ds_read_b64_tr_b16 a[164:165], v21 offset:49152\n\t" "s_add_u32 m0, %[sl], 89088\n\t" "s_nop 0\n\t"
    "s_mov_b64 exec, s[82:83]\n\t" "global_load_lds_dwordx4 v4, s[78:79]\n\t" "s_mov_b64 exec, -1\n\t" "s_add_u32 s78, s78, 229376\n\t" "s_addc_u32 s79, s79, 0\n\t"
    "v_mfma_f32_16x16x32_bf16 v[72:75], a[172:175], a[80:83], v[72:75]\n\t" "ds_read_b64_tr_b16 a[166:167], v29 offset:49152\n\t" "s_waitcnt lgkmcnt(4) vmcnt(12)\n\t"
    "v_mfma_f32_16x16x32_bf16 v[48:51], a[176:179], a[88:91], v[48:51]\n\t" "ds_read_b64_tr_b16 a[168:169], v22 offset:49152\n\t" "v_mfma_f32_16x16x32_bf16 v[52:55], a[176:179], a[92:95], v[52:55]\n\t"
    "ds_read_b64_tr_b16 a[170:171], v30 offset:49152\n\t" "v_mfma_f32_16x16x32_bf16 v[60:63], a[180:183], a[92:95], v[60:63]\n\t" "ds_read_b64_tr_b16 a[172:173], v23 offset:49152\n\t"
    "s_add_u32 m0, %[sl], 100352\n\t" "s_nop 0\n\t" "global_load_lds_dwordx4 v1, s[78:79]\n\t" "v_mfma_f32_16x16x32_bf16 v[56:59], a[180:183], a[88:91], v[56:59]\n\t"
    "ds_read_b64_tr_b16 a[174:175], v31 offset:49152\n\t" "v_mfma_f32_16x16x32_bf16 v[64:67], a[184:187], a[88:91], v[64:67]\n\t" "ds_read_b64_tr_b16 a[176:177], v20 offset:53248\n\t"
    "v_mfma_f32_16x16x32_bf16 v[68:71], a[184:187], a[92:95], v[68:71]\n\t" "ds_read_b64_tr_b16 a[178:179], v28 offset:53248\n\t" "v_mfma_f32_16x16x32_bf16 v[76:79], a[188:191], a[92:95], v[76:79]\n\t"
    "ds_read_b64_tr_b16 a[180:181], v21 offset:53248\n\t" "s_add_u32 m0, %[sl], 101376\n\t" "s_nop 0\n\t" "global_load_lds_dwordx4 v2, s[78:79]\n\t"
    "v_mfma_f32_16x16x32_bf16 v[72:75], a[188:191], a[88:91], v[72:75]\n\t" "ds_read_b64_tr_b16 a[182:183], v29 offset:53248\n\t" "s_waitcnt lgkmcnt(4) vmcnt(12)\n\t"
    "v_mfma_f32_16x16x32_bf16 v[48:51], a[160:163], a[96:99], v[48:51]\n\t" "ds_read_b64_tr_b16 a[184:185], v22 offset:53248\n\t" "v_mfma_f32_16x16x32_bf16 v[52:55], a[160:163], a[100:103], v[52:55]\n\t"
    "ds_read_b64_tr_b16 a[186:187], v30 offset:53248\n\t" "v_mfma_f32_16x16x32_bf16 v[60:63], a[164:167], a[100:103], v[60:63]\n\t" "ds_read_b64_tr_b16 a[188:189], v23 offset:53248\n\t"
    "s_add_u32 m0, %[sl], 102400\n\t" "s_nop 0\n\t" "global_load_lds_dwordx4 v3, s[78:79]\n\t" "v_mfma_f32_16x16x32_bf16 v[56:59], a[164:167], a[96:99], v[56:59]\n\t"
    "ds_read_b64_tr_b16 a[190:191], v31 offset:53248\n\t" "v_mfma_f32_16x16x32_bf16 v[64:67], a[168:171], a[96:99], v[64:67]\n\t" "ds_read_b64_tr_b16 a[160:161], v20 offset:57344\n\t"
    "v_mfma_f32_16x16x32_bf16 v[68:71], a[168:171], a[100:103], v[68:71]\n\t" "ds_read_b64_tr_b16 a[162:163], v28 offset:57344\n\t"
    "v_mfma_f32_16x16x32_bf16 v[76:79], a[172:175], a[100:103], v[76:79]\n\t" "ds_read_b64_tr_b16 a[164:165], v21 offset:57344\n\t" "s_add_u32 m0, %[sl], 103424\n\t" "s_nop 0\n\t"
    "s_mov_b64 exec, s[82:83]\n\t" "global_load_lds_dwordx4 v4, s[78:79]\n\t" "s_mov_b64 exec, -1\n\t" "s_add_u32 s78, s78, 229376\n\t" "s_addc_u32 s79, s79, 0\n\t"
    "v_mfma_f32_16x16x32_bf16 v[72:75], a[172:175], a[96:99], v[72:75]\n\t" "ds_read_b64_tr_b16 a[166:167], v29 offset:57344\n\t" "s_waitcnt lgkmcnt(4) vmcnt(12)\n\t"
    "v_mfma_f32_16x16x32_bf16 v[48:51], a[176:179], a[104:107], v[48:51]\n\t" "ds_read_b64_tr_b16 a[168:169], v22 offset:57344\n\t"
    "v_mfma_f32_16x16x32_bf16 v[52:55], a[176:179], a[108:111], v[52:55]\n\t" "ds_read_b64_tr_b16 a[170:171], v30 offset:57344\n\t"
    "v_mfma_f32_16x16x32_bf16 v[60:63], a[180:183], a[108:111], v[60:63]\n\t" "ds_read_b64_tr_b16 a[172:173], v23 offset:57344\n\t"
    "v_mfma_f32_16x16x32_bf16 v[56:59], a[180:183], a[104:107], v[56:59]\n\t" "ds_read_b64_tr_b16 a[174:175], v31 offset:57344\n\t"
    "v_mfma_f32_16x16x32_bf16 v[64:67], a[184:187], a[104:107], v[64:67]\n\t" "ds_read_b64_tr_b16 a[176:177], v20 offset:61440\n\t"
    "v_mfma_f32_16x16x32_bf16 v[68:71], a[184:187], a[108:111], v[68:71]\n\t" "ds_read_b64_tr_b16 a[178:179], v28 offset:61440\n\t"
    "v_mfma_f32_16x16x32_bf16 v[76:79], a[188:191], a[108:111], v[76:79]\n\t" "ds_read_b64_tr_b16 a[180:181], v21 offset:61440\n\t"
    "v_mfma_f32_16x16x32_bf16 v[72:75], a[188:191], a[104:107], v[72:75]\n\t" "ds_read_b64_tr_b16 a[182:183], v29 offset:61440\n\t" "s_waitcnt lgkmcnt(4) vmcnt(10)\n\t"
    "v_mfma_f32_16x16x32_bf16 v[48:51], a[160:163], a[112:115], v[48:51]\n\t" "ds_read_b64_tr_b16 a[184:185], v22 offset:61440\n\t"
    "v_mfma_f32_16x16x32_bf16 v[52:55], a[160:163], a[116:119], v[52:55]\n\t" "ds_read_b64_tr_b16 a[186:187], v30 offset:61440\n\t"
    "v_mfma_f32_16x16x32_bf16 v[60:63], a[164:167], a[116:119], v[60:63]\n\t" "ds_read_b64_tr_b16 a[188:189], v23 offset:61440\n\t"
    "v_mfma_f32_16x16x32_bf16 v[56:59], a[164:167], a[112:115], v[56:59]\n\t" "ds_read_b64_tr_b16 a[190:191], v31 offset:61440\n\t"
    "v_mfma_f32_16x16x32_bf16 v[64:67], a[168:171], a[112:115], v[64:67]\n\t" "v_mfma_f32_16x16x32_bf16 v[68:71], a[168:171], a[116:119], v[68:71]\n\t"
    "v_mfma_f32_16x16x32_bf16 v[76:79], a[172:175], a[116:119], v[76:79]\n\t" "v_mfma_f32_16x16x32_bf16 v[72:75], a[172:175], a[112:115], v[72:75]\n\t" "s_waitcnt lgkmcnt(0) vmcnt(8)\n\t"
    "v_mfma_f32_16x16x32_bf16 v[48:51], a[176:179], a[120:123], v[48:51]\n\t" "v_mfma_f32_16x16x32_bf16 v[52:55], a[176:179], a[124:127], v[52:55]\n\t"
    "v_mfma_f32_16x16x32_bf16 v[60:63], a[180:183], a[124:127], v[60:63]\n\t" "v_mfma_f32_16x16x32_bf16 v[56:59], a[180:183], a[120:123], v[56:59]\n\t"
    "v_mfma_f32_16x16x32_bf16 v[64:67], a[184:187], a[120:123], v[64:67]\n\t" "v_mfma_f32_16x16x32_bf16 v[68:71], a[184:187], a[124:127], v[68:71]\n\t"
    "v_mfma_f32_16x16x32_bf16 v[76:79], a[188:191], a[124:127], v[76:79]\n\t" "v_mfma_f32_16x16x32_bf16 v[72:75], a[188:191], a[120:123], v[72:75]\n\t" "s_nop 1\n\t"
    "v_pk_fma_f32 v[48:49], v[124:125], v[48:49], v[80:81]\n\t" "v_pk_fma_f32 v[50:51], v[126:127], v[50:51], v[82:83]\n\t" "v_pk_fma_f32 v[56:57], v[128:129], v[56:57], v[88:89]\n\t"
    "v_pk_fma_f32 v[58:59], v[130:131], v[58:59], v[90:91]\n\t" "v_pk_mul_f32 v[156:157], v[48:49], v[140:141]\n\t" "v_pk_mul_f32 v[158:159], v[50:51], v[142:143]\n\t"
    "v_pk_mul_f32 v[160:161], v[56:57], v[144:145]\n\t" "v_pk_mul_f32 v[162:163], v[58:59], v[146:147]\n\t" "v_cvt_pk_bf16_f32 v164, v156, v157\n\t" "v_cvt_pk_bf16_f32 v165, v158, v159\n\t"
    "v_cvt_pk_bf16_f32 v166, v160, v161\n\t" "v_cvt_pk_bf16_f32 v167, v162, v163\n\t" "global_store_dwordx4 v0, v[164:167], %[sy0] offset:0 sc1\n\t"
    "v_pk_fma_f32 v[52:53], v[124:125], v[52:53], v[84:85]\n\t" "v_pk_fma_f32 v[54:55], v[126:127], v[54:55], v[86:87]\n\t" "v_pk_fma_f32 v[60:61], v[128:129], v[60:61], v[92:93]\n\t"
    "v_pk_fma_f32 v[62:63], v[130:131], v[62:63], v[94:95]\n\t" "v_pk_mul_f32 v[172:173], v[52:53], v[140:141]\n\t" "v_pk_mul_f32 v[174:175], v[54:55], v[142:143]\n\t"
    "v_pk_mul_f32 v[176:177], v[60:61], v[144:145]\n\t" "v_pk_mul_f32 v[178:179], v[62:63], v[146:147]\n\t" "v_cvt_pk_bf16_f32 v180, v172, v173\n\t" "v_cvt_pk_bf16_f32 v181, v174, v175\n\t"
    "v_cvt_pk_bf16_f32 v182, v176, v177\n\t" "v_cvt_pk_bf16_f32 v183, v178, v179\n\t" "global_store_dwordx4 v0, v[180:183], %[sy0] offset:1024 sc1\n\t"
    "v_pk_fma_f32 v[64:65], v[132:133], v[64:65], v[96:97]\n\t" "v_pk_fma_f32 v[66:67], v[134:135], v[66:67], v[98:99]\n\t" "v_pk_fma_f32 v[72:73], v[136:137], v[72:73], v[104:105]\n\t"
    "v_pk_fma_f32 v[74:75], v[138:139], v[74:75], v[106:107]\n\t" "v_pk_mul_f32 v[156:157], v[64:65], v[148:149]\n\t" "v_pk_mul_f32 v[158:159], v[66:67], v[150:151]\n\t"
    "v_pk_mul_f32 v[160:161], v[72:73], v[152:153]\n\t" "v_pk_mul_f32 v[162:163], v[74:75], v[154:155]\n\t" "v_cvt_pk_bf16_f32 v168, v156, v157\n\t" "v_cvt_pk_bf16_f32 v169, v158, v159\n\t"
    "v_cvt_pk_bf16_f32 v170, v160, v161\n\t" "v_cvt_pk_bf16_f32 v171, v162, v163\n\t" "global_store_dwordx4 v0, v[168:171], %[sy1] offset:0 sc1\n\t"
    "v_pk_fma_f32 v[68:69], v[132:133], v[68:69], v[100:101]\n\t" "v_pk_fma_f32 v[70:71], v[134:135], v[70:71], v[102:103]\n\t" "v_pk_fma_f32 v[76:77], v[136:137], v[76:77], v[108:109]\n\t"
    "v_pk_fma_f32 v[78:79], v[138:139], v[78:79], v[110:111]\n\t" "v_pk_mul_f32 v[172:173], v[68:69], v[148:149]\n\t" "v_pk_mul_f32 v[174:175], v[70:71], v[150:151]\n\t"
    "v_pk_mul_f32 v[176:177], v[76:77], v[152:153]\n\t" "v_pk_mul_f32 v[178:179], v[78:79], v[154:155]\n\t" "v_cvt_pk_bf16_f32 v184, v172, v173\n\t" "v_cvt_pk_bf16_f32 v185, v174, v175\n\t"
    "v_cvt_pk_bf16_f32 v186, v176, v177\n\t" "v_cvt_pk_bf16_f32 v187, v178, v179\n\t" "global_store_dwordx4 v0, v[184:187], %[sy1] offset:1024 sc1\n\t" "v_pk_mul_f32 v[114:115], v[48:49], v[48:49]\n\t"
    "v_pk_mul_f32 v[118:119], v[52:53], v[52:53]\n\t" "v_pk_add_f32 v[112:113], v[50:51], v[48:49]\n\t" "v_pk_fma_f32 v[114:115], v[50:51], v[50:51], v[114:115]\n\t"
    "v_pk_add_f32 v[116:117], v[54:55], v[52:53]\n\t" "v_pk_fma_f32 v[118:119], v[54:55], v[54:55], v[118:119]\n\t" "v_pk_add_f32 v[112:113], v[56:57], v[112:113]\n\t"
    "v_pk_fma_f32 v[114:115], v[56:57], v[56:57], v[114:115]\n\t" "v_pk_add_f32 v[116:117], v[60:61], v[116:117]\n\t" "v_pk_fma_f32 v[118:119], v[60:61], v[60:61], v[118:119]\n\t"
    "v_pk_add_f32 v[112:113], v[58:59], v[112:113]\n\t" "v_pk_fma_f32 v[114:115], v[58:59], v[58:59], v[114:115]\n\t" "v_pk_add_f32 v[116:117], v[62:63], v[116:117]\n\t"
    "v_pk_fma_f32 v[118:119], v[62:63], v[62:63], v[118:119]\n\t" "v_pk_add_f32 v[112:113], v[64:65], v[112:113]\n\t" "v_pk_fma_f32 v[114:115], v[64:65], v[64:65], v[114:115]\n\t"
    "v_pk_add_f32 v[116:117], v[68:69], v[116:117]\n\t" "v_pk_fma_f32 v[118:119], v[68:69], v[68:69], v[118:119]\n\t" "v_pk_add_f32 v[112:113], v[66:67], v[112:113]\n\t"
    "v_pk_fma_f32 v[114:115], v[66:67], v[66:67], v[114:115]\n\t" "v_pk_add_f32 v[116:117], v[70:71], v[116:117]\n\t" "v_pk_fma_f32 v[118:119], v[70:71], v[70:71], v[118:119]\n\t"
    "v_pk_add_f32 v[112:113], v[72:73], v[112:113]\n\t" "v_pk_fma_f32 v[114:115], v[72:73], v[72:73], v[114:115]\n\t" "v_pk_add_f32 v[116:117], v[76:77], v[116:117]\n\t"
    "v_pk_fma_f32 v[118:119], v[76:77], v[76:77], v[118:119]\n\t" "v_pk_add_f32 v[112:113], v[74:75], v[112:113]\n\t" "v_pk_fma_f32 v[114:115], v[74:75], v[74:75], v[114:115]\n\t"
    "v_pk_add_f32 v[116:117], v[78:79], v[116:117]\n\t" "v_pk_fma_f32 v[118:119], v[78:79], v[78:79], v[118:119]\n\t" "v_add_f32 v188, v112, v113\n\t" "v_add_f32 v189, v114, v115\n\t"
    "v_add_f32 v190, v116, v117\n\t" "v_add_f32_e64 v191, v118, v119\n\t" "v_pk_add_f32 v[48:49], v[48:49], v[236:237] op_sel:[0,0] op_sel_hi:[1,0]\n\t"
    "v_pk_add_f32 v[50:51], v[50:51], v[236:237] op_sel:[0,0] op_sel_hi:[1,0]\n\t" "v_permlane16_swap_b32 v188, v190\n\t" "v_permlane16_swap_b32 v189, v191\n\t" "v_add_f32 v188, v188, v190\n\t"
    "v_add_f32 v189, v189, v191\n\t" "v_mov_b32 v190, v188\n\t" "v_mov_b32 v191, v189\n\t" "v_pk_add_f32 v[56:57], v[56:57], v[236:237] op_sel:[0,0] op_sel_hi:[1,0]\n\t"
    "v_pk_add_f32 v[58:59], v[58:59], v[236:237] op_sel:[0,0] op_sel_hi:[1,0]\n\t" "v_permlane32_swap_b32 v188, v190\n\t" "v_permlane32_swap_b32 v189, v191\n\t" "v_add_f32 v188, v188, v190\n\t"
    "v_add_f32_e64 v189, v189, v191\n\t" "s_mov_b32 exec_hi, 0\n\t" "global_store_dwordx2 v34, v[188:189], %[ss] sc1\n\t" "s_mov_b64 exec, -1\n\t"
    "v_pk_add_f32 v[64:65], v[64:65], v[236:237] op_sel:[0,0] op_sel_hi:[1,0]\n\t" "v_pk_add_f32 v[66:67], v[66:67], v[236:237] op_sel:[0,0] op_sel_hi:[1,0]\n\t"
    "v_pk_add_f32 v[72:73], v[72:73], v[236:237] op_sel:[0,0] op_sel_hi:[1,0]\n\t" "v_pk_add_f32 v[74:75], v[74:75], v[236:237] op_sel:[0,0] op_sel_hi:[1,0]\n\t"
    "v_pk_add_f32 v[52:53], v[52:53], v[236:237] op_sel:[0,1] op_sel_hi:[1,1]\n\t" "v_pk_add_f32 v[54:55], v[54:55], v[236:237] op_sel:[0,1] op_sel_hi:[1,1]\n\t"
    "v_pk_add_f32 v[60:61], v[60:61], v[236:237] op_sel:[0,1] op_sel_hi:[1,1]\n\t" "v_pk_add_f32 v[62:63], v[62:63], v[236:237] op_sel:[0,1] op_sel_hi:[1,1]\n\t"
    "v_pk_add_f32 v[68:69], v[68:69], v[236:237] op_sel:[0,1] op_sel_hi:[1,1]\n\t" "v_pk_add_f32 v[70:71], v[70:71], v[236:237] op_sel:[0,1] op_sel_hi:[1,1]\n\t"
    "v_pk_add_f32 v[76:77], v[76:77], v[236:237] op_sel:[0,1] op_sel_hi:[1,1]\n\t" "v_pk_add_f32 v[78:79], v[78:79], v[236:237] op_sel:[0,1] op_sel_hi:[1,1]\n\t" "v_cvt_pk_bf16_f32 v80, v48, v49\n\t"
    "v_cvt_pk_bf16_f32 v81, v50, v51\n\t" "v_cvt_pk_bf16_f32 v82, v56, v57\n\t" "v_cvt_pk_bf16_f32 v83, v58, v59\n\t" "v_cvt_pk_bf16_f32 v84, v64, v65\n\t" "v_cvt_pk_bf16_f32 v85, v66, v67\n\t"
    "v_cvt_pk_bf16_f32 v86, v72, v73\n\t" "v_cvt_pk_bf16_f32 v87, v74, v75\n\t" "v_cvt_pk_bf16_f32 v88, v52, v53\n\t" "v_cvt_pk_bf16_f32 v89, v54, v55\n\t" "v_cvt_pk_bf16_f32 v90, v60, v61\n\t"
    "v_cvt_pk_bf16_f32 v91, v62, v63\n\t" "v_cvt_pk_bf16_f32 v92, v68, v69\n\t" "v_cvt_pk_bf16_f32 v93, v70, v71\n\t" "v_cvt_pk_bf16_f32 v94, v76, v77\n\t" "v_cvt_pk_bf16_f32 v95, v78, v79\n\t"
    "v_mov_b64 v[192:193], %[tag]\n\t" "v_mov_b32_e64 v194, 0\n\t" "s_waitcnt vmcnt(0)\n\t" "s_barrier\n\t" "s_cmp_eq_u32 %[wid], 0\n\t" "s_cselect_b64 exec, 1, 0\n\t"
    "global_store_dwordx2 v194, v[192:193], %[sf] sc1\n\t" "s_mov_b64 exec, -1\n\t" "ds_write_b32 v36, v194\n\t" "ds_write_b128 v37, v[80:83] offset:0\n\t" "ds_write_b128 v37, v[84:87] offset:4096\n\t"
    "ds_write_b128 v37, v[88:91] offset:8192\n\t" "ds_write_b128 v37, v[92:95] offset:12288\n\t" "s_add_u32 m0, %[sl], 0\n\t" "s_nop 0\n\t" "global_load_lds_dwordx4 v1, s[78:79]\n\t"
    "s_add_u32 m0, %[sl], 1024\n\t" "s_nop 0\n\t" "global_load_lds_dwordx4 v2, s[78:79]\n\t" "s_add_u32 m0, %[sl], 2048\n\t" "s_nop 0\n\t" "global_load_lds_dwordx4 v3, s[78:79]\n\t"
    "s_add_u32 m0, %[sl], 3072\n\t" "s_nop 0\n\t" "s_mov_b64 exec, s[82:83]\n\t" "global_load_lds_dwordx4 v4, s[78:79]\n\t" "s_mov_b64 exec, -1\n\t" "s_add_u32 s78, s78, 229376\n\t"
    "s_addc_u32 s79, s79, 0\n\t" "s_add_u32 m0, %[sl], 14336\n\t" "s_nop 0\n\t" "global_load_lds_dwordx4 v1, s[78:79]\n\t" "s_add_u32 m0, %[sl], 15360\n\t" "s_nop 0\n\t"
    "global_load_lds_dwordx4 v2, s[78:79]\n\t" "s_add_u32 m0, %[sl], 16384\n\t" "s_nop 0\n\t" "global_load_lds_dwordx4 v3, s[78:79]\n\t" "s_add_u32 m0, %[sl], 17408\n\t" "s_nop 0\n\t"
    "s_mov_b64 exec, s[82:83]\n\t" "global_load_lds_dwordx4 v4, s[78:79]\n\t" "s_mov_b64 exec, -1\n\t" "s_add_u32 s78, s78, 229376\n\t" "s_addc_u32 s79, s79, 0\n\t" "s_branch .Lend_%=\n\t"
    ".Lslow_%=:\n\t" "s_sleep 2\n\t" "global_load_dwordx4 v[112:115], v0, %[sg0] sc1\n\t" "global_load_dwordx4 v[116:119], v0, %[sg1] sc1\n\t" "global_load_dwordx4 v[120:123], v0, %[sg2] sc1\n\t"
    "s_waitcnt vmcnt(0)\n\t" "v_xad_u32 v156, v112, v113, v164\n\t" "v_xad_u32 v157, v116, v117, v164\n\t" "v_xad_u32 v158, v120, v121, v164\n\t" "v_xad_u32 v156, v114, v113, v156\n\t"
    "v_xad_u32 v157, v118, v117, v157\n\t" "v_xad_u32 v158, v122, v121, v158\n\t" "v_xor_b32 v156, v156, v115\n\t" "v_bitop3_b32 v156, v156, v157, v119 bitop3:0xf6\n\t"
    "v_bitop3_b32 v156, v156, v158, v123 bitop3:0xf6\n\t" "v_cmp_ne_u32_e64 vcc, 0, v156\n\t" "v_perm_b32 v115, v112, v113, s49\n\t" "v_perm_b32 v119, v116, v117, s49\n\t"
    "v_perm_b32 v123, v120, v121, s49\n\t" "s_nop 1\n\t" "s_cbranch_vccz .Lgo_%=\n\t" "s_sub_u32 s44, s44, 1\n\t" "s_cbranch_scc0 .Lslow_%=\n\t" "s_branch .Lgo_%=\n\t" ".Lend_%=:\n\t"
    : "+{v[80:83]}"(xr[0]), "+{v[84:87]}"(xr[1]), "+{v[88:91]}"(xr[2]), "+{v[92:95]}"(xr[3]), "+{v[96:99]}"(xr[4]), "+{v[100:103]}"(xr[5]), "+{v[104:107]}"(xr[6]), "+{v[108:111]}"(xr[7])
    : "{v0}"(va), "{v15}"(tw), "{v32}"(tr), "{v33}"(ve), "{v34}"(vs), "{v36}"(zb), "{v37}"(xw), "{v236}"(mu[0]), "{v237}"(mu[1]), "{v1}"(vb[0]), "{v2}"(vb[1]), "{v3}"(vb[2]), "{v4}"(vb[3]), "{v16}"(vr[0]), "{v17}"(vr[1]), "{v18}"(vr[2]), "{v19}"(vr[3]), "{v20}"(vr[4]), "{v21}"(vr[5]), "{v22}"(vr[6]), "{v23}"(vr[7]), "{v24}"(vr[8]), "{v25}"(vr[9]), "{v26}"(vr[10]), "{v27}"(vr[11]), "{v28}"(vr[12]), "{v29}"(vr[13]), "{v30}"(vr[14]), "{v31}"(vr[15]), [sa] "s"(sa + 8192), [tag] "s"(tag), [wid] "s"(wid), [role] "s"(role), [sf] "s"(sf), [sg0] "s"(sg0), [sg1] "s"(sg1), [sg2] "s"(sg2), [se] "s"(se), [sy0] "s"(sy0), [sy1] "s"(sy1), [ss] "s"(ss), [sb] "s"(sb), [sl] "s"(sl), [cd] "s"(cd)
    : "v48", "v49", "v50", "v51", "v52", "v53", "v54", "v55", "v56", "v57", "v58", "v59", "v60", "v61", "v62", "v63", "v64", "v65", "v66", "v67", "v68", "v69", "v70", "v71", "v72", "v73", "v74", "v75", "v76", "v77", "v78", "v79", "v112", "v113", "v114", "v115", "v116", "v117", "v118", "v119", "v120", "v121", "v122", "v123", "v124", "v125", "v126", "v127", "v128", "v129", "v130", "v131", "v132", "v133", "v134", "v135", "v136", "v137", "v138", "v139", "v140", "v141", "v142", "v143", "v144", "v145", "v146", "v147", "v148", "v149", "v150", "v151", "v152", "v153", "v154", "v155", "v156", "v157", "v158", "v159", "v160", "v161", "v162", "v163", "v164", "v165", "v166", "v167", "v168", "v169", "v170", "v171", "v172", "v173", "v174", "v175", "v176", "v177", "v178", "v179", "v180", "v181", "v182", "v183", "v184", "v185", "v186", "v187", "v188", "v189", "v190", "v191", "v192", "v193", "v194", "v195", "a0", "a1", "a2", "a3", "a4", "a5", "a6", "a7", "a8", "a9", "a10", "a11", "a12", "a13", "a14", "a15", "a16", "a17", "a18", "a19", "a20", "a21", "a22", "a23", "a24", "a25", "a26", "a27", "a28", "a29", "a30", "a31", "a32", "a33", "a34", "a35", "a36", "a37", "a38", "a39", "a40", "a41", "a42", "a43", "a44", "a45", "a46", "a47", "a48", "a49", "a50", "a51", "a52", "a53", "a54", "a55", "a56", "a57", "a58", "a59", "a60", "a61", "a62", "a63", "a64", "a65", "a66", "a67", "a68", "a69", "a70", "a71", "a72", "a73", "a74", "a75", "a76", "a77", "a78", "a79", "a80", "a81", "a82", "a83", "a84", "a85", "a86", "a87", "a88", "a89", "a90", "a91", "a92", "a93", "a94", "a95", "a96", "a97", "a98", "a99", "a100", "a101", "a102", "a103", "a104", "a105", "a106", "a107", "a108", "a109", "a110", "a111", "a112", "a113", "a114", "a115", "a116", "a117", "a118", "a119", "a120", "a121", "a122", "a123", "a124", "a125", "a126", "a127", "a128", "a129", "a130", "a131", "a132", "a133", "a134", "a135", "a136", "a137", "a138", "a139", "a140", "a141", "a142", "a143", "a144", "a145", "a146", "a147", "a148", "a149", "a150", "a151", "a152", "a153", "a154", "a155", "a156", "a157", "a158", "a159", "a160", "a161", "a162", "a163", "a164", "a165", "a166", "a167", "a168", "a169", "a170", "a171", "a172", "a173", "a174", "a175", "a176", "a177", "a178", "a179", "a180", "a181", "a182", "a183", "a184", "a185", "a186", "a187", "a188", "a189", "a190", "a191", "s40", "s41", "s42", "s43", "s44", "s45", "s46", "s47", "s48", "s49", "s50", "s78", "s79", "s82", "s83", "vcc", "scc", "memory");
  }
  asm volatile("" : "+s"(bx), "+s"(wid), "+s"(ws)); asm volatile("" : "+s"(tag)); asm volatile("" : "+v"(tid));
  {
  const int xcd = bx & 7, loc = bx >> 3;
  const int lane = tid & 63, rot = xcd & 1, rb = (xcd & 1) * 4 + (loc & 3), cb = (xcd >> 1) * 8 + (loc >> 2);
  GW(); const unsigned va = lane * 16; const char* sa = ws + (unsigned)(W_Y + rb * 524288 + wid * YW + rot * 16 * YS); const char* sq = ws + W_TAGS + YF * 8; const char* scnt = ws + (unsigned)(W_TAGS + HF * 8 + FA(rb * 32 + cb));
  const unsigned fi = lane < 32 ? rb * 32 + lane : ((xcd >> 1) * 4 + (lane & 3)) * 16 + (lane & 4) * 2 + (loc >> 2), vf = FA(fi);
  auto grp = [&](int i) { const int q = i + (wid < 3 ? wid : 0); return i == 3 ? 3 : q < 3 ? q : q - 3; };
  unsigned vr[8], gt[4], go[4];
  const unsigned ct = ldsb + (lane < 16 ? TAB + lane * 16 : ZB);
#pragma unroll
  for (int m = 0; m < 4; ++m) {
    vr[2 * m] = ldsb + GB + (lane >> 2) * 224 + (lane & 3) * 8 + (m < 3 ? grp(m) * 64 + (cb & 1) * 32 : (cb & 1) ? 0 : 192); vr[2 * m + 1] = vr[2 * m] + 4 * 14336;
    gt[m] = grp(m) * 1024;
    go[m] = m < 3 ? ((cb & 1) * 4 + grp(m)) * 2048 : 3 * 2048 + (cb & 1) * 1024;
  }
  const unsigned vs = (unsigned)((lane >> 4) * 4 * S_ + (lane & 15)) * 8, ve = (lane & 1) * 64 + (lane >> 4) * 16;
  unsigned long long fl;
  asm volatile("" : "=&{v[24:25]}"(fl) : "v"(vf), "s"(sq), "v"(vr[0]), "v"(vr[3]), "v"(vr[5]), "v"(vr[7]), "v"(ct), "v"(vs), "v"(ve), "s"(gt[0]), "s"(gt[1]), "s"(gt[2]), "s"(go[0]), "s"(go[1]), "s"(go[2]), "s"(go[3]), "s"(sa), "s"(sb), "s"(scnt) : "memory");
  asm volatile("" : "+s"(bx), "+s"(wid), "+s"(ws)); asm volatile("" : "+v"(tid));
  {
  const int xcd = bx & 7, loc = bx >> 3, lane = tid & 63, rot = xcd & 1, rb = (xcd & 1) * 4 + (loc & 3), cb = (xcd >> 1) * 8 + (loc >> 2);
  const u16* ef = (const u16*)(ws + W_EF) + rb * 2048; const f32x2* stats = (const f32x2*)(ws + W_STATS); u16* hh = (u16*)(ws + W_HH); float* uvp = (float*)(ws + W_UVP);
  const char* se = (const char*)ef + ((rb + 1) & 7) * 512;
  const char* ss = (const char*)(stats + rb * 256 + wid * 64);
  {
    const unsigned vp = ((lane & 1) * 4 + (lane >> 4)) * 144 + rb * 16, cr0 = ldsb + RAW + (tid >> 2) * 144 + (tid & 3) * 4, cr1 = cr0 + (wid < 3 ? 64 * 144 : 0), srl = ldsb + RAW + wid * 4096;
    const unsigned cw = ldsb + TAB + (tid >> 5) * 256 + (tid & 15) * 16 + ((tid >> 4) & 1) * 8;
    const size_t su = (size_t)uvp + cb * 16384, sh = (size_t)hh + ((size_t)(rb * 8 + wid * 2) * 112 + (cb >> 1) * 7) * 2048;
    const unsigned shl = (unsigned)sh, shh = (unsigned)(sh >> 32);
    const size_t su0 = su + grp(0) * 4608, su6 = su + 6 * 2304, sr = su + wid * 4096;
    const unsigned long long m01 = wid < 3 ? 0x0003000300030003ull : 0ull, m6 = wid < 3 ? 0ull : 0x0003000300030003ull;
    const char* tp0 = (const char*)pfw + min((bx * 4 + wid) * 7168, 1024 * 7168 - 57 * 128);
    const int wa = rot ? -31 * YS : YS, wb = rot ? -31 * 32 * 7168 : 32 * 7168, e1 = (rb & 1) ? -1 : (rb >> 1) & 1, e0 = (rb & 1) ? (rb >> 1) & 1 : -1;
    const int k0 = KD; unsigned tw[3];
#pragma unroll
    for (int i = 0; i < 3; ++i) { const int q = k0 + 8 + 16 * i + (loc & 3) * 4 + wid; tw[i] = (q >= 56 ? q - 56 : q) * 65536; }
    const unsigned vbd = f_vb(wid, lane) + k0 * 65536, sld = ldsb + wid * 1024;
    const char* sbd = (const char*)(pfw + (size_t)(xcd & 1) * 1792 * D_ + ROT * 128);
    asm volatile(".p2align 3\n\t" "s_mov_b32 s80, 0\n\t" "s_cmp_eq_u32 s80, 1\n\t" "s_cselect_b32 s86, %[wa], 16384\n\t" "s_ashr_i32 s87, s86, 31\n\t" "s_cmp_eq_u32 s80, 1\n\t" "s_cselect_b32 s88, %[wb], 229376\n\t"
    "s_ashr_i32 s89, s88, 31\n\t" "s_mov_b64 s[76:77], %[sa]\n\t" "s_mov_b64 s[78:79], %[sb]\n\t" "s_mov_b32 s82, -1\n\t" "s_mov_b32 s83, 0\n\t" "s_mov_b32 s94, 0x10000\n\t"
    "s_mov_b64 s[84:85], %[tp0]\n\t" "v_accvgpr_write_b32 a200, 0\n\t" "v_accvgpr_write_b32 a201, 0\n\t" "v_accvgpr_write_b32 a202, 0\n\t" "v_accvgpr_write_b32 a203, 0\n\t"
    "v_accvgpr_write_b32 a204, 0\n\t" "v_accvgpr_write_b32 a205, 0\n\t" "v_accvgpr_write_b32 a206, 0\n\t" "v_accvgpr_write_b32 a207, 0\n\t" "v_accvgpr_write_b32 a208, 0\n\t"
    "v_accvgpr_write_b32 a209, 0\n\t" "v_accvgpr_write_b32 a210, 0\n\t" "v_accvgpr_write_b32 a211, 0\n\t" "v_accvgpr_write_b32 a212, 0\n\t" "v_accvgpr_write_b32 a213, 0\n\t"
    "v_accvgpr_write_b32 a214, 0\n\t" "v_accvgpr_write_b32 a215, 0\n\t" "s_mov_b32 s81, 0x400000\n\t" "s_cmp_eq_u32 %[sld], %[sl0]\n\t" "s_cselect_b64 exec, -1, 0\n\t"
    "global_load_dwordx2 v[24:25], %[vf], %[sq] sc1\n\t" ".Lpy_%=:\n\t" "s_waitcnt vmcnt(0)\n\t" "v_cmp_ne_u64 vcc, %[tag], v[24:25]\n\t" "s_nop 4\n\t" "s_cbranch_vccz .Lry_%=\n\t"
    "s_mov_b64 exec, vcc\n\t" "s_sleep 1\n\t" "global_load_dwordx2 v[24:25], %[vf], %[sq] sc1\n\t" "s_sub_u32 s81, s81, 1\n\t" "s_cbranch_scc0 .Lpy_%=\n\t" ".Lry_%=:\n\t" "s_mov_b64 exec, -1\n\t"
    "s_barrier\n\t" "global_load_dwordx4 a[0:3], %[va], s[76:77] offset:0 sc1\n\t" "global_load_dwordx4 a[4:7], %[va], s[76:77] offset:1024 sc1\n\t"
    "global_load_dwordx4 a[8:11], %[va], s[76:77] offset:2048 sc1\n\t" "global_load_dwordx4 a[12:15], %[va], s[76:77] offset:3072 sc1\n\t" "s_add_u32 s76, s76, 16384\n\t" "s_addc_u32 s77, s77, 0\n\t"
    "ds_read_b64_tr_b16 a[128:129], %[vr1] offset:28672\n\t" "ds_read_b64_tr_b16 a[130:131], %[vr1] offset:32256\n\t" "ds_read_b64_tr_b16 a[132:133], %[vr1] offset:28704\n\t"
    "ds_read_b64_tr_b16 a[134:135], %[vr1] offset:32288\n\t" "ds_read_b64_tr_b16 a[136:137], %[vr3] offset:28672\n\t" "ds_read_b64_tr_b16 a[138:139], %[vr3] offset:32256\n\t"
    "ds_read_b64_tr_b16 a[140:141], %[vr3] offset:28704\n\t" "ds_read_b64_tr_b16 a[142:143], %[vr3] offset:32288\n\t" "global_load_dwordx4 a[16:19], %[va], s[76:77] offset:0 sc1\n\t"
    "global_load_dwordx4 a[20:23], %[va], s[76:77] offset:1024 sc1\n\t" "global_load_dwordx4 a[24:27], %[va], s[76:77] offset:2048 sc1\n\t"
    "global_load_dwordx4 a[28:31], %[va], s[76:77] offset:3072 sc1\n\t" "s_add_u32 s76, s76, 16384\n\t" "s_addc_u32 s77, s77, 0\n\t" "global_load_dwordx4 a[32:35], %[va], s[76:77] offset:0 sc1\n\t"
    "global_load_dwordx4 a[36:39], %[va], s[76:77] offset:1024 sc1\n\t" "global_load_dwordx4 a[40:43], %[va], s[76:77] offset:2048 sc1\n\t"
    "global_load_dwordx4 a[44:47], %[va], s[76:77] offset:3072 sc1\n\t" "s_add_u32 s76, s76, 16384\n\t" "s_addc_u32 s77, s77, 0\n\t" "s_waitcnt vmcnt(8)\n\t" "s_waitcnt lgkmcnt(4)\n\t"
    "v_mfma_f32_16x16x32_bf16 v[32:35], a[128:131], a[0:3], 0\n\t" "ds_read_b64_tr_b16 a[144:145], %[vr5] offset:28672\n\t" "s_mov_b64 s[90:91], %[ss]\n\t"
    "v_mfma_f32_16x16x32_bf16 v[36:39], a[128:131], a[4:7], 0\n\t" "ds_read_b64_tr_b16 a[146:147], %[vr5] offset:32256\n\t" "v_accvgpr_read_b32 v30, %[vs]\n\t"
    "v_mfma_f32_16x16x32_bf16 v[40:43], a[128:131], a[8:11], 0\n\t" "global_load_dwordx4 a[48:51], %[va], s[76:77] offset:0 sc1\n\t" "v_accvgpr_read_b32 v31, %[ve]\n\t"
    "v_mfma_f32_16x16x32_bf16 v[44:47], a[128:131], a[12:15], 0\n\t" "v_mfma_f32_16x16x32_bf16 v[48:51], a[132:135], a[0:3], 0\n\t" "ds_read_b64_tr_b16 a[148:149], %[vr5] offset:28704\n\t"
    "global_load_dwordx4 a[184:187], v31, %[se] offset:0 sc1\n\t" "v_mfma_f32_16x16x32_bf16 v[52:55], a[132:135], a[4:7], 0\n\t" "ds_read_b64_tr_b16 a[150:151], %[vr5] offset:32288\n\t"
    "s_add_u32 m0, %[sl], 28672\n\t" "v_mfma_f32_16x16x32_bf16 v[56:59], a[132:135], a[8:11], 0\n\t" "global_load_lds_dwordx4 %[vb0], s[78:79]\n\t"
    "global_load_dwordx4 a[188:191], v31, %[se] offset:128 sc1\n\t" "v_mfma_f32_16x16x32_bf16 v[60:63], a[132:135], a[12:15], 0\n\t" "s_waitcnt lgkmcnt(4)\n\t"
    "v_mfma_f32_16x16x32_bf16 v[64:67], a[136:139], a[0:3], 0\n\t" "ds_read_b64_tr_b16 a[152:153], %[vr7] offset:28672\n\t" "global_load_dwordx4 a[192:195], v31, %[se] offset:256 sc1\n\t"
    "v_mfma_f32_16x16x32_bf16 v[68:71], a[136:139], a[4:7], 0\n\t" "ds_read_b64_tr_b16 a[154:155], %[vr7] offset:32256\n\t" "v_mfma_f32_16x16x32_bf16 v[72:75], a[136:139], a[8:11], 0\n\t"
    "global_load_dwordx4 a[52:55], %[va], s[76:77] offset:1024 sc1\n\t" "global_load_dwordx4 a[196:199], v31, %[se] offset:384 sc1\n\t" "v_mfma_f32_16x16x32_bf16 v[76:79], a[136:139], a[12:15], 0\n\t"
    "v_mfma_f32_16x16x32_bf16 v[80:83], a[140:143], a[0:3], 0\n\t" "ds_read_b64_tr_b16 a[156:157], %[vr1] offset:35840\n\t" "global_load_dwordx2 a[216:217], v30, s[90:91] offset:0 sc1\n\t"
    "v_mfma_f32_16x16x32_bf16 v[84:87], a[140:143], a[4:7], 0\n\t" "ds_read_b64_tr_b16 a[158:159], %[vr1] offset:39424\n\t" "s_add_u32 m0, %[sl], 29696\n\t"
    "v_mfma_f32_16x16x32_bf16 v[88:91], a[140:143], a[8:11], 0\n\t" "global_load_lds_dwordx4 %[vb1], s[78:79]\n\t" "v_mfma_f32_16x16x32_bf16 v[92:95], a[140:143], a[12:15], 0\n\t"
    "s_waitcnt lgkmcnt(4)\n\t" "v_mfma_f32_16x16x32_bf16 v[96:99], a[144:147], a[0:3], 0\n\t" "ds_read_b64_tr_b16 a[128:129], %[vr1] offset:35872\n\t"
    "v_mfma_f32_16x16x32_bf16 v[100:103], a[144:147], a[4:7], 0\n\t" "ds_read_b64_tr_b16 a[130:131], %[vr1] offset:39456\n\t" "v_mfma_f32_16x16x32_bf16 v[104:107], a[144:147], a[8:11], 0\n\t"
    "v_mfma_f32_16x16x32_bf16 v[108:111], a[144:147], a[12:15], 0\n\t" "v_mfma_f32_16x16x32_bf16 v[112:115], a[148:151], a[0:3], 0\n\t" "ds_read_b64_tr_b16 a[132:133], %[vr3] offset:35840\n\t"
    "v_mfma_f32_16x16x32_bf16 v[116:119], a[148:151], a[4:7], 0\n\t" "ds_read_b64_tr_b16 a[134:135], %[vr3] offset:39424\n\t" "v_mfma_f32_16x16x32_bf16 v[120:123], a[148:151], a[8:11], 0\n\t"
    "global_load_dwordx4 a[56:59], %[va], s[76:77] offset:2048 sc1\n\t" "v_mfma_f32_16x16x32_bf16 v[124:127], a[148:151], a[12:15], 0\n\t" "s_waitcnt lgkmcnt(4)\n\t"
    "v_mfma_f32_16x16x32_bf16 v[128:131], a[152:155], a[0:3], 0\n\t" "ds_read_b64_tr_b16 a[136:137], %[vr3] offset:35872\n\t" "global_load_dwordx2 a[218:219], v30, s[90:91] offset:128 sc1\n\t"
    "v_mfma_f32_16x16x32_bf16 v[132:135], a[152:155], a[4:7], 0\n\t" "ds_read_b64_tr_b16 a[138:139], %[vr3] offset:39456\n\t" "s_add_u32 m0, %[sl], 30720\n\t"
    "v_mfma_f32_16x16x32_bf16 v[136:139], a[152:155], a[8:11], 0\n\t" "global_load_lds_dwordx4 %[vb2], s[78:79]\n\t" "v_mfma_f32_16x16x32_bf16 v[140:143], a[152:155], a[12:15], 0\n\t"
    "v_mfma_f32_16x16x32_bf16 v[144:147], a[156:159], a[0:3], 0\n\t" "ds_read_b64_tr_b16 a[140:141], %[vr5] offset:35840\n\t" "v_mfma_f32_16x16x32_bf16 v[148:151], a[156:159], a[4:7], 0\n\t"
    "ds_read_b64_tr_b16 a[142:143], %[vr5] offset:39424\n\t" "v_mfma_f32_16x16x32_bf16 v[152:155], a[156:159], a[8:11], 0\n\t" "global_load_dwordx4 a[60:63], %[va], s[76:77] offset:3072 sc1\n\t"
    "v_mfma_f32_16x16x32_bf16 v[156:159], a[156:159], a[12:15], 0\n\t" "s_add_u32 s76, s76, 16384\n\t" "s_addc_u32 s77, s77, 0\n\t" "s_waitcnt lgkmcnt(4)\n\t"
    "v_mfma_f32_16x16x32_bf16 v[160:163], a[128:131], a[0:3], 0\n\t" "ds_read_b64_tr_b16 a[144:145], %[vr5] offset:35872\n\t" "v_mfma_f32_16x16x32_bf16 v[164:167], a[128:131], a[4:7], 0\n\t"
    "ds_read_b64_tr_b16 a[146:147], %[vr5] offset:39456\n\t" "v_mfma_f32_16x16x32_bf16 v[168:171], a[128:131], a[8:11], 0\n\t" "v_mfma_f32_16x16x32_bf16 v[172:175], a[128:131], a[12:15], 0\n\t"
    "v_mfma_f32_16x16x32_bf16 v[176:179], a[132:135], a[0:3], 0\n\t" "ds_read_b64_tr_b16 a[148:149], %[vr7] offset:35840\n\t" "global_load_dwordx2 a[220:221], v30, s[90:91] offset:256 sc1\n\t"
    "v_mfma_f32_16x16x32_bf16 v[180:183], a[132:135], a[4:7], 0\n\t" "ds_read_b64_tr_b16 a[150:151], %[vr7] offset:39424\n\t" "s_add_u32 m0, %[sl], 31744\n\t"
    "v_mfma_f32_16x16x32_bf16 v[184:187], a[132:135], a[8:11], 0\n\t" "s_mov_b64 exec, s[82:83]\n\t" "global_load_lds_dwordx4 %[vb3], s[78:79]\n\t" "s_mov_b64 exec, -1\n\t"
    "v_mfma_f32_16x16x32_bf16 v[188:191], a[132:135], a[12:15], 0\n\t" "s_add_u32 s78, s78, 229376\n\t" "s_addc_u32 s79, s79, 0\n\t" "s_waitcnt lgkmcnt(4)\n\t"
    "v_mfma_f32_16x16x32_bf16 v[192:195], a[136:139], a[0:3], 0\n\t" "s_waitcnt vmcnt(19)\n\t" "s_barrier\n\t" "ds_read_b64_tr_b16 a[152:153], %[vr1] offset:43008\n\t"
    "v_mfma_f32_16x16x32_bf16 v[196:199], a[136:139], a[4:7], 0\n\t" "ds_read_b64_tr_b16 a[154:155], %[vr1] offset:46592\n\t" "v_mfma_f32_16x16x32_bf16 v[200:203], a[136:139], a[8:11], 0\n\t"
    "global_load_dwordx4 a[64:67], %[va], s[76:77] offset:0 sc1\n\t" "v_mfma_f32_16x16x32_bf16 v[204:207], a[136:139], a[12:15], 0\n\t" "v_mfma_f32_16x16x32_bf16 v[208:211], a[140:143], a[0:3], 0\n\t"
    "ds_read_b64_tr_b16 a[156:157], %[vr1] offset:43040\n\t" "v_mfma_f32_16x16x32_bf16 v[212:215], a[140:143], a[4:7], 0\n\t" "ds_read_b64_tr_b16 a[158:159], %[vr1] offset:46624\n\t"
    "s_add_u32 m0, %[sl], 43008\n\t" "v_mfma_f32_16x16x32_bf16 v[216:219], a[140:143], a[8:11], 0\n\t" "global_load_lds_dwordx4 %[vb0], s[78:79]\n\t"
    "v_mfma_f32_16x16x32_bf16 v[220:223], a[140:143], a[12:15], 0\n\t" "s_waitcnt lgkmcnt(4)\n\t" "v_mfma_f32_16x16x32_bf16 v[224:227], a[144:147], a[0:3], 0\n\t"
    "ds_read_b64_tr_b16 a[128:129], %[vr3] offset:43008\n\t" "global_load_dwordx2 a[222:223], v30, s[90:91] offset:384 sc1\n\t" "v_mfma_f32_16x16x32_bf16 v[228:231], a[144:147], a[4:7], 0\n\t"
    "ds_read_b64_tr_b16 a[130:131], %[vr3] offset:46592\n\t" "v_mfma_f32_16x16x32_bf16 v[232:235], a[144:147], a[8:11], 0\n\t" "global_load_dword v29, %[va], %[cd]\n\t"
    "v_mfma_f32_16x16x32_bf16 v[236:239], a[144:147], a[12:15], 0\n\t" "v_mfma_f32_16x16x32_bf16 v[240:243], a[148:151], a[0:3], 0\n\t" "ds_read_b64_tr_b16 a[132:133], %[vr3] offset:43040\n\t"
    "v_mfma_f32_16x16x32_bf16 v[244:247], a[148:151], a[4:7], 0\n\t" "ds_read_b64_tr_b16 a[134:135], %[vr3] offset:46624\n\t" "v_mfma_f32_16x16x32_bf16 v[248:251], a[148:151], a[8:11], 0\n\t"
    "v_mfma_f32_16x16x32_bf16 v[252:255], a[148:151], a[12:15], 0\n\t" "s_waitcnt lgkmcnt(4)\n\t" "v_mfma_f32_16x16x32_bf16 v[32:35], a[152:155], a[16:19], v[32:35]\n\t"
    "ds_read_b64_tr_b16 a[136:137], %[vr5] offset:43008\n\t" "v_mfma_f32_16x16x32_bf16 v[36:39], a[152:155], a[20:23], v[36:39]\n\t" "ds_read_b64_tr_b16 a[138:139], %[vr5] offset:46592\n\t"
    "v_mfma_f32_16x16x32_bf16 v[40:43], a[152:155], a[24:27], v[40:43]\n\t" "global_load_dwordx4 a[68:71], %[va], s[76:77] offset:1024 sc1\n\t"
    "v_mfma_f32_16x16x32_bf16 v[44:47], a[152:155], a[28:31], v[44:47]\n\t" "v_mfma_f32_16x16x32_bf16 v[48:51], a[156:159], a[16:19], v[48:51]\n\t" "ds_read_b64_tr_b16 a[140:141], %[vr5] offset:43040\n\t"
    "s_add_u32 s90, s90, 16384\n\t" "s_addc_u32 s91, s91, 0\n\t" "v_mfma_f32_16x16x32_bf16 v[52:55], a[156:159], a[20:23], v[52:55]\n\t" "ds_read_b64_tr_b16 a[142:143], %[vr5] offset:46624\n\t"
    "global_load_dwordx2 a[224:225], v30, s[90:91] offset:0 sc1\n\t" "s_add_u32 m0, %[sl], 44032\n\t" "v_mfma_f32_16x16x32_bf16 v[56:59], a[156:159], a[24:27], v[56:59]\n\t"
    "global_load_lds_dwordx4 %[vb1], s[78:79]\n\t" "v_mfma_f32_16x16x32_bf16 v[60:63], a[156:159], a[28:31], v[60:63]\n\t" "s_waitcnt lgkmcnt(4)\n\t"
    "v_mfma_f32_16x16x32_bf16 v[64:67], a[128:131], a[16:19], v[64:67]\n\t" "ds_read_b64_tr_b16 a[144:145], %[vr7] offset:43008\n\t" "v_mfma_f32_16x16x32_bf16 v[68:71], a[128:131], a[20:23], v[68:71]\n\t"
    "ds_read_b64_tr_b16 a[146:147], %[vr7] offset:46592\n\t" "v_mfma_f32_16x16x32_bf16 v[72:75], a[128:131], a[24:27], v[72:75]\n\t" "global_load_dwordx4 a[72:75], %[va], s[76:77] offset:2048 sc1\n\t"
    "v_mfma_f32_16x16x32_bf16 v[76:79], a[128:131], a[28:31], v[76:79]\n\t" "v_mfma_f32_16x16x32_bf16 v[80:83], a[132:135], a[16:19], v[80:83]\n\t" "ds_read_b64_tr_b16 a[148:149], %[vr1] offset:50176\n\t"
    "v_mfma_f32_16x16x32_bf16 v[84:87], a[132:135], a[20:23], v[84:87]\n\t" "ds_read_b64_tr_b16 a[150:151], %[vr1] offset:53760\n\t" "s_add_u32 m0, %[sl], 45056\n\t"
    "v_mfma_f32_16x16x32_bf16 v[88:91], a[132:135], a[24:27], v[88:91]\n\t" "global_load_lds_dwordx4 %[vb2], s[78:79]\n\t" "v_mfma_f32_16x16x32_bf16 v[92:95], a[132:135], a[28:31], v[92:95]\n\t"
    "s_waitcnt lgkmcnt(4)\n\t" "v_mfma_f32_16x16x32_bf16 v[96:99], a[136:139], a[16:19], v[96:99]\n\t" "ds_read_b64_tr_b16 a[152:153], %[vr1] offset:50208\n\t"
    "v_mfma_f32_16x16x32_bf16 v[100:103], a[136:139], a[20:23], v[100:103]\n\t" "ds_read_b64_tr_b16 a[154:155], %[vr1] offset:53792\n\t" "global_load_dwordx2 a[226:227], v30, s[90:91] offset:128 sc1\n\t"
    "v_mfma_f32_16x16x32_bf16 v[104:107], a[136:139], a[24:27], v[104:107]\n\t" "v_mfma_f32_16x16x32_bf16 v[108:111], a[136:139], a[28:31], v[108:111]\n\t"
    "v_mfma_f32_16x16x32_bf16 v[112:115], a[140:143], a[16:19], v[112:115]\n\t" "ds_read_b64_tr_b16 a[156:157], %[vr3] offset:50176\n\t"
    "v_mfma_f32_16x16x32_bf16 v[116:119], a[140:143], a[20:23], v[116:119]\n\t" "ds_read_b64_tr_b16 a[158:159], %[vr3] offset:53760\n\t"
    "v_mfma_f32_16x16x32_bf16 v[120:123], a[140:143], a[24:27], v[120:123]\n\t" "global_load_dwordx4 a[76:79], %[va], s[76:77] offset:3072 sc1\n\t"
    "v_mfma_f32_16x16x32_bf16 v[124:127], a[140:143], a[28:31], v[124:127]\n\t" "s_add_u32 s76, s76, 16384\n\t" "s_addc_u32 s77, s77, 0\n\t" "s_waitcnt lgkmcnt(4)\n\t"
    "v_mfma_f32_16x16x32_bf16 v[128:131], a[144:147], a[16:19], v[128:131]\n\t" "ds_read_b64_tr_b16 a[128:129], %[vr3] offset:50208\n\t"
    "v_mfma_f32_16x16x32_bf16 v[132:135], a[144:147], a[20:23], v[132:135]\n\t" "ds_read_b64_tr_b16 a[130:131], %[vr3] offset:53792\n\t" "s_add_u32 m0, %[sl], 46080\n\t"
    "v_mfma_f32_16x16x32_bf16 v[136:139], a[144:147], a[24:27], v[136:139]\n\t" "s_mov_b64 exec, s[82:83]\n\t" "global_load_lds_dwordx4 %[vb3], s[78:79]\n\t" "s_mov_b64 exec, -1\n\t"
    "v_mfma_f32_16x16x32_bf16 v[140:143], a[144:147], a[28:31], v[140:143]\n\t" "s_add_u32 s78, s78, 229376\n\t" "s_addc_u32 s79, s79, 0\n\t"
    "v_mfma_f32_16x16x32_bf16 v[144:147], a[148:151], a[16:19], v[144:147]\n\t" "ds_read_b64_tr_b16 a[132:133], %[vr5] offset:50176\n\t"
    "v_mfma_f32_16x16x32_bf16 v[148:151], a[148:151], a[20:23], v[148:151]\n\t" "ds_read_b64_tr_b16 a[134:135], %[vr5] offset:53760\n\t" "global_load_dwordx2 a[228:229], v30, s[90:91] offset:256 sc1\n\t"
    "v_mfma_f32_16x16x32_bf16 v[152:155], a[148:151], a[24:27], v[152:155]\n\t" "global_load_dwordx4 a[80:83], %[va], s[76:77] offset:0 sc1\n\t"
    "v_mfma_f32_16x16x32_bf16 v[156:159], a[148:151], a[28:31], v[156:159]\n\t" "s_waitcnt lgkmcnt(4)\n\t" "v_mfma_f32_16x16x32_bf16 v[160:163], a[152:155], a[16:19], v[160:163]\n\t"
    "ds_read_b64_tr_b16 a[136:137], %[vr5] offset:50208\n\t" "v_mfma_f32_16x16x32_bf16 v[164:167], a[152:155], a[20:23], v[164:167]\n\t" "ds_read_b64_tr_b16 a[138:139], %[vr5] offset:53792\n\t"
    "v_mfma_f32_16x16x32_bf16 v[168:171], a[152:155], a[24:27], v[168:171]\n\t" "v_mfma_f32_16x16x32_bf16 v[172:175], a[152:155], a[28:31], v[172:175]\n\t"
    "v_mfma_f32_16x16x32_bf16 v[176:179], a[156:159], a[16:19], v[176:179]\n\t" "ds_read_b64_tr_b16 a[140:141], %[vr7] offset:50176\n\t"
    "v_mfma_f32_16x16x32_bf16 v[180:183], a[156:159], a[20:23], v[180:183]\n\t" "ds_read_b64_tr_b16 a[142:143], %[vr7] offset:53760\n\t" "s_add_u32 m0, %[sl], 57344\n\t"
    "v_mfma_f32_16x16x32_bf16 v[184:187], a[156:159], a[24:27], v[184:187]\n\t" "global_load_lds_dwordx4 %[vb0], s[78:79]\n\t" "v_mfma_f32_16x16x32_bf16 v[188:191], a[156:159], a[28:31], v[188:191]\n\t"
    "s_waitcnt lgkmcnt(4)\n\t" "v_mfma_f32_16x16x32_bf16 v[192:195], a[128:131], a[16:19], v[192:195]\n\t" "s_barrier\n\t" "ds_read_b64_tr_b16 a[144:145], %[vr0] offset:0\n\t"
    "v_mfma_f32_16x16x32_bf16 v[196:199], a[128:131], a[20:23], v[196:199]\n\t" "ds_read_b64_tr_b16 a[146:147], %[vr0] offset:3584\n\t" "global_load_dwordx2 a[230:231], v30, s[90:91] offset:384 sc1\n\t"
    "v_mfma_f32_16x16x32_bf16 v[200:203], a[128:131], a[24:27], v[200:203]\n\t" "global_load_dwordx4 a[84:87], %[va], s[76:77] offset:1024 sc1\n\t"
    "v_mfma_f32_16x16x32_bf16 v[204:207], a[128:131], a[28:31], v[204:207]\n\t" "v_mfma_f32_16x16x32_bf16 v[208:211], a[132:135], a[16:19], v[208:211]\n\t"
    "ds_read_b64_tr_b16 a[148:149], %[vr0] offset:14336\n\t" "v_mfma_f32_16x16x32_bf16 v[212:215], a[132:135], a[20:23], v[212:215]\n\t" "ds_read_b64_tr_b16 a[150:151], %[vr0] offset:17920\n\t"
    "s_add_u32 m0, %[sl], 58368\n\t" "v_mfma_f32_16x16x32_bf16 v[216:219], a[132:135], a[24:27], v[216:219]\n\t" "global_load_lds_dwordx4 %[vb1], s[78:79]\n\t"
    "v_mfma_f32_16x16x32_bf16 v[220:223], a[132:135], a[28:31], v[220:223]\n\t" "s_waitcnt lgkmcnt(4)\n\t" "v_mfma_f32_16x16x32_bf16 v[224:227], a[136:139], a[16:19], v[224:227]\n\t"
    "ds_read_b64_tr_b16 a[152:153], %[vr0] offset:32\n\t" "v_mfma_f32_16x16x32_bf16 v[228:231], a[136:139], a[20:23], v[228:231]\n\t" "ds_read_b64_tr_b16 a[154:155], %[vr0] offset:3616\n\t"
    "v_mfma_f32_16x16x32_bf16 v[232:235], a[136:139], a[24:27], v[232:235]\n\t" "v_mfma_f32_16x16x32_bf16 v[236:239], a[136:139], a[28:31], v[236:239]\n\t"
    "v_mfma_f32_16x16x32_bf16 v[240:243], a[140:143], a[16:19], v[240:243]\n\t" "ds_read_b64_tr_b16 a[156:157], %[vr0] offset:14368\n\t"
    "v_mfma_f32_16x16x32_bf16 v[244:247], a[140:143], a[20:23], v[244:247]\n\t" "ds_read_b64_tr_b16 a[158:159], %[vr0] offset:17952\n\t" "s_add_u32 s90, s90, 16384\n\t" "s_addc_u32 s91, s91, 0\n\t"
    "v_mfma_f32_16x16x32_bf16 v[248:251], a[140:143], a[24:27], v[248:251]\n\t" "global_load_dwordx2 a[232:233], v30, s[90:91] offset:0 sc1\n\t"
    "v_mfma_f32_16x16x32_bf16 v[252:255], a[140:143], a[28:31], v[252:255]\n\t" "s_waitcnt lgkmcnt(4) vmcnt(21)\n\t" "v_mfma_f32_16x16x32_bf16 v[32:35], a[144:147], a[32:35], v[32:35]\n\t" "s_barrier\n\t"
    "ds_read_b64_tr_b16 a[128:129], %[vr2] offset:0\n\t" "v_mfma_f32_16x16x32_bf16 v[32:35], a[148:151], a[48:51], v[32:35]\n\t" "ds_read_b64_tr_b16 a[130:131], %[vr2] offset:3584\n\t"
    "v_mfma_f32_16x16x32_bf16 v[36:39], a[144:147], a[36:39], v[36:39]\n\t" "global_load_dwordx4 a[88:91], %[va], s[76:77] offset:2048 sc1\n\t"
    "v_mfma_f32_16x16x32_bf16 v[36:39], a[148:151], a[52:55], v[36:39]\n\t" "v_mfma_f32_16x16x32_bf16 v[40:43], a[144:147], a[40:43], v[40:43]\n\t" "ds_read_b64_tr_b16 a[132:133], %[vr2] offset:14336\n\t"
    "v_mfma_f32_16x16x32_bf16 v[40:43], a[148:151], a[56:59], v[40:43]\n\t" "ds_read_b64_tr_b16 a[134:135], %[vr2] offset:17920\n\t" "s_add_u32 m0, %[sl], 59392\n\t"
    "v_mfma_f32_16x16x32_bf16 v[44:47], a[144:147], a[44:47], v[44:47]\n\t" "global_load_lds_dwordx4 %[vb2], s[78:79]\n\t" "v_mfma_f32_16x16x32_bf16 v[44:47], a[148:151], a[60:63], v[44:47]\n\t"
    "s_waitcnt lgkmcnt(4)\n\t" "v_mfma_f32_16x16x32_bf16 v[48:51], a[152:155], a[32:35], v[48:51]\n\t" "ds_read_b64_tr_b16 a[136:137], %[vr2] offset:32\n\t"
    "v_mfma_f32_16x16x32_bf16 v[48:51], a[156:159], a[48:51], v[48:51]\n\t" "ds_read_b64_tr_b16 a[138:139], %[vr2] offset:3616\n\t" "v_mfma_f32_16x16x32_bf16 v[52:55], a[152:155], a[36:39], v[52:55]\n\t"
    "global_load_dwordx4 a[92:95], %[va], s[76:77] offset:3072 sc1\n\t" "global_load_dwordx2 a[234:235], v30, s[90:91] offset:128 sc1\n\t"
    "v_mfma_f32_16x16x32_bf16 v[52:55], a[156:159], a[52:55], v[52:55]\n\t" "s_add_u32 s76, s76, 16384\n\t" "s_addc_u32 s77, s77, 0\n\t"
    "v_mfma_f32_16x16x32_bf16 v[56:59], a[152:155], a[40:43], v[56:59]\n\t" "ds_read_b64_tr_b16 a[140:141], %[vr2] offset:14368\n\t" "v_mfma_f32_16x16x32_bf16 v[56:59], a[156:159], a[56:59], v[56:59]\n\t"
    "ds_read_b64_tr_b16 a[142:143], %[vr2] offset:17952\n\t" "s_add_u32 m0, %[sl], 60416\n\t" "v_mfma_f32_16x16x32_bf16 v[60:63], a[152:155], a[44:47], v[60:63]\n\t" "s_mov_b64 exec, s[82:83]\n\t"
    "global_load_lds_dwordx4 %[vb3], s[78:79]\n\t" "s_mov_b64 exec, -1\n\t" "v_mfma_f32_16x16x32_bf16 v[60:63], a[156:159], a[60:63], v[60:63]\n\t" "s_add_u32 s78, s78, 229376\n\t"
    "s_addc_u32 s79, s79, 0\n\t" "s_waitcnt lgkmcnt(4)\n\t" "v_mfma_f32_16x16x32_bf16 v[64:67], a[128:131], a[32:35], v[64:67]\n\t" "ds_read_b64_tr_b16 a[144:145], %[vr4] offset:0\n\t"
    "v_mfma_f32_16x16x32_bf16 v[64:67], a[132:135], a[48:51], v[64:67]\n\t" "ds_read_b64_tr_b16 a[146:147], %[vr4] offset:3584\n\t" "v_mfma_f32_16x16x32_bf16 v[68:71], a[128:131], a[36:39], v[68:71]\n\t"
    "v_mfma_f32_16x16x32_bf16 v[68:71], a[132:135], a[52:55], v[68:71]\n\t" "v_mfma_f32_16x16x32_bf16 v[72:75], a[128:131], a[40:43], v[72:75]\n\t" "ds_read_b64_tr_b16 a[148:149], %[vr4] offset:14336\n\t"
    "v_mfma_f32_16x16x32_bf16 v[72:75], a[132:135], a[56:59], v[72:75]\n\t" "ds_read_b64_tr_b16 a[150:151], %[vr4] offset:17920\n\t" "v_mfma_f32_16x16x32_bf16 v[76:79], a[128:131], a[44:47], v[76:79]\n\t"
    "global_load_dwordx4 a[96:99], %[va], s[76:77] offset:0 sc1\n\t" "global_load_dwordx2 a[236:237], v30, s[90:91] offset:256 sc1\n\t"
    "v_mfma_f32_16x16x32_bf16 v[76:79], a[132:135], a[60:63], v[76:79]\n\t" "s_waitcnt lgkmcnt(4)\n\t" "v_mfma_f32_16x16x32_bf16 v[80:83], a[136:139], a[32:35], v[80:83]\n\t"
    "ds_read_b64_tr_b16 a[152:153], %[vr4] offset:32\n\t" "v_mfma_f32_16x16x32_bf16 v[80:83], a[140:143], a[48:51], v[80:83]\n\t" "ds_read_b64_tr_b16 a[154:155], %[vr4] offset:3616\n\t"
    "s_add_u32 m0, %[sl], 71680\n\t" "v_mfma_f32_16x16x32_bf16 v[84:87], a[136:139], a[36:39], v[84:87]\n\t" "global_load_lds_dwordx4 %[vb0], s[78:79]\n\t"
    "v_mfma_f32_16x16x32_bf16 v[84:87], a[140:143], a[52:55], v[84:87]\n\t" "v_mfma_f32_16x16x32_bf16 v[88:91], a[136:139], a[40:43], v[88:91]\n\t" "ds_read_b64_tr_b16 a[156:157], %[vr4] offset:14368\n\t"
    "v_mfma_f32_16x16x32_bf16 v[88:91], a[140:143], a[56:59], v[88:91]\n\t" "ds_read_b64_tr_b16 a[158:159], %[vr4] offset:17952\n\t" "v_mfma_f32_16x16x32_bf16 v[92:95], a[136:139], a[44:47], v[92:95]\n\t"
    "global_load_dwordx4 a[100:103], %[va], s[76:77] offset:1024 sc1\n\t" "v_mfma_f32_16x16x32_bf16 v[92:95], a[140:143], a[60:63], v[92:95]\n\t" "s_waitcnt lgkmcnt(4)\n\t"
    "v_mfma_f32_16x16x32_bf16 v[96:99], a[144:147], a[32:35], v[96:99]\n\t" "ds_read_b64_tr_b16 a[128:129], %[vr6] offset:0\n\t" "v_mfma_f32_16x16x32_bf16 v[96:99], a[148:151], a[48:51], v[96:99]\n\t"
    "ds_read_b64_tr_b16 a[130:131], %[vr6] offset:3584\n\t" "v_mfma_f32_16x16x32_bf16 v[100:103], a[144:147], a[36:39], v[100:103]\n\t" "global_load_dwordx2 a[238:239], v30, s[90:91] offset:384 sc1\n\t"
    "v_mfma_f32_16x16x32_bf16 v[100:103], a[148:151], a[52:55], v[100:103]\n\t" "v_mfma_f32_16x16x32_bf16 v[104:107], a[144:147], a[40:43], v[104:107]\n\t"
    "ds_read_b64_tr_b16 a[132:133], %[vr6] offset:14336\n\t" "v_mfma_f32_16x16x32_bf16 v[104:107], a[148:151], a[56:59], v[104:107]\n\t" "ds_read_b64_tr_b16 a[134:135], %[vr6] offset:17920\n\t"
    "s_add_u32 m0, %[sl], 72704\n\t" "v_mfma_f32_16x16x32_bf16 v[108:111], a[144:147], a[44:47], v[108:111]\n\t" "global_load_lds_dwordx4 %[vb1], s[78:79]\n\t"
    "v_mfma_f32_16x16x32_bf16 v[108:111], a[148:151], a[60:63], v[108:111]\n\t" "s_waitcnt lgkmcnt(4)\n\t" "v_mfma_f32_16x16x32_bf16 v[112:115], a[152:155], a[32:35], v[112:115]\n\t"
    "ds_read_b64_tr_b16 a[136:137], %[vr0] offset:7168\n\t" "v_mfma_f32_16x16x32_bf16 v[112:115], a[156:159], a[48:51], v[112:115]\n\t" "ds_read_b64_tr_b16 a[138:139], %[vr0] offset:10752\n\t"
    "v_mfma_f32_16x16x32_bf16 v[116:119], a[152:155], a[36:39], v[116:119]\n\t" "global_load_dwordx4 a[104:107], %[va], s[76:77] offset:2048 sc1\n\t"
    "v_mfma_f32_16x16x32_bf16 v[116:119], a[156:159], a[52:55], v[116:119]\n\t" "v_mfma_f32_16x16x32_bf16 v[120:123], a[152:155], a[40:43], v[120:123]\n\t"
    "ds_read_b64_tr_b16 a[140:141], %[vr0] offset:21504\n\t" "v_mfma_f32_16x16x32_bf16 v[120:123], a[156:159], a[56:59], v[120:123]\n\t" "ds_read_b64_tr_b16 a[142:143], %[vr0] offset:25088\n\t"
    "s_add_u32 m0, %[sl], 73728\n\t" "v_mfma_f32_16x16x32_bf16 v[124:127], a[152:155], a[44:47], v[124:127]\n\t" "global_load_lds_dwordx4 %[vb2], s[78:79]\n\t" "s_add_u32 s90, s90, 16384\n\t"
    "s_addc_u32 s91, s91, 0\n\t" "v_mfma_f32_16x16x32_bf16 v[124:127], a[156:159], a[60:63], v[124:127]\n\t" "global_load_dwordx2 a[240:241], v30, s[90:91] offset:0 sc1\n\t" "s_waitcnt lgkmcnt(4)\n\t"
    "v_mfma_f32_16x16x32_bf16 v[128:131], a[128:131], a[32:35], v[128:131]\n\t" "ds_read_b64_tr_b16 a[144:145], %[vr0] offset:7200\n\t"
    "v_mfma_f32_16x16x32_bf16 v[128:131], a[132:135], a[48:51], v[128:131]\n\t" "ds_read_b64_tr_b16 a[146:147], %[vr0] offset:10784\n\t"
    "v_mfma_f32_16x16x32_bf16 v[132:135], a[128:131], a[36:39], v[132:135]\n\t" "v_mfma_f32_16x16x32_bf16 v[132:135], a[132:135], a[52:55], v[132:135]\n\t"
    "v_mfma_f32_16x16x32_bf16 v[136:139], a[128:131], a[40:43], v[136:139]\n\t" "ds_read_b64_tr_b16 a[148:149], %[vr0] offset:21536\n\t"
    "v_mfma_f32_16x16x32_bf16 v[136:139], a[132:135], a[56:59], v[136:139]\n\t" "ds_read_b64_tr_b16 a[150:151], %[vr0] offset:25120\n\t"
    "v_mfma_f32_16x16x32_bf16 v[140:143], a[128:131], a[44:47], v[140:143]\n\t" "v_mfma_f32_16x16x32_bf16 v[140:143], a[132:135], a[60:63], v[140:143]\n\t" "s_waitcnt lgkmcnt(4)\n\t"
    "v_mfma_f32_16x16x32_bf16 v[144:147], a[136:139], a[32:35], v[144:147]\n\t" "ds_read_b64_tr_b16 a[152:153], %[vr2] offset:7168\n\t"
    "v_mfma_f32_16x16x32_bf16 v[144:147], a[140:143], a[48:51], v[144:147]\n\t" "ds_read_b64_tr_b16 a[154:155], %[vr2] offset:10752\n\t"
    "v_mfma_f32_16x16x32_bf16 v[148:151], a[136:139], a[36:39], v[148:151]\n\t" "global_load_dwordx4 a[108:111], %[va], s[76:77] offset:3072 sc1\n\t"
    "v_mfma_f32_16x16x32_bf16 v[148:151], a[140:143], a[52:55], v[148:151]\n\t" "s_add_u32 s76, s76, 16384\n\t" "s_addc_u32 s77, s77, 0\n\t"
    "global_load_dwordx2 a[242:243], v30, s[90:91] offset:128 sc1\n\t" "v_mfma_f32_16x16x32_bf16 v[152:155], a[136:139], a[40:43], v[152:155]\n\t" "ds_read_b64_tr_b16 a[156:157], %[vr2] offset:21504\n\t"
    "v_mfma_f32_16x16x32_bf16 v[152:155], a[140:143], a[56:59], v[152:155]\n\t" "ds_read_b64_tr_b16 a[158:159], %[vr2] offset:25088\n\t" "s_add_u32 m0, %[sl], 74752\n\t"
    "v_mfma_f32_16x16x32_bf16 v[156:159], a[136:139], a[44:47], v[156:159]\n\t" "s_mov_b64 exec, s[82:83]\n\t" "global_load_lds_dwordx4 %[vb3], s[78:79]\n\t" "s_mov_b64 exec, -1\n\t"
    "v_mfma_f32_16x16x32_bf16 v[156:159], a[140:143], a[60:63], v[156:159]\n\t" "s_add_u32 s78, s78, s88\n\t" "s_addc_u32 s79, s79, s89\n\t" "s_waitcnt lgkmcnt(4)\n\t"
    "v_mfma_f32_16x16x32_bf16 v[160:163], a[144:147], a[32:35], v[160:163]\n\t" "ds_read_b64_tr_b16 a[128:129], %[vr2] offset:7200\n\t"
    "v_mfma_f32_16x16x32_bf16 v[160:163], a[148:151], a[48:51], v[160:163]\n\t" "ds_read_b64_tr_b16 a[130:131], %[vr2] offset:10784\n\t"
    "v_mfma_f32_16x16x32_bf16 v[164:167], a[144:147], a[36:39], v[164:167]\n\t" "global_load_dwordx4 a[112:115], %[va], s[76:77] offset:0 sc1\n\t"
    "v_mfma_f32_16x16x32_bf16 v[164:167], a[148:151], a[52:55], v[164:167]\n\t" "v_mfma_f32_16x16x32_bf16 v[168:171], a[144:147], a[40:43], v[168:171]\n\t"
    "ds_read_b64_tr_b16 a[132:133], %[vr2] offset:21536\n\t" "v_mfma_f32_16x16x32_bf16 v[168:171], a[148:151], a[56:59], v[168:171]\n\t" "ds_read_b64_tr_b16 a[134:135], %[vr2] offset:25120\n\t"
    "s_add_u32 m0, %[sl], 86016\n\t" "v_mfma_f32_16x16x32_bf16 v[172:175], a[144:147], a[44:47], v[172:175]\n\t" "global_load_lds_dwordx4 %[vb0], s[78:79]\n\t"
    "v_mfma_f32_16x16x32_bf16 v[172:175], a[148:151], a[60:63], v[172:175]\n\t" "global_load_dwordx2 a[244:245], v30, s[90:91] offset:256 sc1\n\t" "s_waitcnt lgkmcnt(4)\n\t"
    "v_mfma_f32_16x16x32_bf16 v[176:179], a[152:155], a[32:35], v[176:179]\n\t" "ds_read_b64_tr_b16 a[136:137], %[vr4] offset:7168\n\t"
    "v_mfma_f32_16x16x32_bf16 v[176:179], a[156:159], a[48:51], v[176:179]\n\t" "ds_read_b64_tr_b16 a[138:139], %[vr4] offset:10752\n\t"
    "v_mfma_f32_16x16x32_bf16 v[180:183], a[152:155], a[36:39], v[180:183]\n\t" "v_mfma_f32_16x16x32_bf16 v[180:183], a[156:159], a[52:55], v[180:183]\n\t"
    "v_mfma_f32_16x16x32_bf16 v[184:187], a[152:155], a[40:43], v[184:187]\n\t" "ds_read_b64_tr_b16 a[140:141], %[vr4] offset:21504\n\t"
    "v_mfma_f32_16x16x32_bf16 v[184:187], a[156:159], a[56:59], v[184:187]\n\t" "ds_read_b64_tr_b16 a[142:143], %[vr4] offset:25088\n\t"
    "v_mfma_f32_16x16x32_bf16 v[188:191], a[152:155], a[44:47], v[188:191]\n\t" "global_load_dwordx4 a[116:119], %[va], s[76:77] offset:1024 sc1\n\t"
    "v_mfma_f32_16x16x32_bf16 v[188:191], a[156:159], a[60:63], v[188:191]\n\t" "s_waitcnt lgkmcnt(4)\n\t" "v_mfma_f32_16x16x32_bf16 v[192:195], a[128:131], a[32:35], v[192:195]\n\t"
    "ds_read_b64_tr_b16 a[144:145], %[vr4] offset:7200\n\t" "v_mfma_f32_16x16x32_bf16 v[192:195], a[132:135], a[48:51], v[192:195]\n\t" "ds_read_b64_tr_b16 a[146:147], %[vr4] offset:10784\n\t"
    "s_add_u32 m0, %[sl], 87040\n\t" "v_mfma_f32_16x16x32_bf16 v[196:199], a[128:131], a[36:39], v[196:199]\n\t" "global_load_lds_dwordx4 %[vb1], s[78:79]\n\t"
    "v_mfma_f32_16x16x32_bf16 v[196:199], a[132:135], a[52:55], v[196:199]\n\t" "global_load_dwordx2 a[246:247], v30, s[90:91] offset:384 sc1\n\t"
    "v_mfma_f32_16x16x32_bf16 v[200:203], a[128:131], a[40:43], v[200:203]\n\t" "ds_read_b64_tr_b16 a[148:149], %[vr4] offset:21536\n\t"
    "v_mfma_f32_16x16x32_bf16 v[200:203], a[132:135], a[56:59], v[200:203]\n\t" "ds_read_b64_tr_b16 a[150:151], %[vr4] offset:25120\n\t"
    "v_mfma_f32_16x16x32_bf16 v[204:207], a[128:131], a[44:47], v[204:207]\n\t" "global_load_dwordx4 a[120:123], %[va], s[76:77] offset:2048 sc1\n\t"
    "v_mfma_f32_16x16x32_bf16 v[204:207], a[132:135], a[60:63], v[204:207]\n\t" "s_waitcnt lgkmcnt(4)\n\t" "v_mfma_f32_16x16x32_bf16 v[208:211], a[136:139], a[32:35], v[208:211]\n\t"
    "ds_read_b64_tr_b16 a[152:153], %[vr6] offset:7168\n\t" "v_mfma_f32_16x16x32_bf16 v[208:211], a[140:143], a[48:51], v[208:211]\n\t" "ds_read_b64_tr_b16 a[154:155], %[vr6] offset:10752\n\t"
    "v_mfma_f32_16x16x32_bf16 v[212:215], a[136:139], a[36:39], v[212:215]\n\t" "v_mfma_f32_16x16x32_bf16 v[212:215], a[140:143], a[52:55], v[212:215]\n\t"
    "v_mfma_f32_16x16x32_bf16 v[216:219], a[136:139], a[40:43], v[216:219]\n\t" "ds_read_b64_tr_b16 a[156:157], %[vr6] offset:21504\n\t"
    "v_mfma_f32_16x16x32_bf16 v[216:219], a[140:143], a[56:59], v[216:219]\n\t" "ds_read_b64_tr_b16 a[158:159], %[vr6] offset:25088\n\t" "s_add_u32 m0, %[sl], 88064\n\t"
    "v_mfma_f32_16x16x32_bf16 v[220:223], a[136:139], a[44:47], v[220:223]\n\t" "global_load_lds_dwordx4 %[vb2], s[78:79]\n\t" "v_mfma_f32_16x16x32_bf16 v[220:223], a[140:143], a[60:63], v[220:223]\n\t"
    "s_waitcnt lgkmcnt(4)\n\t" "v_mfma_f32_16x16x32_bf16 v[224:227], a[144:147], a[32:35], v[224:227]\n\t" "s_waitcnt vmcnt(32)\n\t" "s_barrier\n\t"
    "ds_read_b64_tr_b16 a[128:129], %[vr0] offset:28672\n\t" "v_mfma_f32_16x16x32_bf16 v[224:227], a[148:151], a[48:51], v[224:227]\n\t" "ds_read_b64_tr_b16 a[130:131], %[vr0] offset:32256\n\t"
    "v_mfma_f32_16x16x32_bf16 v[228:231], a[144:147], a[36:39], v[228:231]\n\t" "global_load_dwordx4 a[124:127], %[va], s[76:77] offset:3072 sc1\n\t"
    "v_mfma_f32_16x16x32_bf16 v[228:231], a[148:151], a[52:55], v[228:231]\n\t" "s_add_u32 s76, s76, s86\n\t" "s_addc_u32 s77, s77, s87\n\t"
    "v_mfma_f32_16x16x32_bf16 v[232:235], a[144:147], a[40:43], v[232:235]\n\t" "ds_read_b64_tr_b16 a[132:133], %[vr0] offset:43008\n\t"
    "v_mfma_f32_16x16x32_bf16 v[232:235], a[148:151], a[56:59], v[232:235]\n\t" "ds_read_b64_tr_b16 a[134:135], %[vr0] offset:46592\n\t" "s_add_u32 m0, %[sl], 89088\n\t"
    "v_mfma_f32_16x16x32_bf16 v[236:239], a[144:147], a[44:47], v[236:239]\n\t" "s_mov_b64 exec, s[82:83]\n\t" "global_load_lds_dwordx4 %[vb3], s[78:79]\n\t" "s_mov_b64 exec, -1\n\t"
    "v_mfma_f32_16x16x32_bf16 v[236:239], a[148:151], a[60:63], v[236:239]\n\t" "s_add_u32 s78, s78, 229376\n\t" "s_addc_u32 s79, s79, 0\n\t" "s_waitcnt lgkmcnt(4)\n\t"
    "v_mfma_f32_16x16x32_bf16 v[240:243], a[152:155], a[32:35], v[240:243]\n\t" "ds_read_b64_tr_b16 a[136:137], %[vr0] offset:28704\n\t"
    "v_mfma_f32_16x16x32_bf16 v[240:243], a[156:159], a[48:51], v[240:243]\n\t" "ds_read_b64_tr_b16 a[138:139], %[vr0] offset:32288\n\t"
    "v_mfma_f32_16x16x32_bf16 v[244:247], a[152:155], a[36:39], v[244:247]\n\t" "v_mfma_f32_16x16x32_bf16 v[244:247], a[156:159], a[52:55], v[244:247]\n\t"
    "v_mfma_f32_16x16x32_bf16 v[248:251], a[152:155], a[40:43], v[248:251]\n\t" "ds_read_b64_tr_b16 a[140:141], %[vr0] offset:43040\n\t"
    "v_mfma_f32_16x16x32_bf16 v[248:251], a[156:159], a[56:59], v[248:251]\n\t" "ds_read_b64_tr_b16 a[142:143], %[vr0] offset:46624\n\t"
    "v_mfma_f32_16x16x32_bf16 v[252:255], a[152:155], a[44:47], v[252:255]\n\t" "v_mfma_f32_16x16x32_bf16 v[252:255], a[156:159], a[60:63], v[252:255]\n\t" ".Lloop_%=:\n\t" "s_cmp_eq_u32 s80, %[e1]\n\t"
    "s_cbranch_scc1 .LE1_%=\n\t" "s_waitcnt lgkmcnt(4) vmcnt(17)\n\t" "v_mfma_f32_16x16x32_bf16 v[32:35], a[128:131], a[64:67], v[32:35]\n\t" "s_barrier\n\t"
    "ds_read_b64_tr_b16 a[144:145], %[vr2] offset:28672\n\t" "v_mfma_f32_16x16x32_bf16 v[32:35], a[132:135], a[80:83], v[32:35]\n\t" "ds_read_b64_tr_b16 a[146:147], %[vr2] offset:32256\n\t"
    "v_mfma_f32_16x16x32_bf16 v[36:39], a[128:131], a[68:71], v[36:39]\n\t" "global_load_dwordx4 a[0:3], %[va], s[76:77] offset:0 sc1\n\t"
    "v_mfma_f32_16x16x32_bf16 v[36:39], a[132:135], a[84:87], v[36:39]\n\t" "v_mfma_f32_16x16x32_bf16 v[40:43], a[128:131], a[72:75], v[40:43]\n\t" "ds_read_b64_tr_b16 a[148:149], %[vr2] offset:43008\n\t"
    "v_mfma_f32_16x16x32_bf16 v[40:43], a[132:135], a[88:91], v[40:43]\n\t" "ds_read_b64_tr_b16 a[150:151], %[vr2] offset:46592\n\t" "s_add_u32 m0, %[sl], 100352\n\t"
    "v_mfma_f32_16x16x32_bf16 v[44:47], a[128:131], a[76:79], v[44:47]\n\t" "global_load_lds_dwordx4 %[vb0], s[78:79]\n\t" "v_mfma_f32_16x16x32_bf16 v[44:47], a[132:135], a[92:95], v[44:47]\n\t"
    "s_waitcnt lgkmcnt(4)\n\t" "v_mfma_f32_16x16x32_bf16 v[48:51], a[136:139], a[64:67], v[48:51]\n\t" "ds_read_b64_tr_b16 a[152:153], %[vr2] offset:28704\n\t"
    "v_mfma_f32_16x16x32_bf16 v[48:51], a[140:143], a[80:83], v[48:51]\n\t" "ds_read_b64_tr_b16 a[154:155], %[vr2] offset:32288\n\t" "v_mfma_f32_16x16x32_bf16 v[52:55], a[136:139], a[68:71], v[52:55]\n\t"
    "v_mfma_f32_16x16x32_bf16 v[52:55], a[140:143], a[84:87], v[52:55]\n\t" "v_mfma_f32_16x16x32_bf16 v[56:59], a[136:139], a[72:75], v[56:59]\n\t" "ds_read_b64_tr_b16 a[156:157], %[vr2] offset:43040\n\t"
    "v_mfma_f32_16x16x32_bf16 v[56:59], a[140:143], a[88:91], v[56:59]\n\t" "ds_read_b64_tr_b16 a[158:159], %[vr2] offset:46624\n\t" "v_mfma_f32_16x16x32_bf16 v[60:63], a[136:139], a[76:79], v[60:63]\n\t"
    "global_load_dwordx4 a[4:7], %[va], s[76:77] offset:1024 sc1\n\t" "v_mfma_f32_16x16x32_bf16 v[60:63], a[140:143], a[92:95], v[60:63]\n\t" "s_waitcnt lgkmcnt(4)\n\t"
    "v_mfma_f32_16x16x32_bf16 v[64:67], a[144:147], a[64:67], v[64:67]\n\t" "ds_read_b64_tr_b16 a[128:129], %[vr4] offset:28672\n\t" "v_mfma_f32_16x16x32_bf16 v[64:67], a[148:151], a[80:83], v[64:67]\n\t"
    "ds_read_b64_tr_b16 a[130:131], %[vr4] offset:32256\n\t" "s_add_u32 m0, %[sl], 101376\n\t" "v_mfma_f32_16x16x32_bf16 v[68:71], a[144:147], a[68:71], v[68:71]\n\t"
    "global_load_lds_dwordx4 %[vb1], s[78:79]\n\t" "v_mfma_f32_16x16x32_bf16 v[68:71], a[148:151], a[84:87], v[68:71]\n\t" "v_mfma_f32_16x16x32_bf16 v[72:75], a[144:147], a[72:75], v[72:75]\n\t"
    "ds_read_b64_tr_b16 a[132:133], %[vr4] offset:43008\n\t" "v_mfma_f32_16x16x32_bf16 v[72:75], a[148:151], a[88:91], v[72:75]\n\t" "ds_read_b64_tr_b16 a[134:135], %[vr4] offset:46592\n\t"
    "v_mfma_f32_16x16x32_bf16 v[76:79], a[144:147], a[76:79], v[76:79]\n\t" "v_mfma_f32_16x16x32_bf16 v[76:79], a[148:151], a[92:95], v[76:79]\n\t" "s_waitcnt lgkmcnt(4)\n\t"
    "v_mfma_f32_16x16x32_bf16 v[80:83], a[152:155], a[64:67], v[80:83]\n\t" "ds_read_b64_tr_b16 a[136:137], %[vr4] offset:28704\n\t" "v_mfma_f32_16x16x32_bf16 v[80:83], a[156:159], a[80:83], v[80:83]\n\t"
    "ds_read_b64_tr_b16 a[138:139], %[vr4] offset:32288\n\t" "v_mfma_f32_16x16x32_bf16 v[84:87], a[152:155], a[68:71], v[84:87]\n\t" "global_load_dwordx4 a[8:11], %[va], s[76:77] offset:2048 sc1\n\t"
    "v_mfma_f32_16x16x32_bf16 v[84:87], a[156:159], a[84:87], v[84:87]\n\t" "v_mfma_f32_16x16x32_bf16 v[88:91], a[152:155], a[72:75], v[88:91]\n\t" "ds_read_b64_tr_b16 a[140:141], %[vr4] offset:43040\n\t"
    "v_mfma_f32_16x16x32_bf16 v[88:91], a[156:159], a[88:91], v[88:91]\n\t" "ds_read_b64_tr_b16 a[142:143], %[vr4] offset:46624\n\t" "v_mfma_f32_16x16x32_bf16 v[92:95], a[152:155], a[76:79], v[92:95]\n\t"
    "v_mfma_f32_16x16x32_bf16 v[92:95], a[156:159], a[92:95], v[92:95]\n\t" "s_waitcnt lgkmcnt(4)\n\t" "v_mfma_f32_16x16x32_bf16 v[96:99], a[128:131], a[64:67], v[96:99]\n\t"
    "ds_read_b64_tr_b16 a[144:145], %[vr6] offset:28672\n\t" "v_mfma_f32_16x16x32_bf16 v[96:99], a[132:135], a[80:83], v[96:99]\n\t" "ds_read_b64_tr_b16 a[146:147], %[vr6] offset:32256\n\t"
    "s_add_u32 m0, %[sl], 102400\n\t" "v_mfma_f32_16x16x32_bf16 v[100:103], a[128:131], a[68:71], v[100:103]\n\t" "global_load_lds_dwordx4 %[vb2], s[78:79]\n\t"
    "v_mfma_f32_16x16x32_bf16 v[100:103], a[132:135], a[84:87], v[100:103]\n\t" "v_mfma_f32_16x16x32_bf16 v[104:107], a[128:131], a[72:75], v[104:107]\n\t"
    "ds_read_b64_tr_b16 a[148:149], %[vr6] offset:43008\n\t" "v_mfma_f32_16x16x32_bf16 v[104:107], a[132:135], a[88:91], v[104:107]\n\t" "ds_read_b64_tr_b16 a[150:151], %[vr6] offset:46592\n\t"
    "v_mfma_f32_16x16x32_bf16 v[108:111], a[128:131], a[76:79], v[108:111]\n\t" "global_load_dwordx4 a[12:15], %[va], s[76:77] offset:3072 sc1\n\t"
    "v_mfma_f32_16x16x32_bf16 v[108:111], a[132:135], a[92:95], v[108:111]\n\t" "s_add_u32 s76, s76, 16384\n\t" "s_addc_u32 s77, s77, 0\n\t" "s_waitcnt lgkmcnt(4)\n\t"
    "v_mfma_f32_16x16x32_bf16 v[112:115], a[136:139], a[64:67], v[112:115]\n\t" "ds_read_b64_tr_b16 a[152:153], %[vr0] offset:35840\n\t"
    "v_mfma_f32_16x16x32_bf16 v[112:115], a[140:143], a[80:83], v[112:115]\n\t" "ds_read_b64_tr_b16 a[154:155], %[vr0] offset:39424\n\t"
    "v_mfma_f32_16x16x32_bf16 v[116:119], a[136:139], a[68:71], v[116:119]\n\t" "v_mfma_f32_16x16x32_bf16 v[116:119], a[140:143], a[84:87], v[116:119]\n\t"
    "v_mfma_f32_16x16x32_bf16 v[120:123], a[136:139], a[72:75], v[120:123]\n\t" "ds_read_b64_tr_b16 a[156:157], %[vr0] offset:50176\n\t"
    "v_mfma_f32_16x16x32_bf16 v[120:123], a[140:143], a[88:91], v[120:123]\n\t" "ds_read_b64_tr_b16 a[158:159], %[vr0] offset:53760\n\t" "s_add_u32 m0, %[sl], 103424\n\t"
    "v_mfma_f32_16x16x32_bf16 v[124:127], a[136:139], a[76:79], v[124:127]\n\t" "s_mov_b64 exec, s[82:83]\n\t" "global_load_lds_dwordx4 %[vb3], s[78:79]\n\t" "s_mov_b64 exec, -1\n\t"
    "v_mfma_f32_16x16x32_bf16 v[124:127], a[140:143], a[92:95], v[124:127]\n\t" "s_add_u32 s78, s78, 229376\n\t" "s_addc_u32 s79, s79, 0\n\t" "s_waitcnt lgkmcnt(4)\n\t"
    "v_mfma_f32_16x16x32_bf16 v[128:131], a[144:147], a[64:67], v[128:131]\n\t" "ds_read_b64_tr_b16 a[128:129], %[vr0] offset:35872\n\t"
    "v_mfma_f32_16x16x32_bf16 v[128:131], a[148:151], a[80:83], v[128:131]\n\t" "ds_read_b64_tr_b16 a[130:131], %[vr0] offset:39456\n\t"
    "v_mfma_f32_16x16x32_bf16 v[132:135], a[144:147], a[68:71], v[132:135]\n\t" "global_load_dword v29, %[va], %[cd]\n\t" "v_mfma_f32_16x16x32_bf16 v[132:135], a[148:151], a[84:87], v[132:135]\n\t"
    "v_mfma_f32_16x16x32_bf16 v[136:139], a[144:147], a[72:75], v[136:139]\n\t" "ds_read_b64_tr_b16 a[132:133], %[vr0] offset:50208\n\t"
    "v_mfma_f32_16x16x32_bf16 v[136:139], a[148:151], a[88:91], v[136:139]\n\t" "ds_read_b64_tr_b16 a[134:135], %[vr0] offset:53792\n\t"
    "v_mfma_f32_16x16x32_bf16 v[140:143], a[144:147], a[76:79], v[140:143]\n\t" "v_mfma_f32_16x16x32_bf16 v[140:143], a[148:151], a[92:95], v[140:143]\n\t" "s_waitcnt lgkmcnt(4)\n\t"
    "v_mfma_f32_16x16x32_bf16 v[144:147], a[152:155], a[64:67], v[144:147]\n\t" "ds_read_b64_tr_b16 a[136:137], %[vr2] offset:35840\n\t"
    "v_mfma_f32_16x16x32_bf16 v[144:147], a[156:159], a[80:83], v[144:147]\n\t" "ds_read_b64_tr_b16 a[138:139], %[vr2] offset:39424\n\t"
    "v_mfma_f32_16x16x32_bf16 v[148:151], a[152:155], a[68:71], v[148:151]\n\t" "global_load_dwordx4 a[16:19], %[va], s[76:77] offset:0 sc1\n\t"
    "v_mfma_f32_16x16x32_bf16 v[148:151], a[156:159], a[84:87], v[148:151]\n\t" "v_mfma_f32_16x16x32_bf16 v[152:155], a[152:155], a[72:75], v[152:155]\n\t"
    "ds_read_b64_tr_b16 a[140:141], %[vr2] offset:50176\n\t" "v_mfma_f32_16x16x32_bf16 v[152:155], a[156:159], a[88:91], v[152:155]\n\t" "ds_read_b64_tr_b16 a[142:143], %[vr2] offset:53760\n\t"
    "s_add_u32 m0, %[sl], 0\n\t" "v_mfma_f32_16x16x32_bf16 v[156:159], a[152:155], a[76:79], v[156:159]\n\t" "global_load_lds_dwordx4 %[vb0], s[78:79]\n\t"
    "v_mfma_f32_16x16x32_bf16 v[156:159], a[156:159], a[92:95], v[156:159]\n\t" "s_waitcnt lgkmcnt(4)\n\t" "v_mfma_f32_16x16x32_bf16 v[160:163], a[128:131], a[64:67], v[160:163]\n\t"
    "ds_read_b64_tr_b16 a[144:145], %[vr2] offset:35872\n\t" "v_mfma_f32_16x16x32_bf16 v[160:163], a[132:135], a[80:83], v[160:163]\n\t" "ds_read_b64_tr_b16 a[146:147], %[vr2] offset:39456\n\t"
    "v_mfma_f32_16x16x32_bf16 v[164:167], a[128:131], a[68:71], v[164:167]\n\t" "v_mfma_f32_16x16x32_bf16 v[164:167], a[132:135], a[84:87], v[164:167]\n\t"
    "v_mfma_f32_16x16x32_bf16 v[168:171], a[128:131], a[72:75], v[168:171]\n\t" "ds_read_b64_tr_b16 a[148:149], %[vr2] offset:50208\n\t"
    "v_mfma_f32_16x16x32_bf16 v[168:171], a[132:135], a[88:91], v[168:171]\n\t" "ds_read_b64_tr_b16 a[150:151], %[vr2] offset:53792\n\t"
    "v_mfma_f32_16x16x32_bf16 v[172:175], a[128:131], a[76:79], v[172:175]\n\t" "global_load_dwordx4 a[20:23], %[va], s[76:77] offset:1024 sc1\n\t"
    "v_mfma_f32_16x16x32_bf16 v[172:175], a[132:135], a[92:95], v[172:175]\n\t" "s_waitcnt lgkmcnt(4)\n\t" "v_mfma_f32_16x16x32_bf16 v[176:179], a[136:139], a[64:67], v[176:179]\n\t"
    "ds_read_b64_tr_b16 a[152:153], %[vr4] offset:35840\n\t" "v_mfma_f32_16x16x32_bf16 v[176:179], a[140:143], a[80:83], v[176:179]\n\t" "ds_read_b64_tr_b16 a[154:155], %[vr4] offset:39424\n\t"
    "s_add_u32 m0, %[sl], 1024\n\t" "v_mfma_f32_16x16x32_bf16 v[180:183], a[136:139], a[68:71], v[180:183]\n\t" "global_load_lds_dwordx4 %[vb1], s[78:79]\n\t"
    "v_mfma_f32_16x16x32_bf16 v[180:183], a[140:143], a[84:87], v[180:183]\n\t" "v_mfma_f32_16x16x32_bf16 v[184:187], a[136:139], a[72:75], v[184:187]\n\t"
    "ds_read_b64_tr_b16 a[156:157], %[vr4] offset:50176\n\t" "v_mfma_f32_16x16x32_bf16 v[184:187], a[140:143], a[88:91], v[184:187]\n\t" "ds_read_b64_tr_b16 a[158:159], %[vr4] offset:53760\n\t"
    "v_mfma_f32_16x16x32_bf16 v[188:191], a[136:139], a[76:79], v[188:191]\n\t" "v_mfma_f32_16x16x32_bf16 v[188:191], a[140:143], a[92:95], v[188:191]\n\t" "s_waitcnt lgkmcnt(4)\n\t"
    "v_mfma_f32_16x16x32_bf16 v[192:195], a[144:147], a[64:67], v[192:195]\n\t" "ds_read_b64_tr_b16 a[128:129], %[vr4] offset:35872\n\t"
    "v_mfma_f32_16x16x32_bf16 v[192:195], a[148:151], a[80:83], v[192:195]\n\t" "ds_read_b64_tr_b16 a[130:131], %[vr4] offset:39456\n\t"
    "v_mfma_f32_16x16x32_bf16 v[196:199], a[144:147], a[68:71], v[196:199]\n\t" "global_load_dwordx4 a[24:27], %[va], s[76:77] offset:2048 sc1\n\t"
    "v_mfma_f32_16x16x32_bf16 v[196:199], a[148:151], a[84:87], v[196:199]\n\t" "v_mfma_f32_16x16x32_bf16 v[200:203], a[144:147], a[72:75], v[200:203]\n\t"
    "ds_read_b64_tr_b16 a[132:133], %[vr4] offset:50208\n\t" "v_mfma_f32_16x16x32_bf16 v[200:203], a[148:151], a[88:91], v[200:203]\n\t" "ds_read_b64_tr_b16 a[134:135], %[vr4] offset:53792\n\t"
    "v_mfma_f32_16x16x32_bf16 v[204:207], a[144:147], a[76:79], v[204:207]\n\t" "v_mfma_f32_16x16x32_bf16 v[204:207], a[148:151], a[92:95], v[204:207]\n\t" "s_waitcnt lgkmcnt(4)\n\t"
    "v_mfma_f32_16x16x32_bf16 v[208:211], a[152:155], a[64:67], v[208:211]\n\t" "ds_read_b64_tr_b16 a[136:137], %[vr6] offset:35840\n\t"
    "v_mfma_f32_16x16x32_bf16 v[208:211], a[156:159], a[80:83], v[208:211]\n\t" "ds_read_b64_tr_b16 a[138:139], %[vr6] offset:39424\n\t" "s_add_u32 m0, %[sl], 2048\n\t"
    "v_mfma_f32_16x16x32_bf16 v[212:215], a[152:155], a[68:71], v[212:215]\n\t" "global_load_lds_dwordx4 %[vb2], s[78:79]\n\t" "v_mfma_f32_16x16x32_bf16 v[212:215], a[156:159], a[84:87], v[212:215]\n\t"
    "v_mfma_f32_16x16x32_bf16 v[216:219], a[152:155], a[72:75], v[216:219]\n\t" "ds_read_b64_tr_b16 a[140:141], %[vr6] offset:50176\n\t"
    "v_mfma_f32_16x16x32_bf16 v[216:219], a[156:159], a[88:91], v[216:219]\n\t" "ds_read_b64_tr_b16 a[142:143], %[vr6] offset:53760\n\t"
    "v_mfma_f32_16x16x32_bf16 v[220:223], a[152:155], a[76:79], v[220:223]\n\t" "global_load_dwordx4 a[28:31], %[va], s[76:77] offset:3072 sc1\n\t"
    "v_mfma_f32_16x16x32_bf16 v[220:223], a[156:159], a[92:95], v[220:223]\n\t" "s_add_u32 s76, s76, 16384\n\t" "s_addc_u32 s77, s77, 0\n\t" "s_waitcnt lgkmcnt(4)\n\t"
    "v_mfma_f32_16x16x32_bf16 v[224:227], a[128:131], a[64:67], v[224:227]\n\t" "s_waitcnt vmcnt(24)\n\t" "s_barrier\n\t" "ds_read_b64_tr_b16 a[144:145], %[vr1] offset:0\n\t"
    "v_mfma_f32_16x16x32_bf16 v[224:227], a[132:135], a[80:83], v[224:227]\n\t" "ds_read_b64_tr_b16 a[146:147], %[vr1] offset:3584\n\t"
    "v_mfma_f32_16x16x32_bf16 v[228:231], a[128:131], a[68:71], v[228:231]\n\t" "v_mfma_f32_16x16x32_bf16 v[228:231], a[132:135], a[84:87], v[228:231]\n\t"
    "v_mfma_f32_16x16x32_bf16 v[232:235], a[128:131], a[72:75], v[232:235]\n\t" "ds_read_b64_tr_b16 a[148:149], %[vr1] offset:14336\n\t"
    "v_mfma_f32_16x16x32_bf16 v[232:235], a[132:135], a[88:91], v[232:235]\n\t" "ds_read_b64_tr_b16 a[150:151], %[vr1] offset:17920\n\t" "s_add_u32 m0, %[sl], 3072\n\t"
    "v_mfma_f32_16x16x32_bf16 v[236:239], a[128:131], a[76:79], v[236:239]\n\t" "s_mov_b64 exec, s[82:83]\n\t" "global_load_lds_dwordx4 %[vb3], s[78:79]\n\t" "s_mov_b64 exec, -1\n\t"
    "v_mfma_f32_16x16x32_bf16 v[236:239], a[132:135], a[92:95], v[236:239]\n\t" "s_add_u32 s78, s78, 229376\n\t" "s_addc_u32 s79, s79, 0\n\t" "s_waitcnt lgkmcnt(4)\n\t"
    "v_mfma_f32_16x16x32_bf16 v[240:243], a[136:139], a[64:67], v[240:243]\n\t" "ds_read_b64_tr_b16 a[152:153], %[vr1] offset:32\n\t"
    "v_mfma_f32_16x16x32_bf16 v[240:243], a[140:143], a[80:83], v[240:243]\n\t" "ds_read_b64_tr_b16 a[154:155], %[vr1] offset:3616\n\t"
    "v_mfma_f32_16x16x32_bf16 v[244:247], a[136:139], a[68:71], v[244:247]\n\t" "v_mfma_f32_16x16x32_bf16 v[244:247], a[140:143], a[84:87], v[244:247]\n\t"
    "v_mfma_f32_16x16x32_bf16 v[248:251], a[136:139], a[72:75], v[248:251]\n\t" "ds_read_b64_tr_b16 a[156:157], %[vr1] offset:14368\n\t"
    "v_mfma_f32_16x16x32_bf16 v[248:251], a[140:143], a[88:91], v[248:251]\n\t" "ds_read_b64_tr_b16 a[158:159], %[vr1] offset:17952\n\t"
    "v_mfma_f32_16x16x32_bf16 v[252:255], a[136:139], a[76:79], v[252:255]\n\t" "v_mfma_f32_16x16x32_bf16 v[252:255], a[140:143], a[92:95], v[252:255]\n\t" "s_waitcnt lgkmcnt(4) vmcnt(18)\n\t"
    "v_mfma_f32_16x16x32_bf16 v[32:35], a[144:147], a[96:99], v[32:35]\n\t" "s_barrier\n\t" "ds_read_b64_tr_b16 a[128:129], %[vr3] offset:0\n\t"
    "v_mfma_f32_16x16x32_bf16 v[32:35], a[148:151], a[112:115], v[32:35]\n\t" "ds_read_b64_tr_b16 a[130:131], %[vr3] offset:3584\n\t"
    "v_mfma_f32_16x16x32_bf16 v[36:39], a[144:147], a[100:103], v[36:39]\n\t" "global_load_dwordx4 a[32:35], %[va], s[76:77] offset:0 sc1\n\t"
    "v_mfma_f32_16x16x32_bf16 v[36:39], a[148:151], a[116:119], v[36:39]\n\t" "v_mfma_f32_16x16x32_bf16 v[40:43], a[144:147], a[104:107], v[40:43]\n\t"
    "ds_read_b64_tr_b16 a[132:133], %[vr3] offset:14336\n\t" "v_mfma_f32_16x16x32_bf16 v[40:43], a[148:151], a[120:123], v[40:43]\n\t" "ds_read_b64_tr_b16 a[134:135], %[vr3] offset:17920\n\t"
    "s_add_u32 m0, %[sl], 14336\n\t" "v_mfma_f32_16x16x32_bf16 v[44:47], a[144:147], a[108:111], v[44:47]\n\t" "global_load_lds_dwordx4 %[vb0], s[78:79]\n\t"
    "v_mfma_f32_16x16x32_bf16 v[44:47], a[148:151], a[124:127], v[44:47]\n\t" "s_waitcnt lgkmcnt(4)\n\t" "v_mfma_f32_16x16x32_bf16 v[48:51], a[152:155], a[96:99], v[48:51]\n\t"
    "ds_read_b64_tr_b16 a[136:137], %[vr3] offset:32\n\t" "v_mfma_f32_16x16x32_bf16 v[48:51], a[156:159], a[112:115], v[48:51]\n\t" "ds_read_b64_tr_b16 a[138:139], %[vr3] offset:3616\n\t"
    "v_mfma_f32_16x16x32_bf16 v[52:55], a[152:155], a[100:103], v[52:55]\n\t" "v_mfma_f32_16x16x32_bf16 v[52:55], a[156:159], a[116:119], v[52:55]\n\t"
    "v_mfma_f32_16x16x32_bf16 v[56:59], a[152:155], a[104:107], v[56:59]\n\t" "ds_read_b64_tr_b16 a[140:141], %[vr3] offset:14368\n\t"
    "v_mfma_f32_16x16x32_bf16 v[56:59], a[156:159], a[120:123], v[56:59]\n\t" "ds_read_b64_tr_b16 a[142:143], %[vr3] offset:17952\n\t"
    "v_mfma_f32_16x16x32_bf16 v[60:63], a[152:155], a[108:111], v[60:63]\n\t" "global_load_dwordx4 a[36:39], %[va], s[76:77] offset:1024 sc1\n\t"
    "v_mfma_f32_16x16x32_bf16 v[60:63], a[156:159], a[124:127], v[60:63]\n\t" "s_waitcnt lgkmcnt(4)\n\t" "v_mfma_f32_16x16x32_bf16 v[64:67], a[128:131], a[96:99], v[64:67]\n\t"
    "ds_read_b64_tr_b16 a[144:145], %[vr5] offset:0\n\t" "v_mfma_f32_16x16x32_bf16 v[64:67], a[132:135], a[112:115], v[64:67]\n\t" "ds_read_b64_tr_b16 a[146:147], %[vr5] offset:3584\n\t"
    "s_add_u32 m0, %[sl], 15360\n\t" "v_mfma_f32_16x16x32_bf16 v[68:71], a[128:131], a[100:103], v[68:71]\n\t" "global_load_lds_dwordx4 %[vb1], s[78:79]\n\t"
    "v_mfma_f32_16x16x32_bf16 v[68:71], a[132:135], a[116:119], v[68:71]\n\t" "v_mfma_f32_16x16x32_bf16 v[72:75], a[128:131], a[104:107], v[72:75]\n\t"
    "ds_read_b64_tr_b16 a[148:149], %[vr5] offset:14336\n\t" "v_mfma_f32_16x16x32_bf16 v[72:75], a[132:135], a[120:123], v[72:75]\n\t" "ds_read_b64_tr_b16 a[150:151], %[vr5] offset:17920\n\t"
    "v_mfma_f32_16x16x32_bf16 v[76:79], a[128:131], a[108:111], v[76:79]\n\t" "v_mfma_f32_16x16x32_bf16 v[76:79], a[132:135], a[124:127], v[76:79]\n\t" "s_waitcnt lgkmcnt(4)\n\t"
    "v_mfma_f32_16x16x32_bf16 v[80:83], a[136:139], a[96:99], v[80:83]\n\t" "ds_read_b64_tr_b16 a[152:153], %[vr5] offset:32\n\t" "v_mfma_f32_16x16x32_bf16 v[80:83], a[140:143], a[112:115], v[80:83]\n\t"
    "ds_read_b64_tr_b16 a[154:155], %[vr5] offset:3616\n\t" "v_mfma_f32_16x16x32_bf16 v[84:87], a[136:139], a[100:103], v[84:87]\n\t" "global_load_dwordx4 a[40:43], %[va], s[76:77] offset:2048 sc1\n\t"
    "v_mfma_f32_16x16x32_bf16 v[84:87], a[140:143], a[116:119], v[84:87]\n\t" "v_mfma_f32_16x16x32_bf16 v[88:91], a[136:139], a[104:107], v[88:91]\n\t"
    "ds_read_b64_tr_b16 a[156:157], %[vr5] offset:14368\n\t" "v_mfma_f32_16x16x32_bf16 v[88:91], a[140:143], a[120:123], v[88:91]\n\t" "ds_read_b64_tr_b16 a[158:159], %[vr5] offset:17952\n\t"
    "v_mfma_f32_16x16x32_bf16 v[92:95], a[136:139], a[108:111], v[92:95]\n\t" "v_mfma_f32_16x16x32_bf16 v[92:95], a[140:143], a[124:127], v[92:95]\n\t" "s_waitcnt lgkmcnt(4)\n\t"
    "v_mfma_f32_16x16x32_bf16 v[96:99], a[144:147], a[96:99], v[96:99]\n\t" "ds_read_b64_tr_b16 a[128:129], %[vr7] offset:0\n\t" "v_mfma_f32_16x16x32_bf16 v[96:99], a[148:151], a[112:115], v[96:99]\n\t"
    "ds_read_b64_tr_b16 a[130:131], %[vr7] offset:3584\n\t" "s_add_u32 m0, %[sl], 16384\n\t" "v_mfma_f32_16x16x32_bf16 v[100:103], a[144:147], a[100:103], v[100:103]\n\t"
    "global_load_lds_dwordx4 %[vb2], s[78:79]\n\t" "v_mfma_f32_16x16x32_bf16 v[100:103], a[148:151], a[116:119], v[100:103]\n\t"
    "v_mfma_f32_16x16x32_bf16 v[104:107], a[144:147], a[104:107], v[104:107]\n\t" "ds_read_b64_tr_b16 a[132:133], %[vr7] offset:14336\n\t"
    "v_mfma_f32_16x16x32_bf16 v[104:107], a[148:151], a[120:123], v[104:107]\n\t" "ds_read_b64_tr_b16 a[134:135], %[vr7] offset:17920\n\t"
    "v_mfma_f32_16x16x32_bf16 v[108:111], a[144:147], a[108:111], v[108:111]\n\t" "global_load_dwordx4 a[44:47], %[va], s[76:77] offset:3072 sc1\n\t"
    "v_mfma_f32_16x16x32_bf16 v[108:111], a[148:151], a[124:127], v[108:111]\n\t" "s_add_u32 s76, s76, 16384\n\t" "s_addc_u32 s77, s77, 0\n\t" "s_waitcnt lgkmcnt(4)\n\t"
    "v_mfma_f32_16x16x32_bf16 v[112:115], a[152:155], a[96:99], v[112:115]\n\t" "ds_read_b64_tr_b16 a[136:137], %[vr1] offset:7168\n\t"
    "v_mfma_f32_16x16x32_bf16 v[112:115], a[156:159], a[112:115], v[112:115]\n\t" "ds_read_b64_tr_b16 a[138:139], %[vr1] offset:10752\n\t"
    "v_mfma_f32_16x16x32_bf16 v[116:119], a[152:155], a[100:103], v[116:119]\n\t" "v_mfma_f32_16x16x32_bf16 v[116:119], a[156:159], a[116:119], v[116:119]\n\t"
    "v_mfma_f32_16x16x32_bf16 v[120:123], a[152:155], a[104:107], v[120:123]\n\t" "ds_read_b64_tr_b16 a[140:141], %[vr1] offset:21504\n\t"
    "v_mfma_f32_16x16x32_bf16 v[120:123], a[156:159], a[120:123], v[120:123]\n\t" "ds_read_b64_tr_b16 a[142:143], %[vr1] offset:25088\n\t" "s_add_u32 m0, %[sl], 17408\n\t"
    "v_mfma_f32_16x16x32_bf16 v[124:127], a[152:155], a[108:111], v[124:127]\n\t" "s_mov_b64 exec, s[82:83]\n\t" "global_load_lds_dwordx4 %[vb3], s[78:79]\n\t" "s_mov_b64 exec, -1\n\t"
    "v_mfma_f32_16x16x32_bf16 v[124:127], a[156:159], a[124:127], v[124:127]\n\t" "s_add_u32 s78, s78, 229376\n\t" "s_addc_u32 s79, s79, 0\n\t" "s_waitcnt lgkmcnt(4)\n\t"
    "v_mfma_f32_16x16x32_bf16 v[128:131], a[128:131], a[96:99], v[128:131]\n\t" "ds_read_b64_tr_b16 a[144:145], %[vr1] offset:7200\n\t"
    "v_mfma_f32_16x16x32_bf16 v[128:131], a[132:135], a[112:115], v[128:131]\n\t" "ds_read_b64_tr_b16 a[146:147], %[vr1] offset:10784\n\t"
    "v_mfma_f32_16x16x32_bf16 v[132:135], a[128:131], a[100:103], v[132:135]\n\t" "v_mfma_f32_16x16x32_bf16 v[132:135], a[132:135], a[116:119], v[132:135]\n\t"
    "v_mfma_f32_16x16x32_bf16 v[136:139], a[128:131], a[104:107], v[136:139]\n\t" "ds_read_b64_tr_b16 a[148:149], %[vr1] offset:21536\n\t"
    "v_mfma_f32_16x16x32_bf16 v[136:139], a[132:135], a[120:123], v[136:139]\n\t" "ds_read_b64_tr_b16 a[150:151], %[vr1] offset:25120\n\t"
    "v_mfma_f32_16x16x32_bf16 v[140:143], a[128:131], a[108:111], v[140:143]\n\t" "v_mfma_f32_16x16x32_bf16 v[140:143], a[132:135], a[124:127], v[140:143]\n\t" "s_waitcnt lgkmcnt(4)\n\t"
    "v_mfma_f32_16x16x32_bf16 v[144:147], a[136:139], a[96:99], v[144:147]\n\t" "ds_read_b64_tr_b16 a[152:153], %[vr3] offset:7168\n\t"
    "v_mfma_f32_16x16x32_bf16 v[144:147], a[140:143], a[112:115], v[144:147]\n\t" "ds_read_b64_tr_b16 a[154:155], %[vr3] offset:10752\n\t"
    "v_mfma_f32_16x16x32_bf16 v[148:151], a[136:139], a[100:103], v[148:151]\n\t" "global_load_dwordx4 a[48:51], %[va], s[76:77] offset:0 sc1\n\t"
    "v_mfma_f32_16x16x32_bf16 v[148:151], a[140:143], a[116:119], v[148:151]\n\t" "v_mfma_f32_16x16x32_bf16 v[152:155], a[136:139], a[104:107], v[152:155]\n\t"
    "ds_read_b64_tr_b16 a[156:157], %[vr3] offset:21504\n\t" "v_mfma_f32_16x16x32_bf16 v[152:155], a[140:143], a[120:123], v[152:155]\n\t" "ds_read_b64_tr_b16 a[158:159], %[vr3] offset:25088\n\t"
    "s_add_u32 m0, %[sl], 28672\n\t" "v_mfma_f32_16x16x32_bf16 v[156:159], a[136:139], a[108:111], v[156:159]\n\t" "global_load_lds_dwordx4 %[vb0], s[78:79]\n\t"
    "v_mfma_f32_16x16x32_bf16 v[156:159], a[140:143], a[124:127], v[156:159]\n\t" "s_waitcnt lgkmcnt(4)\n\t" "v_mfma_f32_16x16x32_bf16 v[160:163], a[144:147], a[96:99], v[160:163]\n\t"
    "ds_read_b64_tr_b16 a[128:129], %[vr3] offset:7200\n\t" "v_mfma_f32_16x16x32_bf16 v[160:163], a[148:151], a[112:115], v[160:163]\n\t" "ds_read_b64_tr_b16 a[130:131], %[vr3] offset:10784\n\t"
    "v_mfma_f32_16x16x32_bf16 v[164:167], a[144:147], a[100:103], v[164:167]\n\t" "v_mfma_f32_16x16x32_bf16 v[164:167], a[148:151], a[116:119], v[164:167]\n\t"
    "v_mfma_f32_16x16x32_bf16 v[168:171], a[144:147], a[104:107], v[168:171]\n\t" "ds_read_b64_tr_b16 a[132:133], %[vr3] offset:21536\n\t"
    "v_mfma_f32_16x16x32_bf16 v[168:171], a[148:151], a[120:123], v[168:171]\n\t" "ds_read_b64_tr_b16 a[134:135], %[vr3] offset:25120\n\t"
    "v_mfma_f32_16x16x32_bf16 v[172:175], a[144:147], a[108:111], v[172:175]\n\t" "global_load_dwordx4 a[52:55], %[va], s[76:77] offset:1024 sc1\n\t"
    "v_mfma_f32_16x16x32_bf16 v[172:175], a[148:151], a[124:127], v[172:175]\n\t" "s_waitcnt lgkmcnt(4)\n\t" "v_mfma_f32_16x16x32_bf16 v[176:179], a[152:155], a[96:99], v[176:179]\n\t"
    "ds_read_b64_tr_b16 a[136:137], %[vr5] offset:7168\n\t" "v_mfma_f32_16x16x32_bf16 v[176:179], a[156:159], a[112:115], v[176:179]\n\t" "ds_read_b64_tr_b16 a[138:139], %[vr5] offset:10752\n\t"
    "s_add_u32 m0, %[sl], 29696\n\t" "v_mfma_f32_16x16x32_bf16 v[180:183], a[152:155], a[100:103], v[180:183]\n\t" "global_load_lds_dwordx4 %[vb1], s[78:79]\n\t"
    "v_mfma_f32_16x16x32_bf16 v[180:183], a[156:159], a[116:119], v[180:183]\n\t" "v_mfma_f32_16x16x32_bf16 v[184:187], a[152:155], a[104:107], v[184:187]\n\t"
    "ds_read_b64_tr_b16 a[140:141], %[vr5] offset:21504\n\t" "v_mfma_f32_16x16x32_bf16 v[184:187], a[156:159], a[120:123], v[184:187]\n\t" "ds_read_b64_tr_b16 a[142:143], %[vr5] offset:25088\n\t"
    "v_mfma_f32_16x16x32_bf16 v[188:191], a[152:155], a[108:111], v[188:191]\n\t" "v_mfma_f32_16x16x32_bf16 v[188:191], a[156:159], a[124:127], v[188:191]\n\t" "s_waitcnt lgkmcnt(4)\n\t"
    "v_mfma_f32_16x16x32_bf16 v[192:195], a[128:131], a[96:99], v[192:195]\n\t" "ds_read_b64_tr_b16 a[144:145], %[vr5] offset:7200\n\t"
    "v_mfma_f32_16x16x32_bf16 v[192:195], a[132:135], a[112:115], v[192:195]\n\t" "ds_read_b64_tr_b16 a[146:147], %[vr5] offset:10784\n\t"
    "v_mfma_f32_16x16x32_bf16 v[196:199], a[128:131], a[100:103], v[196:199]\n\t" "global_load_dwordx4 a[56:59], %[va], s[76:77] offset:2048 sc1\n\t"
    "v_mfma_f32_16x16x32_bf16 v[196:199], a[132:135], a[116:119], v[196:199]\n\t" "v_mfma_f32_16x16x32_bf16 v[200:203], a[128:131], a[104:107], v[200:203]\n\t"
    "ds_read_b64_tr_b16 a[148:149], %[vr5] offset:21536\n\t" "v_mfma_f32_16x16x32_bf16 v[200:203], a[132:135], a[120:123], v[200:203]\n\t" "ds_read_b64_tr_b16 a[150:151], %[vr5] offset:25120\n\t"
    "v_mfma_f32_16x16x32_bf16 v[204:207], a[128:131], a[108:111], v[204:207]\n\t" "v_mfma_f32_16x16x32_bf16 v[204:207], a[132:135], a[124:127], v[204:207]\n\t" "s_waitcnt lgkmcnt(4)\n\t"
    "v_mfma_f32_16x16x32_bf16 v[208:211], a[136:139], a[96:99], v[208:211]\n\t" "ds_read_b64_tr_b16 a[152:153], %[vr7] offset:7168\n\t"
    "v_mfma_f32_16x16x32_bf16 v[208:211], a[140:143], a[112:115], v[208:211]\n\t" "ds_read_b64_tr_b16 a[154:155], %[vr7] offset:10752\n\t" "s_add_u32 m0, %[sl], 30720\n\t"
    "v_mfma_f32_16x16x32_bf16 v[212:215], a[136:139], a[100:103], v[212:215]\n\t" "global_load_lds_dwordx4 %[vb2], s[78:79]\n\t"
    "v_mfma_f32_16x16x32_bf16 v[212:215], a[140:143], a[116:119], v[212:215]\n\t" "v_mfma_f32_16x16x32_bf16 v[216:219], a[136:139], a[104:107], v[216:219]\n\t"
    "ds_read_b64_tr_b16 a[156:157], %[vr7] offset:21504\n\t" "v_mfma_f32_16x16x32_bf16 v[216:219], a[140:143], a[120:123], v[216:219]\n\t" "ds_read_b64_tr_b16 a[158:159], %[vr7] offset:25088\n\t"
    "v_mfma_f32_16x16x32_bf16 v[220:223], a[136:139], a[108:111], v[220:223]\n\t" "global_load_dwordx4 a[60:63], %[va], s[76:77] offset:3072 sc1\n\t"
    "v_mfma_f32_16x16x32_bf16 v[220:223], a[140:143], a[124:127], v[220:223]\n\t" "s_add_u32 s76, s76, 16384\n\t" "s_addc_u32 s77, s77, 0\n\t" "s_waitcnt lgkmcnt(4)\n\t"
    "v_mfma_f32_16x16x32_bf16 v[224:227], a[144:147], a[96:99], v[224:227]\n\t" "s_waitcnt vmcnt(24)\n\t" "s_barrier\n\t" "ds_read_b64_tr_b16 a[128:129], %[vr1] offset:28672\n\t"
    "v_mfma_f32_16x16x32_bf16 v[224:227], a[148:151], a[112:115], v[224:227]\n\t" "ds_read_b64_tr_b16 a[130:131], %[vr1] offset:32256\n\t"
    "v_mfma_f32_16x16x32_bf16 v[228:231], a[144:147], a[100:103], v[228:231]\n\t" "v_mfma_f32_16x16x32_bf16 v[228:231], a[148:151], a[116:119], v[228:231]\n\t"
    "v_mfma_f32_16x16x32_bf16 v[232:235], a[144:147], a[104:107], v[232:235]\n\t" "ds_read_b64_tr_b16 a[132:133], %[vr1] offset:43008\n\t"
    "v_mfma_f32_16x16x32_bf16 v[232:235], a[148:151], a[120:123], v[232:235]\n\t" "ds_read_b64_tr_b16 a[134:135], %[vr1] offset:46592\n\t" "s_add_u32 m0, %[sl], 31744\n\t"
    "v_mfma_f32_16x16x32_bf16 v[236:239], a[144:147], a[108:111], v[236:239]\n\t" "s_mov_b64 exec, s[82:83]\n\t" "global_load_lds_dwordx4 %[vb3], s[78:79]\n\t" "s_mov_b64 exec, -1\n\t"
    "v_mfma_f32_16x16x32_bf16 v[236:239], a[148:151], a[124:127], v[236:239]\n\t" "s_add_u32 s78, s78, 229376\n\t" "s_addc_u32 s79, s79, 0\n\t" "s_waitcnt lgkmcnt(4)\n\t"
    "v_mfma_f32_16x16x32_bf16 v[240:243], a[152:155], a[96:99], v[240:243]\n\t" "ds_read_b64_tr_b16 a[136:137], %[vr1] offset:28704\n\t"
    "v_mfma_f32_16x16x32_bf16 v[240:243], a[156:159], a[112:115], v[240:243]\n\t" "ds_read_b64_tr_b16 a[138:139], %[vr1] offset:32288\n\t"
    "v_mfma_f32_16x16x32_bf16 v[244:247], a[152:155], a[100:103], v[244:247]\n\t" "v_mfma_f32_16x16x32_bf16 v[244:247], a[156:159], a[116:119], v[244:247]\n\t"
    "v_mfma_f32_16x16x32_bf16 v[248:251], a[152:155], a[104:107], v[248:251]\n\t" "ds_read_b64_tr_b16 a[140:141], %[vr1] offset:43040\n\t"
    "v_mfma_f32_16x16x32_bf16 v[248:251], a[156:159], a[120:123], v[248:251]\n\t" "ds_read_b64_tr_b16 a[142:143], %[vr1] offset:46624\n\t"
    "v_mfma_f32_16x16x32_bf16 v[252:255], a[152:155], a[108:111], v[252:255]\n\t" "v_mfma_f32_16x16x32_bf16 v[252:255], a[156:159], a[124:127], v[252:255]\n\t" ".Ld1_%=:\n\t" "s_cmp_eq_u32 s80, 0\n\t"
    "s_cselect_b32 s86, %[wa], 16384\n\t" "s_ashr_i32 s87, s86, 31\n\t" "s_cmp_eq_u32 s80, 0\n\t" "s_cselect_b32 s88, %[wb], 229376\n\t" "s_ashr_i32 s89, s88, 31\n\t" "s_cmp_eq_u32 s80, %[e0]\n\t"
    "s_cbranch_scc1 .LE0_%=\n\t" "s_waitcnt lgkmcnt(4) vmcnt(17)\n\t" "v_mfma_f32_16x16x32_bf16 v[32:35], a[128:131], a[0:3], v[32:35]\n\t" "s_barrier\n\t"
    "ds_read_b64_tr_b16 a[144:145], %[vr3] offset:28672\n\t" "v_mfma_f32_16x16x32_bf16 v[32:35], a[132:135], a[16:19], v[32:35]\n\t" "ds_read_b64_tr_b16 a[146:147], %[vr3] offset:32256\n\t"
    "v_mfma_f32_16x16x32_bf16 v[36:39], a[128:131], a[4:7], v[36:39]\n\t" "global_load_dwordx4 a[64:67], %[va], s[76:77] offset:0 sc1\n\t"
    "v_mfma_f32_16x16x32_bf16 v[36:39], a[132:135], a[20:23], v[36:39]\n\t" "v_mfma_f32_16x16x32_bf16 v[40:43], a[128:131], a[8:11], v[40:43]\n\t" "ds_read_b64_tr_b16 a[148:149], %[vr3] offset:43008\n\t"
    "v_mfma_f32_16x16x32_bf16 v[40:43], a[132:135], a[24:27], v[40:43]\n\t" "ds_read_b64_tr_b16 a[150:151], %[vr3] offset:46592\n\t" "s_add_u32 m0, %[sl], 43008\n\t"
    "v_mfma_f32_16x16x32_bf16 v[44:47], a[128:131], a[12:15], v[44:47]\n\t" "global_load_lds_dwordx4 %[vb0], s[78:79]\n\t" "v_mfma_f32_16x16x32_bf16 v[44:47], a[132:135], a[28:31], v[44:47]\n\t"
    "s_waitcnt lgkmcnt(4)\n\t" "v_mfma_f32_16x16x32_bf16 v[48:51], a[136:139], a[0:3], v[48:51]\n\t" "ds_read_b64_tr_b16 a[152:153], %[vr3] offset:28704\n\t"
    "v_mfma_f32_16x16x32_bf16 v[48:51], a[140:143], a[16:19], v[48:51]\n\t" "ds_read_b64_tr_b16 a[154:155], %[vr3] offset:32288\n\t" "v_mfma_f32_16x16x32_bf16 v[52:55], a[136:139], a[4:7], v[52:55]\n\t"
    "v_mfma_f32_16x16x32_bf16 v[52:55], a[140:143], a[20:23], v[52:55]\n\t" "v_mfma_f32_16x16x32_bf16 v[56:59], a[136:139], a[8:11], v[56:59]\n\t" "ds_read_b64_tr_b16 a[156:157], %[vr3] offset:43040\n\t"
    "v_mfma_f32_16x16x32_bf16 v[56:59], a[140:143], a[24:27], v[56:59]\n\t" "ds_read_b64_tr_b16 a[158:159], %[vr3] offset:46624\n\t" "v_mfma_f32_16x16x32_bf16 v[60:63], a[136:139], a[12:15], v[60:63]\n\t"
    "global_load_dwordx4 a[68:71], %[va], s[76:77] offset:1024 sc1\n\t" "v_mfma_f32_16x16x32_bf16 v[60:63], a[140:143], a[28:31], v[60:63]\n\t" "s_waitcnt lgkmcnt(4)\n\t"
    "v_mfma_f32_16x16x32_bf16 v[64:67], a[144:147], a[0:3], v[64:67]\n\t" "ds_read_b64_tr_b16 a[128:129], %[vr5] offset:28672\n\t" "v_mfma_f32_16x16x32_bf16 v[64:67], a[148:151], a[16:19], v[64:67]\n\t"
    "ds_read_b64_tr_b16 a[130:131], %[vr5] offset:32256\n\t" "s_add_u32 m0, %[sl], 44032\n\t" "v_mfma_f32_16x16x32_bf16 v[68:71], a[144:147], a[4:7], v[68:71]\n\t"
    "global_load_lds_dwordx4 %[vb1], s[78:79]\n\t" "v_mfma_f32_16x16x32_bf16 v[68:71], a[148:151], a[20:23], v[68:71]\n\t" "v_mfma_f32_16x16x32_bf16 v[72:75], a[144:147], a[8:11], v[72:75]\n\t"
    "ds_read_b64_tr_b16 a[132:133], %[vr5] offset:43008\n\t" "v_mfma_f32_16x16x32_bf16 v[72:75], a[148:151], a[24:27], v[72:75]\n\t" "ds_read_b64_tr_b16 a[134:135], %[vr5] offset:46592\n\t"
    "v_mfma_f32_16x16x32_bf16 v[76:79], a[144:147], a[12:15], v[76:79]\n\t" "v_mfma_f32_16x16x32_bf16 v[76:79], a[148:151], a[28:31], v[76:79]\n\t" "s_waitcnt lgkmcnt(4)\n\t"
    "v_mfma_f32_16x16x32_bf16 v[80:83], a[152:155], a[0:3], v[80:83]\n\t" "ds_read_b64_tr_b16 a[136:137], %[vr5] offset:28704\n\t" "v_mfma_f32_16x16x32_bf16 v[80:83], a[156:159], a[16:19], v[80:83]\n\t"
    "ds_read_b64_tr_b16 a[138:139], %[vr5] offset:32288\n\t" "v_mfma_f32_16x16x32_bf16 v[84:87], a[152:155], a[4:7], v[84:87]\n\t" "global_load_dwordx4 a[72:75], %[va], s[76:77] offset:2048 sc1\n\t"
    "v_mfma_f32_16x16x32_bf16 v[84:87], a[156:159], a[20:23], v[84:87]\n\t" "v_mfma_f32_16x16x32_bf16 v[88:91], a[152:155], a[8:11], v[88:91]\n\t" "ds_read_b64_tr_b16 a[140:141], %[vr5] offset:43040\n\t"
    "v_mfma_f32_16x16x32_bf16 v[88:91], a[156:159], a[24:27], v[88:91]\n\t" "ds_read_b64_tr_b16 a[142:143], %[vr5] offset:46624\n\t" "v_mfma_f32_16x16x32_bf16 v[92:95], a[152:155], a[12:15], v[92:95]\n\t"
    "v_mfma_f32_16x16x32_bf16 v[92:95], a[156:159], a[28:31], v[92:95]\n\t" "s_waitcnt lgkmcnt(4)\n\t" "v_mfma_f32_16x16x32_bf16 v[96:99], a[128:131], a[0:3], v[96:99]\n\t"
    "ds_read_b64_tr_b16 a[144:145], %[vr7] offset:28672\n\t" "v_mfma_f32_16x16x32_bf16 v[96:99], a[132:135], a[16:19], v[96:99]\n\t" "ds_read_b64_tr_b16 a[146:147], %[vr7] offset:32256\n\t"
    "s_add_u32 m0, %[sl], 45056\n\t" "v_mfma_f32_16x16x32_bf16 v[100:103], a[128:131], a[4:7], v[100:103]\n\t" "global_load_lds_dwordx4 %[vb2], s[78:79]\n\t"
    "v_mfma_f32_16x16x32_bf16 v[100:103], a[132:135], a[20:23], v[100:103]\n\t" "v_mfma_f32_16x16x32_bf16 v[104:107], a[128:131], a[8:11], v[104:107]\n\t"
    "ds_read_b64_tr_b16 a[148:149], %[vr7] offset:43008\n\t" "v_mfma_f32_16x16x32_bf16 v[104:107], a[132:135], a[24:27], v[104:107]\n\t" "ds_read_b64_tr_b16 a[150:151], %[vr7] offset:46592\n\t"
    "v_mfma_f32_16x16x32_bf16 v[108:111], a[128:131], a[12:15], v[108:111]\n\t" "global_load_dwordx4 a[76:79], %[va], s[76:77] offset:3072 sc1\n\t"
    "v_mfma_f32_16x16x32_bf16 v[108:111], a[132:135], a[28:31], v[108:111]\n\t" "s_add_u32 s76, s76, 16384\n\t" "s_addc_u32 s77, s77, 0\n\t" "s_waitcnt lgkmcnt(4)\n\t"
    "v_mfma_f32_16x16x32_bf16 v[112:115], a[136:139], a[0:3], v[112:115]\n\t" "ds_read_b64_tr_b16 a[152:153], %[vr1] offset:35840\n\t"
    "v_mfma_f32_16x16x32_bf16 v[112:115], a[140:143], a[16:19], v[112:115]\n\t" "ds_read_b64_tr_b16 a[154:155], %[vr1] offset:39424\n\t"
    "v_mfma_f32_16x16x32_bf16 v[116:119], a[136:139], a[4:7], v[116:119]\n\t" "v_mfma_f32_16x16x32_bf16 v[116:119], a[140:143], a[20:23], v[116:119]\n\t"
    "v_mfma_f32_16x16x32_bf16 v[120:123], a[136:139], a[8:11], v[120:123]\n\t" "ds_read_b64_tr_b16 a[156:157], %[vr1] offset:50176\n\t"
    "v_mfma_f32_16x16x32_bf16 v[120:123], a[140:143], a[24:27], v[120:123]\n\t" "ds_read_b64_tr_b16 a[158:159], %[vr1] offset:53760\n\t" "s_add_u32 m0, %[sl], 46080\n\t"
    "v_mfma_f32_16x16x32_bf16 v[124:127], a[136:139], a[12:15], v[124:127]\n\t" "s_mov_b64 exec, s[82:83]\n\t" "global_load_lds_dwordx4 %[vb3], s[78:79]\n\t" "s_mov_b64 exec, -1\n\t"
    "v_mfma_f32_16x16x32_bf16 v[124:127], a[140:143], a[28:31], v[124:127]\n\t" "s_add_u32 s78, s78, 229376\n\t" "s_addc_u32 s79, s79, 0\n\t" "s_waitcnt lgkmcnt(4)\n\t"
    "v_mfma_f32_16x16x32_bf16 v[128:131], a[144:147], a[0:3], v[128:131]\n\t" "ds_read_b64_tr_b16 a[128:129], %[vr1] offset:35872\n\t"
    "v_mfma_f32_16x16x32_bf16 v[128:131], a[148:151], a[16:19], v[128:131]\n\t" "ds_read_b64_tr_b16 a[130:131], %[vr1] offset:39456\n\t"
    "v_mfma_f32_16x16x32_bf16 v[132:135], a[144:147], a[4:7], v[132:135]\n\t" "global_load_dword v29, %[va], %[cd]\n\t" "v_mfma_f32_16x16x32_bf16 v[132:135], a[148:151], a[20:23], v[132:135]\n\t"
    "v_mfma_f32_16x16x32_bf16 v[136:139], a[144:147], a[8:11], v[136:139]\n\t" "ds_read_b64_tr_b16 a[132:133], %[vr1] offset:50208\n\t"
    "v_mfma_f32_16x16x32_bf16 v[136:139], a[148:151], a[24:27], v[136:139]\n\t" "ds_read_b64_tr_b16 a[134:135], %[vr1] offset:53792\n\t"
    "v_mfma_f32_16x16x32_bf16 v[140:143], a[144:147], a[12:15], v[140:143]\n\t" "v_mfma_f32_16x16x32_bf16 v[140:143], a[148:151], a[28:31], v[140:143]\n\t" "s_waitcnt lgkmcnt(4)\n\t"
    "v_mfma_f32_16x16x32_bf16 v[144:147], a[152:155], a[0:3], v[144:147]\n\t" "ds_read_b64_tr_b16 a[136:137], %[vr3] offset:35840\n\t"
    "v_mfma_f32_16x16x32_bf16 v[144:147], a[156:159], a[16:19], v[144:147]\n\t" "ds_read_b64_tr_b16 a[138:139], %[vr3] offset:39424\n\t"
    "v_mfma_f32_16x16x32_bf16 v[148:151], a[152:155], a[4:7], v[148:151]\n\t" "global_load_dwordx4 a[80:83], %[va], s[76:77] offset:0 sc1\n\t"
    "v_mfma_f32_16x16x32_bf16 v[148:151], a[156:159], a[20:23], v[148:151]\n\t" "v_mfma_f32_16x16x32_bf16 v[152:155], a[152:155], a[8:11], v[152:155]\n\t"
    "ds_read_b64_tr_b16 a[140:141], %[vr3] offset:50176\n\t" "v_mfma_f32_16x16x32_bf16 v[152:155], a[156:159], a[24:27], v[152:155]\n\t" "ds_read_b64_tr_b16 a[142:143], %[vr3] offset:53760\n\t"
    "s_add_u32 m0, %[sl], 57344\n\t" "v_mfma_f32_16x16x32_bf16 v[156:159], a[152:155], a[12:15], v[156:159]\n\t" "global_load_lds_dwordx4 %[vb0], s[78:79]\n\t"
    "v_mfma_f32_16x16x32_bf16 v[156:159], a[156:159], a[28:31], v[156:159]\n\t" "s_waitcnt lgkmcnt(4)\n\t" "v_mfma_f32_16x16x32_bf16 v[160:163], a[128:131], a[0:3], v[160:163]\n\t"
    "ds_read_b64_tr_b16 a[144:145], %[vr3] offset:35872\n\t" "v_mfma_f32_16x16x32_bf16 v[160:163], a[132:135], a[16:19], v[160:163]\n\t" "ds_read_b64_tr_b16 a[146:147], %[vr3] offset:39456\n\t"
    "v_mfma_f32_16x16x32_bf16 v[164:167], a[128:131], a[4:7], v[164:167]\n\t" "v_mfma_f32_16x16x32_bf16 v[164:167], a[132:135], a[20:23], v[164:167]\n\t"
    "v_mfma_f32_16x16x32_bf16 v[168:171], a[128:131], a[8:11], v[168:171]\n\t" "ds_read_b64_tr_b16 a[148:149], %[vr3] offset:50208\n\t"
    "v_mfma_f32_16x16x32_bf16 v[168:171], a[132:135], a[24:27], v[168:171]\n\t" "ds_read_b64_tr_b16 a[150:151], %[vr3] offset:53792\n\t"
    "v_mfma_f32_16x16x32_bf16 v[172:175], a[128:131], a[12:15], v[172:175]\n\t" "global_load_dwordx4 a[84:87], %[va], s[76:77] offset:1024 sc1\n\t"
    "v_mfma_f32_16x16x32_bf16 v[172:175], a[132:135], a[28:31], v[172:175]\n\t" "s_waitcnt lgkmcnt(4)\n\t" "v_mfma_f32_16x16x32_bf16 v[176:179], a[136:139], a[0:3], v[176:179]\n\t"
    "ds_read_b64_tr_b16 a[152:153], %[vr5] offset:35840\n\t" "v_mfma_f32_16x16x32_bf16 v[176:179], a[140:143], a[16:19], v[176:179]\n\t" "ds_read_b64_tr_b16 a[154:155], %[vr5] offset:39424\n\t"
    "s_add_u32 m0, %[sl], 58368\n\t" "v_mfma_f32_16x16x32_bf16 v[180:183], a[136:139], a[4:7], v[180:183]\n\t" "global_load_lds_dwordx4 %[vb1], s[78:79]\n\t"
    "v_mfma_f32_16x16x32_bf16 v[180:183], a[140:143], a[20:23], v[180:183]\n\t" "v_mfma_f32_16x16x32_bf16 v[184:187], a[136:139], a[8:11], v[184:187]\n\t"
    "ds_read_b64_tr_b16 a[156:157], %[vr5] offset:50176\n\t" "v_mfma_f32_16x16x32_bf16 v[184:187], a[140:143], a[24:27], v[184:187]\n\t" "ds_read_b64_tr_b16 a[158:159], %[vr5] offset:53760\n\t"
    "v_mfma_f32_16x16x32_bf16 v[188:191], a[136:139], a[12:15], v[188:191]\n\t" "v_mfma_f32_16x16x32_bf16 v[188:191], a[140:143], a[28:31], v[188:191]\n\t" "s_waitcnt lgkmcnt(4)\n\t"
    "v_mfma_f32_16x16x32_bf16 v[192:195], a[144:147], a[0:3], v[192:195]\n\t" "ds_read_b64_tr_b16 a[128:129], %[vr5] offset:35872\n\t"
    "v_mfma_f32_16x16x32_bf16 v[192:195], a[148:151], a[16:19], v[192:195]\n\t" "ds_read_b64_tr_b16 a[130:131], %[vr5] offset:39456\n\t"
    "v_mfma_f32_16x16x32_bf16 v[196:199], a[144:147], a[4:7], v[196:199]\n\t" "global_load_dwordx4 a[88:91], %[va], s[76:77] offset:2048 sc1\n\t"
    "v_mfma_f32_16x16x32_bf16 v[196:199], a[148:151], a[20:23], v[196:199]\n\t" "v_mfma_f32_16x16x32_bf16 v[200:203], a[144:147], a[8:11], v[200:203]\n\t"
    "ds_read_b64_tr_b16 a[132:133], %[vr5] offset:50208\n\t" "v_mfma_f32_16x16x32_bf16 v[200:203], a[148:151], a[24:27], v[200:203]\n\t" "ds_read_b64_tr_b16 a[134:135], %[vr5] offset:53792\n\t"
    "v_mfma_f32_16x16x32_bf16 v[204:207], a[144:147], a[12:15], v[204:207]\n\t" "v_mfma_f32_16x16x32_bf16 v[204:207], a[148:151], a[28:31], v[204:207]\n\t" "s_waitcnt lgkmcnt(4)\n\t"
    "v_mfma_f32_16x16x32_bf16 v[208:211], a[152:155], a[0:3], v[208:211]\n\t" "ds_read_b64_tr_b16 a[136:137], %[vr7] offset:35840\n\t"
    "v_mfma_f32_16x16x32_bf16 v[208:211], a[156:159], a[16:19], v[208:211]\n\t" "ds_read_b64_tr_b16 a[138:139], %[vr7] offset:39424\n\t" "s_add_u32 m0, %[sl], 59392\n\t"
    "v_mfma_f32_16x16x32_bf16 v[212:215], a[152:155], a[4:7], v[212:215]\n\t" "global_load_lds_dwordx4 %[vb2], s[78:79]\n\t" "v_mfma_f32_16x16x32_bf16 v[212:215], a[156:159], a[20:23], v[212:215]\n\t"
    "v_mfma_f32_16x16x32_bf16 v[216:219], a[152:155], a[8:11], v[216:219]\n\t" "ds_read_b64_tr_b16 a[140:141], %[vr7] offset:50176\n\t"
    "v_mfma_f32_16x16x32_bf16 v[216:219], a[156:159], a[24:27], v[216:219]\n\t" "ds_read_b64_tr_b16 a[142:143], %[vr7] offset:53760\n\t"
    "v_mfma_f32_16x16x32_bf16 v[220:223], a[152:155], a[12:15], v[220:223]\n\t" "global_load_dwordx4 a[92:95], %[va], s[76:77] offset:3072 sc1\n\t"
    "v_mfma_f32_16x16x32_bf16 v[220:223], a[156:159], a[28:31], v[220:223]\n\t" "s_add_u32 s76, s76, 16384\n\t" "s_addc_u32 s77, s77, 0\n\t" "s_waitcnt lgkmcnt(4)\n\t"
    "v_mfma_f32_16x16x32_bf16 v[224:227], a[128:131], a[0:3], v[224:227]\n\t" "s_waitcnt vmcnt(24)\n\t" "s_barrier\n\t" "ds_read_b64_tr_b16 a[144:145], %[vr0] offset:0\n\t"
    "v_mfma_f32_16x16x32_bf16 v[224:227], a[132:135], a[16:19], v[224:227]\n\t" "ds_read_b64_tr_b16 a[146:147], %[vr0] offset:3584\n\t"
    "v_mfma_f32_16x16x32_bf16 v[228:231], a[128:131], a[4:7], v[228:231]\n\t" "v_mfma_f32_16x16x32_bf16 v[228:231], a[132:135], a[20:23], v[228:231]\n\t"
    "v_mfma_f32_16x16x32_bf16 v[232:235], a[128:131], a[8:11], v[232:235]\n\t" "ds_read_b64_tr_b16 a[148:149], %[vr0] offset:14336\n\t"
    "v_mfma_f32_16x16x32_bf16 v[232:235], a[132:135], a[24:27], v[232:235]\n\t" "ds_read_b64_tr_b16 a[150:151], %[vr0] offset:17920\n\t" "s_add_u32 m0, %[sl], 60416\n\t"
    "v_mfma_f32_16x16x32_bf16 v[236:239], a[128:131], a[12:15], v[236:239]\n\t" "s_mov_b64 exec, s[82:83]\n\t" "global_load_lds_dwordx4 %[vb3], s[78:79]\n\t" "s_mov_b64 exec, -1\n\t"
    "v_mfma_f32_16x16x32_bf16 v[236:239], a[132:135], a[28:31], v[236:239]\n\t" "s_add_u32 s78, s78, 229376\n\t" "s_addc_u32 s79, s79, 0\n\t" "s_waitcnt lgkmcnt(4)\n\t"
    "v_mfma_f32_16x16x32_bf16 v[240:243], a[136:139], a[0:3], v[240:243]\n\t" "ds_read_b64_tr_b16 a[152:153], %[vr0] offset:32\n\t"
    "v_mfma_f32_16x16x32_bf16 v[240:243], a[140:143], a[16:19], v[240:243]\n\t" "ds_read_b64_tr_b16 a[154:155], %[vr0] offset:3616\n\t"
    "v_mfma_f32_16x16x32_bf16 v[244:247], a[136:139], a[4:7], v[244:247]\n\t" "v_mfma_f32_16x16x32_bf16 v[244:247], a[140:143], a[20:23], v[244:247]\n\t"
    "v_mfma_f32_16x16x32_bf16 v[248:251], a[136:139], a[8:11], v[248:251]\n\t" "ds_read_b64_tr_b16 a[156:157], %[vr0] offset:14368\n\t"
    "v_mfma_f32_16x16x32_bf16 v[248:251], a[140:143], a[24:27], v[248:251]\n\t" "ds_read_b64_tr_b16 a[158:159], %[vr0] offset:17952\n\t"
    "v_mfma_f32_16x16x32_bf16 v[252:255], a[136:139], a[12:15], v[252:255]\n\t" "v_mfma_f32_16x16x32_bf16 v[252:255], a[140:143], a[28:31], v[252:255]\n\t" "s_waitcnt lgkmcnt(4) vmcnt(18)\n\t"
    "v_mfma_f32_16x16x32_bf16 v[32:35], a[144:147], a[32:35], v[32:35]\n\t" "s_barrier\n\t" "ds_read_b64_tr_b16 a[128:129], %[vr2] offset:0\n\t"
    "v_mfma_f32_16x16x32_bf16 v[32:35], a[148:151], a[48:51], v[32:35]\n\t" "ds_read_b64_tr_b16 a[130:131], %[vr2] offset:3584\n\t" "v_mfma_f32_16x16x32_bf16 v[36:39], a[144:147], a[36:39], v[36:39]\n\t"
    "global_load_dwordx4 a[96:99], %[va], s[76:77] offset:0 sc1\n\t" "v_mfma_f32_16x16x32_bf16 v[36:39], a[148:151], a[52:55], v[36:39]\n\t"
    "v_mfma_f32_16x16x32_bf16 v[40:43], a[144:147], a[40:43], v[40:43]\n\t" "ds_read_b64_tr_b16 a[132:133], %[vr2] offset:14336\n\t" "v_mfma_f32_16x16x32_bf16 v[40:43], a[148:151], a[56:59], v[40:43]\n\t"
    "ds_read_b64_tr_b16 a[134:135], %[vr2] offset:17920\n\t" "s_add_u32 m0, %[sl], 71680\n\t" "v_mfma_f32_16x16x32_bf16 v[44:47], a[144:147], a[44:47], v[44:47]\n\t"
    "global_load_lds_dwordx4 %[vb0], s[78:79]\n\t" "v_mfma_f32_16x16x32_bf16 v[44:47], a[148:151], a[60:63], v[44:47]\n\t" "s_waitcnt lgkmcnt(4)\n\t"
    "v_mfma_f32_16x16x32_bf16 v[48:51], a[152:155], a[32:35], v[48:51]\n\t" "ds_read_b64_tr_b16 a[136:137], %[vr2] offset:32\n\t" "v_mfma_f32_16x16x32_bf16 v[48:51], a[156:159], a[48:51], v[48:51]\n\t"
    "ds_read_b64_tr_b16 a[138:139], %[vr2] offset:3616\n\t" "v_mfma_f32_16x16x32_bf16 v[52:55], a[152:155], a[36:39], v[52:55]\n\t" "v_mfma_f32_16x16x32_bf16 v[52:55], a[156:159], a[52:55], v[52:55]\n\t"
    "v_mfma_f32_16x16x32_bf16 v[56:59], a[152:155], a[40:43], v[56:59]\n\t" "ds_read_b64_tr_b16 a[140:141], %[vr2] offset:14368\n\t" "v_mfma_f32_16x16x32_bf16 v[56:59], a[156:159], a[56:59], v[56:59]\n\t"
    "ds_read_b64_tr_b16 a[142:143], %[vr2] offset:17952\n\t" "v_mfma_f32_16x16x32_bf16 v[60:63], a[152:155], a[44:47], v[60:63]\n\t" "global_load_dwordx4 a[100:103], %[va], s[76:77] offset:1024 sc1\n\t"
    "v_mfma_f32_16x16x32_bf16 v[60:63], a[156:159], a[60:63], v[60:63]\n\t" "s_waitcnt lgkmcnt(4)\n\t" "v_mfma_f32_16x16x32_bf16 v[64:67], a[128:131], a[32:35], v[64:67]\n\t"
    "ds_read_b64_tr_b16 a[144:145], %[vr4] offset:0\n\t" "v_mfma_f32_16x16x32_bf16 v[64:67], a[132:135], a[48:51], v[64:67]\n\t" "ds_read_b64_tr_b16 a[146:147], %[vr4] offset:3584\n\t"
    "s_add_u32 m0, %[sl], 72704\n\t" "v_mfma_f32_16x16x32_bf16 v[68:71], a[128:131], a[36:39], v[68:71]\n\t" "global_load_lds_dwordx4 %[vb1], s[78:79]\n\t"
    "v_mfma_f32_16x16x32_bf16 v[68:71], a[132:135], a[52:55], v[68:71]\n\t" "v_mfma_f32_16x16x32_bf16 v[72:75], a[128:131], a[40:43], v[72:75]\n\t" "ds_read_b64_tr_b16 a[148:149], %[vr4] offset:14336\n\t"
    "v_mfma_f32_16x16x32_bf16 v[72:75], a[132:135], a[56:59], v[72:75]\n\t" "ds_read_b64_tr_b16 a[150:151], %[vr4] offset:17920\n\t" "v_mfma_f32_16x16x32_bf16 v[76:79], a[128:131], a[44:47], v[76:79]\n\t"
    "v_mfma_f32_16x16x32_bf16 v[76:79], a[132:135], a[60:63], v[76:79]\n\t" "s_waitcnt lgkmcnt(4)\n\t" "v_mfma_f32_16x16x32_bf16 v[80:83], a[136:139], a[32:35], v[80:83]\n\t"
    "ds_read_b64_tr_b16 a[152:153], %[vr4] offset:32\n\t" "v_mfma_f32_16x16x32_bf16 v[80:83], a[140:143], a[48:51], v[80:83]\n\t" "ds_read_b64_tr_b16 a[154:155], %[vr4] offset:3616\n\t"
    "v_mfma_f32_16x16x32_bf16 v[84:87], a[136:139], a[36:39], v[84:87]\n\t" "global_load_dwordx4 a[104:107], %[va], s[76:77] offset:2048 sc1\n\t"
    "v_mfma_f32_16x16x32_bf16 v[84:87], a[140:143], a[52:55], v[84:87]\n\t" "v_mfma_f32_16x16x32_bf16 v[88:91], a[136:139], a[40:43], v[88:91]\n\t" "ds_read_b64_tr_b16 a[156:157], %[vr4] offset:14368\n\t"
    "v_mfma_f32_16x16x32_bf16 v[88:91], a[140:143], a[56:59], v[88:91]\n\t" "ds_read_b64_tr_b16 a[158:159], %[vr4] offset:17952\n\t" "v_mfma_f32_16x16x32_bf16 v[92:95], a[136:139], a[44:47], v[92:95]\n\t"
    "v_mfma_f32_16x16x32_bf16 v[92:95], a[140:143], a[60:63], v[92:95]\n\t" "s_waitcnt lgkmcnt(4)\n\t" "v_mfma_f32_16x16x32_bf16 v[96:99], a[144:147], a[32:35], v[96:99]\n\t"
    "ds_read_b64_tr_b16 a[128:129], %[vr6] offset:0\n\t" "v_mfma_f32_16x16x32_bf16 v[96:99], a[148:151], a[48:51], v[96:99]\n\t" "ds_read_b64_tr_b16 a[130:131], %[vr6] offset:3584\n\t"
    "s_add_u32 m0, %[sl], 73728\n\t" "v_mfma_f32_16x16x32_bf16 v[100:103], a[144:147], a[36:39], v[100:103]\n\t" "global_load_lds_dwordx4 %[vb2], s[78:79]\n\t"
    "v_mfma_f32_16x16x32_bf16 v[100:103], a[148:151], a[52:55], v[100:103]\n\t" "v_mfma_f32_16x16x32_bf16 v[104:107], a[144:147], a[40:43], v[104:107]\n\t"
    "ds_read_b64_tr_b16 a[132:133], %[vr6] offset:14336\n\t" "v_mfma_f32_16x16x32_bf16 v[104:107], a[148:151], a[56:59], v[104:107]\n\t" "ds_read_b64_tr_b16 a[134:135], %[vr6] offset:17920\n\t"
    "v_mfma_f32_16x16x32_bf16 v[108:111], a[144:147], a[44:47], v[108:111]\n\t" "global_load_dwordx4 a[108:111], %[va], s[76:77] offset:3072 sc1\n\t"
    "v_mfma_f32_16x16x32_bf16 v[108:111], a[148:151], a[60:63], v[108:111]\n\t" "s_add_u32 s76, s76, 16384\n\t" "s_addc_u32 s77, s77, 0\n\t" "s_waitcnt lgkmcnt(4)\n\t"
    "v_mfma_f32_16x16x32_bf16 v[112:115], a[152:155], a[32:35], v[112:115]\n\t" "ds_read_b64_tr_b16 a[136:137], %[vr0] offset:7168\n\t"
    "v_mfma_f32_16x16x32_bf16 v[112:115], a[156:159], a[48:51], v[112:115]\n\t" "ds_read_b64_tr_b16 a[138:139], %[vr0] offset:10752\n\t"
    "v_mfma_f32_16x16x32_bf16 v[116:119], a[152:155], a[36:39], v[116:119]\n\t" "v_mfma_f32_16x16x32_bf16 v[116:119], a[156:159], a[52:55], v[116:119]\n\t"
    "v_mfma_f32_16x16x32_bf16 v[120:123], a[152:155], a[40:43], v[120:123]\n\t" "ds_read_b64_tr_b16 a[140:141], %[vr0] offset:21504\n\t"
    "v_mfma_f32_16x16x32_bf16 v[120:123], a[156:159], a[56:59], v[120:123]\n\t" "ds_read_b64_tr_b16 a[142:143], %[vr0] offset:25088\n\t" "s_add_u32 m0, %[sl], 74752\n\t"
    "v_mfma_f32_16x16x32_bf16 v[124:127], a[152:155], a[44:47], v[124:127]\n\t" "s_mov_b64 exec, s[82:83]\n\t" "global_load_lds_dwordx4 %[vb3], s[78:79]\n\t" "s_mov_b64 exec, -1\n\t"
    "v_mfma_f32_16x16x32_bf16 v[124:127], a[156:159], a[60:63], v[124:127]\n\t" "s_add_u32 s78, s78, s88\n\t" "s_addc_u32 s79, s79, s89\n\t" "s_waitcnt lgkmcnt(4)\n\t"
    "v_mfma_f32_16x16x32_bf16 v[128:131], a[128:131], a[32:35], v[128:131]\n\t" "ds_read_b64_tr_b16 a[144:145], %[vr0] offset:7200\n\t"
    "v_mfma_f32_16x16x32_bf16 v[128:131], a[132:135], a[48:51], v[128:131]\n\t" "ds_read_b64_tr_b16 a[146:147], %[vr0] offset:10784\n\t"
    "v_mfma_f32_16x16x32_bf16 v[132:135], a[128:131], a[36:39], v[132:135]\n\t" "v_mfma_f32_16x16x32_bf16 v[132:135], a[132:135], a[52:55], v[132:135]\n\t"
    "v_mfma_f32_16x16x32_bf16 v[136:139], a[128:131], a[40:43], v[136:139]\n\t" "ds_read_b64_tr_b16 a[148:149], %[vr0] offset:21536\n\t"
    "v_mfma_f32_16x16x32_bf16 v[136:139], a[132:135], a[56:59], v[136:139]\n\t" "ds_read_b64_tr_b16 a[150:151], %[vr0] offset:25120\n\t"
    "v_mfma_f32_16x16x32_bf16 v[140:143], a[128:131], a[44:47], v[140:143]\n\t" "v_mfma_f32_16x16x32_bf16 v[140:143], a[132:135], a[60:63], v[140:143]\n\t" "s_waitcnt lgkmcnt(4)\n\t"
    "v_mfma_f32_16x16x32_bf16 v[144:147], a[136:139], a[32:35], v[144:147]\n\t" "ds_read_b64_tr_b16 a[152:153], %[vr2] offset:7168\n\t"
    "v_mfma_f32_16x16x32_bf16 v[144:147], a[140:143], a[48:51], v[144:147]\n\t" "ds_read_b64_tr_b16 a[154:155], %[vr2] offset:10752\n\t"
    "v_mfma_f32_16x16x32_bf16 v[148:151], a[136:139], a[36:39], v[148:151]\n\t" "global_load_dwordx4 a[112:115], %[va], s[76:77] offset:0 sc1\n\t"
    "v_mfma_f32_16x16x32_bf16 v[148:151], a[140:143], a[52:55], v[148:151]\n\t" "v_mfma_f32_16x16x32_bf16 v[152:155], a[136:139], a[40:43], v[152:155]\n\t"
    "ds_read_b64_tr_b16 a[156:157], %[vr2] offset:21504\n\t" "v_mfma_f32_16x16x32_bf16 v[152:155], a[140:143], a[56:59], v[152:155]\n\t" "ds_read_b64_tr_b16 a[158:159], %[vr2] offset:25088\n\t"
    "s_add_u32 m0, %[sl], 86016\n\t" "v_mfma_f32_16x16x32_bf16 v[156:159], a[136:139], a[44:47], v[156:159]\n\t" "global_load_lds_dwordx4 %[vb0], s[78:79]\n\t"
    "v_mfma_f32_16x16x32_bf16 v[156:159], a[140:143], a[60:63], v[156:159]\n\t" "s_waitcnt lgkmcnt(4)\n\t" "v_mfma_f32_16x16x32_bf16 v[160:163], a[144:147], a[32:35], v[160:163]\n\t"
    "ds_read_b64_tr_b16 a[128:129], %[vr2] offset:7200\n\t" "v_mfma_f32_16x16x32_bf16 v[160:163], a[148:151], a[48:51], v[160:163]\n\t" "ds_read_b64_tr_b16 a[130:131], %[vr2] offset:10784\n\t"
    "v_mfma_f32_16x16x32_bf16 v[164:167], a[144:147], a[36:39], v[164:167]\n\t" "v_mfma_f32_16x16x32_bf16 v[164:167], a[148:151], a[52:55], v[164:167]\n\t"
    "v_mfma_f32_16x16x32_bf16 v[168:171], a[144:147], a[40:43], v[168:171]\n\t" "ds_read_b64_tr_b16 a[132:133], %[vr2] offset:21536\n\t"
    "v_mfma_f32_16x16x32_bf16 v[168:171], a[148:151], a[56:59], v[168:171]\n\t" "ds_read_b64_tr_b16 a[134:135], %[vr2] offset:25120\n\t"
    "v_mfma_f32_16x16x32_bf16 v[172:175], a[144:147], a[44:47], v[172:175]\n\t" "global_load_dwordx4 a[116:119], %[va], s[76:77] offset:1024 sc1\n\t"
    "v_mfma_f32_16x16x32_bf16 v[172:175], a[148:151], a[60:63], v[172:175]\n\t" "s_waitcnt lgkmcnt(4)\n\t" "v_mfma_f32_16x16x32_bf16 v[176:179], a[152:155], a[32:35], v[176:179]\n\t"
    "ds_read_b64_tr_b16 a[136:137], %[vr4] offset:7168\n\t" "v_mfma_f32_16x16x32_bf16 v[176:179], a[156:159], a[48:51], v[176:179]\n\t" "ds_read_b64_tr_b16 a[138:139], %[vr4] offset:10752\n\t"
    "s_add_u32 m0, %[sl], 87040\n\t" "v_mfma_f32_16x16x32_bf16 v[180:183], a[152:155], a[36:39], v[180:183]\n\t" "global_load_lds_dwordx4 %[vb1], s[78:79]\n\t"
    "v_mfma_f32_16x16x32_bf16 v[180:183], a[156:159], a[52:55], v[180:183]\n\t" "v_mfma_f32_16x16x32_bf16 v[184:187], a[152:155], a[40:43], v[184:187]\n\t"
    "ds_read_b64_tr_b16 a[140:141], %[vr4] offset:21504\n\t" "v_mfma_f32_16x16x32_bf16 v[184:187], a[156:159], a[56:59], v[184:187]\n\t" "ds_read_b64_tr_b16 a[142:143], %[vr4] offset:25088\n\t"
    "v_mfma_f32_16x16x32_bf16 v[188:191], a[152:155], a[44:47], v[188:191]\n\t" "v_mfma_f32_16x16x32_bf16 v[188:191], a[156:159], a[60:63], v[188:191]\n\t" "s_waitcnt lgkmcnt(4)\n\t"
    "v_mfma_f32_16x16x32_bf16 v[192:195], a[128:131], a[32:35], v[192:195]\n\t" "ds_read_b64_tr_b16 a[144:145], %[vr4] offset:7200\n\t"
    "v_mfma_f32_16x16x32_bf16 v[192:195], a[132:135], a[48:51], v[192:195]\n\t" "ds_read_b64_tr_b16 a[146:147], %[vr4] offset:10784\n\t"
    "v_mfma_f32_16x16x32_bf16 v[196:199], a[128:131], a[36:39], v[196:199]\n\t" "global_load_dwordx4 a[120:123], %[va], s[76:77] offset:2048 sc1\n\t"
    "v_mfma_f32_16x16x32_bf16 v[196:199], a[132:135], a[52:55], v[196:199]\n\t" "v_mfma_f32_16x16x32_bf16 v[200:203], a[128:131], a[40:43], v[200:203]\n\t"
    "ds_read_b64_tr_b16 a[148:149], %[vr4] offset:21536\n\t" "v_mfma_f32_16x16x32_bf16 v[200:203], a[132:135], a[56:59], v[200:203]\n\t" "ds_read_b64_tr_b16 a[150:151], %[vr4] offset:25120\n\t"
    "v_mfma_f32_16x16x32_bf16 v[204:207], a[128:131], a[44:47], v[204:207]\n\t" "v_mfma_f32_16x16x32_bf16 v[204:207], a[132:135], a[60:63], v[204:207]\n\t" "s_waitcnt lgkmcnt(4)\n\t"
    "v_mfma_f32_16x16x32_bf16 v[208:211], a[136:139], a[32:35], v[208:211]\n\t" "ds_read_b64_tr_b16 a[152:153], %[vr6] offset:7168\n\t"
    "v_mfma_f32_16x16x32_bf16 v[208:211], a[140:143], a[48:51], v[208:211]\n\t" "ds_read_b64_tr_b16 a[154:155], %[vr6] offset:10752\n\t" "s_add_u32 m0, %[sl], 88064\n\t"
    "v_mfma_f32_16x16x32_bf16 v[212:215], a[136:139], a[36:39], v[212:215]\n\t" "global_load_lds_dwordx4 %[vb2], s[78:79]\n\t" "v_mfma_f32_16x16x32_bf16 v[212:215], a[140:143], a[52:55], v[212:215]\n\t"
    "v_mfma_f32_16x16x32_bf16 v[216:219], a[136:139], a[40:43], v[216:219]\n\t" "ds_read_b64_tr_b16 a[156:157], %[vr6] offset:21504\n\t"
    "v_mfma_f32_16x16x32_bf16 v[216:219], a[140:143], a[56:59], v[216:219]\n\t" "ds_read_b64_tr_b16 a[158:159], %[vr6] offset:25088\n\t"
    "v_mfma_f32_16x16x32_bf16 v[220:223], a[136:139], a[44:47], v[220:223]\n\t" "global_load_dwordx4 a[124:127], %[va], s[76:77] offset:3072 sc1\n\t"
    "v_mfma_f32_16x16x32_bf16 v[220:223], a[140:143], a[60:63], v[220:223]\n\t" "s_add_u32 s76, s76, s86\n\t" "s_addc_u32 s77, s77, s87\n\t" "s_waitcnt lgkmcnt(4)\n\t"
    "v_mfma_f32_16x16x32_bf16 v[224:227], a[144:147], a[32:35], v[224:227]\n\t" "s_waitcnt vmcnt(24)\n\t" "s_barrier\n\t" "ds_read_b64_tr_b16 a[128:129], %[vr0] offset:28672\n\t"
    "v_mfma_f32_16x16x32_bf16 v[224:227], a[148:151], a[48:51], v[224:227]\n\t" "ds_read_b64_tr_b16 a[130:131], %[vr0] offset:32256\n\t"
    "v_mfma_f32_16x16x32_bf16 v[228:231], a[144:147], a[36:39], v[228:231]\n\t" "v_mfma_f32_16x16x32_bf16 v[228:231], a[148:151], a[52:55], v[228:231]\n\t"
    "v_mfma_f32_16x16x32_bf16 v[232:235], a[144:147], a[40:43], v[232:235]\n\t" "ds_read_b64_tr_b16 a[132:133], %[vr0] offset:43008\n\t"
    "v_mfma_f32_16x16x32_bf16 v[232:235], a[148:151], a[56:59], v[232:235]\n\t" "ds_read_b64_tr_b16 a[134:135], %[vr0] offset:46592\n\t" "s_add_u32 m0, %[sl], 89088\n\t"
    "v_mfma_f32_16x16x32_bf16 v[236:239], a[144:147], a[44:47], v[236:239]\n\t" "s_mov_b64 exec, s[82:83]\n\t" "global_load_lds_dwordx4 %[vb3], s[78:79]\n\t" "s_mov_b64 exec, -1\n\t"
    "v_mfma_f32_16x16x32_bf16 v[236:239], a[148:151], a[60:63], v[236:239]\n\t" "s_add_u32 s78, s78, 229376\n\t" "s_addc_u32 s79, s79, 0\n\t" "s_waitcnt lgkmcnt(4)\n\t"
    "v_mfma_f32_16x16x32_bf16 v[240:243], a[152:155], a[32:35], v[240:243]\n\t" "ds_read_b64_tr_b16 a[136:137], %[vr0] offset:28704\n\t"
    "v_mfma_f32_16x16x32_bf16 v[240:243], a[156:159], a[48:51], v[240:243]\n\t" "ds_read_b64_tr_b16 a[138:139], %[vr0] offset:32288\n\t"
    "v_mfma_f32_16x16x32_bf16 v[244:247], a[152:155], a[36:39], v[244:247]\n\t" "v_mfma_f32_16x16x32_bf16 v[244:247], a[156:159], a[52:55], v[244:247]\n\t"
    "v_mfma_f32_16x16x32_bf16 v[248:251], a[152:155], a[40:43], v[248:251]\n\t" "ds_read_b64_tr_b16 a[140:141], %[vr0] offset:43040\n\t"
    "v_mfma_f32_16x16x32_bf16 v[248:251], a[156:159], a[56:59], v[248:251]\n\t" "ds_read_b64_tr_b16 a[142:143], %[vr0] offset:46624\n\t"
    "v_mfma_f32_16x16x32_bf16 v[252:255], a[152:155], a[44:47], v[252:255]\n\t" "v_mfma_f32_16x16x32_bf16 v[252:255], a[156:159], a[60:63], v[252:255]\n\t" ".Ld0_%=:\n\t" "s_add_u32 s80, s80, 1\n\t"
    "s_cmp_lt_u32 s80, 2\n\t" "s_cbranch_scc1 .Lloop_%=\n\t" "s_branch .Ltail_%=\n\t" ".LE0_%=:\n\t" "s_waitcnt lgkmcnt(4) vmcnt(17)\n\t"
    "v_mfma_f32_16x16x32_bf16 v[32:35], a[128:131], a[0:3], v[32:35]\n\t" "s_barrier\n\t" "ds_read_b64_tr_b16 a[144:145], %[vr3] offset:28672\n\t"
    "v_mfma_f32_16x16x32_bf16 v[32:35], a[132:135], a[16:19], v[32:35]\n\t" "ds_read_b64_tr_b16 a[146:147], %[vr3] offset:32256\n\t" "v_mfma_f32_16x16x32_bf16 a[160:163], a[128:131], a[184:187], 0\n\t"
    "v_mfma_f32_16x16x32_bf16 v[36:39], a[128:131], a[4:7], v[36:39]\n\t" "global_load_dwordx4 a[64:67], %[va], s[76:77] offset:0 sc1\n\t"
    "v_mfma_f32_16x16x32_bf16 v[36:39], a[132:135], a[20:23], v[36:39]\n\t" "v_mfma_f32_16x16x32_bf16 v[40:43], a[128:131], a[8:11], v[40:43]\n\t" "ds_read_b64_tr_b16 a[148:149], %[vr3] offset:43008\n\t"
    "v_mfma_f32_16x16x32_bf16 v[40:43], a[132:135], a[24:27], v[40:43]\n\t" "ds_read_b64_tr_b16 a[150:151], %[vr3] offset:46592\n\t"
    "v_mfma_f32_16x16x32_bf16 a[160:163], a[132:135], a[188:191], a[160:163]\n\t" "s_add_u32 m0, %[sl], 43008\n\t" "v_mfma_f32_16x16x32_bf16 v[44:47], a[128:131], a[12:15], v[44:47]\n\t"
    "global_load_lds_dwordx4 %[vb0], s[78:79]\n\t" "v_mfma_f32_16x16x32_bf16 v[44:47], a[132:135], a[28:31], v[44:47]\n\t" "s_waitcnt lgkmcnt(4)\n\t"
    "v_mfma_f32_16x16x32_bf16 v[48:51], a[136:139], a[0:3], v[48:51]\n\t" "ds_read_b64_tr_b16 a[152:153], %[vr3] offset:28704\n\t" "v_mfma_f32_16x16x32_bf16 v[48:51], a[140:143], a[16:19], v[48:51]\n\t"
    "ds_read_b64_tr_b16 a[154:155], %[vr3] offset:32288\n\t" "v_mfma_f32_16x16x32_bf16 a[164:167], a[136:139], a[184:187], 0\n\t" "v_mfma_f32_16x16x32_bf16 v[52:55], a[136:139], a[4:7], v[52:55]\n\t"
    "v_mfma_f32_16x16x32_bf16 v[52:55], a[140:143], a[20:23], v[52:55]\n\t" "v_mfma_f32_16x16x32_bf16 v[56:59], a[136:139], a[8:11], v[56:59]\n\t" "ds_read_b64_tr_b16 a[156:157], %[vr3] offset:43040\n\t"
    "v_mfma_f32_16x16x32_bf16 v[56:59], a[140:143], a[24:27], v[56:59]\n\t" "ds_read_b64_tr_b16 a[158:159], %[vr3] offset:46624\n\t"
    "v_mfma_f32_16x16x32_bf16 a[164:167], a[140:143], a[188:191], a[164:167]\n\t" "v_mfma_f32_16x16x32_bf16 v[60:63], a[136:139], a[12:15], v[60:63]\n\t"
    "global_load_dwordx4 a[68:71], %[va], s[76:77] offset:1024 sc1\n\t" "v_mfma_f32_16x16x32_bf16 v[60:63], a[140:143], a[28:31], v[60:63]\n\t" "s_waitcnt lgkmcnt(4)\n\t"
    "v_mfma_f32_16x16x32_bf16 v[64:67], a[144:147], a[0:3], v[64:67]\n\t" "ds_read_b64_tr_b16 a[128:129], %[vr5] offset:28672\n\t" "v_mfma_f32_16x16x32_bf16 v[64:67], a[148:151], a[16:19], v[64:67]\n\t"
    "ds_read_b64_tr_b16 a[130:131], %[vr5] offset:32256\n\t" "s_add_u32 m0, %[sl], 44032\n\t" "v_mfma_f32_16x16x32_bf16 v[68:71], a[144:147], a[4:7], v[68:71]\n\t"
    "global_load_lds_dwordx4 %[vb1], s[78:79]\n\t" "v_mfma_f32_16x16x32_bf16 v[68:71], a[148:151], a[20:23], v[68:71]\n\t" "v_mfma_f32_16x16x32_bf16 v[72:75], a[144:147], a[8:11], v[72:75]\n\t"
    "ds_read_b64_tr_b16 a[132:133], %[vr5] offset:43008\n\t" "v_mfma_f32_16x16x32_bf16 v[72:75], a[148:151], a[24:27], v[72:75]\n\t" "ds_read_b64_tr_b16 a[134:135], %[vr5] offset:46592\n\t"
    "v_mfma_f32_16x16x32_bf16 v[76:79], a[144:147], a[12:15], v[76:79]\n\t" "v_mfma_f32_16x16x32_bf16 v[76:79], a[148:151], a[28:31], v[76:79]\n\t" "s_waitcnt lgkmcnt(4)\n\t"
    "v_mfma_f32_16x16x32_bf16 v[80:83], a[152:155], a[0:3], v[80:83]\n\t" "ds_read_b64_tr_b16 a[136:137], %[vr5] offset:28704\n\t" "v_mfma_f32_16x16x32_bf16 v[80:83], a[156:159], a[16:19], v[80:83]\n\t"
    "ds_read_b64_tr_b16 a[138:139], %[vr5] offset:32288\n\t" "v_mfma_f32_16x16x32_bf16 v[84:87], a[152:155], a[4:7], v[84:87]\n\t" "global_load_dwordx4 a[72:75], %[va], s[76:77] offset:2048 sc1\n\t"
    "v_mfma_f32_16x16x32_bf16 v[84:87], a[156:159], a[20:23], v[84:87]\n\t" "v_mfma_f32_16x16x32_bf16 v[88:91], a[152:155], a[8:11], v[88:91]\n\t" "ds_read_b64_tr_b16 a[140:141], %[vr5] offset:43040\n\t"
    "v_mfma_f32_16x16x32_bf16 v[88:91], a[156:159], a[24:27], v[88:91]\n\t" "ds_read_b64_tr_b16 a[142:143], %[vr5] offset:46624\n\t" "v_mfma_f32_16x16x32_bf16 v[92:95], a[152:155], a[12:15], v[92:95]\n\t"
    "v_mfma_f32_16x16x32_bf16 v[92:95], a[156:159], a[28:31], v[92:95]\n\t" "s_waitcnt lgkmcnt(4)\n\t" "v_mfma_f32_16x16x32_bf16 v[96:99], a[128:131], a[0:3], v[96:99]\n\t"
    "ds_read_b64_tr_b16 a[144:145], %[vr7] offset:28672\n\t" "v_mfma_f32_16x16x32_bf16 v[96:99], a[132:135], a[16:19], v[96:99]\n\t" "ds_read_b64_tr_b16 a[146:147], %[vr7] offset:32256\n\t"
    "s_add_u32 m0, %[sl], 45056\n\t" "v_mfma_f32_16x16x32_bf16 v[100:103], a[128:131], a[4:7], v[100:103]\n\t" "global_load_lds_dwordx4 %[vb2], s[78:79]\n\t"
    "v_mfma_f32_16x16x32_bf16 v[100:103], a[132:135], a[20:23], v[100:103]\n\t" "v_mfma_f32_16x16x32_bf16 v[104:107], a[128:131], a[8:11], v[104:107]\n\t"
    "ds_read_b64_tr_b16 a[148:149], %[vr7] offset:43008\n\t" "v_mfma_f32_16x16x32_bf16 v[104:107], a[132:135], a[24:27], v[104:107]\n\t" "ds_read_b64_tr_b16 a[150:151], %[vr7] offset:46592\n\t"
    "v_mfma_f32_16x16x32_bf16 v[108:111], a[128:131], a[12:15], v[108:111]\n\t" "global_load_dwordx4 a[76:79], %[va], s[76:77] offset:3072 sc1\n\t"
    "v_mfma_f32_16x16x32_bf16 v[108:111], a[132:135], a[28:31], v[108:111]\n\t" "s_add_u32 s76, s76, 16384\n\t" "s_addc_u32 s77, s77, 0\n\t" "s_waitcnt lgkmcnt(4)\n\t"
    "v_mfma_f32_16x16x32_bf16 v[112:115], a[136:139], a[0:3], v[112:115]\n\t" "ds_read_b64_tr_b16 a[152:153], %[vr1] offset:35840\n\t"
    "v_mfma_f32_16x16x32_bf16 v[112:115], a[140:143], a[16:19], v[112:115]\n\t" "ds_read_b64_tr_b16 a[154:155], %[vr1] offset:39424\n\t"
    "v_mfma_f32_16x16x32_bf16 v[116:119], a[136:139], a[4:7], v[116:119]\n\t" "v_mfma_f32_16x16x32_bf16 v[116:119], a[140:143], a[20:23], v[116:119]\n\t"
    "v_mfma_f32_16x16x32_bf16 v[120:123], a[136:139], a[8:11], v[120:123]\n\t" "ds_read_b64_tr_b16 a[156:157], %[vr1] offset:50176\n\t"
    "v_mfma_f32_16x16x32_bf16 v[120:123], a[140:143], a[24:27], v[120:123]\n\t" "ds_read_b64_tr_b16 a[158:159], %[vr1] offset:53760\n\t" "s_add_u32 m0, %[sl], 46080\n\t"
    "v_mfma_f32_16x16x32_bf16 v[124:127], a[136:139], a[12:15], v[124:127]\n\t" "s_mov_b64 exec, s[82:83]\n\t" "global_load_lds_dwordx4 %[vb3], s[78:79]\n\t" "s_mov_b64 exec, -1\n\t"
    "global_load_dword v29, %[va], %[cd]\n\t" "v_mfma_f32_16x16x32_bf16 v[124:127], a[140:143], a[28:31], v[124:127]\n\t" "s_add_u32 s78, s78, 229376\n\t" "s_addc_u32 s79, s79, 0\n\t"
    "s_waitcnt lgkmcnt(4)\n\t" "v_mfma_f32_16x16x32_bf16 v[128:131], a[144:147], a[0:3], v[128:131]\n\t" "ds_read_b64_tr_b16 a[128:129], %[vr1] offset:35872\n\t"
    "v_mfma_f32_16x16x32_bf16 v[128:131], a[148:151], a[16:19], v[128:131]\n\t" "ds_read_b64_tr_b16 a[130:131], %[vr1] offset:39456\n\t"
    "v_mfma_f32_16x16x32_bf16 a[168:171], a[144:147], a[184:187], 0\n\t" "v_mfma_f32_16x16x32_bf16 v[132:135], a[144:147], a[4:7], v[132:135]\n\t"
    "v_mfma_f32_16x16x32_bf16 v[132:135], a[148:151], a[20:23], v[132:135]\n\t" "v_mfma_f32_16x16x32_bf16 v[136:139], a[144:147], a[8:11], v[136:139]\n\t"
    "ds_read_b64_tr_b16 a[132:133], %[vr1] offset:50208\n\t" "v_mfma_f32_16x16x32_bf16 v[136:139], a[148:151], a[24:27], v[136:139]\n\t" "ds_read_b64_tr_b16 a[134:135], %[vr1] offset:53792\n\t"
    "v_mfma_f32_16x16x32_bf16 a[168:171], a[148:151], a[188:191], a[168:171]\n\t" "v_mfma_f32_16x16x32_bf16 v[140:143], a[144:147], a[12:15], v[140:143]\n\t"
    "v_mfma_f32_16x16x32_bf16 v[140:143], a[148:151], a[28:31], v[140:143]\n\t" "s_waitcnt lgkmcnt(4)\n\t" "v_mfma_f32_16x16x32_bf16 v[144:147], a[152:155], a[0:3], v[144:147]\n\t"
    "ds_read_b64_tr_b16 a[136:137], %[vr3] offset:35840\n\t" "v_mfma_f32_16x16x32_bf16 v[144:147], a[156:159], a[16:19], v[144:147]\n\t" "ds_read_b64_tr_b16 a[138:139], %[vr3] offset:39424\n\t"
    "v_mfma_f32_16x16x32_bf16 a[172:175], a[152:155], a[184:187], 0\n\t" "v_mfma_f32_16x16x32_bf16 v[148:151], a[152:155], a[4:7], v[148:151]\n\t"
    "global_load_dwordx4 a[80:83], %[va], s[76:77] offset:0 sc1\n\t" "v_mfma_f32_16x16x32_bf16 v[148:151], a[156:159], a[20:23], v[148:151]\n\t"
    "v_mfma_f32_16x16x32_bf16 v[152:155], a[152:155], a[8:11], v[152:155]\n\t" "ds_read_b64_tr_b16 a[140:141], %[vr3] offset:50176\n\t"
    "v_mfma_f32_16x16x32_bf16 v[152:155], a[156:159], a[24:27], v[152:155]\n\t" "ds_read_b64_tr_b16 a[142:143], %[vr3] offset:53760\n\t"
    "v_mfma_f32_16x16x32_bf16 a[172:175], a[156:159], a[188:191], a[172:175]\n\t" "s_add_u32 m0, %[sl], 57344\n\t" "v_mfma_f32_16x16x32_bf16 v[156:159], a[152:155], a[12:15], v[156:159]\n\t"
    "global_load_lds_dwordx4 %[vb0], s[78:79]\n\t" "v_mfma_f32_16x16x32_bf16 v[156:159], a[156:159], a[28:31], v[156:159]\n\t" "s_waitcnt lgkmcnt(4)\n\t"
    "v_mfma_f32_16x16x32_bf16 v[160:163], a[128:131], a[0:3], v[160:163]\n\t" "ds_read_b64_tr_b16 a[144:145], %[vr3] offset:35872\n\t"
    "v_mfma_f32_16x16x32_bf16 v[160:163], a[132:135], a[16:19], v[160:163]\n\t" "ds_read_b64_tr_b16 a[146:147], %[vr3] offset:39456\n\t"
    "v_mfma_f32_16x16x32_bf16 a[176:179], a[128:131], a[184:187], 0\n\t" "v_mfma_f32_16x16x32_bf16 v[164:167], a[128:131], a[4:7], v[164:167]\n\t"
    "v_mfma_f32_16x16x32_bf16 v[164:167], a[132:135], a[20:23], v[164:167]\n\t" "v_mfma_f32_16x16x32_bf16 v[168:171], a[128:131], a[8:11], v[168:171]\n\t"
    "ds_read_b64_tr_b16 a[148:149], %[vr3] offset:50208\n\t" "v_mfma_f32_16x16x32_bf16 v[168:171], a[132:135], a[24:27], v[168:171]\n\t" "ds_read_b64_tr_b16 a[150:151], %[vr3] offset:53792\n\t"
    "v_mfma_f32_16x16x32_bf16 a[176:179], a[132:135], a[188:191], a[176:179]\n\t" "v_mfma_f32_16x16x32_bf16 v[172:175], a[128:131], a[12:15], v[172:175]\n\t"
    "global_load_dwordx4 a[84:87], %[va], s[76:77] offset:1024 sc1\n\t" "v_mfma_f32_16x16x32_bf16 v[172:175], a[132:135], a[28:31], v[172:175]\n\t" "s_waitcnt lgkmcnt(4)\n\t"
    "v_mfma_f32_16x16x32_bf16 v[176:179], a[136:139], a[0:3], v[176:179]\n\t" "ds_read_b64_tr_b16 a[152:153], %[vr5] offset:35840\n\t"
    "v_mfma_f32_16x16x32_bf16 v[176:179], a[140:143], a[16:19], v[176:179]\n\t" "ds_read_b64_tr_b16 a[154:155], %[vr5] offset:39424\n\t" "s_add_u32 m0, %[sl], 58368\n\t"
    "v_mfma_f32_16x16x32_bf16 v[180:183], a[136:139], a[4:7], v[180:183]\n\t" "global_load_lds_dwordx4 %[vb1], s[78:79]\n\t" "v_mfma_f32_16x16x32_bf16 v[180:183], a[140:143], a[20:23], v[180:183]\n\t"
    "v_mfma_f32_16x16x32_bf16 v[184:187], a[136:139], a[8:11], v[184:187]\n\t" "ds_read_b64_tr_b16 a[156:157], %[vr5] offset:50176\n\t"
    "v_mfma_f32_16x16x32_bf16 v[184:187], a[140:143], a[24:27], v[184:187]\n\t" "ds_read_b64_tr_b16 a[158:159], %[vr5] offset:53760\n\t"
    "v_mfma_f32_16x16x32_bf16 v[188:191], a[136:139], a[12:15], v[188:191]\n\t" "v_mfma_f32_16x16x32_bf16 v[188:191], a[140:143], a[28:31], v[188:191]\n\t" "s_waitcnt lgkmcnt(4)\n\t"
    "v_mfma_f32_16x16x32_bf16 v[192:195], a[144:147], a[0:3], v[192:195]\n\t" "ds_read_b64_tr_b16 a[128:129], %[vr5] offset:35872\n\t"
    "v_mfma_f32_16x16x32_bf16 v[192:195], a[148:151], a[16:19], v[192:195]\n\t" "ds_read_b64_tr_b16 a[130:131], %[vr5] offset:39456\n\t"
    "v_mfma_f32_16x16x32_bf16 v[196:199], a[144:147], a[4:7], v[196:199]\n\t" "global_load_dwordx4 a[88:91], %[va], s[76:77] offset:2048 sc1\n\t"
    "v_mfma_f32_16x16x32_bf16 v[196:199], a[148:151], a[20:23], v[196:199]\n\t" "v_mfma_f32_16x16x32_bf16 v[200:203], a[144:147], a[8:11], v[200:203]\n\t"
    "ds_read_b64_tr_b16 a[132:133], %[vr5] offset:50208\n\t" "v_mfma_f32_16x16x32_bf16 v[200:203], a[148:151], a[24:27], v[200:203]\n\t" "ds_read_b64_tr_b16 a[134:135], %[vr5] offset:53792\n\t"
    "v_mfma_f32_16x16x32_bf16 v[204:207], a[144:147], a[12:15], v[204:207]\n\t" "v_mfma_f32_16x16x32_bf16 v[204:207], a[148:151], a[28:31], v[204:207]\n\t" "s_waitcnt lgkmcnt(4)\n\t"
    "v_mfma_f32_16x16x32_bf16 v[208:211], a[152:155], a[0:3], v[208:211]\n\t" "ds_read_b64_tr_b16 a[136:137], %[vr7] offset:35840\n\t"
    "v_mfma_f32_16x16x32_bf16 v[208:211], a[156:159], a[16:19], v[208:211]\n\t" "ds_read_b64_tr_b16 a[138:139], %[vr7] offset:39424\n\t" "s_add_u32 m0, %[sl], 59392\n\t"
    "v_mfma_f32_16x16x32_bf16 v[212:215], a[152:155], a[4:7], v[212:215]\n\t" "global_load_lds_dwordx4 %[vb2], s[78:79]\n\t" "v_mfma_f32_16x16x32_bf16 v[212:215], a[156:159], a[20:23], v[212:215]\n\t"
    "v_mfma_f32_16x16x32_bf16 v[216:219], a[152:155], a[8:11], v[216:219]\n\t" "ds_read_b64_tr_b16 a[140:141], %[vr7] offset:50176\n\t"
    "v_mfma_f32_16x16x32_bf16 v[216:219], a[156:159], a[24:27], v[216:219]\n\t" "ds_read_b64_tr_b16 a[142:143], %[vr7] offset:53760\n\t"
    "v_mfma_f32_16x16x32_bf16 v[220:223], a[152:155], a[12:15], v[220:223]\n\t" "global_load_dwordx4 a[92:95], %[va], s[76:77] offset:3072 sc1\n\t"
    "v_mfma_f32_16x16x32_bf16 v[220:223], a[156:159], a[28:31], v[220:223]\n\t" "s_add_u32 s76, s76, 16384\n\t" "s_addc_u32 s77, s77, 0\n\t" "s_waitcnt lgkmcnt(4)\n\t"
    "v_mfma_f32_16x16x32_bf16 v[224:227], a[128:131], a[0:3], v[224:227]\n\t" "s_waitcnt vmcnt(24)\n\t" "s_barrier\n\t" "ds_read_b64_tr_b16 a[144:145], %[vr0] offset:0\n\t"
    "v_mfma_f32_16x16x32_bf16 v[224:227], a[132:135], a[16:19], v[224:227]\n\t" "ds_read_b64_tr_b16 a[146:147], %[vr0] offset:3584\n\t"
    "v_mfma_f32_16x16x32_bf16 v[228:231], a[128:131], a[4:7], v[228:231]\n\t" "v_mfma_f32_16x16x32_bf16 v[228:231], a[132:135], a[20:23], v[228:231]\n\t"
    "v_mfma_f32_16x16x32_bf16 v[232:235], a[128:131], a[8:11], v[232:235]\n\t" "ds_read_b64_tr_b16 a[148:149], %[vr0] offset:14336\n\t"
    "v_mfma_f32_16x16x32_bf16 v[232:235], a[132:135], a[24:27], v[232:235]\n\t" "ds_read_b64_tr_b16 a[150:151], %[vr0] offset:17920\n\t" "s_add_u32 m0, %[sl], 60416\n\t"
    "v_mfma_f32_16x16x32_bf16 v[236:239], a[128:131], a[12:15], v[236:239]\n\t" "s_mov_b64 exec, s[82:83]\n\t" "global_load_lds_dwordx4 %[vb3], s[78:79]\n\t" "s_mov_b64 exec, -1\n\t"
    "v_mfma_f32_16x16x32_bf16 v[236:239], a[132:135], a[28:31], v[236:239]\n\t" "s_add_u32 s78, s78, 229376\n\t" "s_addc_u32 s79, s79, 0\n\t" "s_waitcnt lgkmcnt(4)\n\t"
    "v_mfma_f32_16x16x32_bf16 v[240:243], a[136:139], a[0:3], v[240:243]\n\t" "ds_read_b64_tr_b16 a[152:153], %[vr0] offset:32\n\t"
    "v_mfma_f32_16x16x32_bf16 v[240:243], a[140:143], a[16:19], v[240:243]\n\t" "ds_read_b64_tr_b16 a[154:155], %[vr0] offset:3616\n\t" "v_mfma_f32_16x16x32_bf16 a[180:183], a[136:139], a[184:187], 0\n\t"
    "v_mfma_f32_16x16x32_bf16 v[244:247], a[136:139], a[4:7], v[244:247]\n\t" "v_mfma_f32_16x16x32_bf16 v[244:247], a[140:143], a[20:23], v[244:247]\n\t"
    "v_mfma_f32_16x16x32_bf16 v[248:251], a[136:139], a[8:11], v[248:251]\n\t" "ds_read_b64_tr_b16 a[156:157], %[vr0] offset:14368\n\t"
    "v_mfma_f32_16x16x32_bf16 v[248:251], a[140:143], a[24:27], v[248:251]\n\t" "ds_read_b64_tr_b16 a[158:159], %[vr0] offset:17952\n\t"
    "v_mfma_f32_16x16x32_bf16 a[180:183], a[140:143], a[188:191], a[180:183]\n\t" "v_mfma_f32_16x16x32_bf16 v[252:255], a[136:139], a[12:15], v[252:255]\n\t"
    "v_mfma_f32_16x16x32_bf16 v[252:255], a[140:143], a[28:31], v[252:255]\n\t" "s_waitcnt lgkmcnt(4) vmcnt(18)\n\t" "v_mfma_f32_16x16x32_bf16 v[32:35], a[144:147], a[32:35], v[32:35]\n\t" "s_barrier\n\t"
    "ds_read_b64_tr_b16 a[128:129], %[vr2] offset:0\n\t" "v_mfma_f32_16x16x32_bf16 v[32:35], a[148:151], a[48:51], v[32:35]\n\t" "ds_read_b64_tr_b16 a[130:131], %[vr2] offset:3584\n\t"
    "v_mfma_f32_16x16x32_bf16 a[160:163], a[144:147], a[192:195], a[160:163]\n\t" "v_mfma_f32_16x16x32_bf16 v[36:39], a[144:147], a[36:39], v[36:39]\n\t"
    "global_load_dwordx4 a[96:99], %[va], s[76:77] offset:0 sc1\n\t" "v_mfma_f32_16x16x32_bf16 v[36:39], a[148:151], a[52:55], v[36:39]\n\t"
    "v_mfma_f32_16x16x32_bf16 v[40:43], a[144:147], a[40:43], v[40:43]\n\t" "ds_read_b64_tr_b16 a[132:133], %[vr2] offset:14336\n\t" "v_mfma_f32_16x16x32_bf16 v[40:43], a[148:151], a[56:59], v[40:43]\n\t"
    "ds_read_b64_tr_b16 a[134:135], %[vr2] offset:17920\n\t" "v_mfma_f32_16x16x32_bf16 a[160:163], a[148:151], a[196:199], a[160:163]\n\t" "s_add_u32 m0, %[sl], 71680\n\t"
    "v_mfma_f32_16x16x32_bf16 v[44:47], a[144:147], a[44:47], v[44:47]\n\t" "global_load_lds_dwordx4 %[vb0], s[78:79]\n\t" "v_mfma_f32_16x16x32_bf16 v[44:47], a[148:151], a[60:63], v[44:47]\n\t"
    "s_waitcnt lgkmcnt(4)\n\t" "v_mfma_f32_16x16x32_bf16 v[48:51], a[152:155], a[32:35], v[48:51]\n\t" "ds_read_b64_tr_b16 a[136:137], %[vr2] offset:32\n\t"
    "v_mfma_f32_16x16x32_bf16 v[48:51], a[156:159], a[48:51], v[48:51]\n\t" "ds_read_b64_tr_b16 a[138:139], %[vr2] offset:3616\n\t"
    "v_mfma_f32_16x16x32_bf16 a[164:167], a[152:155], a[192:195], a[164:167]\n\t" "v_mfma_f32_16x16x32_bf16 v[52:55], a[152:155], a[36:39], v[52:55]\n\t"
    "v_mfma_f32_16x16x32_bf16 v[52:55], a[156:159], a[52:55], v[52:55]\n\t" "v_mfma_f32_16x16x32_bf16 v[56:59], a[152:155], a[40:43], v[56:59]\n\t" "ds_read_b64_tr_b16 a[140:141], %[vr2] offset:14368\n\t"
    "v_mfma_f32_16x16x32_bf16 v[56:59], a[156:159], a[56:59], v[56:59]\n\t" "ds_read_b64_tr_b16 a[142:143], %[vr2] offset:17952\n\t"
    "v_mfma_f32_16x16x32_bf16 a[164:167], a[156:159], a[196:199], a[164:167]\n\t" "v_mfma_f32_16x16x32_bf16 v[60:63], a[152:155], a[44:47], v[60:63]\n\t"
    "global_load_dwordx4 a[100:103], %[va], s[76:77] offset:1024 sc1\n\t" "v_mfma_f32_16x16x32_bf16 v[60:63], a[156:159], a[60:63], v[60:63]\n\t" "s_waitcnt lgkmcnt(4)\n\t"
    "v_mfma_f32_16x16x32_bf16 v[64:67], a[128:131], a[32:35], v[64:67]\n\t" "ds_read_b64_tr_b16 a[144:145], %[vr4] offset:0\n\t" "v_mfma_f32_16x16x32_bf16 v[64:67], a[132:135], a[48:51], v[64:67]\n\t"
    "ds_read_b64_tr_b16 a[146:147], %[vr4] offset:3584\n\t" "s_add_u32 m0, %[sl], 72704\n\t" "v_mfma_f32_16x16x32_bf16 v[68:71], a[128:131], a[36:39], v[68:71]\n\t"
    "global_load_lds_dwordx4 %[vb1], s[78:79]\n\t" "v_mfma_f32_16x16x32_bf16 v[68:71], a[132:135], a[52:55], v[68:71]\n\t" "v_mfma_f32_16x16x32_bf16 v[72:75], a[128:131], a[40:43], v[72:75]\n\t"
    "ds_read_b64_tr_b16 a[148:149], %[vr4] offset:14336\n\t" "v_mfma_f32_16x16x32_bf16 v[72:75], a[132:135], a[56:59], v[72:75]\n\t" "ds_read_b64_tr_b16 a[150:151], %[vr4] offset:17920\n\t"
    "v_mfma_f32_16x16x32_bf16 v[76:79], a[128:131], a[44:47], v[76:79]\n\t" "v_mfma_f32_16x16x32_bf16 v[76:79], a[132:135], a[60:63], v[76:79]\n\t" "s_waitcnt lgkmcnt(4)\n\t"
    "v_mfma_f32_16x16x32_bf16 v[80:83], a[136:139], a[32:35], v[80:83]\n\t" "ds_read_b64_tr_b16 a[152:153], %[vr4] offset:32\n\t" "v_mfma_f32_16x16x32_bf16 v[80:83], a[140:143], a[48:51], v[80:83]\n\t"
    "ds_read_b64_tr_b16 a[154:155], %[vr4] offset:3616\n\t" "v_mfma_f32_16x16x32_bf16 v[84:87], a[136:139], a[36:39], v[84:87]\n\t" "global_load_dwordx4 a[104:107], %[va], s[76:77] offset:2048 sc1\n\t"
    "v_mfma_f32_16x16x32_bf16 v[84:87], a[140:143], a[52:55], v[84:87]\n\t" "v_mfma_f32_16x16x32_bf16 v[88:91], a[136:139], a[40:43], v[88:91]\n\t" "ds_read_b64_tr_b16 a[156:157], %[vr4] offset:14368\n\t"
    "v_mfma_f32_16x16x32_bf16 v[88:91], a[140:143], a[56:59], v[88:91]\n\t" "ds_read_b64_tr_b16 a[158:159], %[vr4] offset:17952\n\t" "v_mfma_f32_16x16x32_bf16 v[92:95], a[136:139], a[44:47], v[92:95]\n\t"
    "v_mfma_f32_16x16x32_bf16 v[92:95], a[140:143], a[60:63], v[92:95]\n\t" "s_waitcnt lgkmcnt(4)\n\t" "v_mfma_f32_16x16x32_bf16 v[96:99], a[144:147], a[32:35], v[96:99]\n\t"
    "ds_read_b64_tr_b16 a[128:129], %[vr6] offset:0\n\t" "v_mfma_f32_16x16x32_bf16 v[96:99], a[148:151], a[48:51], v[96:99]\n\t" "ds_read_b64_tr_b16 a[130:131], %[vr6] offset:3584\n\t"
    "s_add_u32 m0, %[sl], 73728\n\t" "v_mfma_f32_16x16x32_bf16 v[100:103], a[144:147], a[36:39], v[100:103]\n\t" "global_load_lds_dwordx4 %[vb2], s[78:79]\n\t"
    "v_mfma_f32_16x16x32_bf16 v[100:103], a[148:151], a[52:55], v[100:103]\n\t" "v_mfma_f32_16x16x32_bf16 v[104:107], a[144:147], a[40:43], v[104:107]\n\t"
    "ds_read_b64_tr_b16 a[132:133], %[vr6] offset:14336\n\t" "v_mfma_f32_16x16x32_bf16 v[104:107], a[148:151], a[56:59], v[104:107]\n\t" "ds_read_b64_tr_b16 a[134:135], %[vr6] offset:17920\n\t"
    "v_mfma_f32_16x16x32_bf16 v[108:111], a[144:147], a[44:47], v[108:111]\n\t" "global_load_dwordx4 a[108:111], %[va], s[76:77] offset:3072 sc1\n\t"
    "v_mfma_f32_16x16x32_bf16 v[108:111], a[148:151], a[60:63], v[108:111]\n\t" "s_add_u32 s76, s76, 16384\n\t" "s_addc_u32 s77, s77, 0\n\t" "s_waitcnt lgkmcnt(4)\n\t"
    "v_mfma_f32_16x16x32_bf16 v[112:115], a[152:155], a[32:35], v[112:115]\n\t" "ds_read_b64_tr_b16 a[136:137], %[vr0] offset:7168\n\t"
    "v_mfma_f32_16x16x32_bf16 v[112:115], a[156:159], a[48:51], v[112:115]\n\t" "ds_read_b64_tr_b16 a[138:139], %[vr0] offset:10752\n\t"
    "v_mfma_f32_16x16x32_bf16 v[116:119], a[152:155], a[36:39], v[116:119]\n\t" "v_mfma_f32_16x16x32_bf16 v[116:119], a[156:159], a[52:55], v[116:119]\n\t"
    "v_mfma_f32_16x16x32_bf16 v[120:123], a[152:155], a[40:43], v[120:123]\n\t" "ds_read_b64_tr_b16 a[140:141], %[vr0] offset:21504\n\t"
    "v_mfma_f32_16x16x32_bf16 v[120:123], a[156:159], a[56:59], v[120:123]\n\t" "ds_read_b64_tr_b16 a[142:143], %[vr0] offset:25088\n\t" "s_add_u32 m0, %[sl], 74752\n\t"
    "v_mfma_f32_16x16x32_bf16 v[124:127], a[152:155], a[44:47], v[124:127]\n\t" "s_mov_b64 exec, s[82:83]\n\t" "global_load_lds_dwordx4 %[vb3], s[78:79]\n\t" "s_mov_b64 exec, -1\n\t"
    "v_mfma_f32_16x16x32_bf16 v[124:127], a[156:159], a[60:63], v[124:127]\n\t" "s_add_u32 s78, s78, s88\n\t" "s_addc_u32 s79, s79, s89\n\t" "s_waitcnt lgkmcnt(4)\n\t"
    "v_mfma_f32_16x16x32_bf16 v[128:131], a[128:131], a[32:35], v[128:131]\n\t" "ds_read_b64_tr_b16 a[144:145], %[vr0] offset:7200\n\t"
    "v_mfma_f32_16x16x32_bf16 v[128:131], a[132:135], a[48:51], v[128:131]\n\t" "ds_read_b64_tr_b16 a[146:147], %[vr0] offset:10784\n\t"
    "v_mfma_f32_16x16x32_bf16 a[168:171], a[128:131], a[192:195], a[168:171]\n\t" "v_mfma_f32_16x16x32_bf16 v[132:135], a[128:131], a[36:39], v[132:135]\n\t"
    "v_mfma_f32_16x16x32_bf16 v[132:135], a[132:135], a[52:55], v[132:135]\n\t" "v_mfma_f32_16x16x32_bf16 v[136:139], a[128:131], a[40:43], v[136:139]\n\t"
    "ds_read_b64_tr_b16 a[148:149], %[vr0] offset:21536\n\t" "v_mfma_f32_16x16x32_bf16 v[136:139], a[132:135], a[56:59], v[136:139]\n\t" "ds_read_b64_tr_b16 a[150:151], %[vr0] offset:25120\n\t"
    "v_mfma_f32_16x16x32_bf16 a[168:171], a[132:135], a[196:199], a[168:171]\n\t" "v_mfma_f32_16x16x32_bf16 v[140:143], a[128:131], a[44:47], v[140:143]\n\t"
    "v_mfma_f32_16x16x32_bf16 v[140:143], a[132:135], a[60:63], v[140:143]\n\t" "s_waitcnt lgkmcnt(4)\n\t" "v_mfma_f32_16x16x32_bf16 v[144:147], a[136:139], a[32:35], v[144:147]\n\t"
    "ds_read_b64_tr_b16 a[152:153], %[vr2] offset:7168\n\t" "v_mfma_f32_16x16x32_bf16 v[144:147], a[140:143], a[48:51], v[144:147]\n\t" "ds_read_b64_tr_b16 a[154:155], %[vr2] offset:10752\n\t"
    "v_mfma_f32_16x16x32_bf16 a[172:175], a[136:139], a[192:195], a[172:175]\n\t" "v_mfma_f32_16x16x32_bf16 v[148:151], a[136:139], a[36:39], v[148:151]\n\t"
    "global_load_dwordx4 a[112:115], %[va], s[76:77] offset:0 sc1\n\t" "v_mfma_f32_16x16x32_bf16 v[148:151], a[140:143], a[52:55], v[148:151]\n\t"
    "v_mfma_f32_16x16x32_bf16 v[152:155], a[136:139], a[40:43], v[152:155]\n\t" "ds_read_b64_tr_b16 a[156:157], %[vr2] offset:21504\n\t"
    "v_mfma_f32_16x16x32_bf16 v[152:155], a[140:143], a[56:59], v[152:155]\n\t" "ds_read_b64_tr_b16 a[158:159], %[vr2] offset:25088\n\t"
    "v_mfma_f32_16x16x32_bf16 a[172:175], a[140:143], a[196:199], a[172:175]\n\t" "s_add_u32 m0, %[sl], 86016\n\t" "v_mfma_f32_16x16x32_bf16 v[156:159], a[136:139], a[44:47], v[156:159]\n\t"
    "global_load_lds_dwordx4 %[vb0], s[78:79]\n\t" "v_mfma_f32_16x16x32_bf16 v[156:159], a[140:143], a[60:63], v[156:159]\n\t" "s_waitcnt lgkmcnt(4)\n\t"
    "v_mfma_f32_16x16x32_bf16 v[160:163], a[144:147], a[32:35], v[160:163]\n\t" "ds_read_b64_tr_b16 a[128:129], %[vr2] offset:7200\n\t"
    "v_mfma_f32_16x16x32_bf16 v[160:163], a[148:151], a[48:51], v[160:163]\n\t" "ds_read_b64_tr_b16 a[130:131], %[vr2] offset:10784\n\t"
    "v_mfma_f32_16x16x32_bf16 a[176:179], a[144:147], a[192:195], a[176:179]\n\t" "v_mfma_f32_16x16x32_bf16 v[164:167], a[144:147], a[36:39], v[164:167]\n\t"
    "v_mfma_f32_16x16x32_bf16 v[164:167], a[148:151], a[52:55], v[164:167]\n\t" "v_mfma_f32_16x16x32_bf16 v[168:171], a[144:147], a[40:43], v[168:171]\n\t"
    "ds_read_b64_tr_b16 a[132:133], %[vr2] offset:21536\n\t" "v_mfma_f32_16x16x32_bf16 v[168:171], a[148:151], a[56:59], v[168:171]\n\t" "ds_read_b64_tr_b16 a[134:135], %[vr2] offset:25120\n\t"
    "v_mfma_f32_16x16x32_bf16 a[176:179], a[148:151], a[196:199], a[176:179]\n\t" "v_mfma_f32_16x16x32_bf16 v[172:175], a[144:147], a[44:47], v[172:175]\n\t"
    "global_load_dwordx4 a[116:119], %[va], s[76:77] offset:1024 sc1\n\t" "v_mfma_f32_16x16x32_bf16 v[172:175], a[148:151], a[60:63], v[172:175]\n\t" "s_waitcnt lgkmcnt(4)\n\t"
    "v_mfma_f32_16x16x32_bf16 v[176:179], a[152:155], a[32:35], v[176:179]\n\t" "ds_read_b64_tr_b16 a[136:137], %[vr4] offset:7168\n\t"
    "v_mfma_f32_16x16x32_bf16 v[176:179], a[156:159], a[48:51], v[176:179]\n\t" "ds_read_b64_tr_b16 a[138:139], %[vr4] offset:10752\n\t" "s_add_u32 m0, %[sl], 87040\n\t"
    "v_mfma_f32_16x16x32_bf16 v[180:183], a[152:155], a[36:39], v[180:183]\n\t" "global_load_lds_dwordx4 %[vb1], s[78:79]\n\t" "v_mfma_f32_16x16x32_bf16 v[180:183], a[156:159], a[52:55], v[180:183]\n\t"
    "v_mfma_f32_16x16x32_bf16 v[184:187], a[152:155], a[40:43], v[184:187]\n\t" "ds_read_b64_tr_b16 a[140:141], %[vr4] offset:21504\n\t"
    "v_mfma_f32_16x16x32_bf16 v[184:187], a[156:159], a[56:59], v[184:187]\n\t" "ds_read_b64_tr_b16 a[142:143], %[vr4] offset:25088\n\t"
    "v_mfma_f32_16x16x32_bf16 v[188:191], a[152:155], a[44:47], v[188:191]\n\t" "v_mfma_f32_16x16x32_bf16 v[188:191], a[156:159], a[60:63], v[188:191]\n\t" "s_waitcnt lgkmcnt(4)\n\t"
    "v_mfma_f32_16x16x32_bf16 v[192:195], a[128:131], a[32:35], v[192:195]\n\t" "ds_read_b64_tr_b16 a[144:145], %[vr4] offset:7200\n\t"
    "v_mfma_f32_16x16x32_bf16 v[192:195], a[132:135], a[48:51], v[192:195]\n\t" "ds_read_b64_tr_b16 a[146:147], %[vr4] offset:10784\n\t"
    "v_mfma_f32_16x16x32_bf16 v[196:199], a[128:131], a[36:39], v[196:199]\n\t" "global_load_dwordx4 a[120:123], %[va], s[76:77] offset:2048 sc1\n\t"
    "v_mfma_f32_16x16x32_bf16 v[196:199], a[132:135], a[52:55], v[196:199]\n\t" "v_mfma_f32_16x16x32_bf16 v[200:203], a[128:131], a[40:43], v[200:203]\n\t"
    "ds_read_b64_tr_b16 a[148:149], %[vr4] offset:21536\n\t" "v_mfma_f32_16x16x32_bf16 v[200:203], a[132:135], a[56:59], v[200:203]\n\t" "ds_read_b64_tr_b16 a[150:151], %[vr4] offset:25120\n\t"
    "v_mfma_f32_16x16x32_bf16 v[204:207], a[128:131], a[44:47], v[204:207]\n\t" "v_mfma_f32_16x16x32_bf16 v[204:207], a[132:135], a[60:63], v[204:207]\n\t" "s_waitcnt lgkmcnt(4)\n\t"
    "v_mfma_f32_16x16x32_bf16 v[208:211], a[136:139], a[32:35], v[208:211]\n\t" "ds_read_b64_tr_b16 a[152:153], %[vr6] offset:7168\n\t"
    "v_mfma_f32_16x16x32_bf16 v[208:211], a[140:143], a[48:51], v[208:211]\n\t" "ds_read_b64_tr_b16 a[154:155], %[vr6] offset:10752\n\t" "s_add_u32 m0, %[sl], 88064\n\t"
    "v_mfma_f32_16x16x32_bf16 v[212:215], a[136:139], a[36:39], v[212:215]\n\t" "global_load_lds_dwordx4 %[vb2], s[78:79]\n\t" "v_mfma_f32_16x16x32_bf16 v[212:215], a[140:143], a[52:55], v[212:215]\n\t"
    "v_mfma_f32_16x16x32_bf16 v[216:219], a[136:139], a[40:43], v[216:219]\n\t" "ds_read_b64_tr_b16 a[156:157], %[vr6] offset:21504\n\t"
    "v_mfma_f32_16x16x32_bf16 v[216:219], a[140:143], a[56:59], v[216:219]\n\t" "ds_read_b64_tr_b16 a[158:159], %[vr6] offset:25088\n\t"
    "v_mfma_f32_16x16x32_bf16 v[220:223], a[136:139], a[44:47], v[220:223]\n\t" "global_load_dwordx4 a[124:127], %[va], s[76:77] offset:3072 sc1\n\t"
    "v_mfma_f32_16x16x32_bf16 v[220:223], a[140:143], a[60:63], v[220:223]\n\t" "s_add_u32 s76, s76, s86\n\t" "s_addc_u32 s77, s77, s87\n\t" "s_waitcnt lgkmcnt(4)\n\t"
    "v_mfma_f32_16x16x32_bf16 v[224:227], a[144:147], a[32:35], v[224:227]\n\t" "s_waitcnt vmcnt(24)\n\t" "s_barrier\n\t" "ds_read_b64_tr_b16 a[128:129], %[vr0] offset:28672\n\t"
    "v_mfma_f32_16x16x32_bf16 v[224:227], a[148:151], a[48:51], v[224:227]\n\t" "ds_read_b64_tr_b16 a[130:131], %[vr0] offset:32256\n\t"
    "v_mfma_f32_16x16x32_bf16 v[228:231], a[144:147], a[36:39], v[228:231]\n\t" "v_mfma_f32_16x16x32_bf16 v[228:231], a[148:151], a[52:55], v[228:231]\n\t"
    "v_mfma_f32_16x16x32_bf16 v[232:235], a[144:147], a[40:43], v[232:235]\n\t" "ds_read_b64_tr_b16 a[132:133], %[vr0] offset:43008\n\t"
    "v_mfma_f32_16x16x32_bf16 v[232:235], a[148:151], a[56:59], v[232:235]\n\t" "ds_read_b64_tr_b16 a[134:135], %[vr0] offset:46592\n\t" "s_add_u32 m0, %[sl], 89088\n\t"
    "v_mfma_f32_16x16x32_bf16 v[236:239], a[144:147], a[44:47], v[236:239]\n\t" "s_mov_b64 exec, s[82:83]\n\t" "global_load_lds_dwordx4 %[vb3], s[78:79]\n\t" "s_mov_b64 exec, -1\n\t"
    "v_mfma_f32_16x16x32_bf16 v[236:239], a[148:151], a[60:63], v[236:239]\n\t" "s_add_u32 s78, s78, 229376\n\t" "s_addc_u32 s79, s79, 0\n\t" "s_waitcnt lgkmcnt(4)\n\t"
    "v_mfma_f32_16x16x32_bf16 v[240:243], a[152:155], a[32:35], v[240:243]\n\t" "ds_read_b64_tr_b16 a[136:137], %[vr0] offset:28704\n\t"
    "v_mfma_f32_16x16x32_bf16 v[240:243], a[156:159], a[48:51], v[240:243]\n\t" "ds_read_b64_tr_b16 a[138:139], %[vr0] offset:32288\n\t"
    "v_mfma_f32_16x16x32_bf16 a[180:183], a[152:155], a[192:195], a[180:183]\n\t" "v_mfma_f32_16x16x32_bf16 v[244:247], a[152:155], a[36:39], v[244:247]\n\t"
    "v_mfma_f32_16x16x32_bf16 v[244:247], a[156:159], a[52:55], v[244:247]\n\t" "v_mfma_f32_16x16x32_bf16 v[248:251], a[152:155], a[40:43], v[248:251]\n\t"
    "ds_read_b64_tr_b16 a[140:141], %[vr0] offset:43040\n\t" "v_mfma_f32_16x16x32_bf16 v[248:251], a[156:159], a[56:59], v[248:251]\n\t" "ds_read_b64_tr_b16 a[142:143], %[vr0] offset:46624\n\t"
    "v_mfma_f32_16x16x32_bf16 a[180:183], a[156:159], a[196:199], a[180:183]\n\t" "v_mfma_f32_16x16x32_bf16 v[252:255], a[152:155], a[44:47], v[252:255]\n\t"
    "v_mfma_f32_16x16x32_bf16 v[252:255], a[156:159], a[60:63], v[252:255]\n\t" "v_accvgpr_read_b32 v31, %[cp]\n\t" "s_nop 7\n\t" "s_mov_b64 exec, %[m01]\n\t"
    "global_store_dwordx4 v31, a[160:163], %[su0] offset:0 sc1\n\t" "global_store_dwordx4 v31, a[164:167], %[su0] offset:2304 sc1\n\t" "global_store_dwordx4 v31, a[172:175], %[su0] offset:1152 sc1\n\t"
    "global_store_dwordx4 v31, a[176:179], %[su0] offset:3456 sc1\n\t" "s_mov_b64 exec, %[m6]\n\t" "global_store_dwordx4 v31, a[168:171], %[su6] offset:0 sc1\n\t"
    "global_store_dwordx4 v31, a[180:183], %[su6] offset:1152 sc1\n\t" "s_mov_b64 exec, -1\n\t" "s_branch .Ld0_%=\n\t" ".LE1_%=:\n\t" "s_waitcnt lgkmcnt(4) vmcnt(17)\n\t"
    "v_mfma_f32_16x16x32_bf16 v[32:35], a[128:131], a[64:67], v[32:35]\n\t" "s_barrier\n\t" "ds_read_b64_tr_b16 a[144:145], %[vr2] offset:28672\n\t"
    "v_mfma_f32_16x16x32_bf16 v[32:35], a[132:135], a[80:83], v[32:35]\n\t" "ds_read_b64_tr_b16 a[146:147], %[vr2] offset:32256\n\t" "v_mfma_f32_16x16x32_bf16 a[160:163], a[128:131], a[184:187], 0\n\t"
    "v_mfma_f32_16x16x32_bf16 v[36:39], a[128:131], a[68:71], v[36:39]\n\t" "global_load_dwordx4 a[0:3], %[va], s[76:77] offset:0 sc1\n\t"
    "v_mfma_f32_16x16x32_bf16 v[36:39], a[132:135], a[84:87], v[36:39]\n\t" "v_mfma_f32_16x16x32_bf16 v[40:43], a[128:131], a[72:75], v[40:43]\n\t" "ds_read_b64_tr_b16 a[148:149], %[vr2] offset:43008\n\t"
    "v_mfma_f32_16x16x32_bf16 v[40:43], a[132:135], a[88:91], v[40:43]\n\t" "ds_read_b64_tr_b16 a[150:151], %[vr2] offset:46592\n\t"
    "v_mfma_f32_16x16x32_bf16 a[160:163], a[132:135], a[188:191], a[160:163]\n\t" "s_add_u32 m0, %[sl], 100352\n\t" "v_mfma_f32_16x16x32_bf16 v[44:47], a[128:131], a[76:79], v[44:47]\n\t"
    "global_load_lds_dwordx4 %[vb0], s[78:79]\n\t" "v_mfma_f32_16x16x32_bf16 v[44:47], a[132:135], a[92:95], v[44:47]\n\t" "s_waitcnt lgkmcnt(4)\n\t"
    "v_mfma_f32_16x16x32_bf16 v[48:51], a[136:139], a[64:67], v[48:51]\n\t" "ds_read_b64_tr_b16 a[152:153], %[vr2] offset:28704\n\t" "v_mfma_f32_16x16x32_bf16 v[48:51], a[140:143], a[80:83], v[48:51]\n\t"
    "ds_read_b64_tr_b16 a[154:155], %[vr2] offset:32288\n\t" "v_mfma_f32_16x16x32_bf16 a[164:167], a[136:139], a[184:187], 0\n\t" "v_mfma_f32_16x16x32_bf16 v[52:55], a[136:139], a[68:71], v[52:55]\n\t"
    "v_mfma_f32_16x16x32_bf16 v[52:55], a[140:143], a[84:87], v[52:55]\n\t" "v_mfma_f32_16x16x32_bf16 v[56:59], a[136:139], a[72:75], v[56:59]\n\t" "ds_read_b64_tr_b16 a[156:157], %[vr2] offset:43040\n\t"
    "v_mfma_f32_16x16x32_bf16 v[56:59], a[140:143], a[88:91], v[56:59]\n\t" "ds_read_b64_tr_b16 a[158:159], %[vr2] offset:46624\n\t"
    "v_mfma_f32_16x16x32_bf16 a[164:167], a[140:143], a[188:191], a[164:167]\n\t" "v_mfma_f32_16x16x32_bf16 v[60:63], a[136:139], a[76:79], v[60:63]\n\t"
    "global_load_dwordx4 a[4:7], %[va], s[76:77] offset:1024 sc1\n\t" "v_mfma_f32_16x16x32_bf16 v[60:63], a[140:143], a[92:95], v[60:63]\n\t" "s_waitcnt lgkmcnt(4)\n\t"
    "v_mfma_f32_16x16x32_bf16 v[64:67], a[144:147], a[64:67], v[64:67]\n\t" "ds_read_b64_tr_b16 a[128:129], %[vr4] offset:28672\n\t" "v_mfma_f32_16x16x32_bf16 v[64:67], a[148:151], a[80:83], v[64:67]\n\t"
    "ds_read_b64_tr_b16 a[130:131], %[vr4] offset:32256\n\t" "s_add_u32 m0, %[sl], 101376\n\t" "v_mfma_f32_16x16x32_bf16 v[68:71], a[144:147], a[68:71], v[68:71]\n\t"
    "global_load_lds_dwordx4 %[vb1], s[78:79]\n\t" "v_mfma_f32_16x16x32_bf16 v[68:71], a[148:151], a[84:87], v[68:71]\n\t" "v_mfma_f32_16x16x32_bf16 v[72:75], a[144:147], a[72:75], v[72:75]\n\t"
    "ds_read_b64_tr_b16 a[132:133], %[vr4] offset:43008\n\t" "v_mfma_f32_16x16x32_bf16 v[72:75], a[148:151], a[88:91], v[72:75]\n\t" "ds_read_b64_tr_b16 a[134:135], %[vr4] offset:46592\n\t"
    "v_mfma_f32_16x16x32_bf16 v[76:79], a[144:147], a[76:79], v[76:79]\n\t" "v_mfma_f32_16x16x32_bf16 v[76:79], a[148:151], a[92:95], v[76:79]\n\t" "s_waitcnt lgkmcnt(4)\n\t"
    "v_mfma_f32_16x16x32_bf16 v[80:83], a[152:155], a[64:67], v[80:83]\n\t" "ds_read_b64_tr_b16 a[136:137], %[vr4] offset:28704\n\t" "v_mfma_f32_16x16x32_bf16 v[80:83], a[156:159], a[80:83], v[80:83]\n\t"
    "ds_read_b64_tr_b16 a[138:139], %[vr4] offset:32288\n\t" "v_mfma_f32_16x16x32_bf16 v[84:87], a[152:155], a[68:71], v[84:87]\n\t" "global_load_dwordx4 a[8:11], %[va], s[76:77] offset:2048 sc1\n\t"
    "v_mfma_f32_16x16x32_bf16 v[84:87], a[156:159], a[84:87], v[84:87]\n\t" "v_mfma_f32_16x16x32_bf16 v[88:91], a[152:155], a[72:75], v[88:91]\n\t" "ds_read_b64_tr_b16 a[140:141], %[vr4] offset:43040\n\t"
    "v_mfma_f32_16x16x32_bf16 v[88:91], a[156:159], a[88:91], v[88:91]\n\t" "ds_read_b64_tr_b16 a[142:143], %[vr4] offset:46624\n\t" "v_mfma_f32_16x16x32_bf16 v[92:95], a[152:155], a[76:79], v[92:95]\n\t"
    "v_mfma_f32_16x16x32_bf16 v[92:95], a[156:159], a[92:95], v[92:95]\n\t" "s_waitcnt lgkmcnt(4)\n\t" "v_mfma_f32_16x16x32_bf16 v[96:99], a[128:131], a[64:67], v[96:99]\n\t"
    "ds_read_b64_tr_b16 a[144:145], %[vr6] offset:28672\n\t" "v_mfma_f32_16x16x32_bf16 v[96:99], a[132:135], a[80:83], v[96:99]\n\t" "ds_read_b64_tr_b16 a[146:147], %[vr6] offset:32256\n\t"
    "s_add_u32 m0, %[sl], 102400\n\t" "v_mfma_f32_16x16x32_bf16 v[100:103], a[128:131], a[68:71], v[100:103]\n\t" "global_load_lds_dwordx4 %[vb2], s[78:79]\n\t"
    "v_mfma_f32_16x16x32_bf16 v[100:103], a[132:135], a[84:87], v[100:103]\n\t" "v_mfma_f32_16x16x32_bf16 v[104:107], a[128:131], a[72:75], v[104:107]\n\t"
    "ds_read_b64_tr_b16 a[148:149], %[vr6] offset:43008\n\t" "v_mfma_f32_16x16x32_bf16 v[104:107], a[132:135], a[88:91], v[104:107]\n\t" "ds_read_b64_tr_b16 a[150:151], %[vr6] offset:46592\n\t"
    "v_mfma_f32_16x16x32_bf16 v[108:111], a[128:131], a[76:79], v[108:111]\n\t" "global_load_dwordx4 a[12:15], %[va], s[76:77] offset:3072 sc1\n\t"
    "v_mfma_f32_16x16x32_bf16 v[108:111], a[132:135], a[92:95], v[108:111]\n\t" "s_add_u32 s76, s76, 16384\n\t" "s_addc_u32 s77, s77, 0\n\t" "s_waitcnt lgkmcnt(4)\n\t"
    "v_mfma_f32_16x16x32_bf16 v[112:115], a[136:139], a[64:67], v[112:115]\n\t" "ds_read_b64_tr_b16 a[152:153], %[vr0] offset:35840\n\t"
    "v_mfma_f32_16x16x32_bf16 v[112:115], a[140:143], a[80:83], v[112:115]\n\t" "ds_read_b64_tr_b16 a[154:155], %[vr0] offset:39424\n\t"
    "v_mfma_f32_16x16x32_bf16 v[116:119], a[136:139], a[68:71], v[116:119]\n\t" "v_mfma_f32_16x16x32_bf16 v[116:119], a[140:143], a[84:87], v[116:119]\n\t"
    "v_mfma_f32_16x16x32_bf16 v[120:123], a[136:139], a[72:75], v[120:123]\n\t" "ds_read_b64_tr_b16 a[156:157], %[vr0] offset:50176\n\t"
    "v_mfma_f32_16x16x32_bf16 v[120:123], a[140:143], a[88:91], v[120:123]\n\t" "ds_read_b64_tr_b16 a[158:159], %[vr0] offset:53760\n\t" "s_add_u32 m0, %[sl], 103424\n\t"
    "v_mfma_f32_16x16x32_bf16 v[124:127], a[136:139], a[76:79], v[124:127]\n\t" "s_mov_b64 exec, s[82:83]\n\t" "global_load_lds_dwordx4 %[vb3], s[78:79]\n\t" "s_mov_b64 exec, -1\n\t"
    "global_load_dword v29, %[va], %[cd]\n\t" "v_mfma_f32_16x16x32_bf16 v[124:127], a[140:143], a[92:95], v[124:127]\n\t" "s_add_u32 s78, s78, 229376\n\t" "s_addc_u32 s79, s79, 0\n\t"
    "s_waitcnt lgkmcnt(4)\n\t" "v_mfma_f32_16x16x32_bf16 v[128:131], a[144:147], a[64:67], v[128:131]\n\t" "ds_read_b64_tr_b16 a[128:129], %[vr0] offset:35872\n\t"
    "v_mfma_f32_16x16x32_bf16 v[128:131], a[148:151], a[80:83], v[128:131]\n\t" "ds_read_b64_tr_b16 a[130:131], %[vr0] offset:39456\n\t"
    "v_mfma_f32_16x16x32_bf16 a[168:171], a[144:147], a[184:187], 0\n\t" "v_mfma_f32_16x16x32_bf16 v[132:135], a[144:147], a[68:71], v[132:135]\n\t"
    "v_mfma_f32_16x16x32_bf16 v[132:135], a[148:151], a[84:87], v[132:135]\n\t" "v_mfma_f32_16x16x32_bf16 v[136:139], a[144:147], a[72:75], v[136:139]\n\t"
    "ds_read_b64_tr_b16 a[132:133], %[vr0] offset:50208\n\t" "v_mfma_f32_16x16x32_bf16 v[136:139], a[148:151], a[88:91], v[136:139]\n\t" "ds_read_b64_tr_b16 a[134:135], %[vr0] offset:53792\n\t"
    "v_mfma_f32_16x16x32_bf16 a[168:171], a[148:151], a[188:191], a[168:171]\n\t" "v_mfma_f32_16x16x32_bf16 v[140:143], a[144:147], a[76:79], v[140:143]\n\t"
    "v_mfma_f32_16x16x32_bf16 v[140:143], a[148:151], a[92:95], v[140:143]\n\t" "s_waitcnt lgkmcnt(4)\n\t" "v_mfma_f32_16x16x32_bf16 v[144:147], a[152:155], a[64:67], v[144:147]\n\t"
    "ds_read_b64_tr_b16 a[136:137], %[vr2] offset:35840\n\t" "v_mfma_f32_16x16x32_bf16 v[144:147], a[156:159], a[80:83], v[144:147]\n\t" "ds_read_b64_tr_b16 a[138:139], %[vr2] offset:39424\n\t"
    "v_mfma_f32_16x16x32_bf16 a[172:175], a[152:155], a[184:187], 0\n\t" "v_mfma_f32_16x16x32_bf16 v[148:151], a[152:155], a[68:71], v[148:151]\n\t"
    "global_load_dwordx4 a[16:19], %[va], s[76:77] offset:0 sc1\n\t" "v_mfma_f32_16x16x32_bf16 v[148:151], a[156:159], a[84:87], v[148:151]\n\t"
    "v_mfma_f32_16x16x32_bf16 v[152:155], a[152:155], a[72:75], v[152:155]\n\t" "ds_read_b64_tr_b16 a[140:141], %[vr2] offset:50176\n\t"
    "v_mfma_f32_16x16x32_bf16 v[152:155], a[156:159], a[88:91], v[152:155]\n\t" "ds_read_b64_tr_b16 a[142:143], %[vr2] offset:53760\n\t"
    "v_mfma_f32_16x16x32_bf16 a[172:175], a[156:159], a[188:191], a[172:175]\n\t" "s_add_u32 m0, %[sl], 0\n\t" "v_mfma_f32_16x16x32_bf16 v[156:159], a[152:155], a[76:79], v[156:159]\n\t"
    "global_load_lds_dwordx4 %[vb0], s[78:79]\n\t" "v_mfma_f32_16x16x32_bf16 v[156:159], a[156:159], a[92:95], v[156:159]\n\t" "s_waitcnt lgkmcnt(4)\n\t"
    "v_mfma_f32_16x16x32_bf16 v[160:163], a[128:131], a[64:67], v[160:163]\n\t" "ds_read_b64_tr_b16 a[144:145], %[vr2] offset:35872\n\t"
    "v_mfma_f32_16x16x32_bf16 v[160:163], a[132:135], a[80:83], v[160:163]\n\t" "ds_read_b64_tr_b16 a[146:147], %[vr2] offset:39456\n\t"
    "v_mfma_f32_16x16x32_bf16 a[176:179], a[128:131], a[184:187], 0\n\t" "v_mfma_f32_16x16x32_bf16 v[164:167], a[128:131], a[68:71], v[164:167]\n\t"
    "v_mfma_f32_16x16x32_bf16 v[164:167], a[132:135], a[84:87], v[164:167]\n\t" "v_mfma_f32_16x16x32_bf16 v[168:171], a[128:131], a[72:75], v[168:171]\n\t"
    "ds_read_b64_tr_b16 a[148:149], %[vr2] offset:50208\n\t" "v_mfma_f32_16x16x32_bf16 v[168:171], a[132:135], a[88:91], v[168:171]\n\t" "ds_read_b64_tr_b16 a[150:151], %[vr2] offset:53792\n\t"
    "v_mfma_f32_16x16x32_bf16 a[176:179], a[132:135], a[188:191], a[176:179]\n\t" "v_mfma_f32_16x16x32_bf16 v[172:175], a[128:131], a[76:79], v[172:175]\n\t"
    "global_load_dwordx4 a[20:23], %[va], s[76:77] offset:1024 sc1\n\t" "v_mfma_f32_16x16x32_bf16 v[172:175], a[132:135], a[92:95], v[172:175]\n\t" "s_waitcnt lgkmcnt(4)\n\t"
    "v_mfma_f32_16x16x32_bf16 v[176:179], a[136:139], a[64:67], v[176:179]\n\t" "ds_read_b64_tr_b16 a[152:153], %[vr4] offset:35840\n\t"
    "v_mfma_f32_16x16x32_bf16 v[176:179], a[140:143], a[80:83], v[176:179]\n\t" "ds_read_b64_tr_b16 a[154:155], %[vr4] offset:39424\n\t" "s_add_u32 m0, %[sl], 1024\n\t"
    "v_mfma_f32_16x16x32_bf16 v[180:183], a[136:139], a[68:71], v[180:183]\n\t" "global_load_lds_dwordx4 %[vb1], s[78:79]\n\t" "v_mfma_f32_16x16x32_bf16 v[180:183], a[140:143], a[84:87], v[180:183]\n\t"
    "v_mfma_f32_16x16x32_bf16 v[184:187], a[136:139], a[72:75], v[184:187]\n\t" "ds_read_b64_tr_b16 a[156:157], %[vr4] offset:50176\n\t"
    "v_mfma_f32_16x16x32_bf16 v[184:187], a[140:143], a[88:91], v[184:187]\n\t" "ds_read_b64_tr_b16 a[158:159], %[vr4] offset:53760\n\t"
    "v_mfma_f32_16x16x32_bf16 v[188:191], a[136:139], a[76:79], v[188:191]\n\t" "v_mfma_f32_16x16x32_bf16 v[188:191], a[140:143], a[92:95], v[188:191]\n\t" "s_waitcnt lgkmcnt(4)\n\t"
    "v_mfma_f32_16x16x32_bf16 v[192:195], a[144:147], a[64:67], v[192:195]\n\t" "ds_read_b64_tr_b16 a[128:129], %[vr4] offset:35872\n\t"
    "v_mfma_f32_16x16x32_bf16 v[192:195], a[148:151], a[80:83], v[192:195]\n\t" "ds_read_b64_tr_b16 a[130:131], %[vr4] offset:39456\n\t"
    "v_mfma_f32_16x16x32_bf16 v[196:199], a[144:147], a[68:71], v[196:199]\n\t" "global_load_dwordx4 a[24:27], %[va], s[76:77] offset:2048 sc1\n\t"
    "v_mfma_f32_16x16x32_bf16 v[196:199], a[148:151], a[84:87], v[196:199]\n\t" "v_mfma_f32_16x16x32_bf16 v[200:203], a[144:147], a[72:75], v[200:203]\n\t"
    "ds_read_b64_tr_b16 a[132:133], %[vr4] offset:50208\n\t" "v_mfma_f32_16x16x32_bf16 v[200:203], a[148:151], a[88:91], v[200:203]\n\t" "ds_read_b64_tr_b16 a[134:135], %[vr4] offset:53792\n\t"
    "v_mfma_f32_16x16x32_bf16 v[204:207], a[144:147], a[76:79], v[204:207]\n\t" "v_mfma_f32_16x16x32_bf16 v[204:207], a[148:151], a[92:95], v[204:207]\n\t" "s_waitcnt lgkmcnt(4)\n\t"
    "v_mfma_f32_16x16x32_bf16 v[208:211], a[152:155], a[64:67], v[208:211]\n\t" "ds_read_b64_tr_b16 a[136:137], %[vr6] offset:35840\n\t"
    "v_mfma_f32_16x16x32_bf16 v[208:211], a[156:159], a[80:83], v[208:211]\n\t" "ds_read_b64_tr_b16 a[138:139], %[vr6] offset:39424\n\t" "s_add_u32 m0, %[sl], 2048\n\t"
    "v_mfma_f32_16x16x32_bf16 v[212:215], a[152:155], a[68:71], v[212:215]\n\t" "global_load_lds_dwordx4 %[vb2], s[78:79]\n\t" "v_mfma_f32_16x16x32_bf16 v[212:215], a[156:159], a[84:87], v[212:215]\n\t"
    "v_mfma_f32_16x16x32_bf16 v[216:219], a[152:155], a[72:75], v[216:219]\n\t" "ds_read_b64_tr_b16 a[140:141], %[vr6] offset:50176\n\t"
    "v_mfma_f32_16x16x32_bf16 v[216:219], a[156:159], a[88:91], v[216:219]\n\t" "ds_read_b64_tr_b16 a[142:143], %[vr6] offset:53760\n\t"
    "v_mfma_f32_16x16x32_bf16 v[220:223], a[152:155], a[76:79], v[220:223]\n\t" "global_load_dwordx4 a[28:31], %[va], s[76:77] offset:3072 sc1\n\t"
    "v_mfma_f32_16x16x32_bf16 v[220:223], a[156:159], a[92:95], v[220:223]\n\t" "s_add_u32 s76, s76, 16384\n\t" "s_addc_u32 s77, s77, 0\n\t" "s_waitcnt lgkmcnt(4)\n\t"
    "v_mfma_f32_16x16x32_bf16 v[224:227], a[128:131], a[64:67], v[224:227]\n\t" "s_waitcnt vmcnt(24)\n\t" "s_barrier\n\t" "ds_read_b64_tr_b16 a[144:145], %[vr1] offset:0\n\t"
    "v_mfma_f32_16x16x32_bf16 v[224:227], a[132:135], a[80:83], v[224:227]\n\t" "ds_read_b64_tr_b16 a[146:147], %[vr1] offset:3584\n\t"
    "v_mfma_f32_16x16x32_bf16 v[228:231], a[128:131], a[68:71], v[228:231]\n\t" "v_mfma_f32_16x16x32_bf16 v[228:231], a[132:135], a[84:87], v[228:231]\n\t"
    "v_mfma_f32_16x16x32_bf16 v[232:235], a[128:131], a[72:75], v[232:235]\n\t" "ds_read_b64_tr_b16 a[148:149], %[vr1] offset:14336\n\t"
    "v_mfma_f32_16x16x32_bf16 v[232:235], a[132:135], a[88:91], v[232:235]\n\t" "ds_read_b64_tr_b16 a[150:151], %[vr1] offset:17920\n\t" "s_add_u32 m0, %[sl], 3072\n\t"
    "v_mfma_f32_16x16x32_bf16 v[236:239], a[128:131], a[76:79], v[236:239]\n\t" "s_mov_b64 exec, s[82:83]\n\t" "global_load_lds_dwordx4 %[vb3], s[78:79]\n\t" "s_mov_b64 exec, -1\n\t"
    "v_mfma_f32_16x16x32_bf16 v[236:239], a[132:135], a[92:95], v[236:239]\n\t" "s_add_u32 s78, s78, 229376\n\t" "s_addc_u32 s79, s79, 0\n\t" "s_waitcnt lgkmcnt(4)\n\t"
    "v_mfma_f32_16x16x32_bf16 v[240:243], a[136:139], a[64:67], v[240:243]\n\t" "ds_read_b64_tr_b16 a[152:153], %[vr1] offset:32\n\t"
    "v_mfma_f32_16x16x32_bf16 v[240:243], a[140:143], a[80:83], v[240:243]\n\t" "ds_read_b64_tr_b16 a[154:155], %[vr1] offset:3616\n\t" "v_mfma_f32_16x16x32_bf16 a[180:183], a[136:139], a[184:187], 0\n\t"
    "v_mfma_f32_16x16x32_bf16 v[244:247], a[136:139], a[68:71], v[244:247]\n\t" "v_mfma_f32_16x16x32_bf16 v[244:247], a[140:143], a[84:87], v[244:247]\n\t"
    "v_mfma_f32_16x16x32_bf16 v[248:251], a[136:139], a[72:75], v[248:251]\n\t" "ds_read_b64_tr_b16 a[156:157], %[vr1] offset:14368\n\t"
    "v_mfma_f32_16x16x32_bf16 v[248:251], a[140:143], a[88:91], v[248:251]\n\t" "ds_read_b64_tr_b16 a[158:159], %[vr1] offset:17952\n\t"
    "v_mfma_f32_16x16x32_bf16 a[180:183], a[140:143], a[188:191], a[180:183]\n\t" "v_mfma_f32_16x16x32_bf16 v[252:255], a[136:139], a[76:79], v[252:255]\n\t"
    "v_mfma_f32_16x16x32_bf16 v[252:255], a[140:143], a[92:95], v[252:255]\n\t" "s_waitcnt lgkmcnt(4) vmcnt(18)\n\t" "v_mfma_f32_16x16x32_bf16 v[32:35], a[144:147], a[96:99], v[32:35]\n\t" "s_barrier\n\t"
    "ds_read_b64_tr_b16 a[128:129], %[vr3] offset:0\n\t" "v_mfma_f32_16x16x32_bf16 v[32:35], a[148:151], a[112:115], v[32:35]\n\t" "ds_read_b64_tr_b16 a[130:131], %[vr3] offset:3584\n\t"
    "v_mfma_f32_16x16x32_bf16 a[160:163], a[144:147], a[192:195], a[160:163]\n\t" "v_mfma_f32_16x16x32_bf16 v[36:39], a[144:147], a[100:103], v[36:39]\n\t"
    "global_load_dwordx4 a[32:35], %[va], s[76:77] offset:0 sc1\n\t" "v_mfma_f32_16x16x32_bf16 v[36:39], a[148:151], a[116:119], v[36:39]\n\t"
    "v_mfma_f32_16x16x32_bf16 v[40:43], a[144:147], a[104:107], v[40:43]\n\t" "ds_read_b64_tr_b16 a[132:133], %[vr3] offset:14336\n\t"
    "v_mfma_f32_16x16x32_bf16 v[40:43], a[148:151], a[120:123], v[40:43]\n\t" "ds_read_b64_tr_b16 a[134:135], %[vr3] offset:17920\n\t"
    "v_mfma_f32_16x16x32_bf16 a[160:163], a[148:151], a[196:199], a[160:163]\n\t" "s_add_u32 m0, %[sl], 14336\n\t" "v_mfma_f32_16x16x32_bf16 v[44:47], a[144:147], a[108:111], v[44:47]\n\t"
    "global_load_lds_dwordx4 %[vb0], s[78:79]\n\t" "v_mfma_f32_16x16x32_bf16 v[44:47], a[148:151], a[124:127], v[44:47]\n\t" "s_waitcnt lgkmcnt(4)\n\t"
    "v_mfma_f32_16x16x32_bf16 v[48:51], a[152:155], a[96:99], v[48:51]\n\t" "ds_read_b64_tr_b16 a[136:137], %[vr3] offset:32\n\t" "v_mfma_f32_16x16x32_bf16 v[48:51], a[156:159], a[112:115], v[48:51]\n\t"
    "ds_read_b64_tr_b16 a[138:139], %[vr3] offset:3616\n\t" "v_mfma_f32_16x16x32_bf16 a[164:167], a[152:155], a[192:195], a[164:167]\n\t"
    "v_mfma_f32_16x16x32_bf16 v[52:55], a[152:155], a[100:103], v[52:55]\n\t" "v_mfma_f32_16x16x32_bf16 v[52:55], a[156:159], a[116:119], v[52:55]\n\t"
    "v_mfma_f32_16x16x32_bf16 v[56:59], a[152:155], a[104:107], v[56:59]\n\t" "ds_read_b64_tr_b16 a[140:141], %[vr3] offset:14368\n\t"
    "v_mfma_f32_16x16x32_bf16 v[56:59], a[156:159], a[120:123], v[56:59]\n\t" "ds_read_b64_tr_b16 a[142:143], %[vr3] offset:17952\n\t"
    "v_mfma_f32_16x16x32_bf16 a[164:167], a[156:159], a[196:199], a[164:167]\n\t" "v_mfma_f32_16x16x32_bf16 v[60:63], a[152:155], a[108:111], v[60:63]\n\t"
    "global_load_dwordx4 a[36:39], %[va], s[76:77] offset:1024 sc1\n\t" "v_mfma_f32_16x16x32_bf16 v[60:63], a[156:159], a[124:127], v[60:63]\n\t" "s_waitcnt lgkmcnt(4)\n\t"
    "v_mfma_f32_16x16x32_bf16 v[64:67], a[128:131], a[96:99], v[64:67]\n\t" "ds_read_b64_tr_b16 a[144:145], %[vr5] offset:0\n\t" "v_mfma_f32_16x16x32_bf16 v[64:67], a[132:135], a[112:115], v[64:67]\n\t"
    "ds_read_b64_tr_b16 a[146:147], %[vr5] offset:3584\n\t" "s_add_u32 m0, %[sl], 15360\n\t" "v_mfma_f32_16x16x32_bf16 v[68:71], a[128:131], a[100:103], v[68:71]\n\t"
    "global_load_lds_dwordx4 %[vb1], s[78:79]\n\t" "v_mfma_f32_16x16x32_bf16 v[68:71], a[132:135], a[116:119], v[68:71]\n\t" "v_mfma_f32_16x16x32_bf16 v[72:75], a[128:131], a[104:107], v[72:75]\n\t"
    "ds_read_b64_tr_b16 a[148:149], %[vr5] offset:14336\n\t" "v_mfma_f32_16x16x32_bf16 v[72:75], a[132:135], a[120:123], v[72:75]\n\t" "ds_read_b64_tr_b16 a[150:151], %[vr5] offset:17920\n\t"
    "v_mfma_f32_16x16x32_bf16 v[76:79], a[128:131], a[108:111], v[76:79]\n\t" "v_mfma_f32_16x16x32_bf16 v[76:79], a[132:135], a[124:127], v[76:79]\n\t" "s_waitcnt lgkmcnt(4)\n\t"
    "v_mfma_f32_16x16x32_bf16 v[80:83], a[136:139], a[96:99], v[80:83]\n\t" "ds_read_b64_tr_b16 a[152:153], %[vr5] offset:32\n\t" "v_mfma_f32_16x16x32_bf16 v[80:83], a[140:143], a[112:115], v[80:83]\n\t"
    "ds_read_b64_tr_b16 a[154:155], %[vr5] offset:3616\n\t" "v_mfma_f32_16x16x32_bf16 v[84:87], a[136:139], a[100:103], v[84:87]\n\t" "global_load_dwordx4 a[40:43], %[va], s[76:77] offset:2048 sc1\n\t"
    "v_mfma_f32_16x16x32_bf16 v[84:87], a[140:143], a[116:119], v[84:87]\n\t" "v_mfma_f32_16x16x32_bf16 v[88:91], a[136:139], a[104:107], v[88:91]\n\t"
    "ds_read_b64_tr_b16 a[156:157], %[vr5] offset:14368\n\t" "v_mfma_f32_16x16x32_bf16 v[88:91], a[140:143], a[120:123], v[88:91]\n\t" "ds_read_b64_tr_b16 a[158:159], %[vr5] offset:17952\n\t"
    "v_mfma_f32_16x16x32_bf16 v[92:95], a[136:139], a[108:111], v[92:95]\n\t" "v_mfma_f32_16x16x32_bf16 v[92:95], a[140:143], a[124:127], v[92:95]\n\t" "s_waitcnt lgkmcnt(4)\n\t"
    "v_mfma_f32_16x16x32_bf16 v[96:99], a[144:147], a[96:99], v[96:99]\n\t" "ds_read_b64_tr_b16 a[128:129], %[vr7] offset:0\n\t" "v_mfma_f32_16x16x32_bf16 v[96:99], a[148:151], a[112:115], v[96:99]\n\t"
    "ds_read_b64_tr_b16 a[130:131], %[vr7] offset:3584\n\t" "s_add_u32 m0, %[sl], 16384\n\t" "v_mfma_f32_16x16x32_bf16 v[100:103], a[144:147], a[100:103], v[100:103]\n\t"
    "global_load_lds_dwordx4 %[vb2], s[78:79]\n\t" "v_mfma_f32_16x16x32_bf16 v[100:103], a[148:151], a[116:119], v[100:103]\n\t"
    "v_mfma_f32_16x16x32_bf16 v[104:107], a[144:147], a[104:107], v[104:107]\n\t" "ds_read_b64_tr_b16 a[132:133], %[vr7] offset:14336\n\t"
    "v_mfma_f32_16x16x32_bf16 v[104:107], a[148:151], a[120:123], v[104:107]\n\t" "ds_read_b64_tr_b16 a[134:135], %[vr7] offset:17920\n\t"
    "v_mfma_f32_16x16x32_bf16 v[108:111], a[144:147], a[108:111], v[108:111]\n\t" "global_load_dwordx4 a[44:47], %[va], s[76:77] offset:3072 sc1\n\t"
    "v_mfma_f32_16x16x32_bf16 v[108:111], a[148:151], a[124:127], v[108:111]\n\t" "s_add_u32 s76, s76, 16384\n\t" "s_addc_u32 s77, s77, 0\n\t" "s_waitcnt lgkmcnt(4)\n\t"
    "v_mfma_f32_16x16x32_bf16 v[112:115], a[152:155], a[96:99], v[112:115]\n\t" "ds_read_b64_tr_b16 a[136:137], %[vr1] offset:7168\n\t"
    "v_mfma_f32_16x16x32_bf16 v[112:115], a[156:159], a[112:115], v[112:115]\n\t" "ds_read_b64_tr_b16 a[138:139], %[vr1] offset:10752\n\t"
    "v_mfma_f32_16x16x32_bf16 v[116:119], a[152:155], a[100:103], v[116:119]\n\t" "v_mfma_f32_16x16x32_bf16 v[116:119], a[156:159], a[116:119], v[116:119]\n\t"
    "v_mfma_f32_16x16x32_bf16 v[120:123], a[152:155], a[104:107], v[120:123]\n\t" "ds_read_b64_tr_b16 a[140:141], %[vr1] offset:21504\n\t"
    "v_mfma_f32_16x16x32_bf16 v[120:123], a[156:159], a[120:123], v[120:123]\n\t" "ds_read_b64_tr_b16 a[142:143], %[vr1] offset:25088\n\t" "s_add_u32 m0, %[sl], 17408\n\t"
    "v_mfma_f32_16x16x32_bf16 v[124:127], a[152:155], a[108:111], v[124:127]\n\t" "s_mov_b64 exec, s[82:83]\n\t" "global_load_lds_dwordx4 %[vb3], s[78:79]\n\t" "s_mov_b64 exec, -1\n\t"
    "v_mfma_f32_16x16x32_bf16 v[124:127], a[156:159], a[124:127], v[124:127]\n\t" "s_add_u32 s78, s78, 229376\n\t" "s_addc_u32 s79, s79, 0\n\t" "s_waitcnt lgkmcnt(4)\n\t"
    "v_mfma_f32_16x16x32_bf16 v[128:131], a[128:131], a[96:99], v[128:131]\n\t" "ds_read_b64_tr_b16 a[144:145], %[vr1] offset:7200\n\t"
    "v_mfma_f32_16x16x32_bf16 v[128:131], a[132:135], a[112:115], v[128:131]\n\t" "ds_read_b64_tr_b16 a[146:147], %[vr1] offset:10784\n\t"
    "v_mfma_f32_16x16x32_bf16 a[168:171], a[128:131], a[192:195], a[168:171]\n\t" "v_mfma_f32_16x16x32_bf16 v[132:135], a[128:131], a[100:103], v[132:135]\n\t"
    "v_mfma_f32_16x16x32_bf16 v[132:135], a[132:135], a[116:119], v[132:135]\n\t" "v_mfma_f32_16x16x32_bf16 v[136:139], a[128:131], a[104:107], v[136:139]\n\t"
    "ds_read_b64_tr_b16 a[148:149], %[vr1] offset:21536\n\t" "v_mfma_f32_16x16x32_bf16 v[136:139], a[132:135], a[120:123], v[136:139]\n\t" "ds_read_b64_tr_b16 a[150:151], %[vr1] offset:25120\n\t"
    "v_mfma_f32_16x16x32_bf16 a[168:171], a[132:135], a[196:199], a[168:171]\n\t" "v_mfma_f32_16x16x32_bf16 v[140:143], a[128:131], a[108:111], v[140:143]\n\t"
    "v_mfma_f32_16x16x32_bf16 v[140:143], a[132:135], a[124:127], v[140:143]\n\t" "s_waitcnt lgkmcnt(4)\n\t" "v_mfma_f32_16x16x32_bf16 v[144:147], a[136:139], a[96:99], v[144:147]\n\t"
    "ds_read_b64_tr_b16 a[152:153], %[vr3] offset:7168\n\t" "v_mfma_f32_16x16x32_bf16 v[144:147], a[140:143], a[112:115], v[144:147]\n\t" "ds_read_b64_tr_b16 a[154:155], %[vr3] offset:10752\n\t"
    "v_mfma_f32_16x16x32_bf16 a[172:175], a[136:139], a[192:195], a[172:175]\n\t" "v_mfma_f32_16x16x32_bf16 v[148:151], a[136:139], a[100:103], v[148:151]\n\t"
    "global_load_dwordx4 a[48:51], %[va], s[76:77] offset:0 sc1\n\t" "v_mfma_f32_16x16x32_bf16 v[148:151], a[140:143], a[116:119], v[148:151]\n\t"
    "v_mfma_f32_16x16x32_bf16 v[152:155], a[136:139], a[104:107], v[152:155]\n\t" "ds_read_b64_tr_b16 a[156:157], %[vr3] offset:21504\n\t"
    "v_mfma_f32_16x16x32_bf16 v[152:155], a[140:143], a[120:123], v[152:155]\n\t" "ds_read_b64_tr_b16 a[158:159], %[vr3] offset:25088\n\t"
    "v_mfma_f32_16x16x32_bf16 a[172:175], a[140:143], a[196:199], a[172:175]\n\t" "s_add_u32 m0, %[sl], 28672\n\t" "v_mfma_f32_16x16x32_bf16 v[156:159], a[136:139], a[108:111], v[156:159]\n\t"
    "global_load_lds_dwordx4 %[vb0], s[78:79]\n\t" "v_mfma_f32_16x16x32_bf16 v[156:159], a[140:143], a[124:127], v[156:159]\n\t" "s_waitcnt lgkmcnt(4)\n\t"
    "v_mfma_f32_16x16x32_bf16 v[160:163], a[144:147], a[96:99], v[160:163]\n\t" "ds_read_b64_tr_b16 a[128:129], %[vr3] offset:7200\n\t"
    "v_mfma_f32_16x16x32_bf16 v[160:163], a[148:151], a[112:115], v[160:163]\n\t" "ds_read_b64_tr_b16 a[130:131], %[vr3] offset:10784\n\t"
    "v_mfma_f32_16x16x32_bf16 a[176:179], a[144:147], a[192:195], a[176:179]\n\t" "v_mfma_f32_16x16x32_bf16 v[164:167], a[144:147], a[100:103], v[164:167]\n\t"
    "v_mfma_f32_16x16x32_bf16 v[164:167], a[148:151], a[116:119], v[164:167]\n\t" "v_mfma_f32_16x16x32_bf16 v[168:171], a[144:147], a[104:107], v[168:171]\n\t"
    "ds_read_b64_tr_b16 a[132:133], %[vr3] offset:21536\n\t" "v_mfma_f32_16x16x32_bf16 v[168:171], a[148:151], a[120:123], v[168:171]\n\t" "ds_read_b64_tr_b16 a[134:135], %[vr3] offset:25120\n\t"
    "v_mfma_f32_16x16x32_bf16 a[176:179], a[148:151], a[196:199], a[176:179]\n\t" "v_mfma_f32_16x16x32_bf16 v[172:175], a[144:147], a[108:111], v[172:175]\n\t"
    "global_load_dwordx4 a[52:55], %[va], s[76:77] offset:1024 sc1\n\t" "v_mfma_f32_16x16x32_bf16 v[172:175], a[148:151], a[124:127], v[172:175]\n\t" "s_waitcnt lgkmcnt(4)\n\t"
    "v_mfma_f32_16x16x32_bf16 v[176:179], a[152:155], a[96:99], v[176:179]\n\t" "ds_read_b64_tr_b16 a[136:137], %[vr5] offset:7168\n\t"
    "v_mfma_f32_16x16x32_bf16 v[176:179], a[156:159], a[112:115], v[176:179]\n\t" "ds_read_b64_tr_b16 a[138:139], %[vr5] offset:10752\n\t" "s_add_u32 m0, %[sl], 29696\n\t"
    "v_mfma_f32_16x16x32_bf16 v[180:183], a[152:155], a[100:103], v[180:183]\n\t" "global_load_lds_dwordx4 %[vb1], s[78:79]\n\t"
    "v_mfma_f32_16x16x32_bf16 v[180:183], a[156:159], a[116:119], v[180:183]\n\t" "v_mfma_f32_16x16x32_bf16 v[184:187], a[152:155], a[104:107], v[184:187]\n\t"
    "ds_read_b64_tr_b16 a[140:141], %[vr5] offset:21504\n\t" "v_mfma_f32_16x16x32_bf16 v[184:187], a[156:159], a[120:123], v[184:187]\n\t" "ds_read_b64_tr_b16 a[142:143], %[vr5] offset:25088\n\t"
    "v_mfma_f32_16x16x32_bf16 v[188:191], a[152:155], a[108:111], v[188:191]\n\t" "v_mfma_f32_16x16x32_bf16 v[188:191], a[156:159], a[124:127], v[188:191]\n\t" "s_waitcnt lgkmcnt(4)\n\t"
    "v_mfma_f32_16x16x32_bf16 v[192:195], a[128:131], a[96:99], v[192:195]\n\t" "ds_read_b64_tr_b16 a[144:145], %[vr5] offset:7200\n\t"
    "v_mfma_f32_16x16x32_bf16 v[192:195], a[132:135], a[112:115], v[192:195]\n\t" "ds_read_b64_tr_b16 a[146:147], %[vr5] offset:10784\n\t"
    "v_mfma_f32_16x16x32_bf16 v[196:199], a[128:131], a[100:103], v[196:199]\n\t" "global_load_dwordx4 a[56:59], %[va], s[76:77] offset:2048 sc1\n\t"
    "v_mfma_f32_16x16x32_bf16 v[196:199], a[132:135], a[116:119], v[196:199]\n\t" "v_mfma_f32_16x16x32_bf16 v[200:203], a[128:131], a[104:107], v[200:203]\n\t"
    "ds_read_b64_tr_b16 a[148:149], %[vr5] offset:21536\n\t" "v_mfma_f32_16x16x32_bf16 v[200:203], a[132:135], a[120:123], v[200:203]\n\t" "ds_read_b64_tr_b16 a[150:151], %[vr5] offset:25120\n\t"
    "v_mfma_f32_16x16x32_bf16 v[204:207], a[128:131], a[108:111], v[204:207]\n\t" "v_mfma_f32_16x16x32_bf16 v[204:207], a[132:135], a[124:127], v[204:207]\n\t" "s_waitcnt lgkmcnt(4)\n\t"
    "v_mfma_f32_16x16x32_bf16 v[208:211], a[136:139], a[96:99], v[208:211]\n\t" "ds_read_b64_tr_b16 a[152:153], %[vr7] offset:7168\n\t"
    "v_mfma_f32_16x16x32_bf16 v[208:211], a[140:143], a[112:115], v[208:211]\n\t" "ds_read_b64_tr_b16 a[154:155], %[vr7] offset:10752\n\t" "s_add_u32 m0, %[sl], 30720\n\t"
    "v_mfma_f32_16x16x32_bf16 v[212:215], a[136:139], a[100:103], v[212:215]\n\t" "global_load_lds_dwordx4 %[vb2], s[78:79]\n\t"
    "v_mfma_f32_16x16x32_bf16 v[212:215], a[140:143], a[116:119], v[212:215]\n\t" "v_mfma_f32_16x16x32_bf16 v[216:219], a[136:139], a[104:107], v[216:219]\n\t"
    "ds_read_b64_tr_b16 a[156:157], %[vr7] offset:21504\n\t" "v_mfma_f32_16x16x32_bf16 v[216:219], a[140:143], a[120:123], v[216:219]\n\t" "ds_read_b64_tr_b16 a[158:159], %[vr7] offset:25088\n\t"
    "v_mfma_f32_16x16x32_bf16 v[220:223], a[136:139], a[108:111], v[220:223]\n\t" "global_load_dwordx4 a[60:63], %[va], s[76:77] offset:3072 sc1\n\t"
    "v_mfma_f32_16x16x32_bf16 v[220:223], a[140:143], a[124:127], v[220:223]\n\t" "s_add_u32 s76, s76, 16384\n\t" "s_addc_u32 s77, s77, 0\n\t" "s_waitcnt lgkmcnt(4)\n\t"
    "v_mfma_f32_16x16x32_bf16 v[224:227], a[144:147], a[96:99], v[224:227]\n\t" "s_waitcnt vmcnt(24)\n\t" "s_barrier\n\t" "ds_read_b64_tr_b16 a[128:129], %[vr1] offset:28672\n\t"
    "v_mfma_f32_16x16x32_bf16 v[224:227], a[148:151], a[112:115], v[224:227]\n\t" "ds_read_b64_tr_b16 a[130:131], %[vr1] offset:32256\n\t"
    "v_mfma_f32_16x16x32_bf16 v[228:231], a[144:147], a[100:103], v[228:231]\n\t" "v_mfma_f32_16x16x32_bf16 v[228:231], a[148:151], a[116:119], v[228:231]\n\t"
    "v_mfma_f32_16x16x32_bf16 v[232:235], a[144:147], a[104:107], v[232:235]\n\t" "ds_read_b64_tr_b16 a[132:133], %[vr1] offset:43008\n\t"
    "v_mfma_f32_16x16x32_bf16 v[232:235], a[148:151], a[120:123], v[232:235]\n\t" "ds_read_b64_tr_b16 a[134:135], %[vr1] offset:46592\n\t" "s_add_u32 m0, %[sl], 31744\n\t"
    "v_mfma_f32_16x16x32_bf16 v[236:239], a[144:147], a[108:111], v[236:239]\n\t" "s_mov_b64 exec, s[82:83]\n\t" "global_load_lds_dwordx4 %[vb3], s[78:79]\n\t" "s_mov_b64 exec, -1\n\t"
    "v_mfma_f32_16x16x32_bf16 v[236:239], a[148:151], a[124:127], v[236:239]\n\t" "s_add_u32 s78, s78, 229376\n\t" "s_addc_u32 s79, s79, 0\n\t" "s_waitcnt lgkmcnt(4)\n\t"
    "v_mfma_f32_16x16x32_bf16 v[240:243], a[152:155], a[96:99], v[240:243]\n\t" "ds_read_b64_tr_b16 a[136:137], %[vr1] offset:28704\n\t"
    "v_mfma_f32_16x16x32_bf16 v[240:243], a[156:159], a[112:115], v[240:243]\n\t" "ds_read_b64_tr_b16 a[138:139], %[vr1] offset:32288\n\t"
    "v_mfma_f32_16x16x32_bf16 a[180:183], a[152:155], a[192:195], a[180:183]\n\t" "v_mfma_f32_16x16x32_bf16 v[244:247], a[152:155], a[100:103], v[244:247]\n\t"
    "v_mfma_f32_16x16x32_bf16 v[244:247], a[156:159], a[116:119], v[244:247]\n\t" "v_mfma_f32_16x16x32_bf16 v[248:251], a[152:155], a[104:107], v[248:251]\n\t"
    "ds_read_b64_tr_b16 a[140:141], %[vr1] offset:43040\n\t" "v_mfma_f32_16x16x32_bf16 v[248:251], a[156:159], a[120:123], v[248:251]\n\t" "ds_read_b64_tr_b16 a[142:143], %[vr1] offset:46624\n\t"
    "v_mfma_f32_16x16x32_bf16 a[180:183], a[156:159], a[196:199], a[180:183]\n\t" "v_mfma_f32_16x16x32_bf16 v[252:255], a[152:155], a[108:111], v[252:255]\n\t"
    "v_mfma_f32_16x16x32_bf16 v[252:255], a[156:159], a[124:127], v[252:255]\n\t" "v_accvgpr_read_b32 v31, %[cp]\n\t" "s_nop 7\n\t" "s_mov_b64 exec, %[m01]\n\t"
    "global_store_dwordx4 v31, a[160:163], %[su0] offset:0 sc1\n\t" "global_store_dwordx4 v31, a[164:167], %[su0] offset:2304 sc1\n\t" "global_store_dwordx4 v31, a[172:175], %[su0] offset:1152 sc1\n\t"
    "global_store_dwordx4 v31, a[176:179], %[su0] offset:3456 sc1\n\t" "s_mov_b64 exec, %[m6]\n\t" "global_store_dwordx4 v31, a[168:171], %[su6] offset:0 sc1\n\t"
    "global_store_dwordx4 v31, a[180:183], %[su6] offset:1152 sc1\n\t" "s_mov_b64 exec, -1\n\t" "s_branch .Ld1_%=\n\t" ".Lslow0_%=:\n\t" "s_sleep 2\n\t" "v_accvgpr_read_b32 v31, %[cr0]\n\t"
    "s_mov_b64 s[96:97], 0\n\t" "v_subrev_u32_e64 v31, %[rb], v31\n\t" "global_load_dword v24, v31, %[su] offset:0 sc1\n\t" "global_load_dword v25, v31, %[su] offset:16 sc1\n\t"
    "global_load_dword v26, v31, %[su] offset:32 sc1\n\t" "global_load_dword v27, v31, %[su] offset:48 sc1\n\t" "global_load_dword v28, v31, %[su] offset:64 sc1\n\t"
    "global_load_dword v29, v31, %[su] offset:80 sc1\n\t" "global_load_dword v30, v31, %[su] offset:96 sc1\n\t" "global_load_dword v31, v31, %[su] offset:112 sc1\n\t" "s_waitcnt vmcnt(0)\n\t"
    "v_cmp_eq_u32 vcc, -1, v24\n\t" "s_nop 1\n\t" "s_or_b64 s[96:97], s[96:97], vcc\n\t" "v_cmp_eq_u32 vcc, -1, v25\n\t" "s_nop 1\n\t" "s_or_b64 s[96:97], s[96:97], vcc\n\t"
    "v_cmp_eq_u32 vcc, -1, v26\n\t" "s_nop 1\n\t" "s_or_b64 s[96:97], s[96:97], vcc\n\t" "v_cmp_eq_u32 vcc, -1, v27\n\t" "s_nop 1\n\t" "s_or_b64 s[96:97], s[96:97], vcc\n\t"
    "v_cmp_eq_u32 vcc, -1, v28\n\t" "s_nop 1\n\t" "s_or_b64 s[96:97], s[96:97], vcc\n\t" "v_cmp_eq_u32 vcc, -1, v29\n\t" "s_nop 1\n\t" "s_or_b64 s[96:97], s[96:97], vcc\n\t"
    "v_cmp_eq_u32 vcc, -1, v30\n\t" "s_nop 1\n\t" "s_or_b64 s[96:97], s[96:97], vcc\n\t" "v_cmp_eq_u32 vcc, -1, v31\n\t" "s_nop 1\n\t" "s_or_b64 s[96:97], s[96:97], vcc\n\t" "v_add_f32 v24, v24, v25\n\t"
    "v_add_f32 v26, v26, v27\n\t" "v_add_f32 v28, v28, v29\n\t" "v_add_f32 v30, v30, v31\n\t" "v_add_f32 v24, v24, v26\n\t" "v_add_f32 v28, v28, v30\n\t" "v_add_f32_e64 v24, v24, v28\n\t"
    "s_cbranch_scc0 .Lgo0_%=\n\t" "s_sub_u32 s94, s94, 1\n\t" "s_cbranch_scc0 .Lslow0_%=\n\t" "s_branch .Lgo0_%=\n\t" ".Lslow1_%=:\n\t" "s_sleep 2\n\t" "v_accvgpr_read_b32 v31, %[cr1]\n\t"
    "s_mov_b64 s[96:97], 0\n\t" "v_subrev_u32 v31, %[rb], v31\n\t" "global_load_dword v24, v31, %[su] offset:0 sc1\n\t" "global_load_dword v25, v31, %[su] offset:16 sc1\n\t"
    "global_load_dword v26, v31, %[su] offset:32 sc1\n\t" "global_load_dword v27, v31, %[su] offset:48 sc1\n\t" "global_load_dword v28, v31, %[su] offset:64 sc1\n\t"
    "global_load_dword v29, v31, %[su] offset:80 sc1\n\t" "global_load_dword v30, v31, %[su] offset:96 sc1\n\t" "global_load_dword v31, v31, %[su] offset:112 sc1\n\t" "s_waitcnt vmcnt(0)\n\t"
    "v_cmp_eq_u32 vcc, -1, v24\n\t" "s_nop 1\n\t" "s_or_b64 s[96:97], s[96:97], vcc\n\t" "v_cmp_eq_u32 vcc, -1, v25\n\t" "s_nop 1\n\t" "s_or_b64 s[96:97], s[96:97], vcc\n\t"
    "v_cmp_eq_u32 vcc, -1, v26\n\t" "s_nop 1\n\t" "s_or_b64 s[96:97], s[96:97], vcc\n\t" "v_cmp_eq_u32 vcc, -1, v27\n\t" "s_nop 1\n\t" "s_or_b64 s[96:97], s[96:97], vcc\n\t"
    "v_cmp_eq_u32 vcc, -1, v28\n\t" "s_nop 1\n\t" "s_or_b64 s[96:97], s[96:97], vcc\n\t" "v_cmp_eq_u32 vcc, -1, v29\n\t" "s_nop 1\n\t" "s_or_b64 s[96:97], s[96:97], vcc\n\t"
    "v_cmp_eq_u32 vcc, -1, v30\n\t" "s_nop 1\n\t" "s_or_b64 s[96:97], s[96:97], vcc\n\t" "v_cmp_eq_u32 vcc, -1, v31\n\t" "s_nop 1\n\t" "s_or_b64 s[96:97], s[96:97], vcc\n\t" "v_add_f32 v24, v24, v25\n\t"
    "v_add_f32 v26, v26, v27\n\t" "v_add_f32 v28, v28, v29\n\t" "v_add_f32 v30, v30, v31\n\t" "v_add_f32 v24, v24, v26\n\t" "v_add_f32 v28, v28, v30\n\t" "v_add_f32_e64 v24, v24, v28\n\t"
    "s_cbranch_scc0 .Lgo1_%=\n\t" "s_sub_u32 s94, s94, 1\n\t" "s_cbranch_scc0 .Lslow1_%=\n\t" "s_branch .Lgo1_%=\n\t" ".Ltail_%=:\n\t" "s_cmp_eq_u32 s80, 1\n\t" "s_cselect_b32 s86, %[wa], 16384\n\t"
    "s_ashr_i32 s87, s86, 31\n\t" "s_cmp_eq_u32 s80, 1\n\t" "s_cselect_b32 s88, %[wb], 229376\n\t" "s_ashr_i32 s89, s88, 31\n\t" "s_waitcnt lgkmcnt(4) vmcnt(17)\n\t"
    "v_mfma_f32_16x16x32_bf16 v[32:35], a[128:131], a[64:67], v[32:35]\n\t" "s_barrier\n\t" "ds_read_b64_tr_b16 a[144:145], %[vr2] offset:28672\n\t" "v_accvgpr_read_b32 v24, a216\n\t"
    "v_mfma_f32_16x16x32_bf16 v[32:35], a[132:135], a[80:83], v[32:35]\n\t" "ds_read_b64_tr_b16 a[146:147], %[vr2] offset:32256\n\t" "v_accvgpr_read_b32 v25, a217\n\t"
    "v_mfma_f32_16x16x32_bf16 v[36:39], a[128:131], a[68:71], v[36:39]\n\t" "global_load_dwordx4 a[0:3], %[va], s[76:77] offset:0 sc1\n\t" "v_accvgpr_read_b32 v28, a218\n\t"
    "v_mfma_f32_16x16x32_bf16 v[36:39], a[132:135], a[84:87], v[36:39]\n\t" "v_accvgpr_read_b32 v29, a219\n\t" "v_mfma_f32_16x16x32_bf16 v[40:43], a[128:131], a[72:75], v[40:43]\n\t"
    "ds_read_b64_tr_b16 a[148:149], %[vr2] offset:43008\n\t" "v_accvgpr_read_b32 v26, a224\n\t" "v_mfma_f32_16x16x32_bf16 v[40:43], a[132:135], a[88:91], v[40:43]\n\t"
    "ds_read_b64_tr_b16 a[150:151], %[vr2] offset:46592\n\t" "v_accvgpr_read_b32 v27, a225\n\t" "s_add_u32 m0, %[sl], 100352\n\t" "v_mfma_f32_16x16x32_bf16 v[44:47], a[128:131], a[76:79], v[44:47]\n\t"
    "global_load_lds_dwordx4 %[vb0], s[78:79]\n\t" "v_accvgpr_read_b32 v30, a226\n\t" "v_mfma_f32_16x16x32_bf16 v[44:47], a[132:135], a[92:95], v[44:47]\n\t" "v_accvgpr_read_b32 v31, a227\n\t"
    "s_waitcnt lgkmcnt(4)\n\t" "v_mfma_f32_16x16x32_bf16 v[48:51], a[136:139], a[64:67], v[48:51]\n\t" "ds_read_b64_tr_b16 a[152:153], %[vr2] offset:28704\n\t" "v_add_f32_e64 v24, v24, v26\n\t"
    "v_mfma_f32_16x16x32_bf16 v[48:51], a[140:143], a[80:83], v[48:51]\n\t" "ds_read_b64_tr_b16 a[154:155], %[vr2] offset:32288\n\t" "v_add_f32_e64 v25, v25, v27\n\t"
    "v_mfma_f32_16x16x32_bf16 v[52:55], a[136:139], a[68:71], v[52:55]\n\t" "global_load_dwordx4 a[4:7], %[va], s[76:77] offset:1024 sc1\n\t" "v_add_f32_e64 v28, v28, v30\n\t"
    "v_mfma_f32_16x16x32_bf16 v[52:55], a[140:143], a[84:87], v[52:55]\n\t" "v_add_f32_e64 v29, v29, v31\n\t" "v_mfma_f32_16x16x32_bf16 v[56:59], a[136:139], a[72:75], v[56:59]\n\t"
    "ds_read_b64_tr_b16 a[156:157], %[vr2] offset:43040\n\t" "v_accvgpr_read_b32 v26, a232\n\t" "v_mfma_f32_16x16x32_bf16 v[56:59], a[140:143], a[88:91], v[56:59]\n\t"
    "ds_read_b64_tr_b16 a[158:159], %[vr2] offset:46624\n\t" "v_accvgpr_read_b32 v27, a233\n\t" "s_add_u32 m0, %[sl], 101376\n\t" "v_mfma_f32_16x16x32_bf16 v[60:63], a[136:139], a[76:79], v[60:63]\n\t"
    "global_load_lds_dwordx4 %[vb1], s[78:79]\n\t" "v_accvgpr_read_b32 v30, a234\n\t" "v_mfma_f32_16x16x32_bf16 v[60:63], a[140:143], a[92:95], v[60:63]\n\t" "v_accvgpr_read_b32 v31, a235\n\t"
    "s_waitcnt lgkmcnt(4)\n\t" "v_mfma_f32_16x16x32_bf16 v[64:67], a[144:147], a[64:67], v[64:67]\n\t" "ds_read_b64_tr_b16 a[128:129], %[vr4] offset:28672\n\t" "v_add_f32 v24, v24, v26\n\t"
    "v_mfma_f32_16x16x32_bf16 v[64:67], a[148:151], a[80:83], v[64:67]\n\t" "ds_read_b64_tr_b16 a[130:131], %[vr4] offset:32256\n\t" "v_add_f32_e64 v25, v25, v27\n\t"
    "v_mfma_f32_16x16x32_bf16 v[68:71], a[144:147], a[68:71], v[68:71]\n\t" "v_add_f32_e64 v28, v28, v30\n\t" "v_mfma_f32_16x16x32_bf16 v[68:71], a[148:151], a[84:87], v[68:71]\n\t"
    "v_add_f32_e64 v29, v29, v31\n\t" "v_mfma_f32_16x16x32_bf16 v[72:75], a[144:147], a[72:75], v[72:75]\n\t" "ds_read_b64_tr_b16 a[132:133], %[vr4] offset:43008\n\t" "v_accvgpr_read_b32 v26, a240\n\t"
    "v_mfma_f32_16x16x32_bf16 v[72:75], a[148:151], a[88:91], v[72:75]\n\t" "ds_read_b64_tr_b16 a[134:135], %[vr4] offset:46592\n\t" "v_accvgpr_read_b32 v27, a241\n\t"
    "v_mfma_f32_16x16x32_bf16 v[76:79], a[144:147], a[76:79], v[76:79]\n\t" "global_load_dwordx4 a[8:11], %[va], s[76:77] offset:2048 sc1\n\t" "v_accvgpr_read_b32 v30, a242\n\t"
    "v_mfma_f32_16x16x32_bf16 v[76:79], a[148:151], a[92:95], v[76:79]\n\t" "v_accvgpr_read_b32 v31, a243\n\t" "s_waitcnt lgkmcnt(4)\n\t"
    "v_mfma_f32_16x16x32_bf16 v[80:83], a[152:155], a[64:67], v[80:83]\n\t" "ds_read_b64_tr_b16 a[136:137], %[vr4] offset:28704\n\t" "v_add_f32 v24, v24, v26\n\t"
    "v_mfma_f32_16x16x32_bf16 v[80:83], a[156:159], a[80:83], v[80:83]\n\t" "ds_read_b64_tr_b16 a[138:139], %[vr4] offset:32288\n\t" "v_add_f32_e64 v25, v25, v27\n\t" "s_add_u32 m0, %[sl], 102400\n\t"
    "v_mfma_f32_16x16x32_bf16 v[84:87], a[152:155], a[68:71], v[84:87]\n\t" "global_load_lds_dwordx4 %[vb2], s[78:79]\n\t" "v_add_f32_e64 v28, v28, v30\n\t"
    "v_mfma_f32_16x16x32_bf16 v[84:87], a[156:159], a[84:87], v[84:87]\n\t" "v_add_f32_e64 v29, v29, v31\n\t" "v_mfma_f32_16x16x32_bf16 v[88:91], a[152:155], a[72:75], v[88:91]\n\t"
    "ds_read_b64_tr_b16 a[140:141], %[vr4] offset:43040\n\t" "v_mov_b32_e64 v26, v24\n\t" "v_mfma_f32_16x16x32_bf16 v[88:91], a[156:159], a[88:91], v[88:91]\n\t"
    "ds_read_b64_tr_b16 a[142:143], %[vr4] offset:46624\n\t" "v_mov_b32_e64 v27, v25\n\t" "v_mfma_f32_16x16x32_bf16 v[92:95], a[152:155], a[76:79], v[92:95]\n\t"
    "global_load_dwordx4 a[12:15], %[va], s[76:77] offset:3072 sc1\n\t" "v_mov_b32_e64 v30, v28\n\t" "v_mfma_f32_16x16x32_bf16 v[92:95], a[156:159], a[92:95], v[92:95]\n\t" "s_add_u32 s76, s76, 16384\n\t"
    "s_addc_u32 s77, s77, 0\n\t" "v_mov_b32_e64 v31, v29\n\t" "s_waitcnt lgkmcnt(4)\n\t" "v_mfma_f32_16x16x32_bf16 v[96:99], a[128:131], a[64:67], v[96:99]\n\t"
    "ds_read_b64_tr_b16 a[144:145], %[vr6] offset:28672\n\t" "s_nop 0\n\t" "v_mfma_f32_16x16x32_bf16 v[96:99], a[132:135], a[80:83], v[96:99]\n\t" "ds_read_b64_tr_b16 a[146:147], %[vr6] offset:32256\n\t"
    "s_nop 0\n\t" "v_mfma_f32_16x16x32_bf16 v[100:103], a[128:131], a[68:71], v[100:103]\n\t" "v_permlane16_swap_b32 v24, v26\n\t"
    "v_mfma_f32_16x16x32_bf16 v[100:103], a[132:135], a[84:87], v[100:103]\n\t" "v_permlane16_swap_b32 v25, v27\n\t" "v_mfma_f32_16x16x32_bf16 v[104:107], a[128:131], a[72:75], v[104:107]\n\t"
    "ds_read_b64_tr_b16 a[148:149], %[vr6] offset:43008\n\t" "v_permlane16_swap_b32 v28, v30\n\t" "v_mfma_f32_16x16x32_bf16 v[104:107], a[132:135], a[88:91], v[104:107]\n\t"
    "ds_read_b64_tr_b16 a[150:151], %[vr6] offset:46592\n\t" "v_permlane16_swap_b32 v29, v31\n\t" "s_add_u32 m0, %[sl], 103424\n\t"
    "v_mfma_f32_16x16x32_bf16 v[108:111], a[128:131], a[76:79], v[108:111]\n\t" "s_mov_b64 exec, s[82:83]\n\t" "global_load_lds_dwordx4 %[vb3], s[78:79]\n\t" "s_mov_b64 exec, -1\n\t"
    "v_add_f32_e64 v24, v24, v26\n\t" "v_mfma_f32_16x16x32_bf16 v[108:111], a[132:135], a[92:95], v[108:111]\n\t" "s_add_u32 s78, s78, 229376\n\t" "s_addc_u32 s79, s79, 0\n\t"
    "v_add_f32_e64 v25, v25, v27\n\t" "s_waitcnt lgkmcnt(4)\n\t" "v_mfma_f32_16x16x32_bf16 v[112:115], a[136:139], a[64:67], v[112:115]\n\t" "ds_read_b64_tr_b16 a[152:153], %[vr0] offset:35840\n\t"
    "v_add_f32_e64 v28, v28, v30\n\t" "v_mfma_f32_16x16x32_bf16 v[112:115], a[140:143], a[80:83], v[112:115]\n\t" "ds_read_b64_tr_b16 a[154:155], %[vr0] offset:39424\n\t" "v_add_f32_e64 v29, v29, v31\n\t"
    "v_mfma_f32_16x16x32_bf16 v[116:119], a[136:139], a[68:71], v[116:119]\n\t" "global_load_dwordx4 a[16:19], %[va], s[76:77] offset:0 sc1\n\t" "v_mov_b32_e64 v26, v24\n\t"
    "v_mfma_f32_16x16x32_bf16 v[116:119], a[140:143], a[84:87], v[116:119]\n\t" "v_mov_b32_e64 v27, v25\n\t" "v_mfma_f32_16x16x32_bf16 v[120:123], a[136:139], a[72:75], v[120:123]\n\t"
    "ds_read_b64_tr_b16 a[156:157], %[vr0] offset:50176\n\t" "v_mov_b32_e64 v30, v28\n\t" "v_mfma_f32_16x16x32_bf16 v[120:123], a[140:143], a[88:91], v[120:123]\n\t"
    "ds_read_b64_tr_b16 a[158:159], %[vr0] offset:53760\n\t" "v_mov_b32 v31, v29\n\t" "s_add_u32 m0, %[sl], 0\n\t" "v_mfma_f32_16x16x32_bf16 v[124:127], a[136:139], a[76:79], v[124:127]\n\t"
    "global_load_lds_dwordx4 %[vb0], s[78:79]\n\t" "s_nop 0\n\t" "v_mfma_f32_16x16x32_bf16 v[124:127], a[140:143], a[92:95], v[124:127]\n\t" "s_nop 0\n\t" "s_waitcnt lgkmcnt(4)\n\t"
    "v_mfma_f32_16x16x32_bf16 v[128:131], a[144:147], a[64:67], v[128:131]\n\t" "ds_read_b64_tr_b16 a[128:129], %[vr0] offset:35872\n\t" "v_permlane32_swap_b32 v24, v26\n\t"
    "v_mfma_f32_16x16x32_bf16 v[128:131], a[148:151], a[80:83], v[128:131]\n\t" "ds_read_b64_tr_b16 a[130:131], %[vr0] offset:39456\n\t" "v_permlane32_swap_b32 v25, v27\n\t"
    "v_mfma_f32_16x16x32_bf16 v[132:135], a[144:147], a[68:71], v[132:135]\n\t" "v_permlane32_swap_b32 v28, v30\n\t" "v_mfma_f32_16x16x32_bf16 v[132:135], a[148:151], a[84:87], v[132:135]\n\t"
    "v_permlane32_swap_b32 v29, v31\n\t" "v_mfma_f32_16x16x32_bf16 v[136:139], a[144:147], a[72:75], v[136:139]\n\t" "ds_read_b64_tr_b16 a[132:133], %[vr0] offset:50208\n\t" "v_add_f32 v24, v24, v26\n\t"
    "v_mfma_f32_16x16x32_bf16 v[136:139], a[148:151], a[88:91], v[136:139]\n\t" "ds_read_b64_tr_b16 a[134:135], %[vr0] offset:53792\n\t" "v_add_f32_e64 v25, v25, v27\n\t"
    "v_mfma_f32_16x16x32_bf16 v[140:143], a[144:147], a[76:79], v[140:143]\n\t" "v_add_f32_e64 v28, v28, v30\n\t" "v_mfma_f32_16x16x32_bf16 v[140:143], a[148:151], a[92:95], v[140:143]\n\t"
    "v_add_f32 v29, v29, v31\n\t" "s_waitcnt lgkmcnt(4)\n\t" "v_mfma_f32_16x16x32_bf16 v[144:147], a[152:155], a[64:67], v[144:147]\n\t" "ds_read_b64_tr_b16 a[136:137], %[vr2] offset:35840\n\t"
    "v_mul_f32 v24, 0xba800000, v24\n\t" "v_mfma_f32_16x16x32_bf16 v[144:147], a[156:159], a[80:83], v[144:147]\n\t" "ds_read_b64_tr_b16 a[138:139], %[vr2] offset:39424\n\t"
    "v_mul_f32 v25, 0x3a800000, v25\n\t" "v_mfma_f32_16x16x32_bf16 v[148:151], a[152:155], a[68:71], v[148:151]\n\t" "global_load_dwordx4 a[20:23], %[va], s[76:77] offset:1024 sc1\n\t"
    "v_mul_f32 v28, 0xba800000, v28\n\t" "v_mfma_f32_16x16x32_bf16 v[148:151], a[156:159], a[84:87], v[148:151]\n\t" "v_mul_f32 v29, 0x3a800000, v29\n\t"
    "v_mfma_f32_16x16x32_bf16 v[152:155], a[152:155], a[72:75], v[152:155]\n\t" "ds_read_b64_tr_b16 a[140:141], %[vr2] offset:50176\n\t" "v_fma_f32 v25, -v24, v24, v25\n\t"
    "v_mfma_f32_16x16x32_bf16 v[152:155], a[156:159], a[88:91], v[152:155]\n\t" "ds_read_b64_tr_b16 a[142:143], %[vr2] offset:53760\n\t" "v_and_b32 v26, 0xffff0000, v24\n\t"
    "s_add_u32 m0, %[sl], 1024\n\t" "v_mfma_f32_16x16x32_bf16 v[156:159], a[152:155], a[76:79], v[156:159]\n\t" "global_load_lds_dwordx4 %[vb1], s[78:79]\n\t" "v_fma_f32 v29, -v28, v28, v29\n\t"
    "v_mfma_f32_16x16x32_bf16 v[156:159], a[156:159], a[92:95], v[156:159]\n\t" "v_and_b32 v30, 0xffff0000, v28\n\t" "s_waitcnt lgkmcnt(4)\n\t"
    "v_mfma_f32_16x16x32_bf16 v[160:163], a[128:131], a[64:67], v[160:163]\n\t" "ds_read_b64_tr_b16 a[144:145], %[vr2] offset:35872\n\t" "v_max_f32 v25, 0, v25\n\t"
    "v_mfma_f32_16x16x32_bf16 v[160:163], a[132:135], a[80:83], v[160:163]\n\t" "ds_read_b64_tr_b16 a[146:147], %[vr2] offset:39456\n\t" "v_sub_f32_e64 v24, v24, v26\n\t"
    "v_mfma_f32_16x16x32_bf16 v[164:167], a[128:131], a[68:71], v[164:167]\n\t" "global_load_dwordx4 a[24:27], %[va], s[76:77] offset:2048 sc1\n\t" "v_max_f32_e64 v29, 0, v29\n\t"
    "v_mfma_f32_16x16x32_bf16 v[164:167], a[132:135], a[84:87], v[164:167]\n\t" "v_sub_f32_e64 v28, v28, v30\n\t" "v_mfma_f32_16x16x32_bf16 v[168:171], a[128:131], a[72:75], v[168:171]\n\t"
    "ds_read_b64_tr_b16 a[148:149], %[vr2] offset:50208\n\t" "v_add_f32 v25, 0x3727c5ac, v25\n\t" "v_mfma_f32_16x16x32_bf16 v[168:171], a[132:135], a[88:91], v[168:171]\n\t"
    "ds_read_b64_tr_b16 a[150:151], %[vr2] offset:53792\n\t" "v_cvt_pk_bf16_f32 v26, v26, v26\n\t" "v_mfma_f32_16x16x32_bf16 v[172:175], a[128:131], a[76:79], v[172:175]\n\t"
    "global_load_dwordx4 a[28:31], %[va], s[76:77] offset:3072 sc1\n\t" "v_add_f32 v29, 0x3727c5ac, v29\n\t" "v_mfma_f32_16x16x32_bf16 v[172:175], a[132:135], a[92:95], v[172:175]\n\t"
    "s_add_u32 s76, s76, 16384\n\t" "s_addc_u32 s77, s77, 0\n\t" "v_cvt_pk_bf16_f32 v30, v30, v30\n\t" "s_waitcnt lgkmcnt(4)\n\t"
    "v_mfma_f32_16x16x32_bf16 v[176:179], a[136:139], a[64:67], v[176:179]\n\t" "ds_read_b64_tr_b16 a[152:153], %[vr4] offset:35840\n\t" "v_rsq_f32_e64 v27, v25\n\t"
    "v_mfma_f32_16x16x32_bf16 v[176:179], a[140:143], a[80:83], v[176:179]\n\t" "ds_read_b64_tr_b16 a[154:155], %[vr4] offset:39424\n\t" "v_cvt_pk_bf16_f32 v24, v24, v24\n\t"
    "v_mfma_f32_16x16x32_bf16 v[180:183], a[136:139], a[68:71], v[180:183]\n\t" "v_rsq_f32_e64 v31, v29\n\t" "v_mfma_f32_16x16x32_bf16 v[180:183], a[140:143], a[84:87], v[180:183]\n\t"
    "v_cvt_pk_bf16_f32 v28, v28, v28\n\t" "v_mfma_f32_16x16x32_bf16 v[184:187], a[136:139], a[72:75], v[184:187]\n\t" "ds_read_b64_tr_b16 a[156:157], %[vr4] offset:50176\n\t"
    "v_mul_f32_e64 v20, v25, v27\n\t" "v_mfma_f32_16x16x32_bf16 v[184:187], a[140:143], a[88:91], v[184:187]\n\t" "ds_read_b64_tr_b16 a[158:159], %[vr4] offset:53760\n\t"
    "v_mul_f32 v16, 0xbfb8aa3b, v27\n\t" "s_add_u32 m0, %[sl], 2048\n\t" "v_mfma_f32_16x16x32_bf16 v[188:191], a[136:139], a[76:79], v[188:191]\n\t" "global_load_lds_dwordx4 %[vb2], s[78:79]\n\t"
    "v_mul_f32_e64 v21, v29, v31\n\t" "v_mfma_f32_16x16x32_bf16 v[188:191], a[140:143], a[92:95], v[188:191]\n\t" "v_mul_f32 v17, 0xbfb8aa3b, v31\n\t" "s_waitcnt lgkmcnt(4)\n\t"
    "v_mfma_f32_16x16x32_bf16 v[192:195], a[144:147], a[64:67], v[192:195]\n\t" "ds_read_b64_tr_b16 a[128:129], %[vr4] offset:35872\n\t" "v_and_b32 v27, 0xffff0000, v20\n\t"
    "v_mfma_f32_16x16x32_bf16 v[192:195], a[148:151], a[80:83], v[192:195]\n\t" "ds_read_b64_tr_b16 a[130:131], %[vr4] offset:39456\n\t" "s_nop 0\n\t"
    "v_mfma_f32_16x16x32_bf16 v[196:199], a[144:147], a[68:71], v[196:199]\n\t" "global_load_dwordx4 a[32:35], %[va], s[76:77] offset:0 sc1\n\t" "v_and_b32 v31, 0xffff0000, v21\n\t"
    "v_mfma_f32_16x16x32_bf16 v[196:199], a[148:151], a[84:87], v[196:199]\n\t" "v_sub_f32_e64 v25, v20, v27\n\t" "v_mfma_f32_16x16x32_bf16 v[200:203], a[144:147], a[72:75], v[200:203]\n\t"
    "ds_read_b64_tr_b16 a[132:133], %[vr4] offset:50208\n\t" "v_cvt_pk_bf16_f32 v27, v27, v27\n\t" "v_mfma_f32_16x16x32_bf16 v[200:203], a[148:151], a[88:91], v[200:203]\n\t"
    "ds_read_b64_tr_b16 a[134:135], %[vr4] offset:53792\n\t" "v_sub_f32_e64 v29, v21, v31\n\t" "s_add_u32 m0, %[sl], 3072\n\t" "v_mfma_f32_16x16x32_bf16 v[204:207], a[144:147], a[76:79], v[204:207]\n\t"
    "s_mov_b64 exec, s[82:83]\n\t" "global_load_lds_dwordx4 %[vb3], s[78:79]\n\t" "s_mov_b64 exec, -1\n\t" "v_cvt_pk_bf16_f32 v31, v31, v31\n\t"
    "v_mfma_f32_16x16x32_bf16 v[204:207], a[148:151], a[92:95], v[204:207]\n\t" "s_add_u32 s78, s78, 229376\n\t" "s_addc_u32 s79, s79, 0\n\t" "v_cvt_pk_bf16_f32 v25, v25, v25\n\t"
    "s_waitcnt lgkmcnt(4)\n\t" "v_mfma_f32_16x16x32_bf16 v[208:211], a[152:155], a[64:67], v[208:211]\n\t" "ds_read_b64_tr_b16 a[136:137], %[vr6] offset:35840\n\t" "s_nop 0\n\t"
    "v_mfma_f32_16x16x32_bf16 v[208:211], a[156:159], a[80:83], v[208:211]\n\t" "ds_read_b64_tr_b16 a[138:139], %[vr6] offset:39424\n\t" "v_cvt_pk_bf16_f32 v29, v29, v29\n\t"
    "v_mfma_f32_16x16x32_bf16 v[212:215], a[152:155], a[68:71], v[212:215]\n\t" "s_mov_b64 exec, 0xffff\n\t" "v_accvgpr_write_b32 a200, v26\n\t" "v_accvgpr_write_b32 a201, v24\n\t"
    "v_accvgpr_write_b32 a202, v27\n\t" "v_accvgpr_write_b32 a203, v25\n\t" "v_accvgpr_write_b32 a204, v30\n\t" "v_accvgpr_write_b32 a205, v28\n\t" "v_accvgpr_write_b32 a206, v31\n\t"
    "v_accvgpr_write_b32 a207, v29\n\t" "s_mov_b64 exec, -1\n\t" "v_mfma_f32_16x16x32_bf16 v[212:215], a[156:159], a[84:87], v[212:215]\n\t" "v_accvgpr_read_b32 v24, a220\n\t"
    "v_mfma_f32_16x16x32_bf16 v[216:219], a[152:155], a[72:75], v[216:219]\n\t" "ds_read_b64_tr_b16 a[140:141], %[vr6] offset:50176\n\t" "v_accvgpr_read_b32 v25, a221\n\t"
    "v_mfma_f32_16x16x32_bf16 v[216:219], a[156:159], a[88:91], v[216:219]\n\t" "ds_read_b64_tr_b16 a[142:143], %[vr6] offset:53760\n\t" "v_accvgpr_read_b32 v28, a222\n\t"
    "v_mfma_f32_16x16x32_bf16 v[220:223], a[152:155], a[76:79], v[220:223]\n\t" "global_load_dwordx4 a[36:39], %[va], s[76:77] offset:1024 sc1\n\t" "v_accvgpr_read_b32 v29, a223\n\t"
    "v_mfma_f32_16x16x32_bf16 v[220:223], a[156:159], a[92:95], v[220:223]\n\t" "v_accvgpr_read_b32 v26, a228\n\t" "s_waitcnt lgkmcnt(4)\n\t"
    "v_mfma_f32_16x16x32_bf16 v[224:227], a[128:131], a[64:67], v[224:227]\n\t" "s_waitcnt vmcnt(26)\n\t" "s_barrier\n\t" "ds_read_b64_tr_b16 a[144:145], %[vr1] offset:0\n\t"
    "v_accvgpr_read_b32 v27, a229\n\t" "v_mfma_f32_16x16x32_bf16 v[224:227], a[132:135], a[80:83], v[224:227]\n\t" "ds_read_b64_tr_b16 a[146:147], %[vr1] offset:3584\n\t"
    "v_accvgpr_read_b32 v30, a230\n\t" "v_mfma_f32_16x16x32_bf16 v[228:231], a[128:131], a[68:71], v[228:231]\n\t" "global_load_dwordx4 a[40:43], %[va], s[76:77] offset:2048 sc1\n\t"
    "v_accvgpr_read_b32 v31, a231\n\t" "v_mfma_f32_16x16x32_bf16 v[228:231], a[132:135], a[84:87], v[228:231]\n\t" "v_add_f32 v24, v24, v26\n\t"
    "v_mfma_f32_16x16x32_bf16 v[232:235], a[128:131], a[72:75], v[232:235]\n\t" "ds_read_b64_tr_b16 a[148:149], %[vr1] offset:14336\n\t" "v_add_f32_e64 v25, v25, v27\n\t"
    "v_mfma_f32_16x16x32_bf16 v[232:235], a[132:135], a[88:91], v[232:235]\n\t" "ds_read_b64_tr_b16 a[150:151], %[vr1] offset:17920\n\t" "v_add_f32_e64 v28, v28, v30\n\t" "s_add_u32 m0, %[sl], 14336\n\t"
    "v_mfma_f32_16x16x32_bf16 v[236:239], a[128:131], a[76:79], v[236:239]\n\t" "global_load_lds_dwordx4 %[vb0], s[78:79]\n\t" "v_add_f32_e64 v29, v29, v31\n\t"
    "v_mfma_f32_16x16x32_bf16 v[236:239], a[132:135], a[92:95], v[236:239]\n\t" "v_accvgpr_read_b32 v26, a236\n\t" "s_waitcnt lgkmcnt(4)\n\t"
    "v_mfma_f32_16x16x32_bf16 v[240:243], a[136:139], a[64:67], v[240:243]\n\t" "ds_read_b64_tr_b16 a[152:153], %[vr1] offset:32\n\t" "v_accvgpr_read_b32 v27, a237\n\t"
    "v_mfma_f32_16x16x32_bf16 v[240:243], a[140:143], a[80:83], v[240:243]\n\t" "ds_read_b64_tr_b16 a[154:155], %[vr1] offset:3616\n\t" "v_accvgpr_read_b32 v30, a238\n\t"
    "v_mfma_f32_16x16x32_bf16 v[244:247], a[136:139], a[68:71], v[244:247]\n\t" "v_accvgpr_read_b32 v31, a239\n\t" "v_mfma_f32_16x16x32_bf16 v[244:247], a[140:143], a[84:87], v[244:247]\n\t"
    "v_add_f32 v24, v24, v26\n\t" "v_mfma_f32_16x16x32_bf16 v[248:251], a[136:139], a[72:75], v[248:251]\n\t" "ds_read_b64_tr_b16 a[156:157], %[vr1] offset:14368\n\t" "v_add_f32_e64 v25, v25, v27\n\t"
    "v_mfma_f32_16x16x32_bf16 v[248:251], a[140:143], a[88:91], v[248:251]\n\t" "ds_read_b64_tr_b16 a[158:159], %[vr1] offset:17952\n\t" "v_add_f32_e64 v28, v28, v30\n\t"
    "v_mfma_f32_16x16x32_bf16 v[252:255], a[136:139], a[76:79], v[252:255]\n\t" "v_add_f32_e64 v29, v29, v31\n\t" "v_mfma_f32_16x16x32_bf16 v[252:255], a[140:143], a[92:95], v[252:255]\n\t"
    "v_accvgpr_read_b32 v26, a244\n\t" "s_waitcnt lgkmcnt(4) vmcnt(21)\n\t" "v_mfma_f32_16x16x32_bf16 v[32:35], a[144:147], a[96:99], v[32:35]\n\t" "s_barrier\n\t"
    "ds_read_b64_tr_b16 a[128:129], %[vr3] offset:0\n\t" "v_accvgpr_read_b32 v27, a245\n\t" "v_mfma_f32_16x16x32_bf16 v[32:35], a[148:151], a[112:115], v[32:35]\n\t"
    "ds_read_b64_tr_b16 a[130:131], %[vr3] offset:3584\n\t" "v_accvgpr_read_b32 v30, a246\n\t" "v_mfma_f32_16x16x32_bf16 v[36:39], a[144:147], a[100:103], v[36:39]\n\t"
    "global_load_dwordx4 a[44:47], %[va], s[76:77] offset:3072 sc1\n\t" "s_mov_b32 m0, %[srl]\n\t" "s_nop 0\n\t" "global_load_lds_dwordx4 %[va], %[sr] offset:0 sc1\n\t" "v_accvgpr_read_b32 v31, a247\n\t"
    "v_mfma_f32_16x16x32_bf16 v[36:39], a[148:151], a[116:119], v[36:39]\n\t" "s_add_u32 s76, s76, 16384\n\t" "s_addc_u32 s77, s77, 0\n\t" "v_add_f32 v24, v24, v26\n\t"
    "v_mfma_f32_16x16x32_bf16 v[40:43], a[144:147], a[104:107], v[40:43]\n\t" "ds_read_b64_tr_b16 a[132:133], %[vr3] offset:14336\n\t" "v_add_f32_e64 v25, v25, v27\n\t"
    "v_mfma_f32_16x16x32_bf16 v[40:43], a[148:151], a[120:123], v[40:43]\n\t" "ds_read_b64_tr_b16 a[134:135], %[vr3] offset:17920\n\t" "v_add_f32_e64 v28, v28, v30\n\t" "s_add_u32 m0, %[sl], 15360\n\t"
    "v_mfma_f32_16x16x32_bf16 v[44:47], a[144:147], a[108:111], v[44:47]\n\t" "global_load_lds_dwordx4 %[vb1], s[78:79]\n\t" "s_mov_b32 m0, %[srl]\n\t" "s_nop 0\n\t"
    "global_load_lds_dwordx4 %[va], %[sr] offset:1024 sc1\n\t" "v_add_f32_e64 v29, v29, v31\n\t" "v_mfma_f32_16x16x32_bf16 v[44:47], a[148:151], a[124:127], v[44:47]\n\t" "v_mov_b32 v26, v24\n\t"
    "s_waitcnt lgkmcnt(4)\n\t" "v_mfma_f32_16x16x32_bf16 v[48:51], a[152:155], a[96:99], v[48:51]\n\t" "ds_read_b64_tr_b16 a[136:137], %[vr3] offset:32\n\t" "v_mov_b32_e64 v27, v25\n\t"
    "v_mfma_f32_16x16x32_bf16 v[48:51], a[156:159], a[112:115], v[48:51]\n\t" "ds_read_b64_tr_b16 a[138:139], %[vr3] offset:3616\n\t" "v_mov_b32_e64 v30, v28\n\t"
    "v_mfma_f32_16x16x32_bf16 v[52:55], a[152:155], a[100:103], v[52:55]\n\t" "global_load_dwordx4 a[48:51], %[va], s[76:77] offset:0 sc1\n\t" "s_mov_b32 m0, %[srl]\n\t" "s_nop 0\n\t"
    "global_load_lds_dwordx4 %[va], %[sr] offset:2048 sc1\n\t" "v_mov_b32_e64 v31, v29\n\t" "v_mfma_f32_16x16x32_bf16 v[52:55], a[156:159], a[116:119], v[52:55]\n\t" "s_nop 0\n\t"
    "v_mfma_f32_16x16x32_bf16 v[56:59], a[152:155], a[104:107], v[56:59]\n\t" "ds_read_b64_tr_b16 a[140:141], %[vr3] offset:14368\n\t" "s_nop 0\n\t"
    "v_mfma_f32_16x16x32_bf16 v[56:59], a[156:159], a[120:123], v[56:59]\n\t" "ds_read_b64_tr_b16 a[142:143], %[vr3] offset:17952\n\t" "v_permlane16_swap_b32 v24, v26\n\t" "s_add_u32 m0, %[sl], 16384\n\t"
    "v_mfma_f32_16x16x32_bf16 v[60:63], a[152:155], a[108:111], v[60:63]\n\t" "global_load_lds_dwordx4 %[vb2], s[78:79]\n\t" "s_mov_b32 m0, %[srl]\n\t" "s_nop 0\n\t"
    "global_load_lds_dwordx4 %[va], %[sr] offset:3072 sc1\n\t" "v_permlane16_swap_b32 v25, v27\n\t" "v_mfma_f32_16x16x32_bf16 v[60:63], a[156:159], a[124:127], v[60:63]\n\t"
    "v_permlane16_swap_b32 v28, v30\n\t" "s_waitcnt lgkmcnt(4)\n\t" "v_mfma_f32_16x16x32_bf16 v[64:67], a[128:131], a[96:99], v[64:67]\n\t" "ds_read_b64_tr_b16 a[144:145], %[vr5] offset:0\n\t"
    "v_permlane16_swap_b32 v29, v31\n\t" "v_mfma_f32_16x16x32_bf16 v[64:67], a[132:135], a[112:115], v[64:67]\n\t" "ds_read_b64_tr_b16 a[146:147], %[vr5] offset:3584\n\t" "v_add_f32 v24, v24, v26\n\t"
    "v_mfma_f32_16x16x32_bf16 v[68:71], a[128:131], a[100:103], v[68:71]\n\t" "v_add_f32_e64 v25, v25, v27\n\t" "v_mfma_f32_16x16x32_bf16 v[68:71], a[132:135], a[116:119], v[68:71]\n\t"
    "v_add_f32_e64 v28, v28, v30\n\t" "v_mfma_f32_16x16x32_bf16 v[72:75], a[128:131], a[104:107], v[72:75]\n\t" "ds_read_b64_tr_b16 a[148:149], %[vr5] offset:14336\n\t" "v_add_f32_e64 v29, v29, v31\n\t"
    "v_mfma_f32_16x16x32_bf16 v[72:75], a[132:135], a[120:123], v[72:75]\n\t" "ds_read_b64_tr_b16 a[150:151], %[vr5] offset:17920\n\t" "v_mov_b32_e64 v26, v24\n\t"
    "v_mfma_f32_16x16x32_bf16 v[76:79], a[128:131], a[108:111], v[76:79]\n\t" "global_load_dwordx4 a[52:55], %[va], s[76:77] offset:1024 sc1\n\t" "v_mov_b32_e64 v27, v25\n\t"
    "v_mfma_f32_16x16x32_bf16 v[76:79], a[132:135], a[124:127], v[76:79]\n\t" "v_mov_b32 v30, v28\n\t" "s_waitcnt lgkmcnt(4)\n\t" "v_mfma_f32_16x16x32_bf16 v[80:83], a[136:139], a[96:99], v[80:83]\n\t"
    "ds_read_b64_tr_b16 a[152:153], %[vr5] offset:32\n\t" "v_mov_b32_e64 v31, v29\n\t" "v_mfma_f32_16x16x32_bf16 v[80:83], a[140:143], a[112:115], v[80:83]\n\t"
    "ds_read_b64_tr_b16 a[154:155], %[vr5] offset:3616\n\t" "s_nop 0\n\t" "s_add_u32 m0, %[sl], 17408\n\t" "v_mfma_f32_16x16x32_bf16 v[84:87], a[136:139], a[100:103], v[84:87]\n\t"
    "s_mov_b64 exec, s[82:83]\n\t" "global_load_lds_dwordx4 %[vb3], s[78:79]\n\t" "s_mov_b64 exec, -1\n\t" "s_nop 0\n\t" "v_mfma_f32_16x16x32_bf16 v[84:87], a[140:143], a[116:119], v[84:87]\n\t"
    "s_add_u32 s78, s78, 229376\n\t" "s_addc_u32 s79, s79, 0\n\t" "v_permlane32_swap_b32 v24, v26\n\t" "v_mfma_f32_16x16x32_bf16 v[88:91], a[136:139], a[104:107], v[88:91]\n\t"
    "ds_read_b64_tr_b16 a[156:157], %[vr5] offset:14368\n\t" "v_permlane32_swap_b32 v25, v27\n\t" "v_mfma_f32_16x16x32_bf16 v[88:91], a[140:143], a[120:123], v[88:91]\n\t"
    "ds_read_b64_tr_b16 a[158:159], %[vr5] offset:17952\n\t" "v_permlane32_swap_b32 v28, v30\n\t" "v_mfma_f32_16x16x32_bf16 v[92:95], a[136:139], a[108:111], v[92:95]\n\t"
    "global_load_dwordx4 a[56:59], %[va], s[76:77] offset:2048 sc1\n\t" "v_permlane32_swap_b32 v29, v31\n\t" "v_mfma_f32_16x16x32_bf16 v[92:95], a[140:143], a[124:127], v[92:95]\n\t"
    "v_add_f32_e64 v24, v24, v26\n\t" "s_waitcnt lgkmcnt(4)\n\t" "v_mfma_f32_16x16x32_bf16 v[96:99], a[144:147], a[96:99], v[96:99]\n\t" "ds_read_b64_tr_b16 a[128:129], %[vr7] offset:0\n\t"
    "v_add_f32_e64 v25, v25, v27\n\t" "v_mfma_f32_16x16x32_bf16 v[96:99], a[148:151], a[112:115], v[96:99]\n\t" "ds_read_b64_tr_b16 a[130:131], %[vr7] offset:3584\n\t" "v_add_f32_e64 v28, v28, v30\n\t"
    "v_mfma_f32_16x16x32_bf16 v[100:103], a[144:147], a[100:103], v[100:103]\n\t" "v_add_f32_e64 v29, v29, v31\n\t" "v_mfma_f32_16x16x32_bf16 v[100:103], a[148:151], a[116:119], v[100:103]\n\t"
    "v_mul_f32 v24, 0xba800000, v24\n\t" "v_mfma_f32_16x16x32_bf16 v[104:107], a[144:147], a[104:107], v[104:107]\n\t" "ds_read_b64_tr_b16 a[132:133], %[vr7] offset:14336\n\t"
    "v_mul_f32 v25, 0x3a800000, v25\n\t" "v_mfma_f32_16x16x32_bf16 v[104:107], a[148:151], a[120:123], v[104:107]\n\t" "ds_read_b64_tr_b16 a[134:135], %[vr7] offset:17920\n\t"
    "v_mul_f32 v28, 0xba800000, v28\n\t" "s_add_u32 m0, %[sl], 28672\n\t" "v_mfma_f32_16x16x32_bf16 v[108:111], a[144:147], a[108:111], v[108:111]\n\t" "global_load_lds_dwordx4 %[vb0], s[78:79]\n\t"
    "v_mul_f32 v29, 0x3a800000, v29\n\t" "v_mfma_f32_16x16x32_bf16 v[108:111], a[148:151], a[124:127], v[108:111]\n\t" "v_fma_f32 v25, -v24, v24, v25\n\t" "s_waitcnt lgkmcnt(4)\n\t"
    "v_mfma_f32_16x16x32_bf16 v[112:115], a[152:155], a[96:99], v[112:115]\n\t" "ds_read_b64_tr_b16 a[136:137], %[vr1] offset:7168\n\t" "v_and_b32 v26, 0xffff0000, v24\n\t"
    "v_mfma_f32_16x16x32_bf16 v[112:115], a[156:159], a[112:115], v[112:115]\n\t" "ds_read_b64_tr_b16 a[138:139], %[vr1] offset:10752\n\t" "v_fma_f32 v29, -v28, v28, v29\n\t"
    "v_mfma_f32_16x16x32_bf16 v[116:119], a[152:155], a[100:103], v[116:119]\n\t" "global_load_dwordx4 a[60:63], %[va], s[76:77] offset:3072 sc1\n\t" "v_and_b32 v30, 0xffff0000, v28\n\t"
    "v_mfma_f32_16x16x32_bf16 v[116:119], a[156:159], a[116:119], v[116:119]\n\t" "s_add_u32 s76, s76, 16384\n\t" "s_addc_u32 s77, s77, 0\n\t" "v_max_f32_e64 v25, 0, v25\n\t"
    "v_mfma_f32_16x16x32_bf16 v[120:123], a[152:155], a[104:107], v[120:123]\n\t" "ds_read_b64_tr_b16 a[140:141], %[vr1] offset:21504\n\t" "v_sub_f32_e64 v24, v24, v26\n\t"
    "v_mfma_f32_16x16x32_bf16 v[120:123], a[156:159], a[120:123], v[120:123]\n\t" "ds_read_b64_tr_b16 a[142:143], %[vr1] offset:25088\n\t" "v_max_f32_e64 v29, 0, v29\n\t" "s_add_u32 m0, %[sl], 29696\n\t"
    "v_mfma_f32_16x16x32_bf16 v[124:127], a[152:155], a[108:111], v[124:127]\n\t" "global_load_lds_dwordx4 %[vb1], s[78:79]\n\t" "v_sub_f32_e64 v28, v28, v30\n\t"
    "v_mfma_f32_16x16x32_bf16 v[124:127], a[156:159], a[124:127], v[124:127]\n\t" "v_add_f32 v25, 0x3727c5ac, v25\n\t" "s_waitcnt lgkmcnt(4)\n\t"
    "v_mfma_f32_16x16x32_bf16 v[128:131], a[128:131], a[96:99], v[128:131]\n\t" "ds_read_b64_tr_b16 a[144:145], %[vr1] offset:7200\n\t" "v_cvt_pk_bf16_f32 v26, v26, v26\n\t"
    "v_mfma_f32_16x16x32_bf16 v[128:131], a[132:135], a[112:115], v[128:131]\n\t" "ds_read_b64_tr_b16 a[146:147], %[vr1] offset:10784\n\t" "v_add_f32 v29, 0x3727c5ac, v29\n\t"
    "v_mfma_f32_16x16x32_bf16 v[132:135], a[128:131], a[100:103], v[132:135]\n\t" "v_cvt_pk_bf16_f32 v30, v30, v30\n\t" "v_mfma_f32_16x16x32_bf16 v[132:135], a[132:135], a[116:119], v[132:135]\n\t"
    "v_rsq_f32 v27, v25\n\t" "v_mfma_f32_16x16x32_bf16 v[136:139], a[128:131], a[104:107], v[136:139]\n\t" "ds_read_b64_tr_b16 a[148:149], %[vr1] offset:21536\n\t" "v_cvt_pk_bf16_f32 v24, v24, v24\n\t"
    "v_mfma_f32_16x16x32_bf16 v[136:139], a[132:135], a[120:123], v[136:139]\n\t" "ds_read_b64_tr_b16 a[150:151], %[vr1] offset:25120\n\t" "v_rsq_f32_e64 v31, v29\n\t"
    "v_mfma_f32_16x16x32_bf16 v[140:143], a[128:131], a[108:111], v[140:143]\n\t" "v_cvt_pk_bf16_f32 v28, v28, v28\n\t" "v_mfma_f32_16x16x32_bf16 v[140:143], a[132:135], a[124:127], v[140:143]\n\t"
    "v_mul_f32 v22, v25, v27\n\t" "s_waitcnt lgkmcnt(4)\n\t" "v_mfma_f32_16x16x32_bf16 v[144:147], a[136:139], a[96:99], v[144:147]\n\t" "ds_read_b64_tr_b16 a[152:153], %[vr3] offset:7168\n\t"
    "v_mul_f32 v18, 0xbfb8aa3b, v27\n\t" "v_mfma_f32_16x16x32_bf16 v[144:147], a[140:143], a[112:115], v[144:147]\n\t" "ds_read_b64_tr_b16 a[154:155], %[vr3] offset:10752\n\t"
    "v_mul_f32_e64 v23, v29, v31\n\t" "v_mfma_f32_16x16x32_bf16 v[148:151], a[136:139], a[100:103], v[148:151]\n\t" "global_load_dwordx4 a[64:67], %[va], s[76:77] offset:0 sc1\n\t"
    "v_mul_f32 v19, 0xbfb8aa3b, v31\n\t" "v_mfma_f32_16x16x32_bf16 v[148:151], a[140:143], a[116:119], v[148:151]\n\t" "v_and_b32 v27, 0xffff0000, v22\n\t"
    "v_mfma_f32_16x16x32_bf16 v[152:155], a[136:139], a[104:107], v[152:155]\n\t" "ds_read_b64_tr_b16 a[156:157], %[vr3] offset:21504\n\t" "s_nop 0\n\t"
    "v_mfma_f32_16x16x32_bf16 v[152:155], a[140:143], a[120:123], v[152:155]\n\t" "ds_read_b64_tr_b16 a[158:159], %[vr3] offset:25088\n\t" "v_and_b32 v31, 0xffff0000, v23\n\t"
    "s_add_u32 m0, %[sl], 30720\n\t" "v_mfma_f32_16x16x32_bf16 v[156:159], a[136:139], a[108:111], v[156:159]\n\t" "global_load_lds_dwordx4 %[vb2], s[78:79]\n\t" "v_sub_f32 v25, v22, v27\n\t"
    "v_mfma_f32_16x16x32_bf16 v[156:159], a[140:143], a[124:127], v[156:159]\n\t" "v_cvt_pk_bf16_f32 v27, v27, v27\n\t" "s_waitcnt lgkmcnt(4)\n\t"
    "v_mfma_f32_16x16x32_bf16 v[160:163], a[144:147], a[96:99], v[160:163]\n\t" "ds_read_b64_tr_b16 a[128:129], %[vr3] offset:7200\n\t" "v_sub_f32 v29, v23, v31\n\t"
    "v_mfma_f32_16x16x32_bf16 v[160:163], a[148:151], a[112:115], v[160:163]\n\t" "ds_read_b64_tr_b16 a[130:131], %[vr3] offset:10784\n\t" "v_cvt_pk_bf16_f32 v31, v31, v31\n\t"
    "v_mfma_f32_16x16x32_bf16 v[164:167], a[144:147], a[100:103], v[164:167]\n\t" "global_load_dwordx4 a[68:71], %[va], s[76:77] offset:1024 sc1\n\t" "v_cvt_pk_bf16_f32 v25, v25, v25\n\t"
    "v_mfma_f32_16x16x32_bf16 v[164:167], a[148:151], a[116:119], v[164:167]\n\t" "s_nop 0\n\t" "v_mfma_f32_16x16x32_bf16 v[168:171], a[144:147], a[104:107], v[168:171]\n\t"
    "ds_read_b64_tr_b16 a[132:133], %[vr3] offset:21536\n\t" "v_cvt_pk_bf16_f32 v29, v29, v29\n\t" "v_mfma_f32_16x16x32_bf16 v[168:171], a[148:151], a[120:123], v[168:171]\n\t"
    "ds_read_b64_tr_b16 a[134:135], %[vr3] offset:25120\n\t" "s_mov_b64 exec, 0xffff\n\t" "v_accvgpr_write_b32 a208, v26\n\t" "v_accvgpr_write_b32 a209, v24\n\t" "v_accvgpr_write_b32 a210, v27\n\t"
    "v_accvgpr_write_b32 a211, v25\n\t" "v_accvgpr_write_b32 a212, v30\n\t" "v_accvgpr_write_b32 a213, v28\n\t" "v_accvgpr_write_b32 a214, v31\n\t" "v_accvgpr_write_b32 a215, v29\n\t"
    "s_mov_b64 exec, -1\n\t" "s_add_u32 m0, %[sl], 31744\n\t" "v_mfma_f32_16x16x32_bf16 v[172:175], a[144:147], a[108:111], v[172:175]\n\t" "s_mov_b64 exec, s[82:83]\n\t"
    "global_load_lds_dwordx4 %[vb3], s[78:79]\n\t" "s_mov_b64 exec, -1\n\t" "v_mfma_f32_16x16x32_bf16 v[172:175], a[148:151], a[124:127], v[172:175]\n\t" "s_add_u32 s78, s78, 229376\n\t"
    "s_addc_u32 s79, s79, 0\n\t" "s_waitcnt lgkmcnt(4)\n\t" "v_mfma_f32_16x16x32_bf16 v[176:179], a[152:155], a[96:99], v[176:179]\n\t" "ds_read_b64_tr_b16 a[136:137], %[vr5] offset:7168\n\t"
    "v_mfma_f32_16x16x32_bf16 v[176:179], a[156:159], a[112:115], v[176:179]\n\t" "ds_read_b64_tr_b16 a[138:139], %[vr5] offset:10752\n\t"
    "v_mfma_f32_16x16x32_bf16 v[180:183], a[152:155], a[100:103], v[180:183]\n\t" "v_mfma_f32_16x16x32_bf16 v[180:183], a[156:159], a[116:119], v[180:183]\n\t"
    "v_mfma_f32_16x16x32_bf16 v[184:187], a[152:155], a[104:107], v[184:187]\n\t" "ds_read_b64_tr_b16 a[140:141], %[vr5] offset:21504\n\t"
    "v_mfma_f32_16x16x32_bf16 v[184:187], a[156:159], a[120:123], v[184:187]\n\t" "ds_read_b64_tr_b16 a[142:143], %[vr5] offset:25088\n\t"
    "v_mfma_f32_16x16x32_bf16 v[188:191], a[152:155], a[108:111], v[188:191]\n\t" "global_load_dwordx4 a[72:75], %[va], s[76:77] offset:2048 sc1\n\t"
    "v_mfma_f32_16x16x32_bf16 v[188:191], a[156:159], a[124:127], v[188:191]\n\t" "s_waitcnt lgkmcnt(4)\n\t" "v_mfma_f32_16x16x32_bf16 v[192:195], a[128:131], a[96:99], v[192:195]\n\t"
    "ds_read_b64_tr_b16 a[144:145], %[vr5] offset:7200\n\t" "v_mfma_f32_16x16x32_bf16 v[192:195], a[132:135], a[112:115], v[192:195]\n\t" "ds_read_b64_tr_b16 a[146:147], %[vr5] offset:10784\n\t"
    "s_add_u32 m0, %[sl], 43008\n\t" "v_mfma_f32_16x16x32_bf16 v[196:199], a[128:131], a[100:103], v[196:199]\n\t" "global_load_lds_dwordx4 %[vb0], s[78:79]\n\t"
    "v_mfma_f32_16x16x32_bf16 v[196:199], a[132:135], a[116:119], v[196:199]\n\t" "v_mfma_f32_16x16x32_bf16 v[200:203], a[128:131], a[104:107], v[200:203]\n\t"
    "ds_read_b64_tr_b16 a[148:149], %[vr5] offset:21536\n\t" "v_mfma_f32_16x16x32_bf16 v[200:203], a[132:135], a[120:123], v[200:203]\n\t" "ds_read_b64_tr_b16 a[150:151], %[vr5] offset:25120\n\t"
    "v_mfma_f32_16x16x32_bf16 v[204:207], a[128:131], a[108:111], v[204:207]\n\t" "global_load_dwordx4 a[76:79], %[va], s[76:77] offset:3072 sc1\n\t"
    "v_mfma_f32_16x16x32_bf16 v[204:207], a[132:135], a[124:127], v[204:207]\n\t" "s_add_u32 s76, s76, 16384\n\t" "s_addc_u32 s77, s77, 0\n\t" "s_waitcnt lgkmcnt(4)\n\t"
    "v_mfma_f32_16x16x32_bf16 v[208:211], a[136:139], a[96:99], v[208:211]\n\t" "ds_read_b64_tr_b16 a[152:153], %[vr7] offset:7168\n\t"
    "v_mfma_f32_16x16x32_bf16 v[208:211], a[140:143], a[112:115], v[208:211]\n\t" "ds_read_b64_tr_b16 a[154:155], %[vr7] offset:10752\n\t"
    "v_mfma_f32_16x16x32_bf16 v[212:215], a[136:139], a[100:103], v[212:215]\n\t" "v_mfma_f32_16x16x32_bf16 v[212:215], a[140:143], a[116:119], v[212:215]\n\t"
    "v_mfma_f32_16x16x32_bf16 v[216:219], a[136:139], a[104:107], v[216:219]\n\t" "ds_read_b64_tr_b16 a[156:157], %[vr7] offset:21504\n\t"
    "v_mfma_f32_16x16x32_bf16 v[216:219], a[140:143], a[120:123], v[216:219]\n\t" "ds_read_b64_tr_b16 a[158:159], %[vr7] offset:25088\n\t" "s_add_u32 m0, %[sl], 44032\n\t"
    "v_mfma_f32_16x16x32_bf16 v[220:223], a[136:139], a[108:111], v[220:223]\n\t" "global_load_lds_dwordx4 %[vb1], s[78:79]\n\t"
    "v_mfma_f32_16x16x32_bf16 v[220:223], a[140:143], a[124:127], v[220:223]\n\t" "s_waitcnt lgkmcnt(4)\n\t" "v_mfma_f32_16x16x32_bf16 v[224:227], a[144:147], a[96:99], v[224:227]\n\t"
    "s_waitcnt vmcnt(14)\n\t" "s_barrier\n\t" "ds_read_b64_tr_b16 a[128:129], %[vr1] offset:28672\n\t" "v_mfma_f32_16x16x32_bf16 v[224:227], a[148:151], a[112:115], v[224:227]\n\t"
    "ds_read_b64_tr_b16 a[130:131], %[vr1] offset:32256\n\t" "v_mfma_f32_16x16x32_bf16 v[228:231], a[144:147], a[100:103], v[228:231]\n\t" "global_load_dwordx4 a[80:83], %[va], s[76:77] offset:0 sc1\n\t"
    "v_mfma_f32_16x16x32_bf16 v[228:231], a[148:151], a[116:119], v[228:231]\n\t" "v_mfma_f32_16x16x32_bf16 v[232:235], a[144:147], a[104:107], v[232:235]\n\t"
    "ds_read_b64_tr_b16 a[132:133], %[vr1] offset:43008\n\t" "v_mfma_f32_16x16x32_bf16 v[232:235], a[148:151], a[120:123], v[232:235]\n\t" "ds_read_b64_tr_b16 a[134:135], %[vr1] offset:46592\n\t"
    "s_add_u32 m0, %[sl], 45056\n\t" "v_mfma_f32_16x16x32_bf16 v[236:239], a[144:147], a[108:111], v[236:239]\n\t" "global_load_lds_dwordx4 %[vb2], s[78:79]\n\t"
    "v_mfma_f32_16x16x32_bf16 v[236:239], a[148:151], a[124:127], v[236:239]\n\t" "s_waitcnt lgkmcnt(4)\n\t" "v_mfma_f32_16x16x32_bf16 v[240:243], a[152:155], a[96:99], v[240:243]\n\t"
    "ds_read_b64_tr_b16 a[136:137], %[vr1] offset:28704\n\t" "v_mfma_f32_16x16x32_bf16 v[240:243], a[156:159], a[112:115], v[240:243]\n\t" "ds_read_b64_tr_b16 a[138:139], %[vr1] offset:32288\n\t"
    "v_mfma_f32_16x16x32_bf16 v[244:247], a[152:155], a[100:103], v[244:247]\n\t" "global_load_dword a231, %[va], %[cd]\n\t" "v_mfma_f32_16x16x32_bf16 v[244:247], a[156:159], a[116:119], v[244:247]\n\t"
    "v_mfma_f32_16x16x32_bf16 v[248:251], a[152:155], a[104:107], v[248:251]\n\t" "ds_read_b64_tr_b16 a[140:141], %[vr1] offset:43040\n\t"
    "v_mfma_f32_16x16x32_bf16 v[248:251], a[156:159], a[120:123], v[248:251]\n\t" "ds_read_b64_tr_b16 a[142:143], %[vr1] offset:46624\n\t"
    "v_mfma_f32_16x16x32_bf16 v[252:255], a[152:155], a[108:111], v[252:255]\n\t" "v_mfma_f32_16x16x32_bf16 v[252:255], a[156:159], a[124:127], v[252:255]\n\t" "s_waitcnt lgkmcnt(4)\n\t"
    "v_mfma_f32_16x16x32_bf16 v[32:35], a[128:131], a[0:3], v[32:35]\n\t" "s_barrier\n\t" "ds_read_b64_tr_b16 a[144:145], %[vr3] offset:28672\n\t" "v_add_u32 v31, %[sld], %[va]\n\t"
    "v_mfma_f32_16x16x32_bf16 v[32:35], a[132:135], a[16:19], v[32:35]\n\t" "ds_read_b64_tr_b16 a[146:147], %[vr3] offset:32256\n\t" "v_mfma_f32_16x16x32_bf16 v[36:39], a[128:131], a[4:7], v[36:39]\n\t"
    "global_load_dwordx4 a[84:87], %[va], s[76:77] offset:1024 sc1\n\t" "ds_read_b128 a[232:235], v31 offset:24576\n\t" "v_mfma_f32_16x16x32_bf16 v[36:39], a[132:135], a[20:23], v[36:39]\n\t"
    "ds_read_b128 a[236:239], v31 offset:28672\n\t" "ds_read_b128 a[240:243], v31 offset:32768\n\t" "v_mfma_f32_16x16x32_bf16 v[40:43], a[128:131], a[8:11], v[40:43]\n\t"
    "ds_read_b64_tr_b16 a[148:149], %[vr3] offset:43008\n\t" "ds_read_b128 a[244:247], v31 offset:36864\n\t" "v_mfma_f32_16x16x32_bf16 v[40:43], a[132:135], a[24:27], v[40:43]\n\t"
    "ds_read_b64_tr_b16 a[150:151], %[vr3] offset:46592\n\t" "v_accvgpr_read_b32 v31, %[cr0]\n\t" "s_add_u32 m0, %[sl], 46080\n\t" "v_mfma_f32_16x16x32_bf16 v[44:47], a[128:131], a[12:15], v[44:47]\n\t"
    "s_mov_b64 exec, s[82:83]\n\t" "global_load_lds_dwordx4 %[vb3], s[78:79]\n\t" "s_mov_b64 exec, -1\n\t" "ds_read2_b32 v[24:25], v31 offset0:0 offset1:4\n\t"
    "v_mfma_f32_16x16x32_bf16 v[44:47], a[132:135], a[28:31], v[44:47]\n\t" "s_add_u32 s78, s78, 229376\n\t" "s_addc_u32 s79, s79, 0\n\t" "ds_read2_b32 v[26:27], v31 offset0:8 offset1:12\n\t"
    "ds_read2_b32 v[28:29], v31 offset0:16 offset1:20\n\t" "s_waitcnt lgkmcnt(11)\n\t" "v_mfma_f32_16x16x32_bf16 v[48:51], a[136:139], a[0:3], v[48:51]\n\t"
    "ds_read_b64_tr_b16 a[152:153], %[vr3] offset:28704\n\t" "ds_read2_b32 v[30:31], v31 offset0:24 offset1:28\n\t" "v_mfma_f32_16x16x32_bf16 v[48:51], a[140:143], a[16:19], v[48:51]\n\t"
    "ds_read_b64_tr_b16 a[154:155], %[vr3] offset:32288\n\t" "v_mfma_f32_16x16x32_bf16 v[52:55], a[136:139], a[4:7], v[52:55]\n\t" "global_load_dwordx4 a[88:91], %[va], s[76:77] offset:2048 sc1\n\t"
    "v_mfma_f32_16x16x32_bf16 v[52:55], a[140:143], a[20:23], v[52:55]\n\t" "v_mfma_f32_16x16x32_bf16 v[56:59], a[136:139], a[8:11], v[56:59]\n\t" "ds_read_b64_tr_b16 a[156:157], %[vr3] offset:43040\n\t"
    "v_mfma_f32_16x16x32_bf16 v[56:59], a[140:143], a[24:27], v[56:59]\n\t" "ds_read_b64_tr_b16 a[158:159], %[vr3] offset:46624\n\t" "v_mfma_f32_16x16x32_bf16 v[60:63], a[136:139], a[12:15], v[60:63]\n\t"
    "global_load_dwordx4 a[92:95], %[va], s[76:77] offset:3072 sc1\n\t" "v_mfma_f32_16x16x32_bf16 v[60:63], a[140:143], a[28:31], v[60:63]\n\t" "s_add_u32 s76, s76, 16384\n\t" "s_addc_u32 s77, s77, 0\n\t"
    "s_waitcnt lgkmcnt(3)\n\t" "v_add_f32_e64 v24, v24, v25\n\t" "v_mfma_f32_16x16x32_bf16 v[64:67], a[144:147], a[0:3], v[64:67]\n\t" "ds_read_b64_tr_b16 a[128:129], %[vr5] offset:28672\n\t"
    "v_add_f32_e64 v26, v26, v27\n\t" "v_mfma_f32_16x16x32_bf16 v[64:67], a[148:151], a[16:19], v[64:67]\n\t" "ds_read_b64_tr_b16 a[130:131], %[vr5] offset:32256\n\t" "v_add_f32 v28, v28, v29\n\t"
    "v_add_f32 v30, v30, v31\n\t" "v_mfma_f32_16x16x32_bf16 v[68:71], a[144:147], a[4:7], v[68:71]\n\t" "v_add_f32_e64 v24, v24, v26\n\t"
    "v_mfma_f32_16x16x32_bf16 v[68:71], a[148:151], a[20:23], v[68:71]\n\t" "v_add_f32_e64 v28, v28, v30\n\t" "v_mfma_f32_16x16x32_bf16 v[72:75], a[144:147], a[8:11], v[72:75]\n\t"
    "ds_read_b64_tr_b16 a[132:133], %[vr5] offset:43008\n\t" "v_add_f32_e64 v24, v24, v28\n\t" "v_mfma_f32_16x16x32_bf16 v[72:75], a[148:151], a[24:27], v[72:75]\n\t"
    "ds_read_b64_tr_b16 a[134:135], %[vr5] offset:46592\n\t" "s_add_u32 m0, %[sl], 57344\n\t" "v_mfma_f32_16x16x32_bf16 v[76:79], a[144:147], a[12:15], v[76:79]\n\t"
    "global_load_lds_dwordx4 %[vb0], s[78:79]\n\t" "v_cmp_u_f32_e64 vcc, v24, v24\n\t" "v_mfma_f32_16x16x32_bf16 v[76:79], a[148:151], a[28:31], v[76:79]\n\t" "s_waitcnt lgkmcnt(4)\n\t"
    "v_mfma_f32_16x16x32_bf16 v[80:83], a[152:155], a[0:3], v[80:83]\n\t" "ds_read_b64_tr_b16 a[136:137], %[vr5] offset:28704\n\t" "v_mfma_f32_16x16x32_bf16 v[80:83], a[156:159], a[16:19], v[80:83]\n\t"
    "ds_read_b64_tr_b16 a[138:139], %[vr5] offset:32288\n\t" "v_mfma_f32_16x16x32_bf16 v[84:87], a[152:155], a[4:7], v[84:87]\n\t" "global_load_dwordx4 a[96:99], %[va], s[76:77] offset:0 sc1\n\t"
    "v_mfma_f32_16x16x32_bf16 v[84:87], a[156:159], a[20:23], v[84:87]\n\t" "s_nop 0\n\t" "s_cbranch_vccnz .Lslow0_%=\n\t" ".Lgo0_%=:\n\t" "v_accvgpr_read_b32 v27, %[cw]\n\t"
    "v_mfma_f32_16x16x32_bf16 v[88:91], a[152:155], a[8:11], v[88:91]\n\t" "ds_read_b64_tr_b16 a[140:141], %[vr5] offset:43040\n\t" "v_and_b32 v25, 0xffff0000, v24\n\t"
    "v_mfma_f32_16x16x32_bf16 v[88:91], a[156:159], a[24:27], v[88:91]\n\t" "ds_read_b64_tr_b16 a[142:143], %[vr5] offset:46624\n\t" "s_add_u32 m0, %[sl], 58368\n\t"
    "v_mfma_f32_16x16x32_bf16 v[92:95], a[152:155], a[12:15], v[92:95]\n\t" "global_load_lds_dwordx4 %[vb1], s[78:79]\n\t" "v_add_u32 v27, 0x0, v27\n\t"
    "v_mfma_f32_16x16x32_bf16 v[92:95], a[156:159], a[28:31], v[92:95]\n\t" "v_sub_f32 v24, v24, v25\n\t" "s_waitcnt lgkmcnt(4)\n\t" "v_mfma_f32_16x16x32_bf16 v[96:99], a[128:131], a[0:3], v[96:99]\n\t"
    "ds_read_b64_tr_b16 a[144:145], %[vr7] offset:28672\n\t" "v_cvt_pk_bf16_f32 v24, v25, v24\n\t" "v_mfma_f32_16x16x32_bf16 v[96:99], a[132:135], a[16:19], v[96:99]\n\t"
    "ds_read_b64_tr_b16 a[146:147], %[vr7] offset:32256\n\t" "v_mfma_f32_16x16x32_bf16 v[100:103], a[128:131], a[4:7], v[100:103]\n\t" "ds_write2_b32 v27, v24, v24 offset0:0 offset1:1\n\t"
    "v_mfma_f32_16x16x32_bf16 v[100:103], a[132:135], a[20:23], v[100:103]\n\t" "v_accvgpr_read_b32 v31, %[cr1]\n\t" "v_mfma_f32_16x16x32_bf16 v[104:107], a[128:131], a[8:11], v[104:107]\n\t"
    "ds_read_b64_tr_b16 a[148:149], %[vr7] offset:43008\n\t" "ds_read2_b32 v[24:25], v31 offset0:0 offset1:4\n\t" "v_mfma_f32_16x16x32_bf16 v[104:107], a[132:135], a[24:27], v[104:107]\n\t"
    "ds_read_b64_tr_b16 a[150:151], %[vr7] offset:46592\n\t" "ds_read2_b32 v[26:27], v31 offset0:8 offset1:12\n\t" "ds_read2_b32 v[28:29], v31 offset0:16 offset1:20\n\t"
    "v_mfma_f32_16x16x32_bf16 v[108:111], a[128:131], a[12:15], v[108:111]\n\t" "global_load_dwordx4 a[100:103], %[va], s[76:77] offset:1024 sc1\n\t" "ds_read2_b32 v[30:31], v31 offset0:24 offset1:28\n\t"
    "v_mfma_f32_16x16x32_bf16 v[108:111], a[132:135], a[28:31], v[108:111]\n\t" "s_waitcnt lgkmcnt(9)\n\t" "v_mfma_f32_16x16x32_bf16 v[112:115], a[136:139], a[0:3], v[112:115]\n\t"
    "ds_read_b64_tr_b16 a[152:153], %[vr1] offset:35840\n\t" "v_mfma_f32_16x16x32_bf16 v[112:115], a[140:143], a[16:19], v[112:115]\n\t" "ds_read_b64_tr_b16 a[154:155], %[vr1] offset:39424\n\t"
    "v_mfma_f32_16x16x32_bf16 v[116:119], a[136:139], a[4:7], v[116:119]\n\t" "global_load_dwordx4 a[104:107], %[va], s[76:77] offset:2048 sc1\n\t"
    "v_mfma_f32_16x16x32_bf16 v[116:119], a[140:143], a[20:23], v[116:119]\n\t" "v_mfma_f32_16x16x32_bf16 v[120:123], a[136:139], a[8:11], v[120:123]\n\t"
    "ds_read_b64_tr_b16 a[156:157], %[vr1] offset:50176\n\t" "v_mfma_f32_16x16x32_bf16 v[120:123], a[140:143], a[24:27], v[120:123]\n\t" "ds_read_b64_tr_b16 a[158:159], %[vr1] offset:53760\n\t"
    "s_waitcnt lgkmcnt(4)\n\t" "v_add_f32_e64 v24, v24, v25\n\t" "s_add_u32 m0, %[sl], 59392\n\t" "v_mfma_f32_16x16x32_bf16 v[124:127], a[136:139], a[12:15], v[124:127]\n\t"
    "global_load_lds_dwordx4 %[vb2], s[78:79]\n\t" "v_add_f32_e64 v26, v26, v27\n\t" "v_mfma_f32_16x16x32_bf16 v[124:127], a[140:143], a[28:31], v[124:127]\n\t" "v_add_f32 v28, v28, v29\n\t"
    "v_add_f32 v30, v30, v31\n\t" "v_mfma_f32_16x16x32_bf16 v[128:131], a[144:147], a[0:3], v[128:131]\n\t" "ds_read_b64_tr_b16 a[128:129], %[vr1] offset:35872\n\t" "v_add_f32_e64 v24, v24, v26\n\t"
    "v_mfma_f32_16x16x32_bf16 v[128:131], a[148:151], a[16:19], v[128:131]\n\t" "ds_read_b64_tr_b16 a[130:131], %[vr1] offset:39456\n\t" "v_add_f32_e64 v28, v28, v30\n\t"
    "v_mfma_f32_16x16x32_bf16 v[132:135], a[144:147], a[4:7], v[132:135]\n\t" "v_add_f32_e64 v24, v24, v28\n\t" "v_mfma_f32_16x16x32_bf16 v[132:135], a[148:151], a[20:23], v[132:135]\n\t"
    "v_mfma_f32_16x16x32_bf16 v[136:139], a[144:147], a[8:11], v[136:139]\n\t" "ds_read_b64_tr_b16 a[132:133], %[vr1] offset:50208\n\t" "v_cmp_u_f32_e64 vcc, v24, v24\n\t"
    "v_mfma_f32_16x16x32_bf16 v[136:139], a[148:151], a[24:27], v[136:139]\n\t" "ds_read_b64_tr_b16 a[134:135], %[vr1] offset:53792\n\t"
    "v_mfma_f32_16x16x32_bf16 v[140:143], a[144:147], a[12:15], v[140:143]\n\t" "v_mfma_f32_16x16x32_bf16 v[140:143], a[148:151], a[28:31], v[140:143]\n\t" "s_waitcnt lgkmcnt(4)\n\t"
    "v_mfma_f32_16x16x32_bf16 v[144:147], a[152:155], a[0:3], v[144:147]\n\t" "ds_read_b64_tr_b16 a[136:137], %[vr3] offset:35840\n\t"
    "v_mfma_f32_16x16x32_bf16 v[144:147], a[156:159], a[16:19], v[144:147]\n\t" "ds_read_b64_tr_b16 a[138:139], %[vr3] offset:39424\n\t" "s_nop 0\n\t" "s_cbranch_vccnz .Lslow1_%=\n\t" ".Lgo1_%=:\n\t"
    "v_accvgpr_read_b32 v27, %[cw]\n\t" "v_mfma_f32_16x16x32_bf16 v[148:151], a[152:155], a[4:7], v[148:151]\n\t" "global_load_dwordx4 a[108:111], %[va], s[76:77] offset:3072 sc1\n\t"
    "v_and_b32 v25, 0xffff0000, v24\n\t" "v_mfma_f32_16x16x32_bf16 v[148:151], a[156:159], a[20:23], v[148:151]\n\t" "s_add_u32 s76, s76, 16384\n\t" "s_addc_u32 s77, s77, 0\n\t"
    "v_mfma_f32_16x16x32_bf16 v[152:155], a[152:155], a[8:11], v[152:155]\n\t" "ds_read_b64_tr_b16 a[140:141], %[vr3] offset:50176\n\t" "v_add_u32 v27, 0x800, v27\n\t"
    "v_mfma_f32_16x16x32_bf16 v[152:155], a[156:159], a[24:27], v[152:155]\n\t" "ds_read_b64_tr_b16 a[142:143], %[vr3] offset:53760\n\t" "v_sub_f32_e64 v24, v24, v25\n\t" "s_add_u32 m0, %[sl], 60416\n\t"
    "v_mfma_f32_16x16x32_bf16 v[156:159], a[152:155], a[12:15], v[156:159]\n\t" "s_mov_b64 exec, s[82:83]\n\t" "global_load_lds_dwordx4 %[vb3], s[78:79]\n\t" "s_mov_b64 exec, -1\n\t"
    "v_cvt_pk_bf16_f32 v24, v25, v24\n\t" "v_mfma_f32_16x16x32_bf16 v[156:159], a[156:159], a[28:31], v[156:159]\n\t" "s_add_u32 s78, s78, 229376\n\t" "s_addc_u32 s79, s79, 0\n\t"
    "s_waitcnt lgkmcnt(4)\n\t" "v_mfma_f32_16x16x32_bf16 v[160:163], a[128:131], a[0:3], v[160:163]\n\t" "ds_read_b64_tr_b16 a[144:145], %[vr3] offset:35872\n\t" "s_cmp_lg_u64 %[m01], 0\n\t"
    "s_cselect_b64 exec, -1, 0\n\t" "ds_write2_b32 v27, v24, v24 offset0:0 offset1:1\n\t" "s_mov_b64 exec, -1\n\t" "v_mfma_f32_16x16x32_bf16 v[160:163], a[132:135], a[16:19], v[160:163]\n\t"
    "ds_read_b64_tr_b16 a[146:147], %[vr3] offset:39456\n\t" "v_mfma_f32_16x16x32_bf16 v[164:167], a[128:131], a[4:7], v[164:167]\n\t" "global_load_dwordx4 a[112:115], %[va], s[76:77] offset:0 sc1\n\t"
    "v_mfma_f32_16x16x32_bf16 v[164:167], a[132:135], a[20:23], v[164:167]\n\t" "v_mfma_f32_16x16x32_bf16 v[168:171], a[128:131], a[8:11], v[168:171]\n\t"
    "ds_read_b64_tr_b16 a[148:149], %[vr3] offset:50208\n\t" "v_mfma_f32_16x16x32_bf16 v[168:171], a[132:135], a[24:27], v[168:171]\n\t" "ds_read_b64_tr_b16 a[150:151], %[vr3] offset:53792\n\t"
    "s_add_u32 m0, %[sl], 71680\n\t" "v_mfma_f32_16x16x32_bf16 v[172:175], a[128:131], a[12:15], v[172:175]\n\t" "global_load_lds_dwordx4 %[vb0], s[78:79]\n\t"
    "v_mfma_f32_16x16x32_bf16 v[172:175], a[132:135], a[28:31], v[172:175]\n\t" "s_waitcnt lgkmcnt(5)\n\t" "v_mfma_f32_16x16x32_bf16 v[176:179], a[136:139], a[0:3], v[176:179]\n\t"
    "ds_read_b64_tr_b16 a[152:153], %[vr5] offset:35840\n\t" "v_mfma_f32_16x16x32_bf16 v[176:179], a[140:143], a[16:19], v[176:179]\n\t" "ds_read_b64_tr_b16 a[154:155], %[vr5] offset:39424\n\t"
    "v_mfma_f32_16x16x32_bf16 v[180:183], a[136:139], a[4:7], v[180:183]\n\t" "v_mfma_f32_16x16x32_bf16 v[180:183], a[140:143], a[20:23], v[180:183]\n\t"
    "v_mfma_f32_16x16x32_bf16 v[184:187], a[136:139], a[8:11], v[184:187]\n\t" "ds_read_b64_tr_b16 a[156:157], %[vr5] offset:50176\n\t"
    "v_mfma_f32_16x16x32_bf16 v[184:187], a[140:143], a[24:27], v[184:187]\n\t" "ds_read_b64_tr_b16 a[158:159], %[vr5] offset:53760\n\t"
    "v_mfma_f32_16x16x32_bf16 v[188:191], a[136:139], a[12:15], v[188:191]\n\t" "global_load_dwordx4 a[116:119], %[va], s[76:77] offset:1024 sc1\n\t"
    "v_mfma_f32_16x16x32_bf16 v[188:191], a[140:143], a[28:31], v[188:191]\n\t" "s_waitcnt lgkmcnt(4)\n\t" "v_mfma_f32_16x16x32_bf16 v[192:195], a[144:147], a[0:3], v[192:195]\n\t"
    "ds_read_b64_tr_b16 a[128:129], %[vr5] offset:35872\n\t" "v_mfma_f32_16x16x32_bf16 v[192:195], a[148:151], a[16:19], v[192:195]\n\t" "ds_read_b64_tr_b16 a[130:131], %[vr5] offset:39456\n\t"
    "s_add_u32 m0, %[sl], 72704\n\t" "v_mfma_f32_16x16x32_bf16 v[196:199], a[144:147], a[4:7], v[196:199]\n\t" "global_load_lds_dwordx4 %[vb1], s[78:79]\n\t"
    "v_mfma_f32_16x16x32_bf16 v[196:199], a[148:151], a[20:23], v[196:199]\n\t" "v_mfma_f32_16x16x32_bf16 v[200:203], a[144:147], a[8:11], v[200:203]\n\t"
    "ds_read_b64_tr_b16 a[132:133], %[vr5] offset:50208\n\t" "v_mfma_f32_16x16x32_bf16 v[200:203], a[148:151], a[24:27], v[200:203]\n\t" "ds_read_b64_tr_b16 a[134:135], %[vr5] offset:53792\n\t"
    "v_mfma_f32_16x16x32_bf16 v[204:207], a[144:147], a[12:15], v[204:207]\n\t" "global_load_dwordx4 a[120:123], %[va], s[76:77] offset:2048 sc1\n\t"
    "v_mfma_f32_16x16x32_bf16 v[204:207], a[148:151], a[28:31], v[204:207]\n\t" "s_waitcnt lgkmcnt(4)\n\t" "v_mfma_f32_16x16x32_bf16 v[208:211], a[152:155], a[0:3], v[208:211]\n\t"
    "ds_read_b64_tr_b16 a[136:137], %[vr7] offset:35840\n\t" "v_mfma_f32_16x16x32_bf16 v[208:211], a[156:159], a[16:19], v[208:211]\n\t" "ds_read_b64_tr_b16 a[138:139], %[vr7] offset:39424\n\t"
    "v_mfma_f32_16x16x32_bf16 v[212:215], a[152:155], a[4:7], v[212:215]\n\t" "v_mfma_f32_16x16x32_bf16 v[212:215], a[156:159], a[20:23], v[212:215]\n\t"
    "v_mfma_f32_16x16x32_bf16 v[216:219], a[152:155], a[8:11], v[216:219]\n\t" "ds_read_b64_tr_b16 a[140:141], %[vr7] offset:50176\n\t"
    "v_mfma_f32_16x16x32_bf16 v[216:219], a[156:159], a[24:27], v[216:219]\n\t" "ds_read_b64_tr_b16 a[142:143], %[vr7] offset:53760\n\t" "s_add_u32 m0, %[sl], 73728\n\t"
    "v_mfma_f32_16x16x32_bf16 v[220:223], a[152:155], a[12:15], v[220:223]\n\t" "global_load_lds_dwordx4 %[vb2], s[78:79]\n\t" "v_mfma_f32_16x16x32_bf16 v[220:223], a[156:159], a[28:31], v[220:223]\n\t"
    "s_waitcnt lgkmcnt(4)\n\t" "v_mfma_f32_16x16x32_bf16 v[224:227], a[128:131], a[0:3], v[224:227]\n\t" "s_waitcnt vmcnt(14)\n\t" "s_barrier\n\t" "v_accvgpr_read_b32 v31, %[ct]\n\t"
    "s_mov_b64 exec, 0xffff\n\t" "v_add_u32_e64 v31, %[gt0], v31\n\t" "s_mov_b64 exec, -1\n\t" "ds_read_b128 a[144:147], v31 offset:0\n\t"
    "v_mfma_f32_16x16x32_bf16 v[224:227], a[132:135], a[16:19], v[224:227]\n\t" "v_mfma_f32_16x16x32_bf16 v[228:231], a[128:131], a[4:7], v[228:231]\n\t"
    "global_load_dwordx4 a[124:127], %[va], s[76:77] offset:3072 sc1\n\t" "v_mfma_f32_16x16x32_bf16 v[228:231], a[132:135], a[20:23], v[228:231]\n\t" "s_add_u32 s76, s76, s86\n\t"
    "s_addc_u32 s77, s77, s87\n\t" "v_mfma_f32_16x16x32_bf16 v[232:235], a[128:131], a[8:11], v[232:235]\n\t" "ds_read_b128 a[148:151], v31 offset:256\n\t"
    "v_mfma_f32_16x16x32_bf16 v[232:235], a[132:135], a[24:27], v[232:235]\n\t" "s_add_u32 m0, %[sl], 74752\n\t" "v_mfma_f32_16x16x32_bf16 v[236:239], a[128:131], a[12:15], v[236:239]\n\t"
    "s_mov_b64 exec, s[82:83]\n\t" "global_load_lds_dwordx4 %[vb3], s[78:79]\n\t" "s_mov_b64 exec, -1\n\t" "v_mfma_f32_16x16x32_bf16 v[236:239], a[132:135], a[28:31], v[236:239]\n\t"
    "s_add_u32 s78, s78, s88\n\t" "s_addc_u32 s79, s79, s89\n\t" "s_waitcnt lgkmcnt(2)\n\t" "v_mfma_f32_16x16x32_bf16 v[240:243], a[136:139], a[0:3], v[240:243]\n\t"
    "ds_read_b64_tr_b16 a[152:153], %[vr0] offset:0\n\t" "v_mfma_f32_16x16x32_bf16 v[240:243], a[140:143], a[16:19], v[240:243]\n\t" "ds_read_b64_tr_b16 a[154:155], %[vr0] offset:3584\n\t"
    "v_mfma_f32_16x16x32_bf16 v[244:247], a[136:139], a[4:7], v[244:247]\n\t" "v_mfma_f32_16x16x32_bf16 v[244:247], a[140:143], a[20:23], v[244:247]\n\t"
    "v_mfma_f32_16x16x32_bf16 v[248:251], a[136:139], a[8:11], v[248:251]\n\t" "ds_read_b64_tr_b16 a[156:157], %[vr0] offset:14336\n\t"
    "v_mfma_f32_16x16x32_bf16 v[248:251], a[140:143], a[24:27], v[248:251]\n\t" "ds_read_b64_tr_b16 a[158:159], %[vr0] offset:17920\n\t"
    "v_mfma_f32_16x16x32_bf16 v[252:255], a[136:139], a[12:15], v[252:255]\n\t" "v_mfma_f32_16x16x32_bf16 v[252:255], a[140:143], a[28:31], v[252:255]\n\t" "s_waitcnt lgkmcnt(4)\n\t"
    "v_mfma_f32_16x16x32_bf16 v[32:35], a[144:147], a[200:203], v[32:35]\n\t" "ds_read_b64_tr_b16 a[128:129], %[vr0] offset:7168\n\t"
    "v_mfma_f32_16x16x32_bf16 v[36:39], a[144:147], a[204:207], v[36:39]\n\t" "ds_read_b64_tr_b16 a[130:131], %[vr0] offset:10752\n\t"
    "v_mfma_f32_16x16x32_bf16 v[40:43], a[144:147], a[208:211], v[40:43]\n\t" "v_mfma_f32_16x16x32_bf16 v[44:47], a[144:147], a[212:215], v[44:47]\n\t"
    "v_mfma_f32_16x16x32_bf16 v[144:147], a[148:151], a[200:203], v[144:147]\n\t" "ds_read_b64_tr_b16 a[132:133], %[vr0] offset:21504\n\t"
    "v_mfma_f32_16x16x32_bf16 v[148:151], a[148:151], a[204:207], v[148:151]\n\t" "ds_read_b64_tr_b16 a[134:135], %[vr0] offset:25088\n\t"
    "v_mfma_f32_16x16x32_bf16 v[152:155], a[148:151], a[208:211], v[152:155]\n\t" "v_mfma_f32_16x16x32_bf16 v[156:159], a[148:151], a[212:215], v[156:159]\n\t" "s_waitcnt lgkmcnt(4)\n\t"
    "v_mfma_f32_16x16x32_bf16 v[32:35], a[152:155], a[32:35], v[32:35]\n\t" "ds_read_b64_tr_b16 a[136:137], %[vr0] offset:28672\n\t" "v_mfma_f32_16x16x32_bf16 v[32:35], a[156:159], a[48:51], v[32:35]\n\t"
    "ds_read_b64_tr_b16 a[138:139], %[vr0] offset:32256\n\t" "v_mfma_f32_16x16x32_bf16 v[36:39], a[152:155], a[36:39], v[36:39]\n\t" "v_mfma_f32_16x16x32_bf16 v[36:39], a[156:159], a[52:55], v[36:39]\n\t"
    "v_mfma_f32_16x16x32_bf16 v[40:43], a[152:155], a[40:43], v[40:43]\n\t" "ds_read_b64_tr_b16 a[140:141], %[vr0] offset:43008\n\t" "v_mfma_f32_16x16x32_bf16 v[40:43], a[156:159], a[56:59], v[40:43]\n\t"
    "ds_read_b64_tr_b16 a[142:143], %[vr0] offset:46592\n\t" "v_mfma_f32_16x16x32_bf16 v[44:47], a[152:155], a[44:47], v[44:47]\n\t" "v_mfma_f32_16x16x32_bf16 v[44:47], a[156:159], a[60:63], v[44:47]\n\t"
    "s_waitcnt lgkmcnt(4)\n\t" "v_mfma_f32_16x16x32_bf16 v[144:147], a[128:131], a[32:35], v[144:147]\n\t" "ds_read_b64_tr_b16 a[144:145], %[vr0] offset:35840\n\t"
    "v_mfma_f32_16x16x32_bf16 v[144:147], a[132:135], a[48:51], v[144:147]\n\t" "ds_read_b64_tr_b16 a[146:147], %[vr0] offset:39424\n\t"
    "v_mfma_f32_16x16x32_bf16 v[148:151], a[128:131], a[36:39], v[148:151]\n\t" "v_mfma_f32_16x16x32_bf16 v[148:151], a[132:135], a[52:55], v[148:151]\n\t"
    "v_mfma_f32_16x16x32_bf16 v[152:155], a[128:131], a[40:43], v[152:155]\n\t" "ds_read_b64_tr_b16 a[148:149], %[vr0] offset:50176\n\t"
    "v_mfma_f32_16x16x32_bf16 v[152:155], a[132:135], a[56:59], v[152:155]\n\t" "ds_read_b64_tr_b16 a[150:151], %[vr0] offset:53760\n\t"
    "v_mfma_f32_16x16x32_bf16 v[156:159], a[128:131], a[44:47], v[156:159]\n\t" "v_mfma_f32_16x16x32_bf16 v[156:159], a[132:135], a[60:63], v[156:159]\n\t" "s_waitcnt lgkmcnt(4)\n\t"
    "v_mfma_f32_16x16x32_bf16 v[32:35], a[136:139], a[64:67], v[32:35]\n\t" "s_waitcnt vmcnt(0)\n\t" "s_barrier\n\t" "v_lshlrev_b32 v30, 3, %[va]\n\t" "s_mov_b32 exec_hi, 0xffffff\n\t"
    "global_load_dword a230, v30, %[tp0]\n\t" "s_mov_b64 exec, -1\n\t" "ds_read_b64_tr_b16 a[152:153], %[vr1] offset:0\n\t" "v_mfma_f32_16x16x32_bf16 v[32:35], a[140:143], a[80:83], v[32:35]\n\t"
    "ds_read_b64_tr_b16 a[154:155], %[vr1] offset:3584\n\t" "v_mfma_f32_16x16x32_bf16 v[36:39], a[136:139], a[68:71], v[36:39]\n\t" "v_mfma_f32_16x16x32_bf16 v[36:39], a[140:143], a[84:87], v[36:39]\n\t"
    "v_mfma_f32_16x16x32_bf16 v[40:43], a[136:139], a[72:75], v[40:43]\n\t" "ds_read_b64_tr_b16 a[156:157], %[vr1] offset:14336\n\t" "v_mfma_f32_16x16x32_bf16 v[40:43], a[140:143], a[88:91], v[40:43]\n\t"
    "ds_read_b64_tr_b16 a[158:159], %[vr1] offset:17920\n\t" "v_mfma_f32_16x16x32_bf16 v[44:47], a[136:139], a[76:79], v[44:47]\n\t" "v_mfma_f32_16x16x32_bf16 v[44:47], a[140:143], a[92:95], v[44:47]\n\t"
    "s_waitcnt lgkmcnt(4)\n\t" "v_mfma_f32_16x16x32_bf16 v[144:147], a[144:147], a[64:67], v[144:147]\n\t" "ds_read_b64_tr_b16 a[128:129], %[vr1] offset:7168\n\t"
    "v_mfma_f32_16x16x32_bf16 v[144:147], a[148:151], a[80:83], v[144:147]\n\t" "ds_read_b64_tr_b16 a[130:131], %[vr1] offset:10752\n\t"
    "v_mfma_f32_16x16x32_bf16 v[148:151], a[144:147], a[68:71], v[148:151]\n\t" "v_mfma_f32_16x16x32_bf16 v[148:151], a[148:151], a[84:87], v[148:151]\n\t"
    "v_mfma_f32_16x16x32_bf16 v[152:155], a[144:147], a[72:75], v[152:155]\n\t" "ds_read_b64_tr_b16 a[132:133], %[vr1] offset:21504\n\t"
    "v_mfma_f32_16x16x32_bf16 v[152:155], a[148:151], a[88:91], v[152:155]\n\t" "ds_read_b64_tr_b16 a[134:135], %[vr1] offset:25088\n\t"
    "v_mfma_f32_16x16x32_bf16 v[156:159], a[144:147], a[76:79], v[156:159]\n\t" "v_mfma_f32_16x16x32_bf16 v[156:159], a[148:151], a[92:95], v[156:159]\n\t" "s_waitcnt lgkmcnt(4)\n\t"
    "v_mfma_f32_16x16x32_bf16 v[32:35], a[152:155], a[96:99], v[32:35]\n\t" "ds_read_b128 a[136:139], v31 offset:512\n\t" "v_mfma_f32_16x16x32_bf16 v[32:35], a[156:159], a[112:115], v[32:35]\n\t"
    "v_mfma_f32_16x16x32_bf16 v[36:39], a[152:155], a[100:103], v[36:39]\n\t" "v_mfma_f32_16x16x32_bf16 v[36:39], a[156:159], a[116:119], v[36:39]\n\t"
    "v_mfma_f32_16x16x32_bf16 v[40:43], a[152:155], a[104:107], v[40:43]\n\t" "ds_read_b128 a[140:143], v31 offset:768\n\t" "v_mfma_f32_16x16x32_bf16 v[40:43], a[156:159], a[120:123], v[40:43]\n\t"
    "v_mfma_f32_16x16x32_bf16 v[44:47], a[152:155], a[108:111], v[44:47]\n\t" "v_mfma_f32_16x16x32_bf16 v[44:47], a[156:159], a[124:127], v[44:47]\n\t" "s_waitcnt lgkmcnt(2)\n\t"
    "v_mfma_f32_16x16x32_bf16 v[144:147], a[128:131], a[96:99], v[144:147]\n\t" "ds_read_b64_tr_b16 a[144:145], %[vr0] offset:32\n\t"
    "v_mfma_f32_16x16x32_bf16 v[144:147], a[132:135], a[112:115], v[144:147]\n\t" "ds_read_b64_tr_b16 a[146:147], %[vr0] offset:3616\n\t"
    "v_mfma_f32_16x16x32_bf16 v[148:151], a[128:131], a[100:103], v[148:151]\n\t" "v_mfma_f32_16x16x32_bf16 v[148:151], a[132:135], a[116:119], v[148:151]\n\t"
    "v_mfma_f32_16x16x32_bf16 v[152:155], a[128:131], a[104:107], v[152:155]\n\t" "ds_read_b64_tr_b16 a[148:149], %[vr0] offset:14368\n\t"
    "v_mfma_f32_16x16x32_bf16 v[152:155], a[132:135], a[120:123], v[152:155]\n\t" "ds_read_b64_tr_b16 a[150:151], %[vr0] offset:17952\n\t"
    "v_mfma_f32_16x16x32_bf16 v[156:159], a[128:131], a[108:111], v[156:159]\n\t" "v_mfma_f32_16x16x32_bf16 v[156:159], a[132:135], a[124:127], v[156:159]\n\t" "s_waitcnt lgkmcnt(4)\n\t"
    "v_mfma_f32_16x16x32_bf16 v[48:51], a[136:139], a[200:203], v[48:51]\n\t" "ds_read_b64_tr_b16 a[152:153], %[vr0] offset:7200\n\t" "v_mul_f32 v32, v32, v16\n\t" "v_mul_f32 v33, v33, v16\n\t"
    "v_mul_f32 v34, v34, v16\n\t" "v_mfma_f32_16x16x32_bf16 v[52:55], a[136:139], a[204:207], v[52:55]\n\t" "ds_read_b64_tr_b16 a[154:155], %[vr0] offset:10784\n\t" "v_mul_f32 v35, v35, v16\n\t"
    "v_exp_f32 v32, v32\n\t" "v_mfma_f32_16x16x32_bf16 v[56:59], a[136:139], a[208:211], v[56:59]\n\t" "v_exp_f32_e64 v33, v33\n\t"
    "v_mfma_f32_16x16x32_bf16 v[60:63], a[136:139], a[212:215], v[60:63]\n\t" "v_exp_f32_e64 v34, v34\n\t" "v_mfma_f32_16x16x32_bf16 v[160:163], a[140:143], a[200:203], v[160:163]\n\t"
    "ds_read_b64_tr_b16 a[156:157], %[vr0] offset:21536\n\t" "v_exp_f32_e64 v35, v35\n\t" "v_fma_f32 v32, v32, v20, v20\n\t" "v_mfma_f32_16x16x32_bf16 v[164:167], a[140:143], a[204:207], v[164:167]\n\t"
    "ds_read_b64_tr_b16 a[158:159], %[vr0] offset:25120\n\t" "v_fma_f32 v33, v33, v20, v20\n\t" "v_fma_f32 v34, v34, v20, v20\n\t"
    "v_mfma_f32_16x16x32_bf16 v[168:171], a[140:143], a[208:211], v[168:171]\n\t" "v_fma_f32 v35, v35, v20, v20\n\t" "v_rcp_f32_e64 v32, v32\n\t"
    "v_mfma_f32_16x16x32_bf16 v[172:175], a[140:143], a[212:215], v[172:175]\n\t" "v_rcp_f32 v33, v33\n\t" "s_waitcnt lgkmcnt(4)\n\t"
    "v_mfma_f32_16x16x32_bf16 v[48:51], a[144:147], a[32:35], v[48:51]\n\t" "ds_read_b64_tr_b16 a[128:129], %[vr0] offset:28704\n\t" "v_rcp_f32 v34, v34\n\t" "v_rcp_f32 v35, v35\n\t"
    "v_mfma_f32_16x16x32_bf16 v[48:51], a[148:151], a[48:51], v[48:51]\n\t" "ds_read_b64_tr_b16 a[130:131], %[vr0] offset:32288\n\t" "v_mul_f32 v144, v144, v32\n\t" "v_mul_f32 v145, v145, v33\n\t"
    "v_mfma_f32_16x16x32_bf16 v[52:55], a[144:147], a[36:39], v[52:55]\n\t" "v_mul_f32 v146, v146, v34\n\t" "v_mul_f32 v147, v147, v35\n\t"
    "v_mfma_f32_16x16x32_bf16 v[52:55], a[148:151], a[52:55], v[52:55]\n\t" "v_cvt_pk_bf16_f32 v32, v144, v145\n\t" "v_cvt_pk_bf16_f32 v33, v146, v147\n\t"
    "v_mfma_f32_16x16x32_bf16 v[56:59], a[144:147], a[40:43], v[56:59]\n\t" "ds_read_b64_tr_b16 a[132:133], %[vr0] offset:43040\n\t" "v_mul_f32 v36, v36, v17\n\t" "v_mul_f32 v37, v37, v17\n\t"
    "v_mul_f32_e64 v38, v38, v17\n\t" "v_mfma_f32_16x16x32_bf16 v[56:59], a[148:151], a[56:59], v[56:59]\n\t" "ds_read_b64_tr_b16 a[134:135], %[vr0] offset:46624\n\t" "v_mul_f32 v39, v39, v17\n\t"
    "v_exp_f32 v36, v36\n\t" "v_mfma_f32_16x16x32_bf16 v[60:63], a[144:147], a[44:47], v[60:63]\n\t" "v_exp_f32_e64 v37, v37\n\t" "v_mfma_f32_16x16x32_bf16 v[60:63], a[148:151], a[60:63], v[60:63]\n\t"
    "v_exp_f32 v38, v38\n\t" "s_waitcnt lgkmcnt(4)\n\t" "v_mfma_f32_16x16x32_bf16 v[160:163], a[152:155], a[32:35], v[160:163]\n\t" "ds_read_b64_tr_b16 a[136:137], %[vr0] offset:35872\n\t"
    "v_exp_f32_e64 v39, v39\n\t" "v_fma_f32 v36, v36, v21, v21\n\t" "v_mfma_f32_16x16x32_bf16 v[160:163], a[156:159], a[48:51], v[160:163]\n\t" "ds_read_b64_tr_b16 a[138:139], %[vr0] offset:39456\n\t"
    "v_fma_f32 v37, v37, v21, v21\n\t" "v_fma_f32 v38, v38, v21, v21\n\t" "v_mfma_f32_16x16x32_bf16 v[164:167], a[152:155], a[36:39], v[164:167]\n\t" "v_fma_f32 v39, v39, v21, v21\n\t"
    "v_rcp_f32_e64 v36, v36\n\t" "v_mfma_f32_16x16x32_bf16 v[164:167], a[156:159], a[52:55], v[164:167]\n\t" "v_rcp_f32_e64 v37, v37\n\t"
    "v_mfma_f32_16x16x32_bf16 v[168:171], a[152:155], a[40:43], v[168:171]\n\t" "ds_read_b64_tr_b16 a[140:141], %[vr0] offset:50208\n\t" "v_rcp_f32 v38, v38\n\t" "v_rcp_f32 v39, v39\n\t"
    "v_mfma_f32_16x16x32_bf16 v[168:171], a[156:159], a[56:59], v[168:171]\n\t" "ds_read_b64_tr_b16 a[142:143], %[vr0] offset:53792\n\t" "v_mul_f32 v148, v148, v36\n\t" "v_mul_f32 v149, v149, v37\n\t"
    "v_mfma_f32_16x16x32_bf16 v[172:175], a[152:155], a[44:47], v[172:175]\n\t" "v_mul_f32 v150, v150, v38\n\t" "v_mul_f32 v151, v151, v39\n\t"
    "v_mfma_f32_16x16x32_bf16 v[172:175], a[156:159], a[60:63], v[172:175]\n\t" "v_cvt_pk_bf16_f32 v36, v148, v149\n\t" "v_cvt_pk_bf16_f32 v37, v150, v151\n\t" "s_waitcnt lgkmcnt(4)\n\t"
    "v_mfma_f32_16x16x32_bf16 v[48:51], a[128:131], a[64:67], v[48:51]\n\t" "ds_read_b64_tr_b16 a[144:145], %[vr1] offset:32\n\t" "v_mul_f32 v40, v40, v18\n\t" "v_mul_f32 v41, v41, v18\n\t"
    "v_mul_f32 v42, v42, v18\n\t" "v_mfma_f32_16x16x32_bf16 v[48:51], a[132:135], a[80:83], v[48:51]\n\t" "ds_read_b64_tr_b16 a[146:147], %[vr1] offset:3616\n\t" "v_mul_f32 v43, v43, v18\n\t"
    "v_exp_f32 v40, v40\n\t" "v_mfma_f32_16x16x32_bf16 v[52:55], a[128:131], a[68:71], v[52:55]\n\t" "v_exp_f32_e64 v41, v41\n\t" "v_mfma_f32_16x16x32_bf16 v[52:55], a[132:135], a[84:87], v[52:55]\n\t"
    "v_exp_f32_e64 v42, v42\n\t" "v_mfma_f32_16x16x32_bf16 v[56:59], a[128:131], a[72:75], v[56:59]\n\t" "ds_read_b64_tr_b16 a[148:149], %[vr1] offset:14368\n\t" "v_exp_f32_e64 v43, v43\n\t"
    "v_fma_f32 v40, v40, v22, v22\n\t" "v_mfma_f32_16x16x32_bf16 v[56:59], a[132:135], a[88:91], v[56:59]\n\t" "ds_read_b64_tr_b16 a[150:151], %[vr1] offset:17952\n\t" "v_fma_f32 v41, v41, v22, v22\n\t"
    "v_fma_f32 v42, v42, v22, v22\n\t" "v_mfma_f32_16x16x32_bf16 v[60:63], a[128:131], a[76:79], v[60:63]\n\t" "v_fma_f32 v43, v43, v22, v22\n\t" "v_rcp_f32_e64 v40, v40\n\t"
    "v_mfma_f32_16x16x32_bf16 v[60:63], a[132:135], a[92:95], v[60:63]\n\t" "v_rcp_f32 v41, v41\n\t" "s_waitcnt lgkmcnt(4)\n\t" "v_mfma_f32_16x16x32_bf16 v[160:163], a[136:139], a[64:67], v[160:163]\n\t"
    "ds_read_b64_tr_b16 a[152:153], %[vr1] offset:7200\n\t" "v_rcp_f32 v42, v42\n\t" "v_rcp_f32 v43, v43\n\t" "v_mfma_f32_16x16x32_bf16 v[160:163], a[140:143], a[80:83], v[160:163]\n\t"
    "ds_read_b64_tr_b16 a[154:155], %[vr1] offset:10784\n\t" "v_mul_f32 v152, v152, v40\n\t" "v_mul_f32 v153, v153, v41\n\t" "v_mfma_f32_16x16x32_bf16 v[164:167], a[136:139], a[68:71], v[164:167]\n\t"
    "v_mul_f32 v154, v154, v42\n\t" "v_mul_f32 v155, v155, v43\n\t" "v_mfma_f32_16x16x32_bf16 v[164:167], a[140:143], a[84:87], v[164:167]\n\t" "v_cvt_pk_bf16_f32 v40, v152, v153\n\t"
    "v_cvt_pk_bf16_f32 v41, v154, v155\n\t" "v_mfma_f32_16x16x32_bf16 v[168:171], a[136:139], a[72:75], v[168:171]\n\t" "ds_read_b64_tr_b16 a[156:157], %[vr1] offset:21536\n\t"
    "v_mul_f32 v44, v44, v19\n\t" "v_mul_f32 v45, v45, v19\n\t" "v_mul_f32_e64 v46, v46, v19\n\t" "v_mfma_f32_16x16x32_bf16 v[168:171], a[140:143], a[88:91], v[168:171]\n\t"
    "ds_read_b64_tr_b16 a[158:159], %[vr1] offset:25120\n\t" "v_mul_f32 v47, v47, v19\n\t" "v_exp_f32 v44, v44\n\t" "v_mfma_f32_16x16x32_bf16 v[172:175], a[136:139], a[76:79], v[172:175]\n\t"
    "v_exp_f32_e64 v45, v45\n\t" "v_mfma_f32_16x16x32_bf16 v[172:175], a[140:143], a[92:95], v[172:175]\n\t" "v_exp_f32 v46, v46\n\t" "s_waitcnt lgkmcnt(4)\n\t"
    "v_mfma_f32_16x16x32_bf16 v[48:51], a[144:147], a[96:99], v[48:51]\n\t" "v_accvgpr_read_b32 v31, %[ct]\n\t" "s_mov_b64 exec, 0xffff\n\t" "v_add_u32 v31, %[gt1], v31\n\t" "s_mov_b64 exec, -1\n\t"
    "ds_read_b128 a[128:131], v31 offset:0\n\t" "v_exp_f32_e64 v47, v47\n\t" "v_fma_f32 v44, v44, v23, v23\n\t" "v_mfma_f32_16x16x32_bf16 v[48:51], a[148:151], a[112:115], v[48:51]\n\t"
    "v_fma_f32 v45, v45, v23, v23\n\t" "v_fma_f32 v46, v46, v23, v23\n\t" "v_mfma_f32_16x16x32_bf16 v[52:55], a[144:147], a[100:103], v[52:55]\n\t" "v_fma_f32 v47, v47, v23, v23\n\t"
    "v_rcp_f32_e64 v44, v44\n\t" "v_mfma_f32_16x16x32_bf16 v[52:55], a[148:151], a[116:119], v[52:55]\n\t" "v_rcp_f32_e64 v45, v45\n\t"
    "v_mfma_f32_16x16x32_bf16 v[56:59], a[144:147], a[104:107], v[56:59]\n\t" "ds_read_b128 a[132:135], v31 offset:256\n\t" "v_rcp_f32 v46, v46\n\t" "v_rcp_f32 v47, v47\n\t"
    "v_mfma_f32_16x16x32_bf16 v[56:59], a[148:151], a[120:123], v[56:59]\n\t" "v_mul_f32 v156, v156, v44\n\t" "v_mul_f32 v157, v157, v45\n\t"
    "v_mfma_f32_16x16x32_bf16 v[60:63], a[144:147], a[108:111], v[60:63]\n\t" "v_mul_f32 v158, v158, v46\n\t" "v_mul_f32 v159, v159, v47\n\t"
    "v_mfma_f32_16x16x32_bf16 v[60:63], a[148:151], a[124:127], v[60:63]\n\t" "v_cvt_pk_bf16_f32 v44, v156, v157\n\t" "v_cvt_pk_bf16_f32 v45, v158, v159\n\t" "s_waitcnt lgkmcnt(2)\n\t"
    "v_mfma_f32_16x16x32_bf16 v[160:163], a[152:155], a[96:99], v[160:163]\n\t" "ds_read_b64_tr_b16 a[136:137], %[vr2] offset:0\n\t"
    "v_mfma_f32_16x16x32_bf16 v[160:163], a[156:159], a[112:115], v[160:163]\n\t" "ds_read_b64_tr_b16 a[138:139], %[vr2] offset:3584\n\t"
    "v_mfma_f32_16x16x32_bf16 v[164:167], a[152:155], a[100:103], v[164:167]\n\t" "global_load_dword a231, %[va], %[cd]\n\t" "v_mfma_f32_16x16x32_bf16 v[164:167], a[156:159], a[116:119], v[164:167]\n\t"
    "v_mfma_f32_16x16x32_bf16 v[168:171], a[152:155], a[104:107], v[168:171]\n\t" "ds_read_b64_tr_b16 a[140:141], %[vr2] offset:14336\n\t"
    "v_mfma_f32_16x16x32_bf16 v[168:171], a[156:159], a[120:123], v[168:171]\n\t" "ds_read_b64_tr_b16 a[142:143], %[vr2] offset:17920\n\t"
    "v_mfma_f32_16x16x32_bf16 v[172:175], a[152:155], a[108:111], v[172:175]\n\t" "v_mfma_f32_16x16x32_bf16 v[172:175], a[156:159], a[124:127], v[172:175]\n\t" "s_waitcnt lgkmcnt(4)\n\t"
    "v_mfma_f32_16x16x32_bf16 v[64:67], a[128:131], a[200:203], v[64:67]\n\t" "ds_read_b64_tr_b16 a[144:145], %[vr2] offset:7168\n\t" "s_add_u32 s90, %[shl], %[go0]\n\t" "s_addc_u32 s91, %[shh], 0\n\t"
    "s_add_u32 s92, s90, 229376\n\t" "s_addc_u32 s93, s91, 0\n\t" "v_mul_f32 v48, v48, v16\n\t" "v_mfma_f32_16x16x32_bf16 v[68:71], a[128:131], a[204:207], v[68:71]\n\t"
    "ds_read_b64_tr_b16 a[146:147], %[vr2] offset:10752\n\t" "v_mul_f32 v49, v49, v16\n\t" "v_mul_f32 v50, v50, v16\n\t" "v_mfma_f32_16x16x32_bf16 v[72:75], a[128:131], a[208:211], v[72:75]\n\t"
    "v_mul_f32 v51, v51, v16\n\t" "v_exp_f32 v48, v48\n\t" "v_mfma_f32_16x16x32_bf16 v[76:79], a[128:131], a[212:215], v[76:79]\n\t" "v_exp_f32_e64 v49, v49\n\t"
    "v_mfma_f32_16x16x32_bf16 v[176:179], a[132:135], a[200:203], v[176:179]\n\t" "ds_read_b64_tr_b16 a[148:149], %[vr2] offset:21504\n\t" "v_exp_f32 v50, v50\n\t" "v_exp_f32 v51, v51\n\t"
    "v_mfma_f32_16x16x32_bf16 v[180:183], a[132:135], a[204:207], v[180:183]\n\t" "ds_read_b64_tr_b16 a[150:151], %[vr2] offset:25088\n\t" "v_fma_f32 v48, v48, v20, v20\n\t"
    "v_fma_f32 v49, v49, v20, v20\n\t" "v_mfma_f32_16x16x32_bf16 v[184:187], a[132:135], a[208:211], v[184:187]\n\t" "v_fma_f32 v50, v50, v20, v20\n\t" "v_fma_f32 v51, v51, v20, v20\n\t"
    "v_mfma_f32_16x16x32_bf16 v[188:191], a[132:135], a[212:215], v[188:191]\n\t" "v_rcp_f32 v48, v48\n\t" "s_waitcnt lgkmcnt(4)\n\t"
    "v_mfma_f32_16x16x32_bf16 v[64:67], a[136:139], a[32:35], v[64:67]\n\t" "ds_read_b64_tr_b16 a[152:153], %[vr2] offset:28672\n\t" "v_rcp_f32 v49, v49\n\t" "v_rcp_f32 v50, v50\n\t"
    "v_mfma_f32_16x16x32_bf16 v[64:67], a[140:143], a[48:51], v[64:67]\n\t" "ds_read_b64_tr_b16 a[154:155], %[vr2] offset:32256\n\t" "v_rcp_f32_e64 v51, v51\n\t"
    "v_mfma_f32_16x16x32_bf16 v[68:71], a[136:139], a[36:39], v[68:71]\n\t" "v_mul_f32 v160, v160, v48\n\t" "v_mul_f32 v161, v161, v49\n\t"
    "v_mfma_f32_16x16x32_bf16 v[68:71], a[140:143], a[52:55], v[68:71]\n\t" "v_mul_f32 v162, v162, v50\n\t" "v_mul_f32 v163, v163, v51\n\t"
    "v_mfma_f32_16x16x32_bf16 v[72:75], a[136:139], a[40:43], v[72:75]\n\t" "ds_read_b64_tr_b16 a[156:157], %[vr2] offset:43008\n\t" "v_cvt_pk_bf16_f32 v34, v160, v161\n\t"
    "v_cvt_pk_bf16_f32 v35, v162, v163\n\t" "global_store_dwordx4 %[va], v[32:35], s[90:91] offset:0 sc1\n\t" "v_mfma_f32_16x16x32_bf16 v[72:75], a[140:143], a[56:59], v[72:75]\n\t"
    "ds_read_b64_tr_b16 a[158:159], %[vr2] offset:46592\n\t" "v_mul_f32 v52, v52, v17\n\t" "v_mul_f32 v53, v53, v17\n\t" "v_mfma_f32_16x16x32_bf16 v[76:79], a[136:139], a[44:47], v[76:79]\n\t"
    "v_mul_f32 v54, v54, v17\n\t" "v_mul_f32 v55, v55, v17\n\t" "v_mfma_f32_16x16x32_bf16 v[76:79], a[140:143], a[60:63], v[76:79]\n\t" "v_exp_f32 v52, v52\n\t" "s_waitcnt lgkmcnt(4)\n\t"
    "v_mfma_f32_16x16x32_bf16 v[176:179], a[144:147], a[32:35], v[176:179]\n\t" "ds_read_b64_tr_b16 a[128:129], %[vr2] offset:35840\n\t" "v_exp_f32 v53, v53\n\t" "v_exp_f32 v54, v54\n\t"
    "v_mfma_f32_16x16x32_bf16 v[176:179], a[148:151], a[48:51], v[176:179]\n\t" "ds_read_b64_tr_b16 a[130:131], %[vr2] offset:39424\n\t" "v_exp_f32_e64 v55, v55\n\t"
    "v_mfma_f32_16x16x32_bf16 v[180:183], a[144:147], a[36:39], v[180:183]\n\t" "v_fma_f32 v52, v52, v21, v21\n\t" "v_fma_f32 v53, v53, v21, v21\n\t"
    "v_mfma_f32_16x16x32_bf16 v[180:183], a[148:151], a[52:55], v[180:183]\n\t" "v_fma_f32 v54, v54, v21, v21\n\t" "v_fma_f32 v55, v55, v21, v21\n\t"
    "v_mfma_f32_16x16x32_bf16 v[184:187], a[144:147], a[40:43], v[184:187]\n\t" "ds_read_b64_tr_b16 a[132:133], %[vr2] offset:50176\n\t" "v_rcp_f32 v52, v52\n\t" "v_rcp_f32 v53, v53\n\t"
    "v_mfma_f32_16x16x32_bf16 v[184:187], a[148:151], a[56:59], v[184:187]\n\t" "ds_read_b64_tr_b16 a[134:135], %[vr2] offset:53760\n\t" "v_rcp_f32_e64 v54, v54\n\t"
    "v_mfma_f32_16x16x32_bf16 v[188:191], a[144:147], a[44:47], v[188:191]\n\t" "v_rcp_f32_e64 v55, v55\n\t" "v_mfma_f32_16x16x32_bf16 v[188:191], a[148:151], a[60:63], v[188:191]\n\t"
    "v_mul_f32 v164, v164, v52\n\t" "v_mul_f32_e64 v165, v165, v53\n\t" "s_waitcnt lgkmcnt(4)\n\t" "v_mfma_f32_16x16x32_bf16 v[64:67], a[152:155], a[64:67], v[64:67]\n\t"
    "ds_read_b64_tr_b16 a[136:137], %[vr3] offset:0\n\t" "v_mul_f32 v166, v166, v54\n\t" "v_mul_f32 v167, v167, v55\n\t" "v_cvt_pk_bf16_f32 v38, v164, v165\n\t"
    "v_mfma_f32_16x16x32_bf16 v[64:67], a[156:159], a[80:83], v[64:67]\n\t" "ds_read_b64_tr_b16 a[138:139], %[vr3] offset:3584\n\t" "v_cvt_pk_bf16_f32 v39, v166, v167\n\t"
    "global_store_dwordx4 %[va], v[36:39], s[90:91] offset:1024 sc1\n\t" "v_mfma_f32_16x16x32_bf16 v[68:71], a[152:155], a[68:71], v[68:71]\n\t" "v_mul_f32 v56, v56, v18\n\t" "v_mul_f32 v57, v57, v18\n\t"
    "v_mfma_f32_16x16x32_bf16 v[68:71], a[156:159], a[84:87], v[68:71]\n\t" "v_mul_f32 v58, v58, v18\n\t" "v_mul_f32 v59, v59, v18\n\t"
    "v_mfma_f32_16x16x32_bf16 v[72:75], a[152:155], a[72:75], v[72:75]\n\t" "ds_read_b64_tr_b16 a[140:141], %[vr3] offset:14336\n\t" "v_exp_f32 v56, v56\n\t" "v_exp_f32 v57, v57\n\t"
    "v_mfma_f32_16x16x32_bf16 v[72:75], a[156:159], a[88:91], v[72:75]\n\t" "ds_read_b64_tr_b16 a[142:143], %[vr3] offset:17920\n\t" "v_exp_f32_e64 v58, v58\n\t"
    "v_mfma_f32_16x16x32_bf16 v[76:79], a[152:155], a[76:79], v[76:79]\n\t" "v_exp_f32_e64 v59, v59\n\t" "v_mfma_f32_16x16x32_bf16 v[76:79], a[156:159], a[92:95], v[76:79]\n\t"
    "v_fma_f32 v56, v56, v22, v22\n\t" "v_fma_f32 v57, v57, v22, v22\n\t" "s_waitcnt lgkmcnt(4)\n\t" "v_mfma_f32_16x16x32_bf16 v[176:179], a[128:131], a[64:67], v[176:179]\n\t"
    "ds_read_b64_tr_b16 a[144:145], %[vr3] offset:7168\n\t" "v_fma_f32 v58, v58, v22, v22\n\t" "v_fma_f32 v59, v59, v22, v22\n\t" "v_rcp_f32 v56, v56\n\t"
    "v_mfma_f32_16x16x32_bf16 v[176:179], a[132:135], a[80:83], v[176:179]\n\t" "ds_read_b64_tr_b16 a[146:147], %[vr3] offset:10752\n\t" "v_rcp_f32_e64 v57, v57\n\t"
    "v_mfma_f32_16x16x32_bf16 v[180:183], a[128:131], a[68:71], v[180:183]\n\t" "v_rcp_f32_e64 v58, v58\n\t" "v_mfma_f32_16x16x32_bf16 v[180:183], a[132:135], a[84:87], v[180:183]\n\t"
    "v_rcp_f32_e64 v59, v59\n\t" "v_mfma_f32_16x16x32_bf16 v[184:187], a[128:131], a[72:75], v[184:187]\n\t" "ds_read_b64_tr_b16 a[148:149], %[vr3] offset:21504\n\t" "v_mul_f32 v168, v168, v56\n\t"
    "v_mul_f32 v169, v169, v57\n\t" "v_mul_f32_e64 v170, v170, v58\n\t" "v_mfma_f32_16x16x32_bf16 v[184:187], a[132:135], a[88:91], v[184:187]\n\t" "ds_read_b64_tr_b16 a[150:151], %[vr3] offset:25088\n\t"
    "v_mul_f32_e64 v171, v171, v59\n\t" "v_cvt_pk_bf16_f32 v42, v168, v169\n\t" "v_mfma_f32_16x16x32_bf16 v[188:191], a[128:131], a[76:79], v[188:191]\n\t" "v_cvt_pk_bf16_f32 v43, v170, v171\n\t"
    "global_store_dwordx4 %[va], v[40:43], s[92:93] offset:0 sc1\n\t" "v_mfma_f32_16x16x32_bf16 v[188:191], a[132:135], a[92:95], v[188:191]\n\t" "v_mul_f32 v60, v60, v19\n\t"
    "v_mul_f32_e64 v61, v61, v19\n\t" "s_waitcnt lgkmcnt(4)\n\t" "v_mfma_f32_16x16x32_bf16 v[64:67], a[136:139], a[96:99], v[64:67]\n\t" "ds_read_b128 a[152:155], v31 offset:512\n\t"
    "v_mul_f32 v62, v62, v19\n\t" "v_mul_f32 v63, v63, v19\n\t" "v_exp_f32_e64 v60, v60\n\t" "v_mfma_f32_16x16x32_bf16 v[64:67], a[140:143], a[112:115], v[64:67]\n\t" "v_exp_f32_e64 v61, v61\n\t"
    "v_mfma_f32_16x16x32_bf16 v[68:71], a[136:139], a[100:103], v[68:71]\n\t" "v_exp_f32_e64 v62, v62\n\t" "v_mfma_f32_16x16x32_bf16 v[68:71], a[140:143], a[116:119], v[68:71]\n\t"
    "v_exp_f32_e64 v63, v63\n\t" "v_mfma_f32_16x16x32_bf16 v[72:75], a[136:139], a[104:107], v[72:75]\n\t" "ds_read_b128 a[156:159], v31 offset:768\n\t" "v_fma_f32 v60, v60, v23, v23\n\t"
    "v_fma_f32 v61, v61, v23, v23\n\t" "v_fma_f32 v62, v62, v23, v23\n\t" "v_mfma_f32_16x16x32_bf16 v[72:75], a[140:143], a[120:123], v[72:75]\n\t" "v_fma_f32 v63, v63, v23, v23\n\t"
    "v_rcp_f32_e64 v60, v60\n\t" "v_mfma_f32_16x16x32_bf16 v[76:79], a[136:139], a[108:111], v[76:79]\n\t" "v_rcp_f32_e64 v61, v61\n\t"
    "v_mfma_f32_16x16x32_bf16 v[76:79], a[140:143], a[124:127], v[76:79]\n\t" "v_rcp_f32 v62, v62\n\t" "s_waitcnt lgkmcnt(2)\n\t"
    "v_mfma_f32_16x16x32_bf16 v[176:179], a[144:147], a[96:99], v[176:179]\n\t" "ds_read_b64_tr_b16 a[128:129], %[vr2] offset:32\n\t" "v_rcp_f32 v63, v63\n\t" "v_mul_f32 v172, v172, v60\n\t"
    "v_mfma_f32_16x16x32_bf16 v[176:179], a[148:151], a[112:115], v[176:179]\n\t" "ds_read_b64_tr_b16 a[130:131], %[vr2] offset:3616\n\t" "v_mul_f32 v173, v173, v61\n\t" "v_mul_f32 v174, v174, v62\n\t"
    "v_mfma_f32_16x16x32_bf16 v[180:183], a[144:147], a[100:103], v[180:183]\n\t" "v_mul_f32_e64 v175, v175, v63\n\t" "v_cvt_pk_bf16_f32 v46, v172, v173\n\t"
    "v_mfma_f32_16x16x32_bf16 v[180:183], a[148:151], a[116:119], v[180:183]\n\t" "v_cvt_pk_bf16_f32 v47, v174, v175\n\t" "global_store_dwordx4 %[va], v[44:47], s[92:93] offset:1024 sc1\n\t"
    "v_mfma_f32_16x16x32_bf16 v[184:187], a[144:147], a[104:107], v[184:187]\n\t" "ds_read_b64_tr_b16 a[132:133], %[vr2] offset:14368\n\t"
    "v_mfma_f32_16x16x32_bf16 v[184:187], a[148:151], a[120:123], v[184:187]\n\t" "ds_read_b64_tr_b16 a[134:135], %[vr2] offset:17952\n\t"
    "v_mfma_f32_16x16x32_bf16 v[188:191], a[144:147], a[108:111], v[188:191]\n\t" "v_mfma_f32_16x16x32_bf16 v[188:191], a[148:151], a[124:127], v[188:191]\n\t" "s_waitcnt lgkmcnt(4)\n\t"
    "v_mfma_f32_16x16x32_bf16 v[80:83], a[152:155], a[200:203], v[80:83]\n\t" "ds_read_b64_tr_b16 a[136:137], %[vr2] offset:7200\n\t" "v_mul_f32 v64, v64, v16\n\t" "v_mul_f32 v65, v65, v16\n\t"
    "v_mul_f32 v66, v66, v16\n\t" "v_mfma_f32_16x16x32_bf16 v[84:87], a[152:155], a[204:207], v[84:87]\n\t" "ds_read_b64_tr_b16 a[138:139], %[vr2] offset:10784\n\t" "v_mul_f32 v67, v67, v16\n\t"
    "v_exp_f32 v64, v64\n\t" "v_mfma_f32_16x16x32_bf16 v[88:91], a[152:155], a[208:211], v[88:91]\n\t" "v_accvgpr_read_b32 v29, %[vbd]\n\t" "s_nop 0\n\t" "s_add_u32 m0, %[sld], 24576\n\t" "s_nop 0\n\t"
    "global_load_lds_dwordx4 v29, %[sbw] offset:0\n\t" "v_exp_f32_e64 v65, v65\n\t" "v_mfma_f32_16x16x32_bf16 v[92:95], a[152:155], a[212:215], v[92:95]\n\t" "v_exp_f32_e64 v66, v66\n\t"
    "v_mfma_f32_16x16x32_bf16 v[192:195], a[156:159], a[200:203], v[192:195]\n\t" "ds_read_b64_tr_b16 a[140:141], %[vr2] offset:21536\n\t" "v_exp_f32_e64 v67, v67\n\t" "v_fma_f32 v64, v64, v20, v20\n\t"
    "v_mfma_f32_16x16x32_bf16 v[196:199], a[156:159], a[204:207], v[196:199]\n\t" "ds_read_b64_tr_b16 a[142:143], %[vr2] offset:25120\n\t" "v_fma_f32 v65, v65, v20, v20\n\t"
    "v_fma_f32 v66, v66, v20, v20\n\t" "v_mfma_f32_16x16x32_bf16 v[200:203], a[156:159], a[208:211], v[200:203]\n\t" "v_fma_f32 v67, v67, v20, v20\n\t" "v_rcp_f32_e64 v64, v64\n\t"
    "v_mfma_f32_16x16x32_bf16 v[204:207], a[156:159], a[212:215], v[204:207]\n\t" "v_rcp_f32 v65, v65\n\t" "s_waitcnt lgkmcnt(4)\n\t"
    "v_mfma_f32_16x16x32_bf16 v[80:83], a[128:131], a[32:35], v[80:83]\n\t" "ds_read_b64_tr_b16 a[144:145], %[vr2] offset:28704\n\t" "v_rcp_f32 v66, v66\n\t" "v_rcp_f32 v67, v67\n\t"
    "v_mfma_f32_16x16x32_bf16 v[80:83], a[132:135], a[48:51], v[80:83]\n\t" "ds_read_b64_tr_b16 a[146:147], %[vr2] offset:32288\n\t" "v_mul_f32 v176, v176, v64\n\t" "v_mul_f32 v177, v177, v65\n\t"
    "v_mfma_f32_16x16x32_bf16 v[84:87], a[128:131], a[36:39], v[84:87]\n\t" "s_add_u32 m0, %[sld], 28544\n\t" "s_nop 0\n\t" "global_load_lds_dwordx4 v29, %[sbw] offset:128\n\t"
    "v_add_u32 v29, 0x10000, v29\n\t" "v_mul_f32 v178, v178, v66\n\t" "v_mul_f32_e64 v179, v179, v67\n\t" "v_mfma_f32_16x16x32_bf16 v[84:87], a[132:135], a[52:55], v[84:87]\n\t"
    "v_cvt_pk_bf16_f32 v64, v176, v177\n\t" "v_cvt_pk_bf16_f32 v65, v178, v179\n\t" "v_mfma_f32_16x16x32_bf16 v[88:91], a[128:131], a[40:43], v[88:91]\n\t"
    "ds_read_b64_tr_b16 a[148:149], %[vr2] offset:43040\n\t" "v_mul_f32 v68, v68, v17\n\t" "v_mul_f32 v69, v69, v17\n\t" "v_mul_f32_e64 v70, v70, v17\n\t"
    "v_mfma_f32_16x16x32_bf16 v[88:91], a[132:135], a[56:59], v[88:91]\n\t" "ds_read_b64_tr_b16 a[150:151], %[vr2] offset:46624\n\t" "v_mul_f32 v71, v71, v17\n\t" "v_exp_f32 v68, v68\n\t"
    "v_mfma_f32_16x16x32_bf16 v[92:95], a[128:131], a[44:47], v[92:95]\n\t" "v_exp_f32_e64 v69, v69\n\t" "v_mfma_f32_16x16x32_bf16 v[92:95], a[132:135], a[60:63], v[92:95]\n\t" "v_exp_f32 v70, v70\n\t"
    "s_waitcnt lgkmcnt(4)\n\t" "v_mfma_f32_16x16x32_bf16 v[192:195], a[136:139], a[32:35], v[192:195]\n\t" "ds_read_b64_tr_b16 a[152:153], %[vr2] offset:35872\n\t" "v_exp_f32_e64 v71, v71\n\t"
    "v_fma_f32 v68, v68, v21, v21\n\t" "v_mfma_f32_16x16x32_bf16 v[192:195], a[140:143], a[48:51], v[192:195]\n\t" "ds_read_b64_tr_b16 a[154:155], %[vr2] offset:39456\n\t"
    "v_fma_f32 v69, v69, v21, v21\n\t" "v_fma_f32 v70, v70, v21, v21\n\t" "v_mfma_f32_16x16x32_bf16 v[196:199], a[136:139], a[36:39], v[196:199]\n\t" "s_add_u32 m0, %[sld], 32768\n\t" "s_nop 0\n\t"
    "global_load_lds_dwordx4 v29, %[sbw] offset:0\n\t" "v_fma_f32 v71, v71, v21, v21\n\t" "v_rcp_f32 v68, v68\n\t" "v_mfma_f32_16x16x32_bf16 v[196:199], a[140:143], a[52:55], v[196:199]\n\t"
    "v_rcp_f32_e64 v69, v69\n\t" "v_mfma_f32_16x16x32_bf16 v[200:203], a[136:139], a[40:43], v[200:203]\n\t" "ds_read_b64_tr_b16 a[156:157], %[vr2] offset:50208\n\t" "v_rcp_f32 v70, v70\n\t"
    "v_rcp_f32 v71, v71\n\t" "v_mfma_f32_16x16x32_bf16 v[200:203], a[140:143], a[56:59], v[200:203]\n\t" "ds_read_b64_tr_b16 a[158:159], %[vr2] offset:53792\n\t" "v_mul_f32 v180, v180, v68\n\t"
    "v_mul_f32 v181, v181, v69\n\t" "v_mfma_f32_16x16x32_bf16 v[204:207], a[136:139], a[44:47], v[204:207]\n\t" "v_mul_f32 v182, v182, v70\n\t" "v_mul_f32 v183, v183, v71\n\t"
    "v_mfma_f32_16x16x32_bf16 v[204:207], a[140:143], a[60:63], v[204:207]\n\t" "v_cvt_pk_bf16_f32 v68, v180, v181\n\t" "v_cvt_pk_bf16_f32 v69, v182, v183\n\t" "s_waitcnt lgkmcnt(4)\n\t"
    "v_mfma_f32_16x16x32_bf16 v[80:83], a[144:147], a[64:67], v[80:83]\n\t" "ds_read_b64_tr_b16 a[128:129], %[vr3] offset:32\n\t" "v_mul_f32 v72, v72, v18\n\t" "v_mul_f32 v73, v73, v18\n\t"
    "v_mul_f32 v74, v74, v18\n\t" "v_mfma_f32_16x16x32_bf16 v[80:83], a[148:151], a[80:83], v[80:83]\n\t" "ds_read_b64_tr_b16 a[130:131], %[vr3] offset:3616\n\t" "v_mul_f32 v75, v75, v18\n\t"
    "v_exp_f32 v72, v72\n\t" "v_mfma_f32_16x16x32_bf16 v[84:87], a[144:147], a[68:71], v[84:87]\n\t" "s_add_u32 m0, %[sld], 36736\n\t" "s_nop 0\n\t" "global_load_lds_dwordx4 v29, %[sbw] offset:128\n\t"
    "v_add_u32 v29, 0x10000, v29\n\t" "v_exp_f32 v73, v73\n\t" "v_mfma_f32_16x16x32_bf16 v[84:87], a[148:151], a[84:87], v[84:87]\n\t" "v_exp_f32_e64 v74, v74\n\t"
    "v_mfma_f32_16x16x32_bf16 v[88:91], a[144:147], a[72:75], v[88:91]\n\t" "ds_read_b64_tr_b16 a[132:133], %[vr3] offset:14368\n\t" "v_exp_f32_e64 v75, v75\n\t" "v_fma_f32 v72, v72, v22, v22\n\t"
    "v_mfma_f32_16x16x32_bf16 v[88:91], a[148:151], a[88:91], v[88:91]\n\t" "ds_read_b64_tr_b16 a[134:135], %[vr3] offset:17952\n\t" "v_fma_f32 v73, v73, v22, v22\n\t" "v_fma_f32 v74, v74, v22, v22\n\t"
    "v_mfma_f32_16x16x32_bf16 v[92:95], a[144:147], a[76:79], v[92:95]\n\t" "v_fma_f32 v75, v75, v22, v22\n\t" "v_rcp_f32_e64 v72, v72\n\t"
    "v_mfma_f32_16x16x32_bf16 v[92:95], a[148:151], a[92:95], v[92:95]\n\t" "v_rcp_f32 v73, v73\n\t" "s_waitcnt lgkmcnt(4)\n\t" "v_mfma_f32_16x16x32_bf16 v[192:195], a[152:155], a[64:67], v[192:195]\n\t"
    "ds_read_b64_tr_b16 a[136:137], %[vr3] offset:7200\n\t" "v_rcp_f32 v74, v74\n\t" "v_rcp_f32 v75, v75\n\t" "v_mfma_f32_16x16x32_bf16 v[192:195], a[156:159], a[80:83], v[192:195]\n\t"
    "ds_read_b64_tr_b16 a[138:139], %[vr3] offset:10784\n\t" "v_mul_f32 v184, v184, v72\n\t" "v_mul_f32 v185, v185, v73\n\t" "v_mfma_f32_16x16x32_bf16 v[196:199], a[152:155], a[68:71], v[196:199]\n\t"
    "s_add_u32 m0, %[sld], 40960\n\t" "s_nop 0\n\t" "global_load_lds_dwordx4 v29, %[sbw] offset:0\n\t" "v_mul_f32 v186, v186, v74\n\t" "v_mul_f32_e64 v187, v187, v75\n\t"
    "v_mfma_f32_16x16x32_bf16 v[196:199], a[156:159], a[84:87], v[196:199]\n\t" "v_cvt_pk_bf16_f32 v72, v184, v185\n\t" "v_cvt_pk_bf16_f32 v73, v186, v187\n\t"
    "v_mfma_f32_16x16x32_bf16 v[200:203], a[152:155], a[72:75], v[200:203]\n\t" "ds_read_b64_tr_b16 a[140:141], %[vr3] offset:21536\n\t" "v_mul_f32 v76, v76, v19\n\t" "v_mul_f32 v77, v77, v19\n\t"
    "v_mul_f32_e64 v78, v78, v19\n\t" "v_mfma_f32_16x16x32_bf16 v[200:203], a[156:159], a[88:91], v[200:203]\n\t" "ds_read_b64_tr_b16 a[142:143], %[vr3] offset:25120\n\t" "v_mul_f32 v79, v79, v19\n\t"
    "v_exp_f32 v76, v76\n\t" "v_mfma_f32_16x16x32_bf16 v[204:207], a[152:155], a[76:79], v[204:207]\n\t" "v_exp_f32_e64 v77, v77\n\t"
    "v_mfma_f32_16x16x32_bf16 v[204:207], a[156:159], a[92:95], v[204:207]\n\t" "v_exp_f32 v78, v78\n\t" "s_waitcnt lgkmcnt(4)\n\t" "v_mfma_f32_16x16x32_bf16 v[80:83], a[128:131], a[96:99], v[80:83]\n\t"
    "v_accvgpr_read_b32 v31, %[ct]\n\t" "s_mov_b64 exec, 0xffff\n\t" "v_add_u32 v31, %[gt2], v31\n\t" "s_mov_b64 exec, -1\n\t" "ds_read_b128 a[144:147], v31 offset:0\n\t" "v_exp_f32_e64 v79, v79\n\t"
    "v_fma_f32 v76, v76, v23, v23\n\t" "v_mfma_f32_16x16x32_bf16 v[80:83], a[132:135], a[112:115], v[80:83]\n\t" "v_fma_f32 v77, v77, v23, v23\n\t" "v_fma_f32 v78, v78, v23, v23\n\t"
    "v_mfma_f32_16x16x32_bf16 v[84:87], a[128:131], a[100:103], v[84:87]\n\t" "s_add_u32 m0, %[sld], 44928\n\t" "s_nop 0\n\t" "global_load_lds_dwordx4 v29, %[sbw] offset:128\n\t"
    "v_add_u32 v29, 0x10000, v29\n\t" "v_fma_f32 v79, v79, v23, v23\n\t" "v_rcp_f32 v76, v76\n\t" "v_mfma_f32_16x16x32_bf16 v[84:87], a[132:135], a[116:119], v[84:87]\n\t" "v_rcp_f32_e64 v77, v77\n\t"
    "v_mfma_f32_16x16x32_bf16 v[88:91], a[128:131], a[104:107], v[88:91]\n\t" "ds_read_b128 a[148:151], v31 offset:256\n\t" "v_rcp_f32 v78, v78\n\t" "v_rcp_f32 v79, v79\n\t"
    "v_mfma_f32_16x16x32_bf16 v[88:91], a[132:135], a[120:123], v[88:91]\n\t" "v_mul_f32 v188, v188, v76\n\t" "v_mul_f32 v189, v189, v77\n\t"
    "v_mfma_f32_16x16x32_bf16 v[92:95], a[128:131], a[108:111], v[92:95]\n\t" "v_mul_f32 v190, v190, v78\n\t" "v_mul_f32 v191, v191, v79\n\t"
    "v_mfma_f32_16x16x32_bf16 v[92:95], a[132:135], a[124:127], v[92:95]\n\t" "v_cvt_pk_bf16_f32 v76, v188, v189\n\t" "v_cvt_pk_bf16_f32 v77, v190, v191\n\t" "s_waitcnt lgkmcnt(2)\n\t"
    "v_mfma_f32_16x16x32_bf16 v[192:195], a[136:139], a[96:99], v[192:195]\n\t" "ds_read_b64_tr_b16 a[152:153], %[vr4] offset:0\n\t"
    "v_mfma_f32_16x16x32_bf16 v[192:195], a[140:143], a[112:115], v[192:195]\n\t" "ds_read_b64_tr_b16 a[154:155], %[vr4] offset:3584\n\t"
    "v_mfma_f32_16x16x32_bf16 v[196:199], a[136:139], a[100:103], v[196:199]\n\t" "s_add_u32 m0, %[sld], 139264\n\t" "s_nop 0\n\t" "global_load_lds_dwordx4 v29, %[sbw] offset:0\n\t"
    "v_mfma_f32_16x16x32_bf16 v[196:199], a[140:143], a[116:119], v[196:199]\n\t" "v_mfma_f32_16x16x32_bf16 v[200:203], a[136:139], a[104:107], v[200:203]\n\t"
    "ds_read_b64_tr_b16 a[156:157], %[vr4] offset:14336\n\t" "v_mfma_f32_16x16x32_bf16 v[200:203], a[140:143], a[120:123], v[200:203]\n\t" "ds_read_b64_tr_b16 a[158:159], %[vr4] offset:17920\n\t"
    "v_mfma_f32_16x16x32_bf16 v[204:207], a[136:139], a[108:111], v[204:207]\n\t" "v_mfma_f32_16x16x32_bf16 v[204:207], a[140:143], a[124:127], v[204:207]\n\t" "s_waitcnt lgkmcnt(4)\n\t"
    "v_mfma_f32_16x16x32_bf16 v[96:99], a[144:147], a[200:203], v[96:99]\n\t" "ds_read_b64_tr_b16 a[128:129], %[vr4] offset:7168\n\t" "s_add_u32 s90, %[shl], %[go1]\n\t" "s_addc_u32 s91, %[shh], 0\n\t"
    "s_add_u32 s92, s90, 229376\n\t" "s_addc_u32 s93, s91, 0\n\t" "v_mul_f32_e64 v80, v80, v16\n\t" "v_mfma_f32_16x16x32_bf16 v[100:103], a[144:147], a[204:207], v[100:103]\n\t"
    "ds_read_b64_tr_b16 a[130:131], %[vr4] offset:10752\n\t" "v_mul_f32 v81, v81, v16\n\t" "v_mul_f32 v82, v82, v16\n\t" "v_mfma_f32_16x16x32_bf16 v[104:107], a[144:147], a[208:211], v[104:107]\n\t"
    "s_add_u32 m0, %[sld], 143232\n\t" "s_nop 0\n\t" "global_load_lds_dwordx4 v29, %[sbw] offset:128\n\t" "v_add_u32 v29, 0x10000, v29\n\t" "v_mul_f32 v83, v83, v16\n\t" "v_exp_f32_e64 v80, v80\n\t"
    "v_mfma_f32_16x16x32_bf16 v[108:111], a[144:147], a[212:215], v[108:111]\n\t" "v_exp_f32_e64 v81, v81\n\t" "v_mfma_f32_16x16x32_bf16 v[208:211], a[148:151], a[200:203], v[208:211]\n\t"
    "ds_read_b64_tr_b16 a[132:133], %[vr4] offset:21504\n\t" "v_exp_f32 v82, v82\n\t" "v_exp_f32 v83, v83\n\t" "v_mfma_f32_16x16x32_bf16 v[212:215], a[148:151], a[204:207], v[212:215]\n\t"
    "ds_read_b64_tr_b16 a[134:135], %[vr4] offset:25088\n\t" "v_fma_f32 v80, v80, v20, v20\n\t" "v_fma_f32 v81, v81, v20, v20\n\t"
    "v_mfma_f32_16x16x32_bf16 v[216:219], a[148:151], a[208:211], v[216:219]\n\t" "v_fma_f32 v82, v82, v20, v20\n\t" "v_fma_f32 v83, v83, v20, v20\n\t"
    "v_mfma_f32_16x16x32_bf16 v[220:223], a[148:151], a[212:215], v[220:223]\n\t" "v_rcp_f32 v80, v80\n\t" "s_waitcnt lgkmcnt(4)\n\t"
    "v_mfma_f32_16x16x32_bf16 v[96:99], a[152:155], a[32:35], v[96:99]\n\t" "ds_read_b64_tr_b16 a[136:137], %[vr4] offset:28672\n\t" "v_rcp_f32 v81, v81\n\t" "v_rcp_f32 v82, v82\n\t"
    "v_mfma_f32_16x16x32_bf16 v[96:99], a[156:159], a[48:51], v[96:99]\n\t" "ds_read_b64_tr_b16 a[138:139], %[vr4] offset:32256\n\t" "v_rcp_f32_e64 v83, v83\n\t"
    "v_mfma_f32_16x16x32_bf16 v[100:103], a[152:155], a[36:39], v[100:103]\n\t" "s_add_u32 m0, %[sld], 147456\n\t" "s_nop 0\n\t" "global_load_lds_dwordx4 v29, %[sbw] offset:0\n\t"
    "v_mul_f32 v192, v192, v80\n\t" "v_mul_f32_e64 v193, v193, v81\n\t" "v_mfma_f32_16x16x32_bf16 v[100:103], a[156:159], a[52:55], v[100:103]\n\t" "v_mul_f32 v194, v194, v82\n\t"
    "v_mul_f32 v195, v195, v83\n\t" "v_mfma_f32_16x16x32_bf16 v[104:107], a[152:155], a[40:43], v[104:107]\n\t" "ds_read_b64_tr_b16 a[140:141], %[vr4] offset:43008\n\t"
    "v_cvt_pk_bf16_f32 v66, v192, v193\n\t" "v_cvt_pk_bf16_f32 v67, v194, v195\n\t" "global_store_dwordx4 %[va], v[64:67], s[90:91] offset:0 sc1\n\t"
    "v_mfma_f32_16x16x32_bf16 v[104:107], a[156:159], a[56:59], v[104:107]\n\t" "ds_read_b64_tr_b16 a[142:143], %[vr4] offset:46592\n\t" "v_mul_f32 v84, v84, v17\n\t" "v_mul_f32 v85, v85, v17\n\t"
    "v_mfma_f32_16x16x32_bf16 v[108:111], a[152:155], a[44:47], v[108:111]\n\t" "v_mul_f32 v86, v86, v17\n\t" "v_mul_f32 v87, v87, v17\n\t"
    "v_mfma_f32_16x16x32_bf16 v[108:111], a[156:159], a[60:63], v[108:111]\n\t" "v_exp_f32 v84, v84\n\t" "s_waitcnt lgkmcnt(4)\n\t"
    "v_mfma_f32_16x16x32_bf16 v[208:211], a[128:131], a[32:35], v[208:211]\n\t" "ds_read_b64_tr_b16 a[144:145], %[vr4] offset:35840\n\t" "v_exp_f32 v85, v85\n\t" "v_exp_f32 v86, v86\n\t"
    "v_mfma_f32_16x16x32_bf16 v[208:211], a[132:135], a[48:51], v[208:211]\n\t" "ds_read_b64_tr_b16 a[146:147], %[vr4] offset:39424\n\t" "v_exp_f32_e64 v87, v87\n\t"
    "v_mfma_f32_16x16x32_bf16 v[212:215], a[128:131], a[36:39], v[212:215]\n\t" "s_add_u32 m0, %[sld], 151424\n\t" "s_nop 0\n\t" "global_load_lds_dwordx4 v29, %[sbw] offset:128\n\t"
    "v_add_u32 v29, 0x10000, v29\n\t" "v_fma_f32 v84, v84, v21, v21\n\t" "v_fma_f32 v85, v85, v21, v21\n\t" "v_mfma_f32_16x16x32_bf16 v[212:215], a[132:135], a[52:55], v[212:215]\n\t"
    "v_fma_f32 v86, v86, v21, v21\n\t" "v_fma_f32 v87, v87, v21, v21\n\t" "v_mfma_f32_16x16x32_bf16 v[216:219], a[128:131], a[40:43], v[216:219]\n\t"
    "ds_read_b64_tr_b16 a[148:149], %[vr4] offset:50176\n\t" "v_rcp_f32 v84, v84\n\t" "v_rcp_f32_e64 v85, v85\n\t" "v_mfma_f32_16x16x32_bf16 v[216:219], a[132:135], a[56:59], v[216:219]\n\t"
    "ds_read_b64_tr_b16 a[150:151], %[vr4] offset:53760\n\t" "v_rcp_f32_e64 v86, v86\n\t" "v_mfma_f32_16x16x32_bf16 v[220:223], a[128:131], a[44:47], v[220:223]\n\t" "v_rcp_f32_e64 v87, v87\n\t"
    "v_mfma_f32_16x16x32_bf16 v[220:223], a[132:135], a[60:63], v[220:223]\n\t" "v_mul_f32 v196, v196, v84\n\t" "v_mul_f32_e64 v197, v197, v85\n\t" "s_waitcnt lgkmcnt(4)\n\t"
    "v_mfma_f32_16x16x32_bf16 v[96:99], a[136:139], a[64:67], v[96:99]\n\t" "ds_read_b64_tr_b16 a[152:153], %[vr5] offset:0\n\t" "v_mul_f32 v198, v198, v86\n\t" "v_mul_f32 v199, v199, v87\n\t"
    "v_cvt_pk_bf16_f32 v70, v196, v197\n\t" "v_mfma_f32_16x16x32_bf16 v[96:99], a[140:143], a[80:83], v[96:99]\n\t" "ds_read_b64_tr_b16 a[154:155], %[vr5] offset:3584\n\t"
    "v_cvt_pk_bf16_f32 v71, v198, v199\n\t" "global_store_dwordx4 %[va], v[68:71], s[90:91] offset:1024 sc1\n\t" "v_mfma_f32_16x16x32_bf16 v[100:103], a[136:139], a[68:71], v[100:103]\n\t"
    "s_add_u32 m0, %[sld], 155648\n\t" "s_nop 0\n\t" "global_load_lds_dwordx4 v29, %[sbw] offset:0\n\t" "v_mul_f32 v88, v88, v18\n\t" "v_mul_f32_e64 v89, v89, v18\n\t"
    "v_mfma_f32_16x16x32_bf16 v[100:103], a[140:143], a[84:87], v[100:103]\n\t" "v_mul_f32 v90, v90, v18\n\t" "v_mul_f32 v91, v91, v18\n\t"
    "v_mfma_f32_16x16x32_bf16 v[104:107], a[136:139], a[72:75], v[104:107]\n\t" "ds_read_b64_tr_b16 a[156:157], %[vr5] offset:14336\n\t" "v_exp_f32 v88, v88\n\t" "v_exp_f32 v89, v89\n\t"
    "v_mfma_f32_16x16x32_bf16 v[104:107], a[140:143], a[88:91], v[104:107]\n\t" "ds_read_b64_tr_b16 a[158:159], %[vr5] offset:17920\n\t" "v_exp_f32_e64 v90, v90\n\t"
    "v_mfma_f32_16x16x32_bf16 v[108:111], a[136:139], a[76:79], v[108:111]\n\t" "v_exp_f32_e64 v91, v91\n\t" "v_mfma_f32_16x16x32_bf16 v[108:111], a[140:143], a[92:95], v[108:111]\n\t"
    "v_fma_f32 v88, v88, v22, v22\n\t" "v_fma_f32 v89, v89, v22, v22\n\t" "s_waitcnt lgkmcnt(4)\n\t" "v_mfma_f32_16x16x32_bf16 v[208:211], a[144:147], a[64:67], v[208:211]\n\t"
    "ds_read_b64_tr_b16 a[128:129], %[vr5] offset:7168\n\t" "v_fma_f32 v90, v90, v22, v22\n\t" "v_fma_f32 v91, v91, v22, v22\n\t" "v_rcp_f32 v88, v88\n\t"
    "v_mfma_f32_16x16x32_bf16 v[208:211], a[148:151], a[80:83], v[208:211]\n\t" "ds_read_b64_tr_b16 a[130:131], %[vr5] offset:10752\n\t" "v_rcp_f32_e64 v89, v89\n\t"
    "v_mfma_f32_16x16x32_bf16 v[212:215], a[144:147], a[68:71], v[212:215]\n\t" "s_add_u32 m0, %[sld], 159616\n\t" "s_nop 0\n\t" "global_load_lds_dwordx4 v29, %[sbw] offset:128\n\t"
    "v_add_u32 v29, 0x10000, v29\n\t" "v_rcp_f32 v90, v90\n\t" "v_mfma_f32_16x16x32_bf16 v[212:215], a[148:151], a[84:87], v[212:215]\n\t" "v_rcp_f32_e64 v91, v91\n\t"
    "v_mfma_f32_16x16x32_bf16 v[216:219], a[144:147], a[72:75], v[216:219]\n\t" "ds_read_b64_tr_b16 a[132:133], %[vr5] offset:21504\n\t" "v_mul_f32 v200, v200, v88\n\t" "v_mul_f32 v201, v201, v89\n\t"
    "v_mul_f32_e64 v202, v202, v90\n\t" "v_mfma_f32_16x16x32_bf16 v[216:219], a[148:151], a[88:91], v[216:219]\n\t" "ds_read_b64_tr_b16 a[134:135], %[vr5] offset:25088\n\t"
    "v_mul_f32_e64 v203, v203, v91\n\t" "v_cvt_pk_bf16_f32 v74, v200, v201\n\t" "v_mfma_f32_16x16x32_bf16 v[220:223], a[144:147], a[76:79], v[220:223]\n\t" "v_cvt_pk_bf16_f32 v75, v202, v203\n\t"
    "global_store_dwordx4 %[va], v[72:75], s[92:93] offset:0 sc1\n\t" "v_mfma_f32_16x16x32_bf16 v[220:223], a[148:151], a[92:95], v[220:223]\n\t" "v_mul_f32 v92, v92, v19\n\t"
    "v_mul_f32_e64 v93, v93, v19\n\t" "s_waitcnt lgkmcnt(4)\n\t" "v_mfma_f32_16x16x32_bf16 v[96:99], a[152:155], a[96:99], v[96:99]\n\t" "ds_read_b128 a[136:139], v31 offset:512\n\t"
    "v_mul_f32 v94, v94, v19\n\t" "v_mul_f32 v95, v95, v19\n\t" "v_exp_f32_e64 v92, v92\n\t" "v_mfma_f32_16x16x32_bf16 v[96:99], a[156:159], a[112:115], v[96:99]\n\t" "v_exp_f32_e64 v93, v93\n\t"
    "v_mfma_f32_16x16x32_bf16 v[100:103], a[152:155], a[100:103], v[100:103]\n\t" "s_add_u32 m0, %[sld], 0\n\t" "s_nop 0\n\t" "global_load_lds_dwordx4 v29, %[sbw] offset:0\n\t"
    "v_exp_f32_e64 v94, v94\n\t" "v_mfma_f32_16x16x32_bf16 v[100:103], a[156:159], a[116:119], v[100:103]\n\t" "v_exp_f32_e64 v95, v95\n\t"
    "v_mfma_f32_16x16x32_bf16 v[104:107], a[152:155], a[104:107], v[104:107]\n\t" "ds_read_b128 a[140:143], v31 offset:768\n\t" "v_fma_f32 v92, v92, v23, v23\n\t" "v_fma_f32 v93, v93, v23, v23\n\t"
    "v_fma_f32 v94, v94, v23, v23\n\t" "v_mfma_f32_16x16x32_bf16 v[104:107], a[156:159], a[120:123], v[104:107]\n\t" "v_fma_f32 v95, v95, v23, v23\n\t" "v_rcp_f32_e64 v92, v92\n\t"
    "v_mfma_f32_16x16x32_bf16 v[108:111], a[152:155], a[108:111], v[108:111]\n\t" "v_rcp_f32_e64 v93, v93\n\t" "v_mfma_f32_16x16x32_bf16 v[108:111], a[156:159], a[124:127], v[108:111]\n\t"
    "v_rcp_f32 v94, v94\n\t" "s_waitcnt lgkmcnt(2)\n\t" "v_mfma_f32_16x16x32_bf16 v[208:211], a[128:131], a[96:99], v[208:211]\n\t" "ds_read_b64_tr_b16 a[144:145], %[vr4] offset:32\n\t"
    "v_rcp_f32 v95, v95\n\t" "v_mul_f32 v204, v204, v92\n\t" "v_mfma_f32_16x16x32_bf16 v[208:211], a[132:135], a[112:115], v[208:211]\n\t" "ds_read_b64_tr_b16 a[146:147], %[vr4] offset:3616\n\t"
    "v_mul_f32 v205, v205, v93\n\t" "v_mul_f32 v206, v206, v94\n\t" "v_mfma_f32_16x16x32_bf16 v[212:215], a[128:131], a[100:103], v[212:215]\n\t" "s_add_u32 m0, %[sld], 3968\n\t" "s_nop 0\n\t"
    "global_load_lds_dwordx4 v29, %[sbw] offset:128\n\t" "v_add_u32 v29, 0x10000, v29\n\t" "v_mul_f32 v207, v207, v95\n\t" "v_cvt_pk_bf16_f32 v78, v204, v205\n\t"
    "v_mfma_f32_16x16x32_bf16 v[212:215], a[132:135], a[116:119], v[212:215]\n\t" "v_cvt_pk_bf16_f32 v79, v206, v207\n\t" "global_store_dwordx4 %[va], v[76:79], s[92:93] offset:1024 sc1\n\t"
    "v_mfma_f32_16x16x32_bf16 v[216:219], a[128:131], a[104:107], v[216:219]\n\t" "ds_read_b64_tr_b16 a[148:149], %[vr4] offset:14368\n\t"
    "v_mfma_f32_16x16x32_bf16 v[216:219], a[132:135], a[120:123], v[216:219]\n\t" "ds_read_b64_tr_b16 a[150:151], %[vr4] offset:17952\n\t"
    "v_mfma_f32_16x16x32_bf16 v[220:223], a[128:131], a[108:111], v[220:223]\n\t" "v_mfma_f32_16x16x32_bf16 v[220:223], a[132:135], a[124:127], v[220:223]\n\t" "s_waitcnt lgkmcnt(4)\n\t"
    "v_mfma_f32_16x16x32_bf16 v[112:115], a[136:139], a[200:203], v[112:115]\n\t" "ds_read_b64_tr_b16 a[152:153], %[vr4] offset:7200\n\t" "v_mul_f32 v96, v96, v16\n\t" "v_mul_f32 v97, v97, v16\n\t"
    "v_mul_f32 v98, v98, v16\n\t" "v_mfma_f32_16x16x32_bf16 v[116:119], a[136:139], a[204:207], v[116:119]\n\t" "ds_read_b64_tr_b16 a[154:155], %[vr4] offset:10784\n\t" "v_mul_f32 v99, v99, v16\n\t"
    "v_exp_f32 v96, v96\n\t" "v_mfma_f32_16x16x32_bf16 v[120:123], a[136:139], a[208:211], v[120:123]\n\t" "s_add_u32 m0, %[sld], 8192\n\t" "s_nop 0\n\t" "global_load_lds_dwordx4 v29, %[sbw] offset:0\n\t"
    "v_exp_f32 v97, v97\n\t" "v_mfma_f32_16x16x32_bf16 v[124:127], a[136:139], a[212:215], v[124:127]\n\t" "v_exp_f32_e64 v98, v98\n\t"
    "v_mfma_f32_16x16x32_bf16 v[224:227], a[140:143], a[200:203], v[224:227]\n\t" "ds_read_b64_tr_b16 a[156:157], %[vr4] offset:21536\n\t" "v_exp_f32_e64 v99, v99\n\t" "v_fma_f32 v96, v96, v20, v20\n\t"
    "v_mfma_f32_16x16x32_bf16 v[228:231], a[140:143], a[204:207], v[228:231]\n\t" "ds_read_b64_tr_b16 a[158:159], %[vr4] offset:25120\n\t" "v_fma_f32 v97, v97, v20, v20\n\t"
    "v_fma_f32 v98, v98, v20, v20\n\t" "v_mfma_f32_16x16x32_bf16 v[232:235], a[140:143], a[208:211], v[232:235]\n\t" "v_fma_f32 v99, v99, v20, v20\n\t" "v_rcp_f32_e64 v96, v96\n\t"
    "v_mfma_f32_16x16x32_bf16 v[236:239], a[140:143], a[212:215], v[236:239]\n\t" "v_rcp_f32 v97, v97\n\t" "s_waitcnt lgkmcnt(4)\n\t"
    "v_mfma_f32_16x16x32_bf16 v[112:115], a[144:147], a[32:35], v[112:115]\n\t" "ds_read_b64_tr_b16 a[128:129], %[vr4] offset:28704\n\t" "v_rcp_f32 v98, v98\n\t" "v_rcp_f32 v99, v99\n\t"
    "v_mfma_f32_16x16x32_bf16 v[112:115], a[148:151], a[48:51], v[112:115]\n\t" "ds_read_b64_tr_b16 a[130:131], %[vr4] offset:32288\n\t" "v_mul_f32 v208, v208, v96\n\t" "v_mul_f32 v209, v209, v97\n\t"
    "v_mfma_f32_16x16x32_bf16 v[116:119], a[144:147], a[36:39], v[116:119]\n\t" "s_add_u32 m0, %[sld], 12160\n\t" "s_nop 0\n\t" "global_load_lds_dwordx4 v29, %[sbw] offset:128\n\t"
    "v_add_u32 v29, 0x10000, v29\n\t" "v_mul_f32 v210, v210, v98\n\t" "v_mul_f32_e64 v211, v211, v99\n\t" "v_mfma_f32_16x16x32_bf16 v[116:119], a[148:151], a[52:55], v[116:119]\n\t"
    "v_cvt_pk_bf16_f32 v96, v208, v209\n\t" "v_cvt_pk_bf16_f32 v97, v210, v211\n\t" "v_mfma_f32_16x16x32_bf16 v[120:123], a[144:147], a[40:43], v[120:123]\n\t"
    "ds_read_b64_tr_b16 a[132:133], %[vr4] offset:43040\n\t" "v_mul_f32 v100, v100, v17\n\t" "v_mul_f32 v101, v101, v17\n\t" "v_mul_f32_e64 v102, v102, v17\n\t"
    "v_mfma_f32_16x16x32_bf16 v[120:123], a[148:151], a[56:59], v[120:123]\n\t" "ds_read_b64_tr_b16 a[134:135], %[vr4] offset:46624\n\t" "v_mul_f32 v103, v103, v17\n\t" "v_exp_f32 v100, v100\n\t"
    "v_mfma_f32_16x16x32_bf16 v[124:127], a[144:147], a[44:47], v[124:127]\n\t" "v_exp_f32_e64 v101, v101\n\t" "v_mfma_f32_16x16x32_bf16 v[124:127], a[148:151], a[60:63], v[124:127]\n\t"
    "v_exp_f32 v102, v102\n\t" "s_waitcnt lgkmcnt(4)\n\t" "v_mfma_f32_16x16x32_bf16 v[224:227], a[152:155], a[32:35], v[224:227]\n\t" "ds_read_b64_tr_b16 a[136:137], %[vr4] offset:35872\n\t"
    "v_exp_f32_e64 v103, v103\n\t" "v_fma_f32 v100, v100, v21, v21\n\t" "v_mfma_f32_16x16x32_bf16 v[224:227], a[156:159], a[48:51], v[224:227]\n\t" "ds_read_b64_tr_b16 a[138:139], %[vr4] offset:39456\n\t"
    "v_fma_f32 v101, v101, v21, v21\n\t" "v_fma_f32 v102, v102, v21, v21\n\t" "v_mfma_f32_16x16x32_bf16 v[228:231], a[152:155], a[36:39], v[228:231]\n\t" "v_fma_f32 v103, v103, v21, v21\n\t"
    "v_rcp_f32_e64 v100, v100\n\t" "v_mfma_f32_16x16x32_bf16 v[228:231], a[156:159], a[52:55], v[228:231]\n\t" "v_rcp_f32_e64 v101, v101\n\t"
    "v_mfma_f32_16x16x32_bf16 v[232:235], a[152:155], a[40:43], v[232:235]\n\t" "ds_read_b64_tr_b16 a[140:141], %[vr4] offset:50208\n\t" "v_rcp_f32 v102, v102\n\t" "v_rcp_f32 v103, v103\n\t"
    "v_mfma_f32_16x16x32_bf16 v[232:235], a[156:159], a[56:59], v[232:235]\n\t" "ds_read_b64_tr_b16 a[142:143], %[vr4] offset:53792\n\t" "v_mul_f32 v212, v212, v100\n\t" "v_mul_f32 v213, v213, v101\n\t"
    "v_mfma_f32_16x16x32_bf16 v[236:239], a[152:155], a[44:47], v[236:239]\n\t" "v_mul_f32 v214, v214, v102\n\t" "v_mul_f32 v215, v215, v103\n\t"
    "v_mfma_f32_16x16x32_bf16 v[236:239], a[156:159], a[60:63], v[236:239]\n\t" "v_cvt_pk_bf16_f32 v100, v212, v213\n\t" "v_cvt_pk_bf16_f32 v101, v214, v215\n\t" "s_waitcnt lgkmcnt(4)\n\t"
    "v_mfma_f32_16x16x32_bf16 v[112:115], a[128:131], a[64:67], v[112:115]\n\t" "ds_read_b64_tr_b16 a[144:145], %[vr5] offset:32\n\t" "v_mul_f32 v104, v104, v18\n\t" "v_mul_f32 v105, v105, v18\n\t"
    "v_mul_f32 v106, v106, v18\n\t" "v_mfma_f32_16x16x32_bf16 v[112:115], a[132:135], a[80:83], v[112:115]\n\t" "ds_read_b64_tr_b16 a[146:147], %[vr5] offset:3616\n\t" "v_mul_f32 v107, v107, v18\n\t"
    "v_exp_f32 v104, v104\n\t" "v_mfma_f32_16x16x32_bf16 v[116:119], a[128:131], a[68:71], v[116:119]\n\t" "v_exp_f32_e64 v105, v105\n\t"
    "v_mfma_f32_16x16x32_bf16 v[116:119], a[132:135], a[84:87], v[116:119]\n\t" "v_exp_f32_e64 v106, v106\n\t" "v_mfma_f32_16x16x32_bf16 v[120:123], a[128:131], a[72:75], v[120:123]\n\t"
    "ds_read_b64_tr_b16 a[148:149], %[vr5] offset:14368\n\t" "v_exp_f32_e64 v107, v107\n\t" "v_fma_f32 v104, v104, v22, v22\n\t" "v_mfma_f32_16x16x32_bf16 v[120:123], a[132:135], a[88:91], v[120:123]\n\t"
    "ds_read_b64_tr_b16 a[150:151], %[vr5] offset:17952\n\t" "v_fma_f32 v105, v105, v22, v22\n\t" "v_fma_f32 v106, v106, v22, v22\n\t"
    "v_mfma_f32_16x16x32_bf16 v[124:127], a[128:131], a[76:79], v[124:127]\n\t" "v_fma_f32 v107, v107, v22, v22\n\t" "v_rcp_f32_e64 v104, v104\n\t"
    "v_mfma_f32_16x16x32_bf16 v[124:127], a[132:135], a[92:95], v[124:127]\n\t" "v_rcp_f32 v105, v105\n\t" "s_waitcnt lgkmcnt(4)\n\t"
    "v_mfma_f32_16x16x32_bf16 v[224:227], a[136:139], a[64:67], v[224:227]\n\t" "ds_read_b64_tr_b16 a[152:153], %[vr5] offset:7200\n\t" "v_rcp_f32 v106, v106\n\t" "v_rcp_f32 v107, v107\n\t"
    "v_mfma_f32_16x16x32_bf16 v[224:227], a[140:143], a[80:83], v[224:227]\n\t" "ds_read_b64_tr_b16 a[154:155], %[vr5] offset:10784\n\t" "v_mul_f32 v216, v216, v104\n\t" "v_mul_f32 v217, v217, v105\n\t"
    "v_mfma_f32_16x16x32_bf16 v[228:231], a[136:139], a[68:71], v[228:231]\n\t" "v_mul_f32 v218, v218, v106\n\t" "v_mul_f32 v219, v219, v107\n\t"
    "v_mfma_f32_16x16x32_bf16 v[228:231], a[140:143], a[84:87], v[228:231]\n\t" "v_cvt_pk_bf16_f32 v104, v216, v217\n\t" "v_cvt_pk_bf16_f32 v105, v218, v219\n\t"
    "v_mfma_f32_16x16x32_bf16 v[232:235], a[136:139], a[72:75], v[232:235]\n\t" "ds_read_b64_tr_b16 a[156:157], %[vr5] offset:21536\n\t" "v_mul_f32 v108, v108, v19\n\t" "v_mul_f32 v109, v109, v19\n\t"
    "v_mul_f32_e64 v110, v110, v19\n\t" "v_mfma_f32_16x16x32_bf16 v[232:235], a[140:143], a[88:91], v[232:235]\n\t" "ds_read_b64_tr_b16 a[158:159], %[vr5] offset:25120\n\t" "v_mul_f32 v111, v111, v19\n\t"
    "v_exp_f32 v108, v108\n\t" "v_mfma_f32_16x16x32_bf16 v[236:239], a[136:139], a[76:79], v[236:239]\n\t" "v_exp_f32_e64 v109, v109\n\t"
    "v_mfma_f32_16x16x32_bf16 v[236:239], a[140:143], a[92:95], v[236:239]\n\t" "v_exp_f32 v110, v110\n\t" "s_waitcnt lgkmcnt(4)\n\t"
    "v_mfma_f32_16x16x32_bf16 v[112:115], a[144:147], a[96:99], v[112:115]\n\t" "v_accvgpr_read_b32 v31, %[ct]\n\t" "s_mov_b64 exec, 0xffff\n\t" "v_add_u32 v31, %[gt3], v31\n\t" "s_mov_b64 exec, -1\n\t"
    "ds_read_b128 a[128:131], v31 offset:0\n\t" "v_exp_f32_e64 v111, v111\n\t" "v_fma_f32 v108, v108, v23, v23\n\t" "v_mfma_f32_16x16x32_bf16 v[112:115], a[148:151], a[112:115], v[112:115]\n\t"
    "v_fma_f32 v109, v109, v23, v23\n\t" "v_fma_f32 v110, v110, v23, v23\n\t" "v_mfma_f32_16x16x32_bf16 v[116:119], a[144:147], a[100:103], v[116:119]\n\t" "v_fma_f32 v111, v111, v23, v23\n\t"
    "v_rcp_f32_e64 v108, v108\n\t" "v_mfma_f32_16x16x32_bf16 v[116:119], a[148:151], a[116:119], v[116:119]\n\t" "v_rcp_f32_e64 v109, v109\n\t"
    "v_mfma_f32_16x16x32_bf16 v[120:123], a[144:147], a[104:107], v[120:123]\n\t" "ds_read_b128 a[132:135], v31 offset:256\n\t" "v_rcp_f32 v110, v110\n\t" "v_rcp_f32 v111, v111\n\t"
    "v_mfma_f32_16x16x32_bf16 v[120:123], a[148:151], a[120:123], v[120:123]\n\t" "v_mul_f32 v220, v220, v108\n\t" "v_mul_f32 v221, v221, v109\n\t"
    "v_mfma_f32_16x16x32_bf16 v[124:127], a[144:147], a[108:111], v[124:127]\n\t" "v_mul_f32 v222, v222, v110\n\t" "v_mul_f32 v223, v223, v111\n\t"
    "v_mfma_f32_16x16x32_bf16 v[124:127], a[148:151], a[124:127], v[124:127]\n\t" "v_cvt_pk_bf16_f32 v108, v220, v221\n\t" "v_cvt_pk_bf16_f32 v109, v222, v223\n\t" "s_waitcnt lgkmcnt(2)\n\t"
    "v_mfma_f32_16x16x32_bf16 v[224:227], a[152:155], a[96:99], v[224:227]\n\t" "ds_read_b64_tr_b16 a[136:137], %[vr6] offset:0\n\t"
    "v_mfma_f32_16x16x32_bf16 v[224:227], a[156:159], a[112:115], v[224:227]\n\t" "ds_read_b64_tr_b16 a[138:139], %[vr6] offset:3584\n\t"
    "v_mfma_f32_16x16x32_bf16 v[228:231], a[152:155], a[100:103], v[228:231]\n\t" "global_load_dword a231, %[va], %[cd]\n\t" "v_mfma_f32_16x16x32_bf16 v[228:231], a[156:159], a[116:119], v[228:231]\n\t"
    "v_mfma_f32_16x16x32_bf16 v[232:235], a[152:155], a[104:107], v[232:235]\n\t" "ds_read_b64_tr_b16 a[140:141], %[vr6] offset:14336\n\t"
    "v_mfma_f32_16x16x32_bf16 v[232:235], a[156:159], a[120:123], v[232:235]\n\t" "ds_read_b64_tr_b16 a[142:143], %[vr6] offset:17920\n\t"
    "v_mfma_f32_16x16x32_bf16 v[236:239], a[152:155], a[108:111], v[236:239]\n\t" "v_mfma_f32_16x16x32_bf16 v[236:239], a[156:159], a[124:127], v[236:239]\n\t" "s_waitcnt lgkmcnt(4)\n\t"
    "v_mfma_f32_16x16x32_bf16 v[128:131], a[128:131], a[200:203], v[128:131]\n\t" "ds_read_b64_tr_b16 a[144:145], %[vr6] offset:7168\n\t" "s_add_u32 s90, %[shl], %[go2]\n\t"
    "s_addc_u32 s91, %[shh], 0\n\t" "s_add_u32 s92, s90, 229376\n\t" "s_addc_u32 s93, s91, 0\n\t" "v_mul_f32 v112, v112, v16\n\t"
    "v_mfma_f32_16x16x32_bf16 v[132:135], a[128:131], a[204:207], v[132:135]\n\t" "ds_read_b64_tr_b16 a[146:147], %[vr6] offset:10752\n\t" "v_mul_f32 v113, v113, v16\n\t" "v_mul_f32 v114, v114, v16\n\t"
    "v_mfma_f32_16x16x32_bf16 v[136:139], a[128:131], a[208:211], v[136:139]\n\t" "v_mul_f32 v115, v115, v16\n\t" "v_exp_f32 v112, v112\n\t"
    "v_mfma_f32_16x16x32_bf16 v[140:143], a[128:131], a[212:215], v[140:143]\n\t" "v_exp_f32_e64 v113, v113\n\t" "v_mfma_f32_16x16x32_bf16 v[240:243], a[132:135], a[200:203], v[240:243]\n\t"
    "ds_read_b64_tr_b16 a[148:149], %[vr6] offset:21504\n\t" "v_exp_f32 v114, v114\n\t" "v_exp_f32 v115, v115\n\t" "v_mfma_f32_16x16x32_bf16 v[244:247], a[132:135], a[204:207], v[244:247]\n\t"
    "ds_read_b64_tr_b16 a[150:151], %[vr6] offset:25088\n\t" "v_fma_f32 v112, v112, v20, v20\n\t" "v_fma_f32 v113, v113, v20, v20\n\t"
    "v_mfma_f32_16x16x32_bf16 v[248:251], a[132:135], a[208:211], v[248:251]\n\t" "v_fma_f32 v114, v114, v20, v20\n\t" "v_fma_f32 v115, v115, v20, v20\n\t"
    "v_mfma_f32_16x16x32_bf16 v[252:255], a[132:135], a[212:215], v[252:255]\n\t" "v_rcp_f32 v112, v112\n\t" "s_waitcnt lgkmcnt(4)\n\t"
    "v_mfma_f32_16x16x32_bf16 v[128:131], a[136:139], a[32:35], v[128:131]\n\t" "ds_read_b64_tr_b16 a[152:153], %[vr6] offset:28672\n\t" "v_rcp_f32 v113, v113\n\t" "v_rcp_f32 v114, v114\n\t"
    "v_mfma_f32_16x16x32_bf16 v[128:131], a[140:143], a[48:51], v[128:131]\n\t" "ds_read_b64_tr_b16 a[154:155], %[vr6] offset:32256\n\t" "v_rcp_f32_e64 v115, v115\n\t"
    "v_mfma_f32_16x16x32_bf16 v[132:135], a[136:139], a[36:39], v[132:135]\n\t" "v_mul_f32 v224, v224, v112\n\t" "v_mul_f32 v225, v225, v113\n\t"
    "v_mfma_f32_16x16x32_bf16 v[132:135], a[140:143], a[52:55], v[132:135]\n\t" "v_mul_f32 v226, v226, v114\n\t" "v_mul_f32 v227, v227, v115\n\t"
    "v_mfma_f32_16x16x32_bf16 v[136:139], a[136:139], a[40:43], v[136:139]\n\t" "ds_read_b64_tr_b16 a[156:157], %[vr6] offset:43008\n\t" "v_cvt_pk_bf16_f32 v98, v224, v225\n\t"
    "v_cvt_pk_bf16_f32 v99, v226, v227\n\t" "global_store_dwordx4 %[va], v[96:99], s[90:91] offset:0 sc1\n\t" "v_mfma_f32_16x16x32_bf16 v[136:139], a[140:143], a[56:59], v[136:139]\n\t"
    "ds_read_b64_tr_b16 a[158:159], %[vr6] offset:46592\n\t" "v_mul_f32 v116, v116, v17\n\t" "v_mul_f32 v117, v117, v17\n\t" "v_mfma_f32_16x16x32_bf16 v[140:143], a[136:139], a[44:47], v[140:143]\n\t"
    "v_mul_f32 v118, v118, v17\n\t" "v_mul_f32 v119, v119, v17\n\t" "v_mfma_f32_16x16x32_bf16 v[140:143], a[140:143], a[60:63], v[140:143]\n\t" "v_exp_f32 v116, v116\n\t" "s_waitcnt lgkmcnt(4)\n\t"
    "v_mfma_f32_16x16x32_bf16 v[240:243], a[144:147], a[32:35], v[240:243]\n\t" "ds_read_b64_tr_b16 a[128:129], %[vr6] offset:35840\n\t" "v_exp_f32 v117, v117\n\t" "v_exp_f32 v118, v118\n\t"
    "v_mfma_f32_16x16x32_bf16 v[240:243], a[148:151], a[48:51], v[240:243]\n\t" "ds_read_b64_tr_b16 a[130:131], %[vr6] offset:39424\n\t" "v_exp_f32_e64 v119, v119\n\t"
    "v_mfma_f32_16x16x32_bf16 v[244:247], a[144:147], a[36:39], v[244:247]\n\t" "v_fma_f32 v116, v116, v21, v21\n\t" "v_fma_f32 v117, v117, v21, v21\n\t"
    "v_mfma_f32_16x16x32_bf16 v[244:247], a[148:151], a[52:55], v[244:247]\n\t" "v_fma_f32 v118, v118, v21, v21\n\t" "v_fma_f32 v119, v119, v21, v21\n\t"
    "v_mfma_f32_16x16x32_bf16 v[248:251], a[144:147], a[40:43], v[248:251]\n\t" "ds_read_b64_tr_b16 a[132:133], %[vr6] offset:50176\n\t" "v_rcp_f32 v116, v116\n\t" "v_rcp_f32 v117, v117\n\t"
    "v_mfma_f32_16x16x32_bf16 v[248:251], a[148:151], a[56:59], v[248:251]\n\t" "ds_read_b64_tr_b16 a[134:135], %[vr6] offset:53760\n\t" "v_rcp_f32_e64 v118, v118\n\t"
    "v_mfma_f32_16x16x32_bf16 v[252:255], a[144:147], a[44:47], v[252:255]\n\t" "v_rcp_f32_e64 v119, v119\n\t" "v_mfma_f32_16x16x32_bf16 v[252:255], a[148:151], a[60:63], v[252:255]\n\t"
    "v_mul_f32 v228, v228, v116\n\t" "v_mul_f32_e64 v229, v229, v117\n\t" "s_waitcnt lgkmcnt(4)\n\t" "v_mfma_f32_16x16x32_bf16 v[128:131], a[152:155], a[64:67], v[128:131]\n\t"
    "ds_read_b64_tr_b16 a[136:137], %[vr7] offset:0\n\t" "v_mul_f32 v230, v230, v118\n\t" "v_mul_f32 v231, v231, v119\n\t" "v_cvt_pk_bf16_f32 v102, v228, v229\n\t"
    "v_mfma_f32_16x16x32_bf16 v[128:131], a[156:159], a[80:83], v[128:131]\n\t" "ds_read_b64_tr_b16 a[138:139], %[vr7] offset:3584\n\t" "v_cvt_pk_bf16_f32 v103, v230, v231\n\t"
    "global_store_dwordx4 %[va], v[100:103], s[90:91] offset:1024 sc1\n\t" "v_mfma_f32_16x16x32_bf16 v[132:135], a[152:155], a[68:71], v[132:135]\n\t" "v_mul_f32 v120, v120, v18\n\t"
    "v_mul_f32 v121, v121, v18\n\t" "v_mfma_f32_16x16x32_bf16 v[132:135], a[156:159], a[84:87], v[132:135]\n\t" "v_mul_f32 v122, v122, v18\n\t" "v_mul_f32 v123, v123, v18\n\t"
    "v_mfma_f32_16x16x32_bf16 v[136:139], a[152:155], a[72:75], v[136:139]\n\t" "ds_read_b64_tr_b16 a[140:141], %[vr7] offset:14336\n\t" "v_exp_f32 v120, v120\n\t" "v_exp_f32 v121, v121\n\t"
    "v_mfma_f32_16x16x32_bf16 v[136:139], a[156:159], a[88:91], v[136:139]\n\t" "ds_read_b64_tr_b16 a[142:143], %[vr7] offset:17920\n\t" "v_exp_f32_e64 v122, v122\n\t"
    "v_mfma_f32_16x16x32_bf16 v[140:143], a[152:155], a[76:79], v[140:143]\n\t" "v_exp_f32_e64 v123, v123\n\t" "v_mfma_f32_16x16x32_bf16 v[140:143], a[156:159], a[92:95], v[140:143]\n\t"
    "v_fma_f32 v120, v120, v22, v22\n\t" "v_fma_f32 v121, v121, v22, v22\n\t" "s_waitcnt lgkmcnt(4)\n\t" "v_mfma_f32_16x16x32_bf16 v[240:243], a[128:131], a[64:67], v[240:243]\n\t"
    "ds_read_b64_tr_b16 a[144:145], %[vr7] offset:7168\n\t" "v_fma_f32 v122, v122, v22, v22\n\t" "v_fma_f32 v123, v123, v22, v22\n\t" "v_rcp_f32 v120, v120\n\t"
    "v_mfma_f32_16x16x32_bf16 v[240:243], a[132:135], a[80:83], v[240:243]\n\t" "ds_read_b64_tr_b16 a[146:147], %[vr7] offset:10752\n\t" "v_rcp_f32_e64 v121, v121\n\t"
    "v_mfma_f32_16x16x32_bf16 v[244:247], a[128:131], a[68:71], v[244:247]\n\t" "v_rcp_f32_e64 v122, v122\n\t" "v_mfma_f32_16x16x32_bf16 v[244:247], a[132:135], a[84:87], v[244:247]\n\t"
    "v_rcp_f32_e64 v123, v123\n\t" "v_mfma_f32_16x16x32_bf16 v[248:251], a[128:131], a[72:75], v[248:251]\n\t" "ds_read_b64_tr_b16 a[148:149], %[vr7] offset:21504\n\t" "v_mul_f32 v232, v232, v120\n\t"
    "v_mul_f32 v233, v233, v121\n\t" "v_mul_f32_e64 v234, v234, v122\n\t" "v_mfma_f32_16x16x32_bf16 v[248:251], a[132:135], a[88:91], v[248:251]\n\t"
    "ds_read_b64_tr_b16 a[150:151], %[vr7] offset:25088\n\t" "v_mul_f32_e64 v235, v235, v123\n\t" "v_cvt_pk_bf16_f32 v106, v232, v233\n\t"
    "v_mfma_f32_16x16x32_bf16 v[252:255], a[128:131], a[76:79], v[252:255]\n\t" "v_cvt_pk_bf16_f32 v107, v234, v235\n\t" "global_store_dwordx4 %[va], v[104:107], s[92:93] offset:0 sc1\n\t"
    "v_mfma_f32_16x16x32_bf16 v[252:255], a[132:135], a[92:95], v[252:255]\n\t" "v_mul_f32 v124, v124, v19\n\t" "v_mul_f32_e64 v125, v125, v19\n\t" "s_waitcnt lgkmcnt(4)\n\t"
    "v_mfma_f32_16x16x32_bf16 v[128:131], a[136:139], a[96:99], v[128:131]\n\t" "v_mul_f32 v126, v126, v19\n\t" "v_mul_f32 v127, v127, v19\n\t" "v_exp_f32_e64 v124, v124\n\t"
    "v_mfma_f32_16x16x32_bf16 v[128:131], a[140:143], a[112:115], v[128:131]\n\t" "v_exp_f32_e64 v125, v125\n\t" "v_mfma_f32_16x16x32_bf16 v[132:135], a[136:139], a[100:103], v[132:135]\n\t"
    "v_exp_f32_e64 v126, v126\n\t" "v_mfma_f32_16x16x32_bf16 v[132:135], a[140:143], a[116:119], v[132:135]\n\t" "v_exp_f32_e64 v127, v127\n\t"
    "v_mfma_f32_16x16x32_bf16 v[136:139], a[136:139], a[104:107], v[136:139]\n\t" "v_fma_f32 v124, v124, v23, v23\n\t" "v_fma_f32 v125, v125, v23, v23\n\t" "v_fma_f32 v126, v126, v23, v23\n\t"
    "v_mfma_f32_16x16x32_bf16 v[136:139], a[140:143], a[120:123], v[136:139]\n\t" "v_fma_f32 v127, v127, v23, v23\n\t" "v_rcp_f32_e64 v124, v124\n\t"
    "v_mfma_f32_16x16x32_bf16 v[140:143], a[136:139], a[108:111], v[140:143]\n\t" "v_rcp_f32_e64 v125, v125\n\t" "v_mfma_f32_16x16x32_bf16 v[140:143], a[140:143], a[124:127], v[140:143]\n\t"
    "v_rcp_f32 v126, v126\n\t" "s_waitcnt lgkmcnt(0)\n\t" "v_mfma_f32_16x16x32_bf16 v[240:243], a[144:147], a[96:99], v[240:243]\n\t" "v_rcp_f32 v127, v127\n\t" "v_mul_f32 v236, v236, v124\n\t"
    "v_mfma_f32_16x16x32_bf16 v[240:243], a[148:151], a[112:115], v[240:243]\n\t" "v_mul_f32 v237, v237, v125\n\t" "v_mul_f32 v238, v238, v126\n\t"
    "v_mfma_f32_16x16x32_bf16 v[244:247], a[144:147], a[100:103], v[244:247]\n\t" "v_mul_f32_e64 v239, v239, v127\n\t" "v_cvt_pk_bf16_f32 v110, v236, v237\n\t"
    "v_mfma_f32_16x16x32_bf16 v[244:247], a[148:151], a[116:119], v[244:247]\n\t" "v_cvt_pk_bf16_f32 v111, v238, v239\n\t" "global_store_dwordx4 %[va], v[108:111], s[92:93] offset:1024 sc1\n\t"
    "v_mfma_f32_16x16x32_bf16 v[248:251], a[144:147], a[104:107], v[248:251]\n\t" "v_mfma_f32_16x16x32_bf16 v[248:251], a[148:151], a[120:123], v[248:251]\n\t"
    "v_mfma_f32_16x16x32_bf16 v[252:255], a[144:147], a[108:111], v[252:255]\n\t" "v_mfma_f32_16x16x32_bf16 v[252:255], a[148:151], a[124:127], v[252:255]\n\t" "s_nop 7\n\t"
    "s_add_u32 s90, %[shl], %[go3]\n\t" "s_addc_u32 s91, %[shh], 0\n\t" "s_add_u32 s92, s90, 229376\n\t" "s_addc_u32 s93, s91, 0\n\t"
    "v_pk_mul_f32 v[128:129], v[128:129], v[16:17] op_sel:[0,0] op_sel_hi:[1,0]\n\t" "v_pk_mul_f32 v[130:131], v[130:131], v[16:17] op_sel:[0,0] op_sel_hi:[1,0]\n\t" "v_exp_f32 v128, v128\n\t"
    "v_exp_f32 v129, v129\n\t" "v_exp_f32 v130, v130\n\t" "v_exp_f32 v131, v131\n\t" "v_pk_fma_f32 v[128:129], v[128:129], v[20:21], v[20:21] op_sel:[0,0,0] op_sel_hi:[1,0,0]\n\t"
    "v_pk_fma_f32 v[130:131], v[130:131], v[20:21], v[20:21] op_sel:[0,0,0] op_sel_hi:[1,0,0]\n\t" "v_rcp_f32 v128, v128\n\t" "v_rcp_f32 v129, v129\n\t" "v_rcp_f32 v130, v130\n\t"
    "v_rcp_f32 v131, v131\n\t" "v_pk_mul_f32 v[240:241], v[240:241], v[128:129]\n\t" "v_pk_mul_f32 v[242:243], v[242:243], v[130:131]\n\t" "v_cvt_pk_bf16_f32 v128, v240, v241\n\t"
    "v_cvt_pk_bf16_f32 v129, v242, v243\n\t" "v_lshrrev_b32_e64 v31, 1, %[va]\n\t" "global_store_dwordx2 v31, v[128:129], s[90:91] offset:0 sc1\n\t"
    "v_pk_mul_f32 v[132:133], v[132:133], v[16:17] op_sel:[0,1] op_sel_hi:[1,1]\n\t" "v_pk_mul_f32 v[134:135], v[134:135], v[16:17] op_sel:[0,1] op_sel_hi:[1,1]\n\t" "v_exp_f32 v132, v132\n\t"
    "v_exp_f32 v133, v133\n\t" "v_exp_f32 v134, v134\n\t" "v_exp_f32 v135, v135\n\t" "v_pk_fma_f32 v[132:133], v[132:133], v[20:21], v[20:21] op_sel:[0,1,1] op_sel_hi:[1,1,1]\n\t"
    "v_pk_fma_f32 v[134:135], v[134:135], v[20:21], v[20:21] op_sel:[0,1,1] op_sel_hi:[1,1,1]\n\t" "v_rcp_f32 v132, v132\n\t" "v_rcp_f32 v133, v133\n\t" "v_rcp_f32 v134, v134\n\t"
    "v_rcp_f32 v135, v135\n\t" "v_pk_mul_f32 v[244:245], v[244:245], v[132:133]\n\t" "v_pk_mul_f32 v[246:247], v[246:247], v[134:135]\n\t" "v_cvt_pk_bf16_f32 v132, v244, v245\n\t"
    "v_cvt_pk_bf16_f32 v133, v246, v247\n\t" "global_store_dwordx2 v31, v[132:133], s[90:91] offset:512 sc1\n\t" "v_pk_mul_f32 v[136:137], v[136:137], v[18:19] op_sel:[0,0] op_sel_hi:[1,0]\n\t"
    "v_pk_mul_f32 v[138:139], v[138:139], v[18:19] op_sel:[0,0] op_sel_hi:[1,0]\n\t" "v_exp_f32 v136, v136\n\t" "v_exp_f32 v137, v137\n\t" "v_exp_f32 v138, v138\n\t" "v_exp_f32 v139, v139\n\t"
    "v_pk_fma_f32 v[136:137], v[136:137], v[22:23], v[22:23] op_sel:[0,0,0] op_sel_hi:[1,0,0]\n\t" "v_pk_fma_f32 v[138:139], v[138:139], v[22:23], v[22:23] op_sel:[0,0,0] op_sel_hi:[1,0,0]\n\t"
    "v_rcp_f32 v136, v136\n\t" "v_rcp_f32 v137, v137\n\t" "v_rcp_f32 v138, v138\n\t" "v_rcp_f32 v139, v139\n\t" "v_pk_mul_f32 v[248:249], v[248:249], v[136:137]\n\t"
    "v_pk_mul_f32 v[250:251], v[250:251], v[138:139]\n\t" "v_cvt_pk_bf16_f32 v136, v248, v249\n\t" "v_cvt_pk_bf16_f32 v137, v250, v251\n\t"
    "global_store_dwordx2 v31, v[136:137], s[92:93] offset:0 sc1\n\t" "v_pk_mul_f32 v[140:141], v[140:141], v[18:19] op_sel:[0,1] op_sel_hi:[1,1]\n\t"
    "v_pk_mul_f32 v[142:143], v[142:143], v[18:19] op_sel:[0,1] op_sel_hi:[1,1]\n\t" "v_exp_f32 v140, v140\n\t" "v_exp_f32 v141, v141\n\t" "v_exp_f32 v142, v142\n\t" "v_exp_f32 v143, v143\n\t"
    "v_pk_fma_f32 v[140:141], v[140:141], v[22:23], v[22:23] op_sel:[0,1,1] op_sel_hi:[1,1,1]\n\t" "v_pk_fma_f32 v[142:143], v[142:143], v[22:23], v[22:23] op_sel:[0,1,1] op_sel_hi:[1,1,1]\n\t"
    "v_rcp_f32 v140, v140\n\t" "v_rcp_f32 v141, v141\n\t" "v_rcp_f32 v142, v142\n\t" "v_rcp_f32 v143, v143\n\t" "v_pk_mul_f32 v[252:253], v[252:253], v[140:141]\n\t"
    "v_pk_mul_f32 v[254:255], v[254:255], v[142:143]\n\t" "v_cvt_pk_bf16_f32 v140, v252, v253\n\t" "v_cvt_pk_bf16_f32 v141, v254, v255\n\t"
    "global_store_dwordx2 v31, v[140:141], s[92:93] offset:512 sc1\n\t" "v_mov_b32 v28, 0\n\t" "v_mov_b64 v[30:31], %[tag]\n\t" "s_waitcnt vmcnt(0)\n\t" "s_barrier\n\t"
    "s_cmp_eq_u32 %[sld], %[sl0]\n\t" "s_cselect_b64 exec, 1, 0\n\t" "global_store_dwordx2 v28, v[30:31], %[sc] sc1\n\t" "s_mov_b64 exec, -1\n\t" "s_add_u32 m0, %[sld], 49152\n\t" "s_nop 0\n\t"
    "global_load_lds_dwordx4 v29, %[sbw] offset:0\n\t" "s_add_u32 m0, %[sld], 53120\n\t" "s_nop 0\n\t" "global_load_lds_dwordx4 v29, %[sbw] offset:128\n\t" "v_add_u32 v29, 0x10000, v29\n\t"
    "s_add_u32 m0, %[sld], 57344\n\t" "s_nop 0\n\t" "global_load_lds_dwordx4 v29, %[sbw] offset:0\n\t" "s_add_u32 m0, %[sld], 61312\n\t" "s_nop 0\n\t" "global_load_lds_dwordx4 v29, %[sbw] offset:128\n\t"
    "v_add_u32 v29, 0x10000, v29\n\t" "s_add_u32 m0, %[sld], 65536\n\t" "s_nop 0\n\t" "global_load_lds_dwordx4 v29, %[sbw] offset:0\n\t" "s_add_u32 m0, %[sld], 69504\n\t" "s_nop 0\n\t"
    "global_load_lds_dwordx4 v29, %[sbw] offset:128\n\t" "v_add_u32 v29, 0x10000, v29\n\t" "s_add_u32 m0, %[sld], 73728\n\t" "s_nop 0\n\t" "global_load_lds_dwordx4 v29, %[sbw] offset:0\n\t"
    "s_add_u32 m0, %[sld], 77696\n\t" "s_nop 0\n\t" "global_load_lds_dwordx4 v29, %[sbw] offset:128\n\t" "v_add_u32 v29, 0x10000, v29\n\t"
    : "+{v[24:25]}"(fl)
    : [va] "{v0}"(va), [ve] "{a248}"(ve), [vb0] "{v1}"(vb[0]), [vb1] "{v2}"(vb[1]), [vb2] "{v3}"(vb[2]), [vb3] "{v4}"(vb[3]), [vr0] "{v5}"(vr[0]), [vr1] "{v6}"(vr[1]), [vr2] "{v7}"(vr[2]), [vr3] "{v8}"(vr[3]), [vr4] "{v9}"(vr[4]), [vr5] "{v10}"(vr[5]), [vr6] "{v11}"(vr[6]), [vr7] "{v12}"(vr[7]), [gt0] "s"(gt[0]), [go0] "s"(go[0]), [gt1] "s"(gt[1]), [go1] "s"(go[1]), [gt2] "s"(gt[2]), [go2] "s"(go[2]), [gt3] "s"(gt[3]), [go3] "s"(go[3]), [ct] "{a249}"(ct), [cr0] "{a250}"(cr0), [cr1] "{a251}"(cr1), [cw] "{a252}"(cw), [cp] "{a253}"(vp), [vs] "{a254}"(vs), [vf] "{v13}"(vf), [sq] "s"(sq), [tag] "s"(tag), [cd] "s"(cd), [sa] "s"(sa), [sb] "s"(sb + 917504), [se] "s"(se), [ss] "s"(ss), [sl] "s"(sl), [su0] "s"(su0), [su6] "s"(su6), [sr] "s"(sr), [su] "s"(su), [srl] "s"(srl), [rb] "s"(ldsb + RAW), [shl] "s"(shl), [shh] "s"(shh), [wa] "s"(wa), [wb] "s"(wb), [e0] "s"(e0), [e1] "s"(e1), [m01] "s"(m01), [m6] "s"(m6), [tp0] "s"(tp0), [vbd] "{a255}"(vbd), [sbw] "s"(sbd), [tw0] "s"(tw[0]), [tw1] "s"(tw[1]), [tw2] "s"(tw[2]), [sld] "s"(sld), [sl0] "s"(ldsb), [sc] "s"(scnt)
    : "v16", "v17", "v18", "v19", "v20", "v21", "v22", "v23", "v26", "v27", "v28", "v29", "v30", "v31", "v32", "v33", "v34", "v35", "v36", "v37", "v38", "v39", "v40", "v41", "v42", "v43", "v44", "v45", "v46", "v47", "v48", "v49", "v50", "v51", "v52", "v53", "v54", "v55", "v56", "v57", "v58", "v59", "v60", "v61", "v62", "v63", "v64", "v65", "v66", "v67", "v68", "v69", "v70", "v71", "v72", "v73", "v74", "v75", "v76", "v77", "v78", "v79", "v80", "v81", "v82", "v83", "v84", "v85", "v86", "v87", "v88", "v89", "v90", "v91", "v92", "v93", "v94", "v95", "v96", "v97", "v98", "v99", "v100", "v101", "v102", "v103", "v104", "v105", "v106", "v107", "v108", "v109", "v110", "v111", "v112", "v113", "v114", "v115", "v116", "v117", "v118", "v119", "v120", "v121", "v122", "v123", "v124", "v125", "v126", "v127", "v128", "v129", "v130", "v131", "v132", "v133", "v134", "v135", "v136", "v137", "v138", "v139", "v140", "v141", "v142", "v143", "v144", "v145", "v146", "v147", "v148", "v149", "v150", "v151", "v152", "v153", "v154", "v155", "v156", "v157", "v158", "v159", "v160", "v161", "v162", "v163", "v164", "v165", "v166", "v167", "v168", "v169", "v170", "v171", "v172", "v173", "v174", "v175", "v176", "v177", "v178", "v179", "v180", "v181", "v182", "v183", "v184", "v185", "v186", "v187", "v188", "v189", "v190", "v191", "v192", "v193", "v194", "v195", "v196", "v197", "v198", "v199", "v200", "v201", "v202", "v203", "v204", "v205", "v206", "v207", "v208", "v209", "v210", "v211", "v212", "v213", "v214", "v215", "v216", "v217", "v218", "v219", "v220", "v221", "v222", "v223", "v224", "v225", "v226", "v227", "v228", "v229", "v230", "v231", "v232", "v233", "v234", "v235", "v236", "v237", "v238", "v239", "v240", "v241", "v242", "v243", "v244", "v245", "v246", "v247", "v248", "v249", "v250", "v251", "v252", "v253", "v254", "v255", "vcc", "a0", "a1", "a2", "a3", "a4", "a5", "a6", "a7", "a8", "a9", "a10", "a11", "a12", "a13", "a14", "a15", "a16", "a17", "a18", "a19", "a20", "a21", "a22", "a23", "a24", "a25", "a26", "a27", "a28", "a29", "a30", "a31", "a32", "a33", "a34", "a35", "a36", "a37", "a38", "a39", "a40", "a41", "a42", "a43", "a44", "a45", "a46", "a47", "a48", "a49", "a50", "a51", "a52", "a53", "a54", "a55", "a56", "a57", "a58", "a59", "a60", "a61", "a62", "a63", "a64", "a65", "a66", "a67", "a68", "a69", "a70", "a71", "a72", "a73", "a74", "a75", "a76", "a77", "a78", "a79", "a80", "a81", "a82", "a83", "a84", "a85", "a86", "a87", "a88", "a89", "a90", "a91", "a92", "a93", "a94", "a95", "a96", "a97", "a98", "a99", "a100", "a101", "a102", "a103", "a104", "a105", "a106", "a107", "a108", "a109", "a110", "a111", "a112", "a113", "a114", "a115", "a116", "a117", "a118", "a119", "a120", "a121", "a122", "a123", "a124", "a125", "a126", "a127", "a128", "a129", "a130", "a131", "a132", "a133", "a134", "a135", "a136", "a137", "a138", "a139", "a140", "a141", "a142", "a143", "a144", "a145", "a146", "a147", "a148", "a149", "a150", "a151", "a152", "a153", "a154", "a155", "a156", "a157", "a158", "a159", "a160", "a161", "a162", "a163", "a164", "a165", "a166", "a167", "a168", "a169", "a170", "a171", "a172", "a173", "a174", "a175", "a176", "a177", "a178", "a179", "a180", "a181", "a182", "a183", "a184", "a185", "a186", "a187", "a188", "a189", "a190", "a191", "a192", "a193", "a194", "a195", "a196", "a197", "a198", "a199", "a200", "a201", "a202", "a203", "a204", "a205", "a206", "a207", "a208", "a209", "a210", "a211", "a212", "a213", "a214", "a215", "a216", "a217", "a218", "a219", "a220", "a221", "a222", "a223", "a224", "a225", "a226", "a227", "a228", "a229", "a230", "a231", "a232", "a233", "a234", "a235", "a236", "a237", "a238", "a239", "a240", "a241", "a242", "a243", "a244", "a245", "a246", "a247", "s76", "s77", "s78", "s79", "s80", "s81", "s82", "s83", "s84", "s85", "s86", "s87", "s88", "s89", "s90", "s91", "s92", "s93", "s94", "s95", "s96", "s97", "s98", "s99", "scc", "memory");
  } } }
  {
  asm volatile("" : "+s"(bx), "+s"(wid), "+s"(ws)); asm volatile("" : "+s"(tag));
  const int xcd = bx & 7, loc = bx >> 3, lane = __builtin_amdgcn_mbcnt_hi(~0u, __builtin_amdgcn_mbcnt_lo(~0u, 0u));
  const int kh = xcd & 1, rb = (xcd >> 1) * 4 + (loc & 3), cb = ROT, fr = lane & 15, fq = lane >> 4;
  unsigned long long fl;
  const unsigned vx = lane * 16, vo = lane * 8, vg = fq * 16, vf = fr * FS, vc = lane * 4 + 4096, sl0 = ldsb + wid * 1024 + (kh ^ 1) * 4096, sl1 = sl0 ^ 4096; const int k0 = KD, wr = 56 - k0;
  unsigned vb0 = f_vb(wid, lane) + (kh ^ 1) * 128 + (k0 + 12) * 65536, vb1 = vb0 ^ 128, vr[24];
#pragma unroll
  for (int j = 0; j < 12; ++j) { vr[j] = ldsb + f_vr(lane, j & 3) + (kh ^ 1) * 4096 + (j >> 2) * 65536; vr[12 + j] = vr[j] ^ 4096; }
  const unsigned one = 0x3f80u << (lane & 1) * 16, tp = ldsb + 114688 + wid * 4096; const u32x2 id = {(lane & 2) ? 0u : one, (lane & 2) ? one : 0u};
  const unsigned tw0 = tp + fr * 128 + (((fq >> 1) ^ (fr & 7)) << 4) + (fq & 1) * 8, tw1 = tw0 ^ 32, tw2 = tw0 ^ 64, tw3 = tw0 ^ 96, tr = tp + (lane >> 3) * 128 + (((lane & 7) ^ (lane >> 3)) << 4), vs = (lane >> 3) * 2048 + (lane & 7) * 16;
  const char* sw = ws + (unsigned)(W_TAGS + HF * 8 + FA((rb >> 1) * 32 + kh * 16));
  const char* sa = ws + (unsigned)(W_HH + ((rb * 4 + wid) * 112 + kh * 56 + k0) * 2048); const char* sb = (const char*)(pfw + (size_t)kh * 1792 * D_ + cb * 128); const char* sg = ws + (unsigned)(W_GB + bx * 256);
  const char* sm = ws + (unsigned)(W_XCH + (bx * 4 + wid) * 4352); const char* st = ws + (unsigned)(W_XCH + ((bx ^ 1) * 4 + wid) * 4352); const u16* so = out + (unsigned)((rb * 128 + wid * 32) * D_ + cb * 128 + kh * 64);
  asm volatile(".p2align 3\n\t" "s_mov_b64 s[40:41], %[sa]\n\t" "s_mov_b32 s45, 0xffc90000\n\t" "s_add_u32 m0, %[sl0], 81920\n\t" "s_nop 0\n\t" "global_load_lds_dwordx4 %[vb0], %[sb]\n\t"
    "v_add_u32 %[vb0], 0x10000, %[vb0]\n\t" "s_add_u32 m0, %[sl1], 81920\n\t" "s_nop 0\n\t" "global_load_lds_dwordx4 %[vb1], %[sb]\n\t" "v_add_u32 %[vb1], 0x10000, %[vb1]\n\t"
    "s_add_u32 m0, %[sl0], 90112\n\t" "s_nop 0\n\t" "global_load_lds_dwordx4 %[vb0], %[sb]\n\t" "v_add_u32 %[vb0], 0x10000, %[vb0]\n\t" "s_add_u32 m0, %[sl1], 90112\n\t" "s_nop 0\n\t"
    "global_load_lds_dwordx4 %[vb1], %[sb]\n\t" "v_add_u32 %[vb1], 0x10000, %[vb1]\n\t" "s_add_u32 m0, %[sl0], 98304\n\t" "s_nop 0\n\t" "global_load_lds_dwordx4 %[vb0], %[sb]\n\t"
    "v_add_u32 %[vb0], 0x10000, %[vb0]\n\t" "s_add_u32 m0, %[sl1], 98304\n\t" "s_nop 0\n\t" "global_load_lds_dwordx4 %[vb1], %[sb]\n\t" "v_add_u32 %[vb1], 0x10000, %[vb1]\n\t"
    "s_add_u32 m0, %[sl0], 106496\n\t" "s_nop 0\n\t" "global_load_lds_dwordx4 %[vb0], %[sb]\n\t" "v_add_u32 %[vb0], 0x10000, %[vb0]\n\t" "s_add_u32 m0, %[sl1], 106496\n\t" "s_nop 0\n\t"
    "global_load_lds_dwordx4 %[vb1], %[sb]\n\t" "v_add_u32 %[vb1], 0x10000, %[vb1]\n\t" "ds_read_b64_tr_b16 a[176:177], %[vr0] offset:24576\n\t" "ds_read_b64_tr_b16 a[178:179], %[vr0] offset:26624\n\t"
    "ds_read_b64_tr_b16 a[180:181], %[vr0] offset:32768\n\t" "ds_read_b64_tr_b16 a[182:183], %[vr0] offset:34816\n\t" "ds_read_b64_tr_b16 a[184:185], %[vr1] offset:24576\n\t"
    "ds_read_b64_tr_b16 a[186:187], %[vr1] offset:26624\n\t" "ds_read_b64_tr_b16 a[188:189], %[vr1] offset:32768\n\t" "ds_read_b64_tr_b16 a[190:191], %[vr1] offset:34816\n\t"
    "ds_read_b64_tr_b16 a[192:193], %[vr2] offset:24576\n\t" "ds_read_b64_tr_b16 a[194:195], %[vr2] offset:26624\n\t" "ds_read_b64_tr_b16 a[196:197], %[vr2] offset:32768\n\t"
    "ds_read_b64_tr_b16 a[198:199], %[vr2] offset:34816\n\t" "s_mov_b32 s44, 0x400000\n\t" "s_cmp_eq_u32 %[wid], 0\n\t" "s_cselect_b64 exec, 0xffff, 0\n\t"
    "global_load_dwordx2 %[fl], %[vf], %[sw] sc1\n\t" "s_waitcnt vmcnt(0)\n\t" ".Lwait_%=:\n\t" "v_cmp_ne_u64 vcc, %[tag], %[fl]\n\t" "s_nop 4\n\t" "s_cbranch_vccz .Lgo_%=\n\t" "s_mov_b64 exec, vcc\n\t"
    "s_sleep 1\n\t" "global_load_dwordx2 %[fl], %[vf], %[sw] sc1\n\t" "s_sub_u32 s44, s44, 1\n\t" "s_waitcnt vmcnt(0)\n\t" "s_cbranch_scc0 .Lwait_%=\n\t" ".Lgo_%=:\n\t" "s_mov_b64 exec, -1\n\t"
    "s_barrier\n\t" "global_load_dwordx4 a[0:3], %[vx], s[40:41] offset:-4096 sc1\n\t" "global_load_dwordx4 a[4:7], %[vx], s[40:41] offset:-3072 sc1\n\t"
    "global_load_dwordx4 a[8:11], %[vx], s[40:41] offset:-2048 sc1\n\t" "global_load_dwordx4 a[12:15], %[vx], s[40:41] offset:-1024 sc1\n\t"
    "global_load_dwordx4 a[16:19], %[vx], s[40:41] offset:0 sc1\n\t" "global_load_dwordx4 a[20:23], %[vx], s[40:41] offset:1024 sc1\n\t" "global_load_dwordx2 a[24:25], %[vo], s[40:41] offset:2048 sc1\n\t"
    "global_load_dwordx2 a[26:27], %[vo], s[40:41] offset:3072 sc1\n\t" "global_load_dwordx2 a[28:29], %[vo], s[40:41] offset:2560 sc1\n\t"
    "global_load_dwordx2 a[30:31], %[vo], s[40:41] offset:3584 sc1\n\t" "s_waitcnt vmcnt(8)\n\t" "s_waitcnt lgkmcnt(4) vmcnt(6)\n\t" "v_mfma_f32_16x16x32_bf16 v[224:227], a[176:179], a[0:3], 0\n\t"
    "ds_read_b64_tr_b16 a[200:201], %[vr3] offset:24576\n\t" "s_add_u32 s40, s40, 8192\n\t" "s_addc_u32 s41, s41, 0\n\t" "v_mfma_f32_16x16x32_bf16 v[224:227], a[180:183], a[8:11], v[224:227]\n\t"
    "ds_read_b64_tr_b16 a[202:203], %[vr3] offset:26624\n\t" "global_load_dwordx4 a[32:35], %[vx], s[40:41] offset:-4096 sc1\n\t" "v_mfma_f32_16x16x32_bf16 v[240:243], a[176:179], a[4:7], 0\n\t"
    "ds_read_b64_tr_b16 a[204:205], %[vr3] offset:32768\n\t" "v_mfma_f32_16x16x32_bf16 v[240:243], a[180:183], a[12:15], v[240:243]\n\t" "ds_read_b64_tr_b16 a[206:207], %[vr3] offset:34816\n\t"
    "v_mfma_f32_16x16x32_bf16 v[228:231], a[184:187], a[0:3], 0\n\t" "ds_read_b64_tr_b16 a[176:177], %[vr12] offset:24576\n\t" "v_mfma_f32_16x16x32_bf16 v[228:231], a[188:191], a[8:11], v[228:231]\n\t"
    "ds_read_b64_tr_b16 a[178:179], %[vr12] offset:26624\n\t" "global_load_dwordx4 a[36:39], %[vx], s[40:41] offset:-3072 sc1\n\t" "v_mfma_f32_16x16x32_bf16 v[244:247], a[184:187], a[4:7], 0\n\t"
    "ds_read_b64_tr_b16 a[180:181], %[vr12] offset:32768\n\t" "v_mfma_f32_16x16x32_bf16 v[244:247], a[188:191], a[12:15], v[244:247]\n\t" "ds_read_b64_tr_b16 a[182:183], %[vr12] offset:34816\n\t"
    "s_waitcnt lgkmcnt(4)\n\t" "v_mfma_f32_16x16x32_bf16 v[232:235], a[192:195], a[0:3], 0\n\t" "ds_read_b64_tr_b16 a[184:185], %[vr13] offset:24576\n\t"
    "v_mfma_f32_16x16x32_bf16 v[232:235], a[196:199], a[8:11], v[232:235]\n\t" "ds_read_b64_tr_b16 a[186:187], %[vr13] offset:26624\n\t"
    "global_load_dwordx4 a[40:43], %[vx], s[40:41] offset:-2048 sc1\n\t" "v_mfma_f32_16x16x32_bf16 v[248:251], a[192:195], a[4:7], 0\n\t" "ds_read_b64_tr_b16 a[188:189], %[vr13] offset:32768\n\t"
    "v_mfma_f32_16x16x32_bf16 v[248:251], a[196:199], a[12:15], v[248:251]\n\t" "ds_read_b64_tr_b16 a[190:191], %[vr13] offset:34816\n\t" "v_mfma_f32_16x16x32_bf16 v[236:239], a[200:203], a[0:3], 0\n\t"
    "ds_read_b64_tr_b16 a[192:193], %[vr14] offset:24576\n\t" "v_mfma_f32_16x16x32_bf16 v[236:239], a[204:207], a[8:11], v[236:239]\n\t" "ds_read_b64_tr_b16 a[194:195], %[vr14] offset:26624\n\t"
    "global_load_dwordx4 a[44:47], %[vx], s[40:41] offset:-1024 sc1\n\t" "v_mfma_f32_16x16x32_bf16 v[252:255], a[200:203], a[4:7], 0\n\t" "ds_read_b64_tr_b16 a[196:197], %[vr14] offset:32768\n\t"
    "v_mfma_f32_16x16x32_bf16 v[252:255], a[204:207], a[12:15], v[252:255]\n\t" "ds_read_b64_tr_b16 a[198:199], %[vr14] offset:34816\n\t" "s_waitcnt lgkmcnt(4)\n\t"
    "v_mfma_f32_16x16x32_bf16 v[160:163], a[176:179], a[0:3], 0\n\t" "ds_read_b64_tr_b16 a[200:201], %[vr15] offset:24576\n\t" "v_mfma_f32_16x16x32_bf16 v[160:163], a[180:183], a[8:11], v[160:163]\n\t"
    "ds_read_b64_tr_b16 a[202:203], %[vr15] offset:26624\n\t" "global_load_dwordx4 a[48:51], %[vx], s[40:41] offset:0 sc1\n\t" "v_mfma_f32_16x16x32_bf16 v[164:167], a[176:179], a[4:7], 0\n\t"
    "ds_read_b64_tr_b16 a[204:205], %[vr15] offset:32768\n\t" "v_mfma_f32_16x16x32_bf16 v[164:167], a[180:183], a[12:15], v[164:167]\n\t" "ds_read_b64_tr_b16 a[206:207], %[vr15] offset:34816\n\t"
    "v_mfma_f32_16x16x32_bf16 v[168:171], a[184:187], a[0:3], 0\n\t" "s_barrier\n\t" "ds_read_b64_tr_b16 a[176:177], %[vr0] offset:40960\n\t"
    "v_mfma_f32_16x16x32_bf16 v[168:171], a[188:191], a[8:11], v[168:171]\n\t" "ds_read_b64_tr_b16 a[178:179], %[vr0] offset:43008\n\t" "global_load_dwordx4 a[52:55], %[vx], s[40:41] offset:1024 sc1\n\t"
    "v_mfma_f32_16x16x32_bf16 v[172:175], a[184:187], a[4:7], 0\n\t" "ds_read_b64_tr_b16 a[180:181], %[vr8] offset:8192\n\t" "v_mfma_f32_16x16x32_bf16 v[172:175], a[188:191], a[12:15], v[172:175]\n\t"
    "ds_read_b64_tr_b16 a[182:183], %[vr8] offset:10240\n\t" "s_waitcnt lgkmcnt(4)\n\t" "v_mfma_f32_16x16x32_bf16 v[176:179], a[192:195], a[0:3], 0\n\t"
    "ds_read_b64_tr_b16 a[184:185], %[vr1] offset:40960\n\t" "v_mfma_f32_16x16x32_bf16 v[176:179], a[196:199], a[8:11], v[176:179]\n\t" "ds_read_b64_tr_b16 a[186:187], %[vr1] offset:43008\n\t"
    "global_load_dwordx4 a[56:59], %[vx], s[40:41] offset:2048 sc1\n\t" "v_mfma_f32_16x16x32_bf16 v[180:183], a[192:195], a[4:7], 0\n\t" "ds_read_b64_tr_b16 a[188:189], %[vr9] offset:8192\n\t"
    "v_mfma_f32_16x16x32_bf16 v[180:183], a[196:199], a[12:15], v[180:183]\n\t" "ds_read_b64_tr_b16 a[190:191], %[vr9] offset:10240\n\t" "v_mfma_f32_16x16x32_bf16 v[184:187], a[200:203], a[0:3], 0\n\t"
    "ds_read_b64_tr_b16 a[192:193], %[vr2] offset:40960\n\t" "v_mfma_f32_16x16x32_bf16 v[184:187], a[204:207], a[8:11], v[184:187]\n\t" "ds_read_b64_tr_b16 a[194:195], %[vr2] offset:43008\n\t"
    "global_load_dwordx4 a[60:63], %[vx], s[40:41] offset:3072 sc1\n\t" "v_mfma_f32_16x16x32_bf16 v[188:191], a[200:203], a[4:7], 0\n\t" "ds_read_b64_tr_b16 a[196:197], %[vr10] offset:8192\n\t"
    "v_mfma_f32_16x16x32_bf16 v[188:191], a[204:207], a[12:15], v[188:191]\n\t" "ds_read_b64_tr_b16 a[198:199], %[vr10] offset:10240\n\t" "s_waitcnt lgkmcnt(4) vmcnt(8)\n\t"
    "v_mfma_f32_16x16x32_bf16 v[224:227], a[176:179], a[16:19], v[224:227]\n\t" "ds_read_b64_tr_b16 a[200:201], %[vr3] offset:40960\n\t" "s_add_u32 s40, s40, 8192\n\t" "s_addc_u32 s41, s41, 0\n\t"
    "v_mfma_f32_16x16x32_bf16 v[224:227], a[180:183], a[24:27], v[224:227]\n\t" "ds_read_b64_tr_b16 a[202:203], %[vr3] offset:43008\n\t"
    "global_load_dwordx4 a[64:67], %[vx], s[40:41] offset:-4096 sc1\n\t" "v_mfma_f32_16x16x32_bf16 v[240:243], a[176:179], a[20:23], v[240:243]\n\t"
    "ds_read_b64_tr_b16 a[204:205], %[vr11] offset:8192\n\t" "v_mfma_f32_16x16x32_bf16 v[240:243], a[180:183], a[28:31], v[240:243]\n\t" "ds_read_b64_tr_b16 a[206:207], %[vr11] offset:10240\n\t"
    "v_mfma_f32_16x16x32_bf16 v[228:231], a[184:187], a[16:19], v[228:231]\n\t" "ds_read_b64_tr_b16 a[176:177], %[vr12] offset:40960\n\t"
    "v_mfma_f32_16x16x32_bf16 v[228:231], a[188:191], a[24:27], v[228:231]\n\t" "ds_read_b64_tr_b16 a[178:179], %[vr12] offset:43008\n\t" "global_load_dword a255, %[vx], %[cd]\n\t"
    "v_mfma_f32_16x16x32_bf16 v[244:247], a[184:187], a[20:23], v[244:247]\n\t" "ds_read_b64_tr_b16 a[180:181], %[vr20] offset:8192\n\t"
    "v_mfma_f32_16x16x32_bf16 v[244:247], a[188:191], a[28:31], v[244:247]\n\t" "ds_read_b64_tr_b16 a[182:183], %[vr20] offset:10240\n\t" "s_waitcnt lgkmcnt(4)\n\t"
    "v_mfma_f32_16x16x32_bf16 v[232:235], a[192:195], a[16:19], v[232:235]\n\t" "ds_read_b64_tr_b16 a[184:185], %[vr13] offset:40960\n\t"
    "global_load_dwordx4 a[68:71], %[vx], s[40:41] offset:-3072 sc1\n\t" "v_mfma_f32_16x16x32_bf16 v[232:235], a[196:199], a[24:27], v[232:235]\n\t"
    "ds_read_b64_tr_b16 a[186:187], %[vr13] offset:43008\n\t" "v_mfma_f32_16x16x32_bf16 v[248:251], a[192:195], a[20:23], v[248:251]\n\t" "ds_read_b64_tr_b16 a[188:189], %[vr21] offset:8192\n\t"
    "v_mfma_f32_16x16x32_bf16 v[248:251], a[196:199], a[28:31], v[248:251]\n\t" "ds_read_b64_tr_b16 a[190:191], %[vr21] offset:10240\n\t"
    "global_load_dwordx4 a[72:75], %[vx], s[40:41] offset:-2048 sc1\n\t" "v_mfma_f32_16x16x32_bf16 v[236:239], a[200:203], a[16:19], v[236:239]\n\t"
    "ds_read_b64_tr_b16 a[192:193], %[vr14] offset:40960\n\t" "v_mfma_f32_16x16x32_bf16 v[236:239], a[204:207], a[24:27], v[236:239]\n\t" "ds_read_b64_tr_b16 a[194:195], %[vr14] offset:43008\n\t"
    "v_mfma_f32_16x16x32_bf16 v[252:255], a[200:203], a[20:23], v[252:255]\n\t" "ds_read_b64_tr_b16 a[196:197], %[vr22] offset:8192\n\t"
    "v_mfma_f32_16x16x32_bf16 v[252:255], a[204:207], a[28:31], v[252:255]\n\t" "ds_read_b64_tr_b16 a[198:199], %[vr22] offset:10240\n\t" "s_waitcnt lgkmcnt(4)\n\t"
    "v_mfma_f32_16x16x32_bf16 v[160:163], a[176:179], a[16:19], v[160:163]\n\t" "ds_read_b64_tr_b16 a[200:201], %[vr15] offset:40960\n\t"
    "global_load_dwordx4 a[76:79], %[vx], s[40:41] offset:-1024 sc1\n\t" "v_mfma_f32_16x16x32_bf16 v[160:163], a[180:183], a[24:27], v[160:163]\n\t"
    "ds_read_b64_tr_b16 a[202:203], %[vr15] offset:43008\n\t" "v_mfma_f32_16x16x32_bf16 v[164:167], a[176:179], a[20:23], v[164:167]\n\t" "ds_read_b64_tr_b16 a[204:205], %[vr23] offset:8192\n\t"
    "v_mfma_f32_16x16x32_bf16 v[164:167], a[180:183], a[28:31], v[164:167]\n\t" "ds_read_b64_tr_b16 a[206:207], %[vr23] offset:10240\n\t" "global_load_dwordx2 a[0:1], %[vo], s[40:41] offset:0 sc1\n\t"
    "v_mfma_f32_16x16x32_bf16 v[168:171], a[184:187], a[16:19], v[168:171]\n\t" "s_barrier\n\t" "ds_read_b64_tr_b16 a[176:177], %[vr8] offset:16384\n\t"
    "v_mfma_f32_16x16x32_bf16 v[168:171], a[188:191], a[24:27], v[168:171]\n\t" "ds_read_b64_tr_b16 a[178:179], %[vr8] offset:18432\n\t" "global_load_dwordx2 a[2:3], %[vo], s[40:41] offset:1024 sc1\n\t"
    "v_mfma_f32_16x16x32_bf16 v[172:175], a[184:187], a[20:23], v[172:175]\n\t" "ds_read_b64_tr_b16 a[180:181], %[vr8] offset:24576\n\t"
    "v_mfma_f32_16x16x32_bf16 v[172:175], a[188:191], a[28:31], v[172:175]\n\t" "ds_read_b64_tr_b16 a[182:183], %[vr8] offset:26624\n\t" "s_waitcnt lgkmcnt(4)\n\t"
    "v_mfma_f32_16x16x32_bf16 v[176:179], a[192:195], a[16:19], v[176:179]\n\t" "ds_read_b64_tr_b16 a[184:185], %[vr9] offset:16384\n\t" "global_load_dwordx2 a[4:5], %[vo], s[40:41] offset:512 sc1\n\t"
    "v_mfma_f32_16x16x32_bf16 v[176:179], a[196:199], a[24:27], v[176:179]\n\t" "ds_read_b64_tr_b16 a[186:187], %[vr9] offset:18432\n\t" "global_load_dwordx2 a[6:7], %[vo], s[40:41] offset:1536 sc1\n\t"
    "v_mfma_f32_16x16x32_bf16 v[180:183], a[192:195], a[20:23], v[180:183]\n\t" "ds_read_b64_tr_b16 a[188:189], %[vr9] offset:24576\n\t"
    "v_mfma_f32_16x16x32_bf16 v[180:183], a[196:199], a[28:31], v[180:183]\n\t" "ds_read_b64_tr_b16 a[190:191], %[vr9] offset:26624\n\t" "global_load_dwordx4 a[8:11], %[vx], s[40:41] offset:2048 sc1\n\t"
    "v_mfma_f32_16x16x32_bf16 v[184:187], a[200:203], a[16:19], v[184:187]\n\t" "ds_read_b64_tr_b16 a[192:193], %[vr10] offset:16384\n\t"
    "v_mfma_f32_16x16x32_bf16 v[184:187], a[204:207], a[24:27], v[184:187]\n\t" "ds_read_b64_tr_b16 a[194:195], %[vr10] offset:18432\n\t"
    "v_mfma_f32_16x16x32_bf16 v[188:191], a[200:203], a[20:23], v[188:191]\n\t" "ds_read_b64_tr_b16 a[196:197], %[vr10] offset:24576\n\t"
    "v_mfma_f32_16x16x32_bf16 v[188:191], a[204:207], a[28:31], v[188:191]\n\t" "ds_read_b64_tr_b16 a[198:199], %[vr10] offset:26624\n\t" "s_waitcnt lgkmcnt(4) vmcnt(14)\n\t"
    "v_mfma_f32_16x16x32_bf16 v[224:227], a[176:179], a[32:35], v[224:227]\n\t" "ds_read_b64_tr_b16 a[200:201], %[vr11] offset:16384\n\t"
    "global_load_dwordx4 a[12:15], %[vx], s[40:41] offset:3072 sc1\n\t" "v_mfma_f32_16x16x32_bf16 v[224:227], a[180:183], a[40:43], v[224:227]\n\t"
    "ds_read_b64_tr_b16 a[202:203], %[vr11] offset:18432\n\t" "s_add_u32 s40, s40, 8192\n\t" "s_addc_u32 s41, s41, 0\n\t" "v_mfma_f32_16x16x32_bf16 v[240:243], a[176:179], a[36:39], v[240:243]\n\t"
    "ds_read_b64_tr_b16 a[204:205], %[vr11] offset:24576\n\t" "v_mfma_f32_16x16x32_bf16 v[240:243], a[180:183], a[44:47], v[240:243]\n\t" "ds_read_b64_tr_b16 a[206:207], %[vr11] offset:26624\n\t"
    "global_load_dwordx4 a[16:19], %[vx], s[40:41] offset:-4096 sc1\n\t" "v_mfma_f32_16x16x32_bf16 v[228:231], a[184:187], a[32:35], v[228:231]\n\t"
    "ds_read_b64_tr_b16 a[176:177], %[vr20] offset:16384\n\t" "v_mfma_f32_16x16x32_bf16 v[228:231], a[188:191], a[40:43], v[228:231]\n\t" "ds_read_b64_tr_b16 a[178:179], %[vr20] offset:18432\n\t"
    "v_mfma_f32_16x16x32_bf16 v[244:247], a[184:187], a[36:39], v[244:247]\n\t" "ds_read_b64_tr_b16 a[180:181], %[vr20] offset:24576\n\t"
    "v_mfma_f32_16x16x32_bf16 v[244:247], a[188:191], a[44:47], v[244:247]\n\t" "ds_read_b64_tr_b16 a[182:183], %[vr20] offset:26624\n\t" "s_waitcnt lgkmcnt(4)\n\t"
    "v_mfma_f32_16x16x32_bf16 v[232:235], a[192:195], a[32:35], v[232:235]\n\t" "ds_read_b64_tr_b16 a[184:185], %[vr21] offset:16384\n\t"
    "global_load_dwordx4 a[20:23], %[vx], s[40:41] offset:-3072 sc1\n\t" "v_mfma_f32_16x16x32_bf16 v[232:235], a[196:199], a[40:43], v[232:235]\n\t"
    "ds_read_b64_tr_b16 a[186:187], %[vr21] offset:18432\n\t" "v_mfma_f32_16x16x32_bf16 v[248:251], a[192:195], a[36:39], v[248:251]\n\t" "ds_read_b64_tr_b16 a[188:189], %[vr21] offset:24576\n\t"
    "v_mfma_f32_16x16x32_bf16 v[248:251], a[196:199], a[44:47], v[248:251]\n\t" "ds_read_b64_tr_b16 a[190:191], %[vr21] offset:26624\n\t"
    "global_load_dwordx4 a[24:27], %[vx], s[40:41] offset:-2048 sc1\n\t" "v_mfma_f32_16x16x32_bf16 v[236:239], a[200:203], a[32:35], v[236:239]\n\t"
    "ds_read_b64_tr_b16 a[192:193], %[vr22] offset:16384\n\t" "v_mfma_f32_16x16x32_bf16 v[236:239], a[204:207], a[40:43], v[236:239]\n\t" "ds_read_b64_tr_b16 a[194:195], %[vr22] offset:18432\n\t"
    "v_mfma_f32_16x16x32_bf16 v[252:255], a[200:203], a[36:39], v[252:255]\n\t" "ds_read_b64_tr_b16 a[196:197], %[vr22] offset:24576\n\t"
    "v_mfma_f32_16x16x32_bf16 v[252:255], a[204:207], a[44:47], v[252:255]\n\t" "ds_read_b64_tr_b16 a[198:199], %[vr22] offset:26624\n\t" "s_waitcnt lgkmcnt(4)\n\t"
    "v_mfma_f32_16x16x32_bf16 v[160:163], a[176:179], a[32:35], v[160:163]\n\t" "ds_read_b64_tr_b16 a[200:201], %[vr23] offset:16384\n\t"
    "global_load_dwordx4 a[28:31], %[vx], s[40:41] offset:-1024 sc1\n\t" "v_mfma_f32_16x16x32_bf16 v[160:163], a[180:183], a[40:43], v[160:163]\n\t"
    "ds_read_b64_tr_b16 a[202:203], %[vr23] offset:18432\n\t" "s_add_u32 m0, %[sl0], 114688\n\t" "v_mfma_f32_16x16x32_bf16 v[164:167], a[176:179], a[36:39], v[164:167]\n\t"
    "ds_read_b64_tr_b16 a[204:205], %[vr23] offset:24576\n\t" "v_mfma_f32_16x16x32_bf16 v[164:167], a[180:183], a[44:47], v[164:167]\n\t" "ds_read_b64_tr_b16 a[206:207], %[vr23] offset:26624\n\t"
    "global_load_lds_dwordx4 %[vb0], %[sb]\n\t" "v_mfma_f32_16x16x32_bf16 v[168:171], a[184:187], a[32:35], v[168:171]\n\t" "s_barrier\n\t" "ds_read_b64_tr_b16 a[176:177], %[vr0] offset:0\n\t"
    "v_mfma_f32_16x16x32_bf16 v[168:171], a[188:191], a[40:43], v[168:171]\n\t" "ds_read_b64_tr_b16 a[178:179], %[vr0] offset:2048\n\t" "v_add_u32 %[vb0], 0x10000, %[vb0]\n\t"
    "v_mfma_f32_16x16x32_bf16 v[172:175], a[184:187], a[36:39], v[172:175]\n\t" "ds_read_b64_tr_b16 a[180:181], %[vr0] offset:8192\n\t" "s_add_u32 m0, %[sl1], 114688\n\t"
    "v_mfma_f32_16x16x32_bf16 v[172:175], a[188:191], a[44:47], v[172:175]\n\t" "ds_read_b64_tr_b16 a[182:183], %[vr0] offset:10240\n\t" "s_waitcnt lgkmcnt(4)\n\t"
    "v_mfma_f32_16x16x32_bf16 v[176:179], a[192:195], a[32:35], v[176:179]\n\t" "ds_read_b64_tr_b16 a[184:185], %[vr1] offset:0\n\t" "global_load_lds_dwordx4 %[vb1], %[sb]\n\t"
    "v_mfma_f32_16x16x32_bf16 v[176:179], a[196:199], a[40:43], v[176:179]\n\t" "ds_read_b64_tr_b16 a[186:187], %[vr1] offset:2048\n\t" "v_add_u32 %[vb1], 0x10000, %[vb1]\n\t"
    "v_mfma_f32_16x16x32_bf16 v[180:183], a[192:195], a[36:39], v[180:183]\n\t" "ds_read_b64_tr_b16 a[188:189], %[vr1] offset:8192\n\t" "s_add_u32 m0, %[sl0], 122880\n\t"
    "v_mfma_f32_16x16x32_bf16 v[180:183], a[196:199], a[44:47], v[180:183]\n\t" "ds_read_b64_tr_b16 a[190:191], %[vr1] offset:10240\n\t" "global_load_lds_dwordx4 %[vb0], %[sb]\n\t"
    "v_mfma_f32_16x16x32_bf16 v[184:187], a[200:203], a[32:35], v[184:187]\n\t" "ds_read_b64_tr_b16 a[192:193], %[vr2] offset:0\n\t" "v_add_u32 %[vb0], 0x10000, %[vb0]\n\t"
    "v_mfma_f32_16x16x32_bf16 v[184:187], a[204:207], a[40:43], v[184:187]\n\t" "ds_read_b64_tr_b16 a[194:195], %[vr2] offset:2048\n\t" "s_add_u32 m0, %[sl1], 122880\n\t"
    "v_mfma_f32_16x16x32_bf16 v[188:191], a[200:203], a[36:39], v[188:191]\n\t" "ds_read_b64_tr_b16 a[196:197], %[vr2] offset:8192\n\t"
    "v_mfma_f32_16x16x32_bf16 v[188:191], a[204:207], a[44:47], v[188:191]\n\t" "ds_read_b64_tr_b16 a[198:199], %[vr2] offset:10240\n\t" "s_waitcnt lgkmcnt(4) vmcnt(18)\n\t"
    "v_mfma_f32_16x16x32_bf16 v[224:227], a[176:179], a[48:51], v[224:227]\n\t" "ds_read_b64_tr_b16 a[200:201], %[vr3] offset:0\n\t" "global_load_lds_dwordx4 %[vb1], %[sb]\n\t"
    "v_mfma_f32_16x16x32_bf16 v[224:227], a[180:183], a[56:59], v[224:227]\n\t" "ds_read_b64_tr_b16 a[202:203], %[vr3] offset:2048\n\t" "v_add_u32 %[vb1], 0x10000, %[vb1]\n\t"
    "v_mfma_f32_4x4x4_16b_bf16 v[128:131], %[id], a[232:233], 0\n\t" "v_mfma_f32_4x4x4_16b_bf16 v[136:139], %[id], a[234:235], 0\n\t" "v_mfma_f32_4x4x4_16b_bf16 v[144:147], %[id], a[236:237], 0\n\t"
    "v_mfma_f32_4x4x4_16b_bf16 v[152:155], %[id], a[238:239], 0\n\t" "v_mfma_f32_4x4x4_16b_bf16 v[132:135], %[id], a[240:241], 0\n\t" "v_mfma_f32_4x4x4_16b_bf16 v[140:143], %[id], a[242:243], 0\n\t"
    "v_mfma_f32_4x4x4_16b_bf16 v[148:151], %[id], a[244:245], 0\n\t" "v_mfma_f32_4x4x4_16b_bf16 v[156:159], %[id], a[246:247], 0\n\t"
    "v_mfma_f32_16x16x32_bf16 v[240:243], a[176:179], a[52:55], v[240:243]\n\t" "ds_read_b64_tr_b16 a[204:205], %[vr3] offset:8192\n\t"
    "v_mfma_f32_16x16x32_bf16 v[240:243], a[180:183], a[60:63], v[240:243]\n\t" "ds_read_b64_tr_b16 a[206:207], %[vr3] offset:10240\n\t" "global_load_dwordx4 a[32:35], %[vx], s[40:41] offset:0 sc1\n\t"
    "v_mfma_f32_16x16x32_bf16 v[228:231], a[184:187], a[48:51], v[228:231]\n\t" "ds_read_b64_tr_b16 a[176:177], %[vr12] offset:0\n\t"
    "v_mfma_f32_16x16x32_bf16 v[228:231], a[188:191], a[56:59], v[228:231]\n\t" "ds_read_b64_tr_b16 a[178:179], %[vr12] offset:2048\n\t"
    "v_mfma_f32_16x16x32_bf16 v[244:247], a[184:187], a[52:55], v[244:247]\n\t" "ds_read_b64_tr_b16 a[180:181], %[vr12] offset:8192\n\t"
    "v_mfma_f32_16x16x32_bf16 v[244:247], a[188:191], a[60:63], v[244:247]\n\t" "ds_read_b64_tr_b16 a[182:183], %[vr12] offset:10240\n\t" "s_waitcnt lgkmcnt(4)\n\t"
    "v_mfma_f32_16x16x32_bf16 v[232:235], a[192:195], a[48:51], v[232:235]\n\t" "ds_read_b64_tr_b16 a[184:185], %[vr13] offset:0\n\t" "global_load_dwordx4 a[36:39], %[vx], s[40:41] offset:1024 sc1\n\t"
    "v_mfma_f32_16x16x32_bf16 v[232:235], a[196:199], a[56:59], v[232:235]\n\t" "ds_read_b64_tr_b16 a[186:187], %[vr13] offset:2048\n\t"
    "v_mfma_f32_16x16x32_bf16 v[248:251], a[192:195], a[52:55], v[248:251]\n\t" "ds_read_b64_tr_b16 a[188:189], %[vr13] offset:8192\n\t"
    "v_mfma_f32_16x16x32_bf16 v[248:251], a[196:199], a[60:63], v[248:251]\n\t" "ds_read_b64_tr_b16 a[190:191], %[vr13] offset:10240\n\t"
    "global_load_dwordx4 a[40:43], %[vx], s[40:41] offset:2048 sc1\n\t" "v_mfma_f32_16x16x32_bf16 v[236:239], a[200:203], a[48:51], v[236:239]\n\t" "ds_read_b64_tr_b16 a[192:193], %[vr14] offset:0\n\t"
    "v_mfma_f32_16x16x32_bf16 v[236:239], a[204:207], a[56:59], v[236:239]\n\t" "ds_read_b64_tr_b16 a[194:195], %[vr14] offset:2048\n\t"
    "v_mfma_f32_16x16x32_bf16 v[252:255], a[200:203], a[52:55], v[252:255]\n\t" "ds_read_b64_tr_b16 a[196:197], %[vr14] offset:8192\n\t"
    "v_mfma_f32_16x16x32_bf16 v[252:255], a[204:207], a[60:63], v[252:255]\n\t" "ds_read_b64_tr_b16 a[198:199], %[vr14] offset:10240\n\t" "s_waitcnt lgkmcnt(4)\n\t"
    "v_mfma_f32_16x16x32_bf16 v[160:163], a[176:179], a[48:51], v[160:163]\n\t" "ds_read_b64_tr_b16 a[200:201], %[vr15] offset:0\n\t" "global_load_dwordx4 a[44:47], %[vx], s[40:41] offset:3072 sc1\n\t"
    "v_mfma_f32_16x16x32_bf16 v[160:163], a[180:183], a[56:59], v[160:163]\n\t" "ds_read_b64_tr_b16 a[202:203], %[vr15] offset:2048\n\t" "s_add_u32 m0, %[sl0], 131072\n\t"
    "v_mfma_f32_16x16x32_bf16 v[164:167], a[176:179], a[52:55], v[164:167]\n\t" "ds_read_b64_tr_b16 a[204:205], %[vr15] offset:8192\n\t"
    "v_mfma_f32_16x16x32_bf16 v[164:167], a[180:183], a[60:63], v[164:167]\n\t" "ds_read_b64_tr_b16 a[206:207], %[vr15] offset:10240\n\t" "global_load_lds_dwordx4 %[vb0], %[sb]\n\t"
    "v_mfma_f32_16x16x32_bf16 v[168:171], a[184:187], a[48:51], v[168:171]\n\t" "s_barrier\n\t" "ds_read_b64_tr_b16 a[176:177], %[vr0] offset:49152\n\t"
    "v_mfma_f32_16x16x32_bf16 v[168:171], a[188:191], a[56:59], v[168:171]\n\t" "ds_read_b64_tr_b16 a[178:179], %[vr0] offset:51200\n\t" "v_add_u32 %[vb0], 0x10000, %[vb0]\n\t"
    "v_mfma_f32_16x16x32_bf16 v[172:175], a[184:187], a[52:55], v[172:175]\n\t" "ds_read_b64_tr_b16 a[180:181], %[vr0] offset:57344\n\t" "s_add_u32 m0, %[sl1], 131072\n\t"
    "v_mfma_f32_16x16x32_bf16 v[172:175], a[188:191], a[60:63], v[172:175]\n\t" "ds_read_b64_tr_b16 a[182:183], %[vr0] offset:59392\n\t" "s_waitcnt lgkmcnt(4)\n\t"
    "v_mfma_f32_16x16x32_bf16 v[176:179], a[192:195], a[48:51], v[176:179]\n\t" "ds_read_b64_tr_b16 a[184:185], %[vr1] offset:49152\n\t" "global_load_lds_dwordx4 %[vb1], %[sb]\n\t"
    "v_mfma_f32_16x16x32_bf16 v[176:179], a[196:199], a[56:59], v[176:179]\n\t" "ds_read_b64_tr_b16 a[186:187], %[vr1] offset:51200\n\t" "v_add_u32 %[vb1], 0x10000, %[vb1]\n\t"
    "v_mfma_f32_16x16x32_bf16 v[180:183], a[192:195], a[52:55], v[180:183]\n\t" "ds_read_b64_tr_b16 a[188:189], %[vr1] offset:57344\n\t" "s_add_u32 m0, %[sl0], 16384\n\t"
    "v_mfma_f32_16x16x32_bf16 v[180:183], a[196:199], a[60:63], v[180:183]\n\t" "ds_read_b64_tr_b16 a[190:191], %[vr1] offset:59392\n\t" "global_load_lds_dwordx4 %[vb0], %[sb]\n\t"
    "v_mfma_f32_16x16x32_bf16 v[184:187], a[200:203], a[48:51], v[184:187]\n\t" "ds_read_b64_tr_b16 a[192:193], %[vr2] offset:49152\n\t" "v_add_u32 %[vb0], 0x10000, %[vb0]\n\t"
    "v_mfma_f32_16x16x32_bf16 v[184:187], a[204:207], a[56:59], v[184:187]\n\t" "ds_read_b64_tr_b16 a[194:195], %[vr2] offset:51200\n\t" "s_add_u32 m0, %[sl1], 16384\n\t"
    "v_mfma_f32_16x16x32_bf16 v[188:191], a[200:203], a[52:55], v[188:191]\n\t" "ds_read_b64_tr_b16 a[196:197], %[vr2] offset:57344\n\t"
    "v_mfma_f32_16x16x32_bf16 v[188:191], a[204:207], a[60:63], v[188:191]\n\t" "ds_read_b64_tr_b16 a[198:199], %[vr2] offset:59392\n\t" "s_waitcnt lgkmcnt(4) vmcnt(21)\n\t"
    "v_mfma_f32_16x16x32_bf16 v[224:227], a[176:179], a[64:67], v[224:227]\n\t" "ds_read_b64_tr_b16 a[200:201], %[vr3] offset:49152\n\t" "global_load_lds_dwordx4 %[vb1], %[sb]\n\t"
    "v_mfma_f32_16x16x32_bf16 v[224:227], a[180:183], a[72:75], v[224:227]\n\t" "ds_read_b64_tr_b16 a[202:203], %[vr3] offset:51200\n\t" "v_add_u32 %[vb1], 0x10000, %[vb1]\n\t"
    "v_mfma_f32_16x16x32_bf16 v[240:243], a[176:179], a[68:71], v[240:243]\n\t" "ds_read_b64_tr_b16 a[204:205], %[vr3] offset:57344\n\t" "s_add_u32 s40, s40, 8192\n\t" "s_addc_u32 s41, s41, 0\n\t"
    "v_mfma_f32_16x16x32_bf16 v[240:243], a[180:183], a[76:79], v[240:243]\n\t" "ds_read_b64_tr_b16 a[206:207], %[vr3] offset:59392\n\t"
    "global_load_dwordx4 a[48:51], %[vx], s[40:41] offset:-4096 sc1\n\t" "v_mfma_f32_16x16x32_bf16 v[228:231], a[184:187], a[64:67], v[228:231]\n\t"
    "ds_read_b64_tr_b16 a[176:177], %[vr12] offset:49152\n\t" "v_mfma_f32_16x16x32_bf16 v[228:231], a[188:191], a[72:75], v[228:231]\n\t" "ds_read_b64_tr_b16 a[178:179], %[vr12] offset:51200\n\t"
    "v_mfma_f32_16x16x32_bf16 v[244:247], a[184:187], a[68:71], v[244:247]\n\t" "ds_read_b64_tr_b16 a[180:181], %[vr12] offset:57344\n\t"
    "v_mfma_f32_16x16x32_bf16 v[244:247], a[188:191], a[76:79], v[244:247]\n\t" "ds_read_b64_tr_b16 a[182:183], %[vr12] offset:59392\n\t" "s_waitcnt lgkmcnt(4)\n\t"
    "v_mfma_f32_16x16x32_bf16 v[232:235], a[192:195], a[64:67], v[232:235]\n\t" "ds_read_b64_tr_b16 a[184:185], %[vr13] offset:49152\n\t"
    "global_load_dwordx4 a[52:55], %[vx], s[40:41] offset:-3072 sc1\n\t" "v_mfma_f32_16x16x32_bf16 v[232:235], a[196:199], a[72:75], v[232:235]\n\t"
    "ds_read_b64_tr_b16 a[186:187], %[vr13] offset:51200\n\t" "v_mfma_f32_16x16x32_bf16 v[248:251], a[192:195], a[68:71], v[248:251]\n\t" "ds_read_b64_tr_b16 a[188:189], %[vr13] offset:57344\n\t"
    "v_mfma_f32_16x16x32_bf16 v[248:251], a[196:199], a[76:79], v[248:251]\n\t" "ds_read_b64_tr_b16 a[190:191], %[vr13] offset:59392\n\t"
    "global_load_dwordx2 a[56:57], %[vo], s[40:41] offset:-2048 sc1\n\t" "v_mfma_f32_16x16x32_bf16 v[236:239], a[200:203], a[64:67], v[236:239]\n\t"
    "ds_read_b64_tr_b16 a[192:193], %[vr14] offset:49152\n\t" "v_mfma_f32_16x16x32_bf16 v[236:239], a[204:207], a[72:75], v[236:239]\n\t" "ds_read_b64_tr_b16 a[194:195], %[vr14] offset:51200\n\t"
    "global_load_dwordx2 a[58:59], %[vo], s[40:41] offset:-1024 sc1\n\t" "v_mfma_f32_16x16x32_bf16 v[252:255], a[200:203], a[68:71], v[252:255]\n\t"
    "ds_read_b64_tr_b16 a[196:197], %[vr14] offset:57344\n\t" "v_mfma_f32_16x16x32_bf16 v[252:255], a[204:207], a[76:79], v[252:255]\n\t" "ds_read_b64_tr_b16 a[198:199], %[vr14] offset:59392\n\t"
    "s_waitcnt lgkmcnt(4)\n\t" "v_mfma_f32_16x16x32_bf16 v[160:163], a[176:179], a[64:67], v[160:163]\n\t" "ds_read_b64_tr_b16 a[200:201], %[vr15] offset:49152\n\t"
    "global_load_dwordx2 a[60:61], %[vo], s[40:41] offset:-1536 sc1\n\t" "v_mfma_f32_16x16x32_bf16 v[160:163], a[180:183], a[72:75], v[160:163]\n\t"
    "ds_read_b64_tr_b16 a[202:203], %[vr15] offset:51200\n\t" "global_load_dwordx2 a[62:63], %[vo], s[40:41] offset:-512 sc1\n\t"
    "v_mfma_f32_16x16x32_bf16 v[164:167], a[176:179], a[68:71], v[164:167]\n\t" "ds_read_b64_tr_b16 a[204:205], %[vr15] offset:57344\n\t" "s_add_u32 m0, %[sl0], 24576\n\t"
    "v_mfma_f32_16x16x32_bf16 v[164:167], a[180:183], a[76:79], v[164:167]\n\t" "ds_read_b64_tr_b16 a[206:207], %[vr15] offset:59392\n\t" "global_load_lds_dwordx4 %[vb0], %[sb]\n\t"
    "v_mfma_f32_16x16x32_bf16 v[168:171], a[184:187], a[64:67], v[168:171]\n\t" "s_barrier\n\t" "ds_read_b64_tr_b16 a[176:177], %[vr4] offset:0\n\t"
    "v_mfma_f32_16x16x32_bf16 v[168:171], a[188:191], a[72:75], v[168:171]\n\t" "ds_read_b64_tr_b16 a[178:179], %[vr4] offset:2048\n\t" "v_add_u32 %[vb0], 0x10000, %[vb0]\n\t"
    "v_mfma_f32_16x16x32_bf16 v[172:175], a[184:187], a[68:71], v[172:175]\n\t" "ds_read_b64_tr_b16 a[180:181], %[vr4] offset:8192\n\t" "s_add_u32 m0, %[sl1], 24576\n\t"
    "v_mfma_f32_16x16x32_bf16 v[172:175], a[188:191], a[76:79], v[172:175]\n\t" "ds_read_b64_tr_b16 a[182:183], %[vr4] offset:10240\n\t" "s_waitcnt lgkmcnt(4)\n\t"
    "v_mfma_f32_16x16x32_bf16 v[176:179], a[192:195], a[64:67], v[176:179]\n\t" "ds_read_b64_tr_b16 a[184:185], %[vr5] offset:0\n\t" "global_load_lds_dwordx4 %[vb1], %[sb]\n\t"
    "v_mfma_f32_16x16x32_bf16 v[176:179], a[196:199], a[72:75], v[176:179]\n\t" "ds_read_b64_tr_b16 a[186:187], %[vr5] offset:2048\n\t" "v_add_u32 %[vb1], 0x10000, %[vb1]\n\t"
    "v_mfma_f32_16x16x32_bf16 v[180:183], a[192:195], a[68:71], v[180:183]\n\t" "ds_read_b64_tr_b16 a[188:189], %[vr5] offset:8192\n\t" "s_add_u32 m0, %[sl0], 32768\n\t"
    "v_mfma_f32_16x16x32_bf16 v[180:183], a[196:199], a[76:79], v[180:183]\n\t" "ds_read_b64_tr_b16 a[190:191], %[vr5] offset:10240\n\t" "global_load_lds_dwordx4 %[vb0], %[sb]\n\t"
    "v_mfma_f32_16x16x32_bf16 v[184:187], a[200:203], a[64:67], v[184:187]\n\t" "ds_read_b64_tr_b16 a[192:193], %[vr6] offset:0\n\t" "v_add_u32 %[vb0], 0x10000, %[vb0]\n\t"
    "v_mfma_f32_16x16x32_bf16 v[184:187], a[204:207], a[72:75], v[184:187]\n\t" "ds_read_b64_tr_b16 a[194:195], %[vr6] offset:2048\n\t" "s_add_u32 m0, %[sl1], 32768\n\t"
    "v_mfma_f32_16x16x32_bf16 v[188:191], a[200:203], a[68:71], v[188:191]\n\t" "ds_read_b64_tr_b16 a[196:197], %[vr6] offset:8192\n\t"
    "v_mfma_f32_16x16x32_bf16 v[188:191], a[204:207], a[76:79], v[188:191]\n\t" "ds_read_b64_tr_b16 a[198:199], %[vr6] offset:10240\n\t" "s_waitcnt lgkmcnt(4) vmcnt(25)\n\t"
    "v_mfma_f32_16x16x32_bf16 v[224:227], a[176:179], a[0:3], v[224:227]\n\t" "ds_read_b64_tr_b16 a[200:201], %[vr7] offset:0\n\t" "global_load_lds_dwordx4 %[vb1], %[sb]\n\t"
    "v_mfma_f32_16x16x32_bf16 v[224:227], a[180:183], a[8:11], v[224:227]\n\t" "ds_read_b64_tr_b16 a[202:203], %[vr7] offset:2048\n\t" "v_add_u32 %[vb1], 0x10000, %[vb1]\n\t"
    "v_mfma_f32_16x16x32_bf16 v[240:243], a[176:179], a[4:7], v[240:243]\n\t" "ds_read_b64_tr_b16 a[204:205], %[vr7] offset:8192\n\t"
    "v_mfma_f32_16x16x32_bf16 v[240:243], a[180:183], a[12:15], v[240:243]\n\t" "ds_read_b64_tr_b16 a[206:207], %[vr7] offset:10240\n\t" "global_load_dwordx4 a[64:67], %[vx], s[40:41] offset:0 sc1\n\t"
    "v_mfma_f32_16x16x32_bf16 v[228:231], a[184:187], a[0:3], v[228:231]\n\t" "ds_read_b64_tr_b16 a[176:177], %[vr16] offset:0\n\t"
    "v_mfma_f32_16x16x32_bf16 v[228:231], a[188:191], a[8:11], v[228:231]\n\t" "ds_read_b64_tr_b16 a[178:179], %[vr16] offset:2048\n\t"
    "v_mfma_f32_16x16x32_bf16 v[244:247], a[184:187], a[4:7], v[244:247]\n\t" "ds_read_b64_tr_b16 a[180:181], %[vr16] offset:8192\n\t"
    "v_mfma_f32_16x16x32_bf16 v[244:247], a[188:191], a[12:15], v[244:247]\n\t" "ds_read_b64_tr_b16 a[182:183], %[vr16] offset:10240\n\t" "s_waitcnt lgkmcnt(4)\n\t"
    "v_mfma_f32_16x16x32_bf16 v[232:235], a[192:195], a[0:3], v[232:235]\n\t" "ds_read_b64_tr_b16 a[184:185], %[vr17] offset:0\n\t" "global_load_dwordx4 a[68:71], %[vx], s[40:41] offset:1024 sc1\n\t"
    "v_mfma_f32_16x16x32_bf16 v[232:235], a[196:199], a[8:11], v[232:235]\n\t" "ds_read_b64_tr_b16 a[186:187], %[vr17] offset:2048\n\t"
    "v_mfma_f32_16x16x32_bf16 v[248:251], a[192:195], a[4:7], v[248:251]\n\t" "ds_read_b64_tr_b16 a[188:189], %[vr17] offset:8192\n\t"
    "v_mfma_f32_16x16x32_bf16 v[248:251], a[196:199], a[12:15], v[248:251]\n\t" "ds_read_b64_tr_b16 a[190:191], %[vr17] offset:10240\n\t"
    "global_load_dwordx4 a[72:75], %[vx], s[40:41] offset:2048 sc1\n\t" "v_mfma_f32_16x16x32_bf16 v[236:239], a[200:203], a[0:3], v[236:239]\n\t" "ds_read_b64_tr_b16 a[192:193], %[vr18] offset:0\n\t"
    "v_mfma_f32_16x16x32_bf16 v[236:239], a[204:207], a[8:11], v[236:239]\n\t" "ds_read_b64_tr_b16 a[194:195], %[vr18] offset:2048\n\t"
    "v_mfma_f32_16x16x32_bf16 v[252:255], a[200:203], a[4:7], v[252:255]\n\t" "ds_read_b64_tr_b16 a[196:197], %[vr18] offset:8192\n\t"
    "v_mfma_f32_16x16x32_bf16 v[252:255], a[204:207], a[12:15], v[252:255]\n\t" "ds_read_b64_tr_b16 a[198:199], %[vr18] offset:10240\n\t" "s_waitcnt lgkmcnt(4)\n\t"
    "v_mfma_f32_16x16x32_bf16 v[160:163], a[176:179], a[0:3], v[160:163]\n\t" "ds_read_b64_tr_b16 a[200:201], %[vr19] offset:0\n\t" "global_load_dwordx4 a[76:79], %[vx], s[40:41] offset:3072 sc1\n\t"
    "v_mfma_f32_16x16x32_bf16 v[160:163], a[180:183], a[8:11], v[160:163]\n\t" "ds_read_b64_tr_b16 a[202:203], %[vr19] offset:2048\n\t" "s_add_u32 m0, %[sl0], 40960\n\t"
    "v_mfma_f32_16x16x32_bf16 v[164:167], a[176:179], a[4:7], v[164:167]\n\t" "ds_read_b64_tr_b16 a[204:205], %[vr19] offset:8192\n\t"
    "v_mfma_f32_16x16x32_bf16 v[164:167], a[180:183], a[12:15], v[164:167]\n\t" "ds_read_b64_tr_b16 a[206:207], %[vr19] offset:10240\n\t" "global_load_lds_dwordx4 %[vb0], %[sb]\n\t"
    "v_mfma_f32_16x16x32_bf16 v[168:171], a[184:187], a[0:3], v[168:171]\n\t" "s_barrier\n\t" "ds_read_b64_tr_b16 a[176:177], %[vr4] offset:16384\n\t"
    "v_mfma_f32_16x16x32_bf16 v[168:171], a[188:191], a[8:11], v[168:171]\n\t" "ds_read_b64_tr_b16 a[178:179], %[vr4] offset:18432\n\t" "v_add_u32 %[vb0], 0x10000, %[vb0]\n\t"
    "v_mfma_f32_16x16x32_bf16 v[172:175], a[184:187], a[4:7], v[172:175]\n\t" "ds_read_b64_tr_b16 a[180:181], %[vr4] offset:24576\n\t" "s_add_u32 m0, %[sl1], 40960\n\t"
    "v_mfma_f32_16x16x32_bf16 v[172:175], a[188:191], a[12:15], v[172:175]\n\t" "ds_read_b64_tr_b16 a[182:183], %[vr4] offset:26624\n\t" "s_waitcnt lgkmcnt(4)\n\t"
    "v_mfma_f32_16x16x32_bf16 v[176:179], a[192:195], a[0:3], v[176:179]\n\t" "ds_read_b64_tr_b16 a[184:185], %[vr5] offset:16384\n\t" "global_load_lds_dwordx4 %[vb1], %[sb]\n\t"
    "v_mfma_f32_16x16x32_bf16 v[176:179], a[196:199], a[8:11], v[176:179]\n\t" "ds_read_b64_tr_b16 a[186:187], %[vr5] offset:18432\n\t" "v_add_u32 %[vb1], 0x10000, %[vb1]\n\t"
    "v_mfma_f32_16x16x32_bf16 v[180:183], a[192:195], a[4:7], v[180:183]\n\t" "ds_read_b64_tr_b16 a[188:189], %[vr5] offset:24576\n\t" "s_add_u32 m0, %[sl0], 139264\n\t"
    "v_mfma_f32_16x16x32_bf16 v[180:183], a[196:199], a[12:15], v[180:183]\n\t" "ds_read_b64_tr_b16 a[190:191], %[vr5] offset:26624\n\t" "global_load_lds_dwordx4 %[vb0], %[sb]\n\t"
    "v_mfma_f32_16x16x32_bf16 v[184:187], a[200:203], a[0:3], v[184:187]\n\t" "ds_read_b64_tr_b16 a[192:193], %[vr6] offset:16384\n\t" "v_add_u32 %[vb0], 0x10000, %[vb0]\n\t"
    "v_mfma_f32_16x16x32_bf16 v[184:187], a[204:207], a[8:11], v[184:187]\n\t" "ds_read_b64_tr_b16 a[194:195], %[vr6] offset:18432\n\t" "s_add_u32 m0, %[sl1], 139264\n\t"
    "v_mfma_f32_16x16x32_bf16 v[188:191], a[200:203], a[4:7], v[188:191]\n\t" "ds_read_b64_tr_b16 a[196:197], %[vr6] offset:24576\n\t"
    "v_mfma_f32_16x16x32_bf16 v[188:191], a[204:207], a[12:15], v[188:191]\n\t" "ds_read_b64_tr_b16 a[198:199], %[vr6] offset:26624\n\t" "s_waitcnt lgkmcnt(4) vmcnt(29)\n\t"
    "v_mfma_f32_16x16x32_bf16 v[224:227], a[176:179], a[16:19], v[224:227]\n\t" "ds_read_b64_tr_b16 a[200:201], %[vr7] offset:16384\n\t" "global_load_lds_dwordx4 %[vb1], %[sb]\n\t"
    "v_mfma_f32_16x16x32_bf16 v[224:227], a[180:183], a[24:27], v[224:227]\n\t" "ds_read_b64_tr_b16 a[202:203], %[vr7] offset:18432\n\t" "v_add_u32 %[vb1], 0x10000, %[vb1]\n\t"
    "v_mfma_f32_16x16x32_bf16 v[240:243], a[176:179], a[20:23], v[240:243]\n\t" "ds_read_b64_tr_b16 a[204:205], %[vr7] offset:24576\n\t" "s_add_u32 s40, s40, 8192\n\t" "s_addc_u32 s41, s41, 0\n\t"
    "v_mfma_f32_16x16x32_bf16 v[240:243], a[180:183], a[28:31], v[240:243]\n\t" "ds_read_b64_tr_b16 a[206:207], %[vr7] offset:26624\n\t" "global_load_dwordx4 a[0:3], %[vx], s[40:41] offset:-4096 sc1\n\t"
    "v_mfma_f32_16x16x32_bf16 v[228:231], a[184:187], a[16:19], v[228:231]\n\t" "ds_read_b64_tr_b16 a[176:177], %[vr16] offset:16384\n\t"
    "v_mfma_f32_16x16x32_bf16 v[228:231], a[188:191], a[24:27], v[228:231]\n\t" "ds_read_b64_tr_b16 a[178:179], %[vr16] offset:18432\n\t"
    "v_mfma_f32_16x16x32_bf16 v[244:247], a[184:187], a[20:23], v[244:247]\n\t" "ds_read_b64_tr_b16 a[180:181], %[vr16] offset:24576\n\t"
    "v_mfma_f32_16x16x32_bf16 v[244:247], a[188:191], a[28:31], v[244:247]\n\t" "ds_read_b64_tr_b16 a[182:183], %[vr16] offset:26624\n\t" "s_waitcnt lgkmcnt(4)\n\t"
    "v_mfma_f32_16x16x32_bf16 v[232:235], a[192:195], a[16:19], v[232:235]\n\t" "ds_read_b64_tr_b16 a[184:185], %[vr17] offset:16384\n\t" "global_load_dwordx4 a[4:7], %[vx], s[40:41] offset:-3072 sc1\n\t"
    "v_mfma_f32_16x16x32_bf16 v[232:235], a[196:199], a[24:27], v[232:235]\n\t" "ds_read_b64_tr_b16 a[186:187], %[vr17] offset:18432\n\t"
    "v_mfma_f32_16x16x32_bf16 v[248:251], a[192:195], a[20:23], v[248:251]\n\t" "ds_read_b64_tr_b16 a[188:189], %[vr17] offset:24576\n\t"
    "v_mfma_f32_16x16x32_bf16 v[248:251], a[196:199], a[28:31], v[248:251]\n\t" "ds_read_b64_tr_b16 a[190:191], %[vr17] offset:26624\n\t"
    "global_load_dwordx4 a[8:11], %[vx], s[40:41] offset:-2048 sc1\n\t" "v_mfma_f32_16x16x32_bf16 v[236:239], a[200:203], a[16:19], v[236:239]\n\t"
    "ds_read_b64_tr_b16 a[192:193], %[vr18] offset:16384\n\t" "v_mfma_f32_16x16x32_bf16 v[236:239], a[204:207], a[24:27], v[236:239]\n\t" "ds_read_b64_tr_b16 a[194:195], %[vr18] offset:18432\n\t"
    "v_mfma_f32_16x16x32_bf16 v[252:255], a[200:203], a[20:23], v[252:255]\n\t" "ds_read_b64_tr_b16 a[196:197], %[vr18] offset:24576\n\t"
    "v_mfma_f32_16x16x32_bf16 v[252:255], a[204:207], a[28:31], v[252:255]\n\t" "ds_read_b64_tr_b16 a[198:199], %[vr18] offset:26624\n\t" "s_waitcnt lgkmcnt(4)\n\t"
    "v_mfma_f32_16x16x32_bf16 v[160:163], a[176:179], a[16:19], v[160:163]\n\t" "ds_read_b64_tr_b16 a[200:201], %[vr19] offset:16384\n\t"
    "global_load_dwordx4 a[12:15], %[vx], s[40:41] offset:-1024 sc1\n\t" "v_mfma_f32_16x16x32_bf16 v[160:163], a[180:183], a[24:27], v[160:163]\n\t"
    "ds_read_b64_tr_b16 a[202:203], %[vr19] offset:18432\n\t" "s_add_u32 m0, %[sl0], 147456\n\t" "v_mfma_f32_16x16x32_bf16 v[164:167], a[176:179], a[20:23], v[164:167]\n\t"
    "ds_read_b64_tr_b16 a[204:205], %[vr19] offset:24576\n\t" "v_mfma_f32_16x16x32_bf16 v[164:167], a[180:183], a[28:31], v[164:167]\n\t" "ds_read_b64_tr_b16 a[206:207], %[vr19] offset:26624\n\t"
    "global_load_lds_dwordx4 %[vb0], %[sb]\n\t" "v_mfma_f32_16x16x32_bf16 v[168:171], a[184:187], a[16:19], v[168:171]\n\t" "s_barrier\n\t" "ds_read_b64_tr_b16 a[176:177], %[vr4] offset:32768\n\t"
    "v_mfma_f32_16x16x32_bf16 v[168:171], a[188:191], a[24:27], v[168:171]\n\t" "ds_read_b64_tr_b16 a[178:179], %[vr4] offset:34816\n\t" "v_add_u32 %[vb0], 0x10000, %[vb0]\n\t"
    "v_mfma_f32_16x16x32_bf16 v[172:175], a[184:187], a[20:23], v[172:175]\n\t" "ds_read_b64_tr_b16 a[180:181], %[vr4] offset:40960\n\t" "s_add_u32 m0, %[sl1], 147456\n\t"
    "v_mfma_f32_16x16x32_bf16 v[172:175], a[188:191], a[28:31], v[172:175]\n\t" "ds_read_b64_tr_b16 a[182:183], %[vr4] offset:43008\n\t" "s_waitcnt lgkmcnt(4)\n\t"
    "v_mfma_f32_16x16x32_bf16 v[176:179], a[192:195], a[16:19], v[176:179]\n\t" "ds_read_b64_tr_b16 a[184:185], %[vr5] offset:32768\n\t" "global_load_lds_dwordx4 %[vb1], %[sb]\n\t"
    "v_mfma_f32_16x16x32_bf16 v[176:179], a[196:199], a[24:27], v[176:179]\n\t" "ds_read_b64_tr_b16 a[186:187], %[vr5] offset:34816\n\t" "v_add_u32 %[vb1], 0x10000, %[vb1]\n\t"
    "v_mfma_f32_16x16x32_bf16 v[180:183], a[192:195], a[20:23], v[180:183]\n\t" "ds_read_b64_tr_b16 a[188:189], %[vr5] offset:40960\n\t" "s_add_u32 m0, %[sl0], 155648\n\t"
    "v_mfma_f32_16x16x32_bf16 v[180:183], a[196:199], a[28:31], v[180:183]\n\t" "ds_read_b64_tr_b16 a[190:191], %[vr5] offset:43008\n\t" "global_load_lds_dwordx4 %[vb0], %[sb]\n\t"
    "v_mfma_f32_16x16x32_bf16 v[184:187], a[200:203], a[16:19], v[184:187]\n\t" "ds_read_b64_tr_b16 a[192:193], %[vr6] offset:32768\n\t" "v_add_u32 %[vb0], 0x10000, %[vb0]\n\t"
    "v_mfma_f32_16x16x32_bf16 v[184:187], a[204:207], a[24:27], v[184:187]\n\t" "ds_read_b64_tr_b16 a[194:195], %[vr6] offset:34816\n\t" "s_add_u32 m0, %[sl1], 155648\n\t"
    "v_mfma_f32_16x16x32_bf16 v[188:191], a[200:203], a[20:23], v[188:191]\n\t" "ds_read_b64_tr_b16 a[196:197], %[vr6] offset:40960\n\t"
    "v_mfma_f32_16x16x32_bf16 v[188:191], a[204:207], a[28:31], v[188:191]\n\t" "ds_read_b64_tr_b16 a[198:199], %[vr6] offset:43008\n\t" "s_waitcnt lgkmcnt(4) vmcnt(29)\n\t"
    "v_mfma_f32_16x16x32_bf16 v[224:227], a[176:179], a[32:35], v[224:227]\n\t" "ds_read_b64_tr_b16 a[200:201], %[vr7] offset:32768\n\t" "global_load_lds_dwordx4 %[vb1], %[sb]\n\t"
    "v_mfma_f32_16x16x32_bf16 v[224:227], a[180:183], a[40:43], v[224:227]\n\t" "ds_read_b64_tr_b16 a[202:203], %[vr7] offset:34816\n\t" "v_add_u32 %[vb1], 0x10000, %[vb1]\n\t"
    "v_mfma_f32_16x16x32_bf16 v[240:243], a[176:179], a[36:39], v[240:243]\n\t" "ds_read_b64_tr_b16 a[204:205], %[vr7] offset:40960\n\t"
    "v_mfma_f32_16x16x32_bf16 v[240:243], a[180:183], a[44:47], v[240:243]\n\t" "ds_read_b64_tr_b16 a[206:207], %[vr7] offset:43008\n\t" "global_load_dword a255, %[vx], %[cd]\n\t"
    "v_mfma_f32_16x16x32_bf16 v[228:231], a[184:187], a[32:35], v[228:231]\n\t" "ds_read_b64_tr_b16 a[176:177], %[vr16] offset:32768\n\t"
    "v_mfma_f32_16x16x32_bf16 v[228:231], a[188:191], a[40:43], v[228:231]\n\t" "ds_read_b64_tr_b16 a[178:179], %[vr16] offset:34816\n\t" "global_load_dwordx4 a[16:19], %[vx], s[40:41] offset:0 sc1\n\t"
    "v_mfma_f32_16x16x32_bf16 v[244:247], a[184:187], a[36:39], v[244:247]\n\t" "ds_read_b64_tr_b16 a[180:181], %[vr16] offset:40960\n\t"
    "v_mfma_f32_16x16x32_bf16 v[244:247], a[188:191], a[44:47], v[244:247]\n\t" "ds_read_b64_tr_b16 a[182:183], %[vr16] offset:43008\n\t" "s_waitcnt lgkmcnt(4)\n\t"
    "v_mfma_f32_16x16x32_bf16 v[232:235], a[192:195], a[32:35], v[232:235]\n\t" "ds_read_b64_tr_b16 a[184:185], %[vr17] offset:32768\n\t"
    "v_mfma_f32_16x16x32_bf16 v[232:235], a[196:199], a[40:43], v[232:235]\n\t" "ds_read_b64_tr_b16 a[186:187], %[vr17] offset:34816\n\t"
    "global_load_dwordx4 a[20:23], %[vx], s[40:41] offset:1024 sc1\n\t" "v_mfma_f32_16x16x32_bf16 v[248:251], a[192:195], a[36:39], v[248:251]\n\t"
    "ds_read_b64_tr_b16 a[188:189], %[vr17] offset:40960\n\t" "v_mfma_f32_16x16x32_bf16 v[248:251], a[196:199], a[44:47], v[248:251]\n\t" "ds_read_b64_tr_b16 a[190:191], %[vr17] offset:43008\n\t"
    "v_mfma_f32_16x16x32_bf16 v[236:239], a[200:203], a[32:35], v[236:239]\n\t" "ds_read_b64_tr_b16 a[192:193], %[vr18] offset:32768\n\t"
    "v_mfma_f32_16x16x32_bf16 v[236:239], a[204:207], a[40:43], v[236:239]\n\t" "ds_read_b64_tr_b16 a[194:195], %[vr18] offset:34816\n\t"
    "global_load_dwordx4 a[24:27], %[vx], s[40:41] offset:2048 sc1\n\t" "v_mfma_f32_16x16x32_bf16 v[252:255], a[200:203], a[36:39], v[252:255]\n\t"
    "ds_read_b64_tr_b16 a[196:197], %[vr18] offset:40960\n\t" "v_mfma_f32_16x16x32_bf16 v[252:255], a[204:207], a[44:47], v[252:255]\n\t" "ds_read_b64_tr_b16 a[198:199], %[vr18] offset:43008\n\t"
    "s_waitcnt lgkmcnt(4)\n\t" "v_mfma_f32_16x16x32_bf16 v[160:163], a[176:179], a[32:35], v[160:163]\n\t" "ds_read_b64_tr_b16 a[200:201], %[vr19] offset:32768\n\t"
    "v_mfma_f32_16x16x32_bf16 v[160:163], a[180:183], a[40:43], v[160:163]\n\t" "ds_read_b64_tr_b16 a[202:203], %[vr19] offset:34816\n\t"
    "global_load_dwordx4 a[28:31], %[vx], s[40:41] offset:3072 sc1\n\t" "v_mfma_f32_16x16x32_bf16 v[164:167], a[176:179], a[36:39], v[164:167]\n\t"
    "ds_read_b64_tr_b16 a[204:205], %[vr19] offset:40960\n\t" "v_mfma_f32_16x16x32_bf16 v[164:167], a[180:183], a[44:47], v[164:167]\n\t" "ds_read_b64_tr_b16 a[206:207], %[vr19] offset:43008\n\t"
    "s_add_u32 m0, %[sl0], 0\n\t" "v_mfma_f32_16x16x32_bf16 v[168:171], a[184:187], a[32:35], v[168:171]\n\t" "s_barrier\n\t" "ds_read_b64_tr_b16 a[176:177], %[vr4] offset:49152\n\t"
    "v_mfma_f32_16x16x32_bf16 v[168:171], a[188:191], a[40:43], v[168:171]\n\t" "ds_read_b64_tr_b16 a[178:179], %[vr4] offset:51200\n\t" "global_load_lds_dwordx4 %[vb0], %[sb]\n\t"
    "v_mfma_f32_16x16x32_bf16 v[172:175], a[184:187], a[36:39], v[172:175]\n\t" "ds_read_b64_tr_b16 a[180:181], %[vr4] offset:57344\n\t" "v_add_u32 %[vb0], 0x10000, %[vb0]\n\t"
    "v_mfma_f32_16x16x32_bf16 v[172:175], a[188:191], a[44:47], v[172:175]\n\t" "ds_read_b64_tr_b16 a[182:183], %[vr4] offset:59392\n\t" "s_waitcnt lgkmcnt(4)\n\t"
    "v_mfma_f32_16x16x32_bf16 v[176:179], a[192:195], a[32:35], v[176:179]\n\t" "ds_read_b64_tr_b16 a[184:185], %[vr5] offset:49152\n\t" "s_add_u32 m0, %[sl1], 0\n\t"
    "v_mfma_f32_16x16x32_bf16 v[176:179], a[196:199], a[40:43], v[176:179]\n\t" "ds_read_b64_tr_b16 a[186:187], %[vr5] offset:51200\n\t" "global_load_lds_dwordx4 %[vb1], %[sb]\n\t"
    "v_mfma_f32_16x16x32_bf16 v[180:183], a[192:195], a[36:39], v[180:183]\n\t" "ds_read_b64_tr_b16 a[188:189], %[vr5] offset:57344\n\t" "v_add_u32 %[vb1], 0x10000, %[vb1]\n\t"
    "v_mfma_f32_16x16x32_bf16 v[180:183], a[196:199], a[44:47], v[180:183]\n\t" "ds_read_b64_tr_b16 a[190:191], %[vr5] offset:59392\n\t" "s_add_u32 m0, %[sl0], 8192\n\t"
    "v_mfma_f32_16x16x32_bf16 v[184:187], a[200:203], a[32:35], v[184:187]\n\t" "ds_read_b64_tr_b16 a[192:193], %[vr6] offset:49152\n\t"
    "v_mfma_f32_16x16x32_bf16 v[184:187], a[204:207], a[40:43], v[184:187]\n\t" "ds_read_b64_tr_b16 a[194:195], %[vr6] offset:51200\n\t" "global_load_lds_dwordx4 %[vb0], %[sb]\n\t"
    "v_mfma_f32_16x16x32_bf16 v[188:191], a[200:203], a[36:39], v[188:191]\n\t" "ds_read_b64_tr_b16 a[196:197], %[vr6] offset:57344\n\t" "v_add_u32 %[vb0], 0x10000, %[vb0]\n\t"
    "v_mfma_f32_16x16x32_bf16 v[188:191], a[204:207], a[44:47], v[188:191]\n\t" "ds_read_b64_tr_b16 a[198:199], %[vr6] offset:59392\n\t" "s_waitcnt lgkmcnt(4) vmcnt(28)\n\t"
    "v_mfma_f32_16x16x32_bf16 v[224:227], a[176:179], a[48:51], v[224:227]\n\t" "ds_read_b64_tr_b16 a[200:201], %[vr7] offset:49152\n\t" "s_add_u32 m0, %[sl1], 8192\n\t"
    "v_mfma_f32_16x16x32_bf16 v[224:227], a[180:183], a[56:59], v[224:227]\n\t" "ds_read_b64_tr_b16 a[202:203], %[vr7] offset:51200\n\t" "global_load_lds_dwordx4 %[vb1], %[sb]\n\t"
    "v_mfma_f32_16x16x32_bf16 v[240:243], a[176:179], a[52:55], v[240:243]\n\t" "ds_read_b64_tr_b16 a[204:205], %[vr7] offset:57344\n\t" "v_add_u32 %[vb1], 0x10000, %[vb1]\n\t"
    "v_mfma_f32_16x16x32_bf16 v[240:243], a[180:183], a[60:63], v[240:243]\n\t" "ds_read_b64_tr_b16 a[206:207], %[vr7] offset:59392\n\t" "s_add_u32 s40, s40, 8192\n\t" "s_addc_u32 s41, s41, 0\n\t"
    "v_mfma_f32_16x16x32_bf16 v[228:231], a[184:187], a[48:51], v[228:231]\n\t" "ds_read_b64_tr_b16 a[176:177], %[vr16] offset:49152\n\t"
    "v_mfma_f32_16x16x32_bf16 v[228:231], a[188:191], a[56:59], v[228:231]\n\t" "ds_read_b64_tr_b16 a[178:179], %[vr16] offset:51200\n\t"
    "global_load_dwordx2 a[32:33], %[vo], s[40:41] offset:-4096 sc1\n\t" "v_mfma_f32_16x16x32_bf16 v[244:247], a[184:187], a[52:55], v[244:247]\n\t"
    "ds_read_b64_tr_b16 a[180:181], %[vr16] offset:57344\n\t" "v_mfma_f32_16x16x32_bf16 v[244:247], a[188:191], a[60:63], v[244:247]\n\t" "ds_read_b64_tr_b16 a[182:183], %[vr16] offset:59392\n\t"
    "s_waitcnt lgkmcnt(4)\n\t" "v_mfma_f32_16x16x32_bf16 v[232:235], a[192:195], a[48:51], v[232:235]\n\t" "ds_read_b64_tr_b16 a[184:185], %[vr17] offset:49152\n\t"
    "global_load_dwordx2 a[34:35], %[vo], s[40:41] offset:-3072 sc1\n\t" "v_mfma_f32_16x16x32_bf16 v[232:235], a[196:199], a[56:59], v[232:235]\n\t"
    "ds_read_b64_tr_b16 a[186:187], %[vr17] offset:51200\n\t" "global_load_dwordx2 a[36:37], %[vo], s[40:41] offset:-3584 sc1\n\t"
    "v_mfma_f32_16x16x32_bf16 v[248:251], a[192:195], a[52:55], v[248:251]\n\t" "ds_read_b64_tr_b16 a[188:189], %[vr17] offset:57344\n\t"
    "v_mfma_f32_16x16x32_bf16 v[248:251], a[196:199], a[60:63], v[248:251]\n\t" "ds_read_b64_tr_b16 a[190:191], %[vr17] offset:59392\n\t"
    "global_load_dwordx2 a[38:39], %[vo], s[40:41] offset:-2560 sc1\n\t" "v_mfma_f32_16x16x32_bf16 v[236:239], a[200:203], a[48:51], v[236:239]\n\t"
    "ds_read_b64_tr_b16 a[192:193], %[vr18] offset:49152\n\t" "v_mfma_f32_16x16x32_bf16 v[236:239], a[204:207], a[56:59], v[236:239]\n\t" "ds_read_b64_tr_b16 a[194:195], %[vr18] offset:51200\n\t"
    "global_load_dwordx4 a[40:43], %[vx], s[40:41] offset:-2048 sc1\n\t" "v_mfma_f32_16x16x32_bf16 v[252:255], a[200:203], a[52:55], v[252:255]\n\t"
    "ds_read_b64_tr_b16 a[196:197], %[vr18] offset:57344\n\t" "v_mfma_f32_16x16x32_bf16 v[252:255], a[204:207], a[60:63], v[252:255]\n\t" "ds_read_b64_tr_b16 a[198:199], %[vr18] offset:59392\n\t"
    "s_waitcnt lgkmcnt(4)\n\t" "v_mfma_f32_16x16x32_bf16 v[160:163], a[176:179], a[48:51], v[160:163]\n\t" "ds_read_b64_tr_b16 a[200:201], %[vr19] offset:49152\n\t"
    "v_mfma_f32_16x16x32_bf16 v[160:163], a[180:183], a[56:59], v[160:163]\n\t" "ds_read_b64_tr_b16 a[202:203], %[vr19] offset:51200\n\t"
    "global_load_dwordx4 a[44:47], %[vx], s[40:41] offset:-1024 sc1\n\t" "v_mfma_f32_16x16x32_bf16 v[164:167], a[176:179], a[52:55], v[164:167]\n\t"
    "ds_read_b64_tr_b16 a[204:205], %[vr19] offset:57344\n\t" "v_mfma_f32_16x16x32_bf16 v[164:167], a[180:183], a[60:63], v[164:167]\n\t" "ds_read_b64_tr_b16 a[206:207], %[vr19] offset:59392\n\t"
    "s_add_u32 m0, %[sl0], 49152\n\t" "v_mfma_f32_16x16x32_bf16 v[168:171], a[184:187], a[48:51], v[168:171]\n\t" "s_barrier\n\t" "ds_read_b64_tr_b16 a[176:177], %[vr8] offset:0\n\t"
    "v_mfma_f32_16x16x32_bf16 v[168:171], a[188:191], a[56:59], v[168:171]\n\t" "ds_read_b64_tr_b16 a[178:179], %[vr8] offset:2048\n\t" "global_load_lds_dwordx4 %[vb0], %[sb]\n\t"
    "v_mfma_f32_16x16x32_bf16 v[172:175], a[184:187], a[52:55], v[172:175]\n\t" "ds_read_b64_tr_b16 a[180:181], %[vr0] offset:16384\n\t" "v_add_u32 %[vb0], 0x10000, %[vb0]\n\t"
    "v_mfma_f32_16x16x32_bf16 v[172:175], a[188:191], a[60:63], v[172:175]\n\t" "ds_read_b64_tr_b16 a[182:183], %[vr0] offset:18432\n\t" "s_waitcnt lgkmcnt(4)\n\t"
    "v_mfma_f32_16x16x32_bf16 v[176:179], a[192:195], a[48:51], v[176:179]\n\t" "ds_read_b64_tr_b16 a[184:185], %[vr9] offset:0\n\t" "s_add_u32 m0, %[sl1], 49152\n\t"
    "v_mfma_f32_16x16x32_bf16 v[176:179], a[196:199], a[56:59], v[176:179]\n\t" "ds_read_b64_tr_b16 a[186:187], %[vr9] offset:2048\n\t" "global_load_lds_dwordx4 %[vb1], %[sb]\n\t"
    "v_mfma_f32_16x16x32_bf16 v[180:183], a[192:195], a[52:55], v[180:183]\n\t" "ds_read_b64_tr_b16 a[188:189], %[vr1] offset:16384\n\t" "v_add_u32 %[vb1], 0x10000, %[vb1]\n\t"
    "v_mfma_f32_16x16x32_bf16 v[180:183], a[196:199], a[60:63], v[180:183]\n\t" "ds_read_b64_tr_b16 a[190:191], %[vr1] offset:18432\n\t" "s_add_u32 m0, %[sl0], 57344\n\t"
    "v_mfma_f32_16x16x32_bf16 v[184:187], a[200:203], a[48:51], v[184:187]\n\t" "ds_read_b64_tr_b16 a[192:193], %[vr10] offset:0\n\t"
    "v_mfma_f32_16x16x32_bf16 v[184:187], a[204:207], a[56:59], v[184:187]\n\t" "ds_read_b64_tr_b16 a[194:195], %[vr10] offset:2048\n\t" "global_load_lds_dwordx4 %[vb0], %[sb]\n\t"
    "v_mfma_f32_16x16x32_bf16 v[188:191], a[200:203], a[52:55], v[188:191]\n\t" "ds_read_b64_tr_b16 a[196:197], %[vr2] offset:16384\n\t" "v_add_u32 %[vb0], 0x10000, %[vb0]\n\t"
    "v_mfma_f32_16x16x32_bf16 v[188:191], a[204:207], a[60:63], v[188:191]\n\t" "ds_read_b64_tr_b16 a[198:199], %[vr2] offset:18432\n\t" "s_waitcnt lgkmcnt(4) vmcnt(30)\n\t"
    "v_mfma_f32_16x16x32_bf16 v[224:227], a[176:179], a[64:67], v[224:227]\n\t" "ds_read_b64_tr_b16 a[200:201], %[vr11] offset:0\n\t" "s_add_u32 m0, %[sl1], 57344\n\t"
    "v_mfma_f32_16x16x32_bf16 v[224:227], a[180:183], a[72:75], v[224:227]\n\t" "ds_read_b64_tr_b16 a[202:203], %[vr11] offset:2048\n\t" "global_load_lds_dwordx4 %[vb1], %[sb]\n\t"
    "v_mfma_f32_16x16x32_bf16 v[240:243], a[176:179], a[68:71], v[240:243]\n\t" "ds_read_b64_tr_b16 a[204:205], %[vr3] offset:16384\n\t" "v_add_u32 %[vb1], 0x10000, %[vb1]\n\t"
    "v_mfma_f32_16x16x32_bf16 v[240:243], a[180:183], a[76:79], v[240:243]\n\t" "ds_read_b64_tr_b16 a[206:207], %[vr3] offset:18432\n\t"
    "v_mfma_f32_16x16x32_bf16 v[228:231], a[184:187], a[64:67], v[228:231]\n\t" "ds_read_b64_tr_b16 a[176:177], %[vr20] offset:0\n\t"
    "v_mfma_f32_16x16x32_bf16 v[228:231], a[188:191], a[72:75], v[228:231]\n\t" "ds_read_b64_tr_b16 a[178:179], %[vr20] offset:2048\n\t" "global_load_dwordx4 a[48:51], %[vx], s[40:41] offset:0 sc1\n\t"
    "v_mfma_f32_16x16x32_bf16 v[244:247], a[184:187], a[68:71], v[244:247]\n\t" "ds_read_b64_tr_b16 a[180:181], %[vr12] offset:16384\n\t"
    "v_mfma_f32_16x16x32_bf16 v[244:247], a[188:191], a[76:79], v[244:247]\n\t" "ds_read_b64_tr_b16 a[182:183], %[vr12] offset:18432\n\t" "s_waitcnt lgkmcnt(4)\n\t"
    "v_mfma_f32_16x16x32_bf16 v[232:235], a[192:195], a[64:67], v[232:235]\n\t" "ds_read_b64_tr_b16 a[184:185], %[vr21] offset:0\n\t"
    "v_mfma_f32_16x16x32_bf16 v[232:235], a[196:199], a[72:75], v[232:235]\n\t" "ds_read_b64_tr_b16 a[186:187], %[vr21] offset:2048\n\t" "global_load_dwordx4 a[52:55], %[vx], s[40:41] offset:1024 sc1\n\t"
    "v_mfma_f32_16x16x32_bf16 v[248:251], a[192:195], a[68:71], v[248:251]\n\t" "ds_read_b64_tr_b16 a[188:189], %[vr13] offset:16384\n\t"
    "v_mfma_f32_16x16x32_bf16 v[248:251], a[196:199], a[76:79], v[248:251]\n\t" "ds_read_b64_tr_b16 a[190:191], %[vr13] offset:18432\n\t"
    "v_mfma_f32_16x16x32_bf16 v[236:239], a[200:203], a[64:67], v[236:239]\n\t" "ds_read_b64_tr_b16 a[192:193], %[vr22] offset:0\n\t"
    "v_mfma_f32_16x16x32_bf16 v[236:239], a[204:207], a[72:75], v[236:239]\n\t" "ds_read_b64_tr_b16 a[194:195], %[vr22] offset:2048\n\t" "global_load_dwordx4 a[56:59], %[vx], s[40:41] offset:2048 sc1\n\t"
    "v_mfma_f32_16x16x32_bf16 v[252:255], a[200:203], a[68:71], v[252:255]\n\t" "ds_read_b64_tr_b16 a[196:197], %[vr14] offset:16384\n\t"
    "v_mfma_f32_16x16x32_bf16 v[252:255], a[204:207], a[76:79], v[252:255]\n\t" "ds_read_b64_tr_b16 a[198:199], %[vr14] offset:18432\n\t" "s_waitcnt lgkmcnt(4)\n\t"
    "v_mfma_f32_16x16x32_bf16 v[160:163], a[176:179], a[64:67], v[160:163]\n\t" "ds_read_b64_tr_b16 a[200:201], %[vr23] offset:0\n\t"
    "v_mfma_f32_16x16x32_bf16 v[160:163], a[180:183], a[72:75], v[160:163]\n\t" "ds_read_b64_tr_b16 a[202:203], %[vr23] offset:2048\n\t" "global_load_dwordx4 a[60:63], %[vx], s[40:41] offset:3072 sc1\n\t"
    "v_mfma_f32_16x16x32_bf16 v[164:167], a[176:179], a[68:71], v[164:167]\n\t" "ds_read_b64_tr_b16 a[204:205], %[vr15] offset:16384\n\t"
    "v_mfma_f32_16x16x32_bf16 v[164:167], a[180:183], a[76:79], v[164:167]\n\t" "ds_read_b64_tr_b16 a[206:207], %[vr15] offset:18432\n\t" "s_add_u32 m0, %[sl0], 65536\n\t"
    "v_mfma_f32_16x16x32_bf16 v[168:171], a[184:187], a[64:67], v[168:171]\n\t" "s_barrier\n\t" "ds_read_b64_tr_b16 a[176:177], %[vr0] offset:24576\n\t"
    "v_mfma_f32_16x16x32_bf16 v[168:171], a[188:191], a[72:75], v[168:171]\n\t" "ds_read_b64_tr_b16 a[178:179], %[vr0] offset:26624\n\t" "global_load_lds_dwordx4 %[vb0], %[sb]\n\t"
    "v_mfma_f32_16x16x32_bf16 v[172:175], a[184:187], a[68:71], v[172:175]\n\t" "ds_read_b64_tr_b16 a[180:181], %[vr0] offset:32768\n\t" "v_add_u32 %[vb0], 0x10000, %[vb0]\n\t"
    "v_mfma_f32_16x16x32_bf16 v[172:175], a[188:191], a[76:79], v[172:175]\n\t" "ds_read_b64_tr_b16 a[182:183], %[vr0] offset:34816\n\t" "s_waitcnt lgkmcnt(4)\n\t"
    "v_mfma_f32_16x16x32_bf16 v[176:179], a[192:195], a[64:67], v[176:179]\n\t" "ds_read_b64_tr_b16 a[184:185], %[vr1] offset:24576\n\t" "s_add_u32 m0, %[sl1], 65536\n\t"
    "v_mfma_f32_16x16x32_bf16 v[176:179], a[196:199], a[72:75], v[176:179]\n\t" "ds_read_b64_tr_b16 a[186:187], %[vr1] offset:26624\n\t" "global_load_lds_dwordx4 %[vb1], %[sb]\n\t"
    "v_mfma_f32_16x16x32_bf16 v[180:183], a[192:195], a[68:71], v[180:183]\n\t" "ds_read_b64_tr_b16 a[188:189], %[vr1] offset:32768\n\t" "v_add_u32 %[vb1], 0x10000, %[vb1]\n\t"
    "v_mfma_f32_16x16x32_bf16 v[180:183], a[196:199], a[76:79], v[180:183]\n\t" "ds_read_b64_tr_b16 a[190:191], %[vr1] offset:34816\n\t" "s_add_u32 m0, %[sl0], 73728\n\t"
    "v_mfma_f32_16x16x32_bf16 v[184:187], a[200:203], a[64:67], v[184:187]\n\t" "ds_read_b64_tr_b16 a[192:193], %[vr2] offset:24576\n\t"
    "v_mfma_f32_16x16x32_bf16 v[184:187], a[204:207], a[72:75], v[184:187]\n\t" "ds_read_b64_tr_b16 a[194:195], %[vr2] offset:26624\n\t" "global_load_lds_dwordx4 %[vb0], %[sb]\n\t"
    "v_mfma_f32_16x16x32_bf16 v[188:191], a[200:203], a[68:71], v[188:191]\n\t" "ds_read_b64_tr_b16 a[196:197], %[vr2] offset:32768\n\t" "v_add_u32 %[vb0], 0x10000, %[vb0]\n\t"
    "v_mfma_f32_16x16x32_bf16 v[188:191], a[204:207], a[76:79], v[188:191]\n\t" "ds_read_b64_tr_b16 a[198:199], %[vr2] offset:34816\n\t" "s_waitcnt lgkmcnt(4) vmcnt(30)\n\t"
    "v_mfma_f32_16x16x32_bf16 v[224:227], a[176:179], a[0:3], v[224:227]\n\t" "ds_read_b64_tr_b16 a[200:201], %[vr3] offset:24576\n\t" "s_add_u32 m0, %[sl1], 73728\n\t"
    "v_mfma_f32_16x16x32_bf16 v[224:227], a[180:183], a[8:11], v[224:227]\n\t" "ds_read_b64_tr_b16 a[202:203], %[vr3] offset:26624\n\t" "global_load_lds_dwordx4 %[vb1], %[sb]\n\t"
    "v_mfma_f32_16x16x32_bf16 v[240:243], a[176:179], a[4:7], v[240:243]\n\t" "ds_read_b64_tr_b16 a[204:205], %[vr3] offset:32768\n\t" "v_add_u32 %[vb1], 0x10000, %[vb1]\n\t"
    "v_mfma_f32_16x16x32_bf16 v[240:243], a[180:183], a[12:15], v[240:243]\n\t" "ds_read_b64_tr_b16 a[206:207], %[vr3] offset:34816\n\t" "s_add_u32 s40, s40, 8192\n\t" "s_addc_u32 s41, s41, 0\n\t"
    "v_mfma_f32_16x16x32_bf16 v[228:231], a[184:187], a[0:3], v[228:231]\n\t" "ds_read_b64_tr_b16 a[176:177], %[vr12] offset:24576\n\t"
    "v_mfma_f32_16x16x32_bf16 v[228:231], a[188:191], a[8:11], v[228:231]\n\t" "ds_read_b64_tr_b16 a[178:179], %[vr12] offset:26624\n\t"
    "global_load_dwordx4 a[64:67], %[vx], s[40:41] offset:-4096 sc1\n\t" "v_mfma_f32_16x16x32_bf16 v[244:247], a[184:187], a[4:7], v[244:247]\n\t" "ds_read_b64_tr_b16 a[180:181], %[vr12] offset:32768\n\t"
    "v_mfma_f32_16x16x32_bf16 v[244:247], a[188:191], a[12:15], v[244:247]\n\t" "ds_read_b64_tr_b16 a[182:183], %[vr12] offset:34816\n\t" "s_waitcnt lgkmcnt(4)\n\t"
    "v_mfma_f32_16x16x32_bf16 v[232:235], a[192:195], a[0:3], v[232:235]\n\t" "ds_read_b64_tr_b16 a[184:185], %[vr13] offset:24576\n\t"
    "v_mfma_f32_16x16x32_bf16 v[232:235], a[196:199], a[8:11], v[232:235]\n\t" "ds_read_b64_tr_b16 a[186:187], %[vr13] offset:26624\n\t"
    "global_load_dwordx4 a[68:71], %[vx], s[40:41] offset:-3072 sc1\n\t" "v_mfma_f32_16x16x32_bf16 v[248:251], a[192:195], a[4:7], v[248:251]\n\t" "ds_read_b64_tr_b16 a[188:189], %[vr13] offset:32768\n\t"
    "v_mfma_f32_16x16x32_bf16 v[248:251], a[196:199], a[12:15], v[248:251]\n\t" "ds_read_b64_tr_b16 a[190:191], %[vr13] offset:34816\n\t"
    "v_mfma_f32_16x16x32_bf16 v[236:239], a[200:203], a[0:3], v[236:239]\n\t" "ds_read_b64_tr_b16 a[192:193], %[vr14] offset:24576\n\t"
    "v_mfma_f32_16x16x32_bf16 v[236:239], a[204:207], a[8:11], v[236:239]\n\t" "ds_read_b64_tr_b16 a[194:195], %[vr14] offset:26624\n\t"
    "global_load_dwordx4 a[72:75], %[vx], s[40:41] offset:-2048 sc1\n\t" "v_mfma_f32_16x16x32_bf16 v[252:255], a[200:203], a[4:7], v[252:255]\n\t" "ds_read_b64_tr_b16 a[196:197], %[vr14] offset:32768\n\t"
    "v_mfma_f32_16x16x32_bf16 v[252:255], a[204:207], a[12:15], v[252:255]\n\t" "ds_read_b64_tr_b16 a[198:199], %[vr14] offset:34816\n\t" "s_waitcnt lgkmcnt(4)\n\t"
    "v_mfma_f32_16x16x32_bf16 v[160:163], a[176:179], a[0:3], v[160:163]\n\t" "ds_read_b64_tr_b16 a[200:201], %[vr15] offset:24576\n\t"
    "v_mfma_f32_16x16x32_bf16 v[160:163], a[180:183], a[8:11], v[160:163]\n\t" "ds_read_b64_tr_b16 a[202:203], %[vr15] offset:26624\n\t"
    "global_load_dwordx4 a[76:79], %[vx], s[40:41] offset:-1024 sc1\n\t" "v_mfma_f32_16x16x32_bf16 v[164:167], a[176:179], a[4:7], v[164:167]\n\t" "ds_read_b64_tr_b16 a[204:205], %[vr15] offset:32768\n\t"
    "v_mfma_f32_16x16x32_bf16 v[164:167], a[180:183], a[12:15], v[164:167]\n\t" "ds_read_b64_tr_b16 a[206:207], %[vr15] offset:34816\n\t" "s_add_u32 m0, %[sl0], 81920\n\t"
    "v_mfma_f32_16x16x32_bf16 v[168:171], a[184:187], a[0:3], v[168:171]\n\t" "s_barrier\n\t" "ds_read_b64_tr_b16 a[176:177], %[vr0] offset:40960\n\t"
    "v_mfma_f32_16x16x32_bf16 v[168:171], a[188:191], a[8:11], v[168:171]\n\t" "ds_read_b64_tr_b16 a[178:179], %[vr0] offset:43008\n\t" "global_load_lds_dwordx4 %[vb0], %[sb]\n\t"
    "v_mfma_f32_16x16x32_bf16 v[172:175], a[184:187], a[4:7], v[172:175]\n\t" "ds_read_b64_tr_b16 a[180:181], %[vr8] offset:8192\n\t" "v_add_u32 %[vb0], 0x10000, %[vb0]\n\t"
    "v_mfma_f32_16x16x32_bf16 v[172:175], a[188:191], a[12:15], v[172:175]\n\t" "ds_read_b64_tr_b16 a[182:183], %[vr8] offset:10240\n\t" "s_waitcnt lgkmcnt(4)\n\t"
    "v_mfma_f32_16x16x32_bf16 v[176:179], a[192:195], a[0:3], v[176:179]\n\t" "ds_read_b64_tr_b16 a[184:185], %[vr1] offset:40960\n\t" "s_add_u32 m0, %[sl1], 81920\n\t"
    "v_mfma_f32_16x16x32_bf16 v[176:179], a[196:199], a[8:11], v[176:179]\n\t" "ds_read_b64_tr_b16 a[186:187], %[vr1] offset:43008\n\t" "global_load_lds_dwordx4 %[vb1], %[sb]\n\t"
    "v_mfma_f32_16x16x32_bf16 v[180:183], a[192:195], a[4:7], v[180:183]\n\t" "ds_read_b64_tr_b16 a[188:189], %[vr9] offset:8192\n\t" "v_add_u32 %[vb1], 0x10000, %[vb1]\n\t"
    "v_mfma_f32_16x16x32_bf16 v[180:183], a[196:199], a[12:15], v[180:183]\n\t" "ds_read_b64_tr_b16 a[190:191], %[vr9] offset:10240\n\t" "s_add_u32 m0, %[sl0], 90112\n\t"
    "v_mfma_f32_16x16x32_bf16 v[184:187], a[200:203], a[0:3], v[184:187]\n\t" "ds_read_b64_tr_b16 a[192:193], %[vr2] offset:40960\n\t"
    "v_mfma_f32_16x16x32_bf16 v[184:187], a[204:207], a[8:11], v[184:187]\n\t" "ds_read_b64_tr_b16 a[194:195], %[vr2] offset:43008\n\t" "global_load_lds_dwordx4 %[vb0], %[sb]\n\t"
    "v_mfma_f32_16x16x32_bf16 v[188:191], a[200:203], a[4:7], v[188:191]\n\t" "ds_read_b64_tr_b16 a[196:197], %[vr10] offset:8192\n\t" "v_add_u32 %[vb0], 0x10000, %[vb0]\n\t"
    "v_mfma_f32_16x16x32_bf16 v[188:191], a[204:207], a[12:15], v[188:191]\n\t" "ds_read_b64_tr_b16 a[198:199], %[vr10] offset:10240\n\t" "s_waitcnt lgkmcnt(4) vmcnt(29)\n\t"
    "v_mfma_f32_16x16x32_bf16 v[224:227], a[176:179], a[16:19], v[224:227]\n\t" "ds_read_b64_tr_b16 a[200:201], %[vr3] offset:40960\n\t" "s_add_u32 m0, %[sl1], 90112\n\t"
    "v_mfma_f32_16x16x32_bf16 v[224:227], a[180:183], a[24:27], v[224:227]\n\t" "ds_read_b64_tr_b16 a[202:203], %[vr3] offset:43008\n\t" "global_load_lds_dwordx4 %[vb1], %[sb]\n\t"
    "v_mfma_f32_16x16x32_bf16 v[240:243], a[176:179], a[20:23], v[240:243]\n\t" "ds_read_b64_tr_b16 a[204:205], %[vr11] offset:8192\n\t" "v_add_u32 %[vb1], 0x10000, %[vb1]\n\t"
    "v_mfma_f32_16x16x32_bf16 v[240:243], a[180:183], a[28:31], v[240:243]\n\t" "ds_read_b64_tr_b16 a[206:207], %[vr11] offset:10240\n\t"
    "v_mfma_f32_16x16x32_bf16 v[228:231], a[184:187], a[16:19], v[228:231]\n\t" "ds_read_b64_tr_b16 a[176:177], %[vr12] offset:40960\n\t"
    "v_mfma_f32_16x16x32_bf16 v[228:231], a[188:191], a[24:27], v[228:231]\n\t" "ds_read_b64_tr_b16 a[178:179], %[vr12] offset:43008\n\t" "global_load_dwordx4 a[0:3], %[vx], s[40:41] offset:0 sc1\n\t"
    "v_mfma_f32_16x16x32_bf16 v[244:247], a[184:187], a[20:23], v[244:247]\n\t" "ds_read_b64_tr_b16 a[180:181], %[vr20] offset:8192\n\t"
    "v_mfma_f32_16x16x32_bf16 v[244:247], a[188:191], a[28:31], v[244:247]\n\t" "ds_read_b64_tr_b16 a[182:183], %[vr20] offset:10240\n\t" "s_waitcnt lgkmcnt(4)\n\t"
    "v_mfma_f32_16x16x32_bf16 v[232:235], a[192:195], a[16:19], v[232:235]\n\t" "ds_read_b64_tr_b16 a[184:185], %[vr13] offset:40960\n\t"
    "v_mfma_f32_16x16x32_bf16 v[232:235], a[196:199], a[24:27], v[232:235]\n\t" "ds_read_b64_tr_b16 a[186:187], %[vr13] offset:43008\n\t" "global_load_dwordx4 a[4:7], %[vx], s[40:41] offset:1024 sc1\n\t"
    "v_mfma_f32_16x16x32_bf16 v[248:251], a[192:195], a[20:23], v[248:251]\n\t" "ds_read_b64_tr_b16 a[188:189], %[vr21] offset:8192\n\t"
    "v_mfma_f32_16x16x32_bf16 v[248:251], a[196:199], a[28:31], v[248:251]\n\t" "ds_read_b64_tr_b16 a[190:191], %[vr21] offset:10240\n\t"
    "v_mfma_f32_16x16x32_bf16 v[236:239], a[200:203], a[16:19], v[236:239]\n\t" "ds_read_b64_tr_b16 a[192:193], %[vr14] offset:40960\n\t"
    "v_mfma_f32_16x16x32_bf16 v[236:239], a[204:207], a[24:27], v[236:239]\n\t" "ds_read_b64_tr_b16 a[194:195], %[vr14] offset:43008\n\t" "global_load_dwordx2 a[8:9], %[vo], s[40:41] offset:2048 sc1\n\t"
    "v_mfma_f32_16x16x32_bf16 v[252:255], a[200:203], a[20:23], v[252:255]\n\t" "ds_read_b64_tr_b16 a[196:197], %[vr22] offset:8192\n\t"
    "v_mfma_f32_16x16x32_bf16 v[252:255], a[204:207], a[28:31], v[252:255]\n\t" "ds_read_b64_tr_b16 a[198:199], %[vr22] offset:10240\n\t" "s_waitcnt lgkmcnt(4)\n\t"
    "v_mfma_f32_16x16x32_bf16 v[160:163], a[176:179], a[16:19], v[160:163]\n\t" "ds_read_b64_tr_b16 a[200:201], %[vr15] offset:40960\n\t"
    "global_load_dwordx2 a[10:11], %[vo], s[40:41] offset:3072 sc1\n\t" "v_mfma_f32_16x16x32_bf16 v[160:163], a[180:183], a[24:27], v[160:163]\n\t"
    "ds_read_b64_tr_b16 a[202:203], %[vr15] offset:43008\n\t" "global_load_dwordx2 a[12:13], %[vo], s[40:41] offset:2560 sc1\n\t"
    "v_mfma_f32_16x16x32_bf16 v[164:167], a[176:179], a[20:23], v[164:167]\n\t" "ds_read_b64_tr_b16 a[204:205], %[vr23] offset:8192\n\t"
    "v_mfma_f32_16x16x32_bf16 v[164:167], a[180:183], a[28:31], v[164:167]\n\t" "ds_read_b64_tr_b16 a[206:207], %[vr23] offset:10240\n\t"
    "global_load_dwordx2 a[14:15], %[vo], s[40:41] offset:3584 sc1\n\t" "v_mfma_f32_16x16x32_bf16 v[168:171], a[184:187], a[16:19], v[168:171]\n\t" "s_barrier\n\t"
    "ds_read_b64_tr_b16 a[176:177], %[vr8] offset:16384\n\t" "v_mfma_f32_16x16x32_bf16 v[168:171], a[188:191], a[24:27], v[168:171]\n\t" "ds_read_b64_tr_b16 a[178:179], %[vr8] offset:18432\n\t"
    "s_add_u32 m0, %[sl0], 98304\n\t" "v_mfma_f32_16x16x32_bf16 v[172:175], a[184:187], a[20:23], v[172:175]\n\t" "ds_read_b64_tr_b16 a[180:181], %[vr8] offset:24576\n\t"
    "global_load_lds_dwordx4 %[vb0], %[sb]\n\t" "v_mfma_f32_16x16x32_bf16 v[172:175], a[188:191], a[28:31], v[172:175]\n\t" "ds_read_b64_tr_b16 a[182:183], %[vr8] offset:26624\n\t"
    "s_waitcnt lgkmcnt(4)\n\t" "v_mfma_f32_16x16x32_bf16 v[176:179], a[192:195], a[16:19], v[176:179]\n\t" "ds_read_b64_tr_b16 a[184:185], %[vr9] offset:16384\n\t" "v_add_u32 %[vb0], 0x10000, %[vb0]\n\t"
    "v_mfma_f32_16x16x32_bf16 v[176:179], a[196:199], a[24:27], v[176:179]\n\t" "ds_read_b64_tr_b16 a[186:187], %[vr9] offset:18432\n\t" "s_add_u32 m0, %[sl1], 98304\n\t"
    "v_mfma_f32_16x16x32_bf16 v[180:183], a[192:195], a[20:23], v[180:183]\n\t" "ds_read_b64_tr_b16 a[188:189], %[vr9] offset:24576\n\t" "global_load_lds_dwordx4 %[vb1], %[sb]\n\t"
    "v_mfma_f32_16x16x32_bf16 v[180:183], a[196:199], a[28:31], v[180:183]\n\t" "ds_read_b64_tr_b16 a[190:191], %[vr9] offset:26624\n\t" "v_add_u32 %[vb1], 0x10000, %[vb1]\n\t"
    "v_mfma_f32_16x16x32_bf16 v[184:187], a[200:203], a[16:19], v[184:187]\n\t" "ds_read_b64_tr_b16 a[192:193], %[vr10] offset:16384\n\t" "s_add_u32 m0, %[sl0], 106496\n\t"
    "v_mfma_f32_16x16x32_bf16 v[184:187], a[204:207], a[24:27], v[184:187]\n\t" "ds_read_b64_tr_b16 a[194:195], %[vr10] offset:18432\n\t" "global_load_lds_dwordx4 %[vb0], %[sb]\n\t"
    "v_mfma_f32_16x16x32_bf16 v[188:191], a[200:203], a[20:23], v[188:191]\n\t" "ds_read_b64_tr_b16 a[196:197], %[vr10] offset:24576\n\t" "v_add_u32 %[vb0], 0x10000, %[vb0]\n\t"
    "v_mfma_f32_16x16x32_bf16 v[188:191], a[204:207], a[28:31], v[188:191]\n\t" "ds_read_b64_tr_b16 a[198:199], %[vr10] offset:26624\n\t" "s_waitcnt lgkmcnt(4) vmcnt(29)\n\t"
    "v_mfma_f32_16x16x32_bf16 v[224:227], a[176:179], a[32:35], v[224:227]\n\t" "ds_read_b64_tr_b16 a[200:201], %[vr11] offset:16384\n\t" "s_add_u32 m0, %[sl1], 106496\n\t"
    "v_mfma_f32_16x16x32_bf16 v[224:227], a[180:183], a[40:43], v[224:227]\n\t" "ds_read_b64_tr_b16 a[202:203], %[vr11] offset:18432\n\t" "global_load_lds_dwordx4 %[vb1], %[sb]\n\t"
    "v_mfma_f32_16x16x32_bf16 v[240:243], a[176:179], a[36:39], v[240:243]\n\t" "ds_read_b64_tr_b16 a[204:205], %[vr11] offset:24576\n\t" "v_add_u32 %[vb1], 0x10000, %[vb1]\n\t"
    "v_mfma_f32_16x16x32_bf16 v[240:243], a[180:183], a[44:47], v[240:243]\n\t" "ds_read_b64_tr_b16 a[206:207], %[vr11] offset:26624\n\t" "s_add_u32 s40, s40, 8192\n\t" "s_addc_u32 s41, s41, 0\n\t"
    "v_mfma_f32_16x16x32_bf16 v[228:231], a[184:187], a[32:35], v[228:231]\n\t" "ds_read_b64_tr_b16 a[176:177], %[vr20] offset:16384\n\t"
    "v_mfma_f32_16x16x32_bf16 v[228:231], a[188:191], a[40:43], v[228:231]\n\t" "ds_read_b64_tr_b16 a[178:179], %[vr20] offset:18432\n\t"
    "global_load_dwordx4 a[16:19], %[vx], s[40:41] offset:-4096 sc1\n\t" "v_mfma_f32_16x16x32_bf16 v[244:247], a[184:187], a[36:39], v[244:247]\n\t"
    "ds_read_b64_tr_b16 a[180:181], %[vr20] offset:24576\n\t" "v_mfma_f32_16x16x32_bf16 v[244:247], a[188:191], a[44:47], v[244:247]\n\t" "ds_read_b64_tr_b16 a[182:183], %[vr20] offset:26624\n\t"
    "s_waitcnt lgkmcnt(4)\n\t" "v_mfma_f32_16x16x32_bf16 v[232:235], a[192:195], a[32:35], v[232:235]\n\t" "ds_read_b64_tr_b16 a[184:185], %[vr21] offset:16384\n\t"
    "v_mfma_f32_16x16x32_bf16 v[232:235], a[196:199], a[40:43], v[232:235]\n\t" "ds_read_b64_tr_b16 a[186:187], %[vr21] offset:18432\n\t"
    "global_load_dwordx4 a[20:23], %[vx], s[40:41] offset:-3072 sc1\n\t" "v_mfma_f32_16x16x32_bf16 v[248:251], a[192:195], a[36:39], v[248:251]\n\t"
    "ds_read_b64_tr_b16 a[188:189], %[vr21] offset:24576\n\t" "v_mfma_f32_16x16x32_bf16 v[248:251], a[196:199], a[44:47], v[248:251]\n\t" "ds_read_b64_tr_b16 a[190:191], %[vr21] offset:26624\n\t"
    "v_mfma_f32_16x16x32_bf16 v[236:239], a[200:203], a[32:35], v[236:239]\n\t" "ds_read_b64_tr_b16 a[192:193], %[vr22] offset:16384\n\t"
    "v_mfma_f32_16x16x32_bf16 v[236:239], a[204:207], a[40:43], v[236:239]\n\t" "ds_read_b64_tr_b16 a[194:195], %[vr22] offset:18432\n\t"
    "global_load_dwordx4 a[24:27], %[vx], s[40:41] offset:-2048 sc1\n\t" "v_mfma_f32_16x16x32_bf16 v[252:255], a[200:203], a[36:39], v[252:255]\n\t"
    "ds_read_b64_tr_b16 a[196:197], %[vr22] offset:24576\n\t" "v_mfma_f32_16x16x32_bf16 v[252:255], a[204:207], a[44:47], v[252:255]\n\t" "ds_read_b64_tr_b16 a[198:199], %[vr22] offset:26624\n\t"
    "s_waitcnt lgkmcnt(4)\n\t" "v_mfma_f32_16x16x32_bf16 v[160:163], a[176:179], a[32:35], v[160:163]\n\t" "ds_read_b64_tr_b16 a[200:201], %[vr23] offset:16384\n\t"
    "v_mfma_f32_16x16x32_bf16 v[160:163], a[180:183], a[40:43], v[160:163]\n\t" "ds_read_b64_tr_b16 a[202:203], %[vr23] offset:18432\n\t"
    "global_load_dwordx4 a[28:31], %[vx], s[40:41] offset:-1024 sc1\n\t" "v_mfma_f32_16x16x32_bf16 v[164:167], a[176:179], a[36:39], v[164:167]\n\t"
    "ds_read_b64_tr_b16 a[204:205], %[vr23] offset:24576\n\t" "v_mfma_f32_16x16x32_bf16 v[164:167], a[180:183], a[44:47], v[164:167]\n\t" "ds_read_b64_tr_b16 a[206:207], %[vr23] offset:26624\n\t"
    "s_add_u32 m0, %[sl0], 114688\n\t" "v_mfma_f32_16x16x32_bf16 v[168:171], a[184:187], a[32:35], v[168:171]\n\t" "s_barrier\n\t" "ds_read_b64_tr_b16 a[176:177], %[vr0] offset:0\n\t"
    "v_mfma_f32_16x16x32_bf16 v[168:171], a[188:191], a[40:43], v[168:171]\n\t" "ds_read_b64_tr_b16 a[178:179], %[vr0] offset:2048\n\t" "global_load_lds_dwordx4 %[vb0], %[sb]\n\t"
    "v_mfma_f32_16x16x32_bf16 v[172:175], a[184:187], a[36:39], v[172:175]\n\t" "ds_read_b64_tr_b16 a[180:181], %[vr0] offset:8192\n\t" "v_add_u32 %[vb0], 0x10000, %[vb0]\n\t"
    "v_mfma_f32_16x16x32_bf16 v[172:175], a[188:191], a[44:47], v[172:175]\n\t" "ds_read_b64_tr_b16 a[182:183], %[vr0] offset:10240\n\t" "s_waitcnt lgkmcnt(4)\n\t"
    "v_mfma_f32_16x16x32_bf16 v[176:179], a[192:195], a[32:35], v[176:179]\n\t" "ds_read_b64_tr_b16 a[184:185], %[vr1] offset:0\n\t" "s_add_u32 m0, %[sl1], 114688\n\t"
    "v_mfma_f32_16x16x32_bf16 v[176:179], a[196:199], a[40:43], v[176:179]\n\t" "ds_read_b64_tr_b16 a[186:187], %[vr1] offset:2048\n\t" "global_load_lds_dwordx4 %[vb1], %[sb]\n\t"
    "v_mfma_f32_16x16x32_bf16 v[180:183], a[192:195], a[36:39], v[180:183]\n\t" "ds_read_b64_tr_b16 a[188:189], %[vr1] offset:8192\n\t" "v_add_u32 %[vb1], 0x10000, %[vb1]\n\t"
    "v_mfma_f32_16x16x32_bf16 v[180:183], a[196:199], a[44:47], v[180:183]\n\t" "ds_read_b64_tr_b16 a[190:191], %[vr1] offset:10240\n\t" "s_add_u32 m0, %[sl0], 122880\n\t"
    "v_mfma_f32_16x16x32_bf16 v[184:187], a[200:203], a[32:35], v[184:187]\n\t" "ds_read_b64_tr_b16 a[192:193], %[vr2] offset:0\n\t"
    "v_mfma_f32_16x16x32_bf16 v[184:187], a[204:207], a[40:43], v[184:187]\n\t" "ds_read_b64_tr_b16 a[194:195], %[vr2] offset:2048\n\t" "global_load_lds_dwordx4 %[vb0], %[sb]\n\t"
    "v_mfma_f32_16x16x32_bf16 v[188:191], a[200:203], a[36:39], v[188:191]\n\t" "ds_read_b64_tr_b16 a[196:197], %[vr2] offset:8192\n\t" "v_add_u32 %[vb0], 0x10000, %[vb0]\n\t"
    "v_mfma_f32_16x16x32_bf16 v[188:191], a[204:207], a[44:47], v[188:191]\n\t" "ds_read_b64_tr_b16 a[198:199], %[vr2] offset:10240\n\t" "s_waitcnt lgkmcnt(4) vmcnt(29)\n\t"
    "v_mfma_f32_16x16x32_bf16 v[224:227], a[176:179], a[48:51], v[224:227]\n\t" "ds_read_b64_tr_b16 a[200:201], %[vr3] offset:0\n\t" "s_add_u32 m0, %[sl1], 122880\n\t"
    "v_mfma_f32_16x16x32_bf16 v[224:227], a[180:183], a[56:59], v[224:227]\n\t" "ds_read_b64_tr_b16 a[202:203], %[vr3] offset:2048\n\t" "global_load_lds_dwordx4 %[vb1], %[sb]\n\t"
    "v_mfma_f32_16x16x32_bf16 v[240:243], a[176:179], a[52:55], v[240:243]\n\t" "ds_read_b64_tr_b16 a[204:205], %[vr3] offset:8192\n\t" "v_add_u32 %[vb1], 0x10000, %[vb1]\n\t"
    "v_mfma_f32_16x16x32_bf16 v[240:243], a[180:183], a[60:63], v[240:243]\n\t" "ds_read_b64_tr_b16 a[206:207], %[vr3] offset:10240\n\t"
    "v_mfma_f32_16x16x32_bf16 v[228:231], a[184:187], a[48:51], v[228:231]\n\t" "ds_read_b64_tr_b16 a[176:177], %[vr12] offset:0\n\t"
    "v_mfma_f32_16x16x32_bf16 v[228:231], a[188:191], a[56:59], v[228:231]\n\t" "ds_read_b64_tr_b16 a[178:179], %[vr12] offset:2048\n\t" "global_load_dwordx4 v[112:115], %[vg], %[sg] offset:0 sc1\n\t"
    "v_mfma_f32_16x16x32_bf16 v[244:247], a[184:187], a[52:55], v[244:247]\n\t" "ds_read_b64_tr_b16 a[180:181], %[vr12] offset:8192\n\t"
    "v_mfma_f32_16x16x32_bf16 v[244:247], a[188:191], a[60:63], v[244:247]\n\t" "ds_read_b64_tr_b16 a[182:183], %[vr12] offset:10240\n\t" "s_waitcnt lgkmcnt(4)\n\t"
    "v_mfma_f32_16x16x32_bf16 v[232:235], a[192:195], a[48:51], v[232:235]\n\t" "ds_read_b64_tr_b16 a[184:185], %[vr13] offset:0\n\t" "global_load_dwordx4 a[32:35], %[vx], s[40:41] offset:0 sc1\n\t"
    "v_mfma_f32_16x16x32_bf16 v[232:235], a[196:199], a[56:59], v[232:235]\n\t" "ds_read_b64_tr_b16 a[186:187], %[vr13] offset:2048\n\t"
    "v_mfma_f32_16x16x32_bf16 v[248:251], a[192:195], a[52:55], v[248:251]\n\t" "ds_read_b64_tr_b16 a[188:189], %[vr13] offset:8192\n\t"
    "v_mfma_f32_16x16x32_bf16 v[248:251], a[196:199], a[60:63], v[248:251]\n\t" "ds_read_b64_tr_b16 a[190:191], %[vr13] offset:10240\n\t"
    "global_load_dwordx4 a[36:39], %[vx], s[40:41] offset:1024 sc1\n\t" "v_mfma_f32_16x16x32_bf16 v[236:239], a[200:203], a[48:51], v[236:239]\n\t" "ds_read_b64_tr_b16 a[192:193], %[vr14] offset:0\n\t"
    "v_mfma_f32_16x16x32_bf16 v[236:239], a[204:207], a[56:59], v[236:239]\n\t" "ds_read_b64_tr_b16 a[194:195], %[vr14] offset:2048\n\t"
    "v_mfma_f32_16x16x32_bf16 v[252:255], a[200:203], a[52:55], v[252:255]\n\t" "ds_read_b64_tr_b16 a[196:197], %[vr14] offset:8192\n\t"
    "v_mfma_f32_16x16x32_bf16 v[252:255], a[204:207], a[60:63], v[252:255]\n\t" "ds_read_b64_tr_b16 a[198:199], %[vr14] offset:10240\n\t" "s_waitcnt lgkmcnt(4)\n\t"
    "v_mfma_f32_16x16x32_bf16 v[160:163], a[176:179], a[48:51], v[160:163]\n\t" "ds_read_b64_tr_b16 a[200:201], %[vr15] offset:0\n\t" "global_load_dwordx4 a[40:43], %[vx], s[40:41] offset:2048 sc1\n\t"
    "v_mfma_f32_16x16x32_bf16 v[160:163], a[180:183], a[56:59], v[160:163]\n\t" "ds_read_b64_tr_b16 a[202:203], %[vr15] offset:2048\n\t"
    "v_mfma_f32_16x16x32_bf16 v[164:167], a[176:179], a[52:55], v[164:167]\n\t" "ds_read_b64_tr_b16 a[204:205], %[vr15] offset:8192\n\t"
    "v_mfma_f32_16x16x32_bf16 v[164:167], a[180:183], a[60:63], v[164:167]\n\t" "ds_read_b64_tr_b16 a[206:207], %[vr15] offset:10240\n\t" "global_load_dwordx4 v[116:119], %[vg], %[sg] offset:64 sc1\n\t"
    "v_mfma_f32_16x16x32_bf16 v[168:171], a[184:187], a[48:51], v[168:171]\n\t" "s_barrier\n\t" "ds_read_b64_tr_b16 a[176:177], %[vr0] offset:49152\n\t"
    "v_mfma_f32_16x16x32_bf16 v[168:171], a[188:191], a[56:59], v[168:171]\n\t" "ds_read_b64_tr_b16 a[178:179], %[vr0] offset:51200\n\t" "global_load_dwordx4 a[44:47], %[vx], s[40:41] offset:3072 sc1\n\t"
    "v_mfma_f32_16x16x32_bf16 v[172:175], a[184:187], a[52:55], v[172:175]\n\t" "ds_read_b64_tr_b16 a[180:181], %[vr0] offset:57344\n\t"
    "v_mfma_f32_16x16x32_bf16 v[172:175], a[188:191], a[60:63], v[172:175]\n\t" "ds_read_b64_tr_b16 a[182:183], %[vr0] offset:59392\n\t" "s_waitcnt lgkmcnt(4)\n\t"
    "v_mfma_f32_16x16x32_bf16 v[176:179], a[192:195], a[48:51], v[176:179]\n\t" "ds_read_b64_tr_b16 a[184:185], %[vr1] offset:49152\n\t" "s_add_u32 m0, %[sl0], 131072\n\t"
    "v_mfma_f32_16x16x32_bf16 v[176:179], a[196:199], a[56:59], v[176:179]\n\t" "ds_read_b64_tr_b16 a[186:187], %[vr1] offset:51200\n\t" "global_load_lds_dwordx4 %[vb0], %[sb]\n\t"
    "v_mfma_f32_16x16x32_bf16 v[180:183], a[192:195], a[52:55], v[180:183]\n\t" "ds_read_b64_tr_b16 a[188:189], %[vr1] offset:57344\n\t" "v_add_u32 %[vb0], 0x10000, %[vb0]\n\t"
    "v_mfma_f32_16x16x32_bf16 v[180:183], a[196:199], a[60:63], v[180:183]\n\t" "ds_read_b64_tr_b16 a[190:191], %[vr1] offset:59392\n\t" "s_add_u32 m0, %[sl1], 131072\n\t"
    "v_mfma_f32_16x16x32_bf16 v[184:187], a[200:203], a[48:51], v[184:187]\n\t" "ds_read_b64_tr_b16 a[192:193], %[vr2] offset:49152\n\t"
    "v_mfma_f32_16x16x32_bf16 v[184:187], a[204:207], a[56:59], v[184:187]\n\t" "ds_read_b64_tr_b16 a[194:195], %[vr2] offset:51200\n\t" "global_load_lds_dwordx4 %[vb1], %[sb]\n\t"
    "v_mfma_f32_16x16x32_bf16 v[188:191], a[200:203], a[52:55], v[188:191]\n\t" "ds_read_b64_tr_b16 a[196:197], %[vr2] offset:57344\n\t" "v_add_u32 %[vb1], 0x10000, %[vb1]\n\t"
    "v_mfma_f32_16x16x32_bf16 v[188:191], a[204:207], a[60:63], v[188:191]\n\t" "ds_read_b64_tr_b16 a[198:199], %[vr2] offset:59392\n\t" "s_waitcnt lgkmcnt(4) vmcnt(30)\n\t"
    "v_mfma_f32_16x16x32_bf16 v[224:227], a[176:179], a[64:67], v[224:227]\n\t" "ds_read_b64_tr_b16 a[200:201], %[vr3] offset:49152\n\t" "s_add_u32 m0, %[sl0], 16384\n\t"
    "v_mfma_f32_16x16x32_bf16 v[224:227], a[180:183], a[72:75], v[224:227]\n\t" "ds_read_b64_tr_b16 a[202:203], %[vr3] offset:51200\n\t" "global_load_lds_dwordx4 %[vb0], %[sb]\n\t"
    "v_mfma_f32_16x16x32_bf16 v[240:243], a[176:179], a[68:71], v[240:243]\n\t" "ds_read_b64_tr_b16 a[204:205], %[vr3] offset:57344\n\t" "v_add_u32 %[vb0], 0x10000, %[vb0]\n\t"
    "v_mfma_f32_16x16x32_bf16 v[240:243], a[180:183], a[76:79], v[240:243]\n\t" "ds_read_b64_tr_b16 a[206:207], %[vr3] offset:59392\n\t"
    "v_mfma_f32_16x16x32_bf16 v[228:231], a[184:187], a[64:67], v[228:231]\n\t" "ds_read_b64_tr_b16 a[176:177], %[vr12] offset:49152\n\t"
    "v_mfma_f32_16x16x32_bf16 v[228:231], a[188:191], a[72:75], v[228:231]\n\t" "ds_read_b64_tr_b16 a[178:179], %[vr12] offset:51200\n\t" "global_load_dwordx4 v[120:123], %[vg], %[sg] offset:128 sc1\n\t"
    "v_mfma_f32_16x16x32_bf16 v[244:247], a[184:187], a[68:71], v[244:247]\n\t" "ds_read_b64_tr_b16 a[180:181], %[vr12] offset:57344\n\t"
    "v_mfma_f32_16x16x32_bf16 v[244:247], a[188:191], a[76:79], v[244:247]\n\t" "ds_read_b64_tr_b16 a[182:183], %[vr12] offset:59392\n\t" "s_waitcnt lgkmcnt(4)\n\t"
    "v_mfma_f32_16x16x32_bf16 v[232:235], a[192:195], a[64:67], v[232:235]\n\t" "ds_read_b64_tr_b16 a[184:185], %[vr13] offset:49152\n\t" "global_load_dword a255, %[vx], %[cd]\n\t"
    "v_mfma_f32_16x16x32_bf16 v[232:235], a[196:199], a[72:75], v[232:235]\n\t" "ds_read_b64_tr_b16 a[186:187], %[vr13] offset:51200\n\t" "s_add_u32 s40, s40, 8192\n\t" "s_addc_u32 s41, s41, 0\n\t"
    "v_mfma_f32_16x16x32_bf16 v[248:251], a[192:195], a[68:71], v[248:251]\n\t" "ds_read_b64_tr_b16 a[188:189], %[vr13] offset:57344\n\t"
    "global_load_dwordx4 a[48:51], %[vx], s[40:41] offset:-4096 sc1\n\t" "v_mfma_f32_16x16x32_bf16 v[248:251], a[196:199], a[76:79], v[248:251]\n\t"
    "ds_read_b64_tr_b16 a[190:191], %[vr13] offset:59392\n\t" "v_mfma_f32_16x16x32_bf16 v[236:239], a[200:203], a[64:67], v[236:239]\n\t" "ds_read_b64_tr_b16 a[192:193], %[vr14] offset:49152\n\t"
    "v_mfma_f32_16x16x32_bf16 v[236:239], a[204:207], a[72:75], v[236:239]\n\t" "ds_read_b64_tr_b16 a[194:195], %[vr14] offset:51200\n\t"
    "global_load_dwordx4 a[52:55], %[vx], s[40:41] offset:-3072 sc1\n\t" "v_mfma_f32_16x16x32_bf16 v[252:255], a[200:203], a[68:71], v[252:255]\n\t"
    "ds_read_b64_tr_b16 a[196:197], %[vr14] offset:57344\n\t" "v_mfma_f32_16x16x32_bf16 v[252:255], a[204:207], a[76:79], v[252:255]\n\t" "ds_read_b64_tr_b16 a[198:199], %[vr14] offset:59392\n\t"
    "s_waitcnt lgkmcnt(4)\n\t" "v_mfma_f32_16x16x32_bf16 v[160:163], a[176:179], a[64:67], v[160:163]\n\t" "ds_read_b64_tr_b16 a[200:201], %[vr15] offset:49152\n\t"
    "v_mfma_f32_16x16x32_bf16 v[160:163], a[180:183], a[72:75], v[160:163]\n\t" "ds_read_b64_tr_b16 a[202:203], %[vr15] offset:51200\n\t"
    "global_load_dwordx4 a[56:59], %[vx], s[40:41] offset:-2048 sc1\n\t" "v_mfma_f32_16x16x32_bf16 v[164:167], a[176:179], a[68:71], v[164:167]\n\t"
    "ds_read_b64_tr_b16 a[204:205], %[vr15] offset:57344\n\t" "v_mfma_f32_16x16x32_bf16 v[164:167], a[180:183], a[76:79], v[164:167]\n\t" "ds_read_b64_tr_b16 a[206:207], %[vr15] offset:59392\n\t"
    "v_mfma_f32_16x16x32_bf16 v[168:171], a[184:187], a[64:67], v[168:171]\n\t" "s_barrier\n\t" "ds_read_b64_tr_b16 a[176:177], %[vr4] offset:0\n\t"
    "v_mfma_f32_16x16x32_bf16 v[168:171], a[188:191], a[72:75], v[168:171]\n\t" "ds_read_b64_tr_b16 a[178:179], %[vr4] offset:2048\n\t" "global_load_dwordx4 v[124:127], %[vg], %[sg] offset:192 sc1\n\t"
    "v_mfma_f32_16x16x32_bf16 v[172:175], a[184:187], a[68:71], v[172:175]\n\t" "ds_read_b64_tr_b16 a[180:181], %[vr4] offset:8192\n\t"
    "v_mfma_f32_16x16x32_bf16 v[172:175], a[188:191], a[76:79], v[172:175]\n\t" "ds_read_b64_tr_b16 a[182:183], %[vr4] offset:10240\n\t" "s_waitcnt lgkmcnt(4)\n\t"
    "v_mfma_f32_16x16x32_bf16 v[176:179], a[192:195], a[64:67], v[176:179]\n\t" "ds_read_b64_tr_b16 a[184:185], %[vr5] offset:0\n\t" "global_load_dwordx4 a[60:63], %[vx], s[40:41] offset:-1024 sc1\n\t"
    "v_mfma_f32_16x16x32_bf16 v[176:179], a[196:199], a[72:75], v[176:179]\n\t" "ds_read_b64_tr_b16 a[186:187], %[vr5] offset:2048\n\t" "s_add_u32 m0, %[sl1], 16384\n\t"
    "v_mfma_f32_16x16x32_bf16 v[180:183], a[192:195], a[68:71], v[180:183]\n\t" "ds_read_b64_tr_b16 a[188:189], %[vr5] offset:8192\n\t"
    "v_mfma_f32_16x16x32_bf16 v[180:183], a[196:199], a[76:79], v[180:183]\n\t" "ds_read_b64_tr_b16 a[190:191], %[vr5] offset:10240\n\t" "global_load_lds_dwordx4 %[vb1], %[sb]\n\t"
    "v_mfma_f32_16x16x32_bf16 v[184:187], a[200:203], a[64:67], v[184:187]\n\t" "ds_read_b64_tr_b16 a[192:193], %[vr6] offset:0\n\t" "v_add_u32 %[vb1], 0x10000, %[vb1]\n\t"
    "v_mfma_f32_16x16x32_bf16 v[184:187], a[204:207], a[72:75], v[184:187]\n\t" "ds_read_b64_tr_b16 a[194:195], %[vr6] offset:2048\n\t" "s_add_u32 m0, %[sl0], 24576\n\t"
    "v_mfma_f32_16x16x32_bf16 v[188:191], a[200:203], a[68:71], v[188:191]\n\t" "ds_read_b64_tr_b16 a[196:197], %[vr6] offset:8192\n\t"
    "v_mfma_f32_16x16x32_bf16 v[188:191], a[204:207], a[76:79], v[188:191]\n\t" "ds_read_b64_tr_b16 a[198:199], %[vr6] offset:10240\n\t" "s_waitcnt lgkmcnt(4) vmcnt(29)\n\t"
    "v_mfma_f32_16x16x32_bf16 v[224:227], a[176:179], a[0:3], v[224:227]\n\t" "ds_read_b64_tr_b16 a[200:201], %[vr7] offset:0\n\t" "global_load_lds_dwordx4 %[vb0], %[sb]\n\t"
    "v_mfma_f32_16x16x32_bf16 v[224:227], a[180:183], a[8:11], v[224:227]\n\t" "ds_read_b64_tr_b16 a[202:203], %[vr7] offset:2048\n\t" "v_add_u32 %[vb0], 0x10000, %[vb0]\n\t"
    "v_mfma_f32_16x16x32_bf16 v[240:243], a[176:179], a[4:7], v[240:243]\n\t" "ds_read_b64_tr_b16 a[204:205], %[vr7] offset:8192\n\t"
    "v_mfma_f32_16x16x32_bf16 v[240:243], a[180:183], a[12:15], v[240:243]\n\t" "ds_read_b64_tr_b16 a[206:207], %[vr7] offset:10240\n\t" "global_load_dwordx2 a[64:65], %[vo], s[40:41] offset:0 sc1\n\t"
    "v_mfma_f32_16x16x32_bf16 v[228:231], a[184:187], a[0:3], v[228:231]\n\t" "ds_read_b64_tr_b16 a[176:177], %[vr16] offset:0\n\t"
    "v_mfma_f32_16x16x32_bf16 v[228:231], a[188:191], a[8:11], v[228:231]\n\t" "ds_read_b64_tr_b16 a[178:179], %[vr16] offset:2048\n\t" "global_load_dwordx2 a[66:67], %[vo], s[40:41] offset:1024 sc1\n\t"
    "v_mfma_f32_16x16x32_bf16 v[244:247], a[184:187], a[4:7], v[244:247]\n\t" "ds_read_b64_tr_b16 a[180:181], %[vr16] offset:8192\n\t"
    "v_mfma_f32_16x16x32_bf16 v[244:247], a[188:191], a[12:15], v[244:247]\n\t" "ds_read_b64_tr_b16 a[182:183], %[vr16] offset:10240\n\t" "s_waitcnt lgkmcnt(4)\n\t"
    "v_mfma_f32_16x16x32_bf16 v[232:235], a[192:195], a[0:3], v[232:235]\n\t" "ds_read_b64_tr_b16 a[184:185], %[vr17] offset:0\n\t" "global_load_dwordx2 a[68:69], %[vo], s[40:41] offset:512 sc1\n\t"
    "v_mfma_f32_16x16x32_bf16 v[232:235], a[196:199], a[8:11], v[232:235]\n\t" "ds_read_b64_tr_b16 a[186:187], %[vr17] offset:2048\n\t" "global_load_dwordx2 a[70:71], %[vo], s[40:41] offset:1536 sc1\n\t"
    "v_mfma_f32_16x16x32_bf16 v[248:251], a[192:195], a[4:7], v[248:251]\n\t" "ds_read_b64_tr_b16 a[188:189], %[vr17] offset:8192\n\t" "s_add_u32 m0, %[sl1], 24576\n\t"
    "v_mfma_f32_16x16x32_bf16 v[248:251], a[196:199], a[12:15], v[248:251]\n\t" "ds_read_b64_tr_b16 a[190:191], %[vr17] offset:10240\n\t" "global_load_lds_dwordx4 %[vb1], %[sb]\n\t"
    "v_mfma_f32_16x16x32_bf16 v[236:239], a[200:203], a[0:3], v[236:239]\n\t" "ds_read_b64_tr_b16 a[192:193], %[vr18] offset:0\n\t" "v_add_u32 %[vb1], 0x10000, %[vb1]\n\t"
    "v_mfma_f32_16x16x32_bf16 v[236:239], a[204:207], a[8:11], v[236:239]\n\t" "ds_read_b64_tr_b16 a[194:195], %[vr18] offset:2048\n\t"
    "v_mfma_f32_16x16x32_bf16 v[252:255], a[200:203], a[4:7], v[252:255]\n\t" "ds_read_b64_tr_b16 a[196:197], %[vr18] offset:8192\n\t"
    "v_mfma_f32_16x16x32_bf16 v[252:255], a[204:207], a[12:15], v[252:255]\n\t" "ds_read_b64_tr_b16 a[198:199], %[vr18] offset:10240\n\t" "s_waitcnt lgkmcnt(4)\n\t"
    "v_mfma_f32_16x16x32_bf16 v[160:163], a[176:179], a[0:3], v[160:163]\n\t" "ds_read_b64_tr_b16 a[200:201], %[vr19] offset:0\n\t" "global_load_dwordx4 a[72:75], %[vx], s[40:41] offset:2048 sc1\n\t"
    "v_mfma_f32_16x16x32_bf16 v[160:163], a[180:183], a[8:11], v[160:163]\n\t" "ds_read_b64_tr_b16 a[202:203], %[vr19] offset:2048\n\t"
    "v_mfma_f32_16x16x32_bf16 v[164:167], a[176:179], a[4:7], v[164:167]\n\t" "ds_read_b64_tr_b16 a[204:205], %[vr19] offset:8192\n\t"
    "v_mfma_f32_16x16x32_bf16 v[164:167], a[180:183], a[12:15], v[164:167]\n\t" "ds_read_b64_tr_b16 a[206:207], %[vr19] offset:10240\n\t"
    "global_load_dwordx4 a[76:79], %[vx], s[40:41] offset:3072 sc1\n\t" "v_mfma_f32_16x16x32_bf16 v[168:171], a[184:187], a[0:3], v[168:171]\n\t" "s_barrier\n\t"
    "ds_read_b64_tr_b16 a[176:177], %[vr4] offset:16384\n\t" "v_mfma_f32_16x16x32_bf16 v[168:171], a[188:191], a[8:11], v[168:171]\n\t" "ds_read_b64_tr_b16 a[178:179], %[vr4] offset:18432\n\t"
    "s_add_u32 m0, %[sl0], 32768\n\t" "v_mfma_f32_16x16x32_bf16 v[172:175], a[184:187], a[4:7], v[172:175]\n\t" "ds_read_b64_tr_b16 a[180:181], %[vr4] offset:24576\n\t"
    "v_mfma_f32_16x16x32_bf16 v[172:175], a[188:191], a[12:15], v[172:175]\n\t" "ds_read_b64_tr_b16 a[182:183], %[vr4] offset:26624\n\t" "s_waitcnt lgkmcnt(4)\n\t"
    "v_mfma_f32_16x16x32_bf16 v[176:179], a[192:195], a[0:3], v[176:179]\n\t" "ds_read_b64_tr_b16 a[184:185], %[vr5] offset:16384\n\t" "global_load_lds_dwordx4 %[vb0], %[sb]\n\t"
    "v_mfma_f32_16x16x32_bf16 v[176:179], a[196:199], a[8:11], v[176:179]\n\t" "ds_read_b64_tr_b16 a[186:187], %[vr5] offset:18432\n\t" "v_add_u32 %[vb0], 0x10000, %[vb0]\n\t"
    "v_mfma_f32_16x16x32_bf16 v[180:183], a[192:195], a[4:7], v[180:183]\n\t" "ds_read_b64_tr_b16 a[188:189], %[vr5] offset:24576\n\t" "s_add_u32 m0, %[sl1], 32768\n\t"
    "v_mfma_f32_16x16x32_bf16 v[180:183], a[196:199], a[12:15], v[180:183]\n\t" "ds_read_b64_tr_b16 a[190:191], %[vr5] offset:26624\n\t" "global_load_lds_dwordx4 %[vb1], %[sb]\n\t"
    "v_mfma_f32_16x16x32_bf16 v[184:187], a[200:203], a[0:3], v[184:187]\n\t" "ds_read_b64_tr_b16 a[192:193], %[vr6] offset:16384\n\t" "v_add_u32 %[vb1], 0x10000, %[vb1]\n\t"
    "v_mfma_f32_16x16x32_bf16 v[184:187], a[204:207], a[8:11], v[184:187]\n\t" "ds_read_b64_tr_b16 a[194:195], %[vr6] offset:18432\n\t" "s_add_u32 m0, %[sl0], 40960\n\t"
    "v_mfma_f32_16x16x32_bf16 v[188:191], a[200:203], a[4:7], v[188:191]\n\t" "ds_read_b64_tr_b16 a[196:197], %[vr6] offset:24576\n\t"
    "v_mfma_f32_16x16x32_bf16 v[188:191], a[204:207], a[12:15], v[188:191]\n\t" "ds_read_b64_tr_b16 a[198:199], %[vr6] offset:26624\n\t" "s_waitcnt lgkmcnt(4) vmcnt(31)\n\t"
    "v_mfma_f32_16x16x32_bf16 v[224:227], a[176:179], a[16:19], v[224:227]\n\t" "ds_read_b64_tr_b16 a[200:201], %[vr7] offset:16384\n\t" "global_load_lds_dwordx4 %[vb0], %[sb]\n\t"
    "v_mfma_f32_16x16x32_bf16 v[224:227], a[180:183], a[24:27], v[224:227]\n\t" "ds_read_b64_tr_b16 a[202:203], %[vr7] offset:18432\n\t" "v_add_u32 %[vb0], 0x10000, %[vb0]\n\t"
    "v_mfma_f32_16x16x32_bf16 v[240:243], a[176:179], a[20:23], v[240:243]\n\t" "ds_read_b64_tr_b16 a[204:205], %[vr7] offset:24576\n\t" "s_add_u32 s40, s40, 8192\n\t" "s_addc_u32 s41, s41, 0\n\t"
    "v_mfma_f32_16x16x32_bf16 v[240:243], a[180:183], a[28:31], v[240:243]\n\t" "ds_read_b64_tr_b16 a[206:207], %[vr7] offset:26624\n\t" "global_load_dwordx4 a[0:3], %[vx], s[40:41] offset:-4096 sc1\n\t"
    "v_mfma_f32_16x16x32_bf16 v[228:231], a[184:187], a[16:19], v[228:231]\n\t" "ds_read_b64_tr_b16 a[176:177], %[vr16] offset:16384\n\t"
    "v_mfma_f32_16x16x32_bf16 v[228:231], a[188:191], a[24:27], v[228:231]\n\t" "ds_read_b64_tr_b16 a[178:179], %[vr16] offset:18432\n\t"
    "v_mfma_f32_16x16x32_bf16 v[244:247], a[184:187], a[20:23], v[244:247]\n\t" "ds_read_b64_tr_b16 a[180:181], %[vr16] offset:24576\n\t"
    "v_mfma_f32_16x16x32_bf16 v[244:247], a[188:191], a[28:31], v[244:247]\n\t" "ds_read_b64_tr_b16 a[182:183], %[vr16] offset:26624\n\t" "s_waitcnt lgkmcnt(4)\n\t"
    "v_mfma_f32_16x16x32_bf16 v[232:235], a[192:195], a[16:19], v[232:235]\n\t" "ds_read_b64_tr_b16 a[184:185], %[vr17] offset:16384\n\t" "global_load_dwordx4 a[4:7], %[vx], s[40:41] offset:-3072 sc1\n\t"
    "v_mfma_f32_16x16x32_bf16 v[232:235], a[196:199], a[24:27], v[232:235]\n\t" "ds_read_b64_tr_b16 a[186:187], %[vr17] offset:18432\n\t" "s_add_u32 m0, %[sl1], 40960\n\t"
    "v_mfma_f32_16x16x32_bf16 v[248:251], a[192:195], a[20:23], v[248:251]\n\t" "ds_read_b64_tr_b16 a[188:189], %[vr17] offset:24576\n\t"
    "v_mfma_f32_16x16x32_bf16 v[248:251], a[196:199], a[28:31], v[248:251]\n\t" "ds_read_b64_tr_b16 a[190:191], %[vr17] offset:26624\n\t" "global_load_lds_dwordx4 %[vb1], %[sb]\n\t"
    "v_mfma_f32_16x16x32_bf16 v[236:239], a[200:203], a[16:19], v[236:239]\n\t" "ds_read_b64_tr_b16 a[192:193], %[vr18] offset:16384\n\t" "v_add_u32 %[vb1], 0x10000, %[vb1]\n\t"
    "v_mfma_f32_16x16x32_bf16 v[236:239], a[204:207], a[24:27], v[236:239]\n\t" "ds_read_b64_tr_b16 a[194:195], %[vr18] offset:18432\n\t"
    "v_mfma_f32_16x16x32_bf16 v[252:255], a[200:203], a[20:23], v[252:255]\n\t" "ds_read_b64_tr_b16 a[196:197], %[vr18] offset:24576\n\t"
    "v_mfma_f32_16x16x32_bf16 v[252:255], a[204:207], a[28:31], v[252:255]\n\t" "ds_read_b64_tr_b16 a[198:199], %[vr18] offset:26624\n\t" "s_waitcnt lgkmcnt(4)\n\t"
    "v_mfma_f32_16x16x32_bf16 v[160:163], a[176:179], a[16:19], v[160:163]\n\t" "ds_read_b64_tr_b16 a[200:201], %[vr19] offset:16384\n\t"
    "global_load_dwordx4 a[8:11], %[vx], s[40:41] offset:-2048 sc1\n\t" "v_mfma_f32_16x16x32_bf16 v[160:163], a[180:183], a[24:27], v[160:163]\n\t"
    "ds_read_b64_tr_b16 a[202:203], %[vr19] offset:18432\n\t" "v_mfma_f32_16x16x32_bf16 v[164:167], a[176:179], a[20:23], v[164:167]\n\t" "ds_read_b64_tr_b16 a[204:205], %[vr19] offset:24576\n\t"
    "v_mfma_f32_16x16x32_bf16 v[164:167], a[180:183], a[28:31], v[164:167]\n\t" "ds_read_b64_tr_b16 a[206:207], %[vr19] offset:26624\n\t"
    "global_load_dwordx4 a[12:15], %[vx], s[40:41] offset:-1024 sc1\n\t" "v_mfma_f32_16x16x32_bf16 v[168:171], a[184:187], a[16:19], v[168:171]\n\t" "s_barrier\n\t"
    "ds_read_b64_tr_b16 a[176:177], %[vr4] offset:32768\n\t" "v_mfma_f32_16x16x32_bf16 v[168:171], a[188:191], a[24:27], v[168:171]\n\t" "ds_read_b64_tr_b16 a[178:179], %[vr4] offset:34816\n\t"
    "s_add_u32 m0, %[sl0], 139264\n\t" "v_mfma_f32_16x16x32_bf16 v[172:175], a[184:187], a[20:23], v[172:175]\n\t" "ds_read_b64_tr_b16 a[180:181], %[vr4] offset:40960\n\t"
    "v_mfma_f32_16x16x32_bf16 v[172:175], a[188:191], a[28:31], v[172:175]\n\t" "ds_read_b64_tr_b16 a[182:183], %[vr4] offset:43008\n\t" "s_waitcnt lgkmcnt(4)\n\t"
    "v_mfma_f32_16x16x32_bf16 v[176:179], a[192:195], a[16:19], v[176:179]\n\t" "ds_read_b64_tr_b16 a[184:185], %[vr5] offset:32768\n\t" "global_load_lds_dwordx4 %[vb0], %[sb]\n\t"
    "v_mfma_f32_16x16x32_bf16 v[176:179], a[196:199], a[24:27], v[176:179]\n\t" "ds_read_b64_tr_b16 a[186:187], %[vr5] offset:34816\n\t" "v_add_u32 %[vb0], 0x10000, %[vb0]\n\t"
    "v_mfma_f32_16x16x32_bf16 v[180:183], a[192:195], a[20:23], v[180:183]\n\t" "ds_read_b64_tr_b16 a[188:189], %[vr5] offset:40960\n\t" "s_add_u32 m0, %[sl1], 139264\n\t"
    "v_mfma_f32_16x16x32_bf16 v[180:183], a[196:199], a[28:31], v[180:183]\n\t" "ds_read_b64_tr_b16 a[190:191], %[vr5] offset:43008\n\t" "global_load_lds_dwordx4 %[vb1], %[sb]\n\t"
    "v_mfma_f32_16x16x32_bf16 v[184:187], a[200:203], a[16:19], v[184:187]\n\t" "ds_read_b64_tr_b16 a[192:193], %[vr6] offset:32768\n\t" "v_add_u32 %[vb1], 0x10000, %[vb1]\n\t"
    "v_mfma_f32_16x16x32_bf16 v[184:187], a[204:207], a[24:27], v[184:187]\n\t" "ds_read_b64_tr_b16 a[194:195], %[vr6] offset:34816\n\t" "s_add_u32 m0, %[sl0], 147456\n\t"
    "v_mfma_f32_16x16x32_bf16 v[188:191], a[200:203], a[20:23], v[188:191]\n\t" "ds_read_b64_tr_b16 a[196:197], %[vr6] offset:40960\n\t"
    "v_mfma_f32_16x16x32_bf16 v[188:191], a[204:207], a[28:31], v[188:191]\n\t" "ds_read_b64_tr_b16 a[198:199], %[vr6] offset:43008\n\t" "s_waitcnt lgkmcnt(4) vmcnt(29)\n\t"
    "v_mfma_f32_16x16x32_bf16 v[224:227], a[176:179], a[32:35], v[224:227]\n\t" "ds_read_b64_tr_b16 a[200:201], %[vr7] offset:32768\n\t" "global_load_lds_dwordx4 %[vb0], %[sb]\n\t"
    "v_mfma_f32_16x16x32_bf16 v[224:227], a[180:183], a[40:43], v[224:227]\n\t" "ds_read_b64_tr_b16 a[202:203], %[vr7] offset:34816\n\t" "v_add_u32 %[vb0], 0x10000, %[vb0]\n\t"
    "v_mfma_f32_16x16x32_bf16 v[240:243], a[176:179], a[36:39], v[240:243]\n\t" "ds_read_b64_tr_b16 a[204:205], %[vr7] offset:40960\n\t"
    "v_mfma_f32_16x16x32_bf16 v[240:243], a[180:183], a[44:47], v[240:243]\n\t" "ds_read_b64_tr_b16 a[206:207], %[vr7] offset:43008\n\t" "global_load_dwordx4 a[16:19], %[vx], s[40:41] offset:0 sc1\n\t"
    "v_mfma_f32_16x16x32_bf16 v[228:231], a[184:187], a[32:35], v[228:231]\n\t" "ds_read_b64_tr_b16 a[176:177], %[vr16] offset:32768\n\t"
    "v_mfma_f32_16x16x32_bf16 v[228:231], a[188:191], a[40:43], v[228:231]\n\t" "ds_read_b64_tr_b16 a[178:179], %[vr16] offset:34816\n\t"
    "v_mfma_f32_16x16x32_bf16 v[244:247], a[184:187], a[36:39], v[244:247]\n\t" "ds_read_b64_tr_b16 a[180:181], %[vr16] offset:40960\n\t"
    "v_mfma_f32_16x16x32_bf16 v[244:247], a[188:191], a[44:47], v[244:247]\n\t" "ds_read_b64_tr_b16 a[182:183], %[vr16] offset:43008\n\t" "s_waitcnt lgkmcnt(4)\n\t"
    "v_mfma_f32_16x16x32_bf16 v[232:235], a[192:195], a[32:35], v[232:235]\n\t" "ds_read_b64_tr_b16 a[184:185], %[vr17] offset:32768\n\t"
    "global_load_dwordx4 a[20:23], %[vx], s[40:41] offset:1024 sc1\n\t" "v_mfma_f32_16x16x32_bf16 v[232:235], a[196:199], a[40:43], v[232:235]\n\t"
    "ds_read_b64_tr_b16 a[186:187], %[vr17] offset:34816\n\t" "v_mfma_f32_16x16x32_bf16 v[248:251], a[192:195], a[36:39], v[248:251]\n\t" "ds_read_b64_tr_b16 a[188:189], %[vr17] offset:40960\n\t"
    "v_mfma_f32_16x16x32_bf16 v[248:251], a[196:199], a[44:47], v[248:251]\n\t" "ds_read_b64_tr_b16 a[190:191], %[vr17] offset:43008\n\t"
    "global_load_dwordx4 a[24:27], %[vx], s[40:41] offset:2048 sc1\n\t" "v_mfma_f32_16x16x32_bf16 v[236:239], a[200:203], a[32:35], v[236:239]\n\t"
    "ds_read_b64_tr_b16 a[192:193], %[vr18] offset:32768\n\t" "v_mfma_f32_16x16x32_bf16 v[236:239], a[204:207], a[40:43], v[236:239]\n\t" "ds_read_b64_tr_b16 a[194:195], %[vr18] offset:34816\n\t"
    "v_mfma_f32_16x16x32_bf16 v[252:255], a[200:203], a[36:39], v[252:255]\n\t" "ds_read_b64_tr_b16 a[196:197], %[vr18] offset:40960\n\t"
    "v_mfma_f32_16x16x32_bf16 v[252:255], a[204:207], a[44:47], v[252:255]\n\t" "ds_read_b64_tr_b16 a[198:199], %[vr18] offset:43008\n\t" "s_waitcnt lgkmcnt(4)\n\t"
    "v_mfma_f32_16x16x32_bf16 v[160:163], a[176:179], a[32:35], v[160:163]\n\t" "ds_read_b64_tr_b16 a[200:201], %[vr19] offset:32768\n\t"
    "global_load_dwordx4 a[28:31], %[vx], s[40:41] offset:3072 sc1\n\t" "v_mfma_f32_16x16x32_bf16 v[160:163], a[180:183], a[40:43], v[160:163]\n\t"
    "ds_read_b64_tr_b16 a[202:203], %[vr19] offset:34816\n\t" "s_add_u32 m0, %[sl0], 155648\n\t" "v_mfma_f32_16x16x32_bf16 v[164:167], a[176:179], a[36:39], v[164:167]\n\t"
    "ds_read_b64_tr_b16 a[204:205], %[vr19] offset:40960\n\t" "v_mfma_f32_16x16x32_bf16 v[164:167], a[180:183], a[44:47], v[164:167]\n\t" "ds_read_b64_tr_b16 a[206:207], %[vr19] offset:43008\n\t"
    "global_load_lds_dwordx4 %[vb0], %[sb]\n\t" "v_mfma_f32_16x16x32_bf16 v[168:171], a[184:187], a[32:35], v[168:171]\n\t" "s_barrier\n\t" "ds_read_b64_tr_b16 a[176:177], %[vr4] offset:49152\n\t"
    "v_mfma_f32_16x16x32_bf16 v[168:171], a[188:191], a[40:43], v[168:171]\n\t" "ds_read_b64_tr_b16 a[178:179], %[vr4] offset:51200\n\t" "v_add_u32 %[vb0], 0x10000, %[vb0]\n\t"
    "v_mfma_f32_16x16x32_bf16 v[172:175], a[184:187], a[36:39], v[172:175]\n\t" "ds_read_b64_tr_b16 a[180:181], %[vr4] offset:57344\n\t" "s_add_u32 s40, s40, 8192\n\t" "s_addc_u32 s41, s41, 0\n\t"
    "v_mfma_f32_16x16x32_bf16 v[172:175], a[188:191], a[44:47], v[172:175]\n\t" "ds_read_b64_tr_b16 a[182:183], %[vr4] offset:59392\n\t" "s_waitcnt lgkmcnt(4)\n\t"
    "v_mfma_f32_16x16x32_bf16 v[176:179], a[192:195], a[32:35], v[176:179]\n\t" "ds_read_b64_tr_b16 a[184:185], %[vr5] offset:49152\n\t"
    "global_load_dwordx4 a[80:83], %[vx], s[40:41] offset:-4096 sc1\n\t" "v_mfma_f32_16x16x32_bf16 v[176:179], a[196:199], a[40:43], v[176:179]\n\t"
    "ds_read_b64_tr_b16 a[186:187], %[vr5] offset:51200\n\t" "v_mfma_f32_16x16x32_bf16 v[180:183], a[192:195], a[36:39], v[180:183]\n\t" "ds_read_b64_tr_b16 a[188:189], %[vr5] offset:57344\n\t"
    "v_mfma_f32_16x16x32_bf16 v[180:183], a[196:199], a[44:47], v[180:183]\n\t" "ds_read_b64_tr_b16 a[190:191], %[vr5] offset:59392\n\t"
    "global_load_dwordx4 a[84:87], %[vx], s[40:41] offset:-3072 sc1\n\t" "v_mfma_f32_16x16x32_bf16 v[184:187], a[200:203], a[32:35], v[184:187]\n\t"
    "ds_read_b64_tr_b16 a[192:193], %[vr6] offset:49152\n\t" "v_mfma_f32_16x16x32_bf16 v[184:187], a[204:207], a[40:43], v[184:187]\n\t" "ds_read_b64_tr_b16 a[194:195], %[vr6] offset:51200\n\t"
    "s_add_u32 m0, %[sl0], 0\n\t" "v_mfma_f32_16x16x32_bf16 v[188:191], a[200:203], a[36:39], v[188:191]\n\t" "ds_read_b64_tr_b16 a[196:197], %[vr6] offset:57344\n\t"
    "v_mfma_f32_16x16x32_bf16 v[188:191], a[204:207], a[44:47], v[188:191]\n\t" "ds_read_b64_tr_b16 a[198:199], %[vr6] offset:59392\n\t" "s_waitcnt lgkmcnt(4) vmcnt(27)\n\t"
    "v_mfma_f32_16x16x32_bf16 v[224:227], a[176:179], a[48:51], v[224:227]\n\t" "ds_read_b64_tr_b16 a[200:201], %[vr7] offset:49152\n\t" "global_load_lds_dwordx4 %[vb0], %[sb]\n\t"
    "v_mfma_f32_16x16x32_bf16 v[224:227], a[180:183], a[56:59], v[224:227]\n\t" "ds_read_b64_tr_b16 a[202:203], %[vr7] offset:51200\n\t" "v_add_u32 %[vb0], 0x10000, %[vb0]\n\t"
    "v_mfma_f32_16x16x32_bf16 v[240:243], a[176:179], a[52:55], v[240:243]\n\t" "ds_read_b64_tr_b16 a[204:205], %[vr7] offset:57344\n\t"
    "v_mfma_f32_16x16x32_bf16 v[240:243], a[180:183], a[60:63], v[240:243]\n\t" "ds_read_b64_tr_b16 a[206:207], %[vr7] offset:59392\n\t"
    "global_load_dwordx2 a[88:89], %[vo], s[40:41] offset:-2048 sc1\n\t" "v_mfma_f32_16x16x32_bf16 v[228:231], a[184:187], a[48:51], v[228:231]\n\t"
    "ds_read_b64_tr_b16 a[176:177], %[vr16] offset:49152\n\t" "v_mfma_f32_16x16x32_bf16 v[228:231], a[188:191], a[56:59], v[228:231]\n\t" "ds_read_b64_tr_b16 a[178:179], %[vr16] offset:51200\n\t"
    "global_load_dwordx2 a[90:91], %[vo], s[40:41] offset:-1024 sc1\n\t" "v_mfma_f32_16x16x32_bf16 v[244:247], a[184:187], a[52:55], v[244:247]\n\t"
    "ds_read_b64_tr_b16 a[180:181], %[vr16] offset:57344\n\t" "v_mfma_f32_16x16x32_bf16 v[244:247], a[188:191], a[60:63], v[244:247]\n\t" "ds_read_b64_tr_b16 a[182:183], %[vr16] offset:59392\n\t"
    "s_waitcnt lgkmcnt(4)\n\t" "v_mfma_f32_16x16x32_bf16 v[232:235], a[192:195], a[48:51], v[232:235]\n\t" "ds_read_b64_tr_b16 a[184:185], %[vr17] offset:49152\n\t"
    "global_load_dwordx2 a[92:93], %[vo], s[40:41] offset:-1536 sc1\n\t" "v_mfma_f32_16x16x32_bf16 v[232:235], a[196:199], a[56:59], v[232:235]\n\t"
    "ds_read_b64_tr_b16 a[186:187], %[vr17] offset:51200\n\t" "global_load_dwordx2 a[94:95], %[vo], s[40:41] offset:-512 sc1\n\t"
    "v_mfma_f32_16x16x32_bf16 v[248:251], a[192:195], a[52:55], v[248:251]\n\t" "ds_read_b64_tr_b16 a[188:189], %[vr17] offset:57344\n\t" "s_add_u32 m0, %[sl0], 8192\n\t"
    "v_mfma_f32_16x16x32_bf16 v[248:251], a[196:199], a[60:63], v[248:251]\n\t" "ds_read_b64_tr_b16 a[190:191], %[vr17] offset:59392\n\t" "global_load_lds_dwordx4 %[vb0], %[sb]\n\t"
    "v_mfma_f32_16x16x32_bf16 v[236:239], a[200:203], a[48:51], v[236:239]\n\t" "ds_read_b64_tr_b16 a[192:193], %[vr18] offset:49152\n\t" "v_add_u32 %[vb0], 0x10000, %[vb0]\n\t"
    "v_mfma_f32_16x16x32_bf16 v[236:239], a[204:207], a[56:59], v[236:239]\n\t" "ds_read_b64_tr_b16 a[194:195], %[vr18] offset:51200\n\t"
    "v_mfma_f32_16x16x32_bf16 v[252:255], a[200:203], a[52:55], v[252:255]\n\t" "ds_read_b64_tr_b16 a[196:197], %[vr18] offset:57344\n\t"
    "v_mfma_f32_16x16x32_bf16 v[252:255], a[204:207], a[60:63], v[252:255]\n\t" "ds_read_b64_tr_b16 a[198:199], %[vr18] offset:59392\n\t" "s_waitcnt lgkmcnt(4)\n\t"
    "v_mfma_f32_16x16x32_bf16 v[160:163], a[176:179], a[48:51], v[160:163]\n\t" "ds_read_b64_tr_b16 a[200:201], %[vr19] offset:49152\n\t" "global_load_dwordx4 a[96:99], %[vx], s[40:41] offset:0 sc1\n\t"
    "v_mfma_f32_16x16x32_bf16 v[160:163], a[180:183], a[56:59], v[160:163]\n\t" "ds_read_b64_tr_b16 a[202:203], %[vr19] offset:51200\n\t"
    "v_mfma_f32_16x16x32_bf16 v[164:167], a[176:179], a[52:55], v[164:167]\n\t" "ds_read_b64_tr_b16 a[204:205], %[vr19] offset:57344\n\t"
    "v_mfma_f32_16x16x32_bf16 v[164:167], a[180:183], a[60:63], v[164:167]\n\t" "ds_read_b64_tr_b16 a[206:207], %[vr19] offset:59392\n\t"
    "global_load_dwordx4 a[100:103], %[vx], s[40:41] offset:1024 sc1\n\t" "v_mfma_f32_16x16x32_bf16 v[168:171], a[184:187], a[48:51], v[168:171]\n\t" "s_waitcnt vmcnt(34)\n\t" "s_barrier\n\t"
    "ds_read_b64_tr_b16 a[176:177], %[vr8] offset:0\n\t" "v_mfma_f32_16x16x32_bf16 v[168:171], a[188:191], a[56:59], v[168:171]\n\t" "ds_read_b64_tr_b16 a[178:179], %[vr8] offset:2048\n\t"
    "s_add_u32 m0, %[sl0], 49152\n\t" "v_mfma_f32_16x16x32_bf16 v[172:175], a[184:187], a[52:55], v[172:175]\n\t" "ds_read_b64_tr_b16 a[180:181], %[vr0] offset:16384\n\t"
    "v_mfma_f32_16x16x32_bf16 v[172:175], a[188:191], a[60:63], v[172:175]\n\t" "ds_read_b64_tr_b16 a[182:183], %[vr0] offset:18432\n\t" "s_waitcnt lgkmcnt(4)\n\t"
    "v_mfma_f32_16x16x32_bf16 v[176:179], a[192:195], a[48:51], v[176:179]\n\t" "ds_read_b64_tr_b16 a[184:185], %[vr9] offset:0\n\t" "global_load_lds_dwordx4 %[vb0], %[sb]\n\t"
    "v_mfma_f32_16x16x32_bf16 v[176:179], a[196:199], a[56:59], v[176:179]\n\t" "ds_read_b64_tr_b16 a[186:187], %[vr9] offset:2048\n\t" "v_add_u32 %[vb0], 0x10000, %[vb0]\n\t"
    "v_mfma_f32_16x16x32_bf16 v[180:183], a[192:195], a[52:55], v[180:183]\n\t" "ds_read_b64_tr_b16 a[188:189], %[vr1] offset:16384\n\t"
    "v_mfma_f32_16x16x32_bf16 v[180:183], a[196:199], a[60:63], v[180:183]\n\t" "ds_read_b64_tr_b16 a[190:191], %[vr1] offset:18432\n\t"
    "global_load_dwordx4 a[104:107], %[vx], s[40:41] offset:2048 sc1\n\t" "v_mfma_f32_16x16x32_bf16 v[184:187], a[200:203], a[48:51], v[184:187]\n\t" "ds_read_b64_tr_b16 a[192:193], %[vr10] offset:0\n\t"
    "v_mfma_f32_16x16x32_bf16 v[184:187], a[204:207], a[56:59], v[184:187]\n\t" "ds_read_b64_tr_b16 a[194:195], %[vr10] offset:2048\n\t"
    "v_mfma_f32_16x16x32_bf16 v[188:191], a[200:203], a[52:55], v[188:191]\n\t" "ds_read_b64_tr_b16 a[196:197], %[vr2] offset:16384\n\t"
    "v_mfma_f32_16x16x32_bf16 v[188:191], a[204:207], a[60:63], v[188:191]\n\t" "ds_read_b64_tr_b16 a[198:199], %[vr2] offset:18432\n\t" "s_waitcnt lgkmcnt(4) vmcnt(28)\n\t"
    "v_mfma_f32_16x16x32_bf16 v[224:227], a[176:179], a[64:67], v[224:227]\n\t" "ds_read_b64_tr_b16 a[200:201], %[vr11] offset:0\n\t" "global_load_dwordx4 a[108:111], %[vx], s[40:41] offset:3072 sc1\n\t"
    "v_mfma_f32_16x16x32_bf16 v[224:227], a[180:183], a[72:75], v[224:227]\n\t" "ds_read_b64_tr_b16 a[202:203], %[vr11] offset:2048\n\t" "s_add_u32 m0, %[sl0], 57344\n\t"
    "v_mfma_f32_16x16x32_bf16 v[240:243], a[176:179], a[68:71], v[240:243]\n\t" "ds_read_b64_tr_b16 a[204:205], %[vr3] offset:16384\n\t"
    "v_mfma_f32_16x16x32_bf16 v[240:243], a[180:183], a[76:79], v[240:243]\n\t" "ds_read_b64_tr_b16 a[206:207], %[vr3] offset:18432\n\t" "global_load_lds_dwordx4 %[vb0], %[sb]\n\t"
    "v_mfma_f32_16x16x32_bf16 v[228:231], a[184:187], a[64:67], v[228:231]\n\t" "ds_read_b64_tr_b16 a[176:177], %[vr20] offset:0\n\t" "v_add_u32 %[vb0], 0x10000, %[vb0]\n\t"
    "v_mfma_f32_16x16x32_bf16 v[228:231], a[188:191], a[72:75], v[228:231]\n\t" "ds_read_b64_tr_b16 a[178:179], %[vr20] offset:2048\n\t" "s_add_u32 s40, s40, 8192\n\t" "s_addc_u32 s41, s41, 0\n\t"
    "v_mfma_f32_16x16x32_bf16 v[244:247], a[184:187], a[68:71], v[244:247]\n\t" "ds_read_b64_tr_b16 a[180:181], %[vr12] offset:16384\n\t"
    "v_mfma_f32_16x16x32_bf16 v[244:247], a[188:191], a[76:79], v[244:247]\n\t" "ds_read_b64_tr_b16 a[182:183], %[vr12] offset:18432\n\t" "s_waitcnt lgkmcnt(4)\n\t"
    "v_mfma_f32_16x16x32_bf16 v[232:235], a[192:195], a[64:67], v[232:235]\n\t" "ds_read_b64_tr_b16 a[184:185], %[vr21] offset:0\n\t" "global_load_dwordx4 a[112:115], %[vx], s[40:41] offset:-4096 sc1\n\t"
    "v_mfma_f32_16x16x32_bf16 v[232:235], a[196:199], a[72:75], v[232:235]\n\t" "ds_read_b64_tr_b16 a[186:187], %[vr21] offset:2048\n\t"
    "v_mfma_f32_16x16x32_bf16 v[248:251], a[192:195], a[68:71], v[248:251]\n\t" "ds_read_b64_tr_b16 a[188:189], %[vr13] offset:16384\n\t"
    "v_mfma_f32_16x16x32_bf16 v[248:251], a[196:199], a[76:79], v[248:251]\n\t" "ds_read_b64_tr_b16 a[190:191], %[vr13] offset:18432\n\t"
    "global_load_dwordx4 a[116:119], %[vx], s[40:41] offset:-3072 sc1\n\t" "v_mfma_f32_16x16x32_bf16 v[236:239], a[200:203], a[64:67], v[236:239]\n\t" "ds_read_b64_tr_b16 a[192:193], %[vr22] offset:0\n\t"
    "v_mfma_f32_16x16x32_bf16 v[236:239], a[204:207], a[72:75], v[236:239]\n\t" "ds_read_b64_tr_b16 a[194:195], %[vr22] offset:2048\n\t" "s_add_u32 m0, %[sl0], 65536\n\t"
    "v_mfma_f32_16x16x32_bf16 v[252:255], a[200:203], a[68:71], v[252:255]\n\t" "ds_read_b64_tr_b16 a[196:197], %[vr14] offset:16384\n\t"
    "v_mfma_f32_16x16x32_bf16 v[252:255], a[204:207], a[76:79], v[252:255]\n\t" "ds_read_b64_tr_b16 a[198:199], %[vr14] offset:18432\n\t" "s_waitcnt lgkmcnt(4)\n\t"
    "v_mfma_f32_16x16x32_bf16 v[160:163], a[176:179], a[64:67], v[160:163]\n\t" "ds_read_b64_tr_b16 a[200:201], %[vr23] offset:0\n\t" "global_load_lds_dwordx4 %[vb0], %[sb]\n\t"
    "v_mfma_f32_16x16x32_bf16 v[160:163], a[180:183], a[72:75], v[160:163]\n\t" "ds_read_b64_tr_b16 a[202:203], %[vr23] offset:2048\n\t" "v_add_u32 %[vb0], 0x10000, %[vb0]\n\t"
    "v_mfma_f32_16x16x32_bf16 v[164:167], a[176:179], a[68:71], v[164:167]\n\t" "ds_read_b64_tr_b16 a[204:205], %[vr15] offset:16384\n\t"
    "v_mfma_f32_16x16x32_bf16 v[164:167], a[180:183], a[76:79], v[164:167]\n\t" "ds_read_b64_tr_b16 a[206:207], %[vr15] offset:18432\n\t"
    "global_load_dwordx4 a[120:123], %[vx], s[40:41] offset:-2048 sc1\n\t" "v_mfma_f32_16x16x32_bf16 v[168:171], a[184:187], a[64:67], v[168:171]\n\t" "s_waitcnt vmcnt(32)\n\t" "s_barrier\n\t"
    "ds_read_b64_tr_b16 a[176:177], %[vr0] offset:24576\n\t" "v_mfma_f32_16x16x32_bf16 v[168:171], a[188:191], a[72:75], v[168:171]\n\t" "ds_read_b64_tr_b16 a[178:179], %[vr0] offset:26624\n\t"
    "v_mfma_f32_16x16x32_bf16 v[172:175], a[184:187], a[68:71], v[172:175]\n\t" "ds_read_b64_tr_b16 a[180:181], %[vr0] offset:32768\n\t"
    "v_mfma_f32_16x16x32_bf16 v[172:175], a[188:191], a[76:79], v[172:175]\n\t" "ds_read_b64_tr_b16 a[182:183], %[vr0] offset:34816\n\t" "s_waitcnt lgkmcnt(4)\n\t"
    "v_mfma_f32_16x16x32_bf16 v[176:179], a[192:195], a[64:67], v[176:179]\n\t" "ds_read_b64_tr_b16 a[184:185], %[vr1] offset:24576\n\t"
    "global_load_dwordx4 a[124:127], %[vx], s[40:41] offset:-1024 sc1\n\t" "v_mfma_f32_16x16x32_bf16 v[176:179], a[196:199], a[72:75], v[176:179]\n\t"
    "ds_read_b64_tr_b16 a[186:187], %[vr1] offset:26624\n\t" "s_add_u32 m0, %[sl0], 73728\n\t" "v_mfma_f32_16x16x32_bf16 v[180:183], a[192:195], a[68:71], v[180:183]\n\t"
    "ds_read_b64_tr_b16 a[188:189], %[vr1] offset:32768\n\t" "v_mfma_f32_16x16x32_bf16 v[180:183], a[196:199], a[76:79], v[180:183]\n\t" "ds_read_b64_tr_b16 a[190:191], %[vr1] offset:34816\n\t"
    "global_load_lds_dwordx4 %[vb0], %[sb]\n\t" "v_mfma_f32_16x16x32_bf16 v[184:187], a[200:203], a[64:67], v[184:187]\n\t" "ds_read_b64_tr_b16 a[192:193], %[vr2] offset:24576\n\t"
    "v_add_u32 %[vb0], 0x10000, %[vb0]\n\t" "v_mfma_f32_16x16x32_bf16 v[184:187], a[204:207], a[72:75], v[184:187]\n\t" "ds_read_b64_tr_b16 a[194:195], %[vr2] offset:26624\n\t"
    "v_mfma_f32_16x16x32_bf16 v[188:191], a[200:203], a[68:71], v[188:191]\n\t" "ds_read_b64_tr_b16 a[196:197], %[vr2] offset:32768\n\t"
    "v_mfma_f32_16x16x32_bf16 v[188:191], a[204:207], a[76:79], v[188:191]\n\t" "ds_read_b64_tr_b16 a[198:199], %[vr2] offset:34816\n\t" "s_waitcnt lgkmcnt(4) vmcnt(28)\n\t"
    "v_mfma_f32_16x16x32_bf16 v[224:227], a[176:179], a[0:3], v[224:227]\n\t" "ds_read_b64_tr_b16 a[200:201], %[vr3] offset:24576\n\t" "global_load_dwordx4 a[128:131], %[vx], s[40:41] offset:0 sc1\n\t"
    "v_mfma_f32_16x16x32_bf16 v[224:227], a[180:183], a[8:11], v[224:227]\n\t" "ds_read_b64_tr_b16 a[202:203], %[vr3] offset:26624\n\t"
    "v_mfma_f32_16x16x32_bf16 v[240:243], a[176:179], a[4:7], v[240:243]\n\t" "ds_read_b64_tr_b16 a[204:205], %[vr3] offset:32768\n\t"
    "v_mfma_f32_16x16x32_bf16 v[240:243], a[180:183], a[12:15], v[240:243]\n\t" "ds_read_b64_tr_b16 a[206:207], %[vr3] offset:34816\n\t"
    "global_load_dwordx4 a[132:135], %[vx], s[40:41] offset:1024 sc1\n\t" "v_mfma_f32_16x16x32_bf16 v[228:231], a[184:187], a[0:3], v[228:231]\n\t"
    "ds_read_b64_tr_b16 a[176:177], %[vr12] offset:24576\n\t" "v_mfma_f32_16x16x32_bf16 v[228:231], a[188:191], a[8:11], v[228:231]\n\t" "ds_read_b64_tr_b16 a[178:179], %[vr12] offset:26624\n\t"
    "s_add_u32 m0, %[sl0], 81920\n\t" "v_mfma_f32_16x16x32_bf16 v[244:247], a[184:187], a[4:7], v[244:247]\n\t" "ds_read_b64_tr_b16 a[180:181], %[vr12] offset:32768\n\t"
    "v_mfma_f32_16x16x32_bf16 v[244:247], a[188:191], a[12:15], v[244:247]\n\t" "ds_read_b64_tr_b16 a[182:183], %[vr12] offset:34816\n\t" "s_waitcnt lgkmcnt(4)\n\t"
    "v_mfma_f32_16x16x32_bf16 v[232:235], a[192:195], a[0:3], v[232:235]\n\t" "ds_read_b64_tr_b16 a[184:185], %[vr13] offset:24576\n\t" "global_load_lds_dwordx4 %[vb0], %[sb]\n\t"
    "v_mfma_f32_16x16x32_bf16 v[232:235], a[196:199], a[8:11], v[232:235]\n\t" "ds_read_b64_tr_b16 a[186:187], %[vr13] offset:26624\n\t" "v_add_u32 %[vb0], 0x10000, %[vb0]\n\t"
    "v_mfma_f32_16x16x32_bf16 v[248:251], a[192:195], a[4:7], v[248:251]\n\t" "ds_read_b64_tr_b16 a[188:189], %[vr13] offset:32768\n\t"
    "v_mfma_f32_16x16x32_bf16 v[248:251], a[196:199], a[12:15], v[248:251]\n\t" "ds_read_b64_tr_b16 a[190:191], %[vr13] offset:34816\n\t"
    "global_load_dwordx4 a[136:139], %[vx], s[40:41] offset:2048 sc1\n\t" "v_mfma_f32_16x16x32_bf16 v[236:239], a[200:203], a[0:3], v[236:239]\n\t"
    "ds_read_b64_tr_b16 a[192:193], %[vr14] offset:24576\n\t" "v_mfma_f32_16x16x32_bf16 v[236:239], a[204:207], a[8:11], v[236:239]\n\t" "ds_read_b64_tr_b16 a[194:195], %[vr14] offset:26624\n\t"
    "v_mfma_f32_16x16x32_bf16 v[252:255], a[200:203], a[4:7], v[252:255]\n\t" "ds_read_b64_tr_b16 a[196:197], %[vr14] offset:32768\n\t"
    "v_mfma_f32_16x16x32_bf16 v[252:255], a[204:207], a[12:15], v[252:255]\n\t" "ds_read_b64_tr_b16 a[198:199], %[vr14] offset:34816\n\t" "s_waitcnt lgkmcnt(4)\n\t"
    "v_mfma_f32_16x16x32_bf16 v[160:163], a[176:179], a[0:3], v[160:163]\n\t" "ds_read_b64_tr_b16 a[200:201], %[vr15] offset:24576\n\t"
    "global_load_dwordx4 a[140:143], %[vx], s[40:41] offset:3072 sc1\n\t" "v_mfma_f32_16x16x32_bf16 v[160:163], a[180:183], a[8:11], v[160:163]\n\t"
    "ds_read_b64_tr_b16 a[202:203], %[vr15] offset:26624\n\t" "s_add_u32 m0, %[sl0], 90112\n\t" "v_mfma_f32_16x16x32_bf16 v[164:167], a[176:179], a[4:7], v[164:167]\n\t"
    "ds_read_b64_tr_b16 a[204:205], %[vr15] offset:32768\n\t" "v_mfma_f32_16x16x32_bf16 v[164:167], a[180:183], a[12:15], v[164:167]\n\t" "ds_read_b64_tr_b16 a[206:207], %[vr15] offset:34816\n\t"
    "global_load_lds_dwordx4 %[vb0], %[sb]\n\t" "v_mfma_f32_16x16x32_bf16 v[168:171], a[184:187], a[0:3], v[168:171]\n\t" "s_waitcnt vmcnt(32)\n\t" "s_barrier\n\t"
    "ds_read_b64_tr_b16 a[176:177], %[vr0] offset:40960\n\t" "v_mfma_f32_16x16x32_bf16 v[168:171], a[188:191], a[8:11], v[168:171]\n\t" "ds_read_b64_tr_b16 a[178:179], %[vr0] offset:43008\n\t"
    "v_add_u32 %[vb0], 0x10000, %[vb0]\n\t" "v_mfma_f32_16x16x32_bf16 v[172:175], a[184:187], a[4:7], v[172:175]\n\t" "ds_read_b64_tr_b16 a[180:181], %[vr8] offset:8192\n\t" "s_add_u32 s40, s40, 8192\n\t"
    "s_addc_u32 s41, s41, 0\n\t" "v_mfma_f32_16x16x32_bf16 v[172:175], a[188:191], a[12:15], v[172:175]\n\t" "ds_read_b64_tr_b16 a[182:183], %[vr8] offset:10240\n\t" "s_waitcnt lgkmcnt(4)\n\t"
    "v_mfma_f32_16x16x32_bf16 v[176:179], a[192:195], a[0:3], v[176:179]\n\t" "ds_read_b64_tr_b16 a[184:185], %[vr1] offset:40960\n\t"
    "global_load_dwordx2 a[144:145], %[vo], s[40:41] offset:-4096 sc1\n\t" "v_mfma_f32_16x16x32_bf16 v[176:179], a[196:199], a[8:11], v[176:179]\n\t"
    "ds_read_b64_tr_b16 a[186:187], %[vr1] offset:43008\n\t" "global_load_dwordx2 a[146:147], %[vo], s[40:41] offset:-3072 sc1\n\t"
    "v_mfma_f32_16x16x32_bf16 v[180:183], a[192:195], a[4:7], v[180:183]\n\t" "ds_read_b64_tr_b16 a[188:189], %[vr9] offset:8192\n\t"
    "v_mfma_f32_16x16x32_bf16 v[180:183], a[196:199], a[12:15], v[180:183]\n\t" "ds_read_b64_tr_b16 a[190:191], %[vr9] offset:10240\n\t"
    "global_load_dwordx2 a[148:149], %[vo], s[40:41] offset:-3584 sc1\n\t" "v_mfma_f32_16x16x32_bf16 v[184:187], a[200:203], a[0:3], v[184:187]\n\t"
    "ds_read_b64_tr_b16 a[192:193], %[vr2] offset:40960\n\t" "v_mfma_f32_16x16x32_bf16 v[184:187], a[204:207], a[8:11], v[184:187]\n\t" "ds_read_b64_tr_b16 a[194:195], %[vr2] offset:43008\n\t"
    "global_load_dwordx2 a[150:151], %[vo], s[40:41] offset:-2560 sc1\n\t" "v_mfma_f32_16x16x32_bf16 v[188:191], a[200:203], a[4:7], v[188:191]\n\t"
    "ds_read_b64_tr_b16 a[196:197], %[vr10] offset:8192\n\t" "s_add_u32 m0, %[sl0], 98304\n\t" "v_mfma_f32_16x16x32_bf16 v[188:191], a[204:207], a[12:15], v[188:191]\n\t"
    "ds_read_b64_tr_b16 a[198:199], %[vr10] offset:10240\n\t" "s_waitcnt lgkmcnt(4) vmcnt(31)\n\t" "v_mfma_f32_16x16x32_bf16 v[224:227], a[176:179], a[16:19], v[224:227]\n\t"
    "ds_read_b64_tr_b16 a[200:201], %[vr3] offset:40960\n\t" "global_load_lds_dwordx4 %[vb0], %[sb]\n\t" "v_mfma_f32_16x16x32_bf16 v[224:227], a[180:183], a[24:27], v[224:227]\n\t"
    "ds_read_b64_tr_b16 a[202:203], %[vr3] offset:43008\n\t" "v_add_u32 %[vb0], 0x10000, %[vb0]\n\t" "v_mfma_f32_16x16x32_bf16 v[240:243], a[176:179], a[20:23], v[240:243]\n\t"
    "ds_read_b64_tr_b16 a[204:205], %[vr11] offset:8192\n\t" "v_mfma_f32_16x16x32_bf16 v[240:243], a[180:183], a[28:31], v[240:243]\n\t" "ds_read_b64_tr_b16 a[206:207], %[vr11] offset:10240\n\t"
    "global_load_dwordx4 a[152:155], %[vx], s[40:41] offset:-2048 sc1\n\t" "v_mfma_f32_16x16x32_bf16 v[228:231], a[184:187], a[16:19], v[228:231]\n\t"
    "ds_read_b64_tr_b16 a[176:177], %[vr12] offset:40960\n\t" "v_mfma_f32_16x16x32_bf16 v[228:231], a[188:191], a[24:27], v[228:231]\n\t" "ds_read_b64_tr_b16 a[178:179], %[vr12] offset:43008\n\t"
    "v_mfma_f32_16x16x32_bf16 v[244:247], a[184:187], a[20:23], v[244:247]\n\t" "ds_read_b64_tr_b16 a[180:181], %[vr20] offset:8192\n\t"
    "v_mfma_f32_16x16x32_bf16 v[244:247], a[188:191], a[28:31], v[244:247]\n\t" "ds_read_b64_tr_b16 a[182:183], %[vr20] offset:10240\n\t" "s_waitcnt lgkmcnt(4)\n\t"
    "v_mfma_f32_16x16x32_bf16 v[232:235], a[192:195], a[16:19], v[232:235]\n\t" "ds_read_b64_tr_b16 a[184:185], %[vr13] offset:40960\n\t"
    "global_load_dwordx4 a[156:159], %[vx], s[40:41] offset:-1024 sc1\n\t" "v_mfma_f32_16x16x32_bf16 v[232:235], a[196:199], a[24:27], v[232:235]\n\t"
    "ds_read_b64_tr_b16 a[186:187], %[vr13] offset:43008\n\t" "s_add_u32 m0, %[sl0], 106496\n\t" "v_mfma_f32_16x16x32_bf16 v[248:251], a[192:195], a[20:23], v[248:251]\n\t"
    "ds_read_b64_tr_b16 a[188:189], %[vr21] offset:8192\n\t" "v_mfma_f32_16x16x32_bf16 v[248:251], a[196:199], a[28:31], v[248:251]\n\t" "ds_read_b64_tr_b16 a[190:191], %[vr21] offset:10240\n\t"
    "global_load_lds_dwordx4 %[vb0], %[sb]\n\t" "v_mfma_f32_16x16x32_bf16 v[236:239], a[200:203], a[16:19], v[236:239]\n\t" "ds_read_b64_tr_b16 a[192:193], %[vr14] offset:40960\n\t"
    "v_mfma_f32_16x16x32_bf16 v[236:239], a[204:207], a[24:27], v[236:239]\n\t" "ds_read_b64_tr_b16 a[194:195], %[vr14] offset:43008\n\t"
    "v_mfma_f32_16x16x32_bf16 v[252:255], a[200:203], a[20:23], v[252:255]\n\t" "ds_read_b64_tr_b16 a[196:197], %[vr22] offset:8192\n\t"
    "v_mfma_f32_16x16x32_bf16 v[252:255], a[204:207], a[28:31], v[252:255]\n\t" "ds_read_b64_tr_b16 a[198:199], %[vr22] offset:10240\n\t" "s_waitcnt lgkmcnt(4)\n\t"
    "v_mfma_f32_16x16x32_bf16 v[160:163], a[176:179], a[16:19], v[160:163]\n\t" "ds_read_b64_tr_b16 a[200:201], %[vr15] offset:40960\n\t" "global_load_dwordx4 a[160:163], %[vx], s[40:41] offset:0 sc1\n\t"
    "v_mfma_f32_16x16x32_bf16 v[160:163], a[180:183], a[24:27], v[160:163]\n\t" "ds_read_b64_tr_b16 a[202:203], %[vr15] offset:43008\n\t"
    "v_mfma_f32_16x16x32_bf16 v[164:167], a[176:179], a[20:23], v[164:167]\n\t" "ds_read_b64_tr_b16 a[204:205], %[vr23] offset:8192\n\t"
    "v_mfma_f32_16x16x32_bf16 v[164:167], a[180:183], a[28:31], v[164:167]\n\t" "ds_read_b64_tr_b16 a[206:207], %[vr23] offset:10240\n\t"
    "global_load_dwordx4 a[164:167], %[vx], s[40:41] offset:1024 sc1\n\t" "v_mfma_f32_16x16x32_bf16 v[168:171], a[184:187], a[16:19], v[168:171]\n\t" "s_waitcnt vmcnt(36)\n\t" "s_barrier\n\t"
    "ds_read_b64_tr_b16 a[176:177], %[vr8] offset:16384\n\t" "v_mfma_f32_16x16x32_bf16 v[168:171], a[188:191], a[24:27], v[168:171]\n\t" "ds_read_b64_tr_b16 a[178:179], %[vr8] offset:18432\n\t"
    "s_add_u32 m0, %[sl1], 147456\n\t" "v_mfma_f32_16x16x32_bf16 v[172:175], a[184:187], a[20:23], v[172:175]\n\t" "ds_read_b64_tr_b16 a[180:181], %[vr8] offset:24576\n\t"
    "v_mfma_f32_16x16x32_bf16 v[172:175], a[188:191], a[28:31], v[172:175]\n\t" "ds_read_b64_tr_b16 a[182:183], %[vr8] offset:26624\n\t" "s_waitcnt lgkmcnt(4)\n\t"
    "v_mfma_f32_16x16x32_bf16 v[176:179], a[192:195], a[16:19], v[176:179]\n\t" "ds_read_b64_tr_b16 a[184:185], %[vr9] offset:16384\n\t" "global_load_lds_dwordx4 %[vb1], %[sb]\n\t"
    "v_mfma_f32_16x16x32_bf16 v[176:179], a[196:199], a[24:27], v[176:179]\n\t" "ds_read_b64_tr_b16 a[186:187], %[vr9] offset:18432\n\t" "v_add_u32 %[vb1], 0x10000, %[vb1]\n\t"
    "v_mfma_f32_16x16x32_bf16 v[180:183], a[192:195], a[20:23], v[180:183]\n\t" "ds_read_b64_tr_b16 a[188:189], %[vr9] offset:24576\n\t"
    "v_mfma_f32_16x16x32_bf16 v[180:183], a[196:199], a[28:31], v[180:183]\n\t" "ds_read_b64_tr_b16 a[190:191], %[vr9] offset:26624\n\t"
    "global_load_dwordx4 a[168:171], %[vx], s[40:41] offset:2048 sc1\n\t" "v_mfma_f32_16x16x32_bf16 v[184:187], a[200:203], a[16:19], v[184:187]\n\t"
    "ds_read_b64_tr_b16 a[192:193], %[vr10] offset:16384\n\t" "v_mfma_f32_16x16x32_bf16 v[184:187], a[204:207], a[24:27], v[184:187]\n\t" "ds_read_b64_tr_b16 a[194:195], %[vr10] offset:18432\n\t"
    "v_mfma_f32_16x16x32_bf16 v[188:191], a[200:203], a[20:23], v[188:191]\n\t" "ds_read_b64_tr_b16 a[196:197], %[vr10] offset:24576\n\t"
    "v_mfma_f32_16x16x32_bf16 v[188:191], a[204:207], a[28:31], v[188:191]\n\t" "ds_read_b64_tr_b16 a[198:199], %[vr10] offset:26624\n\t" "s_waitcnt lgkmcnt(4) vmcnt(31)\n\t"
    "v_mfma_f32_16x16x32_bf16 v[224:227], a[176:179], a[80:83], v[224:227]\n\t" "ds_read_b64_tr_b16 a[200:201], %[vr11] offset:16384\n\t"
    "global_load_dwordx4 a[172:175], %[vx], s[40:41] offset:3072 sc1\n\t" "v_mfma_f32_16x16x32_bf16 v[224:227], a[180:183], a[88:91], v[224:227]\n\t"
    "ds_read_b64_tr_b16 a[202:203], %[vr11] offset:18432\n\t" "s_add_u32 m0, %[sl1], 155648\n\t" "v_mfma_f32_16x16x32_bf16 v[240:243], a[176:179], a[84:87], v[240:243]\n\t"
    "ds_read_b64_tr_b16 a[204:205], %[vr11] offset:24576\n\t" "global_load_lds_dwordx4 %[vb1], %[sb]\n\t" "v_mfma_f32_16x16x32_bf16 v[240:243], a[180:183], a[92:95], v[240:243]\n\t"
    "ds_read_b64_tr_b16 a[206:207], %[vr11] offset:26624\n\t" "v_add_u32 %[vb1], 0x10000, %[vb1]\n\t" "v_mfma_f32_16x16x32_bf16 v[228:231], a[184:187], a[80:83], v[228:231]\n\t" "s_waitcnt vmcnt(32)\n\t"
    "s_barrier\n\t" "ds_read_b64_tr_b16 a[176:177], %[vr0] offset:0\n\t" "v_mfma_f32_16x16x32_bf16 v[228:231], a[188:191], a[88:91], v[228:231]\n\t" "ds_read_b64_tr_b16 a[178:179], %[vr0] offset:2048\n\t"
    "s_add_u32 m0, %[sl1], 0\n\t" "v_mfma_f32_16x16x32_bf16 v[244:247], a[184:187], a[84:87], v[244:247]\n\t" "ds_read_b64_tr_b16 a[180:181], %[vr0] offset:8192\n\t"
    "global_load_lds_dwordx4 %[vb1], %[sb]\n\t" "v_mfma_f32_16x16x32_bf16 v[244:247], a[188:191], a[92:95], v[244:247]\n\t" "ds_read_b64_tr_b16 a[182:183], %[vr0] offset:10240\n\t"
    "s_waitcnt lgkmcnt(4)\n\t" "v_mfma_f32_16x16x32_bf16 v[232:235], a[192:195], a[80:83], v[232:235]\n\t" "ds_read_b64_tr_b16 a[184:185], %[vr1] offset:0\n\t" "v_add_u32 %[vb1], 0x10000, %[vb1]\n\t"
    "v_mfma_f32_16x16x32_bf16 v[232:235], a[196:199], a[88:91], v[232:235]\n\t" "ds_read_b64_tr_b16 a[186:187], %[vr1] offset:2048\n\t" "s_add_u32 m0, %[sl1], 8192\n\t"
    "v_mfma_f32_16x16x32_bf16 v[248:251], a[192:195], a[84:87], v[248:251]\n\t" "ds_read_b64_tr_b16 a[188:189], %[vr1] offset:8192\n\t" "global_load_lds_dwordx4 %[vb1], %[sb]\n\t"
    "v_mfma_f32_16x16x32_bf16 v[248:251], a[196:199], a[92:95], v[248:251]\n\t" "ds_read_b64_tr_b16 a[190:191], %[vr1] offset:10240\n\t" "v_add_u32 %[vb1], 0x10000, %[vb1]\n\t"
    "v_mfma_f32_16x16x32_bf16 v[236:239], a[200:203], a[80:83], v[236:239]\n\t" "ds_read_b64_tr_b16 a[192:193], %[vr2] offset:0\n\t" "s_add_u32 m0, %[sl1], 49152\n\t"
    "v_mfma_f32_16x16x32_bf16 v[236:239], a[204:207], a[88:91], v[236:239]\n\t" "ds_read_b64_tr_b16 a[194:195], %[vr2] offset:2048\n\t" "global_load_lds_dwordx4 %[vb1], %[sb]\n\t"
    "v_mfma_f32_16x16x32_bf16 v[252:255], a[200:203], a[84:87], v[252:255]\n\t" "ds_read_b64_tr_b16 a[196:197], %[vr2] offset:8192\n\t" "v_add_u32 %[vb1], 0x10000, %[vb1]\n\t"
    "v_mfma_f32_16x16x32_bf16 v[252:255], a[204:207], a[92:95], v[252:255]\n\t" "ds_read_b64_tr_b16 a[198:199], %[vr2] offset:10240\n\t" "s_waitcnt lgkmcnt(4) vmcnt(30)\n\t"
    "v_mfma_f32_16x16x32_bf16 v[224:227], a[176:179], a[96:99], v[224:227]\n\t" "ds_read_b64_tr_b16 a[200:201], %[vr3] offset:0\n\t" "s_add_u32 m0, %[sl1], 57344\n\t"
    "v_mfma_f32_16x16x32_bf16 v[224:227], a[180:183], a[104:107], v[224:227]\n\t" "ds_read_b64_tr_b16 a[202:203], %[vr3] offset:2048\n\t" "global_load_lds_dwordx4 %[vb1], %[sb]\n\t"
    "v_mfma_f32_16x16x32_bf16 v[240:243], a[176:179], a[100:103], v[240:243]\n\t" "ds_read_b64_tr_b16 a[204:205], %[vr3] offset:8192\n\t" "v_add_u32 %[vb1], 0x10000, %[vb1]\n\t"
    "v_mfma_f32_16x16x32_bf16 v[240:243], a[180:183], a[108:111], v[240:243]\n\t" "ds_read_b64_tr_b16 a[206:207], %[vr3] offset:10240\n\t" "s_add_u32 m0, %[sl1], 65536\n\t"
    "v_mfma_f32_16x16x32_bf16 v[228:231], a[184:187], a[96:99], v[228:231]\n\t" "s_waitcnt vmcnt(30)\n\t" "s_barrier\n\t" "ds_read_b64_tr_b16 a[176:177], %[vr0] offset:49152\n\t"
    "v_mfma_f32_16x16x32_bf16 v[228:231], a[188:191], a[104:107], v[228:231]\n\t" "ds_read_b64_tr_b16 a[178:179], %[vr0] offset:51200\n\t" "global_load_lds_dwordx4 %[vb1], %[sb]\n\t"
    "v_mfma_f32_16x16x32_bf16 v[244:247], a[184:187], a[100:103], v[244:247]\n\t" "ds_read_b64_tr_b16 a[180:181], %[vr0] offset:57344\n\t" "v_add_u32 %[vb1], 0x10000, %[vb1]\n\t"
    "v_mfma_f32_16x16x32_bf16 v[244:247], a[188:191], a[108:111], v[244:247]\n\t" "ds_read_b64_tr_b16 a[182:183], %[vr0] offset:59392\n\t" "s_waitcnt lgkmcnt(4)\n\t"
    "v_mfma_f32_16x16x32_bf16 v[232:235], a[192:195], a[96:99], v[232:235]\n\t" "ds_read_b64_tr_b16 a[184:185], %[vr1] offset:49152\n\t" "s_add_u32 m0, %[sl1], 73728\n\t"
    "v_mfma_f32_16x16x32_bf16 v[232:235], a[196:199], a[104:107], v[232:235]\n\t" "ds_read_b64_tr_b16 a[186:187], %[vr1] offset:51200\n\t" "global_load_lds_dwordx4 %[vb1], %[sb]\n\t"
    "v_mfma_f32_16x16x32_bf16 v[248:251], a[192:195], a[100:103], v[248:251]\n\t" "ds_read_b64_tr_b16 a[188:189], %[vr1] offset:57344\n\t" "v_add_u32 %[vb1], 0x10000, %[vb1]\n\t"
    "v_mfma_f32_16x16x32_bf16 v[248:251], a[196:199], a[108:111], v[248:251]\n\t" "ds_read_b64_tr_b16 a[190:191], %[vr1] offset:59392\n\t"
    "v_mfma_f32_16x16x32_bf16 v[236:239], a[200:203], a[96:99], v[236:239]\n\t" "ds_read_b64_tr_b16 a[192:193], %[vr2] offset:49152\n\t"
    "v_mfma_f32_16x16x32_bf16 v[236:239], a[204:207], a[104:107], v[236:239]\n\t" "ds_read_b64_tr_b16 a[194:195], %[vr2] offset:51200\n\t"
    "v_mfma_f32_16x16x32_bf16 v[252:255], a[200:203], a[100:103], v[252:255]\n\t" "ds_read_b64_tr_b16 a[196:197], %[vr2] offset:57344\n\t"
    "v_mfma_f32_16x16x32_bf16 v[252:255], a[204:207], a[108:111], v[252:255]\n\t" "ds_read_b64_tr_b16 a[198:199], %[vr2] offset:59392\n\t" "s_waitcnt lgkmcnt(4) vmcnt(27)\n\t"
    "v_mfma_f32_16x16x32_bf16 v[224:227], a[176:179], a[112:115], v[224:227]\n\t" "ds_read_b64_tr_b16 a[200:201], %[vr3] offset:49152\n\t"
    "v_mfma_f32_16x16x32_bf16 v[224:227], a[180:183], a[120:123], v[224:227]\n\t" "ds_read_b64_tr_b16 a[202:203], %[vr3] offset:51200\n\t"
    "v_mfma_f32_16x16x32_bf16 v[240:243], a[176:179], a[116:119], v[240:243]\n\t" "ds_read_b64_tr_b16 a[204:205], %[vr3] offset:57344\n\t"
    "v_mfma_f32_16x16x32_bf16 v[240:243], a[180:183], a[124:127], v[240:243]\n\t" "ds_read_b64_tr_b16 a[206:207], %[vr3] offset:59392\n\t"
    "v_mfma_f32_16x16x32_bf16 v[228:231], a[184:187], a[112:115], v[228:231]\n\t" "s_waitcnt vmcnt(26)\n\t" "s_barrier\n\t" "ds_read_b64_tr_b16 a[176:177], %[vr4] offset:0\n\t"
    "v_mfma_f32_16x16x32_bf16 v[228:231], a[188:191], a[120:123], v[228:231]\n\t" "ds_read_b64_tr_b16 a[178:179], %[vr4] offset:2048\n\t"
    "v_mfma_f32_16x16x32_bf16 v[244:247], a[184:187], a[116:119], v[244:247]\n\t" "ds_read_b64_tr_b16 a[180:181], %[vr4] offset:8192\n\t"
    "v_mfma_f32_16x16x32_bf16 v[244:247], a[188:191], a[124:127], v[244:247]\n\t" "ds_read_b64_tr_b16 a[182:183], %[vr4] offset:10240\n\t" "s_waitcnt lgkmcnt(4)\n\t"
    "v_mfma_f32_16x16x32_bf16 v[232:235], a[192:195], a[112:115], v[232:235]\n\t" "ds_read_b64_tr_b16 a[184:185], %[vr5] offset:0\n\t"
    "v_mfma_f32_16x16x32_bf16 v[232:235], a[196:199], a[120:123], v[232:235]\n\t" "ds_read_b64_tr_b16 a[186:187], %[vr5] offset:2048\n\t"
    "v_mfma_f32_16x16x32_bf16 v[248:251], a[192:195], a[116:119], v[248:251]\n\t" "ds_read_b64_tr_b16 a[188:189], %[vr5] offset:8192\n\t"
    "v_mfma_f32_16x16x32_bf16 v[248:251], a[196:199], a[124:127], v[248:251]\n\t" "ds_read_b64_tr_b16 a[190:191], %[vr5] offset:10240\n\t"
    "v_mfma_f32_16x16x32_bf16 v[236:239], a[200:203], a[112:115], v[236:239]\n\t" "ds_read_b64_tr_b16 a[192:193], %[vr6] offset:0\n\t"
    "v_mfma_f32_16x16x32_bf16 v[236:239], a[204:207], a[120:123], v[236:239]\n\t" "ds_read_b64_tr_b16 a[194:195], %[vr6] offset:2048\n\t"
    "v_mfma_f32_16x16x32_bf16 v[252:255], a[200:203], a[116:119], v[252:255]\n\t" "ds_read_b64_tr_b16 a[196:197], %[vr6] offset:8192\n\t"
    "v_mfma_f32_16x16x32_bf16 v[252:255], a[204:207], a[124:127], v[252:255]\n\t" "ds_read_b64_tr_b16 a[198:199], %[vr6] offset:10240\n\t" "s_waitcnt lgkmcnt(4) vmcnt(21)\n\t"
    "v_mfma_f32_16x16x32_bf16 v[224:227], a[176:179], a[128:131], v[224:227]\n\t" "ds_read_b64_tr_b16 a[200:201], %[vr7] offset:0\n\t"
    "v_mfma_f32_16x16x32_bf16 v[224:227], a[180:183], a[136:139], v[224:227]\n\t" "ds_read_b64_tr_b16 a[202:203], %[vr7] offset:2048\n\t"
    "v_mfma_f32_16x16x32_bf16 v[240:243], a[176:179], a[132:135], v[240:243]\n\t" "ds_read_b64_tr_b16 a[204:205], %[vr7] offset:8192\n\t"
    "v_mfma_f32_16x16x32_bf16 v[240:243], a[180:183], a[140:143], v[240:243]\n\t" "ds_read_b64_tr_b16 a[206:207], %[vr7] offset:10240\n\t"
    "v_mfma_f32_16x16x32_bf16 v[228:231], a[184:187], a[128:131], v[228:231]\n\t" "s_waitcnt vmcnt(20)\n\t" "s_barrier\n\t" "ds_read_b64_tr_b16 a[176:177], %[vr4] offset:16384\n\t"
    "v_mfma_f32_16x16x32_bf16 v[228:231], a[188:191], a[136:139], v[228:231]\n\t" "ds_read_b64_tr_b16 a[178:179], %[vr4] offset:18432\n\t"
    "v_mfma_f32_16x16x32_bf16 v[244:247], a[184:187], a[132:135], v[244:247]\n\t" "ds_read_b64_tr_b16 a[180:181], %[vr4] offset:24576\n\t"
    "v_mfma_f32_16x16x32_bf16 v[244:247], a[188:191], a[140:143], v[244:247]\n\t" "ds_read_b64_tr_b16 a[182:183], %[vr4] offset:26624\n\t" "s_waitcnt lgkmcnt(4)\n\t"
    "v_mfma_f32_16x16x32_bf16 v[232:235], a[192:195], a[128:131], v[232:235]\n\t" "ds_read_b64_tr_b16 a[184:185], %[vr5] offset:16384\n\t"
    "v_mfma_f32_16x16x32_bf16 v[232:235], a[196:199], a[136:139], v[232:235]\n\t" "ds_read_b64_tr_b16 a[186:187], %[vr5] offset:18432\n\t"
    "v_mfma_f32_16x16x32_bf16 v[248:251], a[192:195], a[132:135], v[248:251]\n\t" "ds_read_b64_tr_b16 a[188:189], %[vr5] offset:24576\n\t"
    "v_mfma_f32_16x16x32_bf16 v[248:251], a[196:199], a[140:143], v[248:251]\n\t" "ds_read_b64_tr_b16 a[190:191], %[vr5] offset:26624\n\t"
    "v_mfma_f32_16x16x32_bf16 v[236:239], a[200:203], a[128:131], v[236:239]\n\t" "ds_read_b64_tr_b16 a[192:193], %[vr6] offset:16384\n\t"
    "v_mfma_f32_16x16x32_bf16 v[236:239], a[204:207], a[136:139], v[236:239]\n\t" "ds_read_b64_tr_b16 a[194:195], %[vr6] offset:18432\n\t"
    "v_mfma_f32_16x16x32_bf16 v[252:255], a[200:203], a[132:135], v[252:255]\n\t" "ds_read_b64_tr_b16 a[196:197], %[vr6] offset:24576\n\t"
    "v_mfma_f32_16x16x32_bf16 v[252:255], a[204:207], a[140:143], v[252:255]\n\t" "ds_read_b64_tr_b16 a[198:199], %[vr6] offset:26624\n\t" "s_waitcnt lgkmcnt(4) vmcnt(13)\n\t"
    "v_mfma_f32_16x16x32_bf16 v[224:227], a[176:179], a[144:147], v[224:227]\n\t" "ds_read_b64_tr_b16 a[200:201], %[vr7] offset:16384\n\t"
    "v_mfma_f32_16x16x32_bf16 v[224:227], a[180:183], a[152:155], v[224:227]\n\t" "ds_read_b64_tr_b16 a[202:203], %[vr7] offset:18432\n\t"
    "v_mfma_f32_16x16x32_bf16 v[240:243], a[176:179], a[148:151], v[240:243]\n\t" "ds_read_b64_tr_b16 a[204:205], %[vr7] offset:24576\n\t"
    "v_mfma_f32_16x16x32_bf16 v[240:243], a[180:183], a[156:159], v[240:243]\n\t" "ds_read_b64_tr_b16 a[206:207], %[vr7] offset:26624\n\t"
    "v_mfma_f32_16x16x32_bf16 v[228:231], a[184:187], a[144:147], v[228:231]\n\t" "s_waitcnt vmcnt(12)\n\t" "s_barrier\n\t" "ds_read_b64_tr_b16 a[176:177], %[vr4] offset:32768\n\t"
    "v_mfma_f32_16x16x32_bf16 v[228:231], a[188:191], a[152:155], v[228:231]\n\t" "ds_read_b64_tr_b16 a[178:179], %[vr4] offset:34816\n\t"
    "v_mfma_f32_16x16x32_bf16 v[244:247], a[184:187], a[148:151], v[244:247]\n\t" "ds_read_b64_tr_b16 a[180:181], %[vr4] offset:40960\n\t"
    "v_mfma_f32_16x16x32_bf16 v[244:247], a[188:191], a[156:159], v[244:247]\n\t" "ds_read_b64_tr_b16 a[182:183], %[vr4] offset:43008\n\t" "s_waitcnt lgkmcnt(4)\n\t"
    "v_mfma_f32_16x16x32_bf16 v[232:235], a[192:195], a[144:147], v[232:235]\n\t" "ds_read_b64_tr_b16 a[184:185], %[vr5] offset:32768\n\t"
    "v_mfma_f32_16x16x32_bf16 v[232:235], a[196:199], a[152:155], v[232:235]\n\t" "ds_read_b64_tr_b16 a[186:187], %[vr5] offset:34816\n\t"
    "v_mfma_f32_16x16x32_bf16 v[248:251], a[192:195], a[148:151], v[248:251]\n\t" "ds_read_b64_tr_b16 a[188:189], %[vr5] offset:40960\n\t"
    "v_mfma_f32_16x16x32_bf16 v[248:251], a[196:199], a[156:159], v[248:251]\n\t" "ds_read_b64_tr_b16 a[190:191], %[vr5] offset:43008\n\t"
    "v_mfma_f32_16x16x32_bf16 v[236:239], a[200:203], a[144:147], v[236:239]\n\t" "ds_read_b64_tr_b16 a[192:193], %[vr6] offset:32768\n\t"
    "v_mfma_f32_16x16x32_bf16 v[236:239], a[204:207], a[152:155], v[236:239]\n\t" "ds_read_b64_tr_b16 a[194:195], %[vr6] offset:34816\n\t"
    "v_mfma_f32_16x16x32_bf16 v[252:255], a[200:203], a[148:151], v[252:255]\n\t" "ds_read_b64_tr_b16 a[196:197], %[vr6] offset:40960\n\t"
    "v_mfma_f32_16x16x32_bf16 v[252:255], a[204:207], a[156:159], v[252:255]\n\t" "ds_read_b64_tr_b16 a[198:199], %[vr6] offset:43008\n\t" "s_waitcnt lgkmcnt(4) vmcnt(7)\n\t"
    "v_mfma_f32_16x16x32_bf16 v[224:227], a[176:179], a[160:163], v[224:227]\n\t" "ds_read_b64_tr_b16 a[200:201], %[vr7] offset:32768\n\t"
    "v_mfma_f32_16x16x32_bf16 v[224:227], a[180:183], a[168:171], v[224:227]\n\t" "ds_read_b64_tr_b16 a[202:203], %[vr7] offset:34816\n\t"
    "v_mfma_f32_16x16x32_bf16 v[240:243], a[176:179], a[164:167], v[240:243]\n\t" "ds_read_b64_tr_b16 a[204:205], %[vr7] offset:40960\n\t"
    "v_mfma_f32_16x16x32_bf16 v[240:243], a[180:183], a[172:175], v[240:243]\n\t" "ds_read_b64_tr_b16 a[206:207], %[vr7] offset:43008\n\t"
    "v_mfma_f32_16x16x32_bf16 v[228:231], a[184:187], a[160:163], v[228:231]\n\t" "s_waitcnt vmcnt(6)\n\t" "s_barrier\n\t" "ds_read_b64_tr_b16 a[176:177], %[vr20] offset:16384\n\t"
    "v_mfma_f32_16x16x32_bf16 v[228:231], a[188:191], a[168:171], v[228:231]\n\t" "ds_read_b64_tr_b16 a[178:179], %[vr20] offset:18432\n\t"
    "v_mfma_f32_16x16x32_bf16 v[244:247], a[184:187], a[164:167], v[244:247]\n\t" "ds_read_b64_tr_b16 a[180:181], %[vr20] offset:24576\n\t"
    "v_mfma_f32_16x16x32_bf16 v[244:247], a[188:191], a[172:175], v[244:247]\n\t" "ds_read_b64_tr_b16 a[182:183], %[vr20] offset:26624\n\t" "s_waitcnt lgkmcnt(4)\n\t"
    "v_mfma_f32_16x16x32_bf16 v[232:235], a[192:195], a[160:163], v[232:235]\n\t" "ds_read_b64_tr_b16 a[184:185], %[vr21] offset:16384\n\t"
    "v_mfma_f32_16x16x32_bf16 v[232:235], a[196:199], a[168:171], v[232:235]\n\t" "ds_read_b64_tr_b16 a[186:187], %[vr21] offset:18432\n\t"
    "v_mfma_f32_16x16x32_bf16 v[248:251], a[192:195], a[164:167], v[248:251]\n\t" "ds_read_b64_tr_b16 a[188:189], %[vr21] offset:24576\n\t"
    "v_mfma_f32_16x16x32_bf16 v[248:251], a[196:199], a[172:175], v[248:251]\n\t" "ds_read_b64_tr_b16 a[190:191], %[vr21] offset:26624\n\t"
    "v_mfma_f32_16x16x32_bf16 v[236:239], a[200:203], a[160:163], v[236:239]\n\t" "ds_read_b64_tr_b16 a[192:193], %[vr22] offset:16384\n\t"
    "v_mfma_f32_16x16x32_bf16 v[236:239], a[204:207], a[168:171], v[236:239]\n\t" "ds_read_b64_tr_b16 a[194:195], %[vr22] offset:18432\n\t"
    "v_mfma_f32_16x16x32_bf16 v[252:255], a[200:203], a[164:167], v[252:255]\n\t" "ds_read_b64_tr_b16 a[196:197], %[vr22] offset:24576\n\t"
    "v_mfma_f32_16x16x32_bf16 v[252:255], a[204:207], a[172:175], v[252:255]\n\t" "ds_read_b64_tr_b16 a[198:199], %[vr22] offset:26624\n\t" "s_waitcnt lgkmcnt(4)\n\t"
    "v_mfma_f32_16x16x32_bf16 v[160:163], a[176:179], a[80:83], v[160:163]\n\t" "ds_read_b64_tr_b16 a[200:201], %[vr23] offset:16384\n\t"
    "v_mfma_f32_16x16x32_bf16 v[160:163], a[180:183], a[88:91], v[160:163]\n\t" "ds_read_b64_tr_b16 a[202:203], %[vr23] offset:18432\n\t"
    "v_mfma_f32_16x16x32_bf16 v[164:167], a[176:179], a[84:87], v[164:167]\n\t" "ds_read_b64_tr_b16 a[204:205], %[vr23] offset:24576\n\t"
    "v_mfma_f32_16x16x32_bf16 v[164:167], a[180:183], a[92:95], v[164:167]\n\t" "ds_read_b64_tr_b16 a[206:207], %[vr23] offset:26624\n\t" "v_cvt_pk_bf16_f32 v224, v224, v225\n\t"
    "v_cvt_pk_bf16_f32 v225, v226, v227\n\t" "v_add3_u32 v207, %[t32], v224, v225\n\t" "v_mfma_f32_16x16x32_bf16 v[168:171], a[184:187], a[80:83], v[168:171]\n\t" "s_waitcnt vmcnt(4)\n\t" "s_barrier\n\t"
    "ds_read_b64_tr_b16 a[176:177], %[vr12] offset:0\n\t" "v_cvt_pk_bf16_f32 v226, v228, v229\n\t" "v_cvt_pk_bf16_f32 v227, v230, v231\n\t" "v_add3_u32 v207, v207, v226, v227\n\t"
    "v_mfma_f32_16x16x32_bf16 v[168:171], a[188:191], a[88:91], v[168:171]\n\t" "ds_read_b64_tr_b16 a[178:179], %[vr12] offset:2048\n\t" "global_store_dwordx4 %[vx], v[224:227], %[sm] offset:0 sc1\n\t"
    "v_cvt_pk_bf16_f32 v232, v232, v233\n\t" "v_cvt_pk_bf16_f32 v233, v234, v235\n\t" "v_add3_u32 v207, v207, v232, v233\n\t" "v_mfma_f32_16x16x32_bf16 v[172:175], a[184:187], a[84:87], v[172:175]\n\t"
    "ds_read_b64_tr_b16 a[180:181], %[vr12] offset:8192\n\t" "v_cvt_pk_bf16_f32 v234, v236, v237\n\t" "v_cvt_pk_bf16_f32 v235, v238, v239\n\t" "v_add3_u32 v207, v207, v234, v235\n\t"
    "v_mfma_f32_16x16x32_bf16 v[172:175], a[188:191], a[92:95], v[172:175]\n\t" "ds_read_b64_tr_b16 a[182:183], %[vr12] offset:10240\n\t"
    "global_store_dwordx4 %[vx], v[232:235], %[sm] offset:1024 sc1\n\t" "v_cvt_pk_bf16_f32 v240, v240, v241\n\t" "v_cvt_pk_bf16_f32 v241, v242, v243\n\t" "v_add3_u32 v207, v207, v240, v241\n\t"
    "s_waitcnt lgkmcnt(4)\n\t" "v_mfma_f32_16x16x32_bf16 v[176:179], a[192:195], a[80:83], v[176:179]\n\t" "ds_read_b64_tr_b16 a[184:185], %[vr13] offset:0\n\t" "v_cvt_pk_bf16_f32 v242, v244, v245\n\t"
    "v_cvt_pk_bf16_f32 v243, v246, v247\n\t" "v_add3_u32 v207, v207, v242, v243\n\t" "v_mfma_f32_16x16x32_bf16 v[176:179], a[196:199], a[88:91], v[176:179]\n\t"
    "ds_read_b64_tr_b16 a[186:187], %[vr13] offset:2048\n\t" "global_store_dwordx4 %[vx], v[240:243], %[sm] offset:2048 sc1\n\t" "v_cvt_pk_bf16_f32 v248, v248, v249\n\t"
    "v_cvt_pk_bf16_f32 v249, v250, v251\n\t" "v_add3_u32 v207, v207, v248, v249\n\t" "v_mfma_f32_16x16x32_bf16 v[180:183], a[192:195], a[84:87], v[180:183]\n\t"
    "ds_read_b64_tr_b16 a[188:189], %[vr13] offset:8192\n\t" "v_cvt_pk_bf16_f32 v250, v252, v253\n\t" "v_cvt_pk_bf16_f32 v251, v254, v255\n\t" "v_add3_u32 v207, v207, v250, v251\n\t"
    "v_mfma_f32_16x16x32_bf16 v[180:183], a[196:199], a[92:95], v[180:183]\n\t" "ds_read_b64_tr_b16 a[190:191], %[vr13] offset:10240\n\t"
    "global_store_dwordx4 %[vx], v[248:251], %[sm] offset:3072 sc1\n\t" "v_mfma_f32_16x16x32_bf16 v[184:187], a[200:203], a[80:83], v[184:187]\n\t" "ds_read_b64_tr_b16 a[192:193], %[vr14] offset:0\n\t"
    "global_store_dword %[vc], v207, %[sm] sc1\n\t" "v_mfma_f32_16x16x32_bf16 v[184:187], a[204:207], a[88:91], v[184:187]\n\t" "ds_read_b64_tr_b16 a[194:195], %[vr14] offset:2048\n\t"
    "s_add_u32 m0, %[sl1], 81920\n\t" "v_mfma_f32_16x16x32_bf16 v[188:191], a[200:203], a[84:87], v[188:191]\n\t" "ds_read_b64_tr_b16 a[196:197], %[vr14] offset:8192\n\t"
    "global_load_lds_dwordx4 %[vb1], %[sb]\n\t" "v_mfma_f32_16x16x32_bf16 v[188:191], a[204:207], a[92:95], v[188:191]\n\t" "ds_read_b64_tr_b16 a[198:199], %[vr14] offset:10240\n\t"
    "s_waitcnt lgkmcnt(4)\n\t" "v_mfma_f32_16x16x32_bf16 v[160:163], a[176:179], a[96:99], v[160:163]\n\t" "ds_read_b64_tr_b16 a[200:201], %[vr15] offset:0\n\t" "v_add_u32 %[vb1], 0x10000, %[vb1]\n\t"
    "v_mfma_f32_16x16x32_bf16 v[160:163], a[180:183], a[104:107], v[160:163]\n\t" "ds_read_b64_tr_b16 a[202:203], %[vr15] offset:2048\n\t" "s_add_u32 m0, %[sl1], 90112\n\t"
    "v_mfma_f32_16x16x32_bf16 v[164:167], a[176:179], a[100:103], v[164:167]\n\t" "ds_read_b64_tr_b16 a[204:205], %[vr15] offset:8192\n\t" "global_load_lds_dwordx4 %[vb1], %[sb]\n\t"
    "v_mfma_f32_16x16x32_bf16 v[164:167], a[180:183], a[108:111], v[164:167]\n\t" "ds_read_b64_tr_b16 a[206:207], %[vr15] offset:10240\n\t" "v_add_u32 %[vb1], 0x10000, %[vb1]\n\t"
    "v_mfma_f32_16x16x32_bf16 v[168:171], a[184:187], a[96:99], v[168:171]\n\t" "s_waitcnt vmcnt(9)\n\t" "s_barrier\n\t" "ds_read_b64_tr_b16 a[176:177], %[vr12] offset:49152\n\t"
    "v_mfma_f32_16x16x32_bf16 v[168:171], a[188:191], a[104:107], v[168:171]\n\t" "ds_read_b64_tr_b16 a[178:179], %[vr12] offset:51200\n\t" "s_add_u32 m0, %[sl1], 98304\n\t"
    "v_mfma_f32_16x16x32_bf16 v[172:175], a[184:187], a[100:103], v[172:175]\n\t" "ds_read_b64_tr_b16 a[180:181], %[vr12] offset:57344\n\t" "global_load_lds_dwordx4 %[vb1], %[sb]\n\t"
    "v_mfma_f32_16x16x32_bf16 v[172:175], a[188:191], a[108:111], v[172:175]\n\t" "ds_read_b64_tr_b16 a[182:183], %[vr12] offset:59392\n\t" "s_waitcnt lgkmcnt(4)\n\t"
    "v_mfma_f32_16x16x32_bf16 v[176:179], a[192:195], a[96:99], v[176:179]\n\t" "ds_read_b64_tr_b16 a[184:185], %[vr13] offset:49152\n\t" "v_add_u32 %[vb1], 0x10000, %[vb1]\n\t"
    "v_mfma_f32_16x16x32_bf16 v[176:179], a[196:199], a[104:107], v[176:179]\n\t" "ds_read_b64_tr_b16 a[186:187], %[vr13] offset:51200\n\t" "s_add_u32 m0, %[sl1], 106496\n\t"
    "v_mfma_f32_16x16x32_bf16 v[180:183], a[192:195], a[100:103], v[180:183]\n\t" "ds_read_b64_tr_b16 a[188:189], %[vr13] offset:57344\n\t" "global_load_lds_dwordx4 %[vb1], %[sb]\n\t"
    "v_mfma_f32_16x16x32_bf16 v[180:183], a[196:199], a[108:111], v[180:183]\n\t" "ds_read_b64_tr_b16 a[190:191], %[vr13] offset:59392\n\t"
    "v_mfma_f32_16x16x32_bf16 v[184:187], a[200:203], a[96:99], v[184:187]\n\t" "ds_read_b64_tr_b16 a[192:193], %[vr14] offset:49152\n\t"
    "v_mfma_f32_16x16x32_bf16 v[184:187], a[204:207], a[104:107], v[184:187]\n\t" "ds_read_b64_tr_b16 a[194:195], %[vr14] offset:51200\n\t"
    "v_mfma_f32_16x16x32_bf16 v[188:191], a[200:203], a[100:103], v[188:191]\n\t" "ds_read_b64_tr_b16 a[196:197], %[vr14] offset:57344\n\t"
    "v_mfma_f32_16x16x32_bf16 v[188:191], a[204:207], a[108:111], v[188:191]\n\t" "ds_read_b64_tr_b16 a[198:199], %[vr14] offset:59392\n\t" "s_waitcnt lgkmcnt(4)\n\t"
    "v_mfma_f32_16x16x32_bf16 v[160:163], a[176:179], a[112:115], v[160:163]\n\t" "ds_read_b64_tr_b16 a[200:201], %[vr15] offset:49152\n\t"
    "v_mfma_f32_16x16x32_bf16 v[160:163], a[180:183], a[120:123], v[160:163]\n\t" "ds_read_b64_tr_b16 a[202:203], %[vr15] offset:51200\n\t"
    "v_mfma_f32_16x16x32_bf16 v[164:167], a[176:179], a[116:119], v[164:167]\n\t" "ds_read_b64_tr_b16 a[204:205], %[vr15] offset:57344\n\t"
    "v_mfma_f32_16x16x32_bf16 v[164:167], a[180:183], a[124:127], v[164:167]\n\t" "ds_read_b64_tr_b16 a[206:207], %[vr15] offset:59392\n\t"
    "v_mfma_f32_16x16x32_bf16 v[168:171], a[184:187], a[112:115], v[168:171]\n\t" "s_waitcnt vmcnt(9)\n\t" "s_barrier\n\t" "ds_read_b64_tr_b16 a[176:177], %[vr16] offset:0\n\t"
    "v_mfma_f32_16x16x32_bf16 v[168:171], a[188:191], a[120:123], v[168:171]\n\t" "ds_read_b64_tr_b16 a[178:179], %[vr16] offset:2048\n\t"
    "v_mfma_f32_16x16x32_bf16 v[172:175], a[184:187], a[116:119], v[172:175]\n\t" "ds_read_b64_tr_b16 a[180:181], %[vr16] offset:8192\n\t"
    "v_mfma_f32_16x16x32_bf16 v[172:175], a[188:191], a[124:127], v[172:175]\n\t" "ds_read_b64_tr_b16 a[182:183], %[vr16] offset:10240\n\t" "s_waitcnt lgkmcnt(4)\n\t"
    "v_mfma_f32_16x16x32_bf16 v[176:179], a[192:195], a[112:115], v[176:179]\n\t" "ds_read_b64_tr_b16 a[184:185], %[vr17] offset:0\n\t"
    "v_mfma_f32_16x16x32_bf16 v[176:179], a[196:199], a[120:123], v[176:179]\n\t" "ds_read_b64_tr_b16 a[186:187], %[vr17] offset:2048\n\t"
    "v_mfma_f32_16x16x32_bf16 v[180:183], a[192:195], a[116:119], v[180:183]\n\t" "ds_read_b64_tr_b16 a[188:189], %[vr17] offset:8192\n\t"
    "v_mfma_f32_16x16x32_bf16 v[180:183], a[196:199], a[124:127], v[180:183]\n\t" "ds_read_b64_tr_b16 a[190:191], %[vr17] offset:10240\n\t"
    "v_mfma_f32_16x16x32_bf16 v[184:187], a[200:203], a[112:115], v[184:187]\n\t" "ds_read_b64_tr_b16 a[192:193], %[vr18] offset:0\n\t"
    "v_mfma_f32_16x16x32_bf16 v[184:187], a[204:207], a[120:123], v[184:187]\n\t" "ds_read_b64_tr_b16 a[194:195], %[vr18] offset:2048\n\t"
    "v_mfma_f32_16x16x32_bf16 v[188:191], a[200:203], a[116:119], v[188:191]\n\t" "ds_read_b64_tr_b16 a[196:197], %[vr18] offset:8192\n\t"
    "v_mfma_f32_16x16x32_bf16 v[188:191], a[204:207], a[124:127], v[188:191]\n\t" "ds_read_b64_tr_b16 a[198:199], %[vr18] offset:10240\n\t" "s_waitcnt lgkmcnt(4)\n\t"
    "v_mfma_f32_16x16x32_bf16 v[160:163], a[176:179], a[128:131], v[160:163]\n\t" "ds_read_b64_tr_b16 a[200:201], %[vr19] offset:0\n\t"
    "v_mfma_f32_16x16x32_bf16 v[160:163], a[180:183], a[136:139], v[160:163]\n\t" "ds_read_b64_tr_b16 a[202:203], %[vr19] offset:2048\n\t"
    "v_mfma_f32_16x16x32_bf16 v[164:167], a[176:179], a[132:135], v[164:167]\n\t" "ds_read_b64_tr_b16 a[204:205], %[vr19] offset:8192\n\t"
    "v_mfma_f32_16x16x32_bf16 v[164:167], a[180:183], a[140:143], v[164:167]\n\t" "ds_read_b64_tr_b16 a[206:207], %[vr19] offset:10240\n\t"
    "v_mfma_f32_16x16x32_bf16 v[168:171], a[184:187], a[128:131], v[168:171]\n\t" "s_waitcnt vmcnt(2)\n\t" "s_barrier\n\t" "ds_read_b64_tr_b16 a[176:177], %[vr16] offset:16384\n\t"
    "v_mfma_f32_16x16x32_bf16 v[168:171], a[188:191], a[136:139], v[168:171]\n\t" "ds_read_b64_tr_b16 a[178:179], %[vr16] offset:18432\n\t"
    "v_mfma_f32_16x16x32_bf16 v[172:175], a[184:187], a[132:135], v[172:175]\n\t" "ds_read_b64_tr_b16 a[180:181], %[vr16] offset:24576\n\t"
    "v_mfma_f32_16x16x32_bf16 v[172:175], a[188:191], a[140:143], v[172:175]\n\t" "ds_read_b64_tr_b16 a[182:183], %[vr16] offset:26624\n\t" "s_waitcnt lgkmcnt(4)\n\t"
    "v_mfma_f32_16x16x32_bf16 v[176:179], a[192:195], a[128:131], v[176:179]\n\t" "ds_read_b64_tr_b16 a[184:185], %[vr17] offset:16384\n\t"
    "v_mfma_f32_16x16x32_bf16 v[176:179], a[196:199], a[136:139], v[176:179]\n\t" "ds_read_b64_tr_b16 a[186:187], %[vr17] offset:18432\n\t"
    "v_mfma_f32_16x16x32_bf16 v[180:183], a[192:195], a[132:135], v[180:183]\n\t" "ds_read_b64_tr_b16 a[188:189], %[vr17] offset:24576\n\t"
    "v_mfma_f32_16x16x32_bf16 v[180:183], a[196:199], a[140:143], v[180:183]\n\t" "ds_read_b64_tr_b16 a[190:191], %[vr17] offset:26624\n\t"
    "v_mfma_f32_16x16x32_bf16 v[184:187], a[200:203], a[128:131], v[184:187]\n\t" "ds_read_b64_tr_b16 a[192:193], %[vr18] offset:16384\n\t"
    "v_mfma_f32_16x16x32_bf16 v[184:187], a[204:207], a[136:139], v[184:187]\n\t" "ds_read_b64_tr_b16 a[194:195], %[vr18] offset:18432\n\t"
    "v_mfma_f32_16x16x32_bf16 v[188:191], a[200:203], a[132:135], v[188:191]\n\t" "ds_read_b64_tr_b16 a[196:197], %[vr18] offset:24576\n\t"
    "v_mfma_f32_16x16x32_bf16 v[188:191], a[204:207], a[140:143], v[188:191]\n\t" "ds_read_b64_tr_b16 a[198:199], %[vr18] offset:26624\n\t" "s_waitcnt lgkmcnt(4)\n\t"
    "v_mfma_f32_16x16x32_bf16 v[160:163], a[176:179], a[144:147], v[160:163]\n\t" "ds_read_b64_tr_b16 a[200:201], %[vr19] offset:16384\n\t" "global_load_dwordx4 v[208:211], %[vx], %[st] offset:0 sc1\n\t"
    "v_mfma_f32_16x16x32_bf16 v[160:163], a[180:183], a[152:155], v[160:163]\n\t" "ds_read_b64_tr_b16 a[202:203], %[vr19] offset:18432\n\t"
    "v_mfma_f32_16x16x32_bf16 v[164:167], a[176:179], a[148:151], v[164:167]\n\t" "ds_read_b64_tr_b16 a[204:205], %[vr19] offset:24576\n\t"
    "global_load_dwordx4 v[212:215], %[vx], %[st] offset:1024 sc1\n\t" "v_mfma_f32_16x16x32_bf16 v[164:167], a[180:183], a[156:159], v[164:167]\n\t"
    "ds_read_b64_tr_b16 a[206:207], %[vr19] offset:26624\n\t" "v_mfma_f32_16x16x32_bf16 v[168:171], a[184:187], a[144:147], v[168:171]\n\t" "s_waitcnt vmcnt(2)\n\t" "s_barrier\n\t"
    "ds_read_b64_tr_b16 a[176:177], %[vr16] offset:32768\n\t" "global_load_dwordx4 v[216:219], %[vx], %[st] offset:2048 sc1\n\t"
    "v_mfma_f32_16x16x32_bf16 v[168:171], a[188:191], a[152:155], v[168:171]\n\t" "ds_read_b64_tr_b16 a[178:179], %[vr16] offset:34816\n\t"
    "v_mfma_f32_16x16x32_bf16 v[172:175], a[184:187], a[148:151], v[172:175]\n\t" "ds_read_b64_tr_b16 a[180:181], %[vr16] offset:40960\n\t"
    "global_load_dwordx4 v[220:223], %[vx], %[st] offset:3072 sc1\n\t" "v_mfma_f32_16x16x32_bf16 v[172:175], a[188:191], a[156:159], v[172:175]\n\t"
    "ds_read_b64_tr_b16 a[182:183], %[vr16] offset:43008\n\t" "global_load_dword v206, %[vc], %[st] sc1\n\t" "s_waitcnt lgkmcnt(4)\n\t"
    "v_mfma_f32_16x16x32_bf16 v[176:179], a[192:195], a[144:147], v[176:179]\n\t" "ds_read_b64_tr_b16 a[184:185], %[vr17] offset:32768\n\t"
    "v_mfma_f32_16x16x32_bf16 v[176:179], a[196:199], a[152:155], v[176:179]\n\t" "ds_read_b64_tr_b16 a[186:187], %[vr17] offset:34816\n\t"
    "v_mfma_f32_16x16x32_bf16 v[180:183], a[192:195], a[148:151], v[180:183]\n\t" "ds_read_b64_tr_b16 a[188:189], %[vr17] offset:40960\n\t"
    "v_mfma_f32_16x16x32_bf16 v[180:183], a[196:199], a[156:159], v[180:183]\n\t" "ds_read_b64_tr_b16 a[190:191], %[vr17] offset:43008\n\t"
    "v_mfma_f32_16x16x32_bf16 v[184:187], a[200:203], a[144:147], v[184:187]\n\t" "ds_read_b64_tr_b16 a[192:193], %[vr18] offset:32768\n\t"
    "v_mfma_f32_16x16x32_bf16 v[184:187], a[204:207], a[152:155], v[184:187]\n\t" "ds_read_b64_tr_b16 a[194:195], %[vr18] offset:34816\n\t"
    "v_mfma_f32_16x16x32_bf16 v[188:191], a[200:203], a[148:151], v[188:191]\n\t" "ds_read_b64_tr_b16 a[196:197], %[vr18] offset:40960\n\t"
    "v_mfma_f32_16x16x32_bf16 v[188:191], a[204:207], a[156:159], v[188:191]\n\t" "ds_read_b64_tr_b16 a[198:199], %[vr18] offset:43008\n\t" "s_waitcnt lgkmcnt(4)\n\t"
    "v_mfma_f32_16x16x32_bf16 v[160:163], a[176:179], a[160:163], v[160:163]\n\t" "ds_read_b64_tr_b16 a[200:201], %[vr19] offset:32768\n\t"
    "v_mfma_f32_16x16x32_bf16 v[160:163], a[180:183], a[168:171], v[160:163]\n\t" "ds_read_b64_tr_b16 a[202:203], %[vr19] offset:34816\n\t"
    "v_mfma_f32_16x16x32_bf16 v[164:167], a[176:179], a[164:167], v[164:167]\n\t" "ds_read_b64_tr_b16 a[204:205], %[vr19] offset:40960\n\t"
    "v_mfma_f32_16x16x32_bf16 v[164:167], a[180:183], a[172:175], v[164:167]\n\t" "ds_read_b64_tr_b16 a[206:207], %[vr19] offset:43008\n\t"
    "v_mfma_f32_16x16x32_bf16 v[168:171], a[184:187], a[160:163], v[168:171]\n\t" "v_mfma_f32_16x16x32_bf16 v[168:171], a[188:191], a[168:171], v[168:171]\n\t"
    "v_mfma_f32_16x16x32_bf16 v[172:175], a[184:187], a[164:167], v[172:175]\n\t" "v_mfma_f32_16x16x32_bf16 v[172:175], a[188:191], a[172:175], v[172:175]\n\t" "s_waitcnt lgkmcnt(0)\n\t"
    "v_mfma_f32_16x16x32_bf16 v[176:179], a[192:195], a[160:163], v[176:179]\n\t" "v_mfma_f32_16x16x32_bf16 v[176:179], a[196:199], a[168:171], v[176:179]\n\t"
    "v_mfma_f32_16x16x32_bf16 v[180:183], a[192:195], a[164:167], v[180:183]\n\t" "v_mfma_f32_16x16x32_bf16 v[180:183], a[196:199], a[172:175], v[180:183]\n\t"
    "v_mfma_f32_16x16x32_bf16 v[184:187], a[200:203], a[160:163], v[184:187]\n\t" "v_mfma_f32_16x16x32_bf16 v[184:187], a[204:207], a[168:171], v[184:187]\n\t"
    "v_mfma_f32_16x16x32_bf16 v[188:191], a[200:203], a[164:167], v[188:191]\n\t" "v_mfma_f32_16x16x32_bf16 v[188:191], a[204:207], a[172:175], v[188:191]\n\t" "s_mov_b32 s44, 0x400000\n\t"
    ".Lpoll_%=:\n\t" "s_waitcnt vmcnt(0)\n\t" "v_add3_u32 v224, %[t32], v208, v209\n\t" "v_add3_u32 v224, v224, v210, v211\n\t" "v_add3_u32 v224, v224, v212, v213\n\t"
    "v_add3_u32 v224, v224, v214, v215\n\t" "v_add3_u32 v224, v224, v216, v217\n\t" "v_add3_u32 v224, v224, v218, v219\n\t" "v_add3_u32 v224, v224, v220, v221\n\t" "v_add3_u32 v224, v224, v222, v223\n\t"
    "v_cmp_ne_u32_e64 vcc, v206, v224\n\t" "s_nop 4\n\t" "s_cbranch_vccz .Lrdy_%=\n\t" "global_load_dwordx4 v[208:211], %[vx], %[st] offset:0 sc1\n\t"
    "global_load_dwordx4 v[212:215], %[vx], %[st] offset:1024 sc1\n\t" "global_load_dwordx4 v[216:219], %[vx], %[st] offset:2048 sc1\n\t" "global_load_dwordx4 v[220:223], %[vx], %[st] offset:3072 sc1\n\t"
    "global_load_dword v206, %[vc], %[st] sc1\n\t" "s_sub_u32 s44, s44, 1\n\t" "s_cbranch_scc0 .Lpoll_%=\n\t" "s_waitcnt vmcnt(0)\n\t" ".Lrdy_%=:\n\t"
    "v_mfma_f32_4x4x4_16b_bf16 v[160:163], %[id], v[208:209], v[160:163]\n\t" "v_mfma_f32_4x4x4_16b_bf16 v[168:171], %[id], v[210:211], v[168:171]\n\t"
    "v_mfma_f32_4x4x4_16b_bf16 v[176:179], %[id], v[212:213], v[176:179]\n\t" "v_mfma_f32_4x4x4_16b_bf16 v[184:187], %[id], v[214:215], v[184:187]\n\t"
    "v_mfma_f32_4x4x4_16b_bf16 v[164:167], %[id], v[216:217], v[164:167]\n\t" "v_mfma_f32_4x4x4_16b_bf16 v[172:175], %[id], v[218:219], v[172:175]\n\t"
    "v_mfma_f32_4x4x4_16b_bf16 v[180:183], %[id], v[220:221], v[180:183]\n\t" "v_mfma_f32_4x4x4_16b_bf16 v[188:191], %[id], v[222:223], v[188:191]\n\t" "s_mov_b64 s[40:41], %[so]\n\t"
    "v_pk_fma_f32 v[160:161], v[112:113], v[160:161], v[128:129]\n\t" "v_pk_fma_f32 v[162:163], v[114:115], v[162:163], v[130:131]\n\t" "v_cvt_pk_bf16_f32 v160, v160, v161\n\t"
    "v_cvt_pk_bf16_f32 v161, v162, v163\n\t" "ds_write_b64 %[tw0], v[160:161] offset:0\n\t" "v_pk_fma_f32 v[168:169], v[116:117], v[168:169], v[136:137]\n\t"
    "v_pk_fma_f32 v[170:171], v[118:119], v[170:171], v[138:139]\n\t" "v_cvt_pk_bf16_f32 v168, v168, v169\n\t" "v_cvt_pk_bf16_f32 v169, v170, v171\n\t" "ds_write_b64 %[tw1], v[168:169] offset:0\n\t"
    "v_pk_fma_f32 v[176:177], v[120:121], v[176:177], v[144:145]\n\t" "v_pk_fma_f32 v[178:179], v[122:123], v[178:179], v[146:147]\n\t" "v_cvt_pk_bf16_f32 v176, v176, v177\n\t"
    "v_cvt_pk_bf16_f32 v177, v178, v179\n\t" "ds_write_b64 %[tw2], v[176:177] offset:0\n\t" "v_pk_fma_f32 v[184:185], v[124:125], v[184:185], v[152:153]\n\t"
    "v_pk_fma_f32 v[186:187], v[126:127], v[186:187], v[154:155]\n\t" "v_cvt_pk_bf16_f32 v184, v184, v185\n\t" "v_cvt_pk_bf16_f32 v185, v186, v187\n\t" "ds_write_b64 %[tw3], v[184:185] offset:0\n\t"
    "ds_read_b128 v[224:227], %[tr] offset:0\n\t" "ds_read_b128 v[228:231], %[tr] offset:1024\n\t" "v_pk_fma_f32 v[164:165], v[112:113], v[164:165], v[132:133]\n\t"
    "v_pk_fma_f32 v[166:167], v[114:115], v[166:167], v[134:135]\n\t" "v_cvt_pk_bf16_f32 v164, v164, v165\n\t" "v_cvt_pk_bf16_f32 v165, v166, v167\n\t" "ds_write_b64 %[tw0], v[164:165] offset:2048\n\t"
    "v_pk_fma_f32 v[172:173], v[116:117], v[172:173], v[140:141]\n\t" "v_pk_fma_f32 v[174:175], v[118:119], v[174:175], v[142:143]\n\t" "v_cvt_pk_bf16_f32 v172, v172, v173\n\t"
    "v_cvt_pk_bf16_f32 v173, v174, v175\n\t" "ds_write_b64 %[tw1], v[172:173] offset:2048\n\t" "v_pk_fma_f32 v[180:181], v[120:121], v[180:181], v[148:149]\n\t"
    "v_pk_fma_f32 v[182:183], v[122:123], v[182:183], v[150:151]\n\t" "v_cvt_pk_bf16_f32 v180, v180, v181\n\t" "v_cvt_pk_bf16_f32 v181, v182, v183\n\t" "ds_write_b64 %[tw2], v[180:181] offset:2048\n\t"
    "v_pk_fma_f32 v[188:189], v[124:125], v[188:189], v[156:157]\n\t" "v_pk_fma_f32 v[190:191], v[126:127], v[190:191], v[158:159]\n\t" "v_cvt_pk_bf16_f32 v188, v188, v189\n\t"
    "v_cvt_pk_bf16_f32 v189, v190, v191\n\t" "ds_write_b64 %[tw3], v[188:189] offset:2048\n\t" "ds_read_b128 v[232:235], %[tr] offset:2048\n\t" "ds_read_b128 v[236:239], %[tr] offset:3072\n\t"
    "s_waitcnt lgkmcnt(7)\n\t" "global_store_dwordx4 %[vs], v[224:227], s[40:41]\n\t" "s_add_u32 s40, s40, 16384\n\t" "s_addc_u32 s41, s41, 0\n\t" "s_waitcnt lgkmcnt(6)\n\t"
    "global_store_dwordx4 %[vs], v[228:231], s[40:41]\n\t" "s_add_u32 s40, s40, 16384\n\t" "s_addc_u32 s41, s41, 0\n\t" "s_waitcnt lgkmcnt(1)\n\t" "global_store_dwordx4 %[vs], v[232:235], s[40:41]\n\t"
    "s_add_u32 s40, s40, 16384\n\t" "s_addc_u32 s41, s41, 0\n\t" "s_waitcnt lgkmcnt(0)\n\t" "global_store_dwordx4 %[vs], v[236:239], s[40:41]\n\t"
    : [fl] "=&v"(fl), [vb0] "+v"(vb0), [vb1] "+v"(vb1)
    : [vr0] "v"(vr[0]), [vr1] "v"(vr[1]), [vr2] "v"(vr[2]), [vr3] "v"(vr[3]), [vr4] "v"(vr[4]), [vr5] "v"(vr[5]), [vr6] "v"(vr[6]), [vr7] "v"(vr[7]), [vr8] "v"(vr[8]), [vr9] "v"(vr[9]), [vr10] "v"(vr[10]), [vr11] "v"(vr[11]), [vr12] "v"(vr[12]), [vr13] "v"(vr[13]), [vr14] "v"(vr[14]), [vr15] "v"(vr[15]), [vr16] "v"(vr[16]), [vr17] "v"(vr[17]), [vr18] "v"(vr[18]), [vr19] "v"(vr[19]), [vr20] "v"(vr[20]), [vr21] "v"(vr[21]), [vr22] "v"(vr[22]), [vr23] "v"(vr[23]), [vo] "v"(vo), [vx] "v"(vx), [vg] "v"(vg), [vf] "v"(vf), [vc] "v"(vc), [id] "v"(id), [tw0] "v"(tw0), [tw1] "v"(tw1), [tw2] "v"(tw2), [tw3] "v"(tw3), [tr] "v"(tr), [vs] "v"(vs), [t32] "s"((unsigned)tag), [sa] "s"(sa + 4096), [tag] "s"(tag), [wid] "s"(wid), [wr] "s"(wr), [sb] "s"(sb), [sl0] "s"(sl0), [sl1] "s"(sl1), [sg] "s"(sg), [sm] "s"(sm), [st] "s"(st), [sw] "s"(sw), [cd] "s"(cd), [so] "s"(so)
    : "v112", "v113", "v114", "v115", "v116", "v117", "v118", "v119", "v120", "v121", "v122", "v123", "v124", "v125", "v126", "v127", "v128", "v129", "v130", "v131", "v132", "v133", "v134", "v135", "v136", "v137", "v138", "v139", "v140", "v141", "v142", "v143", "v144", "v145", "v146", "v147", "v148", "v149", "v150", "v151", "v152", "v153", "v154", "v155", "v156", "v157", "v158", "v159", "v160", "v161", "v162", "v163", "v164", "v165", "v166", "v167", "v168", "v169", "v170", "v171", "v172", "v173", "v174", "v175", "v176", "v177", "v178", "v179", "v180", "v181", "v182", "v183", "v184", "v185", "v186", "v187", "v188", "v189", "v190", "v191", "v192", "v193", "v194", "v195", "v196", "v197", "v198", "v199", "v200", "v201", "v202", "v203", "v204", "v205", "v206", "v207", "v208", "v209", "v210", "v211", "v212", "v213", "v214", "v215", "v216", "v217", "v218", "v219", "v220", "v221", "v222", "v223", "v224", "v225", "v226", "v227", "v228", "v229", "v230", "v231", "v232", "v233", "v234", "v235", "v236", "v237", "v238", "v239", "v240", "v241", "v242", "v243", "v244", "v245", "v246", "v247", "v248", "v249", "v250", "v251", "v252", "v253", "v254", "v255", "vcc", "a0", "a1", "a2", "a3", "a4", "a5", "a6", "a7", "a8", "a9", "a10", "a11", "a12", "a13", "a14", "a15", "a16", "a17", "a18", "a19", "a20", "a21", "a22", "a23", "a24", "a25", "a26", "a27", "a28", "a29", "a30", "a31", "a32", "a33", "a34", "a35", "a36", "a37", "a38", "a39", "a40", "a41", "a42", "a43", "a44", "a45", "a46", "a47", "a48", "a49", "a50", "a51", "a52", "a53", "a54", "a55", "a56", "a57", "a58", "a59", "a60", "a61", "a62", "a63", "a64", "a65", "a66", "a67", "a68", "a69", "a70", "a71", "a72", "a73", "a74", "a75", "a76", "a77", "a78", "a79", "a80", "a81", "a82", "a83", "a84", "a85", "a86", "a87", "a88", "a89", "a90", "a91", "a92", "a93", "a94", "a95", "a96", "a97", "a98", "a99", "a100", "a101", "a102", "a103", "a104", "a105", "a106", "a107", "a108", "a109", "a110", "a111", "a112", "a113", "a114", "a115", "a116", "a117", "a118", "a119", "a120", "a121", "a122", "a123", "a124", "a125", "a126", "a127", "a128", "a129", "a130", "a131", "a132", "a133", "a134", "a135", "a136", "a137", "a138", "a139", "a140", "a141", "a142", "a143", "a144", "a145", "a146", "a147", "a148", "a149", "a150", "a151", "a152", "a153", "a154", "a155", "a156", "a157", "a158", "a159", "a160", "a161", "a162", "a163", "a164", "a165", "a166", "a167", "a168", "a169", "a170", "a171", "a172", "a173", "a174", "a175", "a176", "a177", "a178", "a179", "a180", "a181", "a182", "a183", "a184", "a185", "a186", "a187", "a188", "a189", "a190", "a191", "a192", "a193", "a194", "a195", "a196", "a197", "a198", "a199", "a200", "a201", "a202", "a203", "a204", "a205", "a206", "a207", "a208", "a209", "a210", "a211", "a212", "a213", "a214", "a215", "a216", "a217", "a218", "a219", "a220", "a221", "a222", "a223", "a224", "a225", "a226", "a227", "a228", "a229", "a230", "a231", "a232", "a233", "a234", "a235", "a236", "a237", "a238", "a239", "a240", "a241", "a242", "a243", "a244", "a245", "a246", "a247", "a248", "a249", "a250", "a251", "a252", "a253", "a254", "a255", "s40", "s41", "s42", "s43", "s44", "s45", "scc", "memory");
  }
  asm volatile("k_down_end:");
}

extern "C" void kernel_launch(void* const* d_in, const int* in_sizes, int n_in, void* d_out, int out_size, void* d_ws, size_t ws_size, hipStream_t stream) {
  const u16* x = (const u16*)d_in[0]; const u16* wqkv = (const u16*)d_in[1]; const u16* wo = (const u16*)d_in[2];
  const u16* wg = (const u16*)d_in[4]; const u16* wu = (const u16*)d_in[5]; const u16* wd = (const u16*)d_in[6];
  const u16* c = (const u16*)d_in[7]; const u16* wmod = (const u16*)d_in[8];
  k_main<<<256, 256, 0, stream>>>(c, wmod, x, wqkv, wo, wg, wu, wd, (u16*)d_out, (char*)d_ws);
}
```

```cpp
#include <hip/hip_runtime.h>
typedef unsigned short u16;
typedef float f32x4 __attribute__((ext_vector_type(4)));
typedef float f32x2 __attribute__((ext_vector_type(2)));
typedef unsigned u32x4 __attribute__((ext_vector_type(4)));
typedef unsigned u32x2 __attribute__((ext_vector_type(2)));
#define LAS __attribute__((address_space(3)))
#define DI __device__ __forceinline__

constexpr int S_ = 2048, D_ = 1024, F_ = 3584;

constexpr unsigned W_GB = 0, W_EF = 1 << 16, W_TAGS = W_EF + (1 << 15), W_PART = W_TAGS + (1 << 18), W_MODP = W_PART + (1 << 15), W_UVP = W_MODP + (1 << 16),
                 W_STATS = W_UVP + (1 << 19), W_O = W_STATS + 16 * S_ * 8, W_Y = W_O + S_ * D_ * 2, W_HH = W_Y + S_ * D_ * 2,
                 W_XCH = W_HH + S_ * F_ * 2;

DI unsigned f_vb(int wid, int lane) { const int r = wid * 8 + (lane >> 3), c = lane & 7; return (unsigned)(r * 2048 + (((((c >> 1) ^ ((r >> 1) & 3)) << 1) | (c & 1)) << 4)); }
DI unsigned f_vr(int lane, int j) { const int row = lane >> 2; return (unsigned)(row * 128 + ((j ^ ((row >> 1) & 3)) << 5) + (lane & 3) * 8); }

#define FS 256
#define FA(i) ((i) * FS)
constexpr int OF = 0, YF = 32 * FS, HF = 64 * FS, EP = 96 * FS;
extern "C" __global__ __launch_bounds__(256, 1) void k_main(const u16* __restrict__ c, const u16* __restrict__ wmod, const u16* __restrict__ x, const u16* __restrict__ w, const u16* __restrict__ wo,
                                                            const u16* __restrict__ wg, const u16* __restrict__ wu, const u16* __restrict__ pfw, u16* __restrict__ out, char* ws) {
  const char* code; int len;
  asm volatile("s_getpc_b64 %0\n.Lpc%=:\n\ts_mov_b32 %1, k_down_end-.Lpc%=\n\ts_mov_b64 exec, 3\n\tglobal_load_dword a255, %2, %0\n\ts_mov_b64 exec, -1"
               : "=&s"(code), "=s"(len) : "v"(((blockIdx.x & ~7u) + (threadIdx.x >> 5) + (threadIdx.x & 1)) * 128) : "a255");
  asm volatile("" :: "s"(c), "s"(wmod), "s"(x), "s"(w), "s"(wo), "s"(wg), "s"(wu), "s"(pfw), "s"(out), "s"(ws));
  constexpr int SCT = 16384, PH = 18944, RING = 20480, HALO = 131072;
  __shared__ __attribute__((aligned(1024))) char smem[163840];
  LAS char* lds = (LAS char*)smem;
  const unsigned ldsb = (unsigned)(size_t)lds;
  int wid = __builtin_amdgcn_readfirstlane(threadIdx.x >> 6);
  int bx = blockIdx.x;
#define ROT (((loc >> 2) + 1 * (xcd & 6)) & 7)
#define KD ((loc >> 2) * 56 % 56)
#define IDX() const int xcd = bx & 7, loc = bx >> 3, gk = loc >> 3, gc = xcd * 8 + (loc & 7), rb = (xcd >> 1) * 4 + (loc & 3), cb = (xcd & 1) * 8 + (loc >> 2); const unsigned sl1 = ldsb + wid * 1024; const int rot = ROT, co = rot * 2 + (xcd & 1); const char* so = (const char*)(wo + co * 64)
  unsigned long long tag; unsigned cv; float mu[2]; unsigned vbo; const char* cd; int tid = threadIdx.x;
  { IDX();
  { const int lane = threadIdx.x & 63;
    const unsigned vc = threadIdx.x * 2, vs = (lane >> 2) * 12288 + (((lane & 3) ^ ((lane >> 5) << 1)) << 4), sl4 = ldsb + wid * 4096;
    const u16* sc = c + gk * 256; const u16* sw = wmod + (size_t)(gk * 256 + wid * 64) * 6144 + gc * 32; const char* st = ws + W_TAGS + EP * 8;
    asm volatile( "global_load_ushort v204, %[vc], %[sc]\n\t" "s_mov_b64 s[40:41], %[sw]\n\t" "s_add_u32 m0, %[sl4], 0\n\t" "s_nop 0\n\t" "global_load_lds_dwordx4 %[vs], s[40:41] nt\n\t"
    "s_add_u32 s40, s40, 196608\n\t" "s_addc_u32 s41, s41, 0\n\t" "s_add_u32 m0, %[sl4], 1024\n\t" "s_nop 0\n\t" "global_load_lds_dwordx4 %[vs], s[40:41] nt\n\t" "s_add_u32 s40, s40, 196608\n\t"
    "s_addc_u32 s41, s41, 0\n\t" "s_add_u32 m0, %[sl4], 2048\n\t" "s_nop 0\n\t" "global_load_lds_dwordx4 %[vs], s[40:41] nt\n\t" "s_add_u32 s40, s40, 196608\n\t" "s_addc_u32 s41, s41, 0\n\t"
    "s_add_u32 m0, %[sl4], 3072\n\t" "s_nop 0\n\t" "global_load_lds_dwordx4 %[vs], s[40:41] nt\n\t" "s_load_dwordx2 s[62:63], %[st], 0x0\n\t"
    : "=&{v204}"(cv), "=&{s[62:63]}"(tag)
    : [vc] "v"(vc), [vs] "v"(vs), [sc] "s"(sc), [sw] "s"(sw), [sl4] "s"(sl4), [st] "s"(st)
    : "s40", "s41", "scc", "memory");
  }
  { asm volatile("" : "+v"(tid));
    int lane = tid & 63;
    const unsigned sl = ldsb + (wid & 1) * 1024 + (wid >> 1) * 6144;
    const unsigned dx = (lane >> 3) * 2048 + (((lane & 7) ^ (lane >> 3)) << 4); unsigned d0 = dx + ((rb ? 16 : 0) + wid * 32) * 2048, d1 = d0 + 16384, d2 = d0 + 32768, d3 = d0 + 49152;
    const char* xb0 = (const char*)x + (size_t)(rb ? rb * 128 - 16 : 0) * 2048;
    const unsigned xl = (unsigned)(size_t)xb0, xh_ = (unsigned)((size_t)xb0 >> 32), k0 = rot * 256;
    asm volatile("s_add_u32 s45, %[k0], 0\n\t" "s_and_b32 s45, s45, 0x7ff\n\t" "s_add_u32 s64, %[xl], s45\n\t" "s_addc_u32 s65, %[xh_], 0\n\t" "s_add_u32 m0, %[sl], 20480\n\t" "s_nop 0\n\t"
    "global_load_lds_dwordx4 v196, s[64:65] offset:0\n\t" "s_add_u32 m0, %[sl], 32768\n\t" "s_nop 0\n\t" "global_load_lds_dwordx4 v197, s[64:65] offset:0\n\t" "s_add_u32 m0, %[sl], 22528\n\t"
    "s_nop 0\n\t" "global_load_lds_dwordx4 v198, s[64:65] offset:0\n\t" "s_add_u32 m0, %[sl], 34816\n\t" "s_nop 0\n\t" "global_load_lds_dwordx4 v199, s[64:65] offset:0\n\t"
    :: "{v196}"(d0), "{v197}"(d1), "{v198}"(d2), "{v199}"(d3), [sl] "s"(sl), [xl] "s"(xl), [xh_] "s"(xh_), [k0] "s"(k0)
    : "s45", "s64", "s65", "scc", "memory");
    asm volatile("" : "+v"(tid)); lane = tid & 63;
    const int ww = loc * 4 + wid; unsigned tv;
    { const int g = lane >> 4;
      const unsigned vsr = ldsb + (8 * g + ((lane >> 2) & 3)) * 64 + (((wid ^ g) & 1) << 5) + (lane & 3) * 8, vsc = ldsb + g * 16, vcw = ldsb + SCT + tid * 2;
      const char* su = ws + W_PART + (gk * 512 + gc * 8 + wid * 4) * 16 - ldsb; const u16* sq = w;
      const unsigned t0 = ((ww * 2 + (lane & 1)) * 4 + (xcd >> 1)) * 6144 + (xcd & 1) * 1024 + ((384 - (unsigned)(size_t)w) & 896) + (lane >> 1) * 2048;
      asm volatile( "s_mov_b32 s54, 0x07060503\n\t" "s_mov_b32 s55, 0x07060502\n\t" "s_mov_b32 s56, 0x07060501\n\t" "s_mov_b32 s60, 0x00010001\n\t" "s_mov_b32 s61, s60\n\t" "s_add_u32 s45, %[k0], 0\n\t"
    "s_and_b32 s45, s45, 0x7ff\n\t" "s_add_u32 s64, %[xl], s45\n\t" "s_addc_u32 s65, %[xh_], 0\n\t" "s_add_u32 m0, %[sl], 24448\n\t" "s_nop 0\n\t" "global_load_lds_dwordx4 v196, s[64:65] offset:128\n\t"
    "s_add_u32 m0, %[sl], 36736\n\t" "s_nop 0\n\t" "global_load_lds_dwordx4 v197, s[64:65] offset:128\n\t" "s_add_u32 m0, %[sl], 44928\n\t" "s_nop 0\n\t"
    "global_load_lds_dwordx4 v198, s[64:65] offset:128\n\t" "s_add_u32 m0, %[sl], 57216\n\t" "s_nop 0\n\t" "global_load_lds_dwordx4 v199, s[64:65] offset:128\n\t" "s_mov_b64 exec, 63\n\t"
    "global_load_dword v205, %[t0], %[sq]\n\t" "s_mov_b64 exec, -1\n\t" "s_waitcnt vmcnt(13)\n\t" "v_lshlrev_b32 v204, 16, v204\n\t" "v_mul_f32 v206, 0xbfb8aa3b, v204\n\t"
    "v_exp_f32 v206, v206\n\t" "s_nop 0\n\t" "v_add_f32 v206, 1.0, v206\n\t" "v_rcp_f32 v206, v206\n\t" "s_nop 0\n\t" "v_mul_f32 v204, v204, v206\n\t" "v_cvt_pk_bf16_f32 v204, v204, v204\n\t"
    "ds_write_b16 v233, v204\n\t" "s_waitcnt vmcnt(9)\n\t" "s_waitcnt lgkmcnt(0)\n\t" "s_barrier\n\t" "s_add_u32 s62, s62, 1\n\t" "s_addc_u32 s63, s63, 0\n\t"
    "s_cmp_gt_u32 %[wid], 1\n\t" "s_cbranch_scc1 .Lns_%=\n\t" "ds_read_b64_tr_b16 v[48:49], v228 offset:0\n\t" "ds_read_b64_tr_b16 v[50:51], v228 offset:256\n\t"
    "ds_read_b128 v[80:83], v229 offset:16384\n\t" "ds_read_b64_tr_b16 v[52:53], v228 offset:2048\n\t" "ds_read_b64_tr_b16 v[54:55], v228 offset:2304\n\t" "ds_read_b128 v[84:87], v229 offset:16448\n\t"
    "ds_read_b64_tr_b16 v[56:57], v228 offset:4096\n\t" "ds_read_b64_tr_b16 v[58:59], v228 offset:4352\n\t" "ds_read_b128 v[88:91], v229 offset:16512\n\t"
    "ds_read_b64_tr_b16 v[60:61], v228 offset:6144\n\t" "ds_read_b64_tr_b16 v[62:63], v228 offset:6400\n\t" "ds_read_b128 v[92:95], v229 offset:16576\n\t" "s_waitcnt lgkmcnt(9)\n\t"
    "v_mfma_f32_16x16x32_bf16 v[112:115], v[48:51], v[80:83], 0\n\t" "ds_read_b64_tr_b16 v[64:65], v228 offset:8192\n\t" "ds_read_b64_tr_b16 v[66:67], v228 offset:8448\n\t"
    "ds_read_b128 v[96:99], v229 offset:16640\n\t" "s_waitcnt lgkmcnt(9)\n\t" "v_mfma_f32_16x16x32_bf16 v[112:115], v[52:55], v[84:87], v[112:115]\n\t" "ds_read_b64_tr_b16 v[68:69], v228 offset:10240\n\t"
    "ds_read_b64_tr_b16 v[70:71], v228 offset:10496\n\t" "ds_read_b128 v[100:103], v229 offset:16704\n\t" "s_waitcnt lgkmcnt(9)\n\t"
    "v_mfma_f32_16x16x32_bf16 v[112:115], v[56:59], v[88:91], v[112:115]\n\t" "ds_read_b64_tr_b16 v[72:73], v228 offset:12288\n\t" "ds_read_b64_tr_b16 v[74:75], v228 offset:12544\n\t"
    "ds_read_b128 v[104:107], v229 offset:16768\n\t" "s_waitcnt lgkmcnt(9)\n\t" "v_mfma_f32_16x16x32_bf16 v[112:115], v[60:63], v[92:95], v[112:115]\n\t"
    "ds_read_b64_tr_b16 v[76:77], v228 offset:14336\n\t" "ds_read_b64_tr_b16 v[78:79], v228 offset:14592\n\t" "ds_read_b128 v[108:111], v229 offset:16832\n\t" "s_waitcnt lgkmcnt(9)\n\t"
    "v_mfma_f32_16x16x32_bf16 v[112:115], v[64:67], v[96:99], v[112:115]\n\t" "s_waitcnt lgkmcnt(6)\n\t" "v_mfma_f32_16x16x32_bf16 v[112:115], v[68:71], v[100:103], v[112:115]\n\t"
    "s_waitcnt lgkmcnt(3)\n\t" "v_mfma_f32_16x16x32_bf16 v[112:115], v[72:75], v[104:107], v[112:115]\n\t" "s_waitcnt lgkmcnt(0)\n\t"
    "v_mfma_f32_16x16x32_bf16 v[112:115], v[76:79], v[108:111], v[112:115]\n\t" "s_nop 7\n\t" "v_perm_b32 v116, v112, v115, s54\n\t" "v_perm_b32 v117, v113, v115, s55\n\t"
    "v_perm_b32 v118, v114, v115, s56\n\t" "v_xad_u32 v119, v116, v117, s62\n\t" "s_mov_b64 exec, s[60:61]\n\t" "v_xad_u32 v119, v118, v117, v119\n\t"
    "global_store_dwordx4 v229, v[116:119], %[su] sc1\n\t" "s_mov_b64 exec, -1\n\t" ".Lns_%=:\n\t"
    : "+{v204}"(cv), "=&{v205}"(tv), "+{s[62:63]}"(tag)
    : "{v196}"(d0), "{v197}"(d1), "{v198}"(d2), "{v199}"(d3), [sl] "s"(sl), [xl] "s"(xl), [xh_] "s"(xh_), [k0] "s"(k0), "{v233}"(vcw), "{v228}"(vsr), "{v229}"(vsc), [t0] "v"(t0), [sq] "s"(sq), [su] "s"(su), [wid] "s"(wid)
    : "v48", "v49", "v50", "v51", "v52", "v53", "v54", "v55", "v56", "v57", "v58", "v59", "v60", "v61", "v62", "v63", "v64", "v65", "v66", "v67", "v68", "v69", "v70", "v71", "v72", "v73", "v74", "v75", "v76", "v77", "v78", "v79", "v80", "v81", "v82", "v83", "v84", "v85", "v86", "v87", "v88", "v89", "v90", "v91", "v92", "v93", "v94", "v95", "v96", "v97", "v98", "v99", "v100", "v101", "v102", "v103", "v104", "v105", "v106", "v107", "v108", "v109", "v110", "v111", "v112", "v113", "v114", "v115", "v116", "v117", "v118", "v119", "v206", "s45", "s54", "s55", "s56", "s60", "s61", "s64", "s65", "scc", "memory");
    }
    asm volatile("" : "+v"(tid)); lane = tid & 63;
    const int fr = lane & 15, g = lane >> 4, q = (lane >> 2) & 3, ls = ((tid >> 3) - 4 * rot) & 31;
    unsigned vb = (unsigned)((rot * 128 + wid * 8 + (lane >> 3)) * 6144 + (((lane & 7) ^ ((((lane >> 4) & 1) | ((wid & 1) << 1)) << 1)) << 4));
    unsigned dh0 = dx, dh1 = dh0 + 16384, xr0 = sl + RING + ((lane >> 3) & 1) * 12288 + (lane & 7) * 128 + ((g ^ (lane & 7)) << 4), xr1 = xr0 ^ 64;
    unsigned vh = ldsb + HALO + (g * 16 + (fr ^ g)) * 16, vdt = ldsb + (lane & 3) * 64 + g * 16, vhd = vdt + wid * 512, vhx = vh + wid * 2048;
    unsigned vg = tid * 16 + W_PART + 4096, vtw = ldsb + ls * 256 + (tid & 7) * 8, vhw = ldsb + HALO + ls * 1024 + ((tid >> 1) & 3) * 272 + (tid & 1) * 8, vs8 = ldsb + SCT + (tid >> 3) * 16;
    unsigned vpr = ldsb + PH + fr * 8, vph = vpr + wid * 128, vm = (tid >> 3) * 98304 + (tid & 7) * 16; vbo = f_vb(wid, lane);
    unsigned vr0 = ldsb + RING + (g & 1) * 1024 + (g >> 1) * 6144 + q * 128 + ((wid ^ ((q >> 1) | ((g & 1) << 1))) << 5) + (lane & 3) * 8, vr1 = vr0 ^ 32, vr2 = vr0 ^ 64, vr3 = vr0 ^ 96;
    unsigned vr4 = vr0 + 65536, vr5 = vr1 + 65536, vr6 = vr2 + 65536, vr7 = vr3 + 65536;
    const unsigned l0 = ldsb, lh = ldsb + HALO, lt = lh + 24576 + wid * 128, lr = lh + 22528 + wid * 256, w128 = wid * 128, w4k = wid * 4096, xw = w4k + 4096 + (wid == 1 ? 2048 : 0), xh = (wid >> 1) * 2048 + (wid & 1) * 8, ox = (wid >> 1) * 2048 + W_O + (rb * 4 + wid) * 65536 + cb * 4096, kh = k0 + w128;
    const int rw = rb * 4 + wid;
    const char* sb = (const char*)(w + 1024 + cb * 64); const u16* sm = wmod + (size_t)gk * 256 * 6144 + 2048 + gc * 64;
    const unsigned sf = W_TAGS + OF * 8 + FA(rb * 16 + cb);
    unsigned o0 = min(32768 + (ww * 8 + lane) * 128, len - 4), o1 = sf + (lane & 1) * YF * 8, o2 = W_TAGS + HF * 8 + FA(((xcd & 1) * 4 + (loc & 3)) * 32 + (xcd >> 1) * 8 + (loc >> 2));
    unsigned o3 = ((ww * 2 + (lane & 1)) * 4 + (xcd >> 1)) * 2048 + (xcd & 1) * 128 + (lane & 14) * 128;
    asm volatile("" : "+v"(o0), "+v"(o1), "+v"(o2), "+v"(o3));
    const char* t2 = (lane < 8 ? code : lane < 12 ? ws : (const char*)wo) + (lane < 8 ? o0 : lane < 10 ? o1 : lane < 12 ? o2 : o3);
    const int c3 = (3 - ((unsigned)(size_t)w >> 7)) & 7, w2 = ww - ((loc >> 2) > c3 ? 16 : 0), ix = (w2 >= 56 ? w2 - 56 : w2) * 16 * 1 + lane, r14 = (ix * 4682) >> 16;
    unsigned t3 = (xcd & 1) * 512 * 7168 + r14 * (7168 - 1792) + (xcd >> 1) * 1792 + ix * 128; const u16* pf = w2 >= 56 ? wu : wg;
    unsigned long long tm = (loc >> 2) == c3 ? 0ull : ~0ull >> (64 - 16 * 1);
    cd = code + min(ww * 1024, len - 1024);
    { const char* cd1 = cd; const char* cd = cd1 - wid * 1024;
    asm volatile(".p2align 3\n\t" "s_mov_b32 s43, 0xffa30000\n\t" "s_mov_b32 s46, 0x00100001\n\t" "s_mov_b32 s47, 0x10000100\n\t" "s_lshl_b64 s[48:49], s[46:47], 1\n\t" "s_lshl_b64 s[50:51], s[46:47], 2\n\t"
    "s_lshl_b64 s[52:53], s[46:47], 3\n\t" "s_mov_b32 s57, 0x04000c0c\n\t" "s_mov_b32 s58, 0x0706000c\n\t" "s_add_u32 s45, %[k0], 256\n\t" "s_and_b32 s45, s45, 0x7ff\n\t" "s_add_u32 s64, %[xl], s45\n\t"
    "s_addc_u32 s65, %[xh_], 0\n\t" "s_add_u32 m0, %[sl], 47104\n\t" "s_nop 0\n\t" "global_load_lds_dwordx4 v196, s[64:65] offset:0\n\t" "s_add_u32 m0, %[sl], 59392\n\t" "s_nop 0\n\t"
    "global_load_lds_dwordx4 v197, s[64:65] offset:0\n\t" "s_add_u32 m0, %[sl], 49152\n\t" "s_nop 0\n\t" "global_load_lds_dwordx4 v198, s[64:65] offset:0\n\t" "s_add_u32 m0, %[sl], 61440\n\t"
    "s_nop 0\n\t" "global_load_lds_dwordx4 v199, s[64:65] offset:0\n\t" "s_add_u32 m0, %[sl], 69504\n\t" "s_nop 0\n\t" "global_load_lds_dwordx4 v196, s[64:65] offset:128\n\t"
    "s_add_u32 m0, %[sl], 81792\n\t" "s_nop 0\n\t" "global_load_lds_dwordx4 v197, s[64:65] offset:128\n\t" "s_add_u32 m0, %[sl], 71552\n\t" "s_nop 0\n\t"
    "global_load_lds_dwordx4 v198, s[64:65] offset:128\n\t" "s_add_u32 m0, %[sl], 83840\n\t" "s_nop 0\n\t" "global_load_lds_dwordx4 v199, s[64:65] offset:128\n\t" "ds_read_b128 v[252:255], v234\n\t"
    "v_mov_b32 v112, 0x3f803f80\n\t" "v_mov_b32 v113, 0x3f803f80\n\t" "v_mov_b32 v114, 0x3f803f80\n\t" "v_mov_b32 v115, 0x3f803f80\n\t" "s_mov_b32 s60, 0xfffcfffc\n\t" "s_mov_b32 s61, s60\n\t"
    "s_mov_b32 s44, 0x100000\n\t" "s_barrier\n\t" "s_waitcnt vmcnt(13)\n\t" "ds_read_b128 a[0:3], v202 offset:0\n\t" "ds_read_b128 a[4:7], v202 offset:2048\n\t"
    "ds_read_b128 a[8:11], v203 offset:0\n\t" "ds_read_b128 a[12:15], v203 offset:2048\n\t" "s_waitcnt lgkmcnt(0)\n\t" "s_add_u32 s45, %[kh], 0\n\t" "s_and_b32 s45, s45, 0x7ff\n\t"
    "s_add_u32 s40, %[xl], s45\n\t" "s_addc_u32 s41, %[xh_], 0\n\t" "s_add_u32 m0, %[sl], 20480\n\t" "s_nop 0\n\t" "global_load_lds_dwordx4 v200, s[40:41]\n\t" "s_add_u32 m0, %[sl], 32768\n\t"
    "s_nop 0\n\t" "global_load_lds_dwordx4 v201, s[40:41]\n\t" "s_add_u32 s45, %[kh], 512\n\t" "s_and_b32 s45, s45, 0x7ff\n\t" "s_add_u32 s40, %[xl], s45\n\t" "s_addc_u32 s41, %[xh_], 0\n\t"
    "s_add_u32 m0, %[sl], 22528\n\t" "s_nop 0\n\t" "global_load_lds_dwordx4 v200, s[40:41]\n\t" "s_add_u32 m0, %[sl], 34816\n\t" "s_nop 0\n\t" "global_load_lds_dwordx4 v201, s[40:41]\n\t"
    "s_add_u32 m0, %[sl], 96256\n\t" "s_nop 0\n\t" "global_load_lds_dwordx4 v0, %[sb] offset:-2048\n\t" "global_load_lds_dwordx4 v0, %[sb] offset:0\n\t" "global_load_lds_dwordx4 v0, %[sb] offset:2048\n\t"
    "v_add_u32 v0, 0x30000, v0\n\t" "s_add_u32 m0, %[sl], 108544\n\t" "s_nop 0\n\t" "global_load_lds_dwordx4 v0, %[sb] offset:-2048\n\t" "global_load_lds_dwordx4 v0, %[sb] offset:0\n\t"
    "global_load_lds_dwordx4 v0, %[sb] offset:2048\n\t" "v_add_u32 v0, 0x30000, v0\n\t" "s_waitcnt vmcnt(19)\n\t" "v_mfma_f32_16x16x32_bf16 v[116:119], v[112:115], a[0:3], 0\n\t"
    "ds_read_b128 a[16:19], v202 offset:4096\n\t" "ds_read_b128 a[20:23], v202 offset:24576\n\t" "v_mfma_f32_16x16x32_bf16 v[128:131], a[0:3], a[0:3], 0\n\t" "ds_read_b128 a[24:27], v203 offset:4096\n\t"
    "ds_read_b128 a[28:31], v203 offset:24576\n\t" "v_mfma_f32_16x16x32_bf16 v[120:123], v[112:115], a[4:7], 0\n\t" "s_add_u32 s45, %[kh], 1024\n\t" "s_and_b32 s45, s45, 0x7ff\n\t"
    "s_add_u32 s40, %[xl], s45\n\t" "s_addc_u32 s41, %[xh_], 0\n\t" "v_mfma_f32_16x16x32_bf16 v[132:135], a[4:7], a[4:7], 0\n\t" "v_mfma_f32_16x16x32_bf16 v[116:119], v[112:115], a[8:11], v[116:119]\n\t"
    "v_mfma_f32_16x16x32_bf16 v[128:131], a[8:11], a[8:11], v[128:131]\n\t" "s_waitcnt lgkmcnt(0)\n\t" "s_add_u32 m0, %[sl], 24576\n\t"
    "v_mfma_f32_16x16x32_bf16 v[120:123], v[112:115], a[12:15], v[120:123]\n\t" "global_load_lds_dwordx4 v200, s[40:41]\n\t" "s_add_u32 m0, %[sl], 36864\n\t"
    "v_mfma_f32_16x16x32_bf16 v[132:135], a[12:15], a[12:15], v[132:135]\n\t" "global_load_lds_dwordx4 v201, s[40:41]\n\t" "s_add_u32 s45, %[kh], 1536\n\t" "s_and_b32 s45, s45, 0x7ff\n\t"
    "s_add_u32 s40, %[xl], s45\n\t" "s_addc_u32 s41, %[xh_], 0\n\t" "s_add_u32 m0, %[sl], 45056\n\t" "s_nop 0\n\t" "global_load_lds_dwordx4 v200, s[40:41]\n\t" "s_add_u32 m0, %[sl], 57344\n\t"
    "s_nop 0\n\t" "global_load_lds_dwordx4 v201, s[40:41]\n\t" "s_waitcnt vmcnt(18)\n\t" "v_mfma_f32_16x16x32_bf16 v[116:119], v[112:115], a[16:19], v[116:119]\n\t"
    "ds_read_b128 a[32:35], v202 offset:26624\n\t" "ds_read_b128 a[36:39], v202 offset:28672\n\t" "v_mfma_f32_16x16x32_bf16 v[128:131], a[16:19], a[16:19], v[128:131]\n\t"
    "ds_read_b128 a[40:43], v203 offset:26624\n\t" "ds_read_b128 a[44:47], v203 offset:28672\n\t" "v_mfma_f32_16x16x32_bf16 v[120:123], v[112:115], a[20:23], v[120:123]\n\t"
    "s_add_u32 s45, %[k0], 512\n\t" "s_and_b32 s45, s45, 0x7ff\n\t" "s_add_u32 s64, %[xl], s45\n\t" "s_addc_u32 s65, %[xh_], 0\n\t"
    "v_mfma_f32_16x16x32_bf16 v[132:135], a[20:23], a[20:23], v[132:135]\n\t" "v_mfma_f32_16x16x32_bf16 v[116:119], v[112:115], a[24:27], v[116:119]\n\t"
    "v_mfma_f32_16x16x32_bf16 v[128:131], a[24:27], a[24:27], v[128:131]\n\t" "s_waitcnt lgkmcnt(0)\n\t" "s_add_u32 m0, %[sl], 47104\n\t"
    "v_mfma_f32_16x16x32_bf16 v[120:123], v[112:115], a[28:31], v[120:123]\n\t" "global_load_lds_dwordx4 v196, s[64:65] offset:0\n\t" "s_add_u32 m0, %[sl], 59392\n\t"
    "v_mfma_f32_16x16x32_bf16 v[132:135], a[28:31], a[28:31], v[132:135]\n\t" "global_load_lds_dwordx4 v197, s[64:65] offset:0\n\t" "s_add_u32 m0, %[sl], 49152\n\t" "s_nop 0\n\t"
    "global_load_lds_dwordx4 v198, s[64:65] offset:0\n\t" "s_add_u32 m0, %[sl], 61440\n\t" "s_nop 0\n\t" "global_load_lds_dwordx4 v199, s[64:65] offset:0\n\t" "s_waitcnt vmcnt(18)\n\t"
    "v_mfma_f32_16x16x32_bf16 v[116:119], v[112:115], a[32:35], v[116:119]\n\t" "ds_read_b128 a[48:51], v202 offset:49152\n\t" "ds_read_b128 a[52:55], v202 offset:51200\n\t"
    "v_mfma_f32_16x16x32_bf16 v[128:131], a[32:35], a[32:35], v[128:131]\n\t" "ds_read_b128 a[56:59], v203 offset:49152\n\t" "ds_read_b128 a[60:63], v203 offset:51200\n\t"
    "v_mfma_f32_16x16x32_bf16 v[120:123], v[112:115], a[36:39], v[120:123]\n\t" "v_mfma_f32_16x16x32_bf16 v[132:135], a[36:39], a[36:39], v[132:135]\n\t"
    "v_mfma_f32_16x16x32_bf16 v[116:119], v[112:115], a[40:43], v[116:119]\n\t" "v_mfma_f32_16x16x32_bf16 v[128:131], a[40:43], a[40:43], v[128:131]\n\t" "s_waitcnt lgkmcnt(0)\n\t"
    "s_add_u32 m0, %[sl], 69504\n\t" "v_mfma_f32_16x16x32_bf16 v[120:123], v[112:115], a[44:47], v[120:123]\n\t" "global_load_lds_dwordx4 v196, s[64:65] offset:128\n\t" "s_add_u32 m0, %[sl], 81792\n\t"
    "v_mfma_f32_16x16x32_bf16 v[132:135], a[44:47], a[44:47], v[132:135]\n\t" "global_load_lds_dwordx4 v197, s[64:65] offset:128\n\t" "s_add_u32 m0, %[sl], 71552\n\t" "s_nop 0\n\t"
    "global_load_lds_dwordx4 v198, s[64:65] offset:128\n\t" "s_add_u32 m0, %[sl], 83840\n\t" "s_nop 0\n\t" "global_load_lds_dwordx4 v199, s[64:65] offset:128\n\t" "s_waitcnt vmcnt(18)\n\t"
    "v_mfma_f32_16x16x32_bf16 v[116:119], v[112:115], a[48:51], v[116:119]\n\t" "ds_read_b128 v[48:51], v202 offset:0\n\t" "ds_read_b128 v[20:23], v202 offset:2048\n\t"
    "v_mfma_f32_16x16x32_bf16 v[128:131], a[48:51], a[48:51], v[128:131]\n\t" "ds_read_b128 v[52:55], v203 offset:0\n\t" "ds_read_b128 v[24:27], v203 offset:2048\n\t"
    "v_mfma_f32_16x16x32_bf16 v[120:123], v[112:115], a[52:55], v[120:123]\n\t" "s_add_u32 s45, %[k0], 768\n\t" "s_and_b32 s45, s45, 0x7ff\n\t" "s_add_u32 s64, %[xl], s45\n\t"
    "s_addc_u32 s65, %[xh_], 0\n\t" "v_mfma_f32_16x16x32_bf16 v[132:135], a[52:55], a[52:55], v[132:135]\n\t" "v_mfma_f32_16x16x32_bf16 v[116:119], v[112:115], a[56:59], v[116:119]\n\t"
    "v_mfma_f32_16x16x32_bf16 v[128:131], a[56:59], a[56:59], v[128:131]\n\t" "s_waitcnt lgkmcnt(0)\n\t" "s_add_u32 m0, %[sl], 20480\n\t"
    "v_mfma_f32_16x16x32_bf16 v[120:123], v[112:115], a[60:63], v[120:123]\n\t" "global_load_lds_dwordx4 v196, s[64:65] offset:0\n\t" "s_add_u32 m0, %[sl], 32768\n\t"
    "v_mfma_f32_16x16x32_bf16 v[132:135], a[60:63], a[60:63], v[132:135]\n\t" "global_load_lds_dwordx4 v197, s[64:65] offset:0\n\t" "s_add_u32 m0, %[sl], 22528\n\t" "s_nop 0\n\t"
    "global_load_lds_dwordx4 v198, s[64:65] offset:0\n\t" "s_add_u32 m0, %[sl], 34816\n\t" "s_nop 0\n\t" "global_load_lds_dwordx4 v199, s[64:65] offset:0\n\t" "s_waitcnt vmcnt(12)\n\t"
    "v_mfma_f32_16x16x32_bf16 v[124:127], v[112:115], v[48:51], 0\n\t" "ds_read_b128 v[28:31], v202 offset:4096\n\t" "ds_read_b128 v[36:39], v202 offset:24576\n\t"
    "v_mfma_f32_16x16x32_bf16 v[136:139], v[48:51], v[48:51], 0\n\t" "ds_read_b128 v[32:35], v203 offset:4096\n\t" "ds_read_b128 v[40:43], v203 offset:24576\n\t"
    "v_mfma_f32_16x16x32_bf16 v[124:127], v[112:115], v[20:23], v[124:127]\n\t" "v_mfma_f32_16x16x32_bf16 v[136:139], v[20:23], v[20:23], v[136:139]\n\t"
    "v_mfma_f32_16x16x32_bf16 v[124:127], v[112:115], v[52:55], v[124:127]\n\t" "v_mfma_f32_16x16x32_bf16 v[136:139], v[52:55], v[52:55], v[136:139]\n\t" "s_waitcnt lgkmcnt(0)\n\t"
    "s_add_u32 m0, %[sl], 24448\n\t" "v_mfma_f32_16x16x32_bf16 v[124:127], v[112:115], v[24:27], v[124:127]\n\t" "global_load_lds_dwordx4 v196, s[64:65] offset:128\n\t" "s_add_u32 m0, %[sl], 36736\n\t"
    "v_mfma_f32_16x16x32_bf16 v[136:139], v[24:27], v[24:27], v[136:139]\n\t" "global_load_lds_dwordx4 v197, s[64:65] offset:128\n\t" "s_add_u32 m0, %[sl], 44928\n\t" "s_nop 0\n\t"
    "global_load_lds_dwordx4 v198, s[64:65] offset:128\n\t" "s_add_u32 m0, %[sl], 57216\n\t" "s_nop 0\n\t" "global_load_lds_dwordx4 v199, s[64:65] offset:128\n\t" "s_mov_b64 s[40:41], %[ws]\n\t"
    "global_load_dwordx4 v[80:83], v230, s[40:41] offset:-4096 sc1\n\t" "global_load_dwordx4 v[96:99], v230, s[40:41] offset:0 sc1\n\t" "s_add_u32 s40, s40, 8192\n\t" "s_addc_u32 s41, s41, 0\n\t"
    "global_load_dwordx4 v[84:87], v230, s[40:41] offset:-4096 sc1\n\t" "global_load_dwordx4 v[100:103], v230, s[40:41] offset:0 sc1\n\t" "s_add_u32 s40, s40, 8192\n\t" "s_addc_u32 s41, s41, 0\n\t"
    "global_load_dwordx4 v[88:91], v230, s[40:41] offset:-4096 sc1\n\t" "global_load_dwordx4 v[104:107], v230, s[40:41] offset:0 sc1\n\t" "s_add_u32 s40, s40, 8192\n\t" "s_addc_u32 s41, s41, 0\n\t"
    "global_load_dwordx4 v[92:95], v230, s[40:41] offset:-4096 sc1\n\t" "global_load_dwordx4 v[108:111], v230, s[40:41] offset:0 sc1\n\t" "s_waitcnt vmcnt(20)\n\t"
    "v_mfma_f32_16x16x32_bf16 v[124:127], v[112:115], v[28:31], v[124:127]\n\t" "ds_read_b128 a[64:67], v202 offset:26624\n\t" "ds_read_b128 a[68:71], v202 offset:28672\n\t"
    "v_mfma_f32_16x16x32_bf16 v[136:139], v[28:31], v[28:31], v[136:139]\n\t" "ds_read_b128 a[72:75], v203 offset:26624\n\t" "ds_read_b128 a[76:79], v203 offset:28672\n\t"
    "v_mfma_f32_16x16x32_bf16 v[124:127], v[112:115], v[36:39], v[124:127]\n\t" "s_add_u32 s45, %[k0], 1024\n\t" "s_and_b32 s45, s45, 0x7ff\n\t" "s_add_u32 s64, %[xl], s45\n\t"
    "s_addc_u32 s65, %[xh_], 0\n\t" "v_mfma_f32_16x16x32_bf16 v[136:139], v[36:39], v[36:39], v[136:139]\n\t" "v_mfma_f32_16x16x32_bf16 v[124:127], v[112:115], v[32:35], v[124:127]\n\t"
    "v_mfma_f32_16x16x32_bf16 v[136:139], v[32:35], v[32:35], v[136:139]\n\t" "s_waitcnt lgkmcnt(0)\n\t" "s_add_u32 m0, %[sl], 47104\n\t"
    "v_mfma_f32_16x16x32_bf16 v[124:127], v[112:115], v[40:43], v[124:127]\n\t" "global_load_lds_dwordx4 v196, s[64:65] offset:0\n\t" "s_add_u32 m0, %[sl], 59392\n\t"
    "v_mfma_f32_16x16x32_bf16 v[136:139], v[40:43], v[40:43], v[136:139]\n\t" "global_load_lds_dwordx4 v197, s[64:65] offset:0\n\t" "s_add_u32 m0, %[sl], 49152\n\t" "s_nop 0\n\t"
    "global_load_lds_dwordx4 v198, s[64:65] offset:0\n\t" "s_add_u32 m0, %[sl], 61440\n\t" "s_nop 0\n\t" "global_load_lds_dwordx4 v199, s[64:65] offset:0\n\t" "s_waitcnt vmcnt(20)\n\t"
    "v_mfma_f32_16x16x32_bf16 v[116:119], v[112:115], a[64:67], v[116:119]\n\t" "ds_read_b128 a[80:83], v202 offset:49152\n\t" "ds_read_b128 a[84:87], v202 offset:51200\n\t"
    "v_mfma_f32_16x16x32_bf16 v[128:131], a[64:67], a[64:67], v[128:131]\n\t" "ds_read_b128 a[88:91], v203 offset:49152\n\t" "ds_read_b128 a[92:95], v203 offset:51200\n\t"
    "v_mfma_f32_16x16x32_bf16 v[120:123], v[112:115], a[68:71], v[120:123]\n\t" "v_cndmask_b32 v142, 0, v136, s[46:47]\n\t" "v_cndmask_b32 v142, v142, v137, s[48:49]\n\t"
    "v_cndmask_b32 v142, v142, v138, s[50:51]\n\t" "v_cndmask_b32 v142, v142, v139, s[52:53]\n\t" "v_mfma_f32_16x16x32_bf16 v[132:135], a[68:71], a[68:71], v[132:135]\n\t" "v_mov_b32 v143, v142\n\t"
    "s_nop 1\n\t" "v_permlane16_swap_b32 v142, v143\n\t" "v_add_f32_e64 v142, v142, v143\n\t" "v_mfma_f32_16x16x32_bf16 v[116:119], v[112:115], a[72:75], v[116:119]\n\t" "v_mov_b32 v143, v142\n\t"
    "s_nop 1\n\t" "v_permlane32_swap_b32 v142, v143\n\t" "v_add_f32 v142, v142, v143\n\t" "v_mfma_f32_16x16x32_bf16 v[128:131], a[72:75], a[72:75], v[128:131]\n\t" "s_waitcnt lgkmcnt(0)\n\t"
    "s_add_u32 m0, %[sl], 69504\n\t" "v_mfma_f32_16x16x32_bf16 v[120:123], v[112:115], a[76:79], v[120:123]\n\t" "global_load_lds_dwordx4 v196, s[64:65] offset:128\n\t" "s_add_u32 m0, %[sl], 81792\n\t"
    "v_mfma_f32_16x16x32_bf16 v[132:135], a[76:79], a[76:79], v[132:135]\n\t" "global_load_lds_dwordx4 v197, s[64:65] offset:128\n\t" "s_add_u32 m0, %[sl], 71552\n\t" "s_nop 0\n\t"
    "global_load_lds_dwordx4 v198, s[64:65] offset:128\n\t" "s_add_u32 m0, %[sl], 83840\n\t" "s_nop 0\n\t" "global_load_lds_dwordx4 v199, s[64:65] offset:128\n\t" "s_waitcnt vmcnt(20)\n\t"
    "v_mfma_f32_16x16x32_bf16 v[116:119], v[112:115], a[80:83], v[116:119]\n\t" "ds_read_b128 a[96:99], v202 offset:0\n\t" "ds_read_b128 a[100:103], v202 offset:2048\n\t"
    "v_mfma_f32_16x16x32_bf16 v[128:131], a[80:83], a[80:83], v[128:131]\n\t" "ds_read_b128 a[104:107], v203 offset:0\n\t" "ds_read_b128 a[108:111], v203 offset:2048\n\t"
    "v_mfma_f32_16x16x32_bf16 v[120:123], v[112:115], a[84:87], v[120:123]\n\t" "s_add_u32 s45, %[k0], 1280\n\t" "s_and_b32 s45, s45, 0x7ff\n\t" "s_add_u32 s64, %[xl], s45\n\t"
    "s_addc_u32 s65, %[xh_], 0\n\t" "v_mfma_f32_16x16x32_bf16 v[132:135], a[84:87], a[84:87], v[132:135]\n\t" "v_mfma_f32_16x16x32_bf16 v[116:119], v[112:115], a[88:91], v[116:119]\n\t"
    "v_mfma_f32_16x16x32_bf16 v[128:131], a[88:91], a[88:91], v[128:131]\n\t" "s_waitcnt lgkmcnt(0)\n\t" "s_add_u32 m0, %[sl], 20480\n\t"
    "v_mfma_f32_16x16x32_bf16 v[120:123], v[112:115], a[92:95], v[120:123]\n\t" "global_load_lds_dwordx4 v196, s[64:65] offset:0\n\t" "s_add_u32 m0, %[sl], 32768\n\t"
    "v_mfma_f32_16x16x32_bf16 v[132:135], a[92:95], a[92:95], v[132:135]\n\t" "global_load_lds_dwordx4 v197, s[64:65] offset:0\n\t" "s_add_u32 m0, %[sl], 22528\n\t" "s_nop 0\n\t"
    "global_load_lds_dwordx4 v198, s[64:65] offset:0\n\t" "s_add_u32 m0, %[sl], 34816\n\t" "s_nop 0\n\t" "global_load_lds_dwordx4 v199, s[64:65] offset:0\n\t" "s_waitcnt vmcnt(20)\n\t"
    "v_mfma_f32_16x16x32_bf16 v[116:119], v[112:115], a[96:99], v[116:119]\n\t" "ds_read_b128 a[112:115], v202 offset:4096\n\t" "ds_read_b128 a[116:119], v202 offset:24576\n\t"
    "v_mfma_f32_16x16x32_bf16 v[128:131], a[96:99], a[96:99], v[128:131]\n\t" "ds_read_b128 a[120:123], v203 offset:4096\n\t" "ds_read_b128 a[124:127], v203 offset:24576\n\t"
    "v_mfma_f32_16x16x32_bf16 v[120:123], v[112:115], a[100:103], v[120:123]\n\t" "v_mfma_f32_16x16x32_bf16 v[132:135], a[100:103], a[100:103], v[132:135]\n\t"
    "v_mfma_f32_16x16x32_bf16 v[116:119], v[112:115], a[104:107], v[116:119]\n\t" "v_mfma_f32_16x16x32_bf16 v[128:131], a[104:107], a[104:107], v[128:131]\n\t" "s_waitcnt lgkmcnt(0)\n\t"
    "s_add_u32 m0, %[sl], 24448\n\t" "v_mfma_f32_16x16x32_bf16 v[120:123], v[112:115], a[108:111], v[120:123]\n\t" "global_load_lds_dwordx4 v196, s[64:65] offset:128\n\t" "s_add_u32 m0, %[sl], 36736\n\t"
    "v_mfma_f32_16x16x32_bf16 v[132:135], a[108:111], a[108:111], v[132:135]\n\t" "global_load_lds_dwordx4 v197, s[64:65] offset:128\n\t" "s_add_u32 m0, %[sl], 44928\n\t" "s_nop 0\n\t"
    "global_load_lds_dwordx4 v198, s[64:65] offset:128\n\t" "s_add_u32 m0, %[sl], 57216\n\t" "s_nop 0\n\t" "global_load_lds_dwordx4 v199, s[64:65] offset:128\n\t" "s_waitcnt vmcnt(12)\n\t"
    "v_mfma_f32_16x16x32_bf16 v[116:119], v[112:115], a[112:115], v[116:119]\n\t" "ds_read_b128 a[128:131], v202 offset:26624\n\t" "ds_read_b128 a[132:135], v202 offset:28672\n\t"
    "v_mfma_f32_16x16x32_bf16 v[128:131], a[112:115], a[112:115], v[128:131]\n\t" "ds_read_b128 a[136:139], v203 offset:26624\n\t" "ds_read_b128 a[140:143], v203 offset:28672\n\t"
    "v_mfma_f32_16x16x32_bf16 v[120:123], v[112:115], a[116:119], v[120:123]\n\t" "s_add_u32 s45, %[k0], 1536\n\t" "s_and_b32 s45, s45, 0x7ff\n\t" "s_add_u32 s64, %[xl], s45\n\t"
    "s_addc_u32 s65, %[xh_], 0\n\t" "v_xad_u32 v144, v80, v81, s62\n\t" "v_xad_u32 v145, v84, v85, s62\n\t" "v_xad_u32 v146, v88, v89, s62\n\t"
    "v_mfma_f32_16x16x32_bf16 v[132:135], a[116:119], a[116:119], v[132:135]\n\t" "v_xad_u32 v147, v92, v93, s62\n\t" "v_xad_u32 v148, v96, v97, s62\n\t" "v_xad_u32 v149, v100, v101, s62\n\t"
    "v_xad_u32 v150, v104, v105, s62\n\t" "v_mfma_f32_16x16x32_bf16 v[116:119], v[112:115], a[120:123], v[116:119]\n\t" "v_xad_u32 v151, v108, v109, s62\n\t" "v_xad_u32 v144, v82, v81, v144\n\t"
    "v_xad_u32 v145, v86, v85, v145\n\t" "v_xad_u32 v146, v90, v89, v146\n\t" "v_mfma_f32_16x16x32_bf16 v[128:131], a[120:123], a[120:123], v[128:131]\n\t" "v_xad_u32 v147, v94, v93, v147\n\t"
    "v_xad_u32 v148, v98, v97, v148\n\t" "v_xad_u32 v149, v102, v101, v149\n\t" "v_xad_u32 v150, v106, v105, v150\n\t" "s_waitcnt lgkmcnt(0)\n\t" "s_add_u32 m0, %[sl], 47104\n\t"
    "v_mfma_f32_16x16x32_bf16 v[120:123], v[112:115], a[124:127], v[120:123]\n\t" "global_load_lds_dwordx4 v196, s[64:65] offset:0\n\t" "s_add_u32 m0, %[sl], 59392\n\t"
    "v_mfma_f32_16x16x32_bf16 v[132:135], a[124:127], a[124:127], v[132:135]\n\t" "global_load_lds_dwordx4 v197, s[64:65] offset:0\n\t" "s_add_u32 m0, %[sl], 49152\n\t" "s_nop 0\n\t"
    "global_load_lds_dwordx4 v198, s[64:65] offset:0\n\t" "s_add_u32 m0, %[sl], 61440\n\t" "s_nop 0\n\t" "global_load_lds_dwordx4 v199, s[64:65] offset:0\n\t" "s_waitcnt vmcnt(12)\n\t"
    "v_mfma_f32_16x16x32_bf16 v[116:119], v[112:115], a[128:131], v[116:119]\n\t" "ds_read_b128 a[144:147], v202 offset:49152\n\t" "ds_read_b128 a[148:151], v202 offset:51200\n\t"
    "v_mfma_f32_16x16x32_bf16 v[128:131], a[128:131], a[128:131], v[128:131]\n\t" "ds_read_b128 a[152:155], v203 offset:49152\n\t" "ds_read_b128 a[156:159], v203 offset:51200\n\t"
    "v_mfma_f32_16x16x32_bf16 v[120:123], v[112:115], a[132:135], v[120:123]\n\t" "v_xad_u32 v151, v110, v109, v151\n\t" "v_xor_b32_e64 v140, v144, v83\n\t"
    "v_bitop3_b32 v140, v140, v145, v87 bitop3:0xf6\n\t" "v_bitop3_b32 v140, v140, v146, v91 bitop3:0xf6\n\t" "v_mfma_f32_16x16x32_bf16 v[132:135], a[132:135], a[132:135], v[132:135]\n\t"
    "v_bitop3_b32 v140, v140, v147, v95 bitop3:0xf6\n\t" "v_bitop3_b32 v140, v140, v148, v99 bitop3:0xf6\n\t" "v_bitop3_b32 v140, v140, v149, v103 bitop3:0xf6\n\t"
    "v_bitop3_b32 v140, v140, v150, v107 bitop3:0xf6\n\t" "v_mfma_f32_16x16x32_bf16 v[116:119], v[112:115], a[136:139], v[116:119]\n\t" "v_bitop3_b32 v140, v140, v151, v111 bitop3:0xf6\n\t"
    "v_cmp_ne_u32_e64 vcc, 0, v140\n\t" "v_perm_b32 v83, v80, v81, s57\n\t" "v_perm_b32 v87, v84, v85, s57\n\t" "v_mfma_f32_16x16x32_bf16 v[128:131], a[136:139], a[136:139], v[128:131]\n\t"
    "v_perm_b32 v91, v88, v89, s57\n\t" "v_perm_b32 v95, v92, v93, s57\n\t" "v_perm_b32 v99, v96, v97, s57\n\t" "v_perm_b32 v103, v100, v101, s57\n\t" "s_waitcnt lgkmcnt(0)\n\t"
    "s_add_u32 m0, %[sl], 69504\n\t" "v_mfma_f32_16x16x32_bf16 v[120:123], v[112:115], a[140:143], v[120:123]\n\t" "global_load_lds_dwordx4 v196, s[64:65] offset:128\n\t" "s_add_u32 m0, %[sl], 81792\n\t"
    "v_mfma_f32_16x16x32_bf16 v[132:135], a[140:143], a[140:143], v[132:135]\n\t" "global_load_lds_dwordx4 v197, s[64:65] offset:128\n\t" "s_add_u32 m0, %[sl], 71552\n\t" "s_nop 0\n\t"
    "global_load_lds_dwordx4 v198, s[64:65] offset:128\n\t" "s_add_u32 m0, %[sl], 83840\n\t" "s_nop 0\n\t" "global_load_lds_dwordx4 v199, s[64:65] offset:128\n\t" "s_waitcnt vmcnt(12)\n\t"
    "v_mfma_f32_16x16x32_bf16 v[116:119], v[112:115], a[144:147], v[116:119]\n\t" "ds_read_b128 a[160:163], v202 offset:0\n\t" "ds_read_b128 a[164:167], v202 offset:2048\n\t"
    "v_mfma_f32_16x16x32_bf16 v[128:131], a[144:147], a[144:147], v[128:131]\n\t" "ds_read_b128 a[168:171], v203 offset:0\n\t" "ds_read_b128 a[172:175], v203 offset:2048\n\t"
    "v_mfma_f32_16x16x32_bf16 v[120:123], v[112:115], a[148:151], v[120:123]\n\t" "s_add_u32 s45, %[k0], 1792\n\t" "s_and_b32 s45, s45, 0x7ff\n\t" "s_add_u32 s64, %[xl], s45\n\t"
    "s_addc_u32 s65, %[xh_], 0\n\t" "v_perm_b32 v107, v104, v105, s57\n\t" "v_perm_b32 v111, v108, v109, s57\n\t" "s_cbranch_vccnz .Lslow_%=\n\t" ".Lgo_%=:\n\t" "v_perm_b32 v83, v83, v82, s58\n\t"
    "v_mfma_f32_16x16x32_bf16 v[132:135], a[148:151], a[148:151], v[132:135]\n\t" "v_perm_b32 v87, v87, v86, s58\n\t" "v_perm_b32 v91, v91, v90, s58\n\t" "v_perm_b32 v95, v95, v94, s58\n\t"
    "v_perm_b32 v99, v99, v98, s58\n\t" "v_mfma_f32_16x16x32_bf16 v[116:119], v[112:115], a[152:155], v[116:119]\n\t" "v_perm_b32 v103, v103, v102, s58\n\t" "v_perm_b32 v107, v107, v106, s58\n\t"
    "v_perm_b32 v111, v111, v110, s58\n\t" "v_add_f32 v96, v96, v100\n\t" "v_mfma_f32_16x16x32_bf16 v[128:131], a[152:155], a[152:155], v[128:131]\n\t" "v_add_f32 v97, v97, v101\n\t"
    "v_add_f32 v98, v98, v102\n\t" "v_add_f32 v99, v99, v103\n\t" "v_add_f32_e64 v80, v80, v84\n\t" "s_waitcnt lgkmcnt(0)\n\t" "s_add_u32 m0, %[sl], 20480\n\t"
    "v_mfma_f32_16x16x32_bf16 v[120:123], v[112:115], a[156:159], v[120:123]\n\t" "global_load_lds_dwordx4 v196, s[64:65] offset:0\n\t" "s_add_u32 m0, %[sl], 32768\n\t"
    "v_mfma_f32_16x16x32_bf16 v[132:135], a[156:159], a[156:159], v[132:135]\n\t" "global_load_lds_dwordx4 v197, s[64:65] offset:0\n\t" "s_add_u32 m0, %[sl], 22528\n\t" "s_nop 0\n\t"
    "global_load_lds_dwordx4 v198, s[64:65] offset:0\n\t" "s_add_u32 m0, %[sl], 34816\n\t" "s_nop 0\n\t" "global_load_lds_dwordx4 v199, s[64:65] offset:0\n\t" "s_waitcnt vmcnt(12)\n\t"
    "v_mfma_f32_16x16x32_bf16 v[116:119], v[112:115], a[160:163], v[116:119]\n\t" "ds_read_b128 a[176:179], v202 offset:4096\n\t" "ds_read_b128 a[180:183], v202 offset:24576\n\t"
    "v_mfma_f32_16x16x32_bf16 v[128:131], a[160:163], a[160:163], v[128:131]\n\t" "ds_read_b128 a[184:187], v203 offset:4096\n\t" "ds_read_b128 a[188:191], v203 offset:24576\n\t"
    "v_mfma_f32_16x16x32_bf16 v[120:123], v[112:115], a[164:167], v[120:123]\n\t" "v_add_f32 v81, v81, v85\n\t" "v_add_f32 v82, v82, v86\n\t" "v_add_f32 v83, v83, v87\n\t"
    "v_add_f32_e64 v104, v104, v108\n\t" "v_mfma_f32_16x16x32_bf16 v[132:135], a[164:167], a[164:167], v[132:135]\n\t" "v_add_f32 v105, v105, v109\n\t" "v_add_f32 v106, v106, v110\n\t"
    "v_add_f32 v107, v107, v111\n\t" "v_add_f32 v88, v88, v92\n\t" "v_mfma_f32_16x16x32_bf16 v[116:119], v[112:115], a[168:171], v[116:119]\n\t" "v_add_f32 v89, v89, v93\n\t" "v_add_f32 v90, v90, v94\n\t"
    "v_add_f32 v91, v91, v95\n\t" "v_add_f32 v96, v96, v104\n\t" "v_mfma_f32_16x16x32_bf16 v[128:131], a[168:171], a[168:171], v[128:131]\n\t" "v_add_f32 v97, v97, v105\n\t" "v_add_f32 v98, v98, v106\n\t"
    "v_add_f32 v99, v99, v107\n\t" "v_add_f32_e64 v80, v80, v88\n\t" "s_waitcnt lgkmcnt(0)\n\t" "s_add_u32 m0, %[sl], 24448\n\t"
    "v_mfma_f32_16x16x32_bf16 v[120:123], v[112:115], a[172:175], v[120:123]\n\t" "global_load_lds_dwordx4 v196, s[64:65] offset:128\n\t" "s_add_u32 m0, %[sl], 36736\n\t"
    "v_mfma_f32_16x16x32_bf16 v[132:135], a[172:175], a[172:175], v[132:135]\n\t" "global_load_lds_dwordx4 v197, s[64:65] offset:128\n\t" "s_add_u32 m0, %[sl], 44928\n\t" "s_nop 0\n\t"
    "global_load_lds_dwordx4 v198, s[64:65] offset:128\n\t" "s_add_u32 m0, %[sl], 57216\n\t" "s_nop 0\n\t" "global_load_lds_dwordx4 v199, s[64:65] offset:128\n\t" "s_waitcnt vmcnt(12)\n\t"
    "v_mfma_f32_16x16x32_bf16 v[116:119], v[112:115], a[176:179], v[116:119]\n\t" "ds_read_b128 a[192:195], v202 offset:26624\n\t" "ds_read_b128 a[196:199], v202 offset:28672\n\t"
    "v_add_f32 v81, v81, v89\n\t" "v_add_f32 v82, v82, v90\n\t" "v_add_f32 v83, v83, v91\n\t" "v_cvt_pk_bf16_f32 v96, v96, v97\n\t"
    "v_mfma_f32_16x16x32_bf16 v[128:131], a[176:179], a[176:179], v[128:131]\n\t" "ds_read_b128 a[200:203], v203 offset:26624\n\t" "ds_read_b128 a[204:207], v203 offset:28672\n\t"
    "v_cvt_pk_bf16_f32 v98, v98, v99\n\t" "v_cvt_pk_bf16_f32 v80, v80, v81\n\t" "v_cvt_pk_bf16_f32 v81, v82, v83\n\t" "v_xor_b32_e64 v82, 16, v232\n\t"
    "v_mfma_f32_16x16x32_bf16 v[120:123], v[112:115], a[180:183], v[120:123]\n\t" "v_and_b32 v97, 0xffff0000, v96\n\t" "v_lshlrev_b32_e64 v96, 16, v96\n\t" "v_and_b32 v99, 0xffff0000, v98\n\t"
    "v_lshlrev_b32_e64 v98, 16, v98\n\t" "v_mfma_f32_16x16x32_bf16 v[132:135], a[180:183], a[180:183], v[132:135]\n\t" "v_add_f32 v96, 1.0, v96\n\t" "v_add_f32 v97, 1.0, v97\n\t"
    "v_add_f32 v98, 1.0, v98\n\t" "v_add_f32 v99, 1.0, v99\n\t" "v_mfma_f32_16x16x32_bf16 v[116:119], v[112:115], a[184:187], v[116:119]\n\t" "v_cvt_pk_bf16_f32 v96, v96, v97\n\t"
    "v_cvt_pk_bf16_f32 v97, v98, v99\n\t" "v_mov_b32 v101, 0\n\t" "v_mov_b32 v103, 0\n\t" "v_mfma_f32_16x16x32_bf16 v[128:131], a[184:187], a[184:187], v[128:131]\n\t" "v_mov_b32 v104, 0\n\t"
    "v_mov_b32 v106, 0\n\t" "v_and_b32 v100, 0xffff, v96\n\t" "v_and_b32 v102, 0xffff0000, v96\n\t" "s_waitcnt lgkmcnt(0)\n\t" "v_mfma_f32_16x16x32_bf16 v[120:123], v[112:115], a[188:191], v[120:123]\n\t"
    "v_and_b32 v105, 0xffff, v97\n\t" "v_and_b32 v107, 0xffff0000, v97\n\t" "v_mfma_f32_16x16x32_bf16 v[132:135], a[188:191], a[188:191], v[132:135]\n\t" "s_waitcnt vmcnt(8)\n\t"
    "v_mfma_f32_16x16x32_bf16 v[116:119], v[112:115], a[192:195], v[116:119]\n\t" "ds_read_b128 a[208:211], v202 offset:49152\n\t" "ds_read_b128 a[212:215], v202 offset:51200\n\t"
    "v_mfma_f32_16x16x32_bf16 v[128:131], a[192:195], a[192:195], v[128:131]\n\t" "ds_read_b128 a[216:219], v203 offset:49152\n\t" "ds_read_b128 a[220:223], v203 offset:51200\n\t"
    "v_mfma_f32_16x16x32_bf16 v[120:123], v[112:115], a[196:199], v[120:123]\n\t" "v_mfma_f32_16x16x32_bf16 v[132:135], a[196:199], a[196:199], v[132:135]\n\t"
    "v_mfma_f32_16x16x32_bf16 v[116:119], v[112:115], a[200:203], v[116:119]\n\t" "v_mfma_f32_16x16x32_bf16 v[128:131], a[200:203], a[200:203], v[128:131]\n\t" "s_waitcnt lgkmcnt(0)\n\t"
    "v_mfma_f32_16x16x32_bf16 v[120:123], v[112:115], a[204:207], v[120:123]\n\t" "v_mfma_f32_16x16x32_bf16 v[132:135], a[204:207], a[204:207], v[132:135]\n\t" "s_waitcnt vmcnt(4)\n\t"
    "v_mfma_f32_16x16x32_bf16 v[116:119], v[112:115], a[208:211], v[116:119]\n\t" "ds_read_b128 a[224:227], v202 offset:0\n\t" "ds_read_b128 a[228:231], v202 offset:2048\n\t"
    "v_mfma_f32_16x16x32_bf16 v[128:131], a[208:211], a[208:211], v[128:131]\n\t" "ds_read_b128 a[232:235], v203 offset:0\n\t" "ds_read_b128 a[236:239], v203 offset:2048\n\t"
    "v_mfma_f32_16x16x32_bf16 v[120:123], v[112:115], a[212:215], v[120:123]\n\t" "v_mfma_f32_16x16x32_bf16 v[132:135], a[212:215], a[212:215], v[132:135]\n\t"
    "v_mfma_f32_16x16x32_bf16 v[116:119], v[112:115], a[216:219], v[116:119]\n\t" "v_mfma_f32_16x16x32_bf16 v[128:131], a[216:219], a[216:219], v[128:131]\n\t" "s_waitcnt lgkmcnt(0)\n\t"
    "v_mfma_f32_16x16x32_bf16 v[120:123], v[112:115], a[220:223], v[120:123]\n\t" "v_mfma_f32_16x16x32_bf16 v[132:135], a[220:223], a[220:223], v[132:135]\n\t" "s_waitcnt vmcnt(0)\n\t"
    "v_mfma_f32_16x16x32_bf16 v[116:119], v[112:115], a[224:227], v[116:119]\n\t" "ds_read_b128 a[240:243], v202 offset:4096\n\t" "ds_read_b128 a[244:247], v202 offset:24576\n\t"
    "v_mfma_f32_16x16x32_bf16 v[128:131], a[224:227], a[224:227], v[128:131]\n\t" "ds_read_b128 a[248:251], v203 offset:4096\n\t" "ds_read_b128 a[252:255], v203 offset:24576\n\t"
    "v_mfma_f32_16x16x32_bf16 v[120:123], v[112:115], a[228:231], v[120:123]\n\t" "ds_write2_b32 v235, v142, v124 offset1:1\n\t"
    "v_mfma_f32_16x16x32_bf16 v[132:135], a[228:231], a[228:231], v[132:135]\n\t" "ds_write2_b64 v231, v[100:101], v[102:103] offset0:0 offset1:8\n\t"
    "v_mfma_f32_16x16x32_bf16 v[116:119], v[112:115], a[232:235], v[116:119]\n\t" "ds_write2_b64 v231, v[104:105], v[106:107] offset0:16 offset1:24\n\t"
    "v_mfma_f32_16x16x32_bf16 v[128:131], a[232:235], a[232:235], v[128:131]\n\t" "ds_write_b64 v82, v[80:81]\n\t" "s_waitcnt lgkmcnt(4)\n\t"
    "v_mfma_f32_16x16x32_bf16 v[120:123], v[112:115], a[236:239], v[120:123]\n\t" "ds_write_b64 v232, v[96:97]\n\t" "v_mfma_f32_16x16x32_bf16 v[132:135], a[236:239], a[236:239], v[132:135]\n\t"
    "s_add_u32 m0, %[sl], 120832\n\t" "s_nop 0\n\t" "global_load_lds_dwordx4 v0, %[sb] offset:-2048\n\t" "global_load_lds_dwordx4 v0, %[sb] offset:0\n\t"
    "global_load_lds_dwordx4 v0, %[sb] offset:2048\n\t" "v_add_u32 v0, 0x30000, v0\n\t" "s_waitcnt lgkmcnt(0)\n\t" "s_barrier\n\t" "ds_read_b128 v[220:223], v2 offset:0\n\t"
    "ds_read_b128 v[44:47], v18 offset:0\n\t" "v_mfma_f32_16x16x32_bf16 v[116:119], v[112:115], a[240:243], v[116:119]\n\t" "ds_read_b128 v[242:245], v18 offset:256\n\t"
    "ds_read_b128 v[224:227], v2 offset:256\n\t" "v_mfma_f32_16x16x32_bf16 v[128:131], a[240:243], a[240:243], v[128:131]\n\t" "ds_read2_b64 v[148:151], v246 offset0:0 offset1:16\n\t"
    "ds_read2_b64 v[152:155], v246 offset0:32 offset1:48\n\t" "v_mfma_f32_16x16x32_bf16 v[120:123], v[112:115], a[244:247], v[120:123]\n\t" "ds_read_b64_tr_b16 v[156:157], v8 offset:8192\n\t"
    "ds_read_b64_tr_b16 v[158:159], v8 offset:8704\n\t" "v_mfma_f32_16x16x32_bf16 v[132:135], a[244:247], a[244:247], v[132:135]\n\t" "ds_read_b64_tr_b16 v[160:161], v9 offset:8192\n\t"
    "ds_read_b64_tr_b16 v[162:163], v9 offset:8704\n\t" "v_mfma_f32_16x16x32_bf16 v[116:119], v[112:115], a[248:251], v[116:119]\n\t" "ds_read_b64_tr_b16 v[164:165], v10 offset:8192\n\t"
    "ds_read_b64_tr_b16 v[166:167], v10 offset:8704\n\t" "v_mfma_f32_16x16x32_bf16 v[128:131], a[248:251], a[248:251], v[128:131]\n\t" "ds_read_b64_tr_b16 v[168:169], v8 offset:10240\n\t"
    "ds_read_b64_tr_b16 v[170:171], v8 offset:10752\n\t" "v_mfma_f32_16x16x32_bf16 v[120:123], v[112:115], a[252:255], v[120:123]\n\t" "ds_read_b64_tr_b16 v[172:173], v9 offset:10240\n\t"
    "s_waitcnt lgkmcnt(11)\n\t" "ds_read_b64_tr_b16 v[174:175], v9 offset:10752\n\t" "v_mfma_f32_16x16x32_bf16 v[132:135], a[252:255], a[252:255], v[132:135]\n\t"
    "ds_read_b64_tr_b16 v[176:177], v10 offset:10240\n\t" "ds_read_b64_tr_b16 v[178:179], v10 offset:10752\n\t" "v_mfma_f32_4x4x4_16b_bf16 v[204:207], v[220:221], a[0:1], 0\n\t"
    "v_mfma_f32_4x4x4_16b_bf16 v[208:211], v[222:223], a[2:3], 0\n\t" "v_mfma_f32_4x4x4_16b_bf16 v[212:215], v[220:221], a[4:5], 0\n\t" "v_mfma_f32_4x4x4_16b_bf16 v[216:219], v[222:223], a[6:7], 0\n\t"
    "v_mfma_f32_4x4x4_16b_bf16 v[56:59], v[44:45], v[48:49], 0\n\t" "v_mfma_f32_4x4x4_16b_bf16 v[60:63], v[46:47], v[50:51], 0\n\t" "v_mfma_f32_4x4x4_16b_bf16 v[64:67], v[242:243], v[52:53], 0\n\t"
    "v_mfma_f32_4x4x4_16b_bf16 v[68:71], v[244:245], v[54:55], 0\n\t" "s_waitcnt lgkmcnt(12)\n\t" "v_add_f32 v148, v148, v150\n\t" "v_add_f32 v152, v152, v154\n\t" "v_add_f32 v149, v149, v151\n\t"
    "v_add_f32 v153, v153, v155\n\t" "v_add_f32 v241, v148, v152\n\t" "v_add_f32 v238, v149, v153\n\t" "v_cvt_pk_bf16_f32 v48, v56, v57\n\t" "v_cvt_pk_bf16_f32 v49, v58, v59\n\t"
    "v_cvt_pk_bf16_f32 v50, v60, v61\n\t" "v_cvt_pk_bf16_f32 v51, v62, v63\n\t" "v_cvt_pk_bf16_f32 v52, v64, v65\n\t" "v_cvt_pk_bf16_f32 v53, v66, v67\n\t" "v_cvt_pk_bf16_f32 v54, v68, v69\n\t"
    "v_cvt_pk_bf16_f32 v55, v70, v71\n\t" "s_mov_b64 exec, s[60:61]\n\t" "ds_write_b128 v19, v[48:51] offset:0\n\t" "ds_write_b128 v19, v[52:55] offset:1024\n\t" "s_mov_b64 exec, -1\n\t"
    "v_cvt_pk_bf16_f32 v188, v204, v205\n\t" "v_cvt_pk_bf16_f32 v189, v206, v207\n\t" "v_cvt_pk_bf16_f32 v190, v208, v209\n\t" "v_cvt_pk_bf16_f32 v191, v210, v211\n\t"
    "v_cvt_pk_bf16_f32 v192, v212, v213\n\t" "v_cvt_pk_bf16_f32 v193, v214, v215\n\t" "v_cvt_pk_bf16_f32 v194, v216, v217\n\t" "v_cvt_pk_bf16_f32 v195, v218, v219\n\t" "s_nop 1\n\t"
    "s_waitcnt lgkmcnt(6)\n\t" "v_mfma_f32_16x16x32_bf16 v[56:59], v[156:159], v[188:191], 0\n\t" "ds_read_b64_tr_b16 v[180:181], v7 offset:8192\n\t" "s_add_u32 m0, %[sl], 71680\n\t"
    "v_mfma_f32_16x16x32_bf16 v[60:63], v[156:159], v[192:195], 0\n\t" "ds_read_b64_tr_b16 v[182:183], v7 offset:8704\n\t" "v_mfma_f32_16x16x32_bf16 v[64:67], v[160:163], v[188:191], 0\n\t"
    "ds_read_b64_tr_b16 v[184:185], v7 offset:10240\n\t" "global_load_lds_dwordx4 v0, %[sb] offset:-2048\n\t" "v_mfma_f32_16x16x32_bf16 v[68:71], v[160:163], v[192:195], 0\n\t"
    "ds_read_b64_tr_b16 v[186:187], v7 offset:10752\n\t" "v_mfma_f32_4x4x4_16b_bf16 v[204:207], v[224:225], a[8:9], 0\n\t" "v_mfma_f32_4x4x4_16b_bf16 v[208:211], v[226:227], a[10:11], 0\n\t"
    "v_mfma_f32_4x4x4_16b_bf16 v[212:215], v[224:225], a[12:13], 0\n\t" "v_mfma_f32_4x4x4_16b_bf16 v[216:219], v[226:227], a[14:15], 0\n\t" "v_cndmask_b32 v239, 0, v128, s[46:47]\n\t"
    "v_mfma_f32_16x16x32_bf16 v[72:75], v[164:167], v[188:191], 0\n\t" "ds_read_b64_tr_b16 v[156:157], v7 offset:12288\n\t" "v_cndmask_b32 v239, v239, v129, s[48:49]\n\t"
    "v_mfma_f32_16x16x32_bf16 v[76:79], v[164:167], v[192:195], 0\n\t" "ds_read_b64_tr_b16 v[158:159], v7 offset:12800\n\t" "global_load_lds_dwordx4 v0, %[sb] offset:0\n\t"
    "v_mfma_f32_16x16x32_bf16 v[88:91], v[168:171], v[188:191], 0\n\t" "s_waitcnt lgkmcnt(6)\n\t" "s_barrier\n\t" "ds_read_b128 v[228:231], v1 offset:0\n\t"
    "ds_read_b64_tr_b16 v[160:161], v8 offset:12288\n\t" "v_cvt_pk_bf16_f32 v196, v204, v205\n\t" "v_mfma_f32_16x16x32_bf16 v[92:95], v[168:171], v[192:195], 0\n\t"
    "ds_read_b64_tr_b16 v[162:163], v8 offset:12800\n\t" "v_cvt_pk_bf16_f32 v197, v206, v207\n\t" "s_waitcnt lgkmcnt(5)\n\t" "v_mfma_f32_16x16x32_bf16 v[96:99], v[172:175], v[188:191], 0\n\t"
    "ds_read_b64_tr_b16 v[164:165], v9 offset:12288\n\t" "v_cndmask_b32 v239, v239, v130, s[50:51]\n\t" "v_mfma_f32_16x16x32_bf16 v[100:103], v[172:175], v[192:195], 0\n\t"
    "ds_read_b64_tr_b16 v[166:167], v9 offset:12800\n\t" "v_cvt_pk_bf16_f32 v198, v208, v209\n\t" "v_mfma_f32_16x16x32_bf16 v[104:107], v[176:179], v[188:191], 0\n\t"
    "ds_read_b64_tr_b16 v[168:169], v10 offset:12288\n\t" "v_cvt_pk_bf16_f32 v199, v210, v211\n\t" "v_mfma_f32_16x16x32_bf16 v[108:111], v[176:179], v[192:195], 0\n\t"
    "ds_read_b64_tr_b16 v[170:171], v10 offset:12800\n\t" "ds_read_b128 v[220:223], v2 offset:512\n\t" "v_mfma_f32_16x16x32_bf16 v[48:51], v[180:183], v[188:191], 0\n\t"
    "ds_read_b64_tr_b16 v[172:173], v7 offset:20480\n\t" "v_cvt_pk_bf16_f32 v200, v212, v213\n\t" "v_mfma_f32_16x16x32_bf16 v[52:55], v[180:183], v[192:195], 0\n\t"
    "ds_read_b64_tr_b16 v[174:175], v7 offset:20992\n\t" "v_cvt_pk_bf16_f32 v201, v214, v215\n\t" "s_waitcnt lgkmcnt(9)\n\t" "v_mfma_f32_16x16x32_bf16 v[152:155], v[228:231], v[180:183], 0\n\t"
    "global_load_lds_dwordx4 v0, %[sb] offset:2048\n\t" "s_cmp_eq_u32 %[rot], 7\n\t" "s_cselect_b32 s42, s43, 0x30000\n\t" "v_add_u32 v0, s42, v0\n\t"
    "v_mfma_f32_16x16x32_bf16 v[80:83], v[184:187], v[188:191], 0\n\t" "ds_read_b64_tr_b16 v[176:177], v8 offset:20480\n\t" "v_cvt_pk_bf16_f32 v202, v216, v217\n\t"
    "v_mfma_f32_16x16x32_bf16 v[84:87], v[184:187], v[192:195], 0\n\t" "ds_read_b64_tr_b16 v[178:179], v8 offset:20992\n\t" "v_cvt_pk_bf16_f32 v203, v218, v219\n\t"
    "v_mfma_f32_16x16x32_bf16 v[144:147], v[184:187], v[228:231], 0\n\t" "v_cndmask_b32 v239, v239, v131, s[52:53]\n\t" "v_mov_b32_e64 v236, v116\n\t" "v_cndmask_b32 v240, 0, v132, s[46:47]\n\t"
    "v_cndmask_b32 v240, v240, v133, s[48:49]\n\t" "v_cndmask_b32 v240, v240, v134, s[50:51]\n\t" "v_cndmask_b32 v240, v240, v135, s[52:53]\n\t" "v_mov_b32 v237, v120\n\t" "s_waitcnt lgkmcnt(5)\n\t"
    "v_mfma_f32_16x16x32_bf16 v[112:115], v[188:191], v[156:159], 0\n\t" "ds_read_b64_tr_b16 v[180:181], v9 offset:20480\n\t" "s_add_u32 m0, %[sl], 83968\n\t"
    "v_mfma_f32_16x16x32_bf16 v[116:119], v[192:195], v[156:159], 0\n\t" "ds_read_b64_tr_b16 v[182:183], v9 offset:20992\n\t" "global_load_lds_dwordx4 v0, %[sb] offset:-2048\n\t"
    "v_mfma_f32_16x16x32_bf16 v[148:151], v[228:231], v[156:159], 0\n\t" "ds_read_b128 v[232:235], v1 offset:1024\n\t" "global_load_lds_dwordx4 v0, %[sb] offset:0\n\t"
    "v_mfma_f32_16x16x32_bf16 v[120:123], v[188:191], v[160:163], 0\n\t" "ds_read_b64_tr_b16 v[184:185], v10 offset:20480\n\t" "v_mfma_f32_16x16x32_bf16 v[124:127], v[192:195], v[160:163], 0\n\t"
    "ds_read_b64_tr_b16 v[186:187], v10 offset:20992\n\t" "v_mfma_f32_16x16x32_bf16 v[128:131], v[188:191], v[164:167], 0\n\t" "ds_read_b64_tr_b16 v[156:157], v7 offset:22528\n\t"
    "v_mfma_f32_16x16x32_bf16 v[132:135], v[192:195], v[164:167], 0\n\t" "ds_read_b64_tr_b16 v[158:159], v7 offset:23040\n\t" "global_load_lds_dwordx4 v0, %[sb] offset:2048\n\t"
    "v_mfma_f32_16x16x32_bf16 v[136:139], v[188:191], v[168:171], 0\n\t" "ds_read_b64_tr_b16 v[160:161], v8 offset:22528\n\t" "v_add_u32 v0, 0x30000, v0\n\t"
    "v_mfma_f32_16x16x32_bf16 v[140:143], v[192:195], v[168:171], 0\n\t" "ds_read_b64_tr_b16 v[162:163], v8 offset:23040\n\t" "s_waitcnt lgkmcnt(4)\n\t"
    "v_mfma_f32_16x16x32_bf16 v[48:51], v[172:175], v[196:199], v[48:51]\n\t" "ds_read_b64_tr_b16 v[164:165], v9 offset:22528\n\t" "s_add_u32 m0, %[sl], 22528\n\t"
    "v_mfma_f32_16x16x32_bf16 v[52:55], v[172:175], v[200:203], v[52:55]\n\t" "ds_read_b64_tr_b16 v[166:167], v9 offset:23040\n\t" "global_load_lds_dwordx4 v0, %[sb] offset:-2048\n\t"
    "v_mfma_f32_16x16x32_bf16 v[152:155], v[232:235], v[172:175], v[152:155]\n\t" "global_load_lds_dwordx4 v0, %[sb] offset:0\n\t" "v_mfma_f32_16x16x32_bf16 v[56:59], v[176:179], v[196:199], v[56:59]\n\t"
    "ds_read_b64_tr_b16 v[168:169], v10 offset:22528\n\t" "v_mfma_f32_4x4x4_16b_bf16 v[204:207], v[220:221], a[16:17], 0\n\t" "v_mfma_f32_4x4x4_16b_bf16 v[208:211], v[222:223], a[18:19], 0\n\t"
    "v_mfma_f32_4x4x4_16b_bf16 v[212:215], v[220:221], a[20:21], 0\n\t" "v_mfma_f32_4x4x4_16b_bf16 v[216:219], v[222:223], a[22:23], 0\n\t"
    "v_mfma_f32_16x16x32_bf16 v[60:63], v[176:179], v[200:203], v[60:63]\n\t" "ds_read_b64_tr_b16 v[170:171], v10 offset:23040\n\t"
    "v_mfma_f32_16x16x32_bf16 v[64:67], v[180:183], v[196:199], v[64:67]\n\t" "ds_read_b64_tr_b16 v[172:173], v7 offset:24576\n\t" "global_load_lds_dwordx4 v0, %[sb] offset:2048\n\t"
    "v_mfma_f32_16x16x32_bf16 v[68:71], v[180:183], v[200:203], v[68:71]\n\t" "ds_read_b64_tr_b16 v[174:175], v7 offset:25088\n\t" "v_cvt_pk_bf16_f32 v188, v204, v205\n\t"
    "v_mfma_f32_16x16x32_bf16 v[72:75], v[184:187], v[196:199], v[72:75]\n\t" "ds_read_b64_tr_b16 v[176:177], v8 offset:24576\n\t" "v_cvt_pk_bf16_f32 v189, v206, v207\n\t"
    "v_mfma_f32_16x16x32_bf16 v[76:79], v[184:187], v[200:203], v[76:79]\n\t" "ds_read_b64_tr_b16 v[178:179], v8 offset:25088\n\t" "s_waitcnt lgkmcnt(4)\n\t"
    "v_mfma_f32_16x16x32_bf16 v[80:83], v[156:159], v[196:199], v[80:83]\n\t" "ds_read_b64_tr_b16 v[180:181], v9 offset:24576\n\t" "v_cvt_pk_bf16_f32 v190, v208, v209\n\t"
    "v_mfma_f32_16x16x32_bf16 v[84:87], v[156:159], v[200:203], v[84:87]\n\t" "ds_read_b64_tr_b16 v[182:183], v9 offset:25088\n\t" "v_cvt_pk_bf16_f32 v191, v210, v211\n\t"
    "v_mfma_f32_16x16x32_bf16 v[144:147], v[156:159], v[232:235], v[144:147]\n\t" "ds_read_b128 v[224:227], v2 offset:768\n\t" "v_add_u32 v0, 0x30000, v0\n\t"
    "v_mfma_f32_16x16x32_bf16 v[88:91], v[160:163], v[196:199], v[88:91]\n\t" "ds_read_b64_tr_b16 v[184:185], v10 offset:24576\n\t" "v_cvt_pk_bf16_f32 v192, v212, v213\n\t"
    "v_mfma_f32_16x16x32_bf16 v[92:95], v[160:163], v[200:203], v[92:95]\n\t" "ds_read_b64_tr_b16 v[186:187], v10 offset:25088\n\t" "v_cvt_pk_bf16_f32 v193, v214, v215\n\t"
    "v_mfma_f32_16x16x32_bf16 v[96:99], v[164:167], v[196:199], v[96:99]\n\t" "s_waitcnt vmcnt(9)\n\t" "s_barrier\n\t" "ds_read_b64_tr_b16 v[156:157], v7 offset:32768\n\t"
    "v_mfma_f32_16x16x32_bf16 v[100:103], v[164:167], v[200:203], v[100:103]\n\t" "ds_read_b64_tr_b16 v[158:159], v7 offset:33280\n\t" "v_cvt_pk_bf16_f32 v194, v216, v217\n\t"
    "v_mfma_f32_16x16x32_bf16 v[104:107], v[168:171], v[196:199], v[104:107]\n\t" "ds_read_b64_tr_b16 v[160:161], v8 offset:32768\n\t" "v_cvt_pk_bf16_f32 v195, v218, v219\n\t"
    "v_mfma_f32_16x16x32_bf16 v[108:111], v[168:171], v[200:203], v[108:111]\n\t" "ds_read_b64_tr_b16 v[162:163], v8 offset:33280\n\t" "s_waitcnt lgkmcnt(4)\n\t"
    "v_mfma_f32_16x16x32_bf16 v[112:115], v[196:199], v[172:175], v[112:115]\n\t" "ds_read_b64_tr_b16 v[164:165], v9 offset:32768\n\t" "s_add_u32 m0, %[sl], 34816\n\t"
    "v_mfma_f32_16x16x32_bf16 v[116:119], v[200:203], v[172:175], v[116:119]\n\t" "ds_read_b64_tr_b16 v[166:167], v9 offset:33280\n\t" "global_load_lds_dwordx4 v0, %[sb] offset:-2048\n\t"
    "v_mfma_f32_16x16x32_bf16 v[148:151], v[232:235], v[172:175], v[148:151]\n\t" "ds_read_b128 v[228:231], v1 offset:2048\n\t" "global_load_lds_dwordx4 v0, %[sb] offset:0\n\t"
    "v_mfma_f32_16x16x32_bf16 v[120:123], v[196:199], v[176:179], v[120:123]\n\t" "ds_read_b64_tr_b16 v[168:169], v10 offset:32768\n\t" "global_load_lds_dwordx4 v0, %[sb] offset:2048\n\t"
    "v_mfma_f32_16x16x32_bf16 v[124:127], v[200:203], v[176:179], v[124:127]\n\t" "ds_read_b64_tr_b16 v[170:171], v10 offset:33280\n\t" "v_add_u32 v0, 0x30000, v0\n\t"
    "v_mfma_f32_16x16x32_bf16 v[128:131], v[196:199], v[180:183], v[128:131]\n\t" "ds_read_b64_tr_b16 v[172:173], v7 offset:34816\n\t"
    "v_mfma_f32_16x16x32_bf16 v[132:135], v[200:203], v[180:183], v[132:135]\n\t" "ds_read_b64_tr_b16 v[174:175], v7 offset:35328\n\t" "s_mov_b64 exec, %[tm]\n\t" "global_load_dword v17, v16, %[pf]\n\t"
    "s_mov_b64 exec, -1\n\t" "v_mfma_f32_16x16x32_bf16 v[136:139], v[196:199], v[184:187], v[136:139]\n\t" "ds_read_b64_tr_b16 v[176:177], v8 offset:34816\n\t"
    "v_mfma_f32_16x16x32_bf16 v[140:143], v[200:203], v[184:187], v[140:143]\n\t" "ds_read_b64_tr_b16 v[178:179], v8 offset:35328\n\t" "s_waitcnt lgkmcnt(4)\n\t"
    "v_mfma_f32_16x16x32_bf16 v[48:51], v[156:159], v[188:191], v[48:51]\n\t" "ds_read_b64_tr_b16 v[180:181], v9 offset:34816\n\t" "s_add_u32 m0, %[sl], 47104\n\t"
    "v_mfma_f32_16x16x32_bf16 v[52:55], v[156:159], v[192:195], v[52:55]\n\t" "ds_read_b64_tr_b16 v[182:183], v9 offset:35328\n\t"
    "v_mfma_f32_16x16x32_bf16 v[152:155], v[228:231], v[156:159], v[152:155]\n\t" "global_load_lds_dwordx4 v0, %[sb] offset:-2048\n\t"
    "v_mfma_f32_16x16x32_bf16 v[56:59], v[160:163], v[188:191], v[56:59]\n\t" "ds_read_b64_tr_b16 v[184:185], v10 offset:34816\n\t" "v_mfma_f32_4x4x4_16b_bf16 v[204:207], v[224:225], a[24:25], 0\n\t"
    "v_mfma_f32_4x4x4_16b_bf16 v[208:211], v[226:227], a[26:27], 0\n\t" "v_mfma_f32_4x4x4_16b_bf16 v[212:215], v[224:225], a[28:29], 0\n\t"
    "v_mfma_f32_4x4x4_16b_bf16 v[216:219], v[226:227], a[30:31], 0\n\t" "v_mfma_f32_16x16x32_bf16 v[60:63], v[160:163], v[192:195], v[60:63]\n\t" "ds_read_b64_tr_b16 v[186:187], v10 offset:35328\n\t"
    "v_mfma_f32_16x16x32_bf16 v[64:67], v[164:167], v[188:191], v[64:67]\n\t" "ds_read_b64_tr_b16 v[156:157], v7 offset:36864\n\t" "global_load_lds_dwordx4 v0, %[sb] offset:0\n\t"
    "v_mfma_f32_16x16x32_bf16 v[68:71], v[164:167], v[192:195], v[68:71]\n\t" "ds_read_b64_tr_b16 v[158:159], v7 offset:37376\n\t" "v_cvt_pk_bf16_f32 v196, v204, v205\n\t"
    "v_mfma_f32_16x16x32_bf16 v[72:75], v[168:171], v[188:191], v[72:75]\n\t" "ds_read_b64_tr_b16 v[160:161], v8 offset:36864\n\t" "v_cvt_pk_bf16_f32 v197, v206, v207\n\t"
    "v_mfma_f32_16x16x32_bf16 v[76:79], v[168:171], v[192:195], v[76:79]\n\t" "ds_read_b64_tr_b16 v[162:163], v8 offset:37376\n\t" "s_waitcnt lgkmcnt(4)\n\t"
    "v_mfma_f32_16x16x32_bf16 v[80:83], v[172:175], v[188:191], v[80:83]\n\t" "ds_read_b64_tr_b16 v[164:165], v9 offset:36864\n\t" "v_cvt_pk_bf16_f32 v198, v208, v209\n\t"
    "v_mfma_f32_16x16x32_bf16 v[84:87], v[172:175], v[192:195], v[84:87]\n\t" "ds_read_b64_tr_b16 v[166:167], v9 offset:37376\n\t" "v_cvt_pk_bf16_f32 v199, v210, v211\n\t"
    "v_mfma_f32_16x16x32_bf16 v[144:147], v[172:175], v[228:231], v[144:147]\n\t" "ds_read_b128 v[220:223], v2 offset:1024\n\t" "global_load_lds_dwordx4 v0, %[sb] offset:2048\n\t"
    "v_mfma_f32_16x16x32_bf16 v[88:91], v[176:179], v[188:191], v[88:91]\n\t" "ds_read_b64_tr_b16 v[168:169], v10 offset:36864\n\t" "v_cvt_pk_bf16_f32 v200, v212, v213\n\t"
    "v_mfma_f32_16x16x32_bf16 v[92:95], v[176:179], v[192:195], v[92:95]\n\t" "ds_read_b64_tr_b16 v[170:171], v10 offset:37376\n\t" "v_cvt_pk_bf16_f32 v201, v214, v215\n\t"
    "v_mfma_f32_16x16x32_bf16 v[96:99], v[180:183], v[188:191], v[96:99]\n\t" "s_waitcnt vmcnt(13)\n\t" "s_barrier\n\t" "ds_read_b64_tr_b16 v[172:173], v3 offset:49152\n\t"
    "v_mfma_f32_16x16x32_bf16 v[100:103], v[180:183], v[192:195], v[100:103]\n\t" "ds_read_b64_tr_b16 v[174:175], v3 offset:49664\n\t" "v_cvt_pk_bf16_f32 v202, v216, v217\n\t"
    "v_mfma_f32_16x16x32_bf16 v[104:107], v[184:187], v[188:191], v[104:107]\n\t" "ds_read_b64_tr_b16 v[176:177], v4 offset:49152\n\t" "v_cvt_pk_bf16_f32 v203, v218, v219\n\t"
    "v_mfma_f32_16x16x32_bf16 v[108:111], v[184:187], v[192:195], v[108:111]\n\t" "ds_read_b64_tr_b16 v[178:179], v4 offset:49664\n\t" "s_waitcnt lgkmcnt(4)\n\t"
    "v_mfma_f32_16x16x32_bf16 v[112:115], v[188:191], v[156:159], v[112:115]\n\t" "ds_read_b64_tr_b16 v[180:181], v5 offset:49152\n\t" "s_cmp_eq_u32 %[rot], 6\n\t" "s_cselect_b32 s42, s43, 0x30000\n\t"
    "v_add_u32 v0, s42, v0\n\t" "v_mfma_f32_16x16x32_bf16 v[116:119], v[192:195], v[156:159], v[116:119]\n\t" "ds_read_b64_tr_b16 v[182:183], v5 offset:49664\n\t" "s_add_u32 m0, %[sl], 59392\n\t"
    "v_mfma_f32_16x16x32_bf16 v[148:151], v[228:231], v[156:159], v[148:151]\n\t" "ds_read_b128 v[232:235], v1 offset:3072\n\t" "global_load_lds_dwordx4 v0, %[sb] offset:-2048\n\t"
    "v_mfma_f32_16x16x32_bf16 v[120:123], v[188:191], v[160:163], v[120:123]\n\t" "ds_read_b64_tr_b16 v[184:185], v6 offset:49152\n\t" "global_load_lds_dwordx4 v0, %[sb] offset:0\n\t"
    "v_mfma_f32_16x16x32_bf16 v[124:127], v[192:195], v[160:163], v[124:127]\n\t" "ds_read_b64_tr_b16 v[186:187], v6 offset:49664\n\t"
    "v_mfma_f32_16x16x32_bf16 v[128:131], v[188:191], v[164:167], v[128:131]\n\t" "ds_read_b64_tr_b16 v[156:157], v3 offset:51200\n\t"
    "v_mfma_f32_16x16x32_bf16 v[132:135], v[192:195], v[164:167], v[132:135]\n\t" "ds_read_b64_tr_b16 v[158:159], v3 offset:51712\n\t" "global_load_lds_dwordx4 v0, %[sb] offset:2048\n\t"
    "v_mfma_f32_16x16x32_bf16 v[136:139], v[188:191], v[168:171], v[136:139]\n\t" "ds_read_b64_tr_b16 v[160:161], v4 offset:51200\n\t" "v_add_u32 v0, 0x30000, v0\n\t"
    "v_mfma_f32_16x16x32_bf16 v[140:143], v[192:195], v[168:171], v[140:143]\n\t" "ds_read_b64_tr_b16 v[162:163], v4 offset:51712\n\t" "s_waitcnt lgkmcnt(4)\n\t"
    "v_mfma_f32_16x16x32_bf16 v[48:51], v[172:175], v[196:199], v[48:51]\n\t" "ds_read_b64_tr_b16 v[164:165], v5 offset:51200\n\t" "s_add_u32 m0, %[sl], 96256\n\t"
    "v_mfma_f32_16x16x32_bf16 v[52:55], v[172:175], v[200:203], v[52:55]\n\t" "ds_read_b64_tr_b16 v[166:167], v5 offset:51712\n\t" "ds_read_b128 v[44:47], v18 offset:2048\n\t"
    "v_mfma_f32_16x16x32_bf16 v[152:155], v[232:235], v[172:175], v[152:155]\n\t" "global_load_lds_dwordx4 v0, %[sb] offset:-2048\n\t" "ds_read_b128 v[242:245], v18 offset:2304\n\t"
    "v_mfma_f32_16x16x32_bf16 v[56:59], v[176:179], v[196:199], v[56:59]\n\t" "ds_read_b64_tr_b16 v[168:169], v6 offset:51200\n\t" "v_mfma_f32_4x4x4_16b_bf16 v[204:207], v[220:221], a[32:33], 0\n\t"
    "v_mfma_f32_4x4x4_16b_bf16 v[208:211], v[222:223], a[34:35], 0\n\t" "v_mfma_f32_4x4x4_16b_bf16 v[212:215], v[220:221], a[36:37], 0\n\t"
    "v_mfma_f32_4x4x4_16b_bf16 v[216:219], v[222:223], a[38:39], 0\n\t" "v_mfma_f32_16x16x32_bf16 v[60:63], v[176:179], v[200:203], v[60:63]\n\t" "ds_read_b64_tr_b16 v[170:171], v6 offset:51712\n\t"
    "v_mfma_f32_16x16x32_bf16 v[64:67], v[180:183], v[196:199], v[64:67]\n\t" "ds_read_b64_tr_b16 v[172:173], v3 offset:53248\n\t" "v_mfma_f32_16x16x32_bf16 v[68:71], v[180:183], v[200:203], v[68:71]\n\t"
    "ds_read_b64_tr_b16 v[174:175], v3 offset:53760\n\t" "v_cvt_pk_bf16_f32 v188, v204, v205\n\t" "v_mfma_f32_16x16x32_bf16 v[72:75], v[184:187], v[196:199], v[72:75]\n\t"
    "ds_read_b64_tr_b16 v[176:177], v4 offset:53248\n\t" "v_cvt_pk_bf16_f32 v189, v206, v207\n\t" "v_mfma_f32_16x16x32_bf16 v[76:79], v[184:187], v[200:203], v[76:79]\n\t"
    "ds_read_b64_tr_b16 v[178:179], v4 offset:53760\n\t" "s_waitcnt lgkmcnt(4)\n\t" "v_mfma_f32_16x16x32_bf16 v[80:83], v[156:159], v[196:199], v[80:83]\n\t"
    "ds_read_b64_tr_b16 v[180:181], v5 offset:53248\n\t" "v_cvt_pk_bf16_f32 v190, v208, v209\n\t" "v_mfma_f32_16x16x32_bf16 v[84:87], v[156:159], v[200:203], v[84:87]\n\t"
    "ds_read_b64_tr_b16 v[182:183], v5 offset:53760\n\t" "v_cvt_pk_bf16_f32 v191, v210, v211\n\t" "v_mfma_f32_16x16x32_bf16 v[144:147], v[156:159], v[232:235], v[144:147]\n\t"
    "ds_read_b128 v[224:227], v2 offset:1280\n\t" "global_load_lds_dwordx4 v0, %[sb] offset:0\n\t" "v_mfma_f32_16x16x32_bf16 v[88:91], v[160:163], v[196:199], v[88:91]\n\t"
    "ds_read_b64_tr_b16 v[184:185], v6 offset:53248\n\t" "v_cvt_pk_bf16_f32 v192, v212, v213\n\t" "v_mfma_f32_16x16x32_bf16 v[92:95], v[160:163], v[200:203], v[92:95]\n\t"
    "ds_read_b64_tr_b16 v[186:187], v6 offset:53760\n\t" "v_cvt_pk_bf16_f32 v193, v214, v215\n\t" "v_mfma_f32_16x16x32_bf16 v[96:99], v[164:167], v[196:199], v[96:99]\n\t" "s_waitcnt vmcnt(12)\n\t"
    "s_barrier\n\t" "ds_read_b64_tr_b16 v[156:157], v3 offset:61440\n\t" "v_mfma_f32_16x16x32_bf16 v[100:103], v[164:167], v[200:203], v[100:103]\n\t" "ds_read_b64_tr_b16 v[158:159], v3 offset:61952\n\t"
    "v_cvt_pk_bf16_f32 v194, v216, v217\n\t" "v_mfma_f32_16x16x32_bf16 v[104:107], v[168:171], v[196:199], v[104:107]\n\t" "ds_read_b64_tr_b16 v[160:161], v4 offset:61440\n\t"
    "v_cvt_pk_bf16_f32 v195, v218, v219\n\t" "v_mfma_f32_16x16x32_bf16 v[108:111], v[168:171], v[200:203], v[108:111]\n\t" "ds_read_b64_tr_b16 v[162:163], v4 offset:61952\n\t"
    "v_mfma_f32_4x4x4_16b_bf16 v[204:207], v[44:45], v[20:21], 0\n\t" "v_mfma_f32_4x4x4_16b_bf16 v[208:211], v[46:47], v[22:23], 0\n\t" "v_mfma_f32_4x4x4_16b_bf16 v[212:215], v[242:243], v[24:25], 0\n\t"
    "v_mfma_f32_4x4x4_16b_bf16 v[216:219], v[244:245], v[26:27], 0\n\t" "s_waitcnt lgkmcnt(4)\n\t" "v_mfma_f32_16x16x32_bf16 v[112:115], v[196:199], v[172:175], v[112:115]\n\t"
    "ds_read_b64_tr_b16 v[164:165], v5 offset:61440\n\t" "v_mfma_f32_16x16x32_bf16 v[116:119], v[200:203], v[172:175], v[116:119]\n\t" "ds_read_b64_tr_b16 v[166:167], v5 offset:61952\n\t"
    "v_mfma_f32_16x16x32_bf16 v[148:151], v[232:235], v[172:175], v[148:151]\n\t" "ds_read_b128 v[228:231], v1 offset:4096\n\t" "global_load_lds_dwordx4 v0, %[sb] offset:2048\n\t"
    "v_mfma_f32_16x16x32_bf16 v[120:123], v[196:199], v[176:179], v[120:123]\n\t" "ds_read_b64_tr_b16 v[168:169], v6 offset:61440\n\t" "v_add_u32 v0, 0x30000, v0\n\t"
    "v_mfma_f32_16x16x32_bf16 v[124:127], v[200:203], v[176:179], v[124:127]\n\t" "ds_read_b64_tr_b16 v[170:171], v6 offset:61952\n\t" "v_cvt_pk_bf16_f32 v20, v204, v205\n\t"
    "v_mfma_f32_16x16x32_bf16 v[128:131], v[196:199], v[180:183], v[128:131]\n\t" "ds_read_b64_tr_b16 v[172:173], v3 offset:63488\n\t" "v_cvt_pk_bf16_f32 v21, v206, v207\n\t"
    "v_mfma_f32_16x16x32_bf16 v[132:135], v[200:203], v[180:183], v[132:135]\n\t" "ds_read_b64_tr_b16 v[174:175], v3 offset:64000\n\t" "v_cvt_pk_bf16_f32 v22, v208, v209\n\t"
    "v_mfma_f32_16x16x32_bf16 v[136:139], v[196:199], v[184:187], v[136:139]\n\t" "ds_read_b64_tr_b16 v[176:177], v4 offset:63488\n\t" "v_cvt_pk_bf16_f32 v23, v210, v211\n\t"
    "v_mfma_f32_16x16x32_bf16 v[140:143], v[200:203], v[184:187], v[140:143]\n\t" "ds_read_b64_tr_b16 v[178:179], v4 offset:64000\n\t" "s_waitcnt lgkmcnt(4)\n\t"
    "v_mfma_f32_16x16x32_bf16 v[48:51], v[156:159], v[188:191], v[48:51]\n\t" "ds_read_b64_tr_b16 v[180:181], v5 offset:63488\n\t" "s_add_u32 m0, %[sl], 108544\n\t"
    "v_mfma_f32_16x16x32_bf16 v[52:55], v[156:159], v[192:195], v[52:55]\n\t" "ds_read_b64_tr_b16 v[182:183], v5 offset:64000\n\t" "v_cvt_pk_bf16_f32 v24, v212, v213\n\t"
    "v_mfma_f32_16x16x32_bf16 v[152:155], v[228:231], v[156:159], v[152:155]\n\t" "v_cvt_pk_bf16_f32 v25, v214, v215\n\t" "v_cvt_pk_bf16_f32 v26, v216, v217\n\t" "v_cvt_pk_bf16_f32 v27, v218, v219\n\t"
    "s_mov_b64 exec, s[60:61]\n\t" "ds_write_b128 v19, v[20:23] offset:8192\n\t" "ds_write_b128 v19, v[24:27] offset:9216\n\t" "s_mov_b64 exec, -1\n\t"
    "v_mfma_f32_16x16x32_bf16 v[56:59], v[160:163], v[188:191], v[56:59]\n\t" "ds_read_b64_tr_b16 v[184:185], v6 offset:63488\n\t" "global_load_lds_dwordx4 v0, %[sb] offset:-2048\n\t"
    "v_mfma_f32_4x4x4_16b_bf16 v[204:207], v[224:225], a[40:41], 0\n\t" "v_mfma_f32_4x4x4_16b_bf16 v[208:211], v[226:227], a[42:43], 0\n\t"
    "v_mfma_f32_4x4x4_16b_bf16 v[212:215], v[224:225], a[44:45], 0\n\t" "v_mfma_f32_4x4x4_16b_bf16 v[216:219], v[226:227], a[46:47], 0\n\t"
    "v_mfma_f32_16x16x32_bf16 v[60:63], v[160:163], v[192:195], v[60:63]\n\t" "ds_read_b64_tr_b16 v[186:187], v6 offset:64000\n\t" "v_mfma_f32_16x16x32_bf16 v[64:67], v[164:167], v[188:191], v[64:67]\n\t"
    "ds_read_b64_tr_b16 v[156:157], v7 offset:0\n\t" "v_mfma_f32_16x16x32_bf16 v[68:71], v[164:167], v[192:195], v[68:71]\n\t" "ds_read_b64_tr_b16 v[158:159], v7 offset:512\n\t"
    "v_cvt_pk_bf16_f32 v196, v204, v205\n\t" "v_mfma_f32_16x16x32_bf16 v[72:75], v[168:171], v[188:191], v[72:75]\n\t" "ds_read_b64_tr_b16 v[160:161], v8 offset:0\n\t"
    "v_cvt_pk_bf16_f32 v197, v206, v207\n\t" "v_mfma_f32_16x16x32_bf16 v[76:79], v[168:171], v[192:195], v[76:79]\n\t" "ds_read_b64_tr_b16 v[162:163], v8 offset:512\n\t" "s_waitcnt lgkmcnt(4)\n\t"
    "v_mfma_f32_16x16x32_bf16 v[80:83], v[172:175], v[188:191], v[80:83]\n\t" "ds_read_b64_tr_b16 v[164:165], v9 offset:0\n\t" "v_cvt_pk_bf16_f32 v198, v208, v209\n\t"
    "v_mfma_f32_16x16x32_bf16 v[84:87], v[172:175], v[192:195], v[84:87]\n\t" "ds_read_b64_tr_b16 v[166:167], v9 offset:512\n\t" "v_cvt_pk_bf16_f32 v199, v210, v211\n\t"
    "v_mfma_f32_16x16x32_bf16 v[144:147], v[172:175], v[228:231], v[144:147]\n\t" "ds_read_b128 v[220:223], v2 offset:1536\n\t" "global_load_lds_dwordx4 v0, %[sb] offset:0\n\t"
    "v_mfma_f32_16x16x32_bf16 v[88:91], v[176:179], v[188:191], v[88:91]\n\t" "ds_read_b64_tr_b16 v[168:169], v10 offset:0\n\t" "v_cvt_pk_bf16_f32 v200, v212, v213\n\t"
    "v_mfma_f32_16x16x32_bf16 v[92:95], v[176:179], v[192:195], v[92:95]\n\t" "ds_read_b64_tr_b16 v[170:171], v10 offset:512\n\t" "v_cvt_pk_bf16_f32 v201, v214, v215\n\t"
    "v_mfma_f32_16x16x32_bf16 v[96:99], v[180:183], v[188:191], v[96:99]\n\t" "ds_read_b64_tr_b16 v[172:173], v3 offset:0\n\t" "v_mfma_f32_16x16x32_bf16 v[100:103], v[180:183], v[192:195], v[100:103]\n\t"
    "ds_read_b64_tr_b16 v[174:175], v3 offset:512\n\t" "v_cvt_pk_bf16_f32 v202, v216, v217\n\t" "v_mfma_f32_16x16x32_bf16 v[104:107], v[184:187], v[188:191], v[104:107]\n\t"
    "ds_read_b64_tr_b16 v[176:177], v4 offset:0\n\t" "v_cvt_pk_bf16_f32 v203, v218, v219\n\t" "v_mfma_f32_16x16x32_bf16 v[108:111], v[184:187], v[192:195], v[108:111]\n\t"
    "ds_read_b64_tr_b16 v[178:179], v4 offset:512\n\t" "s_waitcnt lgkmcnt(4)\n\t" "v_mfma_f32_16x16x32_bf16 v[112:115], v[188:191], v[156:159], v[112:115]\n\t"
    "ds_read_b64_tr_b16 v[180:181], v5 offset:0\n\t" "v_mfma_f32_16x16x32_bf16 v[116:119], v[192:195], v[156:159], v[116:119]\n\t" "ds_read_b64_tr_b16 v[182:183], v5 offset:512\n\t"
    "v_mfma_f32_16x16x32_bf16 v[148:151], v[228:231], v[156:159], v[148:151]\n\t" "ds_read_b128 v[232:235], v1 offset:5120\n\t" "global_load_lds_dwordx4 v0, %[sb] offset:2048\n\t"
    "v_mfma_f32_16x16x32_bf16 v[120:123], v[188:191], v[160:163], v[120:123]\n\t" "ds_read_b64_tr_b16 v[184:185], v6 offset:0\n\t" "v_add_u32 v0, 0x30000, v0\n\t"
    "v_mfma_f32_16x16x32_bf16 v[124:127], v[192:195], v[160:163], v[124:127]\n\t" "ds_read_b64_tr_b16 v[186:187], v6 offset:512\n\t"
    "v_mfma_f32_16x16x32_bf16 v[128:131], v[188:191], v[164:167], v[128:131]\n\t" "ds_read_b64_tr_b16 v[156:157], v3 offset:2048\n\t"
    "v_mfma_f32_16x16x32_bf16 v[132:135], v[192:195], v[164:167], v[132:135]\n\t" "ds_read_b64_tr_b16 v[158:159], v3 offset:2560\n\t" "s_mov_b64 exec, 0xfffffff\n\t"
    "global_load_dword v17, v[14:15], off\n\t" "s_mov_b64 exec, -1\n\t" "v_mfma_f32_16x16x32_bf16 v[136:139], v[188:191], v[168:171], v[136:139]\n\t" "ds_read_b64_tr_b16 v[160:161], v4 offset:2048\n\t"
    "v_mfma_f32_16x16x32_bf16 v[140:143], v[192:195], v[168:171], v[140:143]\n\t" "ds_read_b64_tr_b16 v[162:163], v4 offset:2560\n\t" "s_waitcnt lgkmcnt(4)\n\t"
    "v_mfma_f32_16x16x32_bf16 v[48:51], v[172:175], v[196:199], v[48:51]\n\t" "ds_read_b64_tr_b16 v[164:165], v5 offset:2048\n\t" "s_add_u32 m0, %[sl], 120832\n\t"
    "v_mfma_f32_16x16x32_bf16 v[52:55], v[172:175], v[200:203], v[52:55]\n\t" "ds_read_b64_tr_b16 v[166:167], v5 offset:2560\n\t" "ds_read_b128 v[44:47], v18 offset:4096\n\t"
    "v_mfma_f32_16x16x32_bf16 v[152:155], v[232:235], v[172:175], v[152:155]\n\t" "global_load_lds_dwordx4 v0, %[sb] offset:-2048\n\t" "ds_read_b128 v[242:245], v18 offset:4352\n\t"
    "v_mfma_f32_16x16x32_bf16 v[56:59], v[176:179], v[196:199], v[56:59]\n\t" "ds_read_b64_tr_b16 v[168:169], v6 offset:2048\n\t" "v_mov_b32_e64 v20, v239\n\t"
    "v_mfma_f32_4x4x4_16b_bf16 v[204:207], v[220:221], a[48:49], 0\n\t" "v_mfma_f32_4x4x4_16b_bf16 v[208:211], v[222:223], a[50:51], 0\n\t"
    "v_mfma_f32_4x4x4_16b_bf16 v[212:215], v[220:221], a[52:53], 0\n\t" "v_mfma_f32_4x4x4_16b_bf16 v[216:219], v[222:223], a[54:55], 0\n\t" "v_mov_b32_e64 v21, v240\n\t"
    "v_mfma_f32_16x16x32_bf16 v[60:63], v[176:179], v[200:203], v[60:63]\n\t" "ds_read_b64_tr_b16 v[170:171], v6 offset:2560\n\t" "v_mfma_f32_16x16x32_bf16 v[64:67], v[180:183], v[196:199], v[64:67]\n\t"
    "ds_read_b64_tr_b16 v[172:173], v3 offset:4096\n\t" "v_mfma_f32_16x16x32_bf16 v[68:71], v[180:183], v[200:203], v[68:71]\n\t" "ds_read_b64_tr_b16 v[174:175], v3 offset:4608\n\t"
    "v_cvt_pk_bf16_f32 v188, v204, v205\n\t" "v_mfma_f32_16x16x32_bf16 v[72:75], v[184:187], v[196:199], v[72:75]\n\t" "ds_read_b64_tr_b16 v[176:177], v4 offset:4096\n\t"
    "v_cvt_pk_bf16_f32 v189, v206, v207\n\t" "v_mfma_f32_16x16x32_bf16 v[76:79], v[184:187], v[200:203], v[76:79]\n\t" "ds_read_b64_tr_b16 v[178:179], v4 offset:4608\n\t" "s_waitcnt lgkmcnt(4)\n\t"
    "v_mfma_f32_16x16x32_bf16 v[80:83], v[156:159], v[196:199], v[80:83]\n\t" "ds_read_b64_tr_b16 v[180:181], v5 offset:4096\n\t" "v_cvt_pk_bf16_f32 v190, v208, v209\n\t"
    "v_mfma_f32_16x16x32_bf16 v[84:87], v[156:159], v[200:203], v[84:87]\n\t" "ds_read_b64_tr_b16 v[182:183], v5 offset:4608\n\t" "v_cvt_pk_bf16_f32 v191, v210, v211\n\t"
    "v_mfma_f32_16x16x32_bf16 v[144:147], v[156:159], v[232:235], v[144:147]\n\t" "ds_read_b128 v[224:227], v2 offset:1792\n\t" "global_load_lds_dwordx4 v0, %[sb] offset:0\n\t"
    "v_mfma_f32_16x16x32_bf16 v[88:91], v[160:163], v[196:199], v[88:91]\n\t" "ds_read_b64_tr_b16 v[184:185], v6 offset:4096\n\t" "v_cvt_pk_bf16_f32 v192, v212, v213\n\t"
    "v_mfma_f32_16x16x32_bf16 v[92:95], v[160:163], v[200:203], v[92:95]\n\t" "ds_read_b64_tr_b16 v[186:187], v6 offset:4608\n\t" "v_cvt_pk_bf16_f32 v193, v214, v215\n\t"
    "v_mfma_f32_16x16x32_bf16 v[96:99], v[164:167], v[196:199], v[96:99]\n\t" "s_waitcnt vmcnt(12)\n\t" "s_barrier\n\t" "ds_read_b64_tr_b16 v[156:157], v3 offset:12288\n\t"
    "v_mfma_f32_16x16x32_bf16 v[100:103], v[164:167], v[200:203], v[100:103]\n\t" "ds_read_b64_tr_b16 v[158:159], v3 offset:12800\n\t" "v_cvt_pk_bf16_f32 v194, v216, v217\n\t"
    "v_mfma_f32_16x16x32_bf16 v[104:107], v[168:171], v[196:199], v[104:107]\n\t" "ds_read_b64_tr_b16 v[160:161], v4 offset:12288\n\t" "v_cvt_pk_bf16_f32 v195, v218, v219\n\t"
    "v_mfma_f32_16x16x32_bf16 v[108:111], v[168:171], v[200:203], v[108:111]\n\t" "ds_read_b64_tr_b16 v[162:163], v4 offset:12800\n\t" "v_mfma_f32_4x4x4_16b_bf16 v[204:207], v[44:45], v[28:29], 0\n\t"
    "v_mfma_f32_4x4x4_16b_bf16 v[208:211], v[46:47], v[30:31], 0\n\t" "v_mfma_f32_4x4x4_16b_bf16 v[212:215], v[242:243], v[32:33], 0\n\t"
    "v_mfma_f32_4x4x4_16b_bf16 v[216:219], v[244:245], v[34:35], 0\n\t" "s_waitcnt lgkmcnt(4)\n\t" "v_mfma_f32_16x16x32_bf16 v[112:115], v[196:199], v[172:175], v[112:115]\n\t"
    "ds_read_b64_tr_b16 v[164:165], v5 offset:12288\n\t" "v_mfma_f32_16x16x32_bf16 v[116:119], v[200:203], v[172:175], v[116:119]\n\t" "ds_read_b64_tr_b16 v[166:167], v5 offset:12800\n\t"
    "v_mfma_f32_16x16x32_bf16 v[148:151], v[232:235], v[172:175], v[148:151]\n\t" "ds_read_b128 v[228:231], v1 offset:6144\n\t" "global_load_lds_dwordx4 v0, %[sb] offset:2048\n\t"
    "v_mfma_f32_16x16x32_bf16 v[120:123], v[196:199], v[176:179], v[120:123]\n\t" "ds_read_b64_tr_b16 v[168:169], v6 offset:12288\n\t" "s_cmp_eq_u32 %[rot], 5\n\t" "s_cselect_b32 s42, s43, 0x30000\n\t"
    "v_add_u32 v0, s42, v0\n\t" "v_mfma_f32_16x16x32_bf16 v[124:127], v[200:203], v[176:179], v[124:127]\n\t" "ds_read_b64_tr_b16 v[170:171], v6 offset:12800\n\t" "v_cvt_pk_bf16_f32 v28, v204, v205\n\t"
    "v_mfma_f32_16x16x32_bf16 v[128:131], v[196:199], v[180:183], v[128:131]\n\t" "ds_read_b64_tr_b16 v[172:173], v3 offset:14336\n\t" "v_cvt_pk_bf16_f32 v29, v206, v207\n\t"
    "v_mfma_f32_16x16x32_bf16 v[132:135], v[200:203], v[180:183], v[132:135]\n\t" "ds_read_b64_tr_b16 v[174:175], v3 offset:14848\n\t" "v_cvt_pk_bf16_f32 v30, v208, v209\n\t"
    "v_mfma_f32_16x16x32_bf16 v[136:139], v[196:199], v[184:187], v[136:139]\n\t" "ds_read_b64_tr_b16 v[176:177], v4 offset:14336\n\t" "v_cvt_pk_bf16_f32 v31, v210, v211\n\t"
    "v_mfma_f32_16x16x32_bf16 v[140:143], v[200:203], v[184:187], v[140:143]\n\t" "ds_read_b64_tr_b16 v[178:179], v4 offset:14848\n\t" "s_waitcnt lgkmcnt(4)\n\t"
    "v_mfma_f32_16x16x32_bf16 v[48:51], v[156:159], v[188:191], v[48:51]\n\t" "ds_read_b64_tr_b16 v[180:181], v5 offset:14336\n\t" "s_add_u32 m0, %[sl], 71680\n\t"
    "v_mfma_f32_16x16x32_bf16 v[52:55], v[156:159], v[192:195], v[52:55]\n\t" "ds_read_b64_tr_b16 v[182:183], v5 offset:14848\n\t" "v_cvt_pk_bf16_f32 v32, v212, v213\n\t"
    "v_mfma_f32_16x16x32_bf16 v[152:155], v[228:231], v[156:159], v[152:155]\n\t" "v_cvt_pk_bf16_f32 v33, v214, v215\n\t" "v_cvt_pk_bf16_f32 v34, v216, v217\n\t" "v_cvt_pk_bf16_f32 v35, v218, v219\n\t"
    "s_mov_b64 exec, s[60:61]\n\t" "ds_write_b128 v19, v[28:31] offset:16384\n\t" "ds_write_b128 v19, v[32:35] offset:17408\n\t" "s_mov_b64 exec, -1\n\t"
    "v_mfma_f32_16x16x32_bf16 v[56:59], v[160:163], v[188:191], v[56:59]\n\t" "ds_read_b64_tr_b16 v[184:185], v6 offset:14336\n\t" "global_load_lds_dwordx4 v0, %[sb] offset:-2048\n\t"
    "v_mfma_f32_4x4x4_16b_bf16 v[204:207], v[224:225], a[56:57], 0\n\t" "v_mfma_f32_4x4x4_16b_bf16 v[208:211], v[226:227], a[58:59], 0\n\t"
    "v_mfma_f32_4x4x4_16b_bf16 v[212:215], v[224:225], a[60:61], 0\n\t" "v_mfma_f32_4x4x4_16b_bf16 v[216:219], v[226:227], a[62:63], 0\n\t" "v_permlane16_swap_b32 v239, v20\n\t"
    "v_mfma_f32_16x16x32_bf16 v[60:63], v[160:163], v[192:195], v[60:63]\n\t" "ds_read_b64_tr_b16 v[186:187], v6 offset:14848\n\t" "v_permlane16_swap_b32 v240, v21\n\t"
    "v_mfma_f32_16x16x32_bf16 v[64:67], v[164:167], v[188:191], v[64:67]\n\t" "ds_read_b64_tr_b16 v[156:157], v3 offset:16384\n\t" "v_add_f32 v239, v239, v20\n\t"
    "v_mfma_f32_16x16x32_bf16 v[68:71], v[164:167], v[192:195], v[68:71]\n\t" "ds_read_b64_tr_b16 v[158:159], v3 offset:16896\n\t" "v_cvt_pk_bf16_f32 v196, v204, v205\n\t"
    "v_mfma_f32_16x16x32_bf16 v[72:75], v[168:171], v[188:191], v[72:75]\n\t" "ds_read_b64_tr_b16 v[160:161], v4 offset:16384\n\t" "v_cvt_pk_bf16_f32 v197, v206, v207\n\t"
    "v_mfma_f32_16x16x32_bf16 v[76:79], v[168:171], v[192:195], v[76:79]\n\t" "ds_read_b64_tr_b16 v[162:163], v4 offset:16896\n\t" "s_waitcnt lgkmcnt(4)\n\t"
    "v_mfma_f32_16x16x32_bf16 v[80:83], v[172:175], v[188:191], v[80:83]\n\t" "ds_read_b64_tr_b16 v[164:165], v5 offset:16384\n\t" "v_cvt_pk_bf16_f32 v198, v208, v209\n\t"
    "v_mfma_f32_16x16x32_bf16 v[84:87], v[172:175], v[192:195], v[84:87]\n\t" "ds_read_b64_tr_b16 v[166:167], v5 offset:16896\n\t" "v_cvt_pk_bf16_f32 v199, v210, v211\n\t"
    "v_mfma_f32_16x16x32_bf16 v[144:147], v[172:175], v[228:231], v[144:147]\n\t" "ds_read_b128 v[220:223], v2 offset:2048\n\t" "global_load_lds_dwordx4 v0, %[sb] offset:0\n\t"
    "v_mfma_f32_16x16x32_bf16 v[88:91], v[176:179], v[188:191], v[88:91]\n\t" "ds_read_b64_tr_b16 v[168:169], v6 offset:16384\n\t" "v_cvt_pk_bf16_f32 v200, v212, v213\n\t"
    "v_mfma_f32_16x16x32_bf16 v[92:95], v[176:179], v[192:195], v[92:95]\n\t" "ds_read_b64_tr_b16 v[170:171], v6 offset:16896\n\t" "v_cvt_pk_bf16_f32 v201, v214, v215\n\t"
    "v_mfma_f32_16x16x32_bf16 v[96:99], v[180:183], v[188:191], v[96:99]\n\t" "ds_read_b64_tr_b16 v[172:173], v3 offset:24576\n\t" "v_add_f32 v240, v240, v21\n\t"
    "v_mfma_f32_16x16x32_bf16 v[100:103], v[180:183], v[192:195], v[100:103]\n\t" "ds_read_b64_tr_b16 v[174:175], v3 offset:25088\n\t" "v_cvt_pk_bf16_f32 v202, v216, v217\n\t"
    "v_mfma_f32_16x16x32_bf16 v[104:107], v[184:187], v[188:191], v[104:107]\n\t" "ds_read_b64_tr_b16 v[176:177], v4 offset:24576\n\t" "v_cvt_pk_bf16_f32 v203, v218, v219\n\t"
    "v_mfma_f32_16x16x32_bf16 v[108:111], v[184:187], v[192:195], v[108:111]\n\t" "ds_read_b64_tr_b16 v[178:179], v4 offset:25088\n\t" "s_waitcnt lgkmcnt(4)\n\t"
    "v_mfma_f32_16x16x32_bf16 v[112:115], v[188:191], v[156:159], v[112:115]\n\t" "ds_read_b64_tr_b16 v[180:181], v5 offset:24576\n\t" "v_mov_b32 v20, v239\n\t"
    "v_mfma_f32_16x16x32_bf16 v[116:119], v[192:195], v[156:159], v[116:119]\n\t" "ds_read_b64_tr_b16 v[182:183], v5 offset:25088\n\t" "v_mov_b32_e64 v21, v240\n\t"
    "v_mfma_f32_16x16x32_bf16 v[148:151], v[228:231], v[156:159], v[148:151]\n\t" "ds_read_b128 v[232:235], v1 offset:7168\n\t" "global_load_lds_dwordx4 v0, %[sb] offset:2048\n\t"
    "v_mfma_f32_16x16x32_bf16 v[120:123], v[188:191], v[160:163], v[120:123]\n\t" "ds_read_b64_tr_b16 v[184:185], v6 offset:24576\n\t" "v_add_u32 v0, 0x30000, v0\n\t"
    "v_mfma_f32_16x16x32_bf16 v[124:127], v[192:195], v[160:163], v[124:127]\n\t" "ds_read_b64_tr_b16 v[186:187], v6 offset:25088\n\t"
    "v_mfma_f32_16x16x32_bf16 v[128:131], v[188:191], v[164:167], v[128:131]\n\t" "ds_read_b64_tr_b16 v[156:157], v3 offset:26624\n\t"
    "v_mfma_f32_16x16x32_bf16 v[132:135], v[192:195], v[164:167], v[132:135]\n\t" "ds_read_b64_tr_b16 v[158:159], v3 offset:27136\n\t" "v_permlane32_swap_b32 v239, v20\n\t"
    "v_mfma_f32_16x16x32_bf16 v[136:139], v[188:191], v[168:171], v[136:139]\n\t" "ds_read_b64_tr_b16 v[160:161], v4 offset:26624\n\t" "v_permlane32_swap_b32 v240, v21\n\t"
    "v_mfma_f32_16x16x32_bf16 v[140:143], v[192:195], v[168:171], v[140:143]\n\t" "ds_read_b64_tr_b16 v[162:163], v4 offset:27136\n\t" "s_waitcnt lgkmcnt(4)\n\t"
    "v_mfma_f32_16x16x32_bf16 v[48:51], v[172:175], v[196:199], v[48:51]\n\t" "ds_read_b64_tr_b16 v[164:165], v5 offset:26624\n\t" "s_add_u32 m0, %[sl], 83968\n\t"
    "v_mfma_f32_16x16x32_bf16 v[52:55], v[172:175], v[200:203], v[52:55]\n\t" "ds_read_b64_tr_b16 v[166:167], v5 offset:27136\n\t" "ds_read_b128 v[44:47], v18 offset:6144\n\t"
    "v_mfma_f32_16x16x32_bf16 v[152:155], v[232:235], v[172:175], v[152:155]\n\t" "global_load_lds_dwordx4 v0, %[sb] offset:-2048\n\t" "ds_read_b128 v[242:245], v18 offset:6400\n\t"
    "v_mfma_f32_16x16x32_bf16 v[56:59], v[176:179], v[196:199], v[56:59]\n\t" "ds_read_b64_tr_b16 v[168:169], v6 offset:26624\n\t" "v_add_f32 v239, v239, v20\n\t"
    "v_mfma_f32_4x4x4_16b_bf16 v[204:207], v[220:221], a[64:65], 0\n\t" "v_mfma_f32_4x4x4_16b_bf16 v[208:211], v[222:223], a[66:67], 0\n\t"
    "v_mfma_f32_4x4x4_16b_bf16 v[212:215], v[220:221], a[68:69], 0\n\t" "v_mfma_f32_4x4x4_16b_bf16 v[216:219], v[222:223], a[70:71], 0\n\t" "v_add_f32_e64 v240, v240, v21\n\t"
    "v_mfma_f32_16x16x32_bf16 v[60:63], v[176:179], v[200:203], v[60:63]\n\t" "ds_read_b64_tr_b16 v[170:171], v6 offset:27136\n\t" "v_mul_f32 v236, 0x3a800000, v236\n\t"
    "v_mfma_f32_16x16x32_bf16 v[64:67], v[180:183], v[196:199], v[64:67]\n\t" "ds_read_b64_tr_b16 v[172:173], v3 offset:28672\n\t" "v_mul_f32 v237, 0x3a800000, v237\n\t"
    "v_mfma_f32_16x16x32_bf16 v[68:71], v[180:183], v[200:203], v[68:71]\n\t" "ds_read_b64_tr_b16 v[174:175], v3 offset:29184\n\t" "v_cvt_pk_bf16_f32 v188, v204, v205\n\t"
    "v_mfma_f32_16x16x32_bf16 v[72:75], v[184:187], v[196:199], v[72:75]\n\t" "ds_read_b64_tr_b16 v[176:177], v4 offset:28672\n\t" "v_cvt_pk_bf16_f32 v189, v206, v207\n\t"
    "v_mfma_f32_16x16x32_bf16 v[76:79], v[184:187], v[200:203], v[76:79]\n\t" "ds_read_b64_tr_b16 v[178:179], v4 offset:29184\n\t" "s_waitcnt lgkmcnt(4)\n\t"
    "v_mfma_f32_16x16x32_bf16 v[80:83], v[156:159], v[196:199], v[80:83]\n\t" "ds_read_b64_tr_b16 v[180:181], v5 offset:28672\n\t" "v_cvt_pk_bf16_f32 v190, v208, v209\n\t"
    "v_mfma_f32_16x16x32_bf16 v[84:87], v[156:159], v[200:203], v[84:87]\n\t" "ds_read_b64_tr_b16 v[182:183], v5 offset:29184\n\t" "v_cvt_pk_bf16_f32 v191, v210, v211\n\t"
    "v_mfma_f32_16x16x32_bf16 v[144:147], v[156:159], v[232:235], v[144:147]\n\t" "ds_read_b128 v[224:227], v2 offset:2304\n\t" "global_load_lds_dwordx4 v0, %[sb] offset:0\n\t"
    "v_mfma_f32_16x16x32_bf16 v[88:91], v[160:163], v[196:199], v[88:91]\n\t" "ds_read_b64_tr_b16 v[184:185], v6 offset:28672\n\t" "v_cvt_pk_bf16_f32 v192, v212, v213\n\t"
    "v_mfma_f32_16x16x32_bf16 v[92:95], v[160:163], v[200:203], v[92:95]\n\t" "ds_read_b64_tr_b16 v[186:187], v6 offset:29184\n\t" "v_cvt_pk_bf16_f32 v193, v214, v215\n\t"
    "v_mfma_f32_16x16x32_bf16 v[96:99], v[164:167], v[196:199], v[96:99]\n\t" "s_waitcnt vmcnt(12)\n\t" "s_barrier\n\t" "ds_read_b64_tr_b16 v[156:157], v3 offset:36864\n\t"
    "v_mfma_f32_16x16x32_bf16 v[100:103], v[164:167], v[200:203], v[100:103]\n\t" "ds_read_b64_tr_b16 v[158:159], v3 offset:37376\n\t" "v_cvt_pk_bf16_f32 v194, v216, v217\n\t"
    "v_mfma_f32_16x16x32_bf16 v[104:107], v[168:171], v[196:199], v[104:107]\n\t" "ds_read_b64_tr_b16 v[160:161], v4 offset:36864\n\t" "v_cvt_pk_bf16_f32 v195, v218, v219\n\t"
    "v_mfma_f32_16x16x32_bf16 v[108:111], v[168:171], v[200:203], v[108:111]\n\t" "ds_read_b64_tr_b16 v[162:163], v4 offset:37376\n\t" "v_mfma_f32_4x4x4_16b_bf16 v[204:207], v[44:45], v[36:37], 0\n\t"
    "v_mfma_f32_4x4x4_16b_bf16 v[208:211], v[46:47], v[38:39], 0\n\t" "v_mfma_f32_4x4x4_16b_bf16 v[212:215], v[242:243], v[40:41], 0\n\t"
    "v_mfma_f32_4x4x4_16b_bf16 v[216:219], v[244:245], v[42:43], 0\n\t" "s_waitcnt lgkmcnt(4)\n\t" "v_mfma_f32_16x16x32_bf16 v[112:115], v[196:199], v[172:175], v[112:115]\n\t"
    "ds_read_b64_tr_b16 v[164:165], v5 offset:36864\n\t" "v_mfma_f32_16x16x32_bf16 v[116:119], v[200:203], v[172:175], v[116:119]\n\t" "ds_read_b64_tr_b16 v[166:167], v5 offset:37376\n\t"
    "v_mfma_f32_16x16x32_bf16 v[148:151], v[232:235], v[172:175], v[148:151]\n\t" "ds_read_b128 v[228:231], v1 offset:8192\n\t" "global_load_lds_dwordx4 v0, %[sb] offset:2048\n\t"
    "v_mfma_f32_16x16x32_bf16 v[120:123], v[196:199], v[176:179], v[120:123]\n\t" "ds_read_b64_tr_b16 v[168:169], v6 offset:36864\n\t" "v_add_u32 v0, 0x30000, v0\n\t"
    "v_mfma_f32_16x16x32_bf16 v[124:127], v[200:203], v[176:179], v[124:127]\n\t" "ds_read_b64_tr_b16 v[170:171], v6 offset:37376\n\t" "v_add_u32 v16, 0x70000, v16\n\t"
    "v_mfma_f32_16x16x32_bf16 v[128:131], v[196:199], v[180:183], v[128:131]\n\t" "ds_read_b64_tr_b16 v[172:173], v3 offset:38912\n\t" "v_cvt_pk_bf16_f32 v36, v204, v205\n\t"
    "v_mfma_f32_16x16x32_bf16 v[132:135], v[200:203], v[180:183], v[132:135]\n\t" "ds_read_b64_tr_b16 v[174:175], v3 offset:39424\n\t" "s_mov_b64 exec, %[tm]\n\t" "global_load_dword v17, v16, %[pf]\n\t"
    "s_mov_b64 exec, -1\n\t" "v_mfma_f32_16x16x32_bf16 v[136:139], v[196:199], v[184:187], v[136:139]\n\t" "ds_read_b64_tr_b16 v[176:177], v4 offset:38912\n\t" "v_cvt_pk_bf16_f32 v37, v206, v207\n\t"
    "v_mfma_f32_16x16x32_bf16 v[140:143], v[200:203], v[184:187], v[140:143]\n\t" "ds_read_b64_tr_b16 v[178:179], v4 offset:39424\n\t" "s_waitcnt lgkmcnt(4)\n\t"
    "v_mfma_f32_16x16x32_bf16 v[48:51], v[156:159], v[188:191], v[48:51]\n\t" "ds_read_b64_tr_b16 v[180:181], v5 offset:38912\n\t" "s_add_u32 m0, %[sl], 22528\n\t"
    "v_mfma_f32_16x16x32_bf16 v[52:55], v[156:159], v[192:195], v[52:55]\n\t" "ds_read_b64_tr_b16 v[182:183], v5 offset:39424\n\t" "v_cvt_pk_bf16_f32 v38, v208, v209\n\t"
    "v_mfma_f32_16x16x32_bf16 v[152:155], v[228:231], v[156:159], v[152:155]\n\t" "v_cvt_pk_bf16_f32 v39, v210, v211\n\t" "v_cvt_pk_bf16_f32 v40, v212, v213\n\t" "v_cvt_pk_bf16_f32 v41, v214, v215\n\t"
    "v_cvt_pk_bf16_f32 v42, v216, v217\n\t" "v_cvt_pk_bf16_f32 v43, v218, v219\n\t" "s_mov_b64 exec, s[60:61]\n\t" "ds_write_b128 v19, v[36:39] offset:24576\n\t"
    "ds_write_b128 v19, v[40:43] offset:25600\n\t" "s_mov_b64 exec, -1\n\t" "v_mfma_f32_16x16x32_bf16 v[56:59], v[160:163], v[188:191], v[56:59]\n\t" "ds_read_b64_tr_b16 v[184:185], v6 offset:38912\n\t"
    "global_load_lds_dwordx4 v0, %[sb] offset:-2048\n\t" "v_mfma_f32_4x4x4_16b_bf16 v[204:207], v[224:225], a[72:73], 0\n\t" "v_mfma_f32_4x4x4_16b_bf16 v[208:211], v[226:227], a[74:75], 0\n\t"
    "v_mfma_f32_4x4x4_16b_bf16 v[212:215], v[224:225], a[76:77], 0\n\t" "v_mfma_f32_4x4x4_16b_bf16 v[216:219], v[226:227], a[78:79], 0\n\t" "v_mul_f32 v238, 0x3a800000, v238\n\t"
    "v_mfma_f32_16x16x32_bf16 v[60:63], v[160:163], v[192:195], v[60:63]\n\t" "ds_read_b64_tr_b16 v[186:187], v6 offset:39424\n\t" "v_mul_f32 v239, 0x3a800000, v239\n\t"
    "v_mfma_f32_16x16x32_bf16 v[64:67], v[164:167], v[188:191], v[64:67]\n\t" "ds_read_b64_tr_b16 v[156:157], v3 offset:40960\n\t" "global_load_lds_dwordx4 v0, %[sb] offset:0\n\t"
    "v_mfma_f32_16x16x32_bf16 v[68:71], v[164:167], v[192:195], v[68:71]\n\t" "ds_read_b64_tr_b16 v[158:159], v3 offset:41472\n\t" "v_cvt_pk_bf16_f32 v196, v204, v205\n\t"
    "v_mfma_f32_16x16x32_bf16 v[72:75], v[168:171], v[188:191], v[72:75]\n\t" "ds_read_b64_tr_b16 v[160:161], v4 offset:40960\n\t" "v_cvt_pk_bf16_f32 v197, v206, v207\n\t"
    "v_mfma_f32_16x16x32_bf16 v[76:79], v[168:171], v[192:195], v[76:79]\n\t" "ds_read_b64_tr_b16 v[162:163], v4 offset:41472\n\t" "s_waitcnt lgkmcnt(4)\n\t"
    "v_mfma_f32_16x16x32_bf16 v[80:83], v[172:175], v[188:191], v[80:83]\n\t" "ds_read_b64_tr_b16 v[164:165], v5 offset:40960\n\t" "v_cvt_pk_bf16_f32 v198, v208, v209\n\t"
    "v_mfma_f32_16x16x32_bf16 v[84:87], v[172:175], v[192:195], v[84:87]\n\t" "ds_read_b64_tr_b16 v[166:167], v5 offset:41472\n\t" "v_cvt_pk_bf16_f32 v199, v210, v211\n\t"
    "v_mfma_f32_16x16x32_bf16 v[144:147], v[172:175], v[228:231], v[144:147]\n\t" "ds_read_b128 v[220:223], v2 offset:2560\n\t" "global_load_lds_dwordx4 v0, %[sb] offset:2048\n\t"
    "v_mfma_f32_16x16x32_bf16 v[88:91], v[176:179], v[188:191], v[88:91]\n\t" "ds_read_b64_tr_b16 v[168:169], v6 offset:40960\n\t" "v_cvt_pk_bf16_f32 v200, v212, v213\n\t"
    "v_mfma_f32_16x16x32_bf16 v[92:95], v[176:179], v[192:195], v[92:95]\n\t" "ds_read_b64_tr_b16 v[170:171], v6 offset:41472\n\t" "v_cvt_pk_bf16_f32 v201, v214, v215\n\t"
    "v_mfma_f32_16x16x32_bf16 v[96:99], v[180:183], v[188:191], v[96:99]\n\t" "ds_read_b64_tr_b16 v[172:173], v7 offset:8192\n\t" "v_add_u32 v0, 0x30000, v0\n\t"
    "v_mfma_f32_16x16x32_bf16 v[100:103], v[180:183], v[192:195], v[100:103]\n\t" "ds_read_b64_tr_b16 v[174:175], v7 offset:8704\n\t" "v_cvt_pk_bf16_f32 v202, v216, v217\n\t"
    "v_mfma_f32_16x16x32_bf16 v[104:107], v[184:187], v[188:191], v[104:107]\n\t" "ds_read_b64_tr_b16 v[176:177], v8 offset:8192\n\t" "v_cvt_pk_bf16_f32 v203, v218, v219\n\t"
    "v_mfma_f32_16x16x32_bf16 v[108:111], v[184:187], v[192:195], v[108:111]\n\t" "ds_read_b64_tr_b16 v[178:179], v8 offset:8704\n\t" "s_waitcnt lgkmcnt(4)\n\t"
    "v_mfma_f32_16x16x32_bf16 v[112:115], v[188:191], v[156:159], v[112:115]\n\t" "ds_read_b64_tr_b16 v[180:181], v9 offset:8192\n\t" "v_mul_f32 v240, 0x3a800000, v240\n\t"
    "v_mfma_f32_16x16x32_bf16 v[116:119], v[192:195], v[156:159], v[116:119]\n\t" "ds_read_b64_tr_b16 v[182:183], v9 offset:8704\n\t" "v_mul_f32 v241, 0x3a800000, v241\n\t"
    "v_mfma_f32_16x16x32_bf16 v[148:151], v[228:231], v[156:159], v[148:151]\n\t" "ds_read_b128 v[232:235], v1 offset:9216\n\t" "global_load_dwordx4 a[32:35], v11, %[sm] nt\n\t"
    "v_mfma_f32_16x16x32_bf16 v[120:123], v[188:191], v[160:163], v[120:123]\n\t" "ds_read_b64_tr_b16 v[184:185], v10 offset:8192\n\t" "v_add_u32 v11, 0x3000, v11\n\t"
    "v_mfma_f32_16x16x32_bf16 v[124:127], v[192:195], v[160:163], v[124:127]\n\t" "ds_read_b64_tr_b16 v[186:187], v10 offset:8704\n\t" "v_fma_f32 v239, -v236, v236, v239\n\t"
    "v_mfma_f32_16x16x32_bf16 v[128:131], v[188:191], v[164:167], v[128:131]\n\t" "ds_read_b64_tr_b16 v[156:157], v7 offset:10240\n\t" "v_fma_f32 v240, -v237, v237, v240\n\t"
    "v_mfma_f32_16x16x32_bf16 v[132:135], v[192:195], v[164:167], v[132:135]\n\t" "ds_read_b64_tr_b16 v[158:159], v7 offset:10752\n\t" "global_load_dwordx4 a[36:39], v11, %[sm] nt\n\t"
    "v_mfma_f32_16x16x32_bf16 v[136:139], v[188:191], v[168:171], v[136:139]\n\t" "ds_read_b64_tr_b16 v[160:161], v8 offset:10240\n\t" "v_fma_f32 v241, -v238, v238, v241\n\t"
    "v_mfma_f32_16x16x32_bf16 v[140:143], v[192:195], v[168:171], v[140:143]\n\t" "ds_read_b64_tr_b16 v[162:163], v8 offset:10752\n\t" "s_waitcnt lgkmcnt(4)\n\t"
    "v_mfma_f32_16x16x32_bf16 v[48:51], v[172:175], v[196:199], v[48:51]\n\t" "ds_read_b64_tr_b16 v[164:165], v9 offset:10240\n\t" "s_add_u32 m0, %[sl], 34816\n\t"
    "v_mfma_f32_16x16x32_bf16 v[52:55], v[172:175], v[200:203], v[52:55]\n\t" "ds_read_b64_tr_b16 v[166:167], v9 offset:10752\n\t" "v_max_f32_e64 v239, 0, v239\n\t"
    "v_mfma_f32_16x16x32_bf16 v[152:155], v[232:235], v[172:175], v[152:155]\n\t" "global_load_lds_dwordx4 v0, %[sb] offset:-2048\n\t" "v_max_f32_e64 v240, 0, v240\n\t"
    "v_mfma_f32_16x16x32_bf16 v[56:59], v[176:179], v[196:199], v[56:59]\n\t" "ds_read_b64_tr_b16 v[168:169], v10 offset:10240\n\t" "v_max_f32_e64 v241, 0, v241\n\t"
    "v_mfma_f32_4x4x4_16b_bf16 v[204:207], v[220:221], a[80:81], 0\n\t" "v_mfma_f32_4x4x4_16b_bf16 v[208:211], v[222:223], a[82:83], 0\n\t"
    "v_mfma_f32_4x4x4_16b_bf16 v[212:215], v[220:221], a[84:85], 0\n\t" "v_mfma_f32_4x4x4_16b_bf16 v[216:219], v[222:223], a[86:87], 0\n\t" "v_add_f32 v239, 0x3727c5ac, v239\n\t"
    "v_mfma_f32_16x16x32_bf16 v[60:63], v[176:179], v[200:203], v[60:63]\n\t" "ds_read_b64_tr_b16 v[170:171], v10 offset:10752\n\t" "v_add_f32 v240, 0x3727c5ac, v240\n\t"
    "v_mfma_f32_16x16x32_bf16 v[64:67], v[180:183], v[196:199], v[64:67]\n\t" "ds_read_b64_tr_b16 v[172:173], v7 offset:12288\n\t" "global_load_lds_dwordx4 v0, %[sb] offset:0\n\t"
    "v_mfma_f32_16x16x32_bf16 v[68:71], v[180:183], v[200:203], v[68:71]\n\t" "ds_read_b64_tr_b16 v[174:175], v7 offset:12800\n\t" "v_cvt_pk_bf16_f32 v188, v204, v205\n\t"
    "v_mfma_f32_16x16x32_bf16 v[72:75], v[184:187], v[196:199], v[72:75]\n\t" "ds_read_b64_tr_b16 v[176:177], v8 offset:12288\n\t" "v_cvt_pk_bf16_f32 v189, v206, v207\n\t"
    "v_mfma_f32_16x16x32_bf16 v[76:79], v[184:187], v[200:203], v[76:79]\n\t" "ds_read_b64_tr_b16 v[178:179], v8 offset:12800\n\t" "s_waitcnt lgkmcnt(4)\n\t"
    "v_mfma_f32_16x16x32_bf16 v[80:83], v[156:159], v[196:199], v[80:83]\n\t" "ds_read_b64_tr_b16 v[180:181], v9 offset:12288\n\t" "v_cvt_pk_bf16_f32 v190, v208, v209\n\t"
    "v_mfma_f32_16x16x32_bf16 v[84:87], v[156:159], v[200:203], v[84:87]\n\t" "ds_read_b64_tr_b16 v[182:183], v9 offset:12800\n\t" "v_cvt_pk_bf16_f32 v191, v210, v211\n\t"
    "v_mfma_f32_16x16x32_bf16 v[144:147], v[156:159], v[232:235], v[144:147]\n\t" "ds_read_b128 v[224:227], v2 offset:2816\n\t" "global_load_lds_dwordx4 v0, %[sb] offset:2048\n\t"
    "v_mfma_f32_16x16x32_bf16 v[88:91], v[160:163], v[196:199], v[88:91]\n\t" "ds_read_b64_tr_b16 v[184:185], v10 offset:12288\n\t" "v_cvt_pk_bf16_f32 v192, v212, v213\n\t"
    "v_mfma_f32_16x16x32_bf16 v[92:95], v[160:163], v[200:203], v[92:95]\n\t" "ds_read_b64_tr_b16 v[186:187], v10 offset:12800\n\t" "v_cvt_pk_bf16_f32 v193, v214, v215\n\t"
    "v_mfma_f32_16x16x32_bf16 v[96:99], v[164:167], v[196:199], v[96:99]\n\t" "s_waitcnt vmcnt(15)\n\t" "s_barrier\n\t" "ds_read_b64_tr_b16 v[156:157], v7 offset:20480\n\t"
    "v_mfma_f32_16x16x32_bf16 v[100:103], v[164:167], v[200:203], v[100:103]\n\t" "ds_read_b64_tr_b16 v[158:159], v7 offset:20992\n\t" "v_cvt_pk_bf16_f32 v194, v216, v217\n\t"
    "v_mfma_f32_16x16x32_bf16 v[104:107], v[168:171], v[196:199], v[104:107]\n\t" "ds_read_b64_tr_b16 v[160:161], v8 offset:20480\n\t" "v_cvt_pk_bf16_f32 v195, v218, v219\n\t"
    "v_mfma_f32_16x16x32_bf16 v[108:111], v[168:171], v[200:203], v[108:111]\n\t" "ds_read_b64_tr_b16 v[162:163], v8 offset:20992\n\t" "s_waitcnt lgkmcnt(4)\n\t"
    "v_mfma_f32_16x16x32_bf16 v[112:115], v[196:199], v[172:175], v[112:115]\n\t" "ds_read_b64_tr_b16 v[164:165], v9 offset:20480\n\t" "s_cmp_eq_u32 %[rot], 4\n\t" "s_cselect_b32 s42, s43, 0x30000\n\t"
    "v_add_u32 v0, s42, v0\n\t" "v_mfma_f32_16x16x32_bf16 v[116:119], v[200:203], v[172:175], v[116:119]\n\t" "ds_read_b64_tr_b16 v[166:167], v9 offset:20992\n\t" "v_add_u32 v11, 0x3000, v11\n\t"
    "v_mfma_f32_16x16x32_bf16 v[148:151], v[232:235], v[172:175], v[148:151]\n\t" "ds_read_b128 v[228:231], v1 offset:10240\n\t" "global_load_dwordx4 a[40:43], v11, %[sm] nt\n\t"
    "v_mfma_f32_16x16x32_bf16 v[120:123], v[196:199], v[176:179], v[120:123]\n\t" "ds_read_b64_tr_b16 v[168:169], v10 offset:20480\n\t" "v_add_u32 v11, 0x3000, v11\n\t"
    "v_mfma_f32_16x16x32_bf16 v[124:127], v[200:203], v[176:179], v[124:127]\n\t" "ds_read_b64_tr_b16 v[170:171], v10 offset:20992\n\t" "v_add_f32 v241, 0x3727c5ac, v241\n\t"
    "v_mfma_f32_16x16x32_bf16 v[128:131], v[196:199], v[180:183], v[128:131]\n\t" "ds_read_b64_tr_b16 v[172:173], v7 offset:22528\n\t" "v_rsq_f32_e64 v20, v239\n\t"
    "v_mfma_f32_16x16x32_bf16 v[132:135], v[200:203], v[180:183], v[132:135]\n\t" "ds_read_b64_tr_b16 v[174:175], v7 offset:23040\n\t" "global_load_dwordx4 a[44:47], v11, %[sm] nt\n\t"
    "v_mfma_f32_16x16x32_bf16 v[136:139], v[196:199], v[184:187], v[136:139]\n\t" "ds_read_b64_tr_b16 v[176:177], v8 offset:22528\n\t" "v_rsq_f32_e64 v21, v240\n\t"
    "v_mfma_f32_16x16x32_bf16 v[140:143], v[200:203], v[184:187], v[140:143]\n\t" "ds_read_b64_tr_b16 v[178:179], v8 offset:23040\n\t" "s_waitcnt lgkmcnt(4)\n\t"
    "v_mfma_f32_16x16x32_bf16 v[48:51], v[156:159], v[188:191], v[48:51]\n\t" "ds_read_b64_tr_b16 v[180:181], v9 offset:22528\n\t" "s_add_u32 m0, %[sl], 47104\n\t"
    "v_mfma_f32_16x16x32_bf16 v[52:55], v[156:159], v[192:195], v[52:55]\n\t" "ds_read_b64_tr_b16 v[182:183], v9 offset:23040\n\t" "v_rsq_f32 v22, v241\n\t"
    "v_mfma_f32_16x16x32_bf16 v[152:155], v[228:231], v[156:159], v[152:155]\n\t" "global_load_lds_dwordx4 v0, %[sb] offset:-2048\n\t" "v_and_b32_e64 v26, 15, v13\n\t"
    "v_mfma_f32_16x16x32_bf16 v[56:59], v[160:163], v[188:191], v[56:59]\n\t" "ds_read_b64_tr_b16 v[184:185], v10 offset:22528\n\t" "v_lshl_add_u32 v26, v26, 2, %[l0]\n\t"
    "v_mfma_f32_4x4x4_16b_bf16 v[204:207], v[224:225], a[88:89], 0\n\t" "v_mfma_f32_4x4x4_16b_bf16 v[208:211], v[226:227], a[90:91], 0\n\t"
    "v_mfma_f32_4x4x4_16b_bf16 v[212:215], v[224:225], a[92:93], 0\n\t" "v_mfma_f32_4x4x4_16b_bf16 v[216:219], v[226:227], a[94:95], 0\n\t" "v_add_u32_e64 v27, %[w128], v26\n\t"
    "v_mfma_f32_16x16x32_bf16 v[60:63], v[160:163], v[192:195], v[60:63]\n\t" "ds_read_b64_tr_b16 v[186:187], v10 offset:23040\n\t" "v_mul_f32_e64 v239, v239, v20\n\t"
    "v_mfma_f32_16x16x32_bf16 v[64:67], v[164:167], v[188:191], v[64:67]\n\t" "ds_read_b64_tr_b16 v[156:157], v7 offset:24576\n\t" "global_load_lds_dwordx4 v0, %[sb] offset:0\n\t"
    "v_mfma_f32_16x16x32_bf16 v[68:71], v[164:167], v[192:195], v[68:71]\n\t" "ds_read_b64_tr_b16 v[158:159], v7 offset:25088\n\t" "v_cvt_pk_bf16_f32 v196, v204, v205\n\t"
    "v_mfma_f32_16x16x32_bf16 v[72:75], v[168:171], v[188:191], v[72:75]\n\t" "ds_read_b64_tr_b16 v[160:161], v8 offset:24576\n\t" "v_cvt_pk_bf16_f32 v197, v206, v207\n\t"
    "v_mfma_f32_16x16x32_bf16 v[76:79], v[168:171], v[192:195], v[76:79]\n\t" "ds_read_b64_tr_b16 v[162:163], v8 offset:25088\n\t" "s_waitcnt lgkmcnt(4)\n\t"
    "v_mfma_f32_16x16x32_bf16 v[80:83], v[172:175], v[188:191], v[80:83]\n\t" "ds_read_b64_tr_b16 v[164:165], v9 offset:24576\n\t" "v_cvt_pk_bf16_f32 v198, v208, v209\n\t"
    "v_mfma_f32_16x16x32_bf16 v[84:87], v[172:175], v[192:195], v[84:87]\n\t" "ds_read_b64_tr_b16 v[166:167], v9 offset:25088\n\t" "v_cvt_pk_bf16_f32 v199, v210, v211\n\t"
    "v_mfma_f32_16x16x32_bf16 v[144:147], v[172:175], v[228:231], v[144:147]\n\t" "ds_read_b128 v[220:223], v2 offset:3072\n\t" "global_load_lds_dwordx4 v0, %[sb] offset:2048\n\t"
    "v_mfma_f32_16x16x32_bf16 v[88:91], v[176:179], v[188:191], v[88:91]\n\t" "ds_read_b64_tr_b16 v[168:169], v10 offset:24576\n\t" "v_cvt_pk_bf16_f32 v200, v212, v213\n\t"
    "v_mfma_f32_16x16x32_bf16 v[92:95], v[176:179], v[192:195], v[92:95]\n\t" "ds_read_b64_tr_b16 v[170:171], v10 offset:25088\n\t" "v_cvt_pk_bf16_f32 v201, v214, v215\n\t"
    "v_mfma_f32_16x16x32_bf16 v[96:99], v[180:183], v[188:191], v[96:99]\n\t" "ds_read_b64_tr_b16 v[172:173], v7 offset:32768\n\t" "v_add_u32 v0, 0x30000, v0\n\t"
    "v_mfma_f32_16x16x32_bf16 v[100:103], v[180:183], v[192:195], v[100:103]\n\t" "ds_read_b64_tr_b16 v[174:175], v7 offset:33280\n\t" "v_cvt_pk_bf16_f32 v202, v216, v217\n\t"
    "v_mfma_f32_16x16x32_bf16 v[104:107], v[184:187], v[188:191], v[104:107]\n\t" "ds_read_b64_tr_b16 v[176:177], v8 offset:32768\n\t" "v_cvt_pk_bf16_f32 v203, v218, v219\n\t"
    "v_mfma_f32_16x16x32_bf16 v[108:111], v[184:187], v[192:195], v[108:111]\n\t" "ds_read_b64_tr_b16 v[178:179], v8 offset:33280\n\t" "s_waitcnt lgkmcnt(4)\n\t"
    "v_mfma_f32_16x16x32_bf16 v[112:115], v[188:191], v[156:159], v[112:115]\n\t" "ds_read_b64_tr_b16 v[180:181], v9 offset:32768\n\t" "v_add_u32 v11, 0x3000, v11\n\t"
    "v_mfma_f32_16x16x32_bf16 v[116:119], v[192:195], v[156:159], v[116:119]\n\t" "ds_read_b64_tr_b16 v[182:183], v9 offset:33280\n\t" "v_mul_f32_e64 v240, v240, v21\n\t"
    "v_mfma_f32_16x16x32_bf16 v[148:151], v[228:231], v[156:159], v[148:151]\n\t" "ds_read_b128 v[232:235], v1 offset:11264\n\t" "global_load_dwordx4 a[48:51], v11, %[sm] nt\n\t"
    "v_mfma_f32_16x16x32_bf16 v[120:123], v[188:191], v[160:163], v[120:123]\n\t" "ds_read_b64_tr_b16 v[184:185], v10 offset:32768\n\t" "v_add_u32 v11, 0x3000, v11\n\t"
    "v_mfma_f32_16x16x32_bf16 v[124:127], v[192:195], v[160:163], v[124:127]\n\t" "ds_read_b64_tr_b16 v[186:187], v10 offset:33280\n\t" "v_mul_f32_e64 v241, v241, v22\n\t"
    "v_mfma_f32_16x16x32_bf16 v[128:131], v[188:191], v[164:167], v[128:131]\n\t" "ds_read_b64_tr_b16 v[156:157], v7 offset:34816\n\t" "ds_write_b32 v27, v20 offset:17472\n\t"
    "v_mfma_f32_16x16x32_bf16 v[132:135], v[192:195], v[164:167], v[132:135]\n\t" "ds_read_b64_tr_b16 v[158:159], v7 offset:35328\n\t" "global_load_dwordx4 a[52:55], v11, %[sm] nt\n\t"
    "v_mfma_f32_16x16x32_bf16 v[136:139], v[188:191], v[168:171], v[136:139]\n\t" "ds_read_b64_tr_b16 v[160:161], v8 offset:34816\n\t" "ds_write_b32 v27, v21 offset:17536\n\t"
    "v_mfma_f32_16x16x32_bf16 v[140:143], v[192:195], v[168:171], v[140:143]\n\t" "ds_read_b64_tr_b16 v[162:163], v8 offset:35328\n\t" "s_waitcnt lgkmcnt(6)\n\t"
    "v_mfma_f32_16x16x32_bf16 v[48:51], v[172:175], v[196:199], v[48:51]\n\t" "ds_read_b64_tr_b16 v[164:165], v9 offset:34816\n\t" "s_add_u32 m0, %[sl], 59392\n\t"
    "v_mfma_f32_16x16x32_bf16 v[52:55], v[172:175], v[200:203], v[52:55]\n\t" "ds_read_b64_tr_b16 v[166:167], v9 offset:35328\n\t" "ds_write_b32 v26, v22 offset:17408\n\t"
    "v_mfma_f32_16x16x32_bf16 v[152:155], v[232:235], v[172:175], v[152:155]\n\t" "global_load_lds_dwordx4 v0, %[sb] offset:-2048\n\t" "s_mov_b64 s[46:47], 0xffff\n\t"
    "v_mfma_f32_16x16x32_bf16 v[56:59], v[176:179], v[196:199], v[56:59]\n\t" "ds_read_b64_tr_b16 v[168:169], v10 offset:34816\n\t" "s_mov_b32 s48, -1\n\t"
    "v_mfma_f32_4x4x4_16b_bf16 v[204:207], v[220:221], a[96:97], 0\n\t" "v_mfma_f32_4x4x4_16b_bf16 v[208:211], v[222:223], a[98:99], 0\n\t"
    "v_mfma_f32_4x4x4_16b_bf16 v[212:215], v[220:221], a[100:101], 0\n\t" "v_mfma_f32_4x4x4_16b_bf16 v[216:219], v[222:223], a[102:103], 0\n\t" "s_mov_b32 s49, 0\n\t"
    "v_mfma_f32_16x16x32_bf16 v[60:63], v[176:179], v[200:203], v[60:63]\n\t" "ds_read_b64_tr_b16 v[170:171], v10 offset:35328\n\t" "v_and_b32 v23, 0xffff0000, v236\n\t"
    "v_mfma_f32_16x16x32_bf16 v[64:67], v[180:183], v[196:199], v[64:67]\n\t" "ds_read_b64_tr_b16 v[172:173], v7 offset:36864\n\t" "global_load_lds_dwordx4 v0, %[sb] offset:0\n\t"
    "v_mfma_f32_16x16x32_bf16 v[68:71], v[180:183], v[200:203], v[68:71]\n\t" "ds_read_b64_tr_b16 v[174:175], v7 offset:37376\n\t" "v_cvt_pk_bf16_f32 v188, v204, v205\n\t"
    "v_mfma_f32_16x16x32_bf16 v[72:75], v[184:187], v[196:199], v[72:75]\n\t" "ds_read_b64_tr_b16 v[176:177], v8 offset:36864\n\t" "v_cvt_pk_bf16_f32 v189, v206, v207\n\t"
    "v_mfma_f32_16x16x32_bf16 v[76:79], v[184:187], v[200:203], v[76:79]\n\t" "ds_read_b64_tr_b16 v[178:179], v8 offset:37376\n\t" "s_waitcnt lgkmcnt(4)\n\t"
    "v_mfma_f32_16x16x32_bf16 v[80:83], v[156:159], v[196:199], v[80:83]\n\t" "ds_read_b64_tr_b16 v[180:181], v9 offset:36864\n\t" "v_cvt_pk_bf16_f32 v190, v208, v209\n\t"
    "v_mfma_f32_16x16x32_bf16 v[84:87], v[156:159], v[200:203], v[84:87]\n\t" "ds_read_b64_tr_b16 v[182:183], v9 offset:37376\n\t" "v_cvt_pk_bf16_f32 v191, v210, v211\n\t"
    "v_mfma_f32_16x16x32_bf16 v[144:147], v[156:159], v[232:235], v[144:147]\n\t" "ds_read_b128 v[224:227], v2 offset:3328\n\t" "global_load_lds_dwordx4 v0, %[sb] offset:2048\n\t"
    "v_mfma_f32_16x16x32_bf16 v[88:91], v[160:163], v[196:199], v[88:91]\n\t" "ds_read_b64_tr_b16 v[184:185], v10 offset:36864\n\t" "v_cvt_pk_bf16_f32 v192, v212, v213\n\t"
    "v_mfma_f32_16x16x32_bf16 v[92:95], v[160:163], v[200:203], v[92:95]\n\t" "ds_read_b64_tr_b16 v[186:187], v10 offset:37376\n\t" "v_cvt_pk_bf16_f32 v193, v214, v215\n\t"
    "v_mfma_f32_16x16x32_bf16 v[96:99], v[164:167], v[196:199], v[96:99]\n\t" "s_waitcnt vmcnt(19)\n\t" "s_barrier\n\t" "ds_read_b64_tr_b16 v[156:157], v3 offset:49152\n\t"
    "v_mfma_f32_16x16x32_bf16 v[100:103], v[164:167], v[200:203], v[100:103]\n\t" "ds_read_b64_tr_b16 v[158:159], v3 offset:49664\n\t" "v_cvt_pk_bf16_f32 v194, v216, v217\n\t"
    "v_mfma_f32_16x16x32_bf16 v[104:107], v[168:171], v[196:199], v[104:107]\n\t" "ds_read_b64_tr_b16 v[160:161], v4 offset:49152\n\t" "v_cvt_pk_bf16_f32 v195, v218, v219\n\t"
    "v_mfma_f32_16x16x32_bf16 v[108:111], v[168:171], v[200:203], v[108:111]\n\t" "ds_read_b64_tr_b16 v[162:163], v4 offset:49664\n\t" "s_waitcnt lgkmcnt(4)\n\t"
    "v_mfma_f32_16x16x32_bf16 v[112:115], v[196:199], v[172:175], v[112:115]\n\t" "ds_read_b64_tr_b16 v[164:165], v5 offset:49152\n\t" "v_add_u32 v0, 0x30000, v0\n\t"
    "v_mfma_f32_16x16x32_bf16 v[116:119], v[200:203], v[172:175], v[116:119]\n\t" "ds_read_b64_tr_b16 v[166:167], v5 offset:49664\n\t" "v_add_u32 v11, 0x3000, v11\n\t"
    "v_mfma_f32_16x16x32_bf16 v[148:151], v[232:235], v[172:175], v[148:151]\n\t" "ds_read_b128 v[228:231], v1 offset:12288\n\t" "global_load_dwordx4 a[56:59], v11, %[sm] nt\n\t"
    "v_mfma_f32_16x16x32_bf16 v[120:123], v[196:199], v[176:179], v[120:123]\n\t" "ds_read_b64_tr_b16 v[168:169], v6 offset:49152\n\t" "v_add_u32 v11, 0x3000, v11\n\t"
    "v_mfma_f32_16x16x32_bf16 v[124:127], v[200:203], v[176:179], v[124:127]\n\t" "ds_read_b64_tr_b16 v[170:171], v6 offset:49664\n\t" "v_sub_f32 v24, v236, v23\n\t"
    "v_mfma_f32_16x16x32_bf16 v[128:131], v[196:199], v[180:183], v[128:131]\n\t" "ds_read_b64_tr_b16 v[172:173], v3 offset:51200\n\t" "v_cndmask_b32 v23, v24, v23, s[46:47]\n\t"
    "v_mfma_f32_16x16x32_bf16 v[132:135], v[200:203], v[180:183], v[132:135]\n\t" "ds_read_b64_tr_b16 v[174:175], v3 offset:51712\n\t" "global_load_dwordx4 a[60:63], v11, %[sm] nt\n\t"
    "v_mfma_f32_16x16x32_bf16 v[136:139], v[196:199], v[184:187], v[136:139]\n\t" "ds_read_b64_tr_b16 v[176:177], v4 offset:51200\n\t" "v_cvt_pk_bf16_f32 v23, -v23, -v23\n\t"
    "v_mfma_f32_16x16x32_bf16 v[140:143], v[200:203], v[184:187], v[140:143]\n\t" "ds_read_b64_tr_b16 v[178:179], v4 offset:51712\n\t" "s_waitcnt lgkmcnt(4)\n\t"
    "v_mfma_f32_16x16x32_bf16 v[48:51], v[156:159], v[188:191], v[48:51]\n\t" "ds_read_b64_tr_b16 v[180:181], v5 offset:51200\n\t" "s_add_u32 m0, %[sl], 96256\n\t"
    "v_mfma_f32_16x16x32_bf16 v[52:55], v[156:159], v[192:195], v[52:55]\n\t" "ds_read_b64_tr_b16 v[182:183], v5 offset:51712\n\t" "v_cndmask_b32 v246, 0, v23, s[48:49]\n\t"
    "v_mfma_f32_16x16x32_bf16 v[152:155], v[228:231], v[156:159], v[152:155]\n\t" "global_load_lds_dwordx4 v0, %[sb] offset:-2048\n\t" "v_and_b32 v23, 0xffff0000, v239\n\t"
    "v_mfma_f32_16x16x32_bf16 v[56:59], v[160:163], v[188:191], v[56:59]\n\t" "ds_read_b64_tr_b16 v[184:185], v6 offset:51200\n\t" "v_sub_f32 v24, v239, v23\n\t"
    "v_mfma_f32_4x4x4_16b_bf16 v[204:207], v[224:225], a[104:105], 0\n\t" "v_mfma_f32_4x4x4_16b_bf16 v[208:211], v[226:227], a[106:107], 0\n\t"
    "v_mfma_f32_4x4x4_16b_bf16 v[212:215], v[224:225], a[108:109], 0\n\t" "v_mfma_f32_4x4x4_16b_bf16 v[216:219], v[226:227], a[110:111], 0\n\t" "v_cndmask_b32 v23, v24, v23, s[46:47]\n\t"
    "v_mfma_f32_16x16x32_bf16 v[60:63], v[160:163], v[192:195], v[60:63]\n\t" "ds_read_b64_tr_b16 v[186:187], v6 offset:51712\n\t" "v_cvt_pk_bf16_f32 v23, v23, v23\n\t"
    "v_mfma_f32_16x16x32_bf16 v[64:67], v[164:167], v[188:191], v[64:67]\n\t" "ds_read_b64_tr_b16 v[156:157], v3 offset:53248\n\t" "v_cndmask_b32 v247, 0, v23, s[48:49]\n\t"
    "v_mfma_f32_16x16x32_bf16 v[68:71], v[164:167], v[192:195], v[68:71]\n\t" "ds_read_b64_tr_b16 v[158:159], v3 offset:53760\n\t" "v_cvt_pk_bf16_f32 v196, v204, v205\n\t"
    "v_mfma_f32_16x16x32_bf16 v[72:75], v[168:171], v[188:191], v[72:75]\n\t" "ds_read_b64_tr_b16 v[160:161], v4 offset:53248\n\t" "v_cvt_pk_bf16_f32 v197, v206, v207\n\t"
    "v_mfma_f32_16x16x32_bf16 v[76:79], v[168:171], v[192:195], v[76:79]\n\t" "ds_read_b64_tr_b16 v[162:163], v4 offset:53760\n\t" "s_waitcnt lgkmcnt(4)\n\t"
    "v_mfma_f32_16x16x32_bf16 v[80:83], v[172:175], v[188:191], v[80:83]\n\t" "ds_read_b64_tr_b16 v[164:165], v5 offset:53248\n\t" "v_cvt_pk_bf16_f32 v198, v208, v209\n\t"
    "v_mfma_f32_16x16x32_bf16 v[84:87], v[172:175], v[192:195], v[84:87]\n\t" "ds_read_b64_tr_b16 v[166:167], v5 offset:53760\n\t" "v_cvt_pk_bf16_f32 v199, v210, v211\n\t"
    "v_mfma_f32_16x16x32_bf16 v[144:147], v[172:175], v[228:231], v[144:147]\n\t" "ds_read_b128 v[220:223], v2 offset:3584\n\t" "global_load_lds_dwordx4 v0, %[sb] offset:0\n\t"
    "v_mfma_f32_16x16x32_bf16 v[88:91], v[176:179], v[188:191], v[88:91]\n\t" "ds_read_b64_tr_b16 v[168:169], v6 offset:53248\n\t" "v_cvt_pk_bf16_f32 v200, v212, v213\n\t"
    "v_mfma_f32_16x16x32_bf16 v[92:95], v[176:179], v[192:195], v[92:95]\n\t" "ds_read_b64_tr_b16 v[170:171], v6 offset:53760\n\t" "v_cvt_pk_bf16_f32 v201, v214, v215\n\t"
    "v_mfma_f32_16x16x32_bf16 v[96:99], v[180:183], v[188:191], v[96:99]\n\t" "ds_read_b64_tr_b16 v[172:173], v3 offset:61440\n\t" "v_and_b32 v23, 0xffff0000, v237\n\t"
    "v_mfma_f32_16x16x32_bf16 v[100:103], v[180:183], v[192:195], v[100:103]\n\t" "ds_read_b64_tr_b16 v[174:175], v3 offset:61952\n\t" "v_cvt_pk_bf16_f32 v202, v216, v217\n\t"
    "v_mfma_f32_16x16x32_bf16 v[104:107], v[184:187], v[188:191], v[104:107]\n\t" "ds_read_b64_tr_b16 v[176:177], v4 offset:61440\n\t" "v_cvt_pk_bf16_f32 v203, v218, v219\n\t"
    "v_mfma_f32_16x16x32_bf16 v[108:111], v[184:187], v[192:195], v[108:111]\n\t" "ds_read_b64_tr_b16 v[178:179], v4 offset:61952\n\t" "s_waitcnt lgkmcnt(4)\n\t"
    "v_mfma_f32_16x16x32_bf16 v[112:115], v[188:191], v[156:159], v[112:115]\n\t" "ds_read_b64_tr_b16 v[180:181], v5 offset:61440\n\t" "v_sub_f32_e64 v24, v237, v23\n\t"
    "v_mfma_f32_16x16x32_bf16 v[116:119], v[192:195], v[156:159], v[116:119]\n\t" "ds_read_b64_tr_b16 v[182:183], v5 offset:61952\n\t" "v_cndmask_b32 v23, v24, v23, s[46:47]\n\t"
    "v_mfma_f32_16x16x32_bf16 v[148:151], v[228:231], v[156:159], v[148:151]\n\t" "ds_read_b128 v[232:235], v1 offset:13312\n\t" "global_load_lds_dwordx4 v0, %[sb] offset:2048\n\t"
    "v_mfma_f32_16x16x32_bf16 v[120:123], v[188:191], v[160:163], v[120:123]\n\t" "ds_read_b64_tr_b16 v[184:185], v6 offset:61440\n\t" "v_add_u32 v0, 0x30000, v0\n\t"
    "v_mfma_f32_16x16x32_bf16 v[124:127], v[192:195], v[160:163], v[124:127]\n\t" "ds_read_b64_tr_b16 v[186:187], v6 offset:61952\n\t" "v_cvt_pk_bf16_f32 v23, -v23, -v23\n\t"
    "v_mfma_f32_16x16x32_bf16 v[128:131], v[188:191], v[164:167], v[128:131]\n\t" "ds_read_b64_tr_b16 v[156:157], v3 offset:63488\n\t" "v_cndmask_b32 v248, 0, v23, s[48:49]\n\t"
    "v_mfma_f32_16x16x32_bf16 v[132:135], v[192:195], v[164:167], v[132:135]\n\t" "ds_read_b64_tr_b16 v[158:159], v3 offset:64000\n\t" "v_and_b32 v23, 0xffff0000, v240\n\t"
    "v_mfma_f32_16x16x32_bf16 v[136:139], v[188:191], v[168:171], v[136:139]\n\t" "ds_read_b64_tr_b16 v[160:161], v4 offset:63488\n\t" "v_sub_f32_e64 v24, v240, v23\n\t"
    "v_mfma_f32_16x16x32_bf16 v[140:143], v[192:195], v[168:171], v[140:143]\n\t" "ds_read_b64_tr_b16 v[162:163], v4 offset:64000\n\t" "s_waitcnt lgkmcnt(4)\n\t"
    "v_mfma_f32_16x16x32_bf16 v[48:51], v[172:175], v[196:199], v[48:51]\n\t" "ds_read_b64_tr_b16 v[164:165], v5 offset:63488\n\t" "s_add_u32 m0, %[sl], 108544\n\t"
    "v_mfma_f32_16x16x32_bf16 v[52:55], v[172:175], v[200:203], v[52:55]\n\t" "ds_read_b64_tr_b16 v[166:167], v5 offset:64000\n\t" "v_cndmask_b32 v23, v24, v23, s[46:47]\n\t"
    "v_mfma_f32_16x16x32_bf16 v[152:155], v[232:235], v[172:175], v[152:155]\n\t" "global_load_lds_dwordx4 v0, %[sb] offset:-2048\n\t" "v_cvt_pk_bf16_f32 v23, v23, v23\n\t"
    "v_mfma_f32_16x16x32_bf16 v[56:59], v[176:179], v[196:199], v[56:59]\n\t" "ds_read_b64_tr_b16 v[168:169], v6 offset:63488\n\t" "v_cndmask_b32 v249, 0, v23, s[48:49]\n\t"
    "v_mfma_f32_4x4x4_16b_bf16 v[204:207], v[220:221], a[112:113], 0\n\t" "v_mfma_f32_4x4x4_16b_bf16 v[208:211], v[222:223], a[114:115], 0\n\t"
    "v_mfma_f32_4x4x4_16b_bf16 v[212:215], v[220:221], a[116:117], 0\n\t" "v_mfma_f32_4x4x4_16b_bf16 v[216:219], v[222:223], a[118:119], 0\n\t" "v_and_b32 v23, 0xffff0000, v238\n\t"
    "v_mfma_f32_16x16x32_bf16 v[60:63], v[176:179], v[200:203], v[60:63]\n\t" "ds_read_b64_tr_b16 v[170:171], v6 offset:64000\n\t" "v_sub_f32 v24, v238, v23\n\t"
    "v_mfma_f32_16x16x32_bf16 v[64:67], v[180:183], v[196:199], v[64:67]\n\t" "ds_read_b64_tr_b16 v[172:173], v7 offset:0\n\t" "v_cndmask_b32 v23, v24, v23, s[46:47]\n\t"
    "v_mfma_f32_16x16x32_bf16 v[68:71], v[180:183], v[200:203], v[68:71]\n\t" "ds_read_b64_tr_b16 v[174:175], v7 offset:512\n\t" "v_cvt_pk_bf16_f32 v188, v204, v205\n\t"
    "v_mfma_f32_16x16x32_bf16 v[72:75], v[184:187], v[196:199], v[72:75]\n\t" "ds_read_b64_tr_b16 v[176:177], v8 offset:0\n\t" "v_cvt_pk_bf16_f32 v189, v206, v207\n\t"
    "v_mfma_f32_16x16x32_bf16 v[76:79], v[184:187], v[200:203], v[76:79]\n\t" "ds_read_b64_tr_b16 v[178:179], v8 offset:512\n\t" "s_waitcnt lgkmcnt(4)\n\t"
    "v_mfma_f32_16x16x32_bf16 v[80:83], v[156:159], v[196:199], v[80:83]\n\t" "ds_read_b64_tr_b16 v[180:181], v9 offset:0\n\t" "v_cvt_pk_bf16_f32 v190, v208, v209\n\t"
    "v_mfma_f32_16x16x32_bf16 v[84:87], v[156:159], v[200:203], v[84:87]\n\t" "ds_read_b64_tr_b16 v[182:183], v9 offset:512\n\t" "v_cvt_pk_bf16_f32 v191, v210, v211\n\t"
    "v_mfma_f32_16x16x32_bf16 v[144:147], v[156:159], v[232:235], v[144:147]\n\t" "ds_read_b128 v[224:227], v2 offset:3840\n\t" "global_load_lds_dwordx4 v0, %[sb] offset:0\n\t"
    "v_mfma_f32_16x16x32_bf16 v[88:91], v[160:163], v[196:199], v[88:91]\n\t" "ds_read_b64_tr_b16 v[184:185], v10 offset:0\n\t" "v_cvt_pk_bf16_f32 v192, v212, v213\n\t"
    "v_mfma_f32_16x16x32_bf16 v[92:95], v[160:163], v[200:203], v[92:95]\n\t" "ds_read_b64_tr_b16 v[186:187], v10 offset:512\n\t" "v_cvt_pk_bf16_f32 v193, v214, v215\n\t"
    "v_mfma_f32_16x16x32_bf16 v[96:99], v[164:167], v[196:199], v[96:99]\n\t" "s_waitcnt vmcnt(17)\n\t" "s_barrier\n\t" "ds_read_b64_tr_b16 v[156:157], v3 offset:0\n\t"
    "v_mfma_f32_16x16x32_bf16 v[100:103], v[164:167], v[200:203], v[100:103]\n\t" "ds_read_b64_tr_b16 v[158:159], v3 offset:512\n\t" "v_cvt_pk_bf16_f32 v194, v216, v217\n\t"
    "v_mfma_f32_16x16x32_bf16 v[104:107], v[168:171], v[196:199], v[104:107]\n\t" "ds_read_b64_tr_b16 v[160:161], v4 offset:0\n\t" "v_cvt_pk_bf16_f32 v195, v218, v219\n\t"
    "v_mfma_f32_16x16x32_bf16 v[108:111], v[168:171], v[200:203], v[108:111]\n\t" "ds_read_b64_tr_b16 v[162:163], v4 offset:512\n\t" "s_waitcnt lgkmcnt(4)\n\t"
    "v_mfma_f32_16x16x32_bf16 v[112:115], v[196:199], v[172:175], v[112:115]\n\t" "ds_read_b64_tr_b16 v[164:165], v5 offset:0\n\t" "v_cvt_pk_bf16_f32 v23, -v23, -v23\n\t"
    "v_mfma_f32_16x16x32_bf16 v[116:119], v[200:203], v[172:175], v[116:119]\n\t" "ds_read_b64_tr_b16 v[166:167], v5 offset:512\n\t" "v_cndmask_b32 v250, 0, v23, s[48:49]\n\t"
    "v_mfma_f32_16x16x32_bf16 v[148:151], v[232:235], v[172:175], v[148:151]\n\t" "ds_read_b128 v[228:231], v1 offset:14336\n\t" "global_load_lds_dwordx4 v0, %[sb] offset:2048\n\t"
    "v_mfma_f32_16x16x32_bf16 v[120:123], v[196:199], v[176:179], v[120:123]\n\t" "ds_read_b64_tr_b16 v[168:169], v6 offset:0\n\t" "s_cmp_eq_u32 %[rot], 3\n\t" "s_cselect_b32 s42, s43, 0x30000\n\t"
    "v_add_u32 v0, s42, v0\n\t" "v_mfma_f32_16x16x32_bf16 v[124:127], v[200:203], v[176:179], v[124:127]\n\t" "ds_read_b64_tr_b16 v[170:171], v6 offset:512\n\t" "v_add_u32 v16, 0x70000, v16\n\t"
    "v_mfma_f32_16x16x32_bf16 v[128:131], v[196:199], v[180:183], v[128:131]\n\t" "ds_read_b64_tr_b16 v[172:173], v3 offset:2048\n\t" "v_and_b32 v23, 0xffff0000, v241\n\t"
    "v_mfma_f32_16x16x32_bf16 v[132:135], v[200:203], v[180:183], v[132:135]\n\t" "ds_read_b64_tr_b16 v[174:175], v3 offset:2560\n\t" "s_mov_b64 exec, %[tm]\n\t" "global_load_dword a64, v16, %[pf]\n\t"
    "s_mov_b64 exec, -1\n\t" "v_mfma_f32_16x16x32_bf16 v[136:139], v[196:199], v[184:187], v[136:139]\n\t" "ds_read_b64_tr_b16 v[176:177], v4 offset:2048\n\t" "v_sub_f32_e64 v24, v241, v23\n\t"
    "v_mfma_f32_16x16x32_bf16 v[140:143], v[200:203], v[184:187], v[140:143]\n\t" "ds_read_b64_tr_b16 v[178:179], v4 offset:2560\n\t" "s_waitcnt lgkmcnt(4)\n\t"
    "v_mfma_f32_16x16x32_bf16 v[48:51], v[156:159], v[188:191], v[48:51]\n\t" "ds_read_b64_tr_b16 v[180:181], v5 offset:2048\n\t" "s_add_u32 m0, %[sl], 120832\n\t"
    "v_mfma_f32_16x16x32_bf16 v[52:55], v[156:159], v[192:195], v[52:55]\n\t" "ds_read_b64_tr_b16 v[182:183], v5 offset:2560\n\t" "v_cndmask_b32 v23, v24, v23, s[46:47]\n\t"
    "v_mfma_f32_16x16x32_bf16 v[152:155], v[228:231], v[156:159], v[152:155]\n\t" "global_load_lds_dwordx4 v0, %[sb] offset:-2048\n\t" "v_cvt_pk_bf16_f32 v23, v23, v23\n\t"
    "v_mfma_f32_16x16x32_bf16 v[56:59], v[160:163], v[188:191], v[56:59]\n\t" "ds_read_b64_tr_b16 v[184:185], v6 offset:2048\n\t" "v_cndmask_b32 v251, 0, v23, s[48:49]\n\t"
    "v_mfma_f32_4x4x4_16b_bf16 v[204:207], v[224:225], a[120:121], 0\n\t" "v_mfma_f32_4x4x4_16b_bf16 v[208:211], v[226:227], a[122:123], 0\n\t"
    "v_mfma_f32_4x4x4_16b_bf16 v[212:215], v[224:225], a[124:125], 0\n\t" "v_mfma_f32_4x4x4_16b_bf16 v[216:219], v[226:227], a[126:127], 0\n\t" "v_mul_f32 v240, 0x3e8293ee, v20\n\t"
    "v_mfma_f32_16x16x32_bf16 v[60:63], v[160:163], v[192:195], v[60:63]\n\t" "ds_read_b64_tr_b16 v[186:187], v6 offset:2560\n\t" "v_mul_f32 v241, 0x3e8293ee, v21\n\t"
    "v_mfma_f32_16x16x32_bf16 v[64:67], v[164:167], v[188:191], v[64:67]\n\t" "ds_read_b64_tr_b16 v[156:157], v3 offset:4096\n\t" "v_mfma_f32_16x16x32_bf16 v[68:71], v[164:167], v[192:195], v[68:71]\n\t"
    "ds_read_b64_tr_b16 v[158:159], v3 offset:4608\n\t" "v_cvt_pk_bf16_f32 v196, v204, v205\n\t" "v_mfma_f32_16x16x32_bf16 v[72:75], v[168:171], v[188:191], v[72:75]\n\t"
    "ds_read_b64_tr_b16 v[160:161], v4 offset:4096\n\t" "v_cvt_pk_bf16_f32 v197, v206, v207\n\t" "v_mfma_f32_16x16x32_bf16 v[76:79], v[168:171], v[192:195], v[76:79]\n\t"
    "ds_read_b64_tr_b16 v[162:163], v4 offset:4608\n\t" "s_waitcnt lgkmcnt(4)\n\t" "v_mfma_f32_16x16x32_bf16 v[80:83], v[172:175], v[188:191], v[80:83]\n\t"
    "ds_read_b64_tr_b16 v[164:165], v5 offset:4096\n\t" "v_cvt_pk_bf16_f32 v198, v208, v209\n\t" "v_mfma_f32_16x16x32_bf16 v[84:87], v[172:175], v[192:195], v[84:87]\n\t"
    "ds_read_b64_tr_b16 v[166:167], v5 offset:4608\n\t" "v_cvt_pk_bf16_f32 v199, v210, v211\n\t" "v_mfma_f32_16x16x32_bf16 v[144:147], v[172:175], v[228:231], v[144:147]\n\t"
    "ds_read_b128 v[220:223], v2 offset:4096\n\t" "global_load_lds_dwordx4 v0, %[sb] offset:0\n\t" "v_mfma_f32_16x16x32_bf16 v[88:91], v[176:179], v[188:191], v[88:91]\n\t"
    "ds_read_b64_tr_b16 v[168:169], v6 offset:4096\n\t" "v_cvt_pk_bf16_f32 v200, v212, v213\n\t" "v_mfma_f32_16x16x32_bf16 v[92:95], v[176:179], v[192:195], v[92:95]\n\t"
    "ds_read_b64_tr_b16 v[170:171], v6 offset:4608\n\t" "v_cvt_pk_bf16_f32 v201, v214, v215\n\t" "v_mfma_f32_16x16x32_bf16 v[96:99], v[180:183], v[188:191], v[96:99]\n\t"
    "ds_read_b64_tr_b16 v[172:173], v3 offset:12288\n\t" "v_mfma_f32_16x16x32_bf16 v[100:103], v[180:183], v[192:195], v[100:103]\n\t" "ds_read_b64_tr_b16 v[174:175], v3 offset:12800\n\t"
    "v_cvt_pk_bf16_f32 v202, v216, v217\n\t" "v_mfma_f32_16x16x32_bf16 v[104:107], v[184:187], v[188:191], v[104:107]\n\t" "ds_read_b64_tr_b16 v[176:177], v4 offset:12288\n\t"
    "v_cvt_pk_bf16_f32 v203, v218, v219\n\t" "v_mfma_f32_16x16x32_bf16 v[108:111], v[184:187], v[192:195], v[108:111]\n\t" "ds_read_b64_tr_b16 v[178:179], v4 offset:12800\n\t" "s_waitcnt lgkmcnt(4)\n\t"
    "v_mfma_f32_16x16x32_bf16 v[112:115], v[188:191], v[156:159], v[112:115]\n\t" "ds_read_b64_tr_b16 v[180:181], v5 offset:12288\n\t"
    "v_mfma_f32_16x16x32_bf16 v[116:119], v[192:195], v[156:159], v[116:119]\n\t" "ds_read_b64_tr_b16 v[182:183], v5 offset:12800\n\t"
    "v_mfma_f32_16x16x32_bf16 v[148:151], v[228:231], v[156:159], v[148:151]\n\t" "ds_read_b128 v[232:235], v1 offset:15360\n\t" "global_load_lds_dwordx4 v0, %[sb] offset:2048\n\t"
    "v_mfma_f32_16x16x32_bf16 v[120:123], v[188:191], v[160:163], v[120:123]\n\t" "ds_read_b64_tr_b16 v[184:185], v6 offset:12288\n\t" "v_add_u32 v0, 0x30000, v0\n\t"
    "v_mfma_f32_16x16x32_bf16 v[124:127], v[192:195], v[160:163], v[124:127]\n\t" "ds_read_b64_tr_b16 v[186:187], v6 offset:12800\n\t"
    "v_mfma_f32_16x16x32_bf16 v[128:131], v[188:191], v[164:167], v[128:131]\n\t" "ds_read_b64_tr_b16 v[156:157], v3 offset:14336\n\t"
    "v_mfma_f32_16x16x32_bf16 v[132:135], v[192:195], v[164:167], v[132:135]\n\t" "ds_read_b64_tr_b16 v[158:159], v3 offset:14848\n\t"
    "v_mfma_f32_16x16x32_bf16 v[136:139], v[188:191], v[168:171], v[136:139]\n\t" "ds_read_b64_tr_b16 v[160:161], v4 offset:14336\n\t"
    "v_mfma_f32_16x16x32_bf16 v[140:143], v[192:195], v[168:171], v[140:143]\n\t" "ds_read_b64_tr_b16 v[162:163], v4 offset:14848\n\t" "s_waitcnt lgkmcnt(4)\n\t"
    "v_mfma_f32_16x16x32_bf16 v[48:51], v[172:175], v[196:199], v[48:51]\n\t" "ds_read_b64_tr_b16 v[164:165], v5 offset:14336\n\t" "s_add_u32 m0, %[sl], 71680\n\t"
    "v_mfma_f32_16x16x32_bf16 v[52:55], v[172:175], v[200:203], v[52:55]\n\t" "ds_read_b64_tr_b16 v[166:167], v5 offset:14848\n\t"
    "v_mfma_f32_16x16x32_bf16 v[152:155], v[232:235], v[172:175], v[152:155]\n\t" "global_load_lds_dwordx4 v0, %[sb] offset:-2048\n\t"
    "v_mfma_f32_16x16x32_bf16 v[56:59], v[176:179], v[196:199], v[56:59]\n\t" "ds_read_b64_tr_b16 v[168:169], v6 offset:14336\n\t" "v_mfma_f32_4x4x4_16b_bf16 v[204:207], v[220:221], a[128:129], 0\n\t"
    "v_mfma_f32_4x4x4_16b_bf16 v[208:211], v[222:223], a[130:131], 0\n\t" "v_mfma_f32_4x4x4_16b_bf16 v[212:215], v[220:221], a[132:133], 0\n\t"
    "v_mfma_f32_4x4x4_16b_bf16 v[216:219], v[222:223], a[134:135], 0\n\t" "v_mfma_f32_16x16x32_bf16 v[60:63], v[176:179], v[200:203], v[60:63]\n\t" "ds_read_b64_tr_b16 v[170:171], v6 offset:14848\n\t"
    "v_mfma_f32_16x16x32_bf16 v[64:67], v[180:183], v[196:199], v[64:67]\n\t" "ds_read_b64_tr_b16 v[172:173], v3 offset:16384\n\t" "v_mfma_f32_16x16x32_bf16 v[68:71], v[180:183], v[200:203], v[68:71]\n\t"
    "ds_read_b64_tr_b16 v[174:175], v3 offset:16896\n\t" "v_cvt_pk_bf16_f32 v188, v204, v205\n\t" "v_mfma_f32_16x16x32_bf16 v[72:75], v[184:187], v[196:199], v[72:75]\n\t"
    "ds_read_b64_tr_b16 v[176:177], v4 offset:16384\n\t" "v_cvt_pk_bf16_f32 v189, v206, v207\n\t" "v_mfma_f32_16x16x32_bf16 v[76:79], v[184:187], v[200:203], v[76:79]\n\t"
    "ds_read_b64_tr_b16 v[178:179], v4 offset:16896\n\t" "s_waitcnt lgkmcnt(4)\n\t" "v_mfma_f32_16x16x32_bf16 v[80:83], v[156:159], v[196:199], v[80:83]\n\t"
    "ds_read_b64_tr_b16 v[180:181], v5 offset:16384\n\t" "v_cvt_pk_bf16_f32 v190, v208, v209\n\t" "v_mfma_f32_16x16x32_bf16 v[84:87], v[156:159], v[200:203], v[84:87]\n\t"
    "ds_read_b64_tr_b16 v[182:183], v5 offset:16896\n\t" "v_cvt_pk_bf16_f32 v191, v210, v211\n\t" "v_mfma_f32_16x16x32_bf16 v[144:147], v[156:159], v[232:235], v[144:147]\n\t"
    "ds_read_b128 v[224:227], v2 offset:4352\n\t" "global_load_lds_dwordx4 v0, %[sb] offset:0\n\t" "v_mfma_f32_16x16x32_bf16 v[88:91], v[160:163], v[196:199], v[88:91]\n\t"
    "ds_read_b64_tr_b16 v[184:185], v6 offset:16384\n\t" "v_cvt_pk_bf16_f32 v192, v212, v213\n\t" "v_mfma_f32_16x16x32_bf16 v[92:95], v[160:163], v[200:203], v[92:95]\n\t"
    "ds_read_b64_tr_b16 v[186:187], v6 offset:16896\n\t" "v_cvt_pk_bf16_f32 v193, v214, v215\n\t" "v_mfma_f32_16x16x32_bf16 v[96:99], v[164:167], v[196:199], v[96:99]\n\t" "s_waitcnt vmcnt(14)\n\t"
    "s_barrier\n\t" "ds_read_b64_tr_b16 v[156:157], v3 offset:24576\n\t" "v_mfma_f32_16x16x32_bf16 v[100:103], v[164:167], v[200:203], v[100:103]\n\t" "ds_read_b64_tr_b16 v[158:159], v3 offset:25088\n\t"
    "v_cvt_pk_bf16_f32 v194, v216, v217\n\t" "v_mfma_f32_16x16x32_bf16 v[104:107], v[168:171], v[196:199], v[104:107]\n\t" "ds_read_b64_tr_b16 v[160:161], v4 offset:24576\n\t"
    "v_cvt_pk_bf16_f32 v195, v218, v219\n\t" "v_mfma_f32_16x16x32_bf16 v[108:111], v[168:171], v[200:203], v[108:111]\n\t" "ds_read_b64_tr_b16 v[162:163], v4 offset:25088\n\t" "s_waitcnt lgkmcnt(4)\n\t"
    "v_mfma_f32_16x16x32_bf16 v[112:115], v[196:199], v[172:175], v[112:115]\n\t" "ds_read_b64_tr_b16 v[164:165], v5 offset:24576\n\t"
    "v_mfma_f32_16x16x32_bf16 v[116:119], v[200:203], v[172:175], v[116:119]\n\t" "ds_read_b64_tr_b16 v[166:167], v5 offset:25088\n\t"
    "v_mfma_f32_16x16x32_bf16 v[148:151], v[232:235], v[172:175], v[148:151]\n\t" "ds_read_b128 v[228:231], v1 offset:16384\n\t" "global_load_lds_dwordx4 v0, %[sb] offset:2048\n\t"
    "v_mfma_f32_16x16x32_bf16 v[120:123], v[196:199], v[176:179], v[120:123]\n\t" "ds_read_b64_tr_b16 v[168:169], v6 offset:24576\n\t" "v_add_u32 v0, 0x30000, v0\n\t"
    "v_mfma_f32_16x16x32_bf16 v[124:127], v[200:203], v[176:179], v[124:127]\n\t" "ds_read_b64_tr_b16 v[170:171], v6 offset:25088\n\t"
    "v_mfma_f32_16x16x32_bf16 v[128:131], v[196:199], v[180:183], v[128:131]\n\t" "ds_read_b64_tr_b16 v[172:173], v3 offset:26624\n\t"
    "v_mfma_f32_16x16x32_bf16 v[132:135], v[200:203], v[180:183], v[132:135]\n\t" "ds_read_b64_tr_b16 v[174:175], v3 offset:27136\n\t"
    "v_mfma_f32_16x16x32_bf16 v[136:139], v[196:199], v[184:187], v[136:139]\n\t" "ds_read_b64_tr_b16 v[176:177], v4 offset:26624\n\t"
    "v_mfma_f32_16x16x32_bf16 v[140:143], v[200:203], v[184:187], v[140:143]\n\t" "ds_read_b64_tr_b16 v[178:179], v4 offset:27136\n\t" "s_waitcnt lgkmcnt(4)\n\t"
    "v_mfma_f32_16x16x32_bf16 v[48:51], v[156:159], v[188:191], v[48:51]\n\t" "ds_read_b64_tr_b16 v[180:181], v5 offset:26624\n\t" "s_add_u32 m0, %[sl], 83968\n\t"
    "v_mfma_f32_16x16x32_bf16 v[52:55], v[156:159], v[192:195], v[52:55]\n\t" "ds_read_b64_tr_b16 v[182:183], v5 offset:27136\n\t"
    "v_mfma_f32_16x16x32_bf16 v[152:155], v[228:231], v[156:159], v[152:155]\n\t" "global_load_lds_dwordx4 v0, %[sb] offset:-2048\n\t"
    "v_mfma_f32_16x16x32_bf16 v[56:59], v[160:163], v[188:191], v[56:59]\n\t" "ds_read_b64_tr_b16 v[184:185], v6 offset:26624\n\t" "v_mfma_f32_4x4x4_16b_bf16 v[204:207], v[224:225], a[136:137], 0\n\t"
    "v_mfma_f32_4x4x4_16b_bf16 v[208:211], v[226:227], a[138:139], 0\n\t" "v_mfma_f32_4x4x4_16b_bf16 v[212:215], v[224:225], a[140:141], 0\n\t"
    "v_mfma_f32_4x4x4_16b_bf16 v[216:219], v[226:227], a[142:143], 0\n\t" "v_mfma_f32_16x16x32_bf16 v[60:63], v[160:163], v[192:195], v[60:63]\n\t" "ds_read_b64_tr_b16 v[186:187], v6 offset:27136\n\t"
    "v_mfma_f32_16x16x32_bf16 v[64:67], v[164:167], v[188:191], v[64:67]\n\t" "ds_read_b64_tr_b16 v[156:157], v3 offset:28672\n\t" "v_mfma_f32_16x16x32_bf16 v[68:71], v[164:167], v[192:195], v[68:71]\n\t"
    "ds_read_b64_tr_b16 v[158:159], v3 offset:29184\n\t" "v_cvt_pk_bf16_f32 v196, v204, v205\n\t" "v_mfma_f32_16x16x32_bf16 v[72:75], v[168:171], v[188:191], v[72:75]\n\t"
    "ds_read_b64_tr_b16 v[160:161], v4 offset:28672\n\t" "v_cvt_pk_bf16_f32 v197, v206, v207\n\t" "v_mfma_f32_16x16x32_bf16 v[76:79], v[168:171], v[192:195], v[76:79]\n\t"
    "ds_read_b64_tr_b16 v[162:163], v4 offset:29184\n\t" "s_waitcnt lgkmcnt(4)\n\t" "v_mfma_f32_16x16x32_bf16 v[80:83], v[172:175], v[188:191], v[80:83]\n\t"
    "ds_read_b64_tr_b16 v[164:165], v5 offset:28672\n\t" "v_cvt_pk_bf16_f32 v198, v208, v209\n\t" "v_mfma_f32_16x16x32_bf16 v[84:87], v[172:175], v[192:195], v[84:87]\n\t"
    "ds_read_b64_tr_b16 v[166:167], v5 offset:29184\n\t" "v_cvt_pk_bf16_f32 v199, v210, v211\n\t" "v_mfma_f32_16x16x32_bf16 v[144:147], v[172:175], v[228:231], v[144:147]\n\t"
    "ds_read_b128 v[220:223], v2 offset:4608\n\t" "global_load_lds_dwordx4 v0, %[sb] offset:0\n\t" "v_mfma_f32_16x16x32_bf16 v[88:91], v[176:179], v[188:191], v[88:91]\n\t"
    "ds_read_b64_tr_b16 v[168:169], v6 offset:28672\n\t" "v_cvt_pk_bf16_f32 v200, v212, v213\n\t" "v_mfma_f32_16x16x32_bf16 v[92:95], v[176:179], v[192:195], v[92:95]\n\t"
    "ds_read_b64_tr_b16 v[170:171], v6 offset:29184\n\t" "v_cvt_pk_bf16_f32 v201, v214, v215\n\t" "v_mfma_f32_16x16x32_bf16 v[96:99], v[180:183], v[188:191], v[96:99]\n\t"
    "ds_read_b64_tr_b16 v[172:173], v3 offset:36864\n\t" "v_mfma_f32_16x16x32_bf16 v[100:103], v[180:183], v[192:195], v[100:103]\n\t" "ds_read_b64_tr_b16 v[174:175], v3 offset:37376\n\t"
    "v_cvt_pk_bf16_f32 v202, v216, v217\n\t" "v_mfma_f32_16x16x32_bf16 v[104:107], v[184:187], v[188:191], v[104:107]\n\t" "ds_read_b64_tr_b16 v[176:177], v4 offset:36864\n\t"
    "v_cvt_pk_bf16_f32 v203, v218, v219\n\t" "v_mfma_f32_16x16x32_bf16 v[108:111], v[184:187], v[192:195], v[108:111]\n\t" "ds_read_b64_tr_b16 v[178:179], v4 offset:37376\n\t" "s_waitcnt lgkmcnt(4)\n\t"
    "v_mfma_f32_16x16x32_bf16 v[112:115], v[188:191], v[156:159], v[112:115]\n\t" "ds_read_b64_tr_b16 v[180:181], v5 offset:36864\n\t"
    "v_mfma_f32_16x16x32_bf16 v[116:119], v[192:195], v[156:159], v[116:119]\n\t" "ds_read_b64_tr_b16 v[182:183], v5 offset:37376\n\t"
    "v_mfma_f32_16x16x32_bf16 v[148:151], v[228:231], v[156:159], v[148:151]\n\t" "ds_read_b128 v[232:235], v1 offset:17408\n\t" "global_load_lds_dwordx4 v0, %[sb] offset:2048\n\t"
    "v_mfma_f32_16x16x32_bf16 v[120:123], v[188:191], v[160:163], v[120:123]\n\t" "ds_read_b64_tr_b16 v[184:185], v6 offset:36864\n\t" "v_add_u32 v0, 0x30000, v0\n\t"
    "v_mfma_f32_16x16x32_bf16 v[124:127], v[192:195], v[160:163], v[124:127]\n\t" "ds_read_b64_tr_b16 v[186:187], v6 offset:37376\n\t" "v_add_u32 v16, 0x70000, v16\n\t"
    "v_mfma_f32_16x16x32_bf16 v[128:131], v[188:191], v[164:167], v[128:131]\n\t" "ds_read_b64_tr_b16 v[156:157], v3 offset:38912\n\t"
    "v_mfma_f32_16x16x32_bf16 v[132:135], v[192:195], v[164:167], v[132:135]\n\t" "ds_read_b64_tr_b16 v[158:159], v3 offset:39424\n\t" "s_mov_b64 exec, %[tm]\n\t" "global_load_dword a64, v16, %[pf]\n\t"
    "s_mov_b64 exec, -1\n\t" "v_mfma_f32_16x16x32_bf16 v[136:139], v[188:191], v[168:171], v[136:139]\n\t" "ds_read_b64_tr_b16 v[160:161], v4 offset:38912\n\t"
    "v_mfma_f32_16x16x32_bf16 v[140:143], v[192:195], v[168:171], v[140:143]\n\t" "ds_read_b64_tr_b16 v[162:163], v4 offset:39424\n\t" "s_waitcnt lgkmcnt(4)\n\t"
    "v_mfma_f32_16x16x32_bf16 v[48:51], v[172:175], v[196:199], v[48:51]\n\t" "ds_read_b64_tr_b16 v[164:165], v5 offset:38912\n\t" "s_add_u32 m0, %[sl], 22528\n\t"
    "v_mfma_f32_16x16x32_bf16 v[52:55], v[172:175], v[200:203], v[52:55]\n\t" "ds_read_b64_tr_b16 v[166:167], v5 offset:39424\n\t"
    "v_mfma_f32_16x16x32_bf16 v[152:155], v[232:235], v[172:175], v[152:155]\n\t" "global_load_lds_dwordx4 v0, %[sb] offset:-2048\n\t"
    "v_mfma_f32_16x16x32_bf16 v[56:59], v[176:179], v[196:199], v[56:59]\n\t" "ds_read_b64_tr_b16 v[168:169], v6 offset:38912\n\t" "v_mfma_f32_4x4x4_16b_bf16 v[204:207], v[220:221], a[144:145], 0\n\t"
    "v_mfma_f32_4x4x4_16b_bf16 v[208:211], v[222:223], a[146:147], 0\n\t" "v_mfma_f32_4x4x4_16b_bf16 v[212:215], v[220:221], a[148:149], 0\n\t"
    "v_mfma_f32_4x4x4_16b_bf16 v[216:219], v[222:223], a[150:151], 0\n\t" "v_mfma_f32_16x16x32_bf16 v[60:63], v[176:179], v[200:203], v[60:63]\n\t" "ds_read_b64_tr_b16 v[170:171], v6 offset:39424\n\t"
    "v_mfma_f32_16x16x32_bf16 v[64:67], v[180:183], v[196:199], v[64:67]\n\t" "ds_read_b64_tr_b16 v[172:173], v3 offset:40960\n\t" "v_mfma_f32_16x16x32_bf16 v[68:71], v[180:183], v[200:203], v[68:71]\n\t"
    "ds_read_b64_tr_b16 v[174:175], v3 offset:41472\n\t" "v_cvt_pk_bf16_f32 v188, v204, v205\n\t" "v_mfma_f32_16x16x32_bf16 v[72:75], v[184:187], v[196:199], v[72:75]\n\t"
    "ds_read_b64_tr_b16 v[176:177], v4 offset:40960\n\t" "v_cvt_pk_bf16_f32 v189, v206, v207\n\t" "v_mfma_f32_16x16x32_bf16 v[76:79], v[184:187], v[200:203], v[76:79]\n\t"
    "ds_read_b64_tr_b16 v[178:179], v4 offset:41472\n\t" "s_waitcnt lgkmcnt(4)\n\t" "v_mfma_f32_16x16x32_bf16 v[80:83], v[156:159], v[196:199], v[80:83]\n\t"
    "ds_read_b64_tr_b16 v[180:181], v5 offset:40960\n\t" "v_cvt_pk_bf16_f32 v190, v208, v209\n\t" "v_mfma_f32_16x16x32_bf16 v[84:87], v[156:159], v[200:203], v[84:87]\n\t"
    "ds_read_b64_tr_b16 v[182:183], v5 offset:41472\n\t" "v_cvt_pk_bf16_f32 v191, v210, v211\n\t" "v_mfma_f32_16x16x32_bf16 v[144:147], v[156:159], v[232:235], v[144:147]\n\t"
    "ds_read_b128 v[224:227], v2 offset:4864\n\t" "global_load_lds_dwordx4 v0, %[sb] offset:0\n\t" "v_mfma_f32_16x16x32_bf16 v[88:91], v[160:163], v[196:199], v[88:91]\n\t"
    "ds_read_b64_tr_b16 v[184:185], v6 offset:40960\n\t" "v_cvt_pk_bf16_f32 v192, v212, v213\n\t" "v_mfma_f32_16x16x32_bf16 v[92:95], v[160:163], v[200:203], v[92:95]\n\t"
    "ds_read_b64_tr_b16 v[186:187], v6 offset:41472\n\t" "v_cvt_pk_bf16_f32 v193, v214, v215\n\t" "v_mfma_f32_16x16x32_bf16 v[96:99], v[164:167], v[196:199], v[96:99]\n\t" "s_waitcnt vmcnt(13)\n\t"
    "s_barrier\n\t" "ds_read_b64_tr_b16 v[156:157], v7 offset:8192\n\t" "v_mfma_f32_16x16x32_bf16 v[100:103], v[164:167], v[200:203], v[100:103]\n\t" "ds_read_b64_tr_b16 v[158:159], v7 offset:8704\n\t"
    "v_cvt_pk_bf16_f32 v194, v216, v217\n\t" "v_mfma_f32_16x16x32_bf16 v[104:107], v[168:171], v[196:199], v[104:107]\n\t" "ds_read_b64_tr_b16 v[160:161], v8 offset:8192\n\t"
    "v_cvt_pk_bf16_f32 v195, v218, v219\n\t" "v_mfma_f32_16x16x32_bf16 v[108:111], v[168:171], v[200:203], v[108:111]\n\t" "ds_read_b64_tr_b16 v[162:163], v8 offset:8704\n\t" "s_waitcnt lgkmcnt(4)\n\t"
    "v_mfma_f32_16x16x32_bf16 v[112:115], v[196:199], v[172:175], v[112:115]\n\t" "ds_read_b64_tr_b16 v[164:165], v9 offset:8192\n\t"
    "v_mfma_f32_16x16x32_bf16 v[116:119], v[200:203], v[172:175], v[116:119]\n\t" "ds_read_b64_tr_b16 v[166:167], v9 offset:8704\n\t"
    "v_mfma_f32_16x16x32_bf16 v[148:151], v[232:235], v[172:175], v[148:151]\n\t" "ds_read_b128 v[228:231], v1 offset:18432\n\t" "global_load_lds_dwordx4 v0, %[sb] offset:2048\n\t"
    "v_mfma_f32_16x16x32_bf16 v[120:123], v[196:199], v[176:179], v[120:123]\n\t" "ds_read_b64_tr_b16 v[168:169], v10 offset:8192\n\t" "s_cmp_eq_u32 %[rot], 2\n\t" "s_cselect_b32 s42, s43, 0x30000\n\t"
    "v_add_u32 v0, s42, v0\n\t" "v_mfma_f32_16x16x32_bf16 v[124:127], v[200:203], v[176:179], v[124:127]\n\t" "ds_read_b64_tr_b16 v[170:171], v10 offset:8704\n\t" "v_lshlrev_b32_e64 v17, 4, v13\n\t"
    "v_mfma_f32_16x16x32_bf16 v[128:131], v[196:199], v[180:183], v[128:131]\n\t" "ds_read_b64_tr_b16 v[172:173], v7 offset:10240\n\t"
    "v_mfma_f32_16x16x32_bf16 v[132:135], v[200:203], v[180:183], v[132:135]\n\t" "ds_read_b64_tr_b16 v[174:175], v7 offset:10752\n\t" "global_load_dword a64, v17, %[cd]\n\t"
    "v_mfma_f32_16x16x32_bf16 v[136:139], v[196:199], v[184:187], v[136:139]\n\t" "ds_read_b64_tr_b16 v[176:177], v8 offset:10240\n\t"
    "v_mfma_f32_16x16x32_bf16 v[140:143], v[200:203], v[184:187], v[140:143]\n\t" "ds_read_b64_tr_b16 v[178:179], v8 offset:10752\n\t" "s_waitcnt lgkmcnt(4)\n\t"
    "v_mfma_f32_16x16x32_bf16 v[48:51], v[156:159], v[188:191], v[48:51]\n\t" "ds_read_b64_tr_b16 v[180:181], v9 offset:10240\n\t" "s_add_u32 m0, %[sl], 34816\n\t"
    "v_mfma_f32_16x16x32_bf16 v[52:55], v[156:159], v[192:195], v[52:55]\n\t" "ds_read_b64_tr_b16 v[182:183], v9 offset:10752\n\t" "s_mov_b32 s50, 0xaaaaaaaa\n\t"
    "v_mfma_f32_16x16x32_bf16 v[152:155], v[228:231], v[156:159], v[152:155]\n\t" "global_load_lds_dwordx4 v0, %[sb] offset:-2048\n\t" "s_mov_b32 s51, s50\n\t"
    "v_mfma_f32_16x16x32_bf16 v[56:59], v[160:163], v[188:191], v[56:59]\n\t" "ds_read_b64_tr_b16 v[184:185], v10 offset:10240\n\t" "s_mov_b32 s52, 0xfff0ffff\n\t"
    "v_mfma_f32_4x4x4_16b_bf16 v[204:207], v[224:225], a[152:153], 0\n\t" "v_mfma_f32_4x4x4_16b_bf16 v[208:211], v[226:227], a[154:155], 0\n\t"
    "v_mfma_f32_4x4x4_16b_bf16 v[212:215], v[224:225], a[156:157], 0\n\t" "v_mfma_f32_4x4x4_16b_bf16 v[216:219], v[226:227], a[158:159], 0\n\t" "s_mov_b32 s53, 0xf000ff00\n\t"
    "v_mfma_f32_16x16x32_bf16 v[60:63], v[160:163], v[192:195], v[60:63]\n\t" "ds_read_b64_tr_b16 v[186:187], v10 offset:10752\n\t" "s_mov_b32 s54, 0xffc0fffc\n\t"
    "v_mfma_f32_16x16x32_bf16 v[64:67], v[164:167], v[188:191], v[64:67]\n\t" "ds_read_b64_tr_b16 v[156:157], v7 offset:12288\n\t" "s_mov_b32 s55, 0xc000fc00\n\t"
    "v_mfma_f32_16x16x32_bf16 v[68:71], v[164:167], v[192:195], v[68:71]\n\t" "ds_read_b64_tr_b16 v[158:159], v7 offset:12800\n\t" "v_cvt_pk_bf16_f32 v196, v204, v205\n\t"
    "v_mfma_f32_16x16x32_bf16 v[72:75], v[168:171], v[188:191], v[72:75]\n\t" "ds_read_b64_tr_b16 v[160:161], v8 offset:12288\n\t" "v_cvt_pk_bf16_f32 v197, v206, v207\n\t"
    "v_mfma_f32_16x16x32_bf16 v[76:79], v[168:171], v[192:195], v[76:79]\n\t" "ds_read_b64_tr_b16 v[162:163], v8 offset:12800\n\t" "s_waitcnt lgkmcnt(4)\n\t"
    "v_mfma_f32_16x16x32_bf16 v[80:83], v[172:175], v[188:191], v[80:83]\n\t" "ds_read_b64_tr_b16 v[164:165], v9 offset:12288\n\t" "v_cvt_pk_bf16_f32 v198, v208, v209\n\t"
    "v_mfma_f32_16x16x32_bf16 v[84:87], v[172:175], v[192:195], v[84:87]\n\t" "ds_read_b64_tr_b16 v[166:167], v9 offset:12800\n\t" "v_cvt_pk_bf16_f32 v199, v210, v211\n\t"
    "v_mfma_f32_16x16x32_bf16 v[144:147], v[172:175], v[228:231], v[144:147]\n\t" "ds_read_b128 v[220:223], v2 offset:5120\n\t" "global_load_lds_dwordx4 v0, %[sb] offset:0\n\t"
    "v_mfma_f32_16x16x32_bf16 v[88:91], v[176:179], v[188:191], v[88:91]\n\t" "ds_read_b64_tr_b16 v[168:169], v10 offset:12288\n\t" "v_cvt_pk_bf16_f32 v200, v212, v213\n\t"
    "v_mfma_f32_16x16x32_bf16 v[92:95], v[176:179], v[192:195], v[92:95]\n\t" "ds_read_b64_tr_b16 v[170:171], v10 offset:12800\n\t" "v_cvt_pk_bf16_f32 v201, v214, v215\n\t"
    "v_mfma_f32_16x16x32_bf16 v[96:99], v[180:183], v[188:191], v[96:99]\n\t" "ds_read_b64_tr_b16 v[172:173], v7 offset:20480\n\t" "s_cmp_eq_u32 %[rw], 0\n\t" "s_cselect_b64 s[58:59], -1, 0\n\t"
    "v_mfma_f32_16x16x32_bf16 v[100:103], v[180:183], v[192:195], v[100:103]\n\t" "ds_read_b64_tr_b16 v[174:175], v7 offset:20992\n\t" "v_cvt_pk_bf16_f32 v202, v216, v217\n\t"
    "v_mfma_f32_16x16x32_bf16 v[104:107], v[184:187], v[188:191], v[104:107]\n\t" "ds_read_b64_tr_b16 v[176:177], v8 offset:20480\n\t" "v_cvt_pk_bf16_f32 v203, v218, v219\n\t"
    "v_mfma_f32_16x16x32_bf16 v[108:111], v[184:187], v[192:195], v[108:111]\n\t" "ds_read_b64_tr_b16 v[178:179], v8 offset:20992\n\t" "s_waitcnt lgkmcnt(4)\n\t"
    "v_mfma_f32_16x16x32_bf16 v[112:115], v[188:191], v[156:159], v[112:115]\n\t" "ds_read_b64_tr_b16 v[180:181], v9 offset:20480\n\t" "s_andn2_b64 s[56:57], s[58:59], s[52:53]\n\t"
    "v_mfma_f32_16x16x32_bf16 v[116:119], v[192:195], v[156:159], v[116:119]\n\t" "ds_read_b64_tr_b16 v[182:183], v9 offset:20992\n\t" "s_andn2_b64 s[58:59], s[58:59], s[54:55]\n\t"
    "v_mfma_f32_16x16x32_bf16 v[148:151], v[228:231], v[156:159], v[148:151]\n\t" "ds_read_b128 v[232:235], v1 offset:19456\n\t" "global_load_lds_dwordx4 v0, %[sb] offset:2048\n\t"
    "v_mfma_f32_16x16x32_bf16 v[120:123], v[188:191], v[160:163], v[120:123]\n\t" "ds_read_b64_tr_b16 v[184:185], v10 offset:20480\n\t" "v_add_u32 v0, 0x30000, v0\n\t"
    "v_mfma_f32_16x16x32_bf16 v[124:127], v[192:195], v[160:163], v[124:127]\n\t" "ds_read_b64_tr_b16 v[186:187], v10 offset:20992\n\t" "s_mov_b32 s48, 0x00030003\n\t"
    "v_mfma_f32_16x16x32_bf16 v[128:131], v[188:191], v[164:167], v[128:131]\n\t" "ds_read_b64_tr_b16 v[156:157], v7 offset:22528\n\t" "s_mov_b32 s49, s48\n\t"
    "v_mfma_f32_16x16x32_bf16 v[132:135], v[192:195], v[164:167], v[132:135]\n\t" "ds_read_b64_tr_b16 v[158:159], v7 offset:23040\n\t" "v_and_b32 v46, 63, v13\n\t"
    "v_mfma_f32_16x16x32_bf16 v[136:139], v[188:191], v[168:171], v[136:139]\n\t" "ds_read_b64_tr_b16 v[160:161], v8 offset:22528\n\t" "v_and_b32_e64 v47, 15, v13\n\t"
    "v_mfma_f32_16x16x32_bf16 v[140:143], v[192:195], v[168:171], v[140:143]\n\t" "ds_read_b64_tr_b16 v[162:163], v8 offset:23040\n\t" "s_waitcnt lgkmcnt(4)\n\t"
    "v_mfma_f32_16x16x32_bf16 v[48:51], v[172:175], v[196:199], v[48:51]\n\t" "ds_read_b64_tr_b16 v[164:165], v9 offset:22528\n\t" "s_add_u32 m0, %[sl], 47104\n\t"
    "v_mfma_f32_16x16x32_bf16 v[52:55], v[172:175], v[200:203], v[52:55]\n\t" "ds_read_b64_tr_b16 v[166:167], v9 offset:23040\n\t" "v_and_b32 v45, 48, v13\n\t"
    "v_mfma_f32_16x16x32_bf16 v[152:155], v[232:235], v[172:175], v[152:155]\n\t" "global_load_lds_dwordx4 v0, %[sb] offset:-2048\n\t" "v_and_b32_e64 v44, 1, v13\n\t"
    "v_mfma_f32_16x16x32_bf16 v[56:59], v[176:179], v[196:199], v[56:59]\n\t" "ds_read_b64_tr_b16 v[168:169], v10 offset:22528\n\t" "v_lshl_add_u32 v44, v44, 2, v45\n\t"
    "v_mfma_f32_4x4x4_16b_bf16 v[204:207], v[220:221], a[160:161], 0\n\t" "v_mfma_f32_4x4x4_16b_bf16 v[208:211], v[222:223], a[162:163], 0\n\t"
    "v_mfma_f32_4x4x4_16b_bf16 v[212:215], v[220:221], a[164:165], 0\n\t" "v_mfma_f32_4x4x4_16b_bf16 v[216:219], v[222:223], a[166:167], 0\n\t" "v_add_u32_e64 v44, %[w128], v44\n\t"
    "v_mfma_f32_16x16x32_bf16 v[60:63], v[176:179], v[200:203], v[60:63]\n\t" "ds_read_b64_tr_b16 v[170:171], v10 offset:23040\n\t" "v_add_u32_e64 v44, %[l0], v44\n\t"
    "v_mfma_f32_16x16x32_bf16 v[64:67], v[180:183], v[196:199], v[64:67]\n\t" "ds_read_b64_tr_b16 v[172:173], v7 offset:24576\n\t" "v_add_u32_e64 v43, 64, v44\n\t"
    "v_mfma_f32_16x16x32_bf16 v[68:71], v[180:183], v[200:203], v[68:71]\n\t" "ds_read_b64_tr_b16 v[174:175], v7 offset:25088\n\t" "v_cvt_pk_bf16_f32 v188, v204, v205\n\t"
    "v_mfma_f32_16x16x32_bf16 v[72:75], v[184:187], v[196:199], v[72:75]\n\t" "ds_read_b64_tr_b16 v[176:177], v8 offset:24576\n\t" "v_cvt_pk_bf16_f32 v189, v206, v207\n\t"
    "v_mfma_f32_16x16x32_bf16 v[76:79], v[184:187], v[200:203], v[76:79]\n\t" "ds_read_b64_tr_b16 v[178:179], v8 offset:25088\n\t" "s_waitcnt lgkmcnt(4)\n\t"
    "v_mfma_f32_16x16x32_bf16 v[80:83], v[156:159], v[196:199], v[80:83]\n\t" "ds_read_b64_tr_b16 v[180:181], v9 offset:24576\n\t" "v_cvt_pk_bf16_f32 v190, v208, v209\n\t"
    "v_mfma_f32_16x16x32_bf16 v[84:87], v[156:159], v[200:203], v[84:87]\n\t" "ds_read_b64_tr_b16 v[182:183], v9 offset:25088\n\t" "v_cvt_pk_bf16_f32 v191, v210, v211\n\t"
    "v_mfma_f32_16x16x32_bf16 v[144:147], v[156:159], v[232:235], v[144:147]\n\t" "ds_read_b128 v[224:227], v2 offset:5376\n\t" "global_load_lds_dwordx4 v0, %[sb] offset:0\n\t"
    "v_mfma_f32_16x16x32_bf16 v[88:91], v[160:163], v[196:199], v[88:91]\n\t" "ds_read_b64_tr_b16 v[184:185], v10 offset:24576\n\t" "v_cvt_pk_bf16_f32 v192, v212, v213\n\t"
    "v_mfma_f32_16x16x32_bf16 v[92:95], v[160:163], v[200:203], v[92:95]\n\t" "ds_read_b64_tr_b16 v[186:187], v10 offset:25088\n\t" "v_cvt_pk_bf16_f32 v193, v214, v215\n\t"
    "v_mfma_f32_16x16x32_bf16 v[96:99], v[164:167], v[196:199], v[96:99]\n\t" "s_waitcnt vmcnt(13)\n\t" "s_barrier\n\t" "ds_read_b64_tr_b16 v[156:157], v7 offset:32768\n\t"
    "v_mfma_f32_16x16x32_bf16 v[100:103], v[164:167], v[200:203], v[100:103]\n\t" "ds_read_b64_tr_b16 v[158:159], v7 offset:33280\n\t" "v_cvt_pk_bf16_f32 v194, v216, v217\n\t"
    "v_mfma_f32_16x16x32_bf16 v[104:107], v[168:171], v[196:199], v[104:107]\n\t" "ds_read_b64_tr_b16 v[160:161], v8 offset:32768\n\t" "v_cvt_pk_bf16_f32 v195, v218, v219\n\t"
    "v_mfma_f32_16x16x32_bf16 v[108:111], v[168:171], v[200:203], v[108:111]\n\t" "ds_read_b64_tr_b16 v[162:163], v8 offset:33280\n\t" "s_waitcnt lgkmcnt(4)\n\t"
    "v_mfma_f32_16x16x32_bf16 v[112:115], v[196:199], v[172:175], v[112:115]\n\t" "ds_read_b64_tr_b16 v[164:165], v9 offset:32768\n\t" "v_cndmask_b32 v42, v44, v43, s[52:53]\n\t"
    "v_mfma_f32_16x16x32_bf16 v[116:119], v[200:203], v[172:175], v[116:119]\n\t" "ds_read_b64_tr_b16 v[166:167], v9 offset:33280\n\t" "v_cndmask_b32 v43, v44, v43, s[54:55]\n\t"
    "v_mfma_f32_16x16x32_bf16 v[148:151], v[232:235], v[172:175], v[148:151]\n\t" "ds_read_b128 v[228:231], v1 offset:20480\n\t" "global_load_lds_dwordx4 v0, %[sb] offset:2048\n\t"
    "v_mfma_f32_16x16x32_bf16 v[120:123], v[196:199], v[176:179], v[120:123]\n\t" "ds_read_b64_tr_b16 v[168:169], v10 offset:32768\n\t" "v_add_u32 v0, 0x30000, v0\n\t"
    "v_mfma_f32_16x16x32_bf16 v[124:127], v[200:203], v[176:179], v[124:127]\n\t" "ds_read_b64_tr_b16 v[170:171], v10 offset:33280\n\t" "v_add_u32 v16, 0x70000, v16\n\t"
    "v_mfma_f32_16x16x32_bf16 v[128:131], v[196:199], v[180:183], v[128:131]\n\t" "ds_read_b64_tr_b16 v[172:173], v7 offset:34816\n\t" "ds_read_b32 v24, v42 offset:17408\n\t"
    "v_mfma_f32_16x16x32_bf16 v[132:135], v[200:203], v[180:183], v[132:135]\n\t" "ds_read_b64_tr_b16 v[174:175], v7 offset:35328\n\t" "s_mov_b64 exec, %[tm]\n\t" "global_load_dword a64, v16, %[pf]\n\t"
    "s_mov_b64 exec, -1\n\t" "v_mfma_f32_16x16x32_bf16 v[136:139], v[196:199], v[184:187], v[136:139]\n\t" "ds_read_b64_tr_b16 v[176:177], v8 offset:34816\n\t" "ds_read_b32 v25, v43 offset:17416\n\t"
    "v_mfma_f32_16x16x32_bf16 v[140:143], v[200:203], v[184:187], v[140:143]\n\t" "ds_read_b64_tr_b16 v[178:179], v8 offset:35328\n\t" "s_waitcnt lgkmcnt(6)\n\t"
    "v_mfma_f32_16x16x32_bf16 v[48:51], v[156:159], v[188:191], v[48:51]\n\t" "ds_read_b64_tr_b16 v[180:181], v9 offset:34816\n\t" "s_add_u32 m0, %[sl], 59392\n\t"
    "v_mfma_f32_16x16x32_bf16 v[52:55], v[156:159], v[192:195], v[52:55]\n\t" "ds_read_b64_tr_b16 v[182:183], v9 offset:35328\n\t" "ds_read_b32 v26, v42 offset:17472\n\t"
    "v_mfma_f32_16x16x32_bf16 v[152:155], v[228:231], v[156:159], v[152:155]\n\t" "global_load_lds_dwordx4 v0, %[sb] offset:-2048\n\t" "ds_read_b32 v27, v43 offset:17480\n\t"
    "v_mfma_f32_16x16x32_bf16 v[56:59], v[160:163], v[188:191], v[56:59]\n\t" "ds_read_b64_tr_b16 v[184:185], v10 offset:34816\n\t" "v_mov_b32 v40, 0x0c0c0100\n\t"
    "v_mfma_f32_4x4x4_16b_bf16 v[204:207], v[224:225], a[168:169], 0\n\t" "v_mfma_f32_4x4x4_16b_bf16 v[208:211], v[226:227], a[170:171], 0\n\t"
    "v_mfma_f32_4x4x4_16b_bf16 v[212:215], v[224:225], a[172:173], 0\n\t" "v_mfma_f32_4x4x4_16b_bf16 v[216:219], v[226:227], a[174:175], 0\n\t" "v_mov_b32 v41, 0x01000c0c\n\t"
    "v_mfma_f32_16x16x32_bf16 v[60:63], v[160:163], v[192:195], v[60:63]\n\t" "ds_read_b64_tr_b16 v[186:187], v10 offset:35328\n\t" "v_cndmask_b32 v40, v40, v41, s[50:51]\n\t"
    "v_mfma_f32_16x16x32_bf16 v[64:67], v[164:167], v[188:191], v[64:67]\n\t" "ds_read_b64_tr_b16 v[156:157], v7 offset:36864\n\t" "v_mov_b32 v39, 0x0c0c0302\n\t"
    "v_mfma_f32_16x16x32_bf16 v[68:71], v[164:167], v[192:195], v[68:71]\n\t" "ds_read_b64_tr_b16 v[158:159], v7 offset:37376\n\t" "v_cvt_pk_bf16_f32 v196, v204, v205\n\t"
    "v_mfma_f32_16x16x32_bf16 v[72:75], v[168:171], v[188:191], v[72:75]\n\t" "ds_read_b64_tr_b16 v[160:161], v8 offset:36864\n\t" "v_cvt_pk_bf16_f32 v197, v206, v207\n\t"
    "v_mfma_f32_16x16x32_bf16 v[76:79], v[168:171], v[192:195], v[76:79]\n\t" "s_waitcnt lgkmcnt(11)\n\t" "ds_read_b64_tr_b16 v[162:163], v8 offset:37376\n\t" "s_waitcnt lgkmcnt(4)\n\t"
    "v_mfma_f32_16x16x32_bf16 v[80:83], v[172:175], v[188:191], v[80:83]\n\t" "ds_read_b64_tr_b16 v[164:165], v9 offset:36864\n\t" "v_cvt_pk_bf16_f32 v198, v208, v209\n\t"
    "v_mfma_f32_16x16x32_bf16 v[84:87], v[172:175], v[192:195], v[84:87]\n\t" "ds_read_b64_tr_b16 v[166:167], v9 offset:37376\n\t" "v_cvt_pk_bf16_f32 v199, v210, v211\n\t"
    "v_mfma_f32_16x16x32_bf16 v[144:147], v[172:175], v[228:231], v[144:147]\n\t" "ds_read_b128 v[220:223], v2 offset:5632\n\t" "global_load_lds_dwordx4 v0, %[sb] offset:0\n\t"
    "v_mfma_f32_16x16x32_bf16 v[88:91], v[176:179], v[188:191], v[88:91]\n\t" "ds_read_b64_tr_b16 v[168:169], v10 offset:36864\n\t" "v_cvt_pk_bf16_f32 v200, v212, v213\n\t"
    "v_mfma_f32_16x16x32_bf16 v[92:95], v[176:179], v[192:195], v[92:95]\n\t" "ds_read_b64_tr_b16 v[170:171], v10 offset:37376\n\t" "v_cvt_pk_bf16_f32 v201, v214, v215\n\t"
    "v_mfma_f32_16x16x32_bf16 v[96:99], v[180:183], v[188:191], v[96:99]\n\t" "ds_read_b64_tr_b16 v[172:173], v3 offset:49152\n\t" "v_mov_b32 v41, 0x03020c0c\n\t"
    "v_mfma_f32_16x16x32_bf16 v[100:103], v[180:183], v[192:195], v[100:103]\n\t" "ds_read_b64_tr_b16 v[174:175], v3 offset:49664\n\t" "v_cvt_pk_bf16_f32 v202, v216, v217\n\t"
    "v_mfma_f32_16x16x32_bf16 v[104:107], v[184:187], v[188:191], v[104:107]\n\t" "ds_read_b64_tr_b16 v[176:177], v4 offset:49152\n\t" "v_cvt_pk_bf16_f32 v203, v218, v219\n\t"
    "v_mfma_f32_16x16x32_bf16 v[108:111], v[184:187], v[192:195], v[108:111]\n\t" "ds_read_b64_tr_b16 v[178:179], v4 offset:49664\n\t" "s_waitcnt lgkmcnt(4)\n\t"
    "v_mfma_f32_16x16x32_bf16 v[112:115], v[188:191], v[156:159], v[112:115]\n\t" "ds_read_b64_tr_b16 v[180:181], v5 offset:49152\n\t" "v_cndmask_b32 v41, v39, v41, s[50:51]\n\t"
    "v_mfma_f32_16x16x32_bf16 v[116:119], v[192:195], v[156:159], v[116:119]\n\t" "ds_read_b64_tr_b16 v[182:183], v5 offset:49664\n\t" "v_mov_b32 v39, 0x0c0c0c0c\n\t"
    "v_mfma_f32_16x16x32_bf16 v[148:151], v[228:231], v[156:159], v[148:151]\n\t" "ds_read_b128 v[232:235], v1 offset:21504\n\t" "global_load_lds_dwordx4 v0, %[sb] offset:2048\n\t"
    "v_mfma_f32_16x16x32_bf16 v[120:123], v[188:191], v[160:163], v[120:123]\n\t" "ds_read_b64_tr_b16 v[184:185], v6 offset:49152\n\t" "v_add_u32 v0, 0x30000, v0\n\t"
    "v_mfma_f32_16x16x32_bf16 v[124:127], v[192:195], v[160:163], v[124:127]\n\t" "ds_read_b64_tr_b16 v[186:187], v6 offset:49664\n\t" "s_add_u32 m0, %[sl1], 0\n\t"
    "v_mfma_f32_16x16x32_bf16 v[128:131], v[188:191], v[164:167], v[128:131]\n\t" "ds_read_b64_tr_b16 v[156:157], v3 offset:51200\n\t" "v_cndmask_b32 v20, v40, v39, s[52:53]\n\t"
    "v_mfma_f32_16x16x32_bf16 v[132:135], v[192:195], v[164:167], v[132:135]\n\t" "ds_read_b64_tr_b16 v[158:159], v3 offset:51712\n\t" "global_load_lds_dwordx4 v12, %[so]\n\t"
    "v_mfma_f32_16x16x32_bf16 v[136:139], v[188:191], v[168:171], v[136:139]\n\t" "ds_read_b64_tr_b16 v[160:161], v4 offset:51200\n\t" "v_cndmask_b32 v21, v41, v39, s[54:55]\n\t"
    "v_mfma_f32_16x16x32_bf16 v[140:143], v[192:195], v[168:171], v[140:143]\n\t" "ds_read_b64_tr_b16 v[162:163], v4 offset:51712\n\t" "s_waitcnt lgkmcnt(4)\n\t"
    "v_mfma_f32_16x16x32_bf16 v[48:51], v[172:175], v[196:199], v[48:51]\n\t" "ds_read_b64_tr_b16 v[164:165], v5 offset:51200\n\t" "s_add_u32 m0, %[sl], 96256\n\t"
    "v_mfma_f32_16x16x32_bf16 v[52:55], v[172:175], v[200:203], v[52:55]\n\t" "ds_read_b64_tr_b16 v[166:167], v5 offset:51712\n\t" "v_cndmask_b32 v22, v39, v40, s[52:53]\n\t"
    "v_mfma_f32_16x16x32_bf16 v[152:155], v[232:235], v[172:175], v[152:155]\n\t" "global_load_lds_dwordx4 v0, %[sb] offset:-2048\n\t" "v_cndmask_b32 v23, v39, v41, s[54:55]\n\t"
    "v_mfma_f32_16x16x32_bf16 v[56:59], v[176:179], v[196:199], v[56:59]\n\t" "ds_read_b64_tr_b16 v[168:169], v6 offset:51200\n\t" "s_mov_b32 s60, 0xcccccccc\n\t"
    "v_mfma_f32_4x4x4_16b_bf16 v[204:207], v[220:221], a[176:177], 0\n\t" "v_mfma_f32_4x4x4_16b_bf16 v[208:211], v[222:223], a[178:179], 0\n\t"
    "v_mfma_f32_4x4x4_16b_bf16 v[212:215], v[220:221], a[180:181], 0\n\t" "v_mfma_f32_4x4x4_16b_bf16 v[216:219], v[222:223], a[182:183], 0\n\t" "s_mov_b32 s61, s60\n\t"
    "v_mfma_f32_16x16x32_bf16 v[60:63], v[176:179], v[200:203], v[60:63]\n\t" "ds_read_b64_tr_b16 v[170:171], v6 offset:51712\n\t" "v_cndmask_b32 v14, v40, v39, s[60:61]\n\t"
    "v_mfma_f32_16x16x32_bf16 v[64:67], v[180:183], v[196:199], v[64:67]\n\t" "ds_read_b64_tr_b16 v[172:173], v3 offset:53248\n\t" "v_cndmask_b32 v15, v41, v39, s[60:61]\n\t"
    "v_mfma_f32_16x16x32_bf16 v[68:71], v[180:183], v[200:203], v[68:71]\n\t" "ds_read_b64_tr_b16 v[174:175], v3 offset:53760\n\t" "v_cvt_pk_bf16_f32 v188, v204, v205\n\t"
    "v_mfma_f32_16x16x32_bf16 v[72:75], v[184:187], v[196:199], v[72:75]\n\t" "ds_read_b64_tr_b16 v[176:177], v4 offset:53248\n\t" "v_cvt_pk_bf16_f32 v189, v206, v207\n\t"
    "v_mfma_f32_16x16x32_bf16 v[76:79], v[184:187], v[200:203], v[76:79]\n\t" "ds_read_b64_tr_b16 v[178:179], v4 offset:53760\n\t" "s_waitcnt lgkmcnt(4)\n\t"
    "v_mfma_f32_16x16x32_bf16 v[80:83], v[156:159], v[196:199], v[80:83]\n\t" "ds_read_b64_tr_b16 v[180:181], v5 offset:53248\n\t" "v_cvt_pk_bf16_f32 v190, v208, v209\n\t"
    "v_mfma_f32_16x16x32_bf16 v[84:87], v[156:159], v[200:203], v[84:87]\n\t" "ds_read_b64_tr_b16 v[182:183], v5 offset:53760\n\t" "v_cvt_pk_bf16_f32 v191, v210, v211\n\t"
    "v_mfma_f32_16x16x32_bf16 v[144:147], v[156:159], v[232:235], v[144:147]\n\t" "ds_read_b128 v[224:227], v2 offset:5888\n\t" "global_load_lds_dwordx4 v0, %[sb] offset:0\n\t"
    "v_mfma_f32_16x16x32_bf16 v[88:91], v[160:163], v[196:199], v[88:91]\n\t" "ds_read_b64_tr_b16 v[184:185], v6 offset:53248\n\t" "v_cvt_pk_bf16_f32 v192, v212, v213\n\t"
    "v_mfma_f32_16x16x32_bf16 v[92:95], v[160:163], v[200:203], v[92:95]\n\t" "ds_read_b64_tr_b16 v[186:187], v6 offset:53760\n\t" "v_cvt_pk_bf16_f32 v193, v214, v215\n\t"
    "v_mfma_f32_16x16x32_bf16 v[96:99], v[164:167], v[196:199], v[96:99]\n\t" "s_waitcnt vmcnt(14)\n\t" "s_barrier\n\t" "ds_read_b64_tr_b16 v[156:157], v3 offset:61440\n\t"
    "v_mfma_f32_16x16x32_bf16 v[100:103], v[164:167], v[200:203], v[100:103]\n\t" "ds_read_b64_tr_b16 v[158:159], v3 offset:61952\n\t" "v_cvt_pk_bf16_f32 v194, v216, v217\n\t"
    "v_mfma_f32_16x16x32_bf16 v[104:107], v[168:171], v[196:199], v[104:107]\n\t" "ds_read_b64_tr_b16 v[160:161], v4 offset:61440\n\t" "v_cvt_pk_bf16_f32 v195, v218, v219\n\t"
    "v_mfma_f32_16x16x32_bf16 v[108:111], v[168:171], v[200:203], v[108:111]\n\t" "ds_read_b64_tr_b16 v[162:163], v4 offset:61952\n\t" "s_waitcnt lgkmcnt(4)\n\t"
    "v_mfma_f32_16x16x32_bf16 v[112:115], v[196:199], v[172:175], v[112:115]\n\t" "ds_read_b64_tr_b16 v[164:165], v5 offset:61440\n\t" "v_cndmask_b32 v17, v39, v40, s[60:61]\n\t"
    "v_mfma_f32_16x16x32_bf16 v[116:119], v[200:203], v[172:175], v[116:119]\n\t" "ds_read_b64_tr_b16 v[166:167], v5 offset:61952\n\t" "v_cndmask_b32 v19, v39, v41, s[60:61]\n\t"
    "v_mfma_f32_16x16x32_bf16 v[148:151], v[232:235], v[172:175], v[148:151]\n\t" "ds_read_b128 v[228:231], v1 offset:22528\n\t" "global_load_lds_dwordx4 v0, %[sb] offset:2048\n\t"
    "v_mfma_f32_16x16x32_bf16 v[120:123], v[196:199], v[176:179], v[120:123]\n\t" "ds_read_b64_tr_b16 v[168:169], v6 offset:61440\n\t" "s_cmp_eq_u32 %[rot], 1\n\t" "s_cselect_b32 s42, s43, 0x30000\n\t"
    "v_add_u32_e64 v0, s42, v0\n\t" "v_mfma_f32_16x16x32_bf16 v[124:127], v[200:203], v[176:179], v[124:127]\n\t" "ds_read_b64_tr_b16 v[170:171], v6 offset:61952\n\t" "v_add_u32 v12, 0x20000, v12\n\t"
    "v_mfma_f32_16x16x32_bf16 v[128:131], v[196:199], v[180:183], v[128:131]\n\t" "ds_read_b64_tr_b16 v[172:173], v3 offset:63488\n\t" "s_add_u32 m0, %[sl1], 8192\n\t"
    "v_mfma_f32_16x16x32_bf16 v[132:135], v[200:203], v[180:183], v[132:135]\n\t" "ds_read_b64_tr_b16 v[174:175], v3 offset:64000\n\t" "global_load_lds_dwordx4 v12, %[so]\n\t"
    "v_mfma_f32_16x16x32_bf16 v[136:139], v[196:199], v[184:187], v[136:139]\n\t" "ds_read_b64_tr_b16 v[176:177], v4 offset:63488\n\t" "v_and_b32_e64 v11, 7, v13\n\t"
    "v_mfma_f32_16x16x32_bf16 v[140:143], v[200:203], v[184:187], v[140:143]\n\t" "ds_read_b64_tr_b16 v[178:179], v4 offset:64000\n\t" "s_waitcnt lgkmcnt(4)\n\t"
    "v_mfma_f32_16x16x32_bf16 v[48:51], v[156:159], v[188:191], v[48:51]\n\t" "ds_read_b64_tr_b16 v[180:181], v5 offset:63488\n\t" "s_add_u32 m0, %[sl], 108544\n\t"
    "v_mfma_f32_16x16x32_bf16 v[52:55], v[156:159], v[192:195], v[52:55]\n\t" "ds_read_b64_tr_b16 v[182:183], v5 offset:64000\n\t" "v_lshrrev_b32 v18, 1, v45\n\t"
    "v_mfma_f32_16x16x32_bf16 v[152:155], v[228:231], v[156:159], v[152:155]\n\t" "global_load_lds_dwordx4 v0, %[sb] offset:-2048\n\t" "v_lshl_add_u32 v11, v11, 5, v18\n\t"
    "v_mfma_f32_16x16x32_bf16 v[56:59], v[160:163], v[188:191], v[56:59]\n\t" "ds_read_b64_tr_b16 v[184:185], v6 offset:63488\n\t" "v_add_u32_e64 v11, %[lr], v11\n\t"
    "v_mfma_f32_4x4x4_16b_bf16 v[204:207], v[224:225], a[184:185], 0\n\t" "v_mfma_f32_4x4x4_16b_bf16 v[208:211], v[226:227], a[186:187], 0\n\t"
    "v_mfma_f32_4x4x4_16b_bf16 v[212:215], v[224:225], a[188:189], 0\n\t" "v_mfma_f32_4x4x4_16b_bf16 v[216:219], v[226:227], a[190:191], 0\n\t" "v_mul_f32_e64 v28, v240, v24\n\t"
    "v_mfma_f32_16x16x32_bf16 v[60:63], v[160:163], v[192:195], v[60:63]\n\t" "ds_read_b64_tr_b16 v[186:187], v6 offset:64000\n\t" "v_mul_f32_e64 v29, v240, v25\n\t"
    "v_mfma_f32_16x16x32_bf16 v[64:67], v[164:167], v[188:191], v[64:67]\n\t" "ds_read_b64_tr_b16 v[156:157], v7 offset:0\n\t" "v_mul_f32_e64 v30, v241, v26\n\t"
    "v_mfma_f32_16x16x32_bf16 v[68:71], v[164:167], v[192:195], v[68:71]\n\t" "ds_read_b64_tr_b16 v[158:159], v7 offset:512\n\t" "v_cvt_pk_bf16_f32 v196, v204, v205\n\t"
    "v_mfma_f32_16x16x32_bf16 v[72:75], v[168:171], v[188:191], v[72:75]\n\t" "ds_read_b64_tr_b16 v[160:161], v8 offset:0\n\t" "v_cvt_pk_bf16_f32 v197, v206, v207\n\t"
    "v_mfma_f32_16x16x32_bf16 v[76:79], v[168:171], v[192:195], v[76:79]\n\t" "ds_read_b64_tr_b16 v[162:163], v8 offset:512\n\t" "s_waitcnt lgkmcnt(4)\n\t"
    "v_mfma_f32_16x16x32_bf16 v[80:83], v[172:175], v[188:191], v[80:83]\n\t" "ds_read_b64_tr_b16 v[164:165], v9 offset:0\n\t" "v_cvt_pk_bf16_f32 v198, v208, v209\n\t"
    "v_mfma_f32_16x16x32_bf16 v[84:87], v[172:175], v[192:195], v[84:87]\n\t" "ds_read_b64_tr_b16 v[166:167], v9 offset:512\n\t" "v_cvt_pk_bf16_f32 v199, v210, v211\n\t"
    "v_mfma_f32_16x16x32_bf16 v[144:147], v[172:175], v[228:231], v[144:147]\n\t" "ds_read_b128 v[220:223], v2 offset:6144\n\t" "global_load_lds_dwordx4 v0, %[sb] offset:0\n\t"
    "v_mfma_f32_16x16x32_bf16 v[88:91], v[176:179], v[188:191], v[88:91]\n\t" "ds_read_b64_tr_b16 v[168:169], v10 offset:0\n\t" "v_cvt_pk_bf16_f32 v200, v212, v213\n\t"
    "v_mfma_f32_16x16x32_bf16 v[92:95], v[176:179], v[192:195], v[92:95]\n\t" "ds_read_b64_tr_b16 v[170:171], v10 offset:512\n\t" "v_cvt_pk_bf16_f32 v201, v214, v215\n\t"
    "v_mfma_f32_16x16x32_bf16 v[96:99], v[180:183], v[188:191], v[96:99]\n\t" "ds_read_b64_tr_b16 v[172:173], v3 offset:0\n\t" "v_mul_f32 v31, v241, v27\n\t"
    "v_mfma_f32_16x16x32_bf16 v[100:103], v[180:183], v[192:195], v[100:103]\n\t" "ds_read_b64_tr_b16 v[174:175], v3 offset:512\n\t" "v_cvt_pk_bf16_f32 v202, v216, v217\n\t"
    "v_mfma_f32_16x16x32_bf16 v[104:107], v[184:187], v[188:191], v[104:107]\n\t" "ds_read_b64_tr_b16 v[176:177], v4 offset:0\n\t" "v_cvt_pk_bf16_f32 v203, v218, v219\n\t"
    "v_mfma_f32_16x16x32_bf16 v[108:111], v[184:187], v[192:195], v[108:111]\n\t" "ds_read_b64_tr_b16 v[178:179], v4 offset:512\n\t" "s_waitcnt lgkmcnt(4)\n\t"
    "v_mfma_f32_16x16x32_bf16 v[112:115], v[188:191], v[156:159], v[112:115]\n\t" "ds_read_b64_tr_b16 v[180:181], v5 offset:0\n\t" "v_mov_b32 v39, 0xceabfb70\n\t"
    "v_mfma_f32_16x16x32_bf16 v[116:119], v[192:195], v[156:159], v[116:119]\n\t" "ds_read_b64_tr_b16 v[182:183], v5 offset:512\n\t" "v_cndmask_b32 v28, v28, 0, s[56:57]\n\t"
    "v_mfma_f32_16x16x32_bf16 v[148:151], v[228:231], v[156:159], v[148:151]\n\t" "ds_read_b128 v[232:235], v1 offset:23552\n\t" "global_load_lds_dwordx4 v0, %[sb] offset:2048\n\t"
    "v_mfma_f32_16x16x32_bf16 v[120:123], v[188:191], v[160:163], v[120:123]\n\t" "ds_read_b64_tr_b16 v[184:185], v6 offset:0\n\t" "v_add_u32 v0, 0x30000, v0\n\t"
    "v_mfma_f32_16x16x32_bf16 v[124:127], v[192:195], v[160:163], v[124:127]\n\t" "ds_read_b64_tr_b16 v[186:187], v6 offset:512\n\t" "v_add_u32 v16, 0x70000, v16\n\t"
    "v_mfma_f32_16x16x32_bf16 v[128:131], v[188:191], v[164:167], v[128:131]\n\t" "ds_read_b64_tr_b16 v[156:157], v3 offset:2048\n\t" "v_cndmask_b32 v29, v29, 0, s[58:59]\n\t"
    "v_mfma_f32_16x16x32_bf16 v[132:135], v[192:195], v[164:167], v[132:135]\n\t" "ds_read_b64_tr_b16 v[158:159], v3 offset:2560\n\t" "s_mov_b64 exec, %[tm]\n\t" "global_load_dword a64, v16, %[pf]\n\t"
    "s_mov_b64 exec, -1\n\t" "v_mfma_f32_16x16x32_bf16 v[136:139], v[188:191], v[168:171], v[136:139]\n\t" "ds_read_b64_tr_b16 v[160:161], v4 offset:2048\n\t" "v_cndmask_b32 v32, 0, v39, s[56:57]\n\t"
    "v_mfma_f32_16x16x32_bf16 v[140:143], v[192:195], v[168:171], v[140:143]\n\t" "ds_read_b64_tr_b16 v[162:163], v4 offset:2560\n\t" "s_waitcnt lgkmcnt(4)\n\t"
    "v_mfma_f32_16x16x32_bf16 v[48:51], v[172:175], v[196:199], v[48:51]\n\t" "ds_read_b64_tr_b16 v[164:165], v5 offset:2048\n\t" "s_add_u32 m0, %[sl], 120832\n\t"
    "v_mfma_f32_16x16x32_bf16 v[52:55], v[172:175], v[200:203], v[52:55]\n\t" "ds_read_b64_tr_b16 v[166:167], v5 offset:2560\n\t" "v_cndmask_b32 v33, 0, v39, s[58:59]\n\t"
    "v_mfma_f32_16x16x32_bf16 v[152:155], v[232:235], v[172:175], v[152:155]\n\t" "global_load_lds_dwordx4 v0, %[sb] offset:-2048\n\t" "v_lshl_add_u32 v43, v46, 5, %[lh]\n\t"
    "v_mfma_f32_16x16x32_bf16 v[56:59], v[176:179], v[196:199], v[56:59]\n\t" "ds_read_b64_tr_b16 v[168:169], v6 offset:2048\n\t" "v_lshl_add_u32 v36, v47, 3, %[lt]\n\t"
    "v_mfma_f32_4x4x4_16b_bf16 v[204:207], v[220:221], a[192:193], 0\n\t" "v_mfma_f32_4x4x4_16b_bf16 v[208:211], v[222:223], a[194:195], 0\n\t"
    "v_mfma_f32_4x4x4_16b_bf16 v[212:215], v[220:221], a[196:197], 0\n\t" "v_mfma_f32_4x4x4_16b_bf16 v[216:219], v[222:223], a[198:199], 0\n\t" "v_lshl_add_u32 v35, v46, 3, %[lh]\n\t"
    "v_mfma_f32_16x16x32_bf16 v[60:63], v[176:179], v[200:203], v[60:63]\n\t" "ds_read_b64_tr_b16 v[170:171], v6 offset:2560\n\t" "v_add_u32 v35, 0x6800, v35\n\t"
    "v_mfma_f32_16x16x32_bf16 v[64:67], v[180:183], v[196:199], v[64:67]\n\t" "ds_read_b64_tr_b16 v[172:173], v3 offset:4096\n\t" "v_cndmask_b32 v35, v35, v36, s[46:47]\n\t"
    "v_mfma_f32_16x16x32_bf16 v[68:71], v[180:183], v[200:203], v[68:71]\n\t" "ds_read_b64_tr_b16 v[174:175], v3 offset:4608\n\t" "v_cvt_pk_bf16_f32 v188, v204, v205\n\t"
    "v_mfma_f32_16x16x32_bf16 v[72:75], v[184:187], v[196:199], v[72:75]\n\t" "ds_read_b64_tr_b16 v[176:177], v4 offset:4096\n\t" "v_cvt_pk_bf16_f32 v189, v206, v207\n\t"
    "v_mfma_f32_16x16x32_bf16 v[76:79], v[184:187], v[200:203], v[76:79]\n\t" "ds_read_b64_tr_b16 v[178:179], v4 offset:4608\n\t" "s_waitcnt lgkmcnt(4)\n\t"
    "v_mfma_f32_16x16x32_bf16 v[80:83], v[156:159], v[196:199], v[80:83]\n\t" "ds_read_b64_tr_b16 v[180:181], v5 offset:4096\n\t" "v_cvt_pk_bf16_f32 v190, v208, v209\n\t"
    "v_mfma_f32_16x16x32_bf16 v[84:87], v[156:159], v[200:203], v[84:87]\n\t" "ds_read_b64_tr_b16 v[182:183], v5 offset:4608\n\t" "v_cvt_pk_bf16_f32 v191, v210, v211\n\t"
    "v_mfma_f32_16x16x32_bf16 v[144:147], v[156:159], v[232:235], v[144:147]\n\t" "ds_read_b128 v[224:227], v2 offset:6400\n\t" "global_load_lds_dwordx4 v0, %[sb] offset:0\n\t"
    "v_mfma_f32_16x16x32_bf16 v[88:91], v[160:163], v[196:199], v[88:91]\n\t" "ds_read_b64_tr_b16 v[184:185], v6 offset:4096\n\t" "v_cvt_pk_bf16_f32 v192, v212, v213\n\t"
    "v_mfma_f32_16x16x32_bf16 v[92:95], v[160:163], v[200:203], v[92:95]\n\t" "ds_read_b64_tr_b16 v[186:187], v6 offset:4608\n\t" "v_cvt_pk_bf16_f32 v193, v214, v215\n\t"
    "v_mfma_f32_16x16x32_bf16 v[96:99], v[164:167], v[196:199], v[96:99]\n\t" "s_waitcnt vmcnt(15)\n\t" "s_barrier\n\t" "ds_read_b64_tr_b16 v[156:157], v3 offset:12288\n\t"
    "v_mfma_f32_16x16x32_bf16 v[100:103], v[164:167], v[200:203], v[100:103]\n\t" "ds_read_b64_tr_b16 v[158:159], v3 offset:12800\n\t" "v_cvt_pk_bf16_f32 v194, v216, v217\n\t"
    "v_mfma_f32_16x16x32_bf16 v[104:107], v[168:171], v[196:199], v[104:107]\n\t" "ds_read_b64_tr_b16 v[160:161], v4 offset:12288\n\t" "v_cvt_pk_bf16_f32 v195, v218, v219\n\t"
    "v_mfma_f32_16x16x32_bf16 v[108:111], v[168:171], v[200:203], v[108:111]\n\t" "ds_read_b64_tr_b16 v[162:163], v4 offset:12800\n\t" "s_waitcnt lgkmcnt(4)\n\t"
    "v_mfma_f32_16x16x32_bf16 v[112:115], v[196:199], v[172:175], v[112:115]\n\t" "ds_read_b64_tr_b16 v[164:165], v5 offset:12288\n\t" "v_lshl_add_u32 v34, v45, 1, %[lt]\n\t"
    "v_mfma_f32_16x16x32_bf16 v[116:119], v[200:203], v[172:175], v[116:119]\n\t" "ds_read_b64_tr_b16 v[166:167], v5 offset:12800\n\t" "v_lshl_add_u32 v34, v47, 2, v34\n\t"
    "v_mfma_f32_16x16x32_bf16 v[148:151], v[232:235], v[172:175], v[148:151]\n\t" "ds_read_b128 v[228:231], v1 offset:24576\n\t" "global_load_lds_dwordx4 v0, %[sb] offset:2048\n\t"
    "v_mfma_f32_16x16x32_bf16 v[120:123], v[196:199], v[176:179], v[120:123]\n\t" "ds_read_b64_tr_b16 v[168:169], v6 offset:12288\n\t" "v_add_u32 v0, 0x30000, v0\n\t"
    "v_mfma_f32_16x16x32_bf16 v[124:127], v[200:203], v[176:179], v[124:127]\n\t" "ds_read_b64_tr_b16 v[170:171], v6 offset:12800\n\t" "v_add_u32 v12, 0x10000, v12\n\t"
    "v_mfma_f32_16x16x32_bf16 v[128:131], v[196:199], v[180:183], v[128:131]\n\t" "ds_read_b64_tr_b16 v[172:173], v3 offset:14336\n\t" "s_add_u32 m0, %[sl1], 12288\n\t"
    "v_mfma_f32_16x16x32_bf16 v[132:135], v[200:203], v[180:183], v[132:135]\n\t" "ds_read_b64_tr_b16 v[174:175], v3 offset:14848\n\t" "global_load_lds_dwordx4 v12, %[so]\n\t"
    "v_mfma_f32_16x16x32_bf16 v[136:139], v[196:199], v[184:187], v[136:139]\n\t" "ds_read_b64_tr_b16 v[176:177], v4 offset:14336\n\t" "v_add_u32 v34, 0x200, v34\n\t"
    "v_mfma_f32_16x16x32_bf16 v[140:143], v[200:203], v[184:187], v[140:143]\n\t" "ds_read_b64_tr_b16 v[178:179], v4 offset:14848\n\t" "s_waitcnt lgkmcnt(4)\n\t"
    "v_mfma_f32_16x16x32_bf16 v[48:51], v[156:159], v[188:191], v[48:51]\n\t" "ds_read_b64_tr_b16 v[180:181], v5 offset:14336\n\t" "s_add_u32 m0, %[sl], 71680\n\t"
    "v_mfma_f32_16x16x32_bf16 v[52:55], v[156:159], v[192:195], v[52:55]\n\t" "ds_read_b64_tr_b16 v[182:183], v5 offset:14848\n\t" "v_add_u32 v37, 0x6800, v43\n\t"
    "v_mfma_f32_16x16x32_bf16 v[152:155], v[228:231], v[156:159], v[152:155]\n\t" "global_load_lds_dwordx4 v0, %[sb] offset:-2048\n\t" "v_cndmask_b32 v34, v37, v34, s[48:49]\n\t"
    "v_mfma_f32_16x16x32_bf16 v[56:59], v[160:163], v[188:191], v[56:59]\n\t" "ds_read_b64_tr_b16 v[184:185], v6 offset:14336\n\t" "v_xor_b32 v37, 0x80, v36\n\t"
    "v_mfma_f32_4x4x4_16b_bf16 v[204:207], v[224:225], a[200:201], 0\n\t" "v_mfma_f32_4x4x4_16b_bf16 v[208:211], v[226:227], a[202:203], 0\n\t"
    "v_mfma_f32_4x4x4_16b_bf16 v[212:215], v[224:225], a[204:205], 0\n\t" "v_mfma_f32_4x4x4_16b_bf16 v[216:219], v[226:227], a[206:207], 0\n\t" "v_xor_b32 v38, 0x100, v36\n\t"
    "v_mfma_f32_16x16x32_bf16 v[60:63], v[160:163], v[192:195], v[60:63]\n\t" "ds_read_b64_tr_b16 v[186:187], v6 offset:14848\n\t" "v_xor_b32 v39, 0x180, v36\n\t"
    "v_mfma_f32_16x16x32_bf16 v[64:67], v[164:167], v[188:191], v[64:67]\n\t" "ds_read_b64_tr_b16 v[156:157], v3 offset:16384\n\t" "v_add_u32 v40, %[xw], v43\n\t"
    "v_mfma_f32_16x16x32_bf16 v[68:71], v[164:167], v[192:195], v[68:71]\n\t" "ds_read_b64_tr_b16 v[158:159], v3 offset:16896\n\t" "v_cvt_pk_bf16_f32 v196, v204, v205\n\t"
    "v_mfma_f32_16x16x32_bf16 v[72:75], v[168:171], v[188:191], v[72:75]\n\t" "ds_read_b64_tr_b16 v[160:161], v4 offset:16384\n\t" "v_cvt_pk_bf16_f32 v197, v206, v207\n\t"
    "v_mfma_f32_16x16x32_bf16 v[76:79], v[168:171], v[192:195], v[76:79]\n\t" "ds_read_b64_tr_b16 v[162:163], v4 offset:16896\n\t" "s_waitcnt lgkmcnt(4)\n\t"
    "v_mfma_f32_16x16x32_bf16 v[80:83], v[172:175], v[188:191], v[80:83]\n\t" "ds_read_b64_tr_b16 v[164:165], v5 offset:16384\n\t" "v_cvt_pk_bf16_f32 v198, v208, v209\n\t"
    "v_mfma_f32_16x16x32_bf16 v[84:87], v[172:175], v[192:195], v[84:87]\n\t" "ds_read_b64_tr_b16 v[166:167], v5 offset:16896\n\t" "v_cvt_pk_bf16_f32 v199, v210, v211\n\t"
    "v_mfma_f32_16x16x32_bf16 v[144:147], v[172:175], v[228:231], v[144:147]\n\t" "ds_read_b128 v[220:223], v2 offset:6656\n\t" "global_load_lds_dwordx4 v0, %[sb] offset:0\n\t"
    "v_mfma_f32_16x16x32_bf16 v[88:91], v[176:179], v[188:191], v[88:91]\n\t" "ds_read_b64_tr_b16 v[168:169], v6 offset:16384\n\t" "v_cvt_pk_bf16_f32 v200, v212, v213\n\t"
    "v_mfma_f32_16x16x32_bf16 v[92:95], v[176:179], v[192:195], v[92:95]\n\t" "ds_read_b64_tr_b16 v[170:171], v6 offset:16896\n\t" "v_cvt_pk_bf16_f32 v201, v214, v215\n\t"
    "v_mfma_f32_16x16x32_bf16 v[96:99], v[180:183], v[188:191], v[96:99]\n\t" "ds_read_b64_tr_b16 v[172:173], v3 offset:24576\n\t" "v_xor_b32 v41, 0x800, v40\n\t"
    "v_mfma_f32_16x16x32_bf16 v[100:103], v[180:183], v[192:195], v[100:103]\n\t" "ds_read_b64_tr_b16 v[174:175], v3 offset:25088\n\t" "v_cvt_pk_bf16_f32 v202, v216, v217\n\t"
    "v_mfma_f32_16x16x32_bf16 v[104:107], v[184:187], v[188:191], v[104:107]\n\t" "ds_read_b64_tr_b16 v[176:177], v4 offset:24576\n\t" "v_cvt_pk_bf16_f32 v203, v218, v219\n\t"
    "v_mfma_f32_16x16x32_bf16 v[108:111], v[184:187], v[192:195], v[108:111]\n\t" "ds_read_b64_tr_b16 v[178:179], v4 offset:25088\n\t" "s_waitcnt lgkmcnt(4)\n\t"
    "v_mfma_f32_16x16x32_bf16 v[112:115], v[188:191], v[156:159], v[112:115]\n\t" "ds_read_b64_tr_b16 v[180:181], v5 offset:24576\n\t" "v_add_u32_e64 v42, %[xh], v43\n\t"
    "v_mfma_f32_16x16x32_bf16 v[116:119], v[192:195], v[156:159], v[116:119]\n\t" "ds_read_b64_tr_b16 v[182:183], v5 offset:25088\n\t" "v_add_u32_e64 v43, %[w4k], v43\n\t"
    "v_mfma_f32_16x16x32_bf16 v[148:151], v[228:231], v[156:159], v[148:151]\n\t" "ds_read_b128 v[232:235], v1 offset:25600\n\t" "global_load_lds_dwordx4 v0, %[sb] offset:2048\n\t"
    "v_mfma_f32_16x16x32_bf16 v[120:123], v[188:191], v[160:163], v[120:123]\n\t" "ds_read_b64_tr_b16 v[184:185], v6 offset:24576\n\t" "v_add_u32 v0, 0x30000, v0\n\t"
    "v_mfma_f32_16x16x32_bf16 v[124:127], v[192:195], v[160:163], v[124:127]\n\t" "ds_read_b64_tr_b16 v[186:187], v6 offset:25088\n\t" "v_lshl_add_u32 v44, v46, 4, %[ox]\n\t"
    "v_mfma_f32_16x16x32_bf16 v[128:131], v[188:191], v[164:167], v[128:131]\n\t" "ds_read_b64_tr_b16 v[156:157], v3 offset:26624\n\t" "v_xor_b32 v45, 0x800, v44\n\t"
    "v_mfma_f32_16x16x32_bf16 v[132:135], v[192:195], v[164:167], v[132:135]\n\t" "ds_read_b64_tr_b16 v[158:159], v3 offset:27136\n\t"
    "v_mfma_f32_16x16x32_bf16 v[136:139], v[188:191], v[168:171], v[136:139]\n\t" "ds_read_b64_tr_b16 v[160:161], v4 offset:26624\n\t"
    "v_mfma_f32_16x16x32_bf16 v[140:143], v[192:195], v[168:171], v[140:143]\n\t" "ds_read_b64_tr_b16 v[162:163], v4 offset:27136\n\t" "s_waitcnt lgkmcnt(4)\n\t"
    "v_mfma_f32_16x16x32_bf16 v[48:51], v[172:175], v[196:199], v[48:51]\n\t" "ds_read_b64_tr_b16 v[164:165], v5 offset:26624\n\t" "s_add_u32 m0, %[sl], 83968\n\t"
    "v_mfma_f32_16x16x32_bf16 v[52:55], v[172:175], v[200:203], v[52:55]\n\t" "ds_read_b64_tr_b16 v[166:167], v5 offset:27136\n\t"
    "v_mfma_f32_16x16x32_bf16 v[152:155], v[232:235], v[172:175], v[152:155]\n\t" "global_load_lds_dwordx4 v0, %[sb] offset:-2048\n\t"
    "v_mfma_f32_16x16x32_bf16 v[56:59], v[176:179], v[196:199], v[56:59]\n\t" "ds_read_b64_tr_b16 v[168:169], v6 offset:26624\n\t" "v_mfma_f32_4x4x4_16b_bf16 v[204:207], v[220:221], a[208:209], 0\n\t"
    "v_mfma_f32_4x4x4_16b_bf16 v[208:211], v[222:223], a[210:211], 0\n\t" "v_mfma_f32_4x4x4_16b_bf16 v[212:215], v[220:221], a[212:213], 0\n\t"
    "v_mfma_f32_4x4x4_16b_bf16 v[216:219], v[222:223], a[214:215], 0\n\t" "v_mfma_f32_16x16x32_bf16 v[60:63], v[176:179], v[200:203], v[60:63]\n\t" "ds_read_b64_tr_b16 v[170:171], v6 offset:27136\n\t"
    "v_mfma_f32_16x16x32_bf16 v[64:67], v[180:183], v[196:199], v[64:67]\n\t" "ds_read_b64_tr_b16 v[172:173], v3 offset:28672\n\t" "v_mfma_f32_16x16x32_bf16 v[68:71], v[180:183], v[200:203], v[68:71]\n\t"
    "ds_read_b64_tr_b16 v[174:175], v3 offset:29184\n\t" "v_cvt_pk_bf16_f32 v188, v204, v205\n\t" "v_mfma_f32_16x16x32_bf16 v[72:75], v[184:187], v[196:199], v[72:75]\n\t"
    "ds_read_b64_tr_b16 v[176:177], v4 offset:28672\n\t" "v_cvt_pk_bf16_f32 v189, v206, v207\n\t" "v_mfma_f32_16x16x32_bf16 v[76:79], v[184:187], v[200:203], v[76:79]\n\t"
    "ds_read_b64_tr_b16 v[178:179], v4 offset:29184\n\t" "s_waitcnt lgkmcnt(4)\n\t" "v_mfma_f32_16x16x32_bf16 v[80:83], v[156:159], v[196:199], v[80:83]\n\t"
    "ds_read_b64_tr_b16 v[180:181], v5 offset:28672\n\t" "v_cvt_pk_bf16_f32 v190, v208, v209\n\t" "v_mfma_f32_16x16x32_bf16 v[84:87], v[156:159], v[200:203], v[84:87]\n\t"
    "ds_read_b64_tr_b16 v[182:183], v5 offset:29184\n\t" "v_cvt_pk_bf16_f32 v191, v210, v211\n\t" "v_mfma_f32_16x16x32_bf16 v[144:147], v[156:159], v[232:235], v[144:147]\n\t"
    "ds_read_b128 v[224:227], v2 offset:6912\n\t" "global_load_lds_dwordx4 v0, %[sb] offset:0\n\t" "v_mfma_f32_16x16x32_bf16 v[88:91], v[160:163], v[196:199], v[88:91]\n\t"
    "ds_read_b64_tr_b16 v[184:185], v6 offset:28672\n\t" "v_cvt_pk_bf16_f32 v192, v212, v213\n\t" "v_mfma_f32_16x16x32_bf16 v[92:95], v[160:163], v[200:203], v[92:95]\n\t"
    "ds_read_b64_tr_b16 v[186:187], v6 offset:29184\n\t" "v_cvt_pk_bf16_f32 v193, v214, v215\n\t" "v_mfma_f32_16x16x32_bf16 v[96:99], v[164:167], v[196:199], v[96:99]\n\t" "s_waitcnt vmcnt(14)\n\t"
    "s_barrier\n\t" "ds_read_b64_tr_b16 v[156:157], v3 offset:36864\n\t" "v_mfma_f32_16x16x32_bf16 v[100:103], v[164:167], v[200:203], v[100:103]\n\t" "ds_read_b64_tr_b16 v[158:159], v3 offset:37376\n\t"
    "v_cvt_pk_bf16_f32 v194, v216, v217\n\t" "v_mfma_f32_16x16x32_bf16 v[104:107], v[168:171], v[196:199], v[104:107]\n\t" "ds_read_b64_tr_b16 v[160:161], v4 offset:36864\n\t"
    "v_cvt_pk_bf16_f32 v195, v218, v219\n\t" "v_mfma_f32_16x16x32_bf16 v[108:111], v[168:171], v[200:203], v[108:111]\n\t" "ds_read_b64_tr_b16 v[162:163], v4 offset:37376\n\t" "s_waitcnt lgkmcnt(4)\n\t"
    "v_mfma_f32_16x16x32_bf16 v[112:115], v[196:199], v[172:175], v[112:115]\n\t" "ds_read_b64_tr_b16 v[164:165], v5 offset:36864\n\t"
    "v_mfma_f32_16x16x32_bf16 v[116:119], v[200:203], v[172:175], v[116:119]\n\t" "ds_read_b64_tr_b16 v[166:167], v5 offset:37376\n\t"
    "v_mfma_f32_16x16x32_bf16 v[148:151], v[232:235], v[172:175], v[148:151]\n\t" "ds_read_b128 v[228:231], v1 offset:26624\n\t" "global_load_lds_dwordx4 v0, %[sb] offset:2048\n\t"
    "v_mfma_f32_16x16x32_bf16 v[120:123], v[196:199], v[176:179], v[120:123]\n\t" "ds_read_b64_tr_b16 v[168:169], v6 offset:36864\n\t" "v_add_u32 v16, 0x70000, v16\n\t"
    "v_mfma_f32_16x16x32_bf16 v[124:127], v[200:203], v[176:179], v[124:127]\n\t" "ds_read_b64_tr_b16 v[170:171], v6 offset:37376\n\t"
    "v_mfma_f32_16x16x32_bf16 v[128:131], v[196:199], v[180:183], v[128:131]\n\t" "ds_read_b64_tr_b16 v[172:173], v3 offset:38912\n\t"
    "v_mfma_f32_16x16x32_bf16 v[132:135], v[200:203], v[180:183], v[132:135]\n\t" "ds_read_b64_tr_b16 v[174:175], v3 offset:39424\n\t" "s_mov_b64 exec, %[tm]\n\t" "global_load_dword a64, v16, %[pf]\n\t"
    "s_mov_b64 exec, -1\n\t" "v_mfma_f32_16x16x32_bf16 v[136:139], v[196:199], v[184:187], v[136:139]\n\t" "ds_read_b64_tr_b16 v[176:177], v4 offset:38912\n\t"
    "v_mfma_f32_16x16x32_bf16 v[140:143], v[200:203], v[184:187], v[140:143]\n\t" "ds_read_b64_tr_b16 v[178:179], v4 offset:39424\n\t" "s_waitcnt lgkmcnt(4)\n\t"
    "v_mfma_f32_16x16x32_bf16 v[48:51], v[156:159], v[188:191], v[48:51]\n\t" "ds_read_b64_tr_b16 v[180:181], v5 offset:38912\n\t" "v_add_u32 v12, 0x20000, v12\n\t"
    "v_mfma_f32_16x16x32_bf16 v[52:55], v[156:159], v[192:195], v[52:55]\n\t" "ds_read_b64_tr_b16 v[182:183], v5 offset:39424\n\t" "s_add_u32 m0, %[sl1], 20480\n\t"
    "v_mfma_f32_16x16x32_bf16 v[152:155], v[228:231], v[156:159], v[152:155]\n\t" "global_load_lds_dwordx4 v12, %[so]\n\t" "v_add_u32 v12, 0x10000, v12\n\t"
    "v_mfma_f32_16x16x32_bf16 v[56:59], v[160:163], v[188:191], v[56:59]\n\t" "ds_read_b64_tr_b16 v[184:185], v6 offset:38912\n\t" "v_mfma_f32_4x4x4_16b_bf16 v[204:207], v[224:225], a[216:217], 0\n\t"
    "v_mfma_f32_4x4x4_16b_bf16 v[208:211], v[226:227], a[218:219], 0\n\t" "v_mfma_f32_4x4x4_16b_bf16 v[212:215], v[224:225], a[220:221], 0\n\t"
    "v_mfma_f32_4x4x4_16b_bf16 v[216:219], v[226:227], a[222:223], 0\n\t" "v_mfma_f32_16x16x32_bf16 v[60:63], v[160:163], v[192:195], v[60:63]\n\t" "ds_read_b64_tr_b16 v[186:187], v6 offset:39424\n\t"
    "v_mfma_f32_16x16x32_bf16 v[64:67], v[164:167], v[188:191], v[64:67]\n\t" "ds_read_b64_tr_b16 v[156:157], v3 offset:40960\n\t" "v_mfma_f32_16x16x32_bf16 v[68:71], v[164:167], v[192:195], v[68:71]\n\t"
    "ds_read_b64_tr_b16 v[158:159], v3 offset:41472\n\t" "v_cvt_pk_bf16_f32 v196, v204, v205\n\t" "v_mfma_f32_16x16x32_bf16 v[72:75], v[168:171], v[188:191], v[72:75]\n\t"
    "ds_read_b64_tr_b16 v[160:161], v4 offset:40960\n\t" "v_cvt_pk_bf16_f32 v197, v206, v207\n\t" "v_mfma_f32_16x16x32_bf16 v[76:79], v[168:171], v[192:195], v[76:79]\n\t"
    "ds_read_b64_tr_b16 v[162:163], v4 offset:41472\n\t" "s_waitcnt lgkmcnt(4)\n\t" "v_mfma_f32_16x16x32_bf16 v[80:83], v[172:175], v[188:191], v[80:83]\n\t"
    "ds_read_b64_tr_b16 v[164:165], v5 offset:40960\n\t" "v_cvt_pk_bf16_f32 v198, v208, v209\n\t" "v_mfma_f32_16x16x32_bf16 v[84:87], v[172:175], v[192:195], v[84:87]\n\t"
    "ds_read_b64_tr_b16 v[166:167], v5 offset:41472\n\t" "v_cvt_pk_bf16_f32 v199, v210, v211\n\t" "v_mfma_f32_16x16x32_bf16 v[144:147], v[172:175], v[228:231], v[144:147]\n\t"
    "ds_read_b128 v[220:223], v2 offset:7168\n\t" "s_add_u32 m0, %[sl1], 24576\n\t" "v_mfma_f32_16x16x32_bf16 v[88:91], v[176:179], v[188:191], v[88:91]\n\t"
    "ds_read_b64_tr_b16 v[168:169], v6 offset:40960\n\t" "v_cvt_pk_bf16_f32 v200, v212, v213\n\t" "v_mfma_f32_16x16x32_bf16 v[92:95], v[176:179], v[192:195], v[92:95]\n\t"
    "ds_read_b64_tr_b16 v[170:171], v6 offset:41472\n\t" "v_cvt_pk_bf16_f32 v201, v214, v215\n\t" "v_mfma_f32_16x16x32_bf16 v[96:99], v[180:183], v[188:191], v[96:99]\n\t"
    "ds_read_b64_tr_b16 v[172:173], v7 offset:8192\n\t" "global_load_lds_dwordx4 v12, %[so]\n\t" "v_mfma_f32_16x16x32_bf16 v[100:103], v[180:183], v[192:195], v[100:103]\n\t"
    "ds_read_b64_tr_b16 v[174:175], v7 offset:8704\n\t" "v_cvt_pk_bf16_f32 v202, v216, v217\n\t" "v_mfma_f32_16x16x32_bf16 v[104:107], v[184:187], v[188:191], v[104:107]\n\t"
    "ds_read_b64_tr_b16 v[176:177], v8 offset:8192\n\t" "v_cvt_pk_bf16_f32 v203, v218, v219\n\t" "v_mfma_f32_16x16x32_bf16 v[108:111], v[184:187], v[192:195], v[108:111]\n\t"
    "ds_read_b64_tr_b16 v[178:179], v8 offset:8704\n\t" "s_waitcnt lgkmcnt(4)\n\t" "v_mfma_f32_16x16x32_bf16 v[112:115], v[188:191], v[156:159], v[112:115]\n\t"
    "ds_read_b64_tr_b16 v[180:181], v9 offset:8192\n\t" "v_add_u32 v12, 0x10000, v12\n\t" "v_mfma_f32_16x16x32_bf16 v[116:119], v[192:195], v[156:159], v[116:119]\n\t"
    "ds_read_b64_tr_b16 v[182:183], v9 offset:8704\n\t" "s_add_u32 m0, %[sl1], 28672\n\t" "v_mfma_f32_16x16x32_bf16 v[148:151], v[228:231], v[156:159], v[148:151]\n\t"
    "ds_read_b128 v[232:235], v1 offset:27648\n\t" "global_load_lds_dwordx4 v12, %[so]\n\t" "v_mfma_f32_16x16x32_bf16 v[120:123], v[188:191], v[160:163], v[120:123]\n\t"
    "ds_read_b64_tr_b16 v[184:185], v10 offset:8192\n\t" "v_mfma_f32_16x16x32_bf16 v[124:127], v[192:195], v[160:163], v[124:127]\n\t" "ds_read_b64_tr_b16 v[186:187], v10 offset:8704\n\t"
    "v_mfma_f32_16x16x32_bf16 v[128:131], v[188:191], v[164:167], v[128:131]\n\t" "ds_read_b64_tr_b16 v[156:157], v7 offset:10240\n\t"
    "v_mfma_f32_16x16x32_bf16 v[132:135], v[192:195], v[164:167], v[132:135]\n\t" "ds_read_b64_tr_b16 v[158:159], v7 offset:10752\n\t"
    "v_mfma_f32_16x16x32_bf16 v[136:139], v[188:191], v[168:171], v[136:139]\n\t" "ds_read_b64_tr_b16 v[160:161], v8 offset:10240\n\t"
    "v_mfma_f32_16x16x32_bf16 v[140:143], v[192:195], v[168:171], v[140:143]\n\t" "ds_read_b64_tr_b16 v[162:163], v8 offset:10752\n\t" "s_waitcnt lgkmcnt(4)\n\t"
    "v_mfma_f32_16x16x32_bf16 v[48:51], v[172:175], v[196:199], v[48:51]\n\t" "ds_read_b64_tr_b16 v[164:165], v9 offset:10240\n\t" "v_add_u32 v12, 0x10000, v12\n\t"
    "v_mfma_f32_16x16x32_bf16 v[52:55], v[172:175], v[200:203], v[52:55]\n\t" "ds_read_b64_tr_b16 v[166:167], v9 offset:10752\n\t" "s_add_u32 m0, %[sl1], 32768\n\t"
    "v_mfma_f32_16x16x32_bf16 v[152:155], v[232:235], v[172:175], v[152:155]\n\t" "global_load_lds_dwordx4 v12, %[so]\n\t" "v_add_u32 v12, 0x10000, v12\n\t"
    "v_mfma_f32_16x16x32_bf16 v[56:59], v[176:179], v[196:199], v[56:59]\n\t" "ds_read_b64_tr_b16 v[168:169], v10 offset:10240\n\t" "v_mfma_f32_4x4x4_16b_bf16 v[204:207], v[220:221], a[224:225], 0\n\t"
    "v_mfma_f32_4x4x4_16b_bf16 v[208:211], v[222:223], a[226:227], 0\n\t" "v_mfma_f32_4x4x4_16b_bf16 v[212:215], v[220:221], a[228:229], 0\n\t"
    "v_mfma_f32_4x4x4_16b_bf16 v[216:219], v[222:223], a[230:231], 0\n\t" "v_mfma_f32_16x16x32_bf16 v[60:63], v[176:179], v[200:203], v[60:63]\n\t" "ds_read_b64_tr_b16 v[170:171], v10 offset:10752\n\t"
    "v_mfma_f32_16x16x32_bf16 v[64:67], v[180:183], v[196:199], v[64:67]\n\t" "ds_read_b64_tr_b16 v[172:173], v7 offset:12288\n\t" "v_mfma_f32_16x16x32_bf16 v[68:71], v[180:183], v[200:203], v[68:71]\n\t"
    "ds_read_b64_tr_b16 v[174:175], v7 offset:12800\n\t" "v_cvt_pk_bf16_f32 v188, v204, v205\n\t" "v_mfma_f32_16x16x32_bf16 v[72:75], v[184:187], v[196:199], v[72:75]\n\t"
    "ds_read_b64_tr_b16 v[176:177], v8 offset:12288\n\t" "v_cvt_pk_bf16_f32 v189, v206, v207\n\t" "v_mfma_f32_16x16x32_bf16 v[76:79], v[184:187], v[200:203], v[76:79]\n\t"
    "ds_read_b64_tr_b16 v[178:179], v8 offset:12800\n\t" "s_waitcnt lgkmcnt(4)\n\t" "v_mfma_f32_16x16x32_bf16 v[80:83], v[156:159], v[196:199], v[80:83]\n\t"
    "ds_read_b64_tr_b16 v[180:181], v9 offset:12288\n\t" "v_cvt_pk_bf16_f32 v190, v208, v209\n\t" "v_mfma_f32_16x16x32_bf16 v[84:87], v[156:159], v[200:203], v[84:87]\n\t"
    "ds_read_b64_tr_b16 v[182:183], v9 offset:12800\n\t" "v_cvt_pk_bf16_f32 v191, v210, v211\n\t" "v_mfma_f32_16x16x32_bf16 v[144:147], v[156:159], v[232:235], v[144:147]\n\t"
    "ds_read_b128 v[224:227], v2 offset:7424\n\t" "s_add_u32 m0, %[sl1], 36864\n\t" "v_mfma_f32_16x16x32_bf16 v[88:91], v[160:163], v[196:199], v[88:91]\n\t"
    "ds_read_b64_tr_b16 v[184:185], v10 offset:12288\n\t" "v_cvt_pk_bf16_f32 v192, v212, v213\n\t" "v_mfma_f32_16x16x32_bf16 v[92:95], v[160:163], v[200:203], v[92:95]\n\t"
    "ds_read_b64_tr_b16 v[186:187], v10 offset:12800\n\t" "v_cvt_pk_bf16_f32 v193, v214, v215\n\t" "v_mfma_f32_16x16x32_bf16 v[96:99], v[164:167], v[196:199], v[96:99]\n\t" "s_waitcnt vmcnt(12)\n\t"
    "s_barrier\n\t" "ds_read_b64_tr_b16 v[156:157], v7 offset:20480\n\t" "v_mfma_f32_16x16x32_bf16 v[100:103], v[164:167], v[200:203], v[100:103]\n\t" "ds_read_b64_tr_b16 v[158:159], v7 offset:20992\n\t"
    "v_cvt_pk_bf16_f32 v194, v216, v217\n\t" "v_mfma_f32_16x16x32_bf16 v[104:107], v[168:171], v[196:199], v[104:107]\n\t" "ds_read_b64_tr_b16 v[160:161], v8 offset:20480\n\t"
    "v_cvt_pk_bf16_f32 v195, v218, v219\n\t" "v_mfma_f32_16x16x32_bf16 v[108:111], v[168:171], v[200:203], v[108:111]\n\t" "ds_read_b64_tr_b16 v[162:163], v8 offset:20992\n\t" "s_waitcnt lgkmcnt(4)\n\t"
    "v_mfma_f32_16x16x32_bf16 v[112:115], v[196:199], v[172:175], v[112:115]\n\t" "ds_read_b64_tr_b16 v[164:165], v9 offset:20480\n\t" "global_load_lds_dwordx4 v12, %[so]\n\t"
    "v_mfma_f32_16x16x32_bf16 v[116:119], v[200:203], v[172:175], v[116:119]\n\t" "ds_read_b64_tr_b16 v[166:167], v9 offset:20992\n\t" "v_add_u32 v12, 0x10000, v12\n\t"
    "v_mfma_f32_16x16x32_bf16 v[148:151], v[232:235], v[172:175], v[148:151]\n\t" "ds_read_b128 v[228:231], v1 offset:28672\n\t" "s_add_u32 m0, %[sl1], 40960\n\t"
    "v_mfma_f32_16x16x32_bf16 v[120:123], v[196:199], v[176:179], v[120:123]\n\t" "ds_read_b64_tr_b16 v[168:169], v10 offset:20480\n\t" "global_load_lds_dwordx4 v12, %[so]\n\t"
    "v_mfma_f32_16x16x32_bf16 v[124:127], v[200:203], v[176:179], v[124:127]\n\t" "ds_read_b64_tr_b16 v[170:171], v10 offset:20992\n\t"
    "v_mfma_f32_16x16x32_bf16 v[128:131], v[196:199], v[180:183], v[128:131]\n\t" "ds_read_b64_tr_b16 v[172:173], v7 offset:22528\n\t"
    "v_mfma_f32_16x16x32_bf16 v[132:135], v[200:203], v[180:183], v[132:135]\n\t" "ds_read_b64_tr_b16 v[174:175], v7 offset:23040\n\t"
    "v_mfma_f32_16x16x32_bf16 v[136:139], v[196:199], v[184:187], v[136:139]\n\t" "ds_read_b64_tr_b16 v[176:177], v8 offset:22528\n\t"
    "v_mfma_f32_16x16x32_bf16 v[140:143], v[200:203], v[184:187], v[140:143]\n\t" "ds_read_b64_tr_b16 v[178:179], v8 offset:23040\n\t" "s_waitcnt lgkmcnt(4)\n\t"
    "v_mfma_f32_16x16x32_bf16 v[48:51], v[156:159], v[188:191], v[48:51]\n\t" "ds_read_b64_tr_b16 v[180:181], v9 offset:22528\n\t" "v_add_u32 v12, 0xfffa0000, v12\n\t"
    "v_mfma_f32_16x16x32_bf16 v[52:55], v[156:159], v[192:195], v[52:55]\n\t" "ds_read_b64_tr_b16 v[182:183], v9 offset:23040\n\t" "s_add_u32 m0, %[sl1], 16384\n\t"
    "v_mfma_f32_16x16x32_bf16 v[152:155], v[228:231], v[156:159], v[152:155]\n\t" "global_load_lds_dwordx4 v12, %[so]\n\t" "v_add_u32 v12, 0x70000, v12\n\t"
    "v_mfma_f32_16x16x32_bf16 v[56:59], v[160:163], v[188:191], v[56:59]\n\t" "ds_read_b64_tr_b16 v[184:185], v10 offset:22528\n\t" "s_add_u32 m0, %[sl1], 45056\n\t"
    "v_mfma_f32_4x4x4_16b_bf16 v[204:207], v[224:225], a[232:233], 0\n\t" "v_mfma_f32_4x4x4_16b_bf16 v[208:211], v[226:227], a[234:235], 0\n\t"
    "v_mfma_f32_4x4x4_16b_bf16 v[212:215], v[224:225], a[236:237], 0\n\t" "v_mfma_f32_4x4x4_16b_bf16 v[216:219], v[226:227], a[238:239], 0\n\t"
    "v_mfma_f32_16x16x32_bf16 v[60:63], v[160:163], v[192:195], v[60:63]\n\t" "ds_read_b64_tr_b16 v[186:187], v10 offset:23040\n\t"
    "v_mfma_f32_16x16x32_bf16 v[64:67], v[164:167], v[188:191], v[64:67]\n\t" "ds_read_b64_tr_b16 v[156:157], v7 offset:24576\n\t" "global_load_lds_dwordx4 v12, %[so]\n\t"
    "v_mfma_f32_16x16x32_bf16 v[68:71], v[164:167], v[192:195], v[68:71]\n\t" "ds_read_b64_tr_b16 v[158:159], v7 offset:25088\n\t" "v_cvt_pk_bf16_f32 v196, v204, v205\n\t"
    "v_mfma_f32_16x16x32_bf16 v[72:75], v[168:171], v[188:191], v[72:75]\n\t" "ds_read_b64_tr_b16 v[160:161], v8 offset:24576\n\t" "v_cvt_pk_bf16_f32 v197, v206, v207\n\t"
    "v_mfma_f32_16x16x32_bf16 v[76:79], v[168:171], v[192:195], v[76:79]\n\t" "ds_read_b64_tr_b16 v[162:163], v8 offset:25088\n\t" "s_waitcnt lgkmcnt(4)\n\t"
    "v_mfma_f32_16x16x32_bf16 v[80:83], v[172:175], v[188:191], v[80:83]\n\t" "ds_read_b64_tr_b16 v[164:165], v9 offset:24576\n\t" "v_cvt_pk_bf16_f32 v198, v208, v209\n\t"
    "v_mfma_f32_16x16x32_bf16 v[84:87], v[172:175], v[192:195], v[84:87]\n\t" "ds_read_b64_tr_b16 v[166:167], v9 offset:25088\n\t" "v_cvt_pk_bf16_f32 v199, v210, v211\n\t"
    "v_mfma_f32_16x16x32_bf16 v[144:147], v[172:175], v[228:231], v[144:147]\n\t" "ds_read_b128 v[220:223], v2 offset:7680\n\t" "v_add_u32 v12, 0x10000, v12\n\t"
    "v_mfma_f32_16x16x32_bf16 v[88:91], v[176:179], v[188:191], v[88:91]\n\t" "ds_read_b64_tr_b16 v[168:169], v10 offset:24576\n\t" "v_cvt_pk_bf16_f32 v200, v212, v213\n\t"
    "v_mfma_f32_16x16x32_bf16 v[92:95], v[176:179], v[192:195], v[92:95]\n\t" "ds_read_b64_tr_b16 v[170:171], v10 offset:25088\n\t" "v_cvt_pk_bf16_f32 v201, v214, v215\n\t"
    "v_mfma_f32_16x16x32_bf16 v[96:99], v[180:183], v[188:191], v[96:99]\n\t" "ds_read_b64_tr_b16 v[172:173], v7 offset:32768\n\t" "s_add_u32 m0, %[sl1], 49152\n\t"
    "v_mfma_f32_16x16x32_bf16 v[100:103], v[180:183], v[192:195], v[100:103]\n\t" "ds_read_b64_tr_b16 v[174:175], v7 offset:33280\n\t" "v_cvt_pk_bf16_f32 v202, v216, v217\n\t"
    "v_mfma_f32_16x16x32_bf16 v[104:107], v[184:187], v[188:191], v[104:107]\n\t" "ds_read_b64_tr_b16 v[176:177], v8 offset:32768\n\t" "v_cvt_pk_bf16_f32 v203, v218, v219\n\t"
    "v_mfma_f32_16x16x32_bf16 v[108:111], v[184:187], v[192:195], v[108:111]\n\t" "ds_read_b64_tr_b16 v[178:179], v8 offset:33280\n\t" "s_waitcnt lgkmcnt(4)\n\t"
    "v_mfma_f32_16x16x32_bf16 v[112:115], v[188:191], v[156:159], v[112:115]\n\t" "ds_read_b64_tr_b16 v[180:181], v9 offset:32768\n\t" "global_load_lds_dwordx4 v12, %[so]\n\t"
    "v_mfma_f32_16x16x32_bf16 v[116:119], v[192:195], v[156:159], v[116:119]\n\t" "ds_read_b64_tr_b16 v[182:183], v9 offset:33280\n\t" "v_add_u32 v12, 0x10000, v12\n\t"
    "v_mfma_f32_16x16x32_bf16 v[148:151], v[228:231], v[156:159], v[148:151]\n\t" "ds_read_b128 v[232:235], v1 offset:29696\n\t" "s_add_u32 m0, %[sl1], 53248\n\t"
    "v_mfma_f32_16x16x32_bf16 v[120:123], v[188:191], v[160:163], v[120:123]\n\t" "ds_read_b64_tr_b16 v[184:185], v10 offset:32768\n\t" "global_load_lds_dwordx4 v12, %[so]\n\t"
    "v_mfma_f32_16x16x32_bf16 v[124:127], v[192:195], v[160:163], v[124:127]\n\t" "ds_read_b64_tr_b16 v[186:187], v10 offset:33280\n\t" "v_add_u32 v16, 0x70000, v16\n\t"
    "v_mfma_f32_16x16x32_bf16 v[128:131], v[188:191], v[164:167], v[128:131]\n\t" "ds_read_b64_tr_b16 v[156:157], v7 offset:34816\n\t"
    "v_mfma_f32_16x16x32_bf16 v[132:135], v[192:195], v[164:167], v[132:135]\n\t" "ds_read_b64_tr_b16 v[158:159], v7 offset:35328\n\t" "s_mov_b64 exec, %[tm]\n\t" "global_load_dword a64, v16, %[pf]\n\t"
    "s_mov_b64 exec, -1\n\t" "v_mfma_f32_16x16x32_bf16 v[136:139], v[188:191], v[168:171], v[136:139]\n\t" "ds_read_b64_tr_b16 v[160:161], v8 offset:34816\n\t"
    "v_mfma_f32_16x16x32_bf16 v[140:143], v[192:195], v[168:171], v[140:143]\n\t" "ds_read_b64_tr_b16 v[162:163], v8 offset:35328\n\t" "s_waitcnt lgkmcnt(4)\n\t"
    "v_mfma_f32_16x16x32_bf16 v[48:51], v[172:175], v[196:199], v[48:51]\n\t" "ds_read_b64_tr_b16 v[164:165], v9 offset:34816\n\t" "v_add_u32 v12, 0x10000, v12\n\t"
    "v_mfma_f32_16x16x32_bf16 v[52:55], v[172:175], v[200:203], v[52:55]\n\t" "ds_read_b64_tr_b16 v[166:167], v9 offset:35328\n\t" "s_add_u32 m0, %[sl1], 57344\n\t"
    "v_mfma_f32_16x16x32_bf16 v[152:155], v[232:235], v[172:175], v[152:155]\n\t" "global_load_lds_dwordx4 v12, %[so]\n\t" "v_add_u32 v12, 0x10000, v12\n\t"
    "v_mfma_f32_16x16x32_bf16 v[56:59], v[176:179], v[196:199], v[56:59]\n\t" "ds_read_b64_tr_b16 v[168:169], v10 offset:34816\n\t" "v_mfma_f32_4x4x4_16b_bf16 v[204:207], v[220:221], a[240:241], 0\n\t"
    "v_mfma_f32_4x4x4_16b_bf16 v[208:211], v[222:223], a[242:243], 0\n\t" "v_mfma_f32_4x4x4_16b_bf16 v[212:215], v[220:221], a[244:245], 0\n\t"
    "v_mfma_f32_4x4x4_16b_bf16 v[216:219], v[222:223], a[246:247], 0\n\t" "v_mfma_f32_16x16x32_bf16 v[60:63], v[176:179], v[200:203], v[60:63]\n\t" "ds_read_b64_tr_b16 v[170:171], v10 offset:35328\n\t"
    "v_mfma_f32_16x16x32_bf16 v[64:67], v[180:183], v[196:199], v[64:67]\n\t" "ds_read_b64_tr_b16 v[172:173], v7 offset:36864\n\t" "v_mfma_f32_16x16x32_bf16 v[68:71], v[180:183], v[200:203], v[68:71]\n\t"
    "ds_read_b64_tr_b16 v[174:175], v7 offset:37376\n\t" "v_cvt_pk_bf16_f32 v188, v204, v205\n\t" "v_mfma_f32_16x16x32_bf16 v[72:75], v[184:187], v[196:199], v[72:75]\n\t"
    "ds_read_b64_tr_b16 v[176:177], v8 offset:36864\n\t" "v_cvt_pk_bf16_f32 v189, v206, v207\n\t" "v_mfma_f32_16x16x32_bf16 v[76:79], v[184:187], v[200:203], v[76:79]\n\t"
    "ds_read_b64_tr_b16 v[178:179], v8 offset:37376\n\t" "s_waitcnt lgkmcnt(4)\n\t" "v_mfma_f32_16x16x32_bf16 v[80:83], v[156:159], v[196:199], v[80:83]\n\t"
    "ds_read_b64_tr_b16 v[180:181], v9 offset:36864\n\t" "v_cvt_pk_bf16_f32 v190, v208, v209\n\t" "v_mfma_f32_16x16x32_bf16 v[84:87], v[156:159], v[200:203], v[84:87]\n\t"
    "ds_read_b64_tr_b16 v[182:183], v9 offset:37376\n\t" "v_cvt_pk_bf16_f32 v191, v210, v211\n\t" "v_mfma_f32_16x16x32_bf16 v[144:147], v[156:159], v[232:235], v[144:147]\n\t"
    "ds_read_b128 v[224:227], v2 offset:7936\n\t" "s_add_u32 m0, %[sl1], 61440\n\t" "v_mfma_f32_16x16x32_bf16 v[88:91], v[160:163], v[196:199], v[88:91]\n\t"
    "ds_read_b64_tr_b16 v[184:185], v10 offset:36864\n\t" "v_cvt_pk_bf16_f32 v192, v212, v213\n\t" "v_mfma_f32_16x16x32_bf16 v[92:95], v[160:163], v[200:203], v[92:95]\n\t"
    "ds_read_b64_tr_b16 v[186:187], v10 offset:37376\n\t" "v_cvt_pk_bf16_f32 v193, v214, v215\n\t" "v_mfma_f32_16x16x32_bf16 v[96:99], v[164:167], v[196:199], v[96:99]\n\t" "s_waitcnt vmcnt(13)\n\t"
    "s_barrier\n\t" "ds_read_b64_tr_b16 v[156:157], v3 offset:51200\n\t" "v_mfma_f32_16x16x32_bf16 v[100:103], v[164:167], v[200:203], v[100:103]\n\t" "ds_read_b64_tr_b16 v[158:159], v3 offset:51712\n\t"
    "v_cvt_pk_bf16_f32 v194, v216, v217\n\t" "v_mfma_f32_16x16x32_bf16 v[104:107], v[168:171], v[196:199], v[104:107]\n\t" "ds_read_b64_tr_b16 v[160:161], v3 offset:53248\n\t"
    "v_cvt_pk_bf16_f32 v195, v218, v219\n\t" "v_mfma_f32_16x16x32_bf16 v[108:111], v[168:171], v[200:203], v[108:111]\n\t" "ds_read_b64_tr_b16 v[162:163], v3 offset:53760\n\t" "s_waitcnt lgkmcnt(4)\n\t"
    "v_mfma_f32_16x16x32_bf16 v[112:115], v[196:199], v[172:175], v[112:115]\n\t" "ds_read_b64_tr_b16 v[164:165], v3 offset:49152\n\t" "global_load_lds_dwordx4 v12, %[so]\n\t"
    "v_mfma_f32_16x16x32_bf16 v[116:119], v[200:203], v[172:175], v[116:119]\n\t" "ds_read_b64_tr_b16 v[166:167], v3 offset:49664\n\t" "v_add_u32 v12, 0x10000, v12\n\t"
    "v_mfma_f32_16x16x32_bf16 v[148:151], v[232:235], v[172:175], v[148:151]\n\t" "ds_read_b128 v[228:231], v1 offset:30720\n\t" "s_add_u32 m0, %[sl1], 65536\n\t"
    "v_mfma_f32_16x16x32_bf16 v[120:123], v[196:199], v[176:179], v[120:123]\n\t" "ds_read_b64_tr_b16 v[168:169], v3 offset:63488\n\t" "ds_read_b128 v[232:235], v1 offset:31744\n\t"
    "v_mfma_f32_16x16x32_bf16 v[124:127], v[200:203], v[176:179], v[124:127]\n\t" "ds_read_b64_tr_b16 v[170:171], v3 offset:64000\n\t" "global_load_lds_dwordx4 v12, %[so]\n\t"
    "v_mfma_f32_16x16x32_bf16 v[128:131], v[196:199], v[180:183], v[128:131]\n\t" "ds_read_b64_tr_b16 v[172:173], v7 offset:0\n\t" "v_lshlrev_b32 v18, 4, v13\n\t"
    "v_mfma_f32_16x16x32_bf16 v[132:135], v[200:203], v[180:183], v[132:135]\n\t" "ds_read_b64_tr_b16 v[174:175], v7 offset:512\n\t" "global_load_dword a64, v18, %[cd]\n\t"
    "v_mfma_f32_16x16x32_bf16 v[136:139], v[196:199], v[184:187], v[136:139]\n\t" "ds_read_b64_tr_b16 v[176:177], v3 offset:61440\n\t"
    "v_mfma_f32_16x16x32_bf16 v[140:143], v[200:203], v[184:187], v[140:143]\n\t" "ds_read_b64_tr_b16 v[178:179], v3 offset:61952\n\t" "s_waitcnt lgkmcnt(4)\n\t"
    "v_mfma_f32_16x16x32_bf16 v[80:83], v[156:159], v[188:191], v[80:83]\n\t" "ds_read_b64_tr_b16 v[180:181], v4 offset:49152\n\t" "v_add_u32 v12, 0x70000, v12\n\t"
    "v_mfma_f32_16x16x32_bf16 v[84:87], v[156:159], v[192:195], v[84:87]\n\t" "ds_read_b64_tr_b16 v[182:183], v4 offset:49664\n\t" "s_add_u32 m0, %[sl1], 94208\n\t"
    "v_mfma_f32_16x16x32_bf16 v[144:147], v[156:159], v[228:231], v[144:147]\n\t" "global_load_lds_dwordx4 v12, %[so]\n\t" "v_add_u32 v12, 0x10000, v12\n\t"
    "v_mfma_f32_16x16x32_bf16 v[112:115], v[188:191], v[160:163], v[112:115]\n\t" "ds_read_b64_tr_b16 v[184:185], v5 offset:49152\n\t" "v_mfma_f32_4x4x4_16b_bf16 v[204:207], v[224:225], a[248:249], 0\n\t"
    "v_mfma_f32_4x4x4_16b_bf16 v[208:211], v[226:227], a[250:251], 0\n\t" "v_mfma_f32_4x4x4_16b_bf16 v[212:215], v[224:225], a[252:253], 0\n\t"
    "v_mfma_f32_4x4x4_16b_bf16 v[216:219], v[226:227], a[254:255], 0\n\t" "v_mfma_f32_16x16x32_bf16 v[116:119], v[192:195], v[160:163], v[116:119]\n\t" "ds_read_b64_tr_b16 v[186:187], v5 offset:49664\n\t"
    "v_mfma_f32_16x16x32_bf16 v[148:151], v[228:231], v[160:163], v[148:151]\n\t" "v_mfma_f32_16x16x32_bf16 v[48:51], v[164:167], v[188:191], v[48:51]\n\t"
    "ds_read_b64_tr_b16 v[156:157], v6 offset:49152\n\t" "v_cvt_pk_bf16_f32 v196, v204, v205\n\t" "v_mfma_f32_16x16x32_bf16 v[52:55], v[164:167], v[192:195], v[52:55]\n\t"
    "ds_read_b64_tr_b16 v[158:159], v6 offset:49664\n\t" "v_cvt_pk_bf16_f32 v197, v206, v207\n\t" "v_mfma_f32_16x16x32_bf16 v[152:155], v[228:231], v[164:167], v[152:155]\n\t"
    "s_add_u32 m0, %[sl1], 98304\n\t" "v_mfma_f32_16x16x32_bf16 v[144:147], v[168:171], v[232:235], v[144:147]\n\t" "ds_read_b64_tr_b16 v[160:161], v4 offset:51200\n\t"
    "ds_read_b64_tr_b16 v[162:163], v4 offset:51712\n\t" "v_cvt_pk_bf16_f32 v198, v208, v209\n\t" "s_waitcnt lgkmcnt(4)\n\t" "v_mfma_f32_16x16x32_bf16 v[148:151], v[232:235], v[172:175], v[148:151]\n\t"
    "ds_read_b64_tr_b16 v[164:165], v5 offset:51200\n\t" "ds_read_b64_tr_b16 v[166:167], v5 offset:51712\n\t" "v_cvt_pk_bf16_f32 v199, v210, v211\n\t"
    "v_mfma_f32_16x16x32_bf16 v[152:155], v[232:235], v[176:179], v[152:155]\n\t" "ds_read_b64_tr_b16 v[168:169], v6 offset:51200\n\t" "ds_read_b64_tr_b16 v[170:171], v6 offset:51712\n\t"
    "v_mfma_f32_16x16x32_bf16 v[56:59], v[180:183], v[188:191], v[56:59]\n\t" "ds_read_b64_tr_b16 v[172:173], v4 offset:53248\n\t" "v_cvt_pk_bf16_f32 v200, v212, v213\n\t"
    "v_mfma_f32_16x16x32_bf16 v[60:63], v[180:183], v[192:195], v[60:63]\n\t" "ds_read_b64_tr_b16 v[174:175], v4 offset:53760\n\t" "v_cvt_pk_bf16_f32 v201, v214, v215\n\t"
    "v_mfma_f32_16x16x32_bf16 v[64:67], v[184:187], v[188:191], v[64:67]\n\t" "ds_read_b64_tr_b16 v[176:177], v5 offset:53248\n\t" "global_load_lds_dwordx4 v12, %[so]\n\t"
    "v_mfma_f32_16x16x32_bf16 v[68:71], v[184:187], v[192:195], v[68:71]\n\t" "ds_read_b64_tr_b16 v[178:179], v5 offset:53760\n\t" "v_cvt_pk_bf16_f32 v202, v216, v217\n\t" "s_waitcnt lgkmcnt(4)\n\t"
    "v_mfma_f32_16x16x32_bf16 v[72:75], v[156:159], v[188:191], v[72:75]\n\t" "ds_read_b64_tr_b16 v[180:181], v6 offset:53248\n\t" "v_cvt_pk_bf16_f32 v203, v218, v219\n\t"
    "v_mfma_f32_16x16x32_bf16 v[76:79], v[156:159], v[192:195], v[76:79]\n\t" "ds_read_b64_tr_b16 v[182:183], v6 offset:53760\n\t" "v_add_u32 v12, 0x10000, v12\n\t"
    "v_mfma_f32_16x16x32_bf16 v[88:91], v[160:163], v[188:191], v[88:91]\n\t" "ds_read_b64_tr_b16 v[184:185], v3 offset:63488\n\t" "s_add_u32 m0, %[sl1], 102400\n\t"
    "v_mfma_f32_16x16x32_bf16 v[92:95], v[160:163], v[192:195], v[92:95]\n\t" "ds_read_b64_tr_b16 v[186:187], v3 offset:64000\n\t" "v_and_b32 v242, 0xffff0000, v144\n\t"
    "v_mfma_f32_16x16x32_bf16 v[96:99], v[164:167], v[188:191], v[96:99]\n\t" "ds_read_b64_tr_b16 v[156:157], v4 offset:63488\n\t" "global_load_lds_dwordx4 v12, %[so]\n\t"
    "v_mfma_f32_16x16x32_bf16 v[100:103], v[164:167], v[192:195], v[100:103]\n\t" "ds_read_b64_tr_b16 v[158:159], v4 offset:64000\n\t" "v_sub_f32 v18, v144, v242\n\t"
    "v_mfma_f32_16x16x32_bf16 v[104:107], v[168:171], v[188:191], v[104:107]\n\t" "ds_read_b64_tr_b16 v[160:161], v5 offset:63488\n\t" "v_cvt_pk_bf16_f32 v242, v242, v18\n\t"
    "v_mfma_f32_16x16x32_bf16 v[108:111], v[168:171], v[192:195], v[108:111]\n\t" "ds_read_b64_tr_b16 v[162:163], v5 offset:64000\n\t" "s_waitcnt lgkmcnt(4)\n\t"
    "v_mfma_f32_16x16x32_bf16 v[120:123], v[188:191], v[172:175], v[120:123]\n\t" "ds_read_b64_tr_b16 v[164:165], v6 offset:63488\n\t" "v_and_b32 v243, 0xffff0000, v145\n\t"
    "v_mfma_f32_16x16x32_bf16 v[124:127], v[192:195], v[172:175], v[124:127]\n\t" "ds_read_b64_tr_b16 v[166:167], v6 offset:64000\n\t" "v_sub_f32 v18, v145, v243\n\t"
    "v_mfma_f32_16x16x32_bf16 v[128:131], v[188:191], v[176:179], v[128:131]\n\t" "ds_read_b64_tr_b16 v[168:169], v7 offset:0\n\t" "v_cvt_pk_bf16_f32 v243, v243, v18\n\t"
    "v_mfma_f32_16x16x32_bf16 v[132:135], v[192:195], v[176:179], v[132:135]\n\t" "ds_read_b64_tr_b16 v[170:171], v7 offset:512\n\t" "ds_write2_b32 v34, v242, v243 offset0:0 offset1:2\n\t"
    "v_mfma_f32_16x16x32_bf16 v[136:139], v[188:191], v[180:183], v[136:139]\n\t" "ds_read_b64_tr_b16 v[172:173], v8 offset:0\n\t" "v_and_b32 v244, 0xffff0000, v146\n\t"
    "v_mfma_f32_16x16x32_bf16 v[140:143], v[192:195], v[180:183], v[140:143]\n\t" "ds_read_b64_tr_b16 v[174:175], v8 offset:512\n\t" "v_sub_f32_e64 v18, v146, v244\n\t"
    "v_mfma_f32_16x16x32_bf16 v[80:83], v[184:187], v[196:199], v[80:83]\n\t" "ds_read_b64_tr_b16 v[176:177], v9 offset:0\n\t" "v_cvt_pk_bf16_f32 v244, v244, v18\n\t"
    "v_mfma_f32_16x16x32_bf16 v[84:87], v[184:187], v[200:203], v[84:87]\n\t" "ds_read_b64_tr_b16 v[178:179], v9 offset:512\n\t" "s_waitcnt lgkmcnt(5)\n\t"
    "v_mfma_f32_16x16x32_bf16 v[88:91], v[156:159], v[196:199], v[88:91]\n\t" "ds_read_b64_tr_b16 v[180:181], v10 offset:0\n\t" "v_and_b32 v245, 0xffff0000, v147\n\t"
    "v_mfma_f32_16x16x32_bf16 v[92:95], v[156:159], v[200:203], v[92:95]\n\t" "ds_read_b64_tr_b16 v[182:183], v10 offset:512\n\t" "v_sub_f32 v18, v147, v245\n\t"
    "v_mfma_f32_16x16x32_bf16 v[96:99], v[160:163], v[196:199], v[96:99]\n\t" "ds_read_b64_tr_b16 v[184:185], v3 offset:61440\n\t" "v_cvt_pk_bf16_f32 v245, v245, v18\n\t"
    "v_mfma_f32_16x16x32_bf16 v[100:103], v[160:163], v[200:203], v[100:103]\n\t" "ds_read_b64_tr_b16 v[186:187], v3 offset:61952\n\t" "ds_write2_b32 v34, v244, v245 offset0:4 offset1:6\n\t"
    "v_mfma_f32_16x16x32_bf16 v[104:107], v[164:167], v[196:199], v[104:107]\n\t" "ds_read_b64_tr_b16 v[156:157], v4 offset:61440\n\t" "v_and_b32 v238, 0xffff0000, v148\n\t"
    "v_mfma_f32_16x16x32_bf16 v[108:111], v[164:167], v[200:203], v[108:111]\n\t" "ds_read_b64_tr_b16 v[158:159], v4 offset:61952\n\t" "v_sub_f32_e64 v18, v148, v238\n\t"
    "v_mfma_f32_16x16x32_bf16 v[112:115], v[196:199], v[168:171], v[112:115]\n\t" "ds_read_b64_tr_b16 v[160:161], v5 offset:61440\n\t" "v_cvt_pk_bf16_f32 v238, v238, v18\n\t"
    "v_mfma_f32_16x16x32_bf16 v[116:119], v[200:203], v[168:171], v[116:119]\n\t" "ds_read_b64_tr_b16 v[162:163], v5 offset:61952\n\t" "s_waitcnt lgkmcnt(5)\n\t"
    "v_mfma_f32_16x16x32_bf16 v[120:123], v[196:199], v[172:175], v[120:123]\n\t" "ds_read_b64_tr_b16 v[164:165], v6 offset:61440\n\t" "v_and_b32 v239, 0xffff0000, v149\n\t"
    "v_mfma_f32_16x16x32_bf16 v[124:127], v[200:203], v[172:175], v[124:127]\n\t" "ds_read_b64_tr_b16 v[166:167], v6 offset:61952\n\t" "v_sub_f32 v18, v149, v239\n\t"
    "v_mfma_f32_16x16x32_bf16 v[128:131], v[196:199], v[176:179], v[128:131]\n\t" "v_cvt_pk_bf16_f32 v239, v239, v18\n\t" "v_and_b32 v240, 0xffff0000, v152\n\t"
    "v_mfma_f32_16x16x32_bf16 v[132:135], v[200:203], v[176:179], v[132:135]\n\t" "v_sub_f32_e64 v18, v152, v240\n\t" "v_cvt_pk_bf16_f32 v240, v240, v18\n\t"
    "v_mfma_f32_16x16x32_bf16 v[136:139], v[196:199], v[180:183], v[136:139]\n\t" "v_and_b32 v241, 0xffff0000, v153\n\t" "v_sub_f32_e64 v18, v153, v241\n\t"
    "v_mfma_f32_16x16x32_bf16 v[140:143], v[200:203], v[180:183], v[140:143]\n\t" "v_cvt_pk_bf16_f32 v241, v241, v18\n\t" "v_mfma_f32_16x16x32_bf16 v[48:51], v[184:187], v[196:199], v[48:51]\n\t"
    "ds_write2st64_b64 v35, v[238:239], v[240:241] offset0:2 offset1:0\n\t" "v_mfma_f32_16x16x32_bf16 v[52:55], v[184:187], v[200:203], v[52:55]\n\t" "s_waitcnt lgkmcnt(1)\n\t"
    "v_mfma_f32_16x16x32_bf16 v[56:59], v[156:159], v[196:199], v[56:59]\n\t" "s_waitcnt lgkmcnt(0)\n\t" "s_barrier\n\t" "v_mfma_f32_16x16x32_bf16 v[60:63], v[156:159], v[200:203], v[60:63]\n\t"
    "ds_read2st64_b64 v[188:191], v36 offset0:1 offset1:2\n\t" "ds_read2st64_b64 v[192:195], v37 offset0:1 offset1:2\n\t" "v_mfma_f32_16x16x32_bf16 v[64:67], v[160:163], v[196:199], v[64:67]\n\t"
    "ds_read2st64_b64 v[220:223], v38 offset0:1 offset1:2\n\t" "ds_read2st64_b64 v[224:227], v39 offset0:1 offset1:2\n\t" "v_mfma_f32_16x16x32_bf16 v[68:71], v[160:163], v[200:203], v[68:71]\n\t"
    "ds_read_b64 v[228:229], v36\n\t" "ds_read_b64 v[230:231], v37\n\t" "v_mfma_f32_16x16x32_bf16 v[72:75], v[164:167], v[196:199], v[72:75]\n\t" "ds_read_b64 v[232:233], v38\n\t"
    "ds_read_b64 v[234:235], v39\n\t" "v_mfma_f32_16x16x32_bf16 v[76:79], v[164:167], v[200:203], v[76:79]\n\t" "s_waitcnt lgkmcnt(4)\n\t"
    "v_mfma_f32_16x16x16_bf16 v[84:87], v[188:189], v[248:249], v[84:87]\n\t" "s_nop 1\n\t" "v_mfma_f32_16x16x16_bf16 v[92:95], v[192:193], v[248:249], v[92:95]\n\t" "s_nop 1\n\t"
    "v_mfma_f32_16x16x16_bf16 v[100:103], v[220:221], v[248:249], v[100:103]\n\t" "s_nop 1\n\t" "v_mfma_f32_16x16x16_bf16 v[108:111], v[224:225], v[248:249], v[108:111]\n\t"
    "v_cvt_pk_bf16_f32 v164, v84, v85\n\t" "v_cvt_pk_bf16_f32 v165, v86, v87\n\t" "v_mfma_f32_16x16x16_bf16 v[144:147], v[188:189], v[250:251], v[144:147]\n\t" "v_cvt_pk_bf16_f32 v166, v92, v93\n\t"
    "v_cvt_pk_bf16_f32 v167, v94, v95\n\t" "v_mfma_f32_16x16x16_bf16 v[116:119], v[248:249], v[190:191], v[116:119]\n\t" "ds_write2_b64 v40, v[164:165], v[166:167] offset0:1 offset1:0\n\t"
    "v_cvt_pk_bf16_f32 v168, v100, v101\n\t" "v_mfma_f32_16x16x16_bf16 v[124:127], v[248:249], v[194:195], v[124:127]\n\t" "v_cvt_pk_bf16_f32 v169, v102, v103\n\t" "v_cvt_pk_bf16_f32 v170, v108, v109\n\t"
    "v_mfma_f32_16x16x16_bf16 v[132:135], v[248:249], v[222:223], v[132:135]\n\t" "v_cvt_pk_bf16_f32 v171, v110, v111\n\t" "ds_write2_b64 v41, v[168:169], v[170:171] offset0:1 offset1:0\n\t"
    "v_mfma_f32_16x16x16_bf16 v[140:143], v[248:249], v[226:227], v[140:143]\n\t" "v_cvt_pk_bf16_f32 v144, v144, v145\n\t" "v_cvt_pk_bf16_f32 v145, v146, v147\n\t"
    "v_mfma_f32_16x16x16_bf16 v[148:151], v[250:251], v[190:191], v[148:151]\n\t" "v_cvt_pk_bf16_f32 v200, v116, v117\n\t" "v_cvt_pk_bf16_f32 v201, v118, v119\n\t"
    "v_mfma_f32_16x16x16_bf16 v[80:83], v[188:189], v[246:247], v[80:83]\n\t" "v_cvt_pk_bf16_f32 v206, v124, v125\n\t" "v_cvt_pk_bf16_f32 v207, v126, v127\n\t"
    "v_mfma_f32_16x16x16_bf16 v[88:91], v[192:193], v[246:247], v[88:91]\n\t" "ds_write2_b64 v40, v[200:201], v[206:207] offset0:3 offset1:2\n\t" "v_cvt_pk_bf16_f32 v212, v132, v133\n\t"
    "v_mfma_f32_16x16x16_bf16 v[96:99], v[220:221], v[246:247], v[96:99]\n\t" "v_cvt_pk_bf16_f32 v213, v134, v135\n\t" "v_cvt_pk_bf16_f32 v218, v140, v141\n\t"
    "v_mfma_f32_16x16x16_bf16 v[104:107], v[224:225], v[246:247], v[104:107]\n\t" "v_cvt_pk_bf16_f32 v219, v142, v143\n\t" "ds_write2_b64 v41, v[212:213], v[218:219] offset0:3 offset1:2\n\t"
    "s_waitcnt lgkmcnt(4)\n\t" "v_mfma_f32_16x16x16_bf16 v[52:55], v[228:229], v[248:249], v[52:55]\n\t" "v_cvt_pk_bf16_f32 v148, v148, v149\n\t" "v_cvt_pk_bf16_f32 v149, v150, v151\n\t"
    "v_mfma_f32_16x16x16_bf16 v[60:63], v[230:231], v[248:249], v[60:63]\n\t" "ds_write2_b64 v42, v[144:145], v[148:149] offset0:0 offset1:2\n\t" "v_cvt_pk_bf16_f32 v156, v80, v81\n\t"
    "v_mfma_f32_16x16x16_bf16 v[68:71], v[232:233], v[248:249], v[68:71]\n\t" "v_cvt_pk_bf16_f32 v157, v82, v83\n\t" "v_cvt_pk_bf16_f32 v158, v88, v89\n\t"
    "v_mfma_f32_16x16x16_bf16 v[76:79], v[234:235], v[248:249], v[76:79]\n\t" "v_cvt_pk_bf16_f32 v159, v90, v91\n\t" "v_cvt_pk_bf16_f32 v160, v96, v97\n\t"
    "v_mfma_f32_16x16x16_bf16 v[48:51], v[228:229], v[246:247], v[48:51]\n\t" "v_cvt_pk_bf16_f32 v161, v98, v99\n\t" "v_cvt_pk_bf16_f32 v162, v104, v105\n\t"
    "v_mfma_f32_16x16x16_bf16 v[56:59], v[230:231], v[246:247], v[56:59]\n\t" "v_cvt_pk_bf16_f32 v163, v106, v107\n\t" "s_waitcnt lgkmcnt(0)\n\t" "s_barrier\n\t"
    "v_mfma_f32_16x16x16_bf16 v[64:67], v[232:233], v[246:247], v[64:67]\n\t" "ds_read_b128 v[144:147], v43 offset:0\n\t" "ds_read_b128 v[148:151], v43 offset:2048\n\t"
    "v_mfma_f32_16x16x16_bf16 v[72:75], v[234:235], v[246:247], v[72:75]\n\t" "ds_read_b64 v[196:197], v43 offset:16\n\t" "ds_read_b64 v[202:203], v43 offset:24\n\t"
    "v_mfma_f32_16x16x16_bf16 v[112:115], v[246:247], v[190:191], v[112:115]\n\t" "ds_read_b64 v[208:209], v43 offset:2064\n\t" "ds_read_b64 v[214:215], v43 offset:2072\n\t"
    "v_mfma_f32_16x16x16_bf16 v[120:123], v[246:247], v[194:195], v[120:123]\n\t" "v_cvt_pk_bf16_f32 v180, v52, v53\n\t" "v_cvt_pk_bf16_f32 v181, v54, v55\n\t"
    "v_mfma_f32_16x16x16_bf16 v[128:131], v[246:247], v[222:223], v[128:131]\n\t" "v_cvt_pk_bf16_f32 v182, v60, v61\n\t" "v_cvt_pk_bf16_f32 v183, v62, v63\n\t"
    "v_mfma_f32_16x16x16_bf16 v[136:139], v[246:247], v[226:227], v[136:139]\n\t" "v_cvt_pk_bf16_f32 v184, v68, v69\n\t" "v_cvt_pk_bf16_f32 v185, v70, v71\n\t"
    "v_mfma_f32_16x16x32_bf16 v[80:83], v[156:159], v[180:183], 0\n\t" "v_cvt_pk_bf16_f32 v186, v76, v77\n\t" "v_cvt_pk_bf16_f32 v187, v78, v79\n\t"
    "v_mfma_f32_16x16x32_bf16 v[84:87], v[164:167], v[180:183], 0\n\t" "v_cvt_pk_bf16_f32 v172, v48, v49\n\t" "v_cvt_pk_bf16_f32 v173, v50, v51\n\t"
    "v_mfma_f32_16x16x32_bf16 v[88:91], v[160:163], v[184:187], 0\n\t" "v_cvt_pk_bf16_f32 v174, v56, v57\n\t" "v_cvt_pk_bf16_f32 v175, v58, v59\n\t"
    "v_mfma_f32_16x16x32_bf16 v[92:95], v[168:171], v[184:187], 0\n\t" "v_cvt_pk_bf16_f32 v176, v64, v65\n\t" "v_cvt_pk_bf16_f32 v177, v66, v67\n\t" "s_waitcnt lgkmcnt(4)\n\t"
    "v_mfma_f32_16x16x32_bf16 v[96:99], v[144:147], v[172:175], 0\n\t" "v_cvt_pk_bf16_f32 v178, v72, v73\n\t" "v_cvt_pk_bf16_f32 v179, v74, v75\n\t"
    "v_mfma_f32_16x16x32_bf16 v[100:103], v[156:159], v[172:175], 0\n\t" "v_cvt_pk_bf16_f32 v198, v112, v113\n\t" "v_cvt_pk_bf16_f32 v199, v114, v115\n\t"
    "v_mfma_f32_16x16x32_bf16 v[104:107], v[148:151], v[176:179], 0\n\t" "v_cvt_pk_bf16_f32 v204, v120, v121\n\t" "v_cvt_pk_bf16_f32 v205, v122, v123\n\t"
    "v_mfma_f32_16x16x32_bf16 v[108:111], v[160:163], v[176:179], 0\n\t" "v_cvt_pk_bf16_f32 v210, v128, v129\n\t" "v_cvt_pk_bf16_f32 v211, v130, v131\n\t" "v_cvt_pk_bf16_f32 v216, v136, v137\n\t"
    "v_cvt_pk_bf16_f32 v217, v138, v139\n\t" "v_cndmask_b32 v48, v80, v81, s[50:51]\n\t" "v_cndmask_b32 v64, v84, v85, s[50:51]\n\t" "v_cndmask_b32 v49, v82, v83, s[50:51]\n\t"
    "v_cndmask_b32 v65, v86, v87, s[50:51]\n\t" "v_cndmask_b32 v48, v48, v64, s[52:53]\n\t" "v_cndmask_b32 v49, v49, v65, s[54:55]\n\t" "v_mul_f32 v48, v48, v30\n\t" "v_mul_f32 v49, v49, v31\n\t"
    "v_max_f32 v56, v48, v49\n\t" "v_cndmask_b32 v50, v88, v89, s[50:51]\n\t" "v_cndmask_b32 v64, v92, v93, s[50:51]\n\t" "v_cndmask_b32 v51, v90, v91, s[50:51]\n\t"
    "v_cndmask_b32 v65, v94, v95, s[50:51]\n\t" "v_cndmask_b32 v50, v50, v64, s[52:53]\n\t" "v_cndmask_b32 v51, v51, v65, s[54:55]\n\t" "v_mul_f32 v50, v50, v30\n\t" "v_mul_f32 v51, v51, v31\n\t"
    "v_max_f32_e64 v57, v50, v51\n\t" "v_cndmask_b32 v52, v96, v97, s[50:51]\n\t" "v_cndmask_b32 v64, v100, v101, s[50:51]\n\t" "v_cndmask_b32 v53, v98, v99, s[50:51]\n\t"
    "v_cndmask_b32 v65, v102, v103, s[50:51]\n\t" "v_cndmask_b32 v52, v52, v64, s[52:53]\n\t" "v_cndmask_b32 v53, v53, v65, s[54:55]\n\t" "v_fma_f32 v52, v52, v28, v32\n\t"
    "v_fma_f32 v53, v53, v29, v33\n\t" "v_max_f32_e64 v58, v52, v53\n\t" "v_cndmask_b32 v54, v104, v105, s[50:51]\n\t" "v_cndmask_b32 v64, v108, v109, s[50:51]\n\t"
    "v_cndmask_b32 v55, v106, v107, s[50:51]\n\t" "v_cndmask_b32 v65, v110, v111, s[50:51]\n\t" "v_cndmask_b32 v54, v54, v64, s[52:53]\n\t" "v_cndmask_b32 v55, v55, v65, s[54:55]\n\t"
    "v_fma_f32 v54, v54, v28, v32\n\t" "v_fma_f32 v55, v55, v29, v33\n\t" "v_max_f32 v59, v54, v55\n\t" "v_permlane16_swap_b32 v56, v57\n\t" "s_nop 0\n\t" "v_permlane16_swap_b32 v58, v59\n\t"
    "v_max_f32 v56, v56, v57\n\t" "v_max_f32 v58, v58, v59\n\t" "s_nop 0\n\t" "s_nop 0\n\t" "v_permlane32_swap_b32 v56, v58\n\t" "v_max_f32 v56, v56, v58\n\t" "v_mov_b32 v58, v56\n\t" "s_nop 0\n\t"
    "s_nop 0\n\t" "v_permlane32_swap_b32 v56, v58\n\t" "v_mov_b32 v57, v56\n\t" "v_mov_b32 v59, v58\n\t" "s_nop 0\n\t" "v_permlane16_swap_b32 v56, v57\n\t" "v_permlane16_swap_b32 v58, v59\n\t"
    "v_sub_f32 v48, v48, v56\n\t" "v_sub_f32 v49, v49, v56\n\t" "v_sub_f32 v50, v50, v57\n\t" "v_sub_f32 v51, v51, v57\n\t" "v_sub_f32 v52, v52, v58\n\t" "v_sub_f32 v53, v53, v58\n\t"
    "v_sub_f32 v54, v54, v59\n\t" "v_sub_f32 v55, v55, v59\n\t" "v_exp_f32 v48, v48\n\t" "v_exp_f32 v49, v49\n\t" "v_exp_f32 v50, v50\n\t" "v_exp_f32 v51, v51\n\t" "v_exp_f32 v52, v52\n\t"
    "v_exp_f32 v53, v53\n\t" "v_exp_f32 v54, v54\n\t" "v_exp_f32 v55, v55\n\t" "v_add_f32 v56, v48, v49\n\t" "v_add_f32 v57, v50, v51\n\t" "v_add_f32 v58, v52, v53\n\t" "v_add_f32 v59, v54, v55\n\t"
    "v_permlane16_swap_b32 v56, v57\n\t" "v_mul_f32 v48, v48, v26\n\t" "v_permlane16_swap_b32 v58, v59\n\t" "v_add_f32 v56, v56, v57\n\t" "v_add_f32 v58, v58, v59\n\t" "v_mul_f32 v49, v49, v27\n\t"
    "v_mul_f32 v50, v50, v26\n\t" "v_permlane32_swap_b32 v56, v58\n\t" "v_add_f32 v56, v56, v58\n\t" "v_rcp_f32 v56, v56\n\t" "v_mul_f32 v51, v51, v27\n\t" "v_mov_b32 v58, v56\n\t"
    "v_mul_f32 v52, v52, v24\n\t" "v_mul_f32 v53, v53, v25\n\t" "v_permlane32_swap_b32 v56, v58\n\t" "v_mov_b32 v57, v56\n\t" "v_mov_b32 v59, v58\n\t" "v_mul_f32 v54, v54, v24\n\t"
    "v_permlane16_swap_b32 v56, v57\n\t" "v_permlane16_swap_b32 v58, v59\n\t" "v_mul_f32 v55, v55, v25\n\t" "s_waitcnt lgkmcnt(0)\n\t" "v_mul_f32 v48, v48, v56\n\t" "v_mul_f32_e64 v49, v49, v56\n\t"
    "v_cvt_pk_bf16_f32 v48, v48, v49\n\t" "v_perm_b32 v112, v48, v48, v20\n\t" "v_perm_b32 v113, v48, v48, v21\n\t" "v_perm_b32 v114, v48, v48, v22\n\t" "v_perm_b32 v115, v48, v48, v23\n\t"
    "v_mul_f32 v50, v50, v57\n\t" "v_mul_f32 v51, v51, v57\n\t" "v_cvt_pk_bf16_f32 v50, v50, v51\n\t" "v_perm_b32 v116, v50, v50, v20\n\t" "v_perm_b32 v117, v50, v50, v21\n\t"
    "v_perm_b32 v118, v50, v50, v22\n\t" "v_perm_b32 v119, v50, v50, v23\n\t" "v_mfma_f32_16x16x32_bf16 v[80:83], v[198:201], v[112:115], 0\n\t"
    "v_mfma_f32_16x16x32_bf16 v[84:87], v[204:207], v[112:115], 0\n\t" "v_mul_f32 v52, v52, v58\n\t" "v_mul_f32 v53, v53, v58\n\t" "v_cvt_pk_bf16_f32 v52, v52, v53\n\t"
    "v_perm_b32 v120, v52, v52, v20\n\t" "v_perm_b32 v121, v52, v52, v21\n\t" "v_perm_b32 v122, v52, v52, v22\n\t" "v_perm_b32 v123, v52, v52, v23\n\t"
    "v_mfma_f32_16x16x32_bf16 v[88:91], v[210:213], v[116:119], 0\n\t" "v_mfma_f32_16x16x32_bf16 v[92:95], v[216:219], v[116:119], 0\n\t" "v_mul_f32 v54, v54, v59\n\t" "v_mul_f32 v55, v55, v59\n\t"
    "v_cvt_pk_bf16_f32 v54, v54, v55\n\t" "v_perm_b32 v124, v54, v54, v20\n\t" "v_perm_b32 v125, v54, v54, v21\n\t" "v_perm_b32 v126, v54, v54, v22\n\t" "v_perm_b32 v127, v54, v54, v23\n\t"
    "v_mfma_f32_16x16x32_bf16 v[96:99], v[196:199], v[120:123], 0\n\t" "v_mfma_f32_16x16x32_bf16 v[100:103], v[202:205], v[120:123], 0\n\t" "v_cvt_pk_bf16_f32 v128, v80, v81\n\t"
    "v_cvt_pk_bf16_f32 v129, v82, v83\n\t" "v_cvt_pk_bf16_f32 v130, v84, v85\n\t" "v_cvt_pk_bf16_f32 v131, v86, v87\n\t" "global_store_dwordx4 v44, v[128:131], %[ws] offset:1024 sc1\n\t"
    "v_mfma_f32_16x16x32_bf16 v[104:107], v[208:211], v[124:127], 0\n\t" "v_mfma_f32_16x16x32_bf16 v[108:111], v[214:217], v[124:127], 0\n\t" "v_cvt_pk_bf16_f32 v132, v88, v89\n\t"
    "v_cvt_pk_bf16_f32 v133, v90, v91\n\t" "v_cvt_pk_bf16_f32 v134, v92, v93\n\t" "v_cvt_pk_bf16_f32 v135, v94, v95\n\t" "global_store_dwordx4 v45, v[132:135], %[ws] offset:1024 sc1\n\t"
    "v_cvt_pk_bf16_f32 v136, v96, v97\n\t" "v_cvt_pk_bf16_f32 v137, v98, v99\n\t" "v_cvt_pk_bf16_f32 v138, v100, v101\n\t" "v_cvt_pk_bf16_f32 v139, v102, v103\n\t"
    "global_store_dwordx4 v44, v[136:139], %[ws] offset:0 sc1\n\t" "v_cvt_pk_bf16_f32 v140, v104, v105\n\t" "v_cvt_pk_bf16_f32 v141, v106, v107\n\t" "v_cvt_pk_bf16_f32 v142, v108, v109\n\t"
    "v_cvt_pk_bf16_f32 v143, v110, v111\n\t" "global_store_dwordx4 v45, v[140:143], %[ws] offset:0 sc1\n\t" "v_perm_b32 v156, v252, v252, v14\n\t" "v_perm_b32 v157, v252, v252, v17\n\t"
    "v_perm_b32 v158, v252, v252, v15\n\t" "v_perm_b32 v159, v252, v252, v19\n\t" "v_mfma_f32_4x4x4_16b_bf16 v[176:179], v[156:157], a[32:33], 0\n\t"
    "v_mfma_f32_4x4x4_16b_bf16 v[180:183], v[156:157], a[34:35], 0\n\t" "v_perm_b32 v160, v253, v253, v14\n\t" "v_perm_b32 v161, v253, v253, v17\n\t"
    "v_mfma_f32_4x4x4_16b_bf16 v[176:179], v[158:159], a[36:37], v[176:179]\n\t" "v_mfma_f32_4x4x4_16b_bf16 v[180:183], v[158:159], a[38:39], v[180:183]\n\t" "v_perm_b32 v162, v253, v253, v15\n\t"
    "v_perm_b32 v163, v253, v253, v19\n\t" "v_mfma_f32_4x4x4_16b_bf16 v[176:179], v[160:161], a[40:41], v[176:179]\n\t" "v_mfma_f32_4x4x4_16b_bf16 v[180:183], v[160:161], a[42:43], v[180:183]\n\t"
    "v_perm_b32 v164, v254, v254, v14\n\t" "v_perm_b32 v165, v254, v254, v17\n\t" "v_mfma_f32_4x4x4_16b_bf16 v[176:179], v[162:163], a[44:45], v[176:179]\n\t"
    "v_mfma_f32_4x4x4_16b_bf16 v[180:183], v[162:163], a[46:47], v[180:183]\n\t" "v_perm_b32 v166, v254, v254, v15\n\t" "v_perm_b32 v167, v254, v254, v19\n\t"
    "v_mfma_f32_4x4x4_16b_bf16 v[176:179], v[164:165], a[48:49], v[176:179]\n\t" "v_mfma_f32_4x4x4_16b_bf16 v[180:183], v[164:165], a[50:51], v[180:183]\n\t" "v_perm_b32 v168, v255, v255, v14\n\t"
    "v_perm_b32 v169, v255, v255, v17\n\t" "v_mfma_f32_4x4x4_16b_bf16 v[176:179], v[166:167], a[52:53], v[176:179]\n\t" "v_mfma_f32_4x4x4_16b_bf16 v[180:183], v[166:167], a[54:55], v[180:183]\n\t"
    "v_perm_b32 v170, v255, v255, v15\n\t" "v_perm_b32 v171, v255, v255, v19\n\t" "v_mfma_f32_4x4x4_16b_bf16 v[176:179], v[168:169], a[56:57], v[176:179]\n\t"
    "v_mfma_f32_4x4x4_16b_bf16 v[180:183], v[168:169], a[58:59], v[180:183]\n\t" "s_nop 0\n\t" "v_mfma_f32_4x4x4_16b_bf16 v[176:179], v[170:171], a[60:61], v[176:179]\n\t"
    "v_mfma_f32_4x4x4_16b_bf16 v[180:183], v[170:171], a[62:63], v[180:183]\n\t" "s_nop 0\n\t" "s_nop 3\n\t" "v_permlane32_swap_b32 v176, v180\n\t" "v_permlane32_swap_b32 v177, v181\n\t"
    "v_permlane32_swap_b32 v178, v182\n\t" "v_permlane32_swap_b32 v179, v183\n\t" "v_add_f32 v176, v176, v180\n\t" "v_add_f32 v178, v178, v182\n\t" "v_add_f32 v177, v177, v181\n\t"
    "v_add_f32 v179, v179, v183\n\t" "v_permlane16_swap_b32 v176, v178\n\t" "s_nop 0\n\t" "v_permlane16_swap_b32 v177, v179\n\t" "v_add_f32 v176, v176, v178\n\t" "v_add_f32_e64 v177, v177, v179\n\t"
    "s_nop 0\n\t" "v_add_f32_dpp v176, v176, v176 row_ror:8 row_mask:0xf bank_mask:0xf\n\t" "v_add_f32_dpp v177, v177, v177 row_ror:8 row_mask:0xf bank_mask:0xf\n\t" "ds_write_b64 v11, v[176:177]\n\t"
    "v_mov_b64 v[178:179], s[62:63]\n\t" "v_mov_b32 v180, %[sf]\n\t" "s_waitcnt vmcnt(0) lgkmcnt(0)\n\t" "s_barrier\n\t" "s_cmp_eq_u32 %[w128], 0\n\t" "s_cselect_b64 exec, 1, 0\n\t"
    "global_store_dwordx2 v180, v[178:179], %[ws] sc1\n\t" "s_mov_b64 exec, -1\n\t" "v_add_u32 v12, 0xffe80000, v12\n\t" "s_add_u32 m0, %[sl1], 4096\n\t" "s_nop 0\n\t"
    "global_load_lds_dwordx4 v12, %[so]\n\t" "v_add_u32 v12, 0x100000, v12\n\t" "s_add_u32 m0, %[sl1], 69632\n\t" "s_nop 0\n\t" "global_load_lds_dwordx4 v12, %[so]\n\t" "v_add_u32 v12, 0x10000, v12\n\t"
    "s_add_u32 m0, %[sl1], 73728\n\t" "s_nop 0\n\t" "global_load_lds_dwordx4 v12, %[so]\n\t" "v_add_u32 v12, 0x10000, v12\n\t" "s_add_u32 m0, %[sl1], 77824\n\t" "s_nop 0\n\t"
    "global_load_lds_dwordx4 v12, %[so]\n\t" "v_add_u32 v12, 0x10000, v12\n\t" "s_add_u32 m0, %[sl1], 81920\n\t" "s_nop 0\n\t" "global_load_lds_dwordx4 v12, %[so]\n\t" "v_add_u32 v12, 0x10000, v12\n\t"
    "s_add_u32 m0, %[sl1], 86016\n\t" "s_nop 0\n\t" "global_load_lds_dwordx4 v12, %[so]\n\t" "v_add_u32 v12, 0x10000, v12\n\t" "s_add_u32 m0, %[sl1], 90112\n\t" "s_nop 0\n\t"
    "global_load_lds_dwordx4 v12, %[so]\n\t" "v_add_u32 v12, 0x40000, v12\n\t" "s_add_u32 m0, %[sl1], 106496\n\t" "s_nop 0\n\t" "global_load_lds_dwordx4 v12, %[so]\n\t" "s_branch .Lend_%=\n\t"
    ".Lslow_%=:\n\t" "s_sleep 2\n\t" "s_mov_b64 s[40:41], %[ws]\n\t" "global_load_dwordx4 v[80:83], v230, s[40:41] offset:-4096 sc1\n\t"
    "global_load_dwordx4 v[96:99], v230, s[40:41] offset:0 sc1\n\t" "s_add_u32 s40, s40, 8192\n\t" "s_addc_u32 s41, s41, 0\n\t" "global_load_dwordx4 v[84:87], v230, s[40:41] offset:-4096 sc1\n\t"
    "global_load_dwordx4 v[100:103], v230, s[40:41] offset:0 sc1\n\t" "s_add_u32 s40, s40, 8192\n\t" "s_addc_u32 s41, s41, 0\n\t" "global_load_dwordx4 v[88:91], v230, s[40:41] offset:-4096 sc1\n\t"
    "global_load_dwordx4 v[104:107], v230, s[40:41] offset:0 sc1\n\t" "s_add_u32 s40, s40, 8192\n\t" "s_addc_u32 s41, s41, 0\n\t" "global_load_dwordx4 v[92:95], v230, s[40:41] offset:-4096 sc1\n\t"
    "global_load_dwordx4 v[108:111], v230, s[40:41] offset:0 sc1\n\t" "s_waitcnt vmcnt(0)\n\t" "v_xad_u32 v144, v80, v81, s62\n\t" "v_xad_u32 v145, v84, v85, s62\n\t" "v_xad_u32 v146, v88, v89, s62\n\t"
    "v_xad_u32 v147, v92, v93, s62\n\t" "v_xad_u32 v148, v96, v97, s62\n\t" "v_xad_u32 v149, v100, v101, s62\n\t" "v_xad_u32 v150, v104, v105, s62\n\t" "v_xad_u32 v151, v108, v109, s62\n\t"
    "v_xad_u32 v144, v82, v81, v144\n\t" "v_xad_u32 v145, v86, v85, v145\n\t" "v_xad_u32 v146, v90, v89, v146\n\t" "v_xad_u32 v147, v94, v93, v147\n\t" "v_xad_u32 v148, v98, v97, v148\n\t"
    "v_xad_u32 v149, v102, v101, v149\n\t" "v_xad_u32 v150, v106, v105, v150\n\t" "v_xad_u32 v151, v110, v109, v151\n\t" "v_xor_b32_e64 v140, v144, v83\n\t"
    "v_bitop3_b32 v140, v140, v145, v87 bitop3:0xf6\n\t" "v_bitop3_b32 v140, v140, v146, v91 bitop3:0xf6\n\t" "v_bitop3_b32 v140, v140, v147, v95 bitop3:0xf6\n\t"
    "v_bitop3_b32 v140, v140, v148, v99 bitop3:0xf6\n\t" "v_bitop3_b32 v140, v140, v149, v103 bitop3:0xf6\n\t" "v_bitop3_b32 v140, v140, v150, v107 bitop3:0xf6\n\t"
    "v_bitop3_b32 v140, v140, v151, v111 bitop3:0xf6\n\t" "v_cmp_ne_u32_e64 vcc, 0, v140\n\t" "v_perm_b32 v83, v80, v81, s57\n\t" "v_perm_b32 v87, v84, v85, s57\n\t" "v_perm_b32 v91, v88, v89, s57\n\t"
    "v_perm_b32 v95, v92, v93, s57\n\t" "v_perm_b32 v99, v96, v97, s57\n\t" "v_perm_b32 v103, v100, v101, s57\n\t" "v_perm_b32 v107, v104, v105, s57\n\t" "v_perm_b32 v111, v108, v109, s57\n\t"
    "s_cbranch_vccz .Lgo_%=\n\t" "s_sub_u32 s44, s44, 1\n\t" "s_cbranch_scc0 .Lslow_%=\n\t" "s_branch .Lgo_%=\n\t" ".Lend_%=:\n\t"
    : "+{v0}"(vb), "+{v1}"(vh), "+{v2}"(vdt), "+{v11}"(vm), "+{v12}"(vbo), "+{v13}"(tid), "+{v[14:15]}"(t2), "+{v16}"(t3), "+{v18}"(vhd), "+{v19}"(vhx), "+{v196}"(d0), "+{v197}"(d1), "+{v198}"(d2), "+{v199}"(d3), "+{v200}"(dh0), "+{v201}"(dh1), "+{v202}"(xr0), "+{v203}"(xr1), "+{v230}"(vg), "+{v231}"(vtw), "+{v232}"(vhw), "+{v234}"(vs8), "+{v235}"(vph), "+{v246}"(vpr), "+{v3}"(vr0), "+{v4}"(vr1), "+{v5}"(vr2), "+{v6}"(vr3), "+{v7}"(vr4), "+{v8}"(vr5), "+{v9}"(vr6), "+{v10}"(vr7), "+{v205}"(tv), "+{s[62:63]}"(tag), "={v236}"(mu[0]), "={v237}"(mu[1])
    : [l0] "s"(l0), [lh] "s"(lh), [lt] "s"(lt), [lr] "s"(lr), [sl] "s"(sl), [sl1] "s"(sl1), [sb] "s"(sb), [rot] "s"(rot), [xl] "s"(xl), [xh_] "s"(xh_), [k0] "s"(k0), [kh] "s"(kh), [ws] "s"(ws), [sm] "s"(sm), [so] "s"(so), [sf] "s"(sf), [pf] "s"(pf), [tm] "s"(tm), [cd] "s"(cd), [w128] "s"(w128), [w4k] "s"(w4k), [xw] "s"(xw), [xh] "s"(xh), [ox] "s"(ox), [rw] "s"(rw)
    : "v17", "v20", "v21", "v22", "v23", "v24", "v25", "v26", "v27", "v28", "v29", "v30", "v31", "v32", "v33", "v34", "v35", "v36", "v37", "v38", "v39", "v40", "v41", "v42", "v43", "v44", "v45", "v46", "v47", "v48", "v49", "v50", "v51", "v52", "v53", "v54", "v55", "v56", "v57", "v58", "v59", "v60", "v61", "v62", "v63", "v64", "v65", "v66", "v67", "v68", "v69", "v70", "v71", "v72", "v73", "v74", "v75", "v76", "v77", "v78", "v79", "v80", "v81", "v82", "v83", "v84", "v85", "v86", "v87", "v88", "v89", "v90", "v91", "v92", "v93", "v94", "v95", "v96", "v97", "v98", "v99", "v100", "v101", "v102", "v103", "v104", "v105", "v106", "v107", "v108", "v109", "v110", "v111", "v112", "v113", "v114", "v115", "v116", "v117", "v118", "v119", "v120", "v121", "v122", "v123", "v124", "v125", "v126", "v127", "v128", "v129", "v130", "v131", "v132", "v133", "v134", "v135", "v136", "v137", "v138", "v139", "v140", "v141", "v142", "v143", "v144", "v145", "v146", "v147", "v148", "v149", "v150", "v151", "v152", "v153", "v154", "v155", "v156", "v157", "v158", "v159", "v160", "v161", "v162", "v163", "v164", "v165", "v166", "v167", "v168", "v169", "v170", "v171", "v172", "v173", "v174", "v175", "v176", "v177", "v178", "v179", "v180", "v181", "v182", "v183", "v184", "v185", "v186", "v187", "v188", "v189", "v190", "v191", "v192", "v193", "v194", "v195", "v204", "v206", "v207", "v208", "v209", "v210", "v211", "v212", "v213", "v214", "v215", "v216", "v217", "v218", "v219", "v220", "v221", "v222", "v223", "v224", "v225", "v226", "v227", "v228", "v229", "v233", "v238", "v239", "v240", "v241", "v242", "v243", "v244", "v245", "v247", "v248", "v249", "v250", "v251", "v252", "v253", "v254", "v255", "a0", "a1", "a2", "a3", "a4", "a5", "a6", "a7", "a8", "a9", "a10", "a11", "a12", "a13", "a14", "a15", "a16", "a17", "a18", "a19", "a20", "a21", "a22", "a23", "a24", "a25", "a26", "a27", "a28", "a29", "a30", "a31", "a32", "a33", "a34", "a35", "a36", "a37", "a38", "a39", "a40", "a41", "a42", "a43", "a44", "a45", "a46", "a47", "a48", "a49", "a50", "a51", "a52", "a53", "a54", "a55", "a56", "a57", "a58", "a59", "a60", "a61", "a62", "a63", "a64", "a65", "a66", "a67", "a68", "a69", "a70", "a71", "a72", "a73", "a74", "a75", "a76", "a77", "a78", "a79", "a80", "a81", "a82", "a83", "a84", "a85", "a86", "a87", "a88", "a89", "a90", "a91", "a92", "a93", "a94", "a95", "a96", "a97", "a98", "a99", "a100", "a101", "a102", "a103", "a104", "a105", "a106", "a107", "a108", "a109", "a110", "a111", "a112", "a113", "a114", "a115", "a116", "a117", "a118", "a119", "a120", "a121", "a122", "a123", "a124", "a125", "a126", "a127", "a128", "a129", "a130", "a131", "a132", "a133", "a134", "a135", "a136", "a137", "a138", "a139", "a140", "a141", "a142", "a143", "a144", "a145", "a146", "a147", "a148", "a149", "a150", "a151", "a152", "a153", "a154", "a155", "a156", "a157", "a158", "a159", "a160", "a161", "a162", "a163", "a164", "a165", "a166", "a167", "a168", "a169", "a170", "a171", "a172", "a173", "a174", "a175", "a176", "a177", "a178", "a179", "a180", "a181", "a182", "a183", "a184", "a185", "a186", "a187", "a188", "a189", "a190", "a191", "a192", "a193", "a194", "a195", "a196", "a197", "a198", "a199", "a200", "a201", "a202", "a203", "a204", "a205", "a206", "a207", "a208", "a209", "a210", "a211", "a212", "a213", "a214", "a215", "a216", "a217", "a218", "a219", "a220", "a221", "a222", "a223", "a224", "a225", "a226", "a227", "a228", "a229", "a230", "a231", "a232", "a233", "a234", "a235", "a236", "a237", "a238", "a239", "a240", "a241", "a242", "a243", "a244", "a245", "a246", "a247", "a248", "a249", "a250", "a251", "a252", "a253", "a254", "a255", "s40", "s41", "s42", "s43", "s44", "s45", "s46", "s47", "s48", "s49", "s50", "s51", "s52", "s53", "s54", "s55", "s56", "s57", "s58", "s59", "s60", "s61", "s64", "s65", "vcc", "scc", "memory"); }
  } }
  asm volatile("" : "+s"(tag)); asm volatile("" : "+s"(bx), "+s"(wid), "+s"(ws));
  constexpr int YS = 16384, YW = YS == 4096 ? 131072 : 4096;
  constexpr int GB = 49152, RAW = 0, TAB = 16384, ZB = TAB + 3584;
#define GW() const unsigned sl = ldsb + GB + wid * 3584; const char* sb = (const char*)((wid >> 1) ? wu : wg) + (unsigned)(((xcd >> 1) * 8 + (loc >> 2)) * 224 + (xcd & 1) * 16 * 32 * 7168)
  unsigned vb[4]; u32x4 stg[36];
  f32x4 xr[8];
  { unsigned vr[16]; const unsigned va = (tid & 63) * 16; const char* sa;
    { IDX();
    const unsigned vf = (tid & 15) * FS, rr = ldsb + HALO + 22528 + va;
    const char* sq = ws + (unsigned)(W_TAGS + OF * 8 + FA(rb * 16)); sa = ws + (unsigned)(W_O + (rb * 4 + wid) * 65536 + 4096);
    const char* su = ws + (unsigned)(wid == 3 ? W_MODP + gc * 1024 + gk * 256 : W_UVP + ((xcd >> 1) * 8 + (loc >> 2)) * 16384 + ((xcd & 1) * 4 + (loc & 3)) * 2048 + (wid & 1) * 1024);
    const int lane = tid & 63, fr = lane & 15, par = bx & 1; u32x4 id[2];
#pragma unroll
    for (int j = 0; j < 4; ++j) { vr[j] = ldsb + f_vr(lane, j) + (wid & 1) * 2048; vr[4 + j] = vr[j] + 65536; vr[8 + j] = vr[j] ^ 2048; vr[12 + j] = vr[8 + j] + 65536; }
#pragma unroll
    for (int j = 0; j < 8; ++j) id[j >> 2][j & 3] = (lane >> 1) - (fr >> 3) * 12 == (j >> 2) * 16 + (j & 3) ? 0x3f80u << (fr & 1) * 16 : 0u;
    asm volatile( "s_cmp_eq_u32 %[wid], 0\n\t" "s_cselect_b64 s[46:47], 0xffff, 0\n\t" "s_mov_b64 exec, s[46:47]\n\t" "global_load_dwordx2 v[190:191], v14, %[sq] sc1\n\t" "s_mov_b64 exec, -1\n\t"
    "v_sub_f32 v156, 0, v236\n\t" "v_sub_f32 v157, 0, v236\n\t" "v_sub_f32 v158, 0, v236\n\t" "v_sub_f32 v159, 0, v236\n\t" "v_sub_f32 v160, 0, v237\n\t" "v_sub_f32 v161, 0, v237\n\t"
    "v_sub_f32 v162, 0, v237\n\t" "v_sub_f32 v163, 0, v237\n\t" "s_cmp_eq_u32 %[par], 0\n\t" "s_cbranch_scc0 .Lx1_%=\n\t" "s_add_u32 m0, %[sl1], 110592\n\t" "v_add_u32 v12, 0x10000, v12\n\t"
    "v_mfma_f32_16x16x32_bf16 v[80:83], v[40:43], a[0:3], v[156:159]\n\t" "global_load_lds_dwordx4 v12, %[so]\n\t" "s_add_u32 m0, %[sl1], 114688\n\t" "v_add_u32 v12, 0x10000, v12\n\t"
    "v_mfma_f32_16x16x32_bf16 v[84:87], v[40:43], a[4:7], v[160:163]\n\t" "global_load_lds_dwordx4 v12, %[so]\n\t" "s_add_u32 m0, %[sl1], 118784\n\t" "v_add_u32 v12, 0x10000, v12\n\t"
    "v_mfma_f32_16x16x32_bf16 v[88:91], v[44:47], a[0:3], v[156:159]\n\t" "global_load_lds_dwordx4 v12, %[so]\n\t" "s_add_u32 m0, %[sl1], 122880\n\t" "v_add_u32 v12, 0x10000, v12\n\t"
    "v_mfma_f32_16x16x32_bf16 v[92:95], v[44:47], a[4:7], v[160:163]\n\t" "global_load_lds_dwordx4 v12, %[so]\n\t" "s_add_u32 m0, %[sl1], 126976\n\t" "v_add_u32 v12, 0x10000, v12\n\t"
    "v_mfma_f32_16x16x32_bf16 v[96:99], v[40:43], a[8:11], v[156:159]\n\t" "global_load_lds_dwordx4 v12, %[so]\n\t" "v_mfma_f32_16x16x32_bf16 v[100:103], v[40:43], a[12:15], v[160:163]\n\t"
    "v_mfma_f32_16x16x32_bf16 v[104:107], v[44:47], a[8:11], v[156:159]\n\t" "v_mfma_f32_16x16x32_bf16 v[108:111], v[44:47], a[12:15], v[160:163]\n\t" "s_branch .Lx2_%=\n\t" ".Lx1_%=:\n\t"
    "s_add_u32 m0, %[sl1], 110592\n\t" "v_add_u32 v12, 0x10000, v12\n\t" "v_mfma_f32_16x16x32_bf16 v[80:83], v[40:43], a[16:19], v[156:159]\n\t" "global_load_lds_dwordx4 v12, %[so]\n\t"
    "s_add_u32 m0, %[sl1], 114688\n\t" "v_add_u32 v12, 0x10000, v12\n\t" "v_mfma_f32_16x16x32_bf16 v[84:87], v[40:43], a[20:23], v[160:163]\n\t" "global_load_lds_dwordx4 v12, %[so]\n\t"
    "s_add_u32 m0, %[sl1], 118784\n\t" "v_add_u32 v12, 0x10000, v12\n\t" "v_mfma_f32_16x16x32_bf16 v[88:91], v[44:47], a[16:19], v[156:159]\n\t" "global_load_lds_dwordx4 v12, %[so]\n\t"
    "s_add_u32 m0, %[sl1], 122880\n\t" "v_add_u32 v12, 0x10000, v12\n\t" "v_mfma_f32_16x16x32_bf16 v[92:95], v[44:47], a[20:23], v[160:163]\n\t" "global_load_lds_dwordx4 v12, %[so]\n\t"
    "s_add_u32 m0, %[sl1], 126976\n\t" "v_add_u32 v12, 0x10000, v12\n\t" "v_mfma_f32_16x16x32_bf16 v[96:99], v[40:43], a[24:27], v[156:159]\n\t" "global_load_lds_dwordx4 v12, %[so]\n\t"
    "v_mfma_f32_16x16x32_bf16 v[100:103], v[40:43], a[28:31], v[160:163]\n\t" "v_mfma_f32_16x16x32_bf16 v[104:107], v[44:47], a[24:27], v[156:159]\n\t"
    "v_mfma_f32_16x16x32_bf16 v[108:111], v[44:47], a[28:31], v[160:163]\n\t" ".Lx2_%=:\n\t" "v_mov_b64 v[164:165], %[tag]\n\t" "s_cmp_eq_u32 %[wid], 0\n\t" "s_cselect_b64 s[42:43], 0, -1\n\t"
    "v_mov_b32 v168, -1\n\t" "v_mov_b32 v169, -1\n\t" "v_mov_b32 v170, -1\n\t" "v_mov_b32 v171, -1\n\t" "s_cmp_eq_u32 %[wid], 3\n\t" "s_cbranch_scc0 .Lnu_%=\n\t"
    "ds_read_b128 v[172:175], v35 offset:0\n\t" "ds_read_b128 v[176:179], v35 offset:256\n\t" "ds_read_b128 v[180:183], v35 offset:512\n\t" "ds_read_b128 v[184:187], v35 offset:768\n\t"
    "s_mov_b32 s48, 0x07060503\n\t" "s_mov_b32 s49, 0x07060502\n\t" "s_mov_b32 s50, 0x07060501\n\t" "s_waitcnt lgkmcnt(0)\n\t" "v_pk_add_f32 v[172:173], v[172:173], v[176:177]\n\t"
    "v_pk_add_f32 v[174:175], v[174:175], v[178:179]\n\t" "v_pk_add_f32 v[180:181], v[180:181], v[184:185]\n\t" "v_pk_add_f32 v[182:183], v[182:183], v[186:187]\n\t"
    "v_pk_add_f32 v[172:173], v[172:173], v[180:181]\n\t" "v_pk_add_f32 v[174:175], v[174:175], v[182:183]\n\t" "v_perm_b32 v168, v172, v175, s48\n\t" "v_perm_b32 v169, v173, v175, s49\n\t"
    "v_perm_b32 v170, v174, v175, s50\n\t" "v_xad_u32 v171, v168, v169, v164\n\t" "s_mov_b64 s[42:43], 0xffff\n\t" "v_xad_u32 v171, v170, v169, v171\n\t" ".Lnu_%=:\n\t" "s_mov_b64 s[40:41], %[sa]\n\t"
    "s_mov_b32 s44, 0x400000\n\t" "s_mov_b64 exec, s[46:47]\n\t" "s_waitcnt vmcnt(5)\n\t" ".Lpo_%=:\n\t" "v_cmp_ne_u64 vcc, %[tag], v[190:191]\n\t" "s_nop 4\n\t" "s_cbranch_vccz .Lro_%=\n\t"
    "s_mov_b64 exec, vcc\n\t" "s_sleep 1\n\t" "global_load_dwordx2 v[190:191], v14, %[sq] sc1\n\t" "s_sub_u32 s44, s44, 1\n\t" "s_waitcnt vmcnt(0)\n\t" "s_cbranch_scc0 .Lpo_%=\n\t" ".Lro_%=:\n\t"
    "s_mov_b64 exec, -1\n\t" "s_barrier\n\t" "global_load_dwordx4 a[64:67], v0, s[40:41] offset:-4096 sc1\n\t" "global_load_dwordx4 a[68:71], v0, s[40:41] offset:-3072 sc1\n\t"
    "global_load_dwordx4 a[72:75], v0, s[40:41] offset:-2048 sc1\n\t" "global_load_dwordx4 a[76:79], v0, s[40:41] offset:-1024 sc1\n\t" "ds_read_b64_tr_b16 a[160:161], v16 offset:0\n\t"
    "ds_read_b64_tr_b16 a[162:163], v24 offset:0\n\t" "ds_read_b64_tr_b16 a[164:165], v17 offset:0\n\t" "ds_read_b64_tr_b16 a[166:167], v25 offset:0\n\t" "ds_read_b64_tr_b16 a[168:169], v18 offset:0\n\t"
    "ds_read_b64_tr_b16 a[170:171], v26 offset:0\n\t" "ds_read_b64_tr_b16 a[172:173], v19 offset:0\n\t" "ds_read_b64_tr_b16 a[174:175], v27 offset:0\n\t"
    "ds_read_b64_tr_b16 a[176:177], v16 offset:4096\n\t" "ds_read_b64_tr_b16 a[178:179], v24 offset:4096\n\t" "ds_read_b64_tr_b16 a[180:181], v17 offset:4096\n\t"
    "ds_read_b64_tr_b16 a[182:183], v25 offset:4096\n\t" "global_load_dwordx4 a[80:83], v0, s[40:41] offset:0 sc1\n\t" "global_load_dwordx4 a[84:87], v0, s[40:41] offset:1024 sc1\n\t"
    "global_load_dwordx4 a[88:91], v0, s[40:41] offset:2048 sc1\n\t" "global_load_dwordx4 a[92:95], v0, s[40:41] offset:3072 sc1\n\t" "s_add_u32 s40, s40, 8192\n\t" "s_addc_u32 s41, s41, 0\n\t"
    "global_load_dwordx4 a[96:99], v0, s[40:41] offset:-4096 sc1\n\t" "global_load_dwordx4 a[100:103], v0, s[40:41] offset:-3072 sc1\n\t"
    "global_load_dwordx4 a[104:107], v0, s[40:41] offset:-2048 sc1\n\t" "global_load_dwordx4 a[108:111], v0, s[40:41] offset:-1024 sc1\n\t" "s_mov_b64 exec, s[42:43]\n\t"
    "global_store_dwordx4 v0, v[168:171], %[su] sc1\n\t" "s_mov_b64 exec, -1\n\t" "s_nop 1\n\t"
    : "+{v12}"(vbo), "=&{v[80:83]}"(xr[0]), "=&{v[84:87]}"(xr[1]), "=&{v[88:91]}"(xr[2]), "=&{v[92:95]}"(xr[3]), "=&{v[96:99]}"(xr[4]), "=&{v[100:103]}"(xr[5]), "=&{v[104:107]}"(xr[6]), "=&{v[108:111]}"(xr[7])
    : "{v0}"(va), "{v14}"(vf), "{v35}"(rr), "{v236}"(mu[0]), "{v237}"(mu[1]), "{v16}"(vr[0]), "{v17}"(vr[1]), "{v18}"(vr[2]), "{v19}"(vr[3]), "{v20}"(vr[4]), "{v21}"(vr[5]), "{v22}"(vr[6]), "{v23}"(vr[7]), "{v24}"(vr[8]), "{v25}"(vr[9]), "{v26}"(vr[10]), "{v27}"(vr[11]), "{v28}"(vr[12]), "{v29}"(vr[13]), "{v30}"(vr[14]), "{v31}"(vr[15]), "{v[40:43]}"(id[0]), "{v[44:47]}"(id[1]), [tag] "s"(tag), [par] "s"(par), [wid] "s"(wid), [sl1] "s"(sl1), [so] "s"(so), [su] "s"(su), [sa] "s"(sa), [sq] "s"(sq)
    : "v156", "v157", "v158", "v159", "v160", "v161", "v162", "v163", "v164", "v165", "v166", "v167", "v168", "v169", "v170", "v171", "v172", "v173", "v174", "v175", "v176", "v177", "v178", "v179", "v180", "v181", "v182", "v183", "v184", "v185", "v186", "v187", "v188", "v189", "v190", "v191", "v192", "v193", "v194", "v195", "a0", "a1", "a2", "a3", "a4", "a5", "a6", "a7", "a8", "a9", "a10", "a11", "a12", "a13", "a14", "a15", "a16", "a17", "a18", "a19", "a20", "a21", "a22", "a23", "a24", "a25", "a26", "a27", "a28", "a29", "a30", "a31", "a32", "a33", "a34", "a35", "a36", "a37", "a38", "a39", "a40", "a41", "a42", "a43", "a44", "a45", "a46", "a47", "a48", "a49", "a50", "a51", "a52", "a53", "a54", "a55", "a56", "a57", "a58", "a59", "a60", "a61", "a62", "a63", "a64", "a65", "a66", "a67", "a68", "a69", "a70", "a71", "a72", "a73", "a74", "a75", "a76", "a77", "a78", "a79", "a80", "a81", "a82", "a83", "a84", "a85", "a86", "a87", "a88", "a89", "a90", "a91", "a92", "a93", "a94", "a95", "a96", "a97", "a98", "a99", "a100", "a101", "a102", "a103", "a104", "a105", "a106", "a107", "a108", "a109", "a110", "a111", "a112", "a113", "a114", "a115", "a116", "a117", "a118", "a119", "a120", "a121", "a122", "a123", "a124", "a125", "a126", "a127", "a128", "a129", "a130", "a131", "a132", "a133", "a134", "a135", "a136", "a137", "a138", "a139", "a140", "a141", "a142", "a143", "a144", "a145", "a146", "a147", "a148", "a149", "a150", "a151", "a152", "a153", "a154", "a155", "a156", "a157", "a158", "a159", "a160", "a161", "a162", "a163", "a164", "a165", "a166", "a167", "a168", "a169", "a170", "a171", "a172", "a173", "a174", "a175", "a176", "a177", "a178", "a179", "a180", "a181", "a182", "a183", "a184", "a185", "a186", "a187", "a188", "a189", "a190", "a191", "s40", "s41", "s42", "s43", "s44", "s45", "s46", "s47", "s48", "s49", "s50", "s78", "s79", "s82", "s83", "vcc", "scc", "memory");
    }
    asm volatile("" : "+s"(bx), "+s"(wid), "+s"(ws)); asm volatile("" : "+v"(tid));
    IDX(); GW();
    const int lane = tid & 63, fr = lane & 15, fq = lane >> 4, col = fr * 4 + fq, role = wid == 1 ? 1 : (rb & 1) ? 3 : wid + (wid == 3 && bx == 0);
#pragma unroll
    for (int i = 0; i < 4; ++i) { const int idx = min(i * 64 + lane, 223), r = (idx * 293) >> 12; vb[i] = (unsigned)((wid & 1) * 16 * 7168 + r * (7168 - 224) + idx * 16); }
    const unsigned tw = ldsb + HALO + 1024 + wid * 512 + col * 4, tr = ldsb + HALO + 1024 + wid * 512 + fq * 16, vs = (lane & 31) * 8;
    const unsigned ve = wid == 1 ? col * 4 : (fr >> 3) * 128 + (fr & 3) * 16 + (fr & 4) * 2 + fq * 2;
    const char* sg0 = ws + (unsigned)(W_MODP + co * 1024); const char* sg1 = sg0 + 32768; const char* sg2 = sg0 + (unsigned)(16384 + (wid & 1) * 32768);
    const char* se = ws + (unsigned)(wid == 1 ? W_GB + bx * 256 : wid == 3 ? W_TAGS + EP * 8 : W_EF + rb * 2048 + co * 256 + (wid ? 0 : 64));
    const char* sy0 = ws + (unsigned)(W_Y + (rb >> 1) * 524288 + ((rb & 1) * 2 + (wid >> 1)) * YW + 2 * co * YS + (wid & 1) * 2048); const char* sy1 = sy0 + YS; const char* ss = ws + (unsigned)(W_STATS + (cb * S_ + rb * 128 + wid * 32) * 8);
    asm volatile(".p2align 3\n\t" "s_mov_b64 s[40:41], %[sa]\n\t" "s_mov_b32 s49, 0x04000c0c\n\t" "s_mov_b32 s50, 0x0706000c\n\t" "s_mov_b32 s44, 0x400000\n\t" "v_mov_b64 v[164:165], %[tag]\n\t"
    "s_waitcnt vmcnt(10)\n\t" "s_waitcnt lgkmcnt(4)\n\t" "v_mfma_f32_16x16x32_bf16 v[48:51], a[160:163], a[64:67], 0\n\t" "ds_read_b64_tr_b16 a[184:185], v18 offset:4096\n\t"
    "global_load_dwordx4 a[112:115], v0, s[40:41] offset:0 sc1\n\t" "v_mfma_f32_16x16x32_bf16 v[52:55], a[160:163], a[68:71], 0\n\t" "ds_read_b64_tr_b16 a[186:187], v26 offset:4096\n\t"
    "v_mfma_f32_16x16x32_bf16 v[60:63], a[164:167], a[68:71], 0\n\t" "ds_read_b64_tr_b16 a[188:189], v19 offset:4096\n\t" "global_load_dwordx4 a[116:119], v0, s[40:41] offset:1024 sc1\n\t"
    "v_mfma_f32_16x16x32_bf16 v[56:59], a[164:167], a[64:67], 0\n\t" "ds_read_b64_tr_b16 a[190:191], v27 offset:4096\n\t" "v_mfma_f32_16x16x32_bf16 v[64:67], a[168:171], a[64:67], 0\n\t"
    "ds_read_b64_tr_b16 a[160:161], v16 offset:8192\n\t" "global_load_dwordx4 a[120:123], v0, s[40:41] offset:2048 sc1\n\t" "v_mfma_f32_16x16x32_bf16 v[68:71], a[168:171], a[68:71], 0\n\t"
    "ds_read_b64_tr_b16 a[162:163], v24 offset:8192\n\t" "v_mfma_f32_16x16x32_bf16 v[76:79], a[172:175], a[68:71], 0\n\t" "ds_read_b64_tr_b16 a[164:165], v17 offset:8192\n\t"
    "v_mfma_f32_16x16x32_bf16 v[72:75], a[172:175], a[64:67], 0\n\t" "ds_read_b64_tr_b16 a[166:167], v25 offset:8192\n\t" "s_waitcnt lgkmcnt(4) vmcnt(11)\n\t"
    "v_mfma_f32_16x16x32_bf16 v[48:51], a[176:179], a[72:75], v[48:51]\n\t" "s_barrier\n\t" "ds_read_b64_tr_b16 a[168:169], v18 offset:8192\n\t"
    "global_load_dwordx4 a[124:127], v0, s[40:41] offset:3072 sc1\n\t" "v_mfma_f32_16x16x32_bf16 v[52:55], a[176:179], a[76:79], v[52:55]\n\t" "ds_read_b64_tr_b16 a[170:171], v26 offset:8192\n\t"
    "s_add_u32 s40, s40, 8192\n\t" "s_addc_u32 s41, s41, 0\n\t" "v_mfma_f32_16x16x32_bf16 v[60:63], a[180:183], a[76:79], v[60:63]\n\t" "ds_read_b64_tr_b16 a[172:173], v19 offset:8192\n\t"
    "v_mfma_f32_16x16x32_bf16 v[56:59], a[180:183], a[72:75], v[56:59]\n\t" "ds_read_b64_tr_b16 a[174:175], v27 offset:8192\n\t" "v_mfma_f32_16x16x32_bf16 v[64:67], a[184:187], a[72:75], v[64:67]\n\t"
    "ds_read_b64_tr_b16 a[176:177], v16 offset:12288\n\t" "global_load_dwordx4 a[128:131], v0, s[40:41] offset:-4096 sc1\n\t" "v_mfma_f32_16x16x32_bf16 v[68:71], a[184:187], a[76:79], v[68:71]\n\t"
    "ds_read_b64_tr_b16 a[178:179], v24 offset:12288\n\t" "v_mfma_f32_16x16x32_bf16 v[76:79], a[188:191], a[76:79], v[76:79]\n\t" "ds_read_b64_tr_b16 a[180:181], v17 offset:12288\n\t"
    "v_mfma_f32_16x16x32_bf16 v[72:75], a[188:191], a[72:75], v[72:75]\n\t" "ds_read_b64_tr_b16 a[182:183], v25 offset:12288\n\t" "s_waitcnt lgkmcnt(4) vmcnt(11)\n\t"
    "v_mfma_f32_16x16x32_bf16 v[48:51], a[160:163], a[80:83], v[48:51]\n\t" "ds_read_b64_tr_b16 a[184:185], v18 offset:12288\n\t" "global_load_dwordx4 a[132:135], v0, s[40:41] offset:-3072 sc1\n\t"
    "v_mfma_f32_16x16x32_bf16 v[52:55], a[160:163], a[84:87], v[52:55]\n\t" "ds_read_b64_tr_b16 a[186:187], v26 offset:12288\n\t" "v_mfma_f32_16x16x32_bf16 v[60:63], a[164:167], a[84:87], v[60:63]\n\t"
    "ds_read_b64_tr_b16 a[188:189], v19 offset:12288\n\t" "v_mfma_f32_16x16x32_bf16 v[56:59], a[164:167], a[80:83], v[56:59]\n\t" "ds_read_b64_tr_b16 a[190:191], v27 offset:12288\n\t"
    "v_mfma_f32_16x16x32_bf16 v[64:67], a[168:171], a[80:83], v[64:67]\n\t" "ds_read_b64_tr_b16 a[160:161], v16 offset:16384\n\t" "global_load_dwordx4 a[136:139], v0, s[40:41] offset:-2048 sc1\n\t"
    "v_mfma_f32_16x16x32_bf16 v[68:71], a[168:171], a[84:87], v[68:71]\n\t" "ds_read_b64_tr_b16 a[162:163], v24 offset:16384\n\t" "v_mfma_f32_16x16x32_bf16 v[76:79], a[172:175], a[84:87], v[76:79]\n\t"
    "ds_read_b64_tr_b16 a[164:165], v17 offset:16384\n\t" "v_mfma_f32_16x16x32_bf16 v[72:75], a[172:175], a[80:83], v[72:75]\n\t" "ds_read_b64_tr_b16 a[166:167], v25 offset:16384\n\t"
    "s_waitcnt lgkmcnt(4) vmcnt(11)\n\t" "v_mfma_f32_16x16x32_bf16 v[48:51], a[176:179], a[88:91], v[48:51]\n\t" "ds_read_b64_tr_b16 a[168:169], v18 offset:16384\n\t"
    "global_load_dwordx4 a[140:143], v0, s[40:41] offset:-1024 sc1\n\t" "v_mfma_f32_16x16x32_bf16 v[52:55], a[176:179], a[92:95], v[52:55]\n\t" "ds_read_b64_tr_b16 a[170:171], v26 offset:16384\n\t"
    "v_mfma_f32_16x16x32_bf16 v[60:63], a[180:183], a[92:95], v[60:63]\n\t" "ds_read_b64_tr_b16 a[172:173], v19 offset:16384\n\t" "v_mfma_f32_16x16x32_bf16 v[56:59], a[180:183], a[88:91], v[56:59]\n\t"
    "ds_read_b64_tr_b16 a[174:175], v27 offset:16384\n\t" "v_mfma_f32_16x16x32_bf16 v[64:67], a[184:187], a[88:91], v[64:67]\n\t" "ds_read_b64_tr_b16 a[176:177], v16 offset:20480\n\t"
    "global_load_dwordx4 a[144:147], v0, s[40:41] offset:0 sc1\n\t" "v_mfma_f32_16x16x32_bf16 v[68:71], a[184:187], a[92:95], v[68:71]\n\t" "ds_read_b64_tr_b16 a[178:179], v24 offset:20480\n\t"
    "v_mfma_f32_16x16x32_bf16 v[76:79], a[188:191], a[92:95], v[76:79]\n\t" "ds_read_b64_tr_b16 a[180:181], v17 offset:20480\n\t" "v_mfma_f32_16x16x32_bf16 v[72:75], a[188:191], a[88:91], v[72:75]\n\t"
    "ds_read_b64_tr_b16 a[182:183], v25 offset:20480\n\t" "s_waitcnt lgkmcnt(4) vmcnt(11)\n\t" "v_mfma_f32_16x16x32_bf16 v[48:51], a[160:163], a[96:99], v[48:51]\n\t"
    "ds_read_b64_tr_b16 a[184:185], v18 offset:20480\n\t" "global_load_dwordx4 a[148:151], v0, s[40:41] offset:1024 sc1\n\t" "v_mfma_f32_16x16x32_bf16 v[52:55], a[160:163], a[100:103], v[52:55]\n\t"
    "ds_read_b64_tr_b16 a[186:187], v26 offset:20480\n\t" "v_mfma_f32_16x16x32_bf16 v[60:63], a[164:167], a[100:103], v[60:63]\n\t" "ds_read_b64_tr_b16 a[188:189], v19 offset:20480\n\t"
    "v_mfma_f32_16x16x32_bf16 v[56:59], a[164:167], a[96:99], v[56:59]\n\t" "ds_read_b64_tr_b16 a[190:191], v27 offset:20480\n\t" "v_mfma_f32_16x16x32_bf16 v[64:67], a[168:171], a[96:99], v[64:67]\n\t"
    "ds_read_b64_tr_b16 a[160:161], v16 offset:24576\n\t" "global_load_dwordx4 a[152:155], v0, s[40:41] offset:2048 sc1\n\t" "v_mfma_f32_16x16x32_bf16 v[68:71], a[168:171], a[100:103], v[68:71]\n\t"
    "ds_read_b64_tr_b16 a[162:163], v24 offset:24576\n\t" "v_mfma_f32_16x16x32_bf16 v[76:79], a[172:175], a[100:103], v[76:79]\n\t" "ds_read_b64_tr_b16 a[164:165], v17 offset:24576\n\t"
    "v_mfma_f32_16x16x32_bf16 v[72:75], a[172:175], a[96:99], v[72:75]\n\t" "ds_read_b64_tr_b16 a[166:167], v25 offset:24576\n\t" "s_waitcnt lgkmcnt(4) vmcnt(11)\n\t"
    "v_mfma_f32_16x16x32_bf16 v[48:51], a[176:179], a[104:107], v[48:51]\n\t" "ds_read_b64_tr_b16 a[168:169], v18 offset:24576\n\t" "global_load_dwordx4 a[156:159], v0, s[40:41] offset:3072 sc1\n\t"
    "v_mfma_f32_16x16x32_bf16 v[52:55], a[176:179], a[108:111], v[52:55]\n\t" "ds_read_b64_tr_b16 a[170:171], v26 offset:24576\n\t" "s_add_u32 s40, s40, 8192\n\t" "s_addc_u32 s41, s41, 0\n\t"
    "v_mfma_f32_16x16x32_bf16 v[60:63], a[180:183], a[108:111], v[60:63]\n\t" "ds_read_b64_tr_b16 a[172:173], v19 offset:24576\n\t"
    "v_mfma_f32_16x16x32_bf16 v[56:59], a[180:183], a[104:107], v[56:59]\n\t" "ds_read_b64_tr_b16 a[174:175], v27 offset:24576\n\t"
    "v_mfma_f32_16x16x32_bf16 v[64:67], a[184:187], a[104:107], v[64:67]\n\t" "ds_read_b64_tr_b16 a[176:177], v16 offset:28672\n\t" "global_load_dwordx4 a[64:67], v0, s[40:41] offset:-4096 sc1\n\t"
    "v_mfma_f32_16x16x32_bf16 v[68:71], a[184:187], a[108:111], v[68:71]\n\t" "ds_read_b64_tr_b16 a[178:179], v24 offset:28672\n\t"
    "v_mfma_f32_16x16x32_bf16 v[76:79], a[188:191], a[108:111], v[76:79]\n\t" "ds_read_b64_tr_b16 a[180:181], v17 offset:28672\n\t"
    "v_mfma_f32_16x16x32_bf16 v[72:75], a[188:191], a[104:107], v[72:75]\n\t" "ds_read_b64_tr_b16 a[182:183], v25 offset:28672\n\t" "s_waitcnt lgkmcnt(4) vmcnt(11)\n\t"
    "v_mfma_f32_16x16x32_bf16 v[48:51], a[160:163], a[112:115], v[48:51]\n\t" "ds_read_b64_tr_b16 a[184:185], v18 offset:28672\n\t" "global_load_dwordx4 a[68:71], v0, s[40:41] offset:-3072 sc1\n\t"
    "v_mfma_f32_16x16x32_bf16 v[52:55], a[160:163], a[116:119], v[52:55]\n\t" "ds_read_b64_tr_b16 a[186:187], v26 offset:28672\n\t"
    "v_mfma_f32_16x16x32_bf16 v[60:63], a[164:167], a[116:119], v[60:63]\n\t" "ds_read_b64_tr_b16 a[188:189], v19 offset:28672\n\t"
    "v_mfma_f32_16x16x32_bf16 v[56:59], a[164:167], a[112:115], v[56:59]\n\t" "ds_read_b64_tr_b16 a[190:191], v27 offset:28672\n\t"
    "v_mfma_f32_16x16x32_bf16 v[64:67], a[168:171], a[112:115], v[64:67]\n\t" "ds_read_b64_tr_b16 a[160:161], v16 offset:32768\n\t" "global_load_dwordx4 a[72:75], v0, s[40:41] offset:-2048 sc1\n\t"
    "v_mfma_f32_16x16x32_bf16 v[68:71], a[168:171], a[116:119], v[68:71]\n\t" "ds_read_b64_tr_b16 a[162:163], v24 offset:32768\n\t"
    "v_mfma_f32_16x16x32_bf16 v[76:79], a[172:175], a[116:119], v[76:79]\n\t" "ds_read_b64_tr_b16 a[164:165], v17 offset:32768\n\t"
    "v_mfma_f32_16x16x32_bf16 v[72:75], a[172:175], a[112:115], v[72:75]\n\t" "ds_read_b64_tr_b16 a[166:167], v25 offset:32768\n\t" "s_waitcnt lgkmcnt(4) vmcnt(11)\n\t"
    "v_mfma_f32_16x16x32_bf16 v[48:51], a[176:179], a[120:123], v[48:51]\n\t" "ds_read_b64_tr_b16 a[168:169], v18 offset:32768\n\t" "global_load_dwordx4 a[76:79], v0, s[40:41] offset:-1024 sc1\n\t"
    "v_mfma_f32_16x16x32_bf16 v[52:55], a[176:179], a[124:127], v[52:55]\n\t" "ds_read_b64_tr_b16 a[170:171], v26 offset:32768\n\t"
    "v_mfma_f32_16x16x32_bf16 v[60:63], a[180:183], a[124:127], v[60:63]\n\t" "ds_read_b64_tr_b16 a[172:173], v19 offset:32768\n\t"
    "v_mfma_f32_16x16x32_bf16 v[56:59], a[180:183], a[120:123], v[56:59]\n\t" "ds_read_b64_tr_b16 a[174:175], v27 offset:32768\n\t"
    "v_mfma_f32_16x16x32_bf16 v[64:67], a[184:187], a[120:123], v[64:67]\n\t" "ds_read_b64_tr_b16 a[176:177], v16 offset:36864\n\t" "global_load_dwordx4 a[80:83], v0, s[40:41] offset:0 sc1\n\t"
    "v_mfma_f32_16x16x32_bf16 v[68:71], a[184:187], a[124:127], v[68:71]\n\t" "ds_read_b64_tr_b16 a[178:179], v24 offset:36864\n\t"
    "v_mfma_f32_16x16x32_bf16 v[76:79], a[188:191], a[124:127], v[76:79]\n\t" "ds_read_b64_tr_b16 a[180:181], v17 offset:36864\n\t"
    "v_mfma_f32_16x16x32_bf16 v[72:75], a[188:191], a[120:123], v[72:75]\n\t" "ds_read_b64_tr_b16 a[182:183], v25 offset:36864\n\t" "s_waitcnt lgkmcnt(4) vmcnt(11)\n\t"
    "v_mfma_f32_16x16x32_bf16 v[48:51], a[160:163], a[128:131], v[48:51]\n\t" "ds_read_b64_tr_b16 a[184:185], v18 offset:36864\n\t" "global_load_dwordx4 a[84:87], v0, s[40:41] offset:1024 sc1\n\t"
    "v_mfma_f32_16x16x32_bf16 v[52:55], a[160:163], a[132:135], v[52:55]\n\t" "ds_read_b64_tr_b16 a[186:187], v26 offset:36864\n\t"
    "v_mfma_f32_16x16x32_bf16 v[60:63], a[164:167], a[132:135], v[60:63]\n\t" "ds_read_b64_tr_b16 a[188:189], v19 offset:36864\n\t"
    "v_mfma_f32_16x16x32_bf16 v[56:59], a[164:167], a[128:131], v[56:59]\n\t" "ds_read_b64_tr_b16 a[190:191], v27 offset:36864\n\t"
    "v_mfma_f32_16x16x32_bf16 v[64:67], a[168:171], a[128:131], v[64:67]\n\t" "ds_read_b64_tr_b16 a[160:161], v16 offset:40960\n\t" "global_load_dwordx4 a[88:91], v0, s[40:41] offset:2048 sc1\n\t"
    "v_mfma_f32_16x16x32_bf16 v[68:71], a[168:171], a[132:135], v[68:71]\n\t" "ds_read_b64_tr_b16 a[162:163], v24 offset:40960\n\t"
    "v_mfma_f32_16x16x32_bf16 v[76:79], a[172:175], a[132:135], v[76:79]\n\t" "ds_read_b64_tr_b16 a[164:165], v17 offset:40960\n\t"
    "v_mfma_f32_16x16x32_bf16 v[72:75], a[172:175], a[128:131], v[72:75]\n\t" "ds_read_b64_tr_b16 a[166:167], v25 offset:40960\n\t" "s_waitcnt lgkmcnt(4) vmcnt(11)\n\t"
    "v_mfma_f32_16x16x32_bf16 v[48:51], a[176:179], a[136:139], v[48:51]\n\t" "ds_read_b64_tr_b16 a[168:169], v18 offset:40960\n\t" "global_load_dwordx4 a[92:95], v0, s[40:41] offset:3072 sc1\n\t"
    "v_mfma_f32_16x16x32_bf16 v[52:55], a[176:179], a[140:143], v[52:55]\n\t" "ds_read_b64_tr_b16 a[170:171], v26 offset:40960\n\t" "s_add_u32 s40, s40, 8192\n\t" "s_addc_u32 s41, s41, 0\n\t"
    "v_mfma_f32_16x16x32_bf16 v[60:63], a[180:183], a[140:143], v[60:63]\n\t" "ds_read_b64_tr_b16 a[172:173], v19 offset:40960\n\t"
    "v_mfma_f32_16x16x32_bf16 v[56:59], a[180:183], a[136:139], v[56:59]\n\t" "ds_read_b64_tr_b16 a[174:175], v27 offset:40960\n\t"
    "v_mfma_f32_16x16x32_bf16 v[64:67], a[184:187], a[136:139], v[64:67]\n\t" "ds_read_b64_tr_b16 a[176:177], v16 offset:45056\n\t" "global_load_dwordx4 a[96:99], v0, s[40:41] offset:-4096 sc1\n\t"
    "v_mfma_f32_16x16x32_bf16 v[68:71], a[184:187], a[140:143], v[68:71]\n\t" "ds_read_b64_tr_b16 a[178:179], v24 offset:45056\n\t"
    "v_mfma_f32_16x16x32_bf16 v[76:79], a[188:191], a[140:143], v[76:79]\n\t" "ds_read_b64_tr_b16 a[180:181], v17 offset:45056\n\t"
    "v_mfma_f32_16x16x32_bf16 v[72:75], a[188:191], a[136:139], v[72:75]\n\t" "ds_read_b64_tr_b16 a[182:183], v25 offset:45056\n\t" "s_waitcnt lgkmcnt(4) vmcnt(11)\n\t"
    "v_mfma_f32_16x16x32_bf16 v[48:51], a[160:163], a[144:147], v[48:51]\n\t" "ds_read_b64_tr_b16 a[184:185], v18 offset:45056\n\t" "global_load_dwordx4 a[100:103], v0, s[40:41] offset:-3072 sc1\n\t"
    "v_mfma_f32_16x16x32_bf16 v[52:55], a[160:163], a[148:151], v[52:55]\n\t" "ds_read_b64_tr_b16 a[186:187], v26 offset:45056\n\t" "global_load_dwordx4 v[112:115], v0, %[sg0] sc1\n\t"
    "v_mfma_f32_16x16x32_bf16 v[60:63], a[164:167], a[148:151], v[60:63]\n\t" "ds_read_b64_tr_b16 a[188:189], v19 offset:45056\n\t"
    "v_mfma_f32_16x16x32_bf16 v[56:59], a[164:167], a[144:147], v[56:59]\n\t" "ds_read_b64_tr_b16 a[190:191], v27 offset:45056\n\t" "global_load_dwordx4 v[116:119], v0, %[sg1] sc1\n\t"
    "v_mfma_f32_16x16x32_bf16 v[64:67], a[168:171], a[144:147], v[64:67]\n\t" "ds_read_b64_tr_b16 a[160:161], v16 offset:49152\n\t" "global_load_dwordx4 a[104:107], v0, s[40:41] offset:-2048 sc1\n\t"
    "v_mfma_f32_16x16x32_bf16 v[68:71], a[168:171], a[148:151], v[68:71]\n\t" "ds_read_b64_tr_b16 a[162:163], v24 offset:49152\n\t" "global_load_dwordx4 v[120:123], v0, %[sg2] sc1\n\t"
    "v_mfma_f32_16x16x32_bf16 v[76:79], a[172:175], a[148:151], v[76:79]\n\t" "ds_read_b64_tr_b16 a[164:165], v17 offset:49152\n\t"
    "v_mfma_f32_16x16x32_bf16 v[72:75], a[172:175], a[144:147], v[72:75]\n\t" "ds_read_b64_tr_b16 a[166:167], v25 offset:49152\n\t" "global_load_dword a0, v0, %[cd]\n\t"
    "s_waitcnt lgkmcnt(4) vmcnt(15)\n\t" "v_mfma_f32_16x16x32_bf16 v[48:51], a[176:179], a[152:155], v[48:51]\n\t" "ds_read_b64_tr_b16 a[168:169], v18 offset:49152\n\t"
    "global_load_dwordx4 a[108:111], v0, s[40:41] offset:-1024 sc1\n\t" "v_mfma_f32_16x16x32_bf16 v[52:55], a[176:179], a[156:159], v[52:55]\n\t" "ds_read_b64_tr_b16 a[170:171], v26 offset:49152\n\t"
    "v_mfma_f32_16x16x32_bf16 v[60:63], a[180:183], a[156:159], v[60:63]\n\t" "ds_read_b64_tr_b16 a[172:173], v19 offset:49152\n\t"
    "v_mfma_f32_16x16x32_bf16 v[56:59], a[180:183], a[152:155], v[56:59]\n\t" "ds_read_b64_tr_b16 a[174:175], v27 offset:49152\n\t"
    "v_mfma_f32_16x16x32_bf16 v[64:67], a[184:187], a[152:155], v[64:67]\n\t" "ds_read_b64_tr_b16 a[176:177], v16 offset:53248\n\t" "global_load_dwordx4 a[112:115], v0, s[40:41] offset:0 sc1\n\t"
    "v_mfma_f32_16x16x32_bf16 v[68:71], a[184:187], a[156:159], v[68:71]\n\t" "ds_read_b64_tr_b16 a[178:179], v24 offset:53248\n\t"
    "v_mfma_f32_16x16x32_bf16 v[76:79], a[188:191], a[156:159], v[76:79]\n\t" "ds_read_b64_tr_b16 a[180:181], v17 offset:53248\n\t"
    "v_mfma_f32_16x16x32_bf16 v[72:75], a[188:191], a[152:155], v[72:75]\n\t" "ds_read_b64_tr_b16 a[182:183], v25 offset:53248\n\t" "s_waitcnt lgkmcnt(4) vmcnt(15)\n\t"
    "v_mfma_f32_16x16x32_bf16 v[48:51], a[160:163], a[64:67], v[48:51]\n\t" "ds_read_b64_tr_b16 a[184:185], v18 offset:53248\n\t" "global_load_dwordx4 a[116:119], v0, s[40:41] offset:1024 sc1\n\t"
    "v_mfma_f32_16x16x32_bf16 v[52:55], a[160:163], a[68:71], v[52:55]\n\t" "ds_read_b64_tr_b16 a[186:187], v26 offset:53248\n\t" "v_mfma_f32_16x16x32_bf16 v[60:63], a[164:167], a[68:71], v[60:63]\n\t"
    "ds_read_b64_tr_b16 a[188:189], v19 offset:53248\n\t" "v_mfma_f32_16x16x32_bf16 v[56:59], a[164:167], a[64:67], v[56:59]\n\t" "ds_read_b64_tr_b16 a[190:191], v27 offset:53248\n\t"
    "v_mfma_f32_16x16x32_bf16 v[64:67], a[168:171], a[64:67], v[64:67]\n\t" "ds_read_b64_tr_b16 a[160:161], v16 offset:57344\n\t" "global_load_dwordx4 a[120:123], v0, s[40:41] offset:2048 sc1\n\t"
    "v_mfma_f32_16x16x32_bf16 v[68:71], a[168:171], a[68:71], v[68:71]\n\t" "ds_read_b64_tr_b16 a[162:163], v24 offset:57344\n\t" "v_mfma_f32_16x16x32_bf16 v[76:79], a[172:175], a[68:71], v[76:79]\n\t"
    "ds_read_b64_tr_b16 a[164:165], v17 offset:57344\n\t" "v_mfma_f32_16x16x32_bf16 v[72:75], a[172:175], a[64:67], v[72:75]\n\t" "ds_read_b64_tr_b16 a[166:167], v25 offset:57344\n\t"
    "s_waitcnt lgkmcnt(4) vmcnt(15)\n\t" "v_mfma_f32_16x16x32_bf16 v[48:51], a[176:179], a[72:75], v[48:51]\n\t" "ds_read_b64_tr_b16 a[168:169], v18 offset:57344\n\t"
    "global_load_dwordx4 a[124:127], v0, s[40:41] offset:3072 sc1\n\t" "v_mfma_f32_16x16x32_bf16 v[52:55], a[176:179], a[76:79], v[52:55]\n\t" "ds_read_b64_tr_b16 a[170:171], v26 offset:57344\n\t"
    "s_add_u32 s40, s40, 8192\n\t" "s_addc_u32 s41, s41, 0\n\t" "v_mfma_f32_16x16x32_bf16 v[60:63], a[180:183], a[76:79], v[60:63]\n\t" "ds_read_b64_tr_b16 a[172:173], v19 offset:57344\n\t"
    "v_mfma_f32_16x16x32_bf16 v[56:59], a[180:183], a[72:75], v[56:59]\n\t" "ds_read_b64_tr_b16 a[174:175], v27 offset:57344\n\t" "v_mfma_f32_16x16x32_bf16 v[64:67], a[184:187], a[72:75], v[64:67]\n\t"
    "ds_read_b64_tr_b16 a[176:177], v16 offset:61440\n\t" "global_load_dwordx4 a[128:131], v0, s[40:41] offset:-4096 sc1\n\t" "v_mfma_f32_16x16x32_bf16 v[68:71], a[184:187], a[76:79], v[68:71]\n\t"
    "ds_read_b64_tr_b16 a[178:179], v24 offset:61440\n\t" "v_mfma_f32_16x16x32_bf16 v[76:79], a[188:191], a[76:79], v[76:79]\n\t" "ds_read_b64_tr_b16 a[180:181], v17 offset:61440\n\t"
    "v_mfma_f32_16x16x32_bf16 v[72:75], a[188:191], a[72:75], v[72:75]\n\t" "ds_read_b64_tr_b16 a[182:183], v25 offset:61440\n\t" "s_waitcnt lgkmcnt(4) vmcnt(15)\n\t"
    "v_mfma_f32_16x16x32_bf16 v[48:51], a[160:163], a[80:83], v[48:51]\n\t" "ds_read_b64_tr_b16 a[184:185], v18 offset:61440\n\t" "global_load_dwordx4 a[132:135], v0, s[40:41] offset:-3072 sc1\n\t"
    "v_mfma_f32_16x16x32_bf16 v[52:55], a[160:163], a[84:87], v[52:55]\n\t" "ds_read_b64_tr_b16 a[186:187], v26 offset:61440\n\t" "v_mfma_f32_16x16x32_bf16 v[60:63], a[164:167], a[84:87], v[60:63]\n\t"
    "ds_read_b64_tr_b16 a[188:189], v19 offset:61440\n\t" "v_mfma_f32_16x16x32_bf16 v[56:59], a[164:167], a[80:83], v[56:59]\n\t" "ds_read_b64_tr_b16 a[190:191], v27 offset:61440\n\t"
    "v_mfma_f32_16x16x32_bf16 v[64:67], a[168:171], a[80:83], v[64:67]\n\t" "ds_read_b64_tr_b16 a[160:161], v20 offset:0\n\t" "global_load_dwordx4 a[136:139], v0, s[40:41] offset:-2048 sc1\n\t"
    "v_mfma_f32_16x16x32_bf16 v[68:71], a[168:171], a[84:87], v[68:71]\n\t" "ds_read_b64_tr_b16 a[162:163], v28 offset:0\n\t" "v_mfma_f32_16x16x32_bf16 v[76:79], a[172:175], a[84:87], v[76:79]\n\t"
    "ds_read_b64_tr_b16 a[164:165], v21 offset:0\n\t" "v_mfma_f32_16x16x32_bf16 v[72:75], a[172:175], a[80:83], v[72:75]\n\t" "ds_read_b64_tr_b16 a[166:167], v29 offset:0\n\t"
    "s_waitcnt lgkmcnt(4) vmcnt(15)\n\t" "v_mfma_f32_16x16x32_bf16 v[48:51], a[176:179], a[88:91], v[48:51]\n\t" "ds_read_b64_tr_b16 a[168:169], v22 offset:0\n\t"
    "global_load_dwordx4 a[140:143], v0, s[40:41] offset:-1024 sc1\n\t" "v_mfma_f32_16x16x32_bf16 v[52:55], a[176:179], a[92:95], v[52:55]\n\t" "ds_read_b64_tr_b16 a[170:171], v30 offset:0\n\t"
    "v_mfma_f32_16x16x32_bf16 v[60:63], a[180:183], a[92:95], v[60:63]\n\t" "ds_read_b64_tr_b16 a[172:173], v23 offset:0\n\t" "v_mfma_f32_16x16x32_bf16 v[56:59], a[180:183], a[88:91], v[56:59]\n\t"
    "ds_read_b64_tr_b16 a[174:175], v31 offset:0\n\t" "v_mfma_f32_16x16x32_bf16 v[64:67], a[184:187], a[88:91], v[64:67]\n\t" "ds_read_b64_tr_b16 a[176:177], v20 offset:4096\n\t"
    "global_load_dwordx4 a[144:147], v0, s[40:41] offset:0 sc1\n\t" "v_mfma_f32_16x16x32_bf16 v[68:71], a[184:187], a[92:95], v[68:71]\n\t" "ds_read_b64_tr_b16 a[178:179], v28 offset:4096\n\t"
    "v_mfma_f32_16x16x32_bf16 v[76:79], a[188:191], a[92:95], v[76:79]\n\t" "ds_read_b64_tr_b16 a[180:181], v21 offset:4096\n\t" "v_mfma_f32_16x16x32_bf16 v[72:75], a[188:191], a[88:91], v[72:75]\n\t"
    "ds_read_b64_tr_b16 a[182:183], v29 offset:4096\n\t" "s_waitcnt lgkmcnt(4) vmcnt(15)\n\t" "v_mfma_f32_16x16x32_bf16 v[48:51], a[160:163], a[96:99], v[48:51]\n\t"
    "ds_read_b64_tr_b16 a[184:185], v22 offset:4096\n\t" "global_load_dwordx4 a[148:151], v0, s[40:41] offset:1024 sc1\n\t" "s_waitcnt vmcnt(12)\n\t"
    "v_mfma_f32_16x16x32_bf16 v[52:55], a[160:163], a[100:103], v[52:55]\n\t" "ds_read_b64_tr_b16 a[186:187], v30 offset:4096\n\t" "v_xad_u32 v156, v112, v113, v164\n\t"
    "v_xad_u32 v157, v116, v117, v164\n\t" "v_mfma_f32_16x16x32_bf16 v[60:63], a[164:167], a[100:103], v[60:63]\n\t" "ds_read_b64_tr_b16 a[188:189], v23 offset:4096\n\t"
    "v_xad_u32 v158, v120, v121, v164\n\t" "v_xad_u32 v156, v114, v113, v156\n\t" "v_mfma_f32_16x16x32_bf16 v[56:59], a[164:167], a[96:99], v[56:59]\n\t"
    "ds_read_b64_tr_b16 a[190:191], v31 offset:4096\n\t" "v_xad_u32 v157, v118, v117, v157\n\t" "v_xad_u32 v158, v122, v121, v158\n\t"
    "v_mfma_f32_16x16x32_bf16 v[64:67], a[168:171], a[96:99], v[64:67]\n\t" "ds_read_b64_tr_b16 a[160:161], v20 offset:8192\n\t" "global_load_dwordx4 a[152:155], v0, s[40:41] offset:2048 sc1\n\t"
    "v_xor_b32_e64 v156, v156, v115\n\t" "v_mfma_f32_16x16x32_bf16 v[68:71], a[168:171], a[100:103], v[68:71]\n\t" "ds_read_b64_tr_b16 a[162:163], v28 offset:8192\n\t"
    "v_bitop3_b32 v156, v156, v157, v119 bitop3:0xf6\n\t" "v_bitop3_b32 v156, v156, v158, v123 bitop3:0xf6\n\t" "v_mfma_f32_16x16x32_bf16 v[76:79], a[172:175], a[100:103], v[76:79]\n\t"
    "ds_read_b64_tr_b16 a[164:165], v21 offset:8192\n\t" "v_cmp_ne_u32_e64 vcc, 0, v156\n\t" "v_perm_b32 v115, v112, v113, s49\n\t" "v_mfma_f32_16x16x32_bf16 v[72:75], a[172:175], a[96:99], v[72:75]\n\t"
    "ds_read_b64_tr_b16 a[166:167], v29 offset:8192\n\t" "v_perm_b32 v119, v116, v117, s49\n\t" "v_perm_b32 v123, v120, v121, s49\n\t" "s_waitcnt lgkmcnt(4) vmcnt(11)\n\t"
    "v_mfma_f32_16x16x32_bf16 v[48:51], a[176:179], a[104:107], v[48:51]\n\t" "ds_read_b64_tr_b16 a[168:169], v22 offset:8192\n\t" "global_load_dwordx4 a[156:159], v0, s[40:41] offset:3072 sc1\n\t"
    "s_nop 1\n\t" "v_mfma_f32_16x16x32_bf16 v[52:55], a[176:179], a[108:111], v[52:55]\n\t" "ds_read_b64_tr_b16 a[170:171], v30 offset:8192\n\t" "s_add_u32 s40, s40, 8192\n\t" "s_addc_u32 s41, s41, 0\n\t"
    "v_mfma_f32_16x16x32_bf16 v[60:63], a[180:183], a[108:111], v[60:63]\n\t" "ds_read_b64_tr_b16 a[172:173], v23 offset:8192\n\t" "s_cbranch_vccnz .Lslow_%=\n\t" ".Lgo_%=:\n\t"
    "v_mfma_f32_16x16x32_bf16 v[56:59], a[180:183], a[104:107], v[56:59]\n\t" "ds_read_b64_tr_b16 a[174:175], v31 offset:8192\n\t" "v_perm_b32 v115, v115, v114, s50\n\t"
    "v_perm_b32 v119, v119, v118, s50\n\t" "v_mfma_f32_16x16x32_bf16 v[64:67], a[184:187], a[104:107], v[64:67]\n\t" "ds_read_b64_tr_b16 a[176:177], v20 offset:12288\n\t"
    "global_load_dwordx4 a[64:67], v0, s[40:41] offset:-4096 sc1\n\t" "v_perm_b32 v123, v123, v122, s50\n\t" "v_mfma_f32_16x16x32_bf16 v[68:71], a[184:187], a[108:111], v[68:71]\n\t"
    "ds_read_b64_tr_b16 a[178:179], v28 offset:12288\n\t" "v_permlane32_swap_b32 v112, v114\n\t" "v_permlane32_swap_b32 v116, v118\n\t"
    "v_mfma_f32_16x16x32_bf16 v[76:79], a[188:191], a[108:111], v[76:79]\n\t" "ds_read_b64_tr_b16 a[180:181], v21 offset:12288\n\t" "v_permlane32_swap_b32 v120, v122\n\t"
    "v_permlane32_swap_b32 v113, v115\n\t" "v_mfma_f32_16x16x32_bf16 v[72:75], a[188:191], a[104:107], v[72:75]\n\t" "ds_read_b64_tr_b16 a[182:183], v29 offset:12288\n\t"
    "v_permlane32_swap_b32 v117, v119\n\t" "v_permlane32_swap_b32 v121, v123\n\t" "s_waitcnt lgkmcnt(4) vmcnt(11)\n\t" "v_mfma_f32_16x16x32_bf16 v[48:51], a[160:163], a[112:115], v[48:51]\n\t"
    "ds_read_b64_tr_b16 a[184:185], v22 offset:12288\n\t" "global_load_dwordx4 a[68:71], v0, s[40:41] offset:-3072 sc1\n\t" "v_add_f32 v112, v112, v114\n\t"
    "v_mfma_f32_16x16x32_bf16 v[52:55], a[160:163], a[116:119], v[52:55]\n\t" "ds_read_b64_tr_b16 a[186:187], v30 offset:12288\n\t" "v_add_f32 v116, v116, v118\n\t" "v_add_f32 v120, v120, v122\n\t"
    "v_mfma_f32_16x16x32_bf16 v[60:63], a[164:167], a[116:119], v[60:63]\n\t" "ds_read_b64_tr_b16 a[188:189], v23 offset:12288\n\t" "v_add_f32 v113, v113, v115\n\t" "v_add_f32 v117, v117, v119\n\t"
    "v_mfma_f32_16x16x32_bf16 v[56:59], a[164:167], a[112:115], v[56:59]\n\t" "ds_read_b64_tr_b16 a[190:191], v31 offset:12288\n\t" "v_add_f32 v121, v121, v123\n\t" "v_permlane16_swap_b32 v112, v113\n\t"
    "v_mfma_f32_16x16x32_bf16 v[64:67], a[168:171], a[112:115], v[64:67]\n\t" "ds_read_b64_tr_b16 a[160:161], v20 offset:16384\n\t" "global_load_dwordx4 a[72:75], v0, s[40:41] offset:-2048 sc1\n\t"
    "v_permlane16_swap_b32 v116, v117\n\t" "v_mfma_f32_16x16x32_bf16 v[68:71], a[168:171], a[116:119], v[68:71]\n\t" "ds_read_b64_tr_b16 a[162:163], v28 offset:16384\n\t"
    "v_permlane16_swap_b32 v120, v121\n\t" "v_add_f32_e64 v112, v112, v113\n\t" "v_mfma_f32_16x16x32_bf16 v[76:79], a[172:175], a[116:119], v[76:79]\n\t"
    "ds_read_b64_tr_b16 a[164:165], v21 offset:16384\n\t" "v_add_f32 v116, v116, v117\n\t" "v_add_f32 v120, v120, v121\n\t" "v_mfma_f32_16x16x32_bf16 v[72:75], a[172:175], a[112:115], v[72:75]\n\t"
    "ds_read_b64_tr_b16 a[166:167], v29 offset:16384\n\t" "v_cvt_pk_bf16_f32 v112, v112, v112\n\t" "v_cvt_pk_bf16_f32 v116, v116, v116\n\t" "s_waitcnt lgkmcnt(4) vmcnt(11)\n\t"
    "v_mfma_f32_16x16x32_bf16 v[48:51], a[176:179], a[120:123], v[48:51]\n\t" "ds_read_b64_tr_b16 a[168:169], v22 offset:16384\n\t" "global_load_dwordx4 a[76:79], v0, s[40:41] offset:-1024 sc1\n\t"
    "v_cvt_pk_bf16_f32 v120, v120, v120\n\t" "v_mfma_f32_16x16x32_bf16 v[52:55], a[176:179], a[124:127], v[52:55]\n\t" "ds_read_b64_tr_b16 a[170:171], v30 offset:16384\n\t"
    "v_and_b32 v112, 0xffff0000, v112\n\t" "v_and_b32 v116, 0xffff0000, v116\n\t" "v_mfma_f32_16x16x32_bf16 v[60:63], a[180:183], a[124:127], v[60:63]\n\t"
    "ds_read_b64_tr_b16 a[172:173], v23 offset:16384\n\t" "v_and_b32 v120, 0xffff0000, v120\n\t" "v_add_f32 v116, 1.0, v116\n\t" "v_mfma_f32_16x16x32_bf16 v[56:59], a[180:183], a[120:123], v[56:59]\n\t"
    "ds_read_b64_tr_b16 a[174:175], v31 offset:16384\n\t" "v_cvt_pk_bf16_f32 v116, v116, v116\n\t" "v_and_b32 v116, 0xffff0000, v116\n\t"
    "v_mfma_f32_16x16x32_bf16 v[64:67], a[184:187], a[120:123], v[64:67]\n\t" "ds_read_b64_tr_b16 a[176:177], v20 offset:20480\n\t" "global_load_dwordx4 a[80:83], v0, s[40:41] offset:0 sc1\n\t"
    "ds_write2st64_b32 v15, v112, v116 offset1:1\n\t" "v_mfma_f32_16x16x32_bf16 v[68:71], a[184:187], a[124:127], v[68:71]\n\t" "ds_read_b64_tr_b16 a[178:179], v28 offset:20480\n\t"
    "s_cmp_eq_u32 %[role], 0\n\t" "s_cselect_b64 exec, -1, 0\n\t" "global_store_short_d16_hi v33, v120, %[se] sc1\n\t" "s_mov_b64 exec, -1\n\t"
    "v_mfma_f32_16x16x32_bf16 v[76:79], a[188:191], a[124:127], v[76:79]\n\t" "ds_read_b64_tr_b16 a[180:181], v21 offset:20480\n\t" "s_cmp_eq_u32 %[role], 1\n\t" "s_cselect_b64 exec, -1, 0\n\t"
    "global_store_dword v33, v120, %[se] sc1\n\t" "s_mov_b64 exec, -1\n\t" "v_mfma_f32_16x16x32_bf16 v[72:75], a[188:191], a[120:123], v[72:75]\n\t" "ds_read_b64_tr_b16 a[182:183], v29 offset:20480\n\t"
    "s_cmp_eq_u32 %[role], 2\n\t" "s_cselect_b64 exec, -1, 0\n\t" "global_store_short_d16_hi v33, v116, %[se] sc1\n\t" "s_mov_b64 exec, -1\n\t" "s_waitcnt lgkmcnt(5) vmcnt(11)\n\t"
    "v_mfma_f32_16x16x32_bf16 v[48:51], a[160:163], a[128:131], v[48:51]\n\t" "ds_read_b64_tr_b16 a[184:185], v22 offset:20480\n\t" "global_load_dwordx4 a[84:87], v0, s[40:41] offset:1024 sc1\n\t"
    "s_cmp_eq_u32 %[role], 4\n\t" "s_cselect_b64 exec, 1, 0\n\t" "global_store_dwordx2 v0, v[164:165], %[se]\n\t" "s_mov_b64 exec, -1\n\t"
    "v_mfma_f32_16x16x32_bf16 v[52:55], a[160:163], a[132:135], v[52:55]\n\t" "ds_read_b64_tr_b16 a[186:187], v30 offset:20480\n\t" "ds_read_b128 v[124:127], v32 offset:0\n\t"
    "ds_read_b128 v[128:131], v32 offset:64\n\t" "v_mfma_f32_16x16x32_bf16 v[60:63], a[164:167], a[132:135], v[60:63]\n\t" "ds_read_b64_tr_b16 a[188:189], v23 offset:20480\n\t"
    "ds_read_b128 v[132:135], v32 offset:128\n\t" "ds_read_b128 v[136:139], v32 offset:192\n\t" "v_mfma_f32_16x16x32_bf16 v[56:59], a[164:167], a[128:131], v[56:59]\n\t"
    "ds_read_b64_tr_b16 a[190:191], v31 offset:20480\n\t" "ds_read_b128 v[140:143], v32 offset:256\n\t" "ds_read_b128 v[144:147], v32 offset:320\n\t"
    "v_mfma_f32_16x16x32_bf16 v[64:67], a[168:171], a[128:131], v[64:67]\n\t" "s_waitcnt lgkmcnt(11)\n\t" "ds_read_b64_tr_b16 a[160:161], v20 offset:24576\n\t"
    "global_load_dwordx4 a[88:91], v0, s[40:41] offset:2048 sc1\n\t" "v_mfma_f32_16x16x32_bf16 v[68:71], a[168:171], a[132:135], v[68:71]\n\t" "ds_read_b64_tr_b16 a[162:163], v28 offset:24576\n\t"
    "ds_read_b128 v[148:151], v32 offset:384\n\t" "ds_read_b128 v[152:155], v32 offset:448\n\t" "v_mfma_f32_16x16x32_bf16 v[76:79], a[172:175], a[132:135], v[76:79]\n\t" "s_waitcnt lgkmcnt(11)\n\t"
    "ds_read_b64_tr_b16 a[164:165], v21 offset:24576\n\t" "v_mfma_f32_16x16x32_bf16 v[72:75], a[172:175], a[128:131], v[72:75]\n\t" "ds_read_b64_tr_b16 a[166:167], v29 offset:24576\n\t"
    "s_waitcnt lgkmcnt(8) vmcnt(11)\n\t" "v_mfma_f32_16x16x32_bf16 v[48:51], a[176:179], a[136:139], v[48:51]\n\t" "ds_read_b64_tr_b16 a[168:169], v22 offset:24576\n\t"
    "global_load_dwordx4 a[92:95], v0, s[40:41] offset:3072 sc1\n\t" "v_mfma_f32_16x16x32_bf16 v[52:55], a[176:179], a[140:143], v[52:55]\n\t" "ds_read_b64_tr_b16 a[170:171], v30 offset:24576\n\t"
    "s_add_u32 s40, s40, 8192\n\t" "s_addc_u32 s41, s41, 0\n\t" "v_mfma_f32_16x16x32_bf16 v[60:63], a[180:183], a[140:143], v[60:63]\n\t" "ds_read_b64_tr_b16 a[172:173], v23 offset:24576\n\t"
    "v_mfma_f32_16x16x32_bf16 v[56:59], a[180:183], a[136:139], v[56:59]\n\t" "ds_read_b64_tr_b16 a[174:175], v31 offset:24576\n\t"
    "v_mfma_f32_16x16x32_bf16 v[64:67], a[184:187], a[136:139], v[64:67]\n\t" "ds_read_b64_tr_b16 a[176:177], v20 offset:28672\n\t" "global_load_dwordx4 a[96:99], v0, s[40:41] offset:-4096 sc1\n\t"
    "v_mfma_f32_16x16x32_bf16 v[68:71], a[184:187], a[140:143], v[68:71]\n\t" "ds_read_b64_tr_b16 a[178:179], v28 offset:28672\n\t"
    "v_mfma_f32_16x16x32_bf16 v[76:79], a[188:191], a[140:143], v[76:79]\n\t" "ds_read_b64_tr_b16 a[180:181], v21 offset:28672\n\t"
    "v_mfma_f32_16x16x32_bf16 v[72:75], a[188:191], a[136:139], v[72:75]\n\t" "s_waitcnt lgkmcnt(11)\n\t" "ds_read_b64_tr_b16 a[182:183], v29 offset:28672\n\t" "s_waitcnt lgkmcnt(4) vmcnt(11)\n\t"
    "v_mfma_f32_16x16x32_bf16 v[48:51], a[160:163], a[144:147], v[48:51]\n\t" "ds_read_b64_tr_b16 a[184:185], v22 offset:28672\n\t" "global_load_dwordx4 a[100:103], v0, s[40:41] offset:-3072 sc1\n\t"
    "v_mfma_f32_16x16x32_bf16 v[52:55], a[160:163], a[148:151], v[52:55]\n\t" "ds_read_b64_tr_b16 a[186:187], v30 offset:28672\n\t"
    "v_mfma_f32_16x16x32_bf16 v[60:63], a[164:167], a[148:151], v[60:63]\n\t" "ds_read_b64_tr_b16 a[188:189], v23 offset:28672\n\t"
    "v_mfma_f32_16x16x32_bf16 v[56:59], a[164:167], a[144:147], v[56:59]\n\t" "ds_read_b64_tr_b16 a[190:191], v31 offset:28672\n\t"
    "v_mfma_f32_16x16x32_bf16 v[64:67], a[168:171], a[144:147], v[64:67]\n\t" "ds_read_b64_tr_b16 a[160:161], v20 offset:32768\n\t" "global_load_dwordx4 a[104:107], v0, s[40:41] offset:-2048 sc1\n\t"
    "v_mfma_f32_16x16x32_bf16 v[68:71], a[168:171], a[148:151], v[68:71]\n\t" "ds_read_b64_tr_b16 a[162:163], v28 offset:32768\n\t"
    "v_mfma_f32_16x16x32_bf16 v[76:79], a[172:175], a[148:151], v[76:79]\n\t" "ds_read_b64_tr_b16 a[164:165], v21 offset:32768\n\t"
    "v_mfma_f32_16x16x32_bf16 v[72:75], a[172:175], a[144:147], v[72:75]\n\t" "ds_read_b64_tr_b16 a[166:167], v29 offset:32768\n\t" "s_waitcnt lgkmcnt(4) vmcnt(11)\n\t"
    "v_mfma_f32_16x16x32_bf16 v[48:51], a[176:179], a[152:155], v[48:51]\n\t" "ds_read_b64_tr_b16 a[168:169], v22 offset:32768\n\t" "global_load_dwordx4 a[108:111], v0, s[40:41] offset:-1024 sc1\n\t"
    "v_mfma_f32_16x16x32_bf16 v[52:55], a[176:179], a[156:159], v[52:55]\n\t" "ds_read_b64_tr_b16 a[170:171], v30 offset:32768\n\t"
    "v_mfma_f32_16x16x32_bf16 v[60:63], a[180:183], a[156:159], v[60:63]\n\t" "ds_read_b64_tr_b16 a[172:173], v23 offset:32768\n\t"
    "v_mfma_f32_16x16x32_bf16 v[56:59], a[180:183], a[152:155], v[56:59]\n\t" "ds_read_b64_tr_b16 a[174:175], v31 offset:32768\n\t"
    "v_mfma_f32_16x16x32_bf16 v[64:67], a[184:187], a[152:155], v[64:67]\n\t" "ds_read_b64_tr_b16 a[176:177], v20 offset:36864\n\t" "global_load_dwordx4 a[112:115], v0, s[40:41] offset:0 sc1\n\t"
    "v_mfma_f32_16x16x32_bf16 v[68:71], a[184:187], a[156:159], v[68:71]\n\t" "ds_read_b64_tr_b16 a[178:179], v28 offset:36864\n\t"
    "v_mfma_f32_16x16x32_bf16 v[76:79], a[188:191], a[156:159], v[76:79]\n\t" "ds_read_b64_tr_b16 a[180:181], v21 offset:36864\n\t"
    "v_mfma_f32_16x16x32_bf16 v[72:75], a[188:191], a[152:155], v[72:75]\n\t" "ds_read_b64_tr_b16 a[182:183], v29 offset:36864\n\t" "s_waitcnt lgkmcnt(4) vmcnt(11)\n\t"
    "v_mfma_f32_16x16x32_bf16 v[48:51], a[160:163], a[64:67], v[48:51]\n\t" "ds_read_b64_tr_b16 a[184:185], v22 offset:36864\n\t" "global_load_dwordx4 a[116:119], v0, s[40:41] offset:1024 sc1\n\t"
    "v_mfma_f32_16x16x32_bf16 v[52:55], a[160:163], a[68:71], v[52:55]\n\t" "ds_read_b64_tr_b16 a[186:187], v30 offset:36864\n\t" "v_mfma_f32_16x16x32_bf16 v[60:63], a[164:167], a[68:71], v[60:63]\n\t"
    "ds_read_b64_tr_b16 a[188:189], v23 offset:36864\n\t" "v_mfma_f32_16x16x32_bf16 v[56:59], a[164:167], a[64:67], v[56:59]\n\t" "ds_read_b64_tr_b16 a[190:191], v31 offset:36864\n\t"
    "v_mfma_f32_16x16x32_bf16 v[64:67], a[168:171], a[64:67], v[64:67]\n\t" "ds_read_b64_tr_b16 a[160:161], v20 offset:40960\n\t" "global_load_dwordx4 a[120:123], v0, s[40:41] offset:2048 sc1\n\t"
    "v_mfma_f32_16x16x32_bf16 v[68:71], a[168:171], a[68:71], v[68:71]\n\t" "ds_read_b64_tr_b16 a[162:163], v28 offset:40960\n\t" "v_mfma_f32_16x16x32_bf16 v[76:79], a[172:175], a[68:71], v[76:79]\n\t"
    "ds_read_b64_tr_b16 a[164:165], v21 offset:40960\n\t" "v_mfma_f32_16x16x32_bf16 v[72:75], a[172:175], a[64:67], v[72:75]\n\t" "ds_read_b64_tr_b16 a[166:167], v29 offset:40960\n\t"
    "s_waitcnt lgkmcnt(4) vmcnt(11)\n\t" "v_mfma_f32_16x16x32_bf16 v[48:51], a[176:179], a[72:75], v[48:51]\n\t" "ds_read_b64_tr_b16 a[168:169], v22 offset:40960\n\t"
    "global_load_dwordx4 a[124:127], v0, s[40:41] offset:3072 sc1\n\t" "v_mfma_f32_16x16x32_bf16 v[52:55], a[176:179], a[76:79], v[52:55]\n\t" "ds_read_b64_tr_b16 a[170:171], v30 offset:40960\n\t"
    "s_add_u32 s40, s40, 8192\n\t" "s_addc_u32 s41, s41, 0\n\t" "v_mfma_f32_16x16x32_bf16 v[60:63], a[180:183], a[76:79], v[60:63]\n\t" "ds_read_b64_tr_b16 a[172:173], v23 offset:40960\n\t"
    "s_mov_b64 s[78:79], %[sb]\n\t" "s_mov_b32 s82, -1\n\t" "s_mov_b32 s83, 0\n\t" "s_add_u32 m0, %[sl], 86016\n\t" "s_nop 0\n\t" "global_load_lds_dwordx4 v1, s[78:79]\n\t"
    "v_mfma_f32_16x16x32_bf16 v[56:59], a[180:183], a[72:75], v[56:59]\n\t" "ds_read_b64_tr_b16 a[174:175], v31 offset:40960\n\t" "v_mfma_f32_16x16x32_bf16 v[64:67], a[184:187], a[72:75], v[64:67]\n\t"
    "ds_read_b64_tr_b16 a[176:177], v20 offset:45056\n\t" "v_mfma_f32_16x16x32_bf16 v[68:71], a[184:187], a[76:79], v[68:71]\n\t" "ds_read_b64_tr_b16 a[178:179], v28 offset:45056\n\t"
    "v_mfma_f32_16x16x32_bf16 v[76:79], a[188:191], a[76:79], v[76:79]\n\t" "ds_read_b64_tr_b16 a[180:181], v21 offset:45056\n\t" "s_add_u32 m0, %[sl], 87040\n\t" "s_nop 0\n\t"
    "global_load_lds_dwordx4 v2, s[78:79]\n\t" "v_mfma_f32_16x16x32_bf16 v[72:75], a[188:191], a[72:75], v[72:75]\n\t" "ds_read_b64_tr_b16 a[182:183], v29 offset:45056\n\t"
    "s_waitcnt lgkmcnt(4) vmcnt(12)\n\t" "v_mfma_f32_16x16x32_bf16 v[48:51], a[160:163], a[80:83], v[48:51]\n\t" "ds_read_b64_tr_b16 a[184:185], v22 offset:45056\n\t"
    "v_mfma_f32_16x16x32_bf16 v[52:55], a[160:163], a[84:87], v[52:55]\n\t" "ds_read_b64_tr_b16 a[186:187], v30 offset:45056\n\t" "v_mfma_f32_16x16x32_bf16 v[60:63], a[164:167], a[84:87], v[60:63]\n\t"
    "ds_read_b64_tr_b16 a[188:189], v23 offset:45056\n\t" "s_add_u32 m0, %[sl], 88064\n\t" "s_nop 0\n\t" "global_load_lds_dwordx4 v3, s[78:79]\n\t"
    "v_mfma_f32_16x16x32_bf16 v[56:59], a[164:167], a[80:83], v[56:59]\n\t" "ds_read_b64_tr_b16 a[190:191], v31 offset:45056\n\t" "v_mfma_f32_16x16x32_bf16 v[64:67], a[168:171], a[80:83], v[64:67]\n\t"
    "ds_read_b64_tr_b16 a[160:161], v20 offset:49152\n\t" "v_mfma_f32_16x16x32_bf16 v[68:71], a[168:171], a[84:87], v[68:71]\n\t" "ds_read_b64_tr_b16 a[162:163], v28 offset:49152\n\t"
    "v_mfma_f32_16x16x32_bf16 v[76:79], a[172:175], a[84:87], v[76:79]\n\t" "ds_read_b64_tr_b16 a[164:165], v21 offset:49152\n\t" "s_add_u32 m0, %[sl], 89088\n\t" "s_nop 0\n\t"
    "s_mov_b64 exec, s[82:83]\n\t" "global_load_lds_dwordx4 v4, s[78:79]\n\t" "s_mov_b64 exec, -1\n\t" "s_add_u32 s78, s78, 229376\n\t" "s_addc_u32 s79, s79, 0\n\t"
    "v_mfma_f32_16x16x32_bf16 v[72:75], a[172:175], a[80:83], v[72:75]\n\t" "ds_read_b64_tr_b16 a[166:167], v29 offset:49152\n\t" "s_waitcnt lgkmcnt(4) vmcnt(12)\n\t"
    "v_mfma_f32_16x16x32_bf16 v[48:51], a[176:179], a[88:91], v[48:51]\n\t" "ds_read_b64_tr_b16 a[168:169], v22 offset:49152\n\t" "v_mfma_f32_16x16x32_bf16 v[52:55], a[176:179], a[92:95], v[52:55]\n\t"
    "ds_read_b64_tr_b16 a[170:171], v30 offset:49152\n\t" "v_mfma_f32_16x16x32_bf16 v[60:63], a[180:183], a[92:95], v[60:63]\n\t" "ds_read_b64_tr_b16 a[172:173], v23 offset:49152\n\t"
    "s_add_u32 m0, %[sl], 100352\n\t" "s_nop 0\n\t" "global_load_lds_dwordx4 v1, s[78:79]\n\t" "v_mfma_f32_16x16x32_bf16 v[56:59], a[180:183], a[88:91], v[56:59]\n\t"
    "ds_read_b64_tr_b16 a[174:175], v31 offset:49152\n\t" "v_mfma_f32_16x16x32_bf16 v[64:67], a[184:187], a[88:91], v[64:67]\n\t" "ds_read_b64_tr_b16 a[176:177], v20 offset:53248\n\t"
    "v_mfma_f32_16x16x32_bf16 v[68:71], a[184:187], a[92:95], v[68:71]\n\t" "ds_read_b64_tr_b16 a[178:179], v28 offset:53248\n\t" "v_mfma_f32_16x16x32_bf16 v[76:79], a[188:191], a[92:95], v[76:79]\n\t"
    "ds_read_b64_tr_b16 a[180:181], v21 offset:53248\n\t" "s_add_u32 m0, %[sl], 101376\n\t" "s_nop 0\n\t" "global_load_lds_dwordx4 v2, s[78:79]\n\t"
    "v_mfma_f32_16x16x32_bf16 v[72:75], a[188:191], a[88:91], v[72:75]\n\t" "ds_read_b64_tr_b16 a[182:183], v29 offset:53248\n\t" "s_waitcnt lgkmcnt(4) vmcnt(12)\n\t"
    "v_mfma_f32_16x16x32_bf16 v[48:51], a[160:163], a[96:99], v[48:51]\n\t" "ds_read_b64_tr_b16 a[184:185], v22 offset:53248\n\t" "v_mfma_f32_16x16x32_bf16 v[52:55], a[160:163], a[100:103], v[52:55]\n\t"
    "ds_read_b64_tr_b16 a[186:187], v30 offset:53248\n\t" "v_mfma_f32_16x16x32_bf16 v[60:63], a[164:167], a[100:103], v[60:63]\n\t" "ds_read_b64_tr_b16 a[188:189], v23 offset:53248\n\t"
    "s_add_u32 m0, %[sl], 102400\n\t" "s_nop 0\n\t" "global_load_lds_dwordx4 v3, s[78:79]\n\t" "v_mfma_f32_16x16x32_bf16 v[56:59], a[164:167], a[96:99], v[56:59]\n\t"
    "ds_read_b64_tr_b16 a[190:191], v31 offset:53248\n\t" "v_mfma_f32_16x16x32_bf16 v[64:67], a[168:171], a[96:99], v[64:67]\n\t" "ds_read_b64_tr_b16 a[160:161], v20 offset:57344\n\t"
    "v_mfma_f32_16x16x32_bf16 v[68:71], a[168:171], a[100:103], v[68:71]\n\t" "ds_read_b64_tr_b16 a[162:163], v28 offset:57344\n\t"
    "v_mfma_f32_16x16x32_bf16 v[76:79], a[172:175], a[100:103], v[76:79]\n\t" "ds_read_b64_tr_b16 a[164:165], v21 offset:57344\n\t" "s_add_u32 m0, %[sl], 103424\n\t" "s_nop 0\n\t"
    "s_mov_b64 exec, s[82:83]\n\t" "global_load_lds_dwordx4 v4, s[78:79]\n\t" "s_mov_b64 exec, -1\n\t" "s_add_u32 s78, s78, 229376\n\t" "s_addc_u32 s79, s79, 0\n\t"
    "v_mfma_f32_16x16x32_bf16 v[72:75], a[172:175], a[96:99], v[72:75]\n\t" "ds_read_b64_tr_b16 a[166:167], v29 offset:57344\n\t" "s_waitcnt lgkmcnt(4) vmcnt(12)\n\t"
    "v_mfma_f32_16x16x32_bf16 v[48:51], a[176:179], a[104:107], v[48:51]\n\t" "ds_read_b64_tr_b16 a[168:169], v22 offset:57344\n\t"
    "v_mfma_f32_16x16x32_bf16 v[52:55], a[176:179], a[108:111], v[52:55]\n\t" "ds_read_b64_tr_b16 a[170:171], v30 offset:57344\n\t"
    "v_mfma_f32_16x16x32_bf16 v[60:63], a[180:183], a[108:111], v[60:63]\n\t" "ds_read_b64_tr_b16 a[172:173], v23 offset:57344\n\t"
    "v_mfma_f32_16x16x32_bf16 v[56:59], a[180:183], a[104:107], v[56:59]\n\t" "ds_read_b64_tr_b16 a[174:175], v31 offset:57344\n\t"
    "v_mfma_f32_16x16x32_bf16 v[64:67], a[184:187], a[104:107], v[64:67]\n\t" "ds_read_b64_tr_b16 a[176:177], v20 offset:61440\n\t"
    "v_mfma_f32_16x16x32_bf16 v[68:71], a[184:187], a[108:111], v[68:71]\n\t" "ds_read_b64_tr_b16 a[178:179], v28 offset:61440\n\t"
    "v_mfma_f32_16x16x32_bf16 v[76:79], a[188:191], a[108:111], v[76:79]\n\t" "ds_read_b64_tr_b16 a[180:181], v21 offset:61440\n\t"
    "v_mfma_f32_16x16x32_bf16 v[72:75], a[188:191], a[104:107], v[72:75]\n\t" "ds_read_b64_tr_b16 a[182:183], v29 offset:61440\n\t" "s_waitcnt lgkmcnt(4) vmcnt(10)\n\t"
    "v_mfma_f32_16x16x32_bf16 v[48:51], a[160:163], a[112:115], v[48:51]\n\t" "ds_read_b64_tr_b16 a[184:185], v22 offset:61440\n\t"
    "v_mfma_f32_16x16x32_bf16 v[52:55], a[160:163], a[116:119], v[52:55]\n\t" "ds_read_b64_tr_b16 a[186:187], v30 offset:61440\n\t"
    "v_mfma_f32_16x16x32_bf16 v[60:63], a[164:167], a[116:119], v[60:63]\n\t" "ds_read_b64_tr_b16 a[188:189], v23 offset:61440\n\t"
    "v_mfma_f32_16x16x32_bf16 v[56:59], a[164:167], a[112:115], v[56:59]\n\t" "ds_read_b64_tr_b16 a[190:191], v31 offset:61440\n\t"
    "v_mfma_f32_16x16x32_bf16 v[64:67], a[168:171], a[112:115], v[64:67]\n\t" "v_mfma_f32_16x16x32_bf16 v[68:71], a[168:171], a[116:119], v[68:71]\n\t"
    "v_mfma_f32_16x16x32_bf16 v[76:79], a[172:175], a[116:119], v[76:79]\n\t" "v_mfma_f32_16x16x32_bf16 v[72:75], a[172:175], a[112:115], v[72:75]\n\t" "s_waitcnt lgkmcnt(0) vmcnt(8)\n\t"
    "v_mfma_f32_16x16x32_bf16 v[48:51], a[176:179], a[120:123], v[48:51]\n\t" "v_mfma_f32_16x16x32_bf16 v[52:55], a[176:179], a[124:127], v[52:55]\n\t"
    "v_mfma_f32_16x16x32_bf16 v[60:63], a[180:183], a[124:127], v[60:63]\n\t" "v_mfma_f32_16x16x32_bf16 v[56:59], a[180:183], a[120:123], v[56:59]\n\t"
    "v_mfma_f32_16x16x32_bf16 v[64:67], a[184:187], a[120:123], v[64:67]\n\t" "v_mfma_f32_16x16x32_bf16 v[68:71], a[184:187], a[124:127], v[68:71]\n\t"
    "v_mfma_f32_16x16x32_bf16 v[76:79], a[188:191], a[124:127], v[76:79]\n\t" "v_mfma_f32_16x16x32_bf16 v[72:75], a[188:191], a[120:123], v[72:75]\n\t" "s_nop 1\n\t"
    "v_pk_fma_f32 v[48:49], v[124:125], v[48:49], v[80:81]\n\t" "v_pk_fma_f32 v[50:51], v[126:127], v[50:51], v[82:83]\n\t" "v_pk_fma_f32 v[56:57], v[128:129], v[56:57], v[88:89]\n\t"
    "v_pk_fma_f32 v[58:59], v[130:131], v[58:59], v[90:91]\n\t" "v_pk_mul_f32 v[156:157], v[48:49], v[140:141]\n\t" "v_pk_mul_f32 v[158:159], v[50:51], v[142:143]\n\t"
    "v_pk_mul_f32 v[160:161], v[56:57], v[144:145]\n\t" "v_pk_mul_f32 v[162:163], v[58:59], v[146:147]\n\t" "v_cvt_pk_bf16_f32 v164, v156, v157\n\t" "v_cvt_pk_bf16_f32 v165, v158, v159\n\t"
    "v_cvt_pk_bf16_f32 v166, v160, v161\n\t" "v_cvt_pk_bf16_f32 v167, v162, v163\n\t" "global_store_dwordx4 v0, v[164:167], %[sy0] offset:0 sc1\n\t"
    "v_pk_fma_f32 v[52:53], v[124:125], v[52:53], v[84:85]\n\t" "v_pk_fma_f32 v[54:55], v[126:127], v[54:55], v[86:87]\n\t" "v_pk_fma_f32 v[60:61], v[128:129], v[60:61], v[92:93]\n\t"
    "v_pk_fma_f32 v[62:63], v[130:131], v[62:63], v[94:95]\n\t" "v_pk_mul_f32 v[172:173], v[52:53], v[140:141]\n\t" "v_pk_mul_f32 v[174:175], v[54:55], v[142:143]\n\t"
    "v_pk_mul_f32 v[176:177], v[60:61], v[144:145]\n\t" "v_pk_mul_f32 v[178:179], v[62:63], v[146:147]\n\t" "v_cvt_pk_bf16_f32 v180, v172, v173\n\t" "v_cvt_pk_bf16_f32 v181, v174, v175\n\t"
    "v_cvt_pk_bf16_f32 v182, v176, v177\n\t" "v_cvt_pk_bf16_f32 v183, v178, v179\n\t" "global_store_dwordx4 v0, v[180:183], %[sy0] offset:1024 sc1\n\t"
    "v_pk_fma_f32 v[64:65], v[132:133], v[64:65], v[96:97]\n\t" "v_pk_fma_f32 v[66:67], v[134:135], v[66:67], v[98:99]\n\t" "v_pk_fma_f32 v[72:73], v[136:137], v[72:73], v[104:105]\n\t"
    "v_pk_fma_f32 v[74:75], v[138:139], v[74:75], v[106:107]\n\t" "v_pk_mul_f32 v[156:157], v[64:65], v[148:149]\n\t" "v_pk_mul_f32 v[158:159], v[66:67], v[150:151]\n\t"
    "v_pk_mul_f32 v[160:161], v[72:73], v[152:153]\n\t" "v_pk_mul_f32 v[162:163], v[74:75], v[154:155]\n\t" "v_cvt_pk_bf16_f32 v168, v156, v157\n\t" "v_cvt_pk_bf16_f32 v169, v158, v159\n\t"
    "v_cvt_pk_bf16_f32 v170, v160, v161\n\t" "v_cvt_pk_bf16_f32 v171, v162, v163\n\t" "global_store_dwordx4 v0, v[168:171], %[sy1] offset:0 sc1\n\t"
    "v_pk_fma_f32 v[68:69], v[132:133], v[68:69], v[100:101]\n\t" "v_pk_fma_f32 v[70:71], v[134:135], v[70:71], v[102:103]\n\t" "v_pk_fma_f32 v[76:77], v[136:137], v[76:77], v[108:109]\n\t"
    "v_pk_fma_f32 v[78:79], v[138:139], v[78:79], v[110:111]\n\t" "v_pk_mul_f32 v[172:173], v[68:69], v[148:149]\n\t" "v_pk_mul_f32 v[174:175], v[70:71], v[150:151]\n\t"
    "v_pk_mul_f32 v[176:177], v[76:77], v[152:153]\n\t" "v_pk_mul_f32 v[178:179], v[78:79], v[154:155]\n\t" "v_cvt_pk_bf16_f32 v184, v172, v173\n\t" "v_cvt_pk_bf16_f32 v185, v174, v175\n\t"
    "v_cvt_pk_bf16_f32 v186, v176, v177\n\t" "v_cvt_pk_bf16_f32 v187, v178, v179\n\t" "global_store_dwordx4 v0, v[184:187], %[sy1] offset:1024 sc1\n\t" "v_pk_mul_f32 v[114:115], v[48:49], v[48:49]\n\t"
    "v_pk_mul_f32 v[118:119], v[52:53], v[52:53]\n\t" "v_pk_add_f32 v[112:113], v[50:51], v[48:49]\n\t" "v_pk_fma_f32 v[114:115], v[50:51], v[50:51], v[114:115]\n\t"
    "v_pk_add_f32 v[116:117], v[54:55], v[52:53]\n\t" "v_pk_fma_f32 v[118:119], v[54:55], v[54:55], v[118:119]\n\t" "v_pk_add_f32 v[112:113], v[56:57], v[112:113]\n\t"
    "v_pk_fma_f32 v[114:115], v[56:57], v[56:57], v[114:115]\n\t" "v_pk_add_f32 v[116:117], v[60:61], v[116:117]\n\t" "v_pk_fma_f32 v[118:119], v[60:61], v[60:61], v[118:119]\n\t"
    "v_pk_add_f32 v[112:113], v[58:59], v[112:113]\n\t" "v_pk_fma_f32 v[114:115], v[58:59], v[58:59], v[114:115]\n\t" "v_pk_add_f32 v[116:117], v[62:63], v[116:117]\n\t"
    "v_pk_fma_f32 v[118:119], v[62:63], v[62:63], v[118:119]\n\t" "v_pk_add_f32 v[112:113], v[64:65], v[112:113]\n\t" "v_pk_fma_f32 v[114:115], v[64:65], v[64:65], v[114:115]\n\t"
    "v_pk_add_f32 v[116:117], v[68:69], v[116:117]\n\t" "v_pk_fma_f32 v[118:119], v[68:69], v[68:69], v[118:119]\n\t" "v_pk_add_f32 v[112:113], v[66:67], v[112:113]\n\t"
    "v_pk_fma_f32 v[114:115], v[66:67], v[66:67], v[114:115]\n\t" "v_pk_add_f32 v[116:117], v[70:71], v[116:117]\n\t" "v_pk_fma_f32 v[118:119], v[70:71], v[70:71], v[118:119]\n\t"
    "v_pk_add_f32 v[112:113], v[72:73], v[112:113]\n\t" "v_pk_fma_f32 v[114:115], v[72:73], v[72:73], v[114:115]\n\t" "v_pk_add_f32 v[116:117], v[76:77], v[116:117]\n\t"
    "v_pk_fma_f32 v[118:119], v[76:77], v[76:77], v[118:119]\n\t" "v_pk_add_f32 v[112:113], v[74:75], v[112:113]\n\t" "v_pk_fma_f32 v[114:115], v[74:75], v[74:75], v[114:115]\n\t"
    "v_pk_add_f32 v[116:117], v[78:79], v[116:117]\n\t" "v_pk_fma_f32 v[118:119], v[78:79], v[78:79], v[118:119]\n\t" "v_add_f32 v188, v112, v113\n\t" "v_add_f32 v189, v114, v115\n\t"
    "v_add_f32 v190, v116, v117\n\t" "v_add_f32_e64 v191, v118, v119\n\t" "v_pk_add_f32 v[48:49], v[48:49], v[236:237] op_sel:[0,0] op_sel_hi:[1,0]\n\t"
    "v_pk_add_f32 v[50:51], v[50:51], v[236:237] op_sel:[0,0] op_sel_hi:[1,0]\n\t" "v_permlane16_swap_b32 v188, v190\n\t" "v_permlane16_swap_b32 v189, v191\n\t" "v_add_f32 v188, v188, v190\n\t"
    "v_add_f32 v189, v189, v191\n\t" "v_mov_b32 v190, v188\n\t" "v_mov_b32 v191, v189\n\t" "v_pk_add_f32 v[56:57], v[56:57], v[236:237] op_sel:[0,0] op_sel_hi:[1,0]\n\t"
    "v_pk_add_f32 v[58:59], v[58:59], v[236:237] op_sel:[0,0] op_sel_hi:[1,0]\n\t" "v_permlane32_swap_b32 v188, v190\n\t" "v_permlane32_swap_b32 v189, v191\n\t" "v_add_f32 v188, v188, v190\n\t"
    "v_add_f32_e64 v189, v189, v191\n\t" "s_mov_b32 exec_hi, 0\n\t" "global_store_dwordx2 v34, v[188:189], %[ss] sc1\n\t" "s_mov_b64 exec, -1\n\t"
    "v_pk_add_f32 v[64:65], v[64:65], v[236:237] op_sel:[0,0] op_sel_hi:[1,0]\n\t" "v_pk_add_f32 v[66:67], v[66:67], v[236:237] op_sel:[0,0] op_sel_hi:[1,0]\n\t"
    "v_pk_add_f32 v[72:73], v[72:73], v[236:237] op_sel:[0,0] op_sel_hi:[1,0]\n\t" "v_pk_add_f32 v[74:75], v[74:75], v[236:237] op_sel:[0,0] op_sel_hi:[1,0]\n\t"
    "v_pk_add_f32 v[52:53], v[52:53], v[236:237] op_sel:[0,1] op_sel_hi:[1,1]\n\t" "v_pk_add_f32 v[54:55], v[54:55], v[236:237] op_sel:[0,1] op_sel_hi:[1,1]\n\t"
    "v_pk_add_f32 v[60:61], v[60:61], v[236:237] op_sel:[0,1] op_sel_hi:[1,1]\n\t" "v_pk_add_f32 v[62:63], v[62:63], v[236:237] op_sel:[0,1] op_sel_hi:[1,1]\n\t"
    "v_pk_add_f32 v[68:69], v[68:69], v[236:237] op_sel:[0,1] op_sel_hi:[1,1]\n\t" "v_pk_add_f32 v[70:71], v[70:71], v[236:237] op_sel:[0,1] op_sel_hi:[1,1]\n\t"
    "v_pk_add_f32 v[76:77], v[76:77], v[236:237] op_sel:[0,1] op_sel_hi:[1,1]\n\t" "v_pk_add_f32 v[78:79], v[78:79], v[236:237] op_sel:[0,1] op_sel_hi:[1,1]\n\t" "v_cvt_pk_bf16_f32 v80, v48, v49\n\t"
    "v_cvt_pk_bf16_f32 v81, v50, v51\n\t" "v_cvt_pk_bf16_f32 v82, v56, v57\n\t" "v_cvt_pk_bf16_f32 v83, v58, v59\n\t" "v_cvt_pk_bf16_f32 v84, v64, v65\n\t" "v_cvt_pk_bf16_f32 v85, v66, v67\n\t"
    "v_cvt_pk_bf16_f32 v86, v72, v73\n\t" "v_cvt_pk_bf16_f32 v87, v74, v75\n\t" "v_cvt_pk_bf16_f32 v88, v52, v53\n\t" "v_cvt_pk_bf16_f32 v89, v54, v55\n\t" "v_cvt_pk_bf16_f32 v90, v60, v61\n\t"
    "v_cvt_pk_bf16_f32 v91, v62, v63\n\t" "v_cvt_pk_bf16_f32 v92, v68, v69\n\t" "v_cvt_pk_bf16_f32 v93, v70, v71\n\t" "v_cvt_pk_bf16_f32 v94, v76, v77\n\t" "v_cvt_pk_bf16_f32 v95, v78, v79\n\t"
    "s_branch .Lend_%=\n\t" ".Lslow_%=:\n\t" "s_sleep 2\n\t" "global_load_dwordx4 v[112:115], v0, %[sg0] sc1\n\t" "global_load_dwordx4 v[116:119], v0, %[sg1] sc1\n\t"
    "global_load_dwordx4 v[120:123], v0, %[sg2] sc1\n\t" "s_waitcnt vmcnt(0)\n\t" "v_xad_u32 v156, v112, v113, v164\n\t" "v_xad_u32 v157, v116, v117, v164\n\t" "v_xad_u32 v158, v120, v121, v164\n\t"
    "v_xad_u32 v156, v114, v113, v156\n\t" "v_xad_u32 v157, v118, v117, v157\n\t" "v_xad_u32 v158, v122, v121, v158\n\t" "v_xor_b32_e64 v156, v156, v115\n\t"
    "v_bitop3_b32 v156, v156, v157, v119 bitop3:0xf6\n\t" "v_bitop3_b32 v156, v156, v158, v123 bitop3:0xf6\n\t" "v_cmp_ne_u32_e64 vcc, 0, v156\n\t" "v_perm_b32 v115, v112, v113, s49\n\t"
    "v_perm_b32 v119, v116, v117, s49\n\t" "v_perm_b32 v123, v120, v121, s49\n\t" "s_nop 1\n\t" "s_cbranch_vccz .Lgo_%=\n\t" "s_sub_u32 s44, s44, 1\n\t" "s_cbranch_scc0 .Lslow_%=\n\t"
    "s_branch .Lgo_%=\n\t" ".Lend_%=:\n\t"
    : "+{v[80:83]}"(xr[0]), "+{v[84:87]}"(xr[1]), "+{v[88:91]}"(xr[2]), "+{v[92:95]}"(xr[3]), "+{v[96:99]}"(xr[4]), "+{v[100:103]}"(xr[5]), "+{v[104:107]}"(xr[6]), "+{v[108:111]}"(xr[7])
    : "{v0}"(va), "{v15}"(tw), "{v32}"(tr), "{v33}"(ve), "{v34}"(vs), "{v236}"(mu[0]), "{v237}"(mu[1]), "{v1}"(vb[0]), "{v2}"(vb[1]), "{v3}"(vb[2]), "{v4}"(vb[3]), "{v16}"(vr[0]), "{v17}"(vr[1]), "{v18}"(vr[2]), "{v19}"(vr[3]), "{v20}"(vr[4]), "{v21}"(vr[5]), "{v22}"(vr[6]), "{v23}"(vr[7]), "{v24}"(vr[8]), "{v25}"(vr[9]), "{v26}"(vr[10]), "{v27}"(vr[11]), "{v28}"(vr[12]), "{v29}"(vr[13]), "{v30}"(vr[14]), "{v31}"(vr[15]), [sa] "s"(sa + 8192), [tag] "s"(tag), [role] "s"(role), [sg0] "s"(sg0), [sg1] "s"(sg1), [sg2] "s"(sg2), [se] "s"(se), [sy0] "s"(sy0), [sy1] "s"(sy1), [ss] "s"(ss), [sb] "s"(sb), [sl] "s"(sl), [cd] "s"(cd)
    : "v48", "v49", "v50", "v51", "v52", "v53", "v54", "v55", "v56", "v57", "v58", "v59", "v60", "v61", "v62", "v63", "v64", "v65", "v66", "v67", "v68", "v69", "v70", "v71", "v72", "v73", "v74", "v75", "v76", "v77", "v78", "v79", "v112", "v113", "v114", "v115", "v116", "v117", "v118", "v119", "v120", "v121", "v122", "v123", "v124", "v125", "v126", "v127", "v128", "v129", "v130", "v131", "v132", "v133", "v134", "v135", "v136", "v137", "v138", "v139", "v140", "v141", "v142", "v143", "v144", "v145", "v146", "v147", "v148", "v149", "v150", "v151", "v152", "v153", "v154", "v155", "v156", "v157", "v158", "v159", "v160", "v161", "v162", "v163", "v164", "v165", "v166", "v167", "v168", "v169", "v170", "v171", "v172", "v173", "v174", "v175", "v176", "v177", "v178", "v179", "v180", "v181", "v182", "v183", "v184", "v185", "v186", "v187", "v188", "v189", "v190", "v191", "v192", "v193", "v194", "v195", "a0", "a1", "a2", "a3", "a4", "a5", "a6", "a7", "a8", "a9", "a10", "a11", "a12", "a13", "a14", "a15", "a16", "a17", "a18", "a19", "a20", "a21", "a22", "a23", "a24", "a25", "a26", "a27", "a28", "a29", "a30", "a31", "a32", "a33", "a34", "a35", "a36", "a37", "a38", "a39", "a40", "a41", "a42", "a43", "a44", "a45", "a46", "a47", "a48", "a49", "a50", "a51", "a52", "a53", "a54", "a55", "a56", "a57", "a58", "a59", "a60", "a61", "a62", "a63", "a64", "a65", "a66", "a67", "a68", "a69", "a70", "a71", "a72", "a73", "a74", "a75", "a76", "a77", "a78", "a79", "a80", "a81", "a82", "a83", "a84", "a85", "a86", "a87", "a88", "a89", "a90", "a91", "a92", "a93", "a94", "a95", "a96", "a97", "a98", "a99", "a100", "a101", "a102", "a103", "a104", "a105", "a106", "a107", "a108", "a109", "a110", "a111", "a112", "a113", "a114", "a115", "a116", "a117", "a118", "a119", "a120", "a121", "a122", "a123", "a124", "a125", "a126", "a127", "a128", "a129", "a130", "a131", "a132", "a133", "a134", "a135", "a136", "a137", "a138", "a139", "a140", "a141", "a142", "a143", "a144", "a145", "a146", "a147", "a148", "a149", "a150", "a151", "a152", "a153", "a154", "a155", "a156", "a157", "a158", "a159", "a160", "a161", "a162", "a163", "a164", "a165", "a166", "a167", "a168", "a169", "a170", "a171", "a172", "a173", "a174", "a175", "a176", "a177", "a178", "a179", "a180", "a181", "a182", "a183", "a184", "a185", "a186", "a187", "a188", "a189", "a190", "a191", "s40", "s41", "s42", "s43", "s44", "s45", "s46", "s47", "s48", "s49", "s50", "s78", "s79", "s82", "s83", "vcc", "scc", "memory");
  }
  asm volatile("" : "+s"(bx), "+s"(wid), "+s"(ws)); asm volatile("" : "+s"(tag)); asm volatile("" : "+v"(tid));
  {
  const int xcd = bx & 7, loc = bx >> 3, lane = tid & 63, cb = (xcd >> 1) * 8 + (loc >> 2);
  GW();
  auto grp = [&](int i) { const int q = i + (wid < 3 ? wid : 0); return i == 3 ? 3 : q < 3 ? q : q - 3; };
  unsigned vr[8], gt[4], go[4]; u32x4 tmp;
  const unsigned va = lane * 16, ct = ldsb + (lane < 16 ? TAB + lane * 16 : ZB), zb = ldsb + ZB + tid * 4, xw = ldsb + 24576 + tid * 16;
#pragma unroll
  for (int m = 0; m < 4; ++m) {
    vr[2 * m] = ldsb + GB + (lane >> 2) * 224 + (lane & 3) * 8 + (m < 3 ? grp(m) * 64 + (cb & 1) * 32 : (cb & 1) ? 0 : 192); vr[2 * m + 1] = vr[2 * m] + 4 * 14336;
    gt[m] = grp(m) * 1024;
    go[m] = m < 3 ? ((cb & 1) * 4 + grp(m)) * 2048 : 3 * 2048 + (cb & 1) * 1024;
  }
  const unsigned vs = (unsigned)((lane >> 4) * 4 * S_ + (lane & 15)) * 8, ve = (lane & 1) * 64 + (lane >> 4) * 16, vbd = f_vb(wid, lane);
  const unsigned cr0 = ldsb + RAW + (tid >> 2) * 144 + (tid & 3) * 4, cw = ldsb + TAB + (tid >> 5) * 256 + (tid & 15) * 16 + ((tid >> 4) & 1) * 8;
  const char* sf = ws + (unsigned)(W_TAGS + YF * 8 + FA(((xcd >> 1) * 4 + (loc & 3)) * 16 + (xcd & 1) * 8 + (loc >> 2)));
#define PINS0 "v"(vr[1]), "v"(vr[3]), "v"(vr[5]), "v"(vr[7]), "v"(ct), "v"(vs), "v"(ve), "v"(vbd), "v"(cr0), "v"(cw), "s"(gt[0]), "s"(gt[1]), "s"(gt[2]), "s"(go[0]), "s"(go[1]), "s"(go[2]), "s"(go[3])
  asm volatile("s_mov_b64 s[78:79], %[sb]\n\t" "s_mov_b32 s82, -1\n\t" "s_mov_b32 s83, 0\n\t" "v_mov_b64 v[192:193], %[tag]\n\t" "v_mov_b32 v194, 0\n\t" "s_waitcnt vmcnt(0)\n\t" "s_barrier\n\t"
    "s_cmp_eq_u32 %[wid], 0\n\t" "s_cselect_b64 exec, 1, 0\n\t" "global_store_dwordx2 v194, v[192:193], %[sf] sc1\n\t" "s_mov_b64 exec, -1\n\t" "ds_write_b32 v36, v194\n\t"
    "ds_write_b128 v37, v[80:83] offset:0\n\t" "ds_write_b128 v37, v[84:87] offset:4096\n\t" "ds_write_b128 v37, v[88:91] offset:8192\n\t" "ds_write_b128 v37, v[92:95] offset:12288\n\t"
    "s_add_u32 m0, %[sl], 0\n\t" "s_nop 0\n\t" "global_load_lds_dwordx4 v1, s[78:79]\n\t" "s_add_u32 m0, %[sl], 1024\n\t" "s_nop 0\n\t" "global_load_lds_dwordx4 v2, s[78:79]\n\t"
    "s_add_u32 m0, %[sl], 2048\n\t" "s_nop 0\n\t" "global_load_lds_dwordx4 v3, s[78:79]\n\t" "s_add_u32 m0, %[sl], 3072\n\t" "s_nop 0\n\t" "s_mov_b64 exec, s[82:83]\n\t"
    "global_load_lds_dwordx4 v4, s[78:79]\n\t" "s_mov_b64 exec, -1\n\t" "s_add_u32 s78, s78, 229376\n\t" "s_addc_u32 s79, s79, 0\n\t" "s_add_u32 m0, %[sl], 14336\n\t" "s_nop 0\n\t"
    "global_load_lds_dwordx4 v1, s[78:79]\n\t" "s_add_u32 m0, %[sl], 15360\n\t" "s_nop 0\n\t" "global_load_lds_dwordx4 v2, s[78:79]\n\t" "s_add_u32 m0, %[sl], 16384\n\t" "s_nop 0\n\t"
    "global_load_lds_dwordx4 v3, s[78:79]\n\t" "s_add_u32 m0, %[sl], 17408\n\t" "s_nop 0\n\t" "s_mov_b64 exec, s[82:83]\n\t" "global_load_lds_dwordx4 v4, s[78:79]\n\t" "s_mov_b64 exec, -1\n\t"
    "s_add_u32 s78, s78, 229376\n\t" "s_addc_u32 s79, s79, 0\n\t"
    : "=&{v[192:195]}"(tmp)
    : "{v[80:83]}"(xr[0]), "{v[84:87]}"(xr[1]), "{v[88:91]}"(xr[2]), "{v[92:95]}"(xr[3]), "{v36}"(zb), "{v37}"(xw), "{v1}"(vb[0]), "{v2}"(vb[1]), "{v3}"(vb[2]), "{v4}"(vb[3]), [sb] "s"(sb + 458752), [tag] "s"(tag), [wid] "s"(wid), [sf] "s"(sf), [sl] "s"(sl), PINS0
    : "s78", "s79", "s82", "s83", "scc", "memory");
  asm volatile("" : "+s"(bx), "+s"(wid), "+s"(ws)); asm volatile("" : "+v"(tid));
  { { const int xcd = bx & 7, loc = bx >> 3, lane = tid & 63, rot = xcd & 1, rb = (xcd & 1) * 4 + (loc & 3), cb = (xcd >> 1) * 8 + (loc >> 2);
    unsigned f0 = rb * 32 + lane, f1 = ((xcd >> 1) * 4 + (lane & 3)) * 16 + (lane & 4) * 2 + (loc >> 2); asm volatile("" : "+v"(f0), "+v"(f1));
    const unsigned vf = FA(lane < 32 ? f0 : f1) + W_TAGS + YF * 8, vp = ((lane & 1) * 4 + (lane >> 4)) * 144 + rb * 16, cr1 = cr0 + (wid < 3 ? 64 * 144 : 0), srl = ldsb + RAW + wid * 4096, sld = ldsb + wid * 1024;
    const char* sq = ws; const char* sa = ws + (unsigned)(W_Y + rb * 524288 + wid * YW + rot * 16 * YS); const char* scnt = ws + (unsigned)(W_TAGS + HF * 8 + FA(rb * 32 + cb));
    const char* se = ws + (unsigned)(W_EF + rb * 4096 + ((rb + 1) & 7) * 512);
    const char* ss = ws + (unsigned)(W_STATS + rb * 2048 + wid * 512); const char* su = ws + (unsigned)(W_UVP + cb * 16384);
    const size_t sh = (size_t)(ws + (unsigned)(W_HH + ((rb * 8 + wid * 2) * 112 + (cb >> 1) * 7) * 2048));
    const unsigned shl = (unsigned)sh, shh = (unsigned)(sh >> 32);
    const char* su0 = su + (unsigned)(grp(0) * 4608); const char* su6 = su + 6 * 2304; const char* sr = su + (unsigned)(wid * 4096);
    const unsigned long long m01 = wid < 3 ? 0x0003000300030003ull : 0ull, m6 = wid < 3 ? 0ull : 0x0003000300030003ull;
    const char* tp0 = (const char*)pfw + (unsigned)((bx * 4 + wid) * 7168); const char* sbd = (const char*)pfw + (unsigned)((xcd & 1) * 1792 * 2048 + ROT * 256);
    const int wa = rot ? -31 * YS : YS, wb = rot ? -31 * 32 * 7168 : 32 * 7168, e1 = (rb & 1) ? -1 : (rb >> 1) & 1, e0 = (rb & 1) ? (rb >> 1) & 1 : -1;
    unsigned long long fl;
    asm volatile(".p2align 3\n\t" "s_mov_b32 s80, 0\n\t" "s_cmp_eq_u32 s80, 1\n\t" "s_cselect_b32 s86, %[wa], 16384\n\t" "s_ashr_i32 s87, s86, 31\n\t" "s_cmp_eq_u32 s80, 1\n\t" "s_cselect_b32 s88, %[wb], 229376\n\t"
    "s_ashr_i32 s89, s88, 31\n\t" "s_mov_b64 s[78:79], %[sb]\n\t" "s_mov_b32 s82, -1\n\t" "s_mov_b32 s83, 0\n\t" "s_mov_b32 s94, 0x10000\n\t" "s_mov_b64 s[84:85], %[tp0]\n\t"
    "s_mov_b64 s[76:77], %[sa]\n\t" "s_mov_b32 s81, 0x400000\n\t" "s_cmp_eq_u32 %[wid], 0\n\t" "s_cselect_b64 exec, -1, 0\n\t" "global_load_dwordx2 v[24:25], %[vf], %[sq] sc1\n\t" ".Lpy_%=:\n\t"
    "s_waitcnt vmcnt(0)\n\t" "v_cmp_ne_u64 vcc, %[tag], v[24:25]\n\t" "s_nop 4\n\t" "s_cbranch_vccz .Lry_%=\n\t" "s_mov_b64 exec, vcc\n\t" "s_sleep 1\n\t"
    "global_load_dwordx2 v[24:25], %[vf], %[sq] sc1\n\t" "s_sub_u32 s81, s81, 1\n\t" "s_cbranch_scc0 .Lpy_%=\n\t" ".Lry_%=:\n\t" "s_mov_b64 exec, -1\n\t" "s_barrier\n\t"
    "global_load_dwordx4 a[0:3], %[va], s[76:77] offset:0 sc1\n\t" "global_load_dwordx4 a[4:7], %[va], s[76:77] offset:1024 sc1\n\t" "global_load_dwordx4 a[8:11], %[va], s[76:77] offset:2048 sc1\n\t"
    "global_load_dwordx4 a[12:15], %[va], s[76:77] offset:3072 sc1\n\t" "s_add_u32 s76, s76, 16384\n\t" "s_addc_u32 s77, s77, 0\n\t" "ds_read_b64_tr_b16 a[128:129], %[vr1] offset:28672\n\t"
    "ds_read_b64_tr_b16 a[130:131], %[vr1] offset:32256\n\t" "ds_read_b64_tr_b16 a[132:133], %[vr1] offset:28704\n\t" "ds_read_b64_tr_b16 a[134:135], %[vr1] offset:32288\n\t"
    "ds_read_b64_tr_b16 a[136:137], %[vr3] offset:28672\n\t" "ds_read_b64_tr_b16 a[138:139], %[vr3] offset:32256\n\t" "ds_read_b64_tr_b16 a[140:141], %[vr3] offset:28704\n\t"
    "ds_read_b64_tr_b16 a[142:143], %[vr3] offset:32288\n\t" "global_load_dwordx4 a[16:19], %[va], s[76:77] offset:0 sc1\n\t" "global_load_dwordx4 a[20:23], %[va], s[76:77] offset:1024 sc1\n\t"
    "global_load_dwordx4 a[24:27], %[va], s[76:77] offset:2048 sc1\n\t" "global_load_dwordx4 a[28:31], %[va], s[76:77] offset:3072 sc1\n\t" "s_add_u32 s76, s76, 16384\n\t" "s_addc_u32 s77, s77, 0\n\t"
    "global_load_dwordx4 a[32:35], %[va], s[76:77] offset:0 sc1\n\t" "global_load_dwordx4 a[36:39], %[va], s[76:77] offset:1024 sc1\n\t" "global_load_dwordx4 a[40:43], %[va], s[76:77] offset:2048 sc1\n\t"
    "global_load_dwordx4 a[44:47], %[va], s[76:77] offset:3072 sc1\n\t" "s_add_u32 s76, s76, 16384\n\t" "s_addc_u32 s77, s77, 0\n\t" "s_waitcnt vmcnt(8)\n\t" "s_waitcnt lgkmcnt(4)\n\t"
    "v_mfma_f32_16x16x32_bf16 v[32:35], a[128:131], a[0:3], 0\n\t" "ds_read_b64_tr_b16 a[144:145], %[vr5] offset:28672\n\t" "s_mov_b64 s[90:91], %[ss]\n\t"
    "v_mfma_f32_16x16x32_bf16 v[36:39], a[128:131], a[4:7], 0\n\t" "ds_read_b64_tr_b16 a[146:147], %[vr5] offset:32256\n\t" "v_accvgpr_read_b32 v30, %[vs]\n\t"
    "v_mfma_f32_16x16x32_bf16 v[40:43], a[128:131], a[8:11], 0\n\t" "global_load_dwordx4 a[48:51], %[va], s[76:77] offset:0 sc1\n\t" "v_accvgpr_read_b32 v31, %[ve]\n\t"
    "v_mfma_f32_16x16x32_bf16 v[44:47], a[128:131], a[12:15], 0\n\t" "v_mfma_f32_16x16x32_bf16 v[48:51], a[132:135], a[0:3], 0\n\t" "ds_read_b64_tr_b16 a[148:149], %[vr5] offset:28704\n\t"
    "global_load_dwordx4 a[184:187], v31, %[se] offset:0 sc1\n\t" "v_mfma_f32_16x16x32_bf16 v[52:55], a[132:135], a[4:7], 0\n\t" "ds_read_b64_tr_b16 a[150:151], %[vr5] offset:32288\n\t"
    "s_add_u32 m0, %[sl], 28672\n\t" "v_mfma_f32_16x16x32_bf16 v[56:59], a[132:135], a[8:11], 0\n\t" "global_load_lds_dwordx4 %[vb0], s[78:79]\n\t"
    "global_load_dwordx4 a[188:191], v31, %[se] offset:128 sc1\n\t" "v_mfma_f32_16x16x32_bf16 v[60:63], a[132:135], a[12:15], 0\n\t" "s_waitcnt lgkmcnt(4)\n\t"
    "v_mfma_f32_16x16x32_bf16 v[64:67], a[136:139], a[0:3], 0\n\t" "ds_read_b64_tr_b16 a[152:153], %[vr7] offset:28672\n\t" "global_load_dwordx4 a[192:195], v31, %[se] offset:256 sc1\n\t"
    "v_mfma_f32_16x16x32_bf16 v[68:71], a[136:139], a[4:7], 0\n\t" "ds_read_b64_tr_b16 a[154:155], %[vr7] offset:32256\n\t" "v_mfma_f32_16x16x32_bf16 v[72:75], a[136:139], a[8:11], 0\n\t"
    "global_load_dwordx4 a[52:55], %[va], s[76:77] offset:1024 sc1\n\t" "global_load_dwordx4 a[196:199], v31, %[se] offset:384 sc1\n\t" "v_mfma_f32_16x16x32_bf16 v[76:79], a[136:139], a[12:15], 0\n\t"
    "v_mfma_f32_16x16x32_bf16 v[80:83], a[140:143], a[0:3], 0\n\t" "ds_read_b64_tr_b16 a[156:157], %[vr1] offset:35840\n\t" "global_load_dwordx2 a[216:217], v30, s[90:91] offset:0 sc1\n\t"
    "v_mfma_f32_16x16x32_bf16 v[84:87], a[140:143], a[4:7], 0\n\t" "ds_read_b64_tr_b16 a[158:159], %[vr1] offset:39424\n\t" "s_add_u32 m0, %[sl], 29696\n\t"
    "v_mfma_f32_16x16x32_bf16 v[88:91], a[140:143], a[8:11], 0\n\t" "global_load_lds_dwordx4 %[vb1], s[78:79]\n\t" "v_mfma_f32_16x16x32_bf16 v[92:95], a[140:143], a[12:15], 0\n\t"
    "s_waitcnt lgkmcnt(4)\n\t" "v_mfma_f32_16x16x32_bf16 v[96:99], a[144:147], a[0:3], 0\n\t" "ds_read_b64_tr_b16 a[128:129], %[vr1] offset:35872\n\t"
    "v_mfma_f32_16x16x32_bf16 v[100:103], a[144:147], a[4:7], 0\n\t" "ds_read_b64_tr_b16 a[130:131], %[vr1] offset:39456\n\t" "v_mfma_f32_16x16x32_bf16 v[104:107], a[144:147], a[8:11], 0\n\t"
    "v_mfma_f32_16x16x32_bf16 v[108:111], a[144:147], a[12:15], 0\n\t" "v_mfma_f32_16x16x32_bf16 v[112:115], a[148:151], a[0:3], 0\n\t" "ds_read_b64_tr_b16 a[132:133], %[vr3] offset:35840\n\t"
    "v_mfma_f32_16x16x32_bf16 v[116:119], a[148:151], a[4:7], 0\n\t" "ds_read_b64_tr_b16 a[134:135], %[vr3] offset:39424\n\t" "v_mfma_f32_16x16x32_bf16 v[120:123], a[148:151], a[8:11], 0\n\t"
    "global_load_dwordx4 a[56:59], %[va], s[76:77] offset:2048 sc1\n\t" "v_mfma_f32_16x16x32_bf16 v[124:127], a[148:151], a[12:15], 0\n\t" "s_waitcnt lgkmcnt(4)\n\t"
    "v_mfma_f32_16x16x32_bf16 v[128:131], a[152:155], a[0:3], 0\n\t" "ds_read_b64_tr_b16 a[136:137], %[vr3] offset:35872\n\t" "global_load_dwordx2 a[218:219], v30, s[90:91] offset:128 sc1\n\t"
    "v_mfma_f32_16x16x32_bf16 v[132:135], a[152:155], a[4:7], 0\n\t" "ds_read_b64_tr_b16 a[138:139], %[vr3] offset:39456\n\t" "s_add_u32 m0, %[sl], 30720\n\t"
    "v_mfma_f32_16x16x32_bf16 v[136:139], a[152:155], a[8:11], 0\n\t" "global_load_lds_dwordx4 %[vb2], s[78:79]\n\t" "v_mfma_f32_16x16x32_bf16 v[140:143], a[152:155], a[12:15], 0\n\t"
    "v_mfma_f32_16x16x32_bf16 v[144:147], a[156:159], a[0:3], 0\n\t" "ds_read_b64_tr_b16 a[140:141], %[vr5] offset:35840\n\t" "v_mfma_f32_16x16x32_bf16 v[148:151], a[156:159], a[4:7], 0\n\t"
    "ds_read_b64_tr_b16 a[142:143], %[vr5] offset:39424\n\t" "v_mfma_f32_16x16x32_bf16 v[152:155], a[156:159], a[8:11], 0\n\t" "global_load_dwordx4 a[60:63], %[va], s[76:77] offset:3072 sc1\n\t"
    "v_mfma_f32_16x16x32_bf16 v[156:159], a[156:159], a[12:15], 0\n\t" "s_add_u32 s76, s76, 16384\n\t" "s_addc_u32 s77, s77, 0\n\t" "s_waitcnt lgkmcnt(4)\n\t"
    "v_mfma_f32_16x16x32_bf16 v[160:163], a[128:131], a[0:3], 0\n\t" "ds_read_b64_tr_b16 a[144:145], %[vr5] offset:35872\n\t" "v_mfma_f32_16x16x32_bf16 v[164:167], a[128:131], a[4:7], 0\n\t"
    "ds_read_b64_tr_b16 a[146:147], %[vr5] offset:39456\n\t" "v_mfma_f32_16x16x32_bf16 v[168:171], a[128:131], a[8:11], 0\n\t" "v_mfma_f32_16x16x32_bf16 v[172:175], a[128:131], a[12:15], 0\n\t"
    "v_mfma_f32_16x16x32_bf16 v[176:179], a[132:135], a[0:3], 0\n\t" "ds_read_b64_tr_b16 a[148:149], %[vr7] offset:35840\n\t" "global_load_dwordx2 a[220:221], v30, s[90:91] offset:256 sc1\n\t"
    "v_mfma_f32_16x16x32_bf16 v[180:183], a[132:135], a[4:7], 0\n\t" "ds_read_b64_tr_b16 a[150:151], %[vr7] offset:39424\n\t" "s_add_u32 m0, %[sl], 31744\n\t"
    "v_mfma_f32_16x16x32_bf16 v[184:187], a[132:135], a[8:11], 0\n\t" "s_mov_b64 exec, s[82:83]\n\t" "global_load_lds_dwordx4 %[vb3], s[78:79]\n\t" "s_mov_b64 exec, -1\n\t"
    "v_mfma_f32_16x16x32_bf16 v[188:191], a[132:135], a[12:15], 0\n\t" "s_add_u32 s78, s78, 229376\n\t" "s_addc_u32 s79, s79, 0\n\t" "s_waitcnt lgkmcnt(4)\n\t"
    "v_mfma_f32_16x16x32_bf16 v[192:195], a[136:139], a[0:3], 0\n\t" "s_waitcnt vmcnt(19)\n\t" "s_barrier\n\t" "ds_read_b64_tr_b16 a[152:153], %[vr1] offset:43008\n\t"
    "v_mfma_f32_16x16x32_bf16 v[196:199], a[136:139], a[4:7], 0\n\t" "ds_read_b64_tr_b16 a[154:155], %[vr1] offset:46592\n\t" "v_mfma_f32_16x16x32_bf16 v[200:203], a[136:139], a[8:11], 0\n\t"
    "global_load_dwordx4 a[64:67], %[va], s[76:77] offset:0 sc1\n\t" "v_mfma_f32_16x16x32_bf16 v[204:207], a[136:139], a[12:15], 0\n\t" "v_mfma_f32_16x16x32_bf16 v[208:211], a[140:143], a[0:3], 0\n\t"
    "ds_read_b64_tr_b16 a[156:157], %[vr1] offset:43040\n\t" "v_mfma_f32_16x16x32_bf16 v[212:215], a[140:143], a[4:7], 0\n\t" "ds_read_b64_tr_b16 a[158:159], %[vr1] offset:46624\n\t"
    "s_add_u32 m0, %[sl], 43008\n\t" "v_mfma_f32_16x16x32_bf16 v[216:219], a[140:143], a[8:11], 0\n\t" "global_load_lds_dwordx4 %[vb0], s[78:79]\n\t"
    "v_mfma_f32_16x16x32_bf16 v[220:223], a[140:143], a[12:15], 0\n\t" "s_waitcnt lgkmcnt(4)\n\t" "v_mfma_f32_16x16x32_bf16 v[224:227], a[144:147], a[0:3], 0\n\t"
    "ds_read_b64_tr_b16 a[128:129], %[vr3] offset:43008\n\t" "global_load_dwordx2 a[222:223], v30, s[90:91] offset:384 sc1\n\t" "v_mfma_f32_16x16x32_bf16 v[228:231], a[144:147], a[4:7], 0\n\t"
    "ds_read_b64_tr_b16 a[130:131], %[vr3] offset:46592\n\t" "v_mfma_f32_16x16x32_bf16 v[232:235], a[144:147], a[8:11], 0\n\t" "global_load_dword v29, %[va], %[cd]\n\t"
    "v_mfma_f32_16x16x32_bf16 v[236:239], a[144:147], a[12:15], 0\n\t" "v_mfma_f32_16x16x32_bf16 v[240:243], a[148:151], a[0:3], 0\n\t" "ds_read_b64_tr_b16 a[132:133], %[vr3] offset:43040\n\t"
    "v_mfma_f32_16x16x32_bf16 v[244:247], a[148:151], a[4:7], 0\n\t" "ds_read_b64_tr_b16 a[134:135], %[vr3] offset:46624\n\t" "v_mfma_f32_16x16x32_bf16 v[248:251], a[148:151], a[8:11], 0\n\t"
    "v_mfma_f32_16x16x32_bf16 v[252:255], a[148:151], a[12:15], 0\n\t" "s_waitcnt lgkmcnt(4)\n\t" "v_mfma_f32_16x16x32_bf16 v[32:35], a[152:155], a[16:19], v[32:35]\n\t"
    "ds_read_b64_tr_b16 a[136:137], %[vr5] offset:43008\n\t" "v_mfma_f32_16x16x32_bf16 v[36:39], a[152:155], a[20:23], v[36:39]\n\t" "ds_read_b64_tr_b16 a[138:139], %[vr5] offset:46592\n\t"
    "v_mfma_f32_16x16x32_bf16 v[40:43], a[152:155], a[24:27], v[40:43]\n\t" "global_load_dwordx4 a[68:71], %[va], s[76:77] offset:1024 sc1\n\t"
    "v_mfma_f32_16x16x32_bf16 v[44:47], a[152:155], a[28:31], v[44:47]\n\t" "v_mfma_f32_16x16x32_bf16 v[48:51], a[156:159], a[16:19], v[48:51]\n\t" "ds_read_b64_tr_b16 a[140:141], %[vr5] offset:43040\n\t"
    "s_add_u32 s90, s90, 16384\n\t" "s_addc_u32 s91, s91, 0\n\t" "v_mfma_f32_16x16x32_bf16 v[52:55], a[156:159], a[20:23], v[52:55]\n\t" "ds_read_b64_tr_b16 a[142:143], %[vr5] offset:46624\n\t"
    "global_load_dwordx2 a[224:225], v30, s[90:91] offset:0 sc1\n\t" "s_add_u32 m0, %[sl], 44032\n\t" "v_mfma_f32_16x16x32_bf16 v[56:59], a[156:159], a[24:27], v[56:59]\n\t"
    "global_load_lds_dwordx4 %[vb1], s[78:79]\n\t" "v_mfma_f32_16x16x32_bf16 v[60:63], a[156:159], a[28:31], v[60:63]\n\t" "s_waitcnt lgkmcnt(4)\n\t"
    "v_mfma_f32_16x16x32_bf16 v[64:67], a[128:131], a[16:19], v[64:67]\n\t" "ds_read_b64_tr_b16 a[144:145], %[vr7] offset:43008\n\t" "v_mfma_f32_16x16x32_bf16 v[68:71], a[128:131], a[20:23], v[68:71]\n\t"
    "ds_read_b64_tr_b16 a[146:147], %[vr7] offset:46592\n\t" "v_mfma_f32_16x16x32_bf16 v[72:75], a[128:131], a[24:27], v[72:75]\n\t" "global_load_dwordx4 a[72:75], %[va], s[76:77] offset:2048 sc1\n\t"
    "v_mfma_f32_16x16x32_bf16 v[76:79], a[128:131], a[28:31], v[76:79]\n\t" "v_mfma_f32_16x16x32_bf16 v[80:83], a[132:135], a[16:19], v[80:83]\n\t" "ds_read_b64_tr_b16 a[148:149], %[vr1] offset:50176\n\t"
    "v_mfma_f32_16x16x32_bf16 v[84:87], a[132:135], a[20:23], v[84:87]\n\t" "ds_read_b64_tr_b16 a[150:151], %[vr1] offset:53760\n\t" "s_add_u32 m0, %[sl], 45056\n\t"
    "v_mfma_f32_16x16x32_bf16 v[88:91], a[132:135], a[24:27], v[88:91]\n\t" "global_load_lds_dwordx4 %[vb2], s[78:79]\n\t" "v_mfma_f32_16x16x32_bf16 v[92:95], a[132:135], a[28:31], v[92:95]\n\t"
    "s_waitcnt lgkmcnt(4)\n\t" "v_mfma_f32_16x16x32_bf16 v[96:99], a[136:139], a[16:19], v[96:99]\n\t" "ds_read_b64_tr_b16 a[152:153], %[vr1] offset:50208\n\t"
    "v_mfma_f32_16x16x32_bf16 v[100:103], a[136:139], a[20:23], v[100:103]\n\t" "ds_read_b64_tr_b16 a[154:155], %[vr1] offset:53792\n\t" "global_load_dwordx2 a[226:227], v30, s[90:91] offset:128 sc1\n\t"
    "v_mfma_f32_16x16x32_bf16 v[104:107], a[136:139], a[24:27], v[104:107]\n\t" "v_mfma_f32_16x16x32_bf16 v[108:111], a[136:139], a[28:31], v[108:111]\n\t"
    "v_mfma_f32_16x16x32_bf16 v[112:115], a[140:143], a[16:19], v[112:115]\n\t" "ds_read_b64_tr_b16 a[156:157], %[vr3] offset:50176\n\t"
    "v_mfma_f32_16x16x32_bf16 v[116:119], a[140:143], a[20:23], v[116:119]\n\t" "ds_read_b64_tr_b16 a[158:159], %[vr3] offset:53760\n\t"
    "v_mfma_f32_16x16x32_bf16 v[120:123], a[140:143], a[24:27], v[120:123]\n\t" "global_load_dwordx4 a[76:79], %[va], s[76:77] offset:3072 sc1\n\t"
    "v_mfma_f32_16x16x32_bf16 v[124:127], a[140:143], a[28:31], v[124:127]\n\t" "s_add_u32 s76, s76, 16384\n\t" "s_addc_u32 s77, s77, 0\n\t" "s_waitcnt lgkmcnt(4)\n\t"
    "v_mfma_f32_16x16x32_bf16 v[128:131], a[144:147], a[16:19], v[128:131]\n\t" "ds_read_b64_tr_b16 a[128:129], %[vr3] offset:50208\n\t"
    "v_mfma_f32_16x16x32_bf16 v[132:135], a[144:147], a[20:23], v[132:135]\n\t" "ds_read_b64_tr_b16 a[130:131], %[vr3] offset:53792\n\t" "s_add_u32 m0, %[sl], 46080\n\t"
    "v_mfma_f32_16x16x32_bf16 v[136:139], a[144:147], a[24:27], v[136:139]\n\t" "s_mov_b64 exec, s[82:83]\n\t" "global_load_lds_dwordx4 %[vb3], s[78:79]\n\t" "s_mov_b64 exec, -1\n\t"
    "v_mfma_f32_16x16x32_bf16 v[140:143], a[144:147], a[28:31], v[140:143]\n\t" "s_add_u32 s78, s78, 229376\n\t" "s_addc_u32 s79, s79, 0\n\t"
    "v_mfma_f32_16x16x32_bf16 v[144:147], a[148:151], a[16:19], v[144:147]\n\t" "ds_read_b64_tr_b16 a[132:133], %[vr5] offset:50176\n\t"
    "v_mfma_f32_16x16x32_bf16 v[148:151], a[148:151], a[20:23], v[148:151]\n\t" "ds_read_b64_tr_b16 a[134:135], %[vr5] offset:53760\n\t" "global_load_dwordx2 a[228:229], v30, s[90:91] offset:256 sc1\n\t"
    "v_mfma_f32_16x16x32_bf16 v[152:155], a[148:151], a[24:27], v[152:155]\n\t" "global_load_dwordx4 a[80:83], %[va], s[76:77] offset:0 sc1\n\t"
    "v_mfma_f32_16x16x32_bf16 v[156:159], a[148:151], a[28:31], v[156:159]\n\t" "s_waitcnt lgkmcnt(4)\n\t" "v_mfma_f32_16x16x32_bf16 v[160:163], a[152:155], a[16:19], v[160:163]\n\t"
    "ds_read_b64_tr_b16 a[136:137], %[vr5] offset:50208\n\t" "v_mfma_f32_16x16x32_bf16 v[164:167], a[152:155], a[20:23], v[164:167]\n\t" "ds_read_b64_tr_b16 a[138:139], %[vr5] offset:53792\n\t"
    "v_mfma_f32_16x16x32_bf16 v[168:171], a[152:155], a[24:27], v[168:171]\n\t" "v_mfma_f32_16x16x32_bf16 v[172:175], a[152:155], a[28:31], v[172:175]\n\t"
    "v_mfma_f32_16x16x32_bf16 v[176:179], a[156:159], a[16:19], v[176:179]\n\t" "ds_read_b64_tr_b16 a[140:141], %[vr7] offset:50176\n\t"
    "v_mfma_f32_16x16x32_bf16 v[180:183], a[156:159], a[20:23], v[180:183]\n\t" "ds_read_b64_tr_b16 a[142:143], %[vr7] offset:53760\n\t" "s_add_u32 m0, %[sl], 57344\n\t"
    "v_mfma_f32_16x16x32_bf16 v[184:187], a[156:159], a[24:27], v[184:187]\n\t" "global_load_lds_dwordx4 %[vb0], s[78:79]\n\t" "v_mfma_f32_16x16x32_bf16 v[188:191], a[156:159], a[28:31], v[188:191]\n\t"
    "s_waitcnt lgkmcnt(4)\n\t" "v_mfma_f32_16x16x32_bf16 v[192:195], a[128:131], a[16:19], v[192:195]\n\t" "s_barrier\n\t" "ds_read_b64_tr_b16 a[144:145], %[vr0] offset:0\n\t"
    "v_mfma_f32_16x16x32_bf16 v[196:199], a[128:131], a[20:23], v[196:199]\n\t" "ds_read_b64_tr_b16 a[146:147], %[vr0] offset:3584\n\t" "global_load_dwordx2 a[230:231], v30, s[90:91] offset:384 sc1\n\t"
    "v_mfma_f32_16x16x32_bf16 v[200:203], a[128:131], a[24:27], v[200:203]\n\t" "global_load_dwordx4 a[84:87], %[va], s[76:77] offset:1024 sc1\n\t"
    "v_mfma_f32_16x16x32_bf16 v[204:207], a[128:131], a[28:31], v[204:207]\n\t" "v_mfma_f32_16x16x32_bf16 v[208:211], a[132:135], a[16:19], v[208:211]\n\t"
    "ds_read_b64_tr_b16 a[148:149], %[vr0] offset:14336\n\t" "v_mfma_f32_16x16x32_bf16 v[212:215], a[132:135], a[20:23], v[212:215]\n\t" "ds_read_b64_tr_b16 a[150:151], %[vr0] offset:17920\n\t"
    "s_add_u32 m0, %[sl], 58368\n\t" "v_mfma_f32_16x16x32_bf16 v[216:219], a[132:135], a[24:27], v[216:219]\n\t" "global_load_lds_dwordx4 %[vb1], s[78:79]\n\t"
    "v_mfma_f32_16x16x32_bf16 v[220:223], a[132:135], a[28:31], v[220:223]\n\t" "s_waitcnt lgkmcnt(4)\n\t" "v_mfma_f32_16x16x32_bf16 v[224:227], a[136:139], a[16:19], v[224:227]\n\t"
    "ds_read_b64_tr_b16 a[152:153], %[vr0] offset:32\n\t" "v_mfma_f32_16x16x32_bf16 v[228:231], a[136:139], a[20:23], v[228:231]\n\t" "ds_read_b64_tr_b16 a[154:155], %[vr0] offset:3616\n\t"
    "v_mfma_f32_16x16x32_bf16 v[232:235], a[136:139], a[24:27], v[232:235]\n\t" "v_mfma_f32_16x16x32_bf16 v[236:239], a[136:139], a[28:31], v[236:239]\n\t"
    "v_mfma_f32_16x16x32_bf16 v[240:243], a[140:143], a[16:19], v[240:243]\n\t" "ds_read_b64_tr_b16 a[156:157], %[vr0] offset:14368\n\t"
    "v_mfma_f32_16x16x32_bf16 v[244:247], a[140:143], a[20:23], v[244:247]\n\t" "ds_read_b64_tr_b16 a[158:159], %[vr0] offset:17952\n\t" "s_add_u32 s90, s90, 16384\n\t" "s_addc_u32 s91, s91, 0\n\t"
    "v_mfma_f32_16x16x32_bf16 v[248:251], a[140:143], a[24:27], v[248:251]\n\t" "global_load_dwordx2 a[232:233], v30, s[90:91] offset:0 sc1\n\t"
    "v_mfma_f32_16x16x32_bf16 v[252:255], a[140:143], a[28:31], v[252:255]\n\t" "s_waitcnt lgkmcnt(4) vmcnt(21)\n\t" "v_mfma_f32_16x16x32_bf16 v[32:35], a[144:147], a[32:35], v[32:35]\n\t" "s_barrier\n\t"
    "ds_read_b64_tr_b16 a[128:129], %[vr2] offset:0\n\t" "v_mfma_f32_16x16x32_bf16 v[32:35], a[148:151], a[48:51], v[32:35]\n\t" "ds_read_b64_tr_b16 a[130:131], %[vr2] offset:3584\n\t"
    "v_mfma_f32_16x16x32_bf16 v[36:39], a[144:147], a[36:39], v[36:39]\n\t" "global_load_dwordx4 a[88:91], %[va], s[76:77] offset:2048 sc1\n\t"
    "v_mfma_f32_16x16x32_bf16 v[36:39], a[148:151], a[52:55], v[36:39]\n\t" "v_mfma_f32_16x16x32_bf16 v[40:43], a[144:147], a[40:43], v[40:43]\n\t" "ds_read_b64_tr_b16 a[132:133], %[vr2] offset:14336\n\t"
    "v_mfma_f32_16x16x32_bf16 v[40:43], a[148:151], a[56:59], v[40:43]\n\t" "ds_read_b64_tr_b16 a[134:135], %[vr2] offset:17920\n\t" "s_add_u32 m0, %[sl], 59392\n\t"
    "v_mfma_f32_16x16x32_bf16 v[44:47], a[144:147], a[44:47], v[44:47]\n\t" "global_load_lds_dwordx4 %[vb2], s[78:79]\n\t" "v_mfma_f32_16x16x32_bf16 v[44:47], a[148:151], a[60:63], v[44:47]\n\t"
    "s_waitcnt lgkmcnt(4)\n\t" "v_mfma_f32_16x16x32_bf16 v[48:51], a[152:155], a[32:35], v[48:51]\n\t" "ds_read_b64_tr_b16 a[136:137], %[vr2] offset:32\n\t"
    "v_mfma_f32_16x16x32_bf16 v[48:51], a[156:159], a[48:51], v[48:51]\n\t" "ds_read_b64_tr_b16 a[138:139], %[vr2] offset:3616\n\t" "v_mfma_f32_16x16x32_bf16 v[52:55], a[152:155], a[36:39], v[52:55]\n\t"
    "global_load_dwordx4 a[92:95], %[va], s[76:77] offset:3072 sc1\n\t" "global_load_dwordx2 a[234:235], v30, s[90:91] offset:128 sc1\n\t"
    "v_mfma_f32_16x16x32_bf16 v[52:55], a[156:159], a[52:55], v[52:55]\n\t" "s_add_u32 s76, s76, 16384\n\t" "s_addc_u32 s77, s77, 0\n\t"
    "v_mfma_f32_16x16x32_bf16 v[56:59], a[152:155], a[40:43], v[56:59]\n\t" "ds_read_b64_tr_b16 a[140:141], %[vr2] offset:14368\n\t" "v_mfma_f32_16x16x32_bf16 v[56:59], a[156:159], a[56:59], v[56:59]\n\t"
    "ds_read_b64_tr_b16 a[142:143], %[vr2] offset:17952\n\t" "s_add_u32 m0, %[sl], 60416\n\t" "v_mfma_f32_16x16x32_bf16 v[60:63], a[152:155], a[44:47], v[60:63]\n\t" "s_mov_b64 exec, s[82:83]\n\t"
    "global_load_lds_dwordx4 %[vb3], s[78:79]\n\t" "s_mov_b64 exec, -1\n\t" "v_mfma_f32_16x16x32_bf16 v[60:63], a[156:159], a[60:63], v[60:63]\n\t" "s_add_u32 s78, s78, 229376\n\t"
    "s_addc_u32 s79, s79, 0\n\t" "s_waitcnt lgkmcnt(4)\n\t" "v_mfma_f32_16x16x32_bf16 v[64:67], a[128:131], a[32:35], v[64:67]\n\t" "ds_read_b64_tr_b16 a[144:145], %[vr4] offset:0\n\t"
    "v_mfma_f32_16x16x32_bf16 v[64:67], a[132:135], a[48:51], v[64:67]\n\t" "ds_read_b64_tr_b16 a[146:147], %[vr4] offset:3584\n\t" "v_mfma_f32_16x16x32_bf16 v[68:71], a[128:131], a[36:39], v[68:71]\n\t"
    "v_mfma_f32_16x16x32_bf16 v[68:71], a[132:135], a[52:55], v[68:71]\n\t" "v_mfma_f32_16x16x32_bf16 v[72:75], a[128:131], a[40:43], v[72:75]\n\t" "ds_read_b64_tr_b16 a[148:149], %[vr4] offset:14336\n\t"
    "v_mfma_f32_16x16x32_bf16 v[72:75], a[132:135], a[56:59], v[72:75]\n\t" "ds_read_b64_tr_b16 a[150:151], %[vr4] offset:17920\n\t" "v_mfma_f32_16x16x32_bf16 v[76:79], a[128:131], a[44:47], v[76:79]\n\t"
    "global_load_dwordx4 a[96:99], %[va], s[76:77] offset:0 sc1\n\t" "global_load_dwordx2 a[236:237], v30, s[90:91] offset:256 sc1\n\t"
    "v_mfma_f32_16x16x32_bf16 v[76:79], a[132:135], a[60:63], v[76:79]\n\t" "s_waitcnt lgkmcnt(4)\n\t" "v_mfma_f32_16x16x32_bf16 v[80:83], a[136:139], a[32:35], v[80:83]\n\t"
    "ds_read_b64_tr_b16 a[152:153], %[vr4] offset:32\n\t" "v_mfma_f32_16x16x32_bf16 v[80:83], a[140:143], a[48:51], v[80:83]\n\t" "ds_read_b64_tr_b16 a[154:155], %[vr4] offset:3616\n\t"
    "s_add_u32 m0, %[sl], 71680\n\t" "v_mfma_f32_16x16x32_bf16 v[84:87], a[136:139], a[36:39], v[84:87]\n\t" "global_load_lds_dwordx4 %[vb0], s[78:79]\n\t"
    "v_mfma_f32_16x16x32_bf16 v[84:87], a[140:143], a[52:55], v[84:87]\n\t" "v_mfma_f32_16x16x32_bf16 v[88:91], a[136:139], a[40:43], v[88:91]\n\t" "ds_read_b64_tr_b16 a[156:157], %[vr4] offset:14368\n\t"
    "v_mfma_f32_16x16x32_bf16 v[88:91], a[140:143], a[56:59], v[88:91]\n\t" "ds_read_b64_tr_b16 a[158:159], %[vr4] offset:17952\n\t" "v_mfma_f32_16x16x32_bf16 v[92:95], a[136:139], a[44:47], v[92:95]\n\t"
    "global_load_dwordx4 a[100:103], %[va], s[76:77] offset:1024 sc1\n\t" "v_mfma_f32_16x16x32_bf16 v[92:95], a[140:143], a[60:63], v[92:95]\n\t" "s_waitcnt lgkmcnt(4)\n\t"
    "v_mfma_f32_16x16x32_bf16 v[96:99], a[144:147], a[32:35], v[96:99]\n\t" "ds_read_b64_tr_b16 a[128:129], %[vr6] offset:0\n\t" "v_mfma_f32_16x16x32_bf16 v[96:99], a[148:151], a[48:51], v[96:99]\n\t"
    "ds_read_b64_tr_b16 a[130:131], %[vr6] offset:3584\n\t" "v_mfma_f32_16x16x32_bf16 v[100:103], a[144:147], a[36:39], v[100:103]\n\t" "global_load_dwordx2 a[238:239], v30, s[90:91] offset:384 sc1\n\t"
    "v_mfma_f32_16x16x32_bf16 v[100:103], a[148:151], a[52:55], v[100:103]\n\t" "v_mfma_f32_16x16x32_bf16 v[104:107], a[144:147], a[40:43], v[104:107]\n\t"
    "ds_read_b64_tr_b16 a[132:133], %[vr6] offset:14336\n\t" "v_mfma_f32_16x16x32_bf16 v[104:107], a[148:151], a[56:59], v[104:107]\n\t" "ds_read_b64_tr_b16 a[134:135], %[vr6] offset:17920\n\t"
    "s_add_u32 m0, %[sl], 72704\n\t" "v_mfma_f32_16x16x32_bf16 v[108:111], a[144:147], a[44:47], v[108:111]\n\t" "global_load_lds_dwordx4 %[vb1], s[78:79]\n\t"
    "v_mfma_f32_16x16x32_bf16 v[108:111], a[148:151], a[60:63], v[108:111]\n\t" "s_waitcnt lgkmcnt(4)\n\t" "v_mfma_f32_16x16x32_bf16 v[112:115], a[152:155], a[32:35], v[112:115]\n\t"
    "ds_read_b64_tr_b16 a[136:137], %[vr0] offset:7168\n\t" "v_mfma_f32_16x16x32_bf16 v[112:115], a[156:159], a[48:51], v[112:115]\n\t" "ds_read_b64_tr_b16 a[138:139], %[vr0] offset:10752\n\t"
    "v_mfma_f32_16x16x32_bf16 v[116:119], a[152:155], a[36:39], v[116:119]\n\t" "global_load_dwordx4 a[104:107], %[va], s[76:77] offset:2048 sc1\n\t"
    "v_mfma_f32_16x16x32_bf16 v[116:119], a[156:159], a[52:55], v[116:119]\n\t" "v_mfma_f32_16x16x32_bf16 v[120:123], a[152:155], a[40:43], v[120:123]\n\t"
    "ds_read_b64_tr_b16 a[140:141], %[vr0] offset:21504\n\t" "v_mfma_f32_16x16x32_bf16 v[120:123], a[156:159], a[56:59], v[120:123]\n\t" "ds_read_b64_tr_b16 a[142:143], %[vr0] offset:25088\n\t"
    "s_add_u32 m0, %[sl], 73728\n\t" "v_mfma_f32_16x16x32_bf16 v[124:127], a[152:155], a[44:47], v[124:127]\n\t" "global_load_lds_dwordx4 %[vb2], s[78:79]\n\t" "s_add_u32 s90, s90, 16384\n\t"
    "s_addc_u32 s91, s91, 0\n\t" "v_mfma_f32_16x16x32_bf16 v[124:127], a[156:159], a[60:63], v[124:127]\n\t" "global_load_dwordx2 a[240:241], v30, s[90:91] offset:0 sc1\n\t" "s_waitcnt lgkmcnt(4)\n\t"
    "v_mfma_f32_16x16x32_bf16 v[128:131], a[128:131], a[32:35], v[128:131]\n\t" "ds_read_b64_tr_b16 a[144:145], %[vr0] offset:7200\n\t"
    "v_mfma_f32_16x16x32_bf16 v[128:131], a[132:135], a[48:51], v[128:131]\n\t" "ds_read_b64_tr_b16 a[146:147], %[vr0] offset:10784\n\t"
    "v_mfma_f32_16x16x32_bf16 v[132:135], a[128:131], a[36:39], v[132:135]\n\t" "v_mfma_f32_16x16x32_bf16 v[132:135], a[132:135], a[52:55], v[132:135]\n\t"
    "v_mfma_f32_16x16x32_bf16 v[136:139], a[128:131], a[40:43], v[136:139]\n\t" "ds_read_b64_tr_b16 a[148:149], %[vr0] offset:21536\n\t"
    "v_mfma_f32_16x16x32_bf16 v[136:139], a[132:135], a[56:59], v[136:139]\n\t" "ds_read_b64_tr_b16 a[150:151], %[vr0] offset:25120\n\t"
    "v_mfma_f32_16x16x32_bf16 v[140:143], a[128:131], a[44:47], v[140:143]\n\t" "v_mfma_f32_16x16x32_bf16 v[140:143], a[132:135], a[60:63], v[140:143]\n\t" "s_waitcnt lgkmcnt(4)\n\t"
    "v_mfma_f32_16x16x32_bf16 v[144:147], a[136:139], a[32:35], v[144:147]\n\t" "ds_read_b64_tr_b16 a[152:153], %[vr2] offset:7168\n\t"
    "v_mfma_f32_16x16x32_bf16 v[144:147], a[140:143], a[48:51], v[144:147]\n\t" "ds_read_b64_tr_b16 a[154:155], %[vr2] offset:10752\n\t"
    "v_mfma_f32_16x16x32_bf16 v[148:151], a[136:139], a[36:39], v[148:151]\n\t" "global_load_dwordx4 a[108:111], %[va], s[76:77] offset:3072 sc1\n\t"
    "v_mfma_f32_16x16x32_bf16 v[148:151], a[140:143], a[52:55], v[148:151]\n\t" "s_add_u32 s76, s76, 16384\n\t" "s_addc_u32 s77, s77, 0\n\t"
    "global_load_dwordx2 a[242:243], v30, s[90:91] offset:128 sc1\n\t" "v_mfma_f32_16x16x32_bf16 v[152:155], a[136:139], a[40:43], v[152:155]\n\t" "ds_read_b64_tr_b16 a[156:157], %[vr2] offset:21504\n\t"
    "v_mfma_f32_16x16x32_bf16 v[152:155], a[140:143], a[56:59], v[152:155]\n\t" "ds_read_b64_tr_b16 a[158:159], %[vr2] offset:25088\n\t" "s_add_u32 m0, %[sl], 74752\n\t"
    "v_mfma_f32_16x16x32_bf16 v[156:159], a[136:139], a[44:47], v[156:159]\n\t" "s_mov_b64 exec, s[82:83]\n\t" "global_load_lds_dwordx4 %[vb3], s[78:79]\n\t" "s_mov_b64 exec, -1\n\t"
    "v_mfma_f32_16x16x32_bf16 v[156:159], a[140:143], a[60:63], v[156:159]\n\t" "s_add_u32 s78, s78, s88\n\t" "s_addc_u32 s79, s79, s89\n\t" "s_waitcnt lgkmcnt(4)\n\t"
    "v_mfma_f32_16x16x32_bf16 v[160:163], a[144:147], a[32:35], v[160:163]\n\t" "ds_read_b64_tr_b16 a[128:129], %[vr2] offset:7200\n\t"
    "v_mfma_f32_16x16x32_bf16 v[160:163], a[148:151], a[48:51], v[160:163]\n\t" "ds_read_b64_tr_b16 a[130:131], %[vr2] offset:10784\n\t"
    "v_mfma_f32_16x16x32_bf16 v[164:167], a[144:147], a[36:39], v[164:167]\n\t" "global_load_dwordx4 a[112:115], %[va], s[76:77] offset:0 sc1\n\t"
    "v_mfma_f32_16x16x32_bf16 v[164:167], a[148:151], a[52:55], v[164:167]\n\t" "v_mfma_f32_16x16x32_bf16 v[168:171], a[144:147], a[40:43], v[168:171]\n\t"
    "ds_read_b64_tr_b16 a[132:133], %[vr2] offset:21536\n\t" "v_mfma_f32_16x16x32_bf16 v[168:171], a[148:151], a[56:59], v[168:171]\n\t" "ds_read_b64_tr_b16 a[134:135], %[vr2] offset:25120\n\t"
    "s_add_u32 m0, %[sl], 86016\n\t" "v_mfma_f32_16x16x32_bf16 v[172:175], a[144:147], a[44:47], v[172:175]\n\t" "global_load_lds_dwordx4 %[vb0], s[78:79]\n\t"
    "v_mfma_f32_16x16x32_bf16 v[172:175], a[148:151], a[60:63], v[172:175]\n\t" "global_load_dwordx2 a[244:245], v30, s[90:91] offset:256 sc1\n\t" "s_waitcnt lgkmcnt(4)\n\t"
    "v_mfma_f32_16x16x32_bf16 v[176:179], a[152:155], a[32:35], v[176:179]\n\t" "ds_read_b64_tr_b16 a[136:137], %[vr4] offset:7168\n\t"
    "v_mfma_f32_16x16x32_bf16 v[176:179], a[156:159], a[48:51], v[176:179]\n\t" "ds_read_b64_tr_b16 a[138:139], %[vr4] offset:10752\n\t"
    "v_mfma_f32_16x16x32_bf16 v[180:183], a[152:155], a[36:39], v[180:183]\n\t" "v_mfma_f32_16x16x32_bf16 v[180:183], a[156:159], a[52:55], v[180:183]\n\t"
    "v_mfma_f32_16x16x32_bf16 v[184:187], a[152:155], a[40:43], v[184:187]\n\t" "ds_read_b64_tr_b16 a[140:141], %[vr4] offset:21504\n\t"
    "v_mfma_f32_16x16x32_bf16 v[184:187], a[156:159], a[56:59], v[184:187]\n\t" "ds_read_b64_tr_b16 a[142:143], %[vr4] offset:25088\n\t"
    "v_mfma_f32_16x16x32_bf16 v[188:191], a[152:155], a[44:47], v[188:191]\n\t" "global_load_dwordx4 a[116:119], %[va], s[76:77] offset:1024 sc1\n\t"
    "v_mfma_f32_16x16x32_bf16 v[188:191], a[156:159], a[60:63], v[188:191]\n\t" "s_waitcnt lgkmcnt(4)\n\t" "v_mfma_f32_16x16x32_bf16 v[192:195], a[128:131], a[32:35], v[192:195]\n\t"
    "ds_read_b64_tr_b16 a[144:145], %[vr4] offset:7200\n\t" "v_mfma_f32_16x16x32_bf16 v[192:195], a[132:135], a[48:51], v[192:195]\n\t" "ds_read_b64_tr_b16 a[146:147], %[vr4] offset:10784\n\t"
    "s_add_u32 m0, %[sl], 87040\n\t" "v_mfma_f32_16x16x32_bf16 v[196:199], a[128:131], a[36:39], v[196:199]\n\t" "global_load_lds_dwordx4 %[vb1], s[78:79]\n\t"
    "v_mfma_f32_16x16x32_bf16 v[196:199], a[132:135], a[52:55], v[196:199]\n\t" "global_load_dwordx2 a[246:247], v30, s[90:91] offset:384 sc1\n\t"
    "v_mfma_f32_16x16x32_bf16 v[200:203], a[128:131], a[40:43], v[200:203]\n\t" "ds_read_b64_tr_b16 a[148:149], %[vr4] offset:21536\n\t"
    "v_mfma_f32_16x16x32_bf16 v[200:203], a[132:135], a[56:59], v[200:203]\n\t" "ds_read_b64_tr_b16 a[150:151], %[vr4] offset:25120\n\t"
    "v_mfma_f32_16x16x32_bf16 v[204:207], a[128:131], a[44:47], v[204:207]\n\t" "global_load_dwordx4 a[120:123], %[va], s[76:77] offset:2048 sc1\n\t"
    "v_mfma_f32_16x16x32_bf16 v[204:207], a[132:135], a[60:63], v[204:207]\n\t" "s_waitcnt lgkmcnt(4)\n\t" "v_mfma_f32_16x16x32_bf16 v[208:211], a[136:139], a[32:35], v[208:211]\n\t"
    "ds_read_b64_tr_b16 a[152:153], %[vr6] offset:7168\n\t" "v_mfma_f32_16x16x32_bf16 v[208:211], a[140:143], a[48:51], v[208:211]\n\t" "ds_read_b64_tr_b16 a[154:155], %[vr6] offset:10752\n\t"
    "v_mfma_f32_16x16x32_bf16 v[212:215], a[136:139], a[36:39], v[212:215]\n\t" "v_mfma_f32_16x16x32_bf16 v[212:215], a[140:143], a[52:55], v[212:215]\n\t"
    "v_mfma_f32_16x16x32_bf16 v[216:219], a[136:139], a[40:43], v[216:219]\n\t" "ds_read_b64_tr_b16 a[156:157], %[vr6] offset:21504\n\t"
    "v_mfma_f32_16x16x32_bf16 v[216:219], a[140:143], a[56:59], v[216:219]\n\t" "ds_read_b64_tr_b16 a[158:159], %[vr6] offset:25088\n\t" "s_add_u32 m0, %[sl], 88064\n\t"
    "v_mfma_f32_16x16x32_bf16 v[220:223], a[136:139], a[44:47], v[220:223]\n\t" "global_load_lds_dwordx4 %[vb2], s[78:79]\n\t" "v_mfma_f32_16x16x32_bf16 v[220:223], a[140:143], a[60:63], v[220:223]\n\t"
    "s_waitcnt lgkmcnt(4)\n\t" "v_mfma_f32_16x16x32_bf16 v[224:227], a[144:147], a[32:35], v[224:227]\n\t" "s_waitcnt vmcnt(32)\n\t" "s_barrier\n\t"
    "ds_read_b64_tr_b16 a[128:129], %[vr0] offset:28672\n\t" "v_mfma_f32_16x16x32_bf16 v[224:227], a[148:151], a[48:51], v[224:227]\n\t" "ds_read_b64_tr_b16 a[130:131], %[vr0] offset:32256\n\t"
    "v_mfma_f32_16x16x32_bf16 v[228:231], a[144:147], a[36:39], v[228:231]\n\t" "global_load_dwordx4 a[124:127], %[va], s[76:77] offset:3072 sc1\n\t"
    "v_mfma_f32_16x16x32_bf16 v[228:231], a[148:151], a[52:55], v[228:231]\n\t" "s_add_u32 s76, s76, s86\n\t" "s_addc_u32 s77, s77, s87\n\t"
    "v_mfma_f32_16x16x32_bf16 v[232:235], a[144:147], a[40:43], v[232:235]\n\t" "ds_read_b64_tr_b16 a[132:133], %[vr0] offset:43008\n\t"
    "v_mfma_f32_16x16x32_bf16 v[232:235], a[148:151], a[56:59], v[232:235]\n\t" "ds_read_b64_tr_b16 a[134:135], %[vr0] offset:46592\n\t" "s_add_u32 m0, %[sl], 89088\n\t"
    "v_mfma_f32_16x16x32_bf16 v[236:239], a[144:147], a[44:47], v[236:239]\n\t" "s_mov_b64 exec, s[82:83]\n\t" "global_load_lds_dwordx4 %[vb3], s[78:79]\n\t" "s_mov_b64 exec, -1\n\t"
    "v_mfma_f32_16x16x32_bf16 v[236:239], a[148:151], a[60:63], v[236:239]\n\t" "s_add_u32 s78, s78, 229376\n\t" "s_addc_u32 s79, s79, 0\n\t" "s_waitcnt lgkmcnt(4)\n\t"
    "v_mfma_f32_16x16x32_bf16 v[240:243], a[152:155], a[32:35], v[240:243]\n\t" "ds_read_b64_tr_b16 a[136:137], %[vr0] offset:28704\n\t"
    "v_mfma_f32_16x16x32_bf16 v[240:243], a[156:159], a[48:51], v[240:243]\n\t" "ds_read_b64_tr_b16 a[138:139], %[vr0] offset:32288\n\t"
    "v_mfma_f32_16x16x32_bf16 v[244:247], a[152:155], a[36:39], v[244:247]\n\t" "v_mfma_f32_16x16x32_bf16 v[244:247], a[156:159], a[52:55], v[244:247]\n\t"
    "v_mfma_f32_16x16x32_bf16 v[248:251], a[152:155], a[40:43], v[248:251]\n\t" "ds_read_b64_tr_b16 a[140:141], %[vr0] offset:43040\n\t"
    "v_mfma_f32_16x16x32_bf16 v[248:251], a[156:159], a[56:59], v[248:251]\n\t" "ds_read_b64_tr_b16 a[142:143], %[vr0] offset:46624\n\t"
    "v_mfma_f32_16x16x32_bf16 v[252:255], a[152:155], a[44:47], v[252:255]\n\t" "v_mfma_f32_16x16x32_bf16 v[252:255], a[156:159], a[60:63], v[252:255]\n\t" ".Lloop_%=:\n\t" "s_cmp_eq_u32 s80, %[e1]\n\t"
    "s_cbranch_scc1 .LE1_%=\n\t" "s_waitcnt lgkmcnt(4) vmcnt(17)\n\t" "v_mfma_f32_16x16x32_bf16 v[32:35], a[128:131], a[64:67], v[32:35]\n\t" "s_barrier\n\t"
    "ds_read_b64_tr_b16 a[144:145], %[vr2] offset:28672\n\t" "v_mfma_f32_16x16x32_bf16 v[32:35], a[132:135], a[80:83], v[32:35]\n\t" "ds_read_b64_tr_b16 a[146:147], %[vr2] offset:32256\n\t"
    "v_mfma_f32_16x16x32_bf16 v[36:39], a[128:131], a[68:71], v[36:39]\n\t" "global_load_dwordx4 a[0:3], %[va], s[76:77] offset:0 sc1\n\t"
    "v_mfma_f32_16x16x32_bf16 v[36:39], a[132:135], a[84:87], v[36:39]\n\t" "v_mfma_f32_16x16x32_bf16 v[40:43], a[128:131], a[72:75], v[40:43]\n\t" "ds_read_b64_tr_b16 a[148:149], %[vr2] offset:43008\n\t"
    "v_mfma_f32_16x16x32_bf16 v[40:43], a[132:135], a[88:91], v[40:43]\n\t" "ds_read_b64_tr_b16 a[150:151], %[vr2] offset:46592\n\t" "s_add_u32 m0, %[sl], 100352\n\t"
    "v_mfma_f32_16x16x32_bf16 v[44:47], a[128:131], a[76:79], v[44:47]\n\t" "global_load_lds_dwordx4 %[vb0], s[78:79]\n\t" "v_mfma_f32_16x16x32_bf16 v[44:47], a[132:135], a[92:95], v[44:47]\n\t"
    "s_waitcnt lgkmcnt(4)\n\t" "v_mfma_f32_16x16x32_bf16 v[48:51], a[136:139], a[64:67], v[48:51]\n\t" "ds_read_b64_tr_b16 a[152:153], %[vr2] offset:28704\n\t"
    "v_mfma_f32_16x16x32_bf16 v[48:51], a[140:143], a[80:83], v[48:51]\n\t" "ds_read_b64_tr_b16 a[154:155], %[vr2] offset:32288\n\t" "v_mfma_f32_16x16x32_bf16 v[52:55], a[136:139], a[68:71], v[52:55]\n\t"
    "v_mfma_f32_16x16x32_bf16 v[52:55], a[140:143], a[84:87], v[52:55]\n\t" "v_mfma_f32_16x16x32_bf16 v[56:59], a[136:139], a[72:75], v[56:59]\n\t" "ds_read_b64_tr_b16 a[156:157], %[vr2] offset:43040\n\t"
    "v_mfma_f32_16x16x32_bf16 v[56:59], a[140:143], a[88:91], v[56:59]\n\t" "ds_read_b64_tr_b16 a[158:159], %[vr2] offset:46624\n\t" "v_mfma_f32_16x16x32_bf16 v[60:63], a[136:139], a[76:79], v[60:63]\n\t"
    "global_load_dwordx4 a[4:7], %[va], s[76:77] offset:1024 sc1\n\t" "v_mfma_f32_16x16x32_bf16 v[60:63], a[140:143], a[92:95], v[60:63]\n\t" "s_waitcnt lgkmcnt(4)\n\t"
    "v_mfma_f32_16x16x32_bf16 v[64:67], a[144:147], a[64:67], v[64:67]\n\t" "ds_read_b64_tr_b16 a[128:129], %[vr4] offset:28672\n\t" "v_mfma_f32_16x16x32_bf16 v[64:67], a[148:151], a[80:83], v[64:67]\n\t"
    "ds_read_b64_tr_b16 a[130:131], %[vr4] offset:32256\n\t" "s_add_u32 m0, %[sl], 101376\n\t" "v_mfma_f32_16x16x32_bf16 v[68:71], a[144:147], a[68:71], v[68:71]\n\t"
    "global_load_lds_dwordx4 %[vb1], s[78:79]\n\t" "v_mfma_f32_16x16x32_bf16 v[68:71], a[148:151], a[84:87], v[68:71]\n\t" "v_mfma_f32_16x16x32_bf16 v[72:75], a[144:147], a[72:75], v[72:75]\n\t"
    "ds_read_b64_tr_b16 a[132:133], %[vr4] offset:43008\n\t" "v_mfma_f32_16x16x32_bf16 v[72:75], a[148:151], a[88:91], v[72:75]\n\t" "ds_read_b64_tr_b16 a[134:135], %[vr4] offset:46592\n\t"
    "v_mfma_f32_16x16x32_bf16 v[76:79], a[144:147], a[76:79], v[76:79]\n\t" "v_mfma_f32_16x16x32_bf16 v[76:79], a[148:151], a[92:95], v[76:79]\n\t" "s_waitcnt lgkmcnt(4)\n\t"
    "v_mfma_f32_16x16x32_bf16 v[80:83], a[152:155], a[64:67], v[80:83]\n\t" "ds_read_b64_tr_b16 a[136:137], %[vr4] offset:28704\n\t" "v_mfma_f32_16x16x32_bf16 v[80:83], a[156:159], a[80:83], v[80:83]\n\t"
    "ds_read_b64_tr_b16 a[138:139], %[vr4] offset:32288\n\t" "v_mfma_f32_16x16x32_bf16 v[84:87], a[152:155], a[68:71], v[84:87]\n\t" "global_load_dwordx4 a[8:11], %[va], s[76:77] offset:2048 sc1\n\t"
    "v_mfma_f32_16x16x32_bf16 v[84:87], a[156:159], a[84:87], v[84:87]\n\t" "v_mfma_f32_16x16x32_bf16 v[88:91], a[152:155], a[72:75], v[88:91]\n\t" "ds_read_b64_tr_b16 a[140:141], %[vr4] offset:43040\n\t"
    "v_mfma_f32_16x16x32_bf16 v[88:91], a[156:159], a[88:91], v[88:91]\n\t" "ds_read_b64_tr_b16 a[142:143], %[vr4] offset:46624\n\t" "v_mfma_f32_16x16x32_bf16 v[92:95], a[152:155], a[76:79], v[92:95]\n\t"
    "v_mfma_f32_16x16x32_bf16 v[92:95], a[156:159], a[92:95], v[92:95]\n\t" "s_waitcnt lgkmcnt(4)\n\t" "v_mfma_f32_16x16x32_bf16 v[96:99], a[128:131], a[64:67], v[96:99]\n\t"
    "ds_read_b64_tr_b16 a[144:145], %[vr6] offset:28672\n\t" "v_mfma_f32_16x16x32_bf16 v[96:99], a[132:135], a[80:83], v[96:99]\n\t" "ds_read_b64_tr_b16 a[146:147], %[vr6] offset:32256\n\t"
    "s_add_u32 m0, %[sl], 102400\n\t" "v_mfma_f32_16x16x32_bf16 v[100:103], a[128:131], a[68:71], v[100:103]\n\t" "global_load_lds_dwordx4 %[vb2], s[78:79]\n\t"
    "v_mfma_f32_16x16x32_bf16 v[100:103], a[132:135], a[84:87], v[100:103]\n\t" "v_mfma_f32_16x16x32_bf16 v[104:107], a[128:131], a[72:75], v[104:107]\n\t"
    "ds_read_b64_tr_b16 a[148:149], %[vr6] offset:43008\n\t" "v_mfma_f32_16x16x32_bf16 v[104:107], a[132:135], a[88:91], v[104:107]\n\t" "ds_read_b64_tr_b16 a[150:151], %[vr6] offset:46592\n\t"
    "v_mfma_f32_16x16x32_bf16 v[108:111], a[128:131], a[76:79], v[108:111]\n\t" "global_load_dwordx4 a[12:15], %[va], s[76:77] offset:3072 sc1\n\t"
    "v_mfma_f32_16x16x32_bf16 v[108:111], a[132:135], a[92:95], v[108:111]\n\t" "s_add_u32 s76, s76, 16384\n\t" "s_addc_u32 s77, s77, 0\n\t" "s_waitcnt lgkmcnt(4)\n\t"
    "v_mfma_f32_16x16x32_bf16 v[112:115], a[136:139], a[64:67], v[112:115]\n\t" "ds_read_b64_tr_b16 a[152:153], %[vr0] offset:35840\n\t"
    "v_mfma_f32_16x16x32_bf16 v[112:115], a[140:143], a[80:83], v[112:115]\n\t" "ds_read_b64_tr_b16 a[154:155], %[vr0] offset:39424\n\t"
    "v_mfma_f32_16x16x32_bf16 v[116:119], a[136:139], a[68:71], v[116:119]\n\t" "v_mfma_f32_16x16x32_bf16 v[116:119], a[140:143], a[84:87], v[116:119]\n\t"
    "v_mfma_f32_16x16x32_bf16 v[120:123], a[136:139], a[72:75], v[120:123]\n\t" "ds_read_b64_tr_b16 a[156:157], %[vr0] offset:50176\n\t"
    "v_mfma_f32_16x16x32_bf16 v[120:123], a[140:143], a[88:91], v[120:123]\n\t" "ds_read_b64_tr_b16 a[158:159], %[vr0] offset:53760\n\t" "s_add_u32 m0, %[sl], 103424\n\t"
    "v_mfma_f32_16x16x32_bf16 v[124:127], a[136:139], a[76:79], v[124:127]\n\t" "s_mov_b64 exec, s[82:83]\n\t" "global_load_lds_dwordx4 %[vb3], s[78:79]\n\t" "s_mov_b64 exec, -1\n\t"
    "v_mfma_f32_16x16x32_bf16 v[124:127], a[140:143], a[92:95], v[124:127]\n\t" "s_add_u32 s78, s78, 229376\n\t" "s_addc_u32 s79, s79, 0\n\t" "s_waitcnt lgkmcnt(4)\n\t"
    "v_mfma_f32_16x16x32_bf16 v[128:131], a[144:147], a[64:67], v[128:131]\n\t" "ds_read_b64_tr_b16 a[128:129], %[vr0] offset:35872\n\t"
    "v_mfma_f32_16x16x32_bf16 v[128:131], a[148:151], a[80:83], v[128:131]\n\t" "ds_read_b64_tr_b16 a[130:131], %[vr0] offset:39456\n\t"
    "v_mfma_f32_16x16x32_bf16 v[132:135], a[144:147], a[68:71], v[132:135]\n\t" "global_load_dword v29, %[va], %[cd]\n\t" "v_mfma_f32_16x16x32_bf16 v[132:135], a[148:151], a[84:87], v[132:135]\n\t"
    "v_mfma_f32_16x16x32_bf16 v[136:139], a[144:147], a[72:75], v[136:139]\n\t" "ds_read_b64_tr_b16 a[132:133], %[vr0] offset:50208\n\t"
    "v_mfma_f32_16x16x32_bf16 v[136:139], a[148:151], a[88:91], v[136:139]\n\t" "ds_read_b64_tr_b16 a[134:135], %[vr0] offset:53792\n\t"
    "v_mfma_f32_16x16x32_bf16 v[140:143], a[144:147], a[76:79], v[140:143]\n\t" "v_mfma_f32_16x16x32_bf16 v[140:143], a[148:151], a[92:95], v[140:143]\n\t" "s_waitcnt lgkmcnt(4)\n\t"
    "v_mfma_f32_16x16x32_bf16 v[144:147], a[152:155], a[64:67], v[144:147]\n\t" "ds_read_b64_tr_b16 a[136:137], %[vr2] offset:35840\n\t"
    "v_mfma_f32_16x16x32_bf16 v[144:147], a[156:159], a[80:83], v[144:147]\n\t" "ds_read_b64_tr_b16 a[138:139], %[vr2] offset:39424\n\t"
    "v_mfma_f32_16x16x32_bf16 v[148:151], a[152:155], a[68:71], v[148:151]\n\t" "global_load_dwordx4 a[16:19], %[va], s[76:77] offset:0 sc1\n\t"
    "v_mfma_f32_16x16x32_bf16 v[148:151], a[156:159], a[84:87], v[148:151]\n\t" "v_mfma_f32_16x16x32_bf16 v[152:155], a[152:155], a[72:75], v[152:155]\n\t"
    "ds_read_b64_tr_b16 a[140:141], %[vr2] offset:50176\n\t" "v_mfma_f32_16x16x32_bf16 v[152:155], a[156:159], a[88:91], v[152:155]\n\t" "ds_read_b64_tr_b16 a[142:143], %[vr2] offset:53760\n\t"
    "s_add_u32 m0, %[sl], 0\n\t" "v_mfma_f32_16x16x32_bf16 v[156:159], a[152:155], a[76:79], v[156:159]\n\t" "global_load_lds_dwordx4 %[vb0], s[78:79]\n\t"
    "v_mfma_f32_16x16x32_bf16 v[156:159], a[156:159], a[92:95], v[156:159]\n\t" "s_waitcnt lgkmcnt(4)\n\t" "v_mfma_f32_16x16x32_bf16 v[160:163], a[128:131], a[64:67], v[160:163]\n\t"
    "ds_read_b64_tr_b16 a[144:145], %[vr2] offset:35872\n\t" "v_mfma_f32_16x16x32_bf16 v[160:163], a[132:135], a[80:83], v[160:163]\n\t" "ds_read_b64_tr_b16 a[146:147], %[vr2] offset:39456\n\t"
    "v_mfma_f32_16x16x32_bf16 v[164:167], a[128:131], a[68:71], v[164:167]\n\t" "v_mfma_f32_16x16x32_bf16 v[164:167], a[132:135], a[84:87], v[164:167]\n\t"
    "v_mfma_f32_16x16x32_bf16 v[168:171], a[128:131], a[72:75], v[168:171]\n\t" "ds_read_b64_tr_b16 a[148:149], %[vr2] offset:50208\n\t"
    "v_mfma_f32_16x16x32_bf16 v[168:171], a[132:135], a[88:91], v[168:171]\n\t" "ds_read_b64_tr_b16 a[150:151], %[vr2] offset:53792\n\t"
    "v_mfma_f32_16x16x32_bf16 v[172:175], a[128:131], a[76:79], v[172:175]\n\t" "global_load_dwordx4 a[20:23], %[va], s[76:77] offset:1024 sc1\n\t"
    "v_mfma_f32_16x16x32_bf16 v[172:175], a[132:135], a[92:95], v[172:175]\n\t" "s_waitcnt lgkmcnt(4)\n\t" "v_mfma_f32_16x16x32_bf16 v[176:179], a[136:139], a[64:67], v[176:179]\n\t"
    "ds_read_b64_tr_b16 a[152:153], %[vr4] offset:35840\n\t" "v_mfma_f32_16x16x32_bf16 v[176:179], a[140:143], a[80:83], v[176:179]\n\t" "ds_read_b64_tr_b16 a[154:155], %[vr4] offset:39424\n\t"
    "s_add_u32 m0, %[sl], 1024\n\t" "v_mfma_f32_16x16x32_bf16 v[180:183], a[136:139], a[68:71], v[180:183]\n\t" "global_load_lds_dwordx4 %[vb1], s[78:79]\n\t"
    "v_mfma_f32_16x16x32_bf16 v[180:183], a[140:143], a[84:87], v[180:183]\n\t" "v_mfma_f32_16x16x32_bf16 v[184:187], a[136:139], a[72:75], v[184:187]\n\t"
    "ds_read_b64_tr_b16 a[156:157], %[vr4] offset:50176\n\t" "v_mfma_f32_16x16x32_bf16 v[184:187], a[140:143], a[88:91], v[184:187]\n\t" "ds_read_b64_tr_b16 a[158:159], %[vr4] offset:53760\n\t"
    "v_mfma_f32_16x16x32_bf16 v[188:191], a[136:139], a[76:79], v[188:191]\n\t" "v_mfma_f32_16x16x32_bf16 v[188:191], a[140:143], a[92:95], v[188:191]\n\t" "s_waitcnt lgkmcnt(4)\n\t"
    "v_mfma_f32_16x16x32_bf16 v[192:195], a[144:147], a[64:67], v[192:195]\n\t" "ds_read_b64_tr_b16 a[128:129], %[vr4] offset:35872\n\t"
    "v_mfma_f32_16x16x32_bf16 v[192:195], a[148:151], a[80:83], v[192:195]\n\t" "ds_read_b64_tr_b16 a[130:131], %[vr4] offset:39456\n\t"
    "v_mfma_f32_16x16x32_bf16 v[196:199], a[144:147], a[68:71], v[196:199]\n\t" "global_load_dwordx4 a[24:27], %[va], s[76:77] offset:2048 sc1\n\t"
    "v_mfma_f32_16x16x32_bf16 v[196:199], a[148:151], a[84:87], v[196:199]\n\t" "v_mfma_f32_16x16x32_bf16 v[200:203], a[144:147], a[72:75], v[200:203]\n\t"
    "ds_read_b64_tr_b16 a[132:133], %[vr4] offset:50208\n\t" "v_mfma_f32_16x16x32_bf16 v[200:203], a[148:151], a[88:91], v[200:203]\n\t" "ds_read_b64_tr_b16 a[134:135], %[vr4] offset:53792\n\t"
    "v_mfma_f32_16x16x32_bf16 v[204:207], a[144:147], a[76:79], v[204:207]\n\t" "v_mfma_f32_16x16x32_bf16 v[204:207], a[148:151], a[92:95], v[204:207]\n\t" "s_waitcnt lgkmcnt(4)\n\t"
    "v_mfma_f32_16x16x32_bf16 v[208:211], a[152:155], a[64:67], v[208:211]\n\t" "ds_read_b64_tr_b16 a[136:137], %[vr6] offset:35840\n\t"
    "v_mfma_f32_16x16x32_bf16 v[208:211], a[156:159], a[80:83], v[208:211]\n\t" "ds_read_b64_tr_b16 a[138:139], %[vr6] offset:39424\n\t" "s_add_u32 m0, %[sl], 2048\n\t"
    "v_mfma_f32_16x16x32_bf16 v[212:215], a[152:155], a[68:71], v[212:215]\n\t" "global_load_lds_dwordx4 %[vb2], s[78:79]\n\t" "v_mfma_f32_16x16x32_bf16 v[212:215], a[156:159], a[84:87], v[212:215]\n\t"
    "v_mfma_f32_16x16x32_bf16 v[216:219], a[152:155], a[72:75], v[216:219]\n\t" "ds_read_b64_tr_b16 a[140:141], %[vr6] offset:50176\n\t"
    "v_mfma_f32_16x16x32_bf16 v[216:219], a[156:159], a[88:91], v[216:219]\n\t" "ds_read_b64_tr_b16 a[142:143], %[vr6] offset:53760\n\t"
    "v_mfma_f32_16x16x32_bf16 v[220:223], a[152:155], a[76:79], v[220:223]\n\t" "global_load_dwordx4 a[28:31], %[va], s[76:77] offset:3072 sc1\n\t"
    "v_mfma_f32_16x16x32_bf16 v[220:223], a[156:159], a[92:95], v[220:223]\n\t" "s_add_u32 s76, s76, 16384\n\t" "s_addc_u32 s77, s77, 0\n\t" "s_waitcnt lgkmcnt(4)\n\t"
    "v_mfma_f32_16x16x32_bf16 v[224:227], a[128:131], a[64:67], v[224:227]\n\t" "s_waitcnt vmcnt(24)\n\t" "s_barrier\n\t" "ds_read_b64_tr_b16 a[144:145], %[vr1] offset:0\n\t"
    "v_mfma_f32_16x16x32_bf16 v[224:227], a[132:135], a[80:83], v[224:227]\n\t" "ds_read_b64_tr_b16 a[146:147], %[vr1] offset:3584\n\t"
    "v_mfma_f32_16x16x32_bf16 v[228:231], a[128:131], a[68:71], v[228:231]\n\t" "v_mfma_f32_16x16x32_bf16 v[228:231], a[132:135], a[84:87], v[228:231]\n\t"
    "v_mfma_f32_16x16x32_bf16 v[232:235], a[128:131], a[72:75], v[232:235]\n\t" "ds_read_b64_tr_b16 a[148:149], %[vr1] offset:14336\n\t"
    "v_mfma_f32_16x16x32_bf16 v[232:235], a[132:135], a[88:91], v[232:235]\n\t" "ds_read_b64_tr_b16 a[150:151], %[vr1] offset:17920\n\t" "s_add_u32 m0, %[sl], 3072\n\t"
    "v_mfma_f32_16x16x32_bf16 v[236:239], a[128:131], a[76:79], v[236:239]\n\t" "s_mov_b64 exec, s[82:83]\n\t" "global_load_lds_dwordx4 %[vb3], s[78:79]\n\t" "s_mov_b64 exec, -1\n\t"
    "v_mfma_f32_16x16x32_bf16 v[236:239], a[132:135], a[92:95], v[236:239]\n\t" "s_add_u32 s78, s78, 229376\n\t" "s_addc_u32 s79, s79, 0\n\t" "s_waitcnt lgkmcnt(4)\n\t"
    "v_mfma_f32_16x16x32_bf16 v[240:243], a[136:139], a[64:67], v[240:243]\n\t" "ds_read_b64_tr_b16 a[152:153], %[vr1] offset:32\n\t"
    "v_mfma_f32_16x16x32_bf16 v[240:243], a[140:143], a[80:83], v[240:243]\n\t" "ds_read_b64_tr_b16 a[154:155], %[vr1] offset:3616\n\t"
    "v_mfma_f32_16x16x32_bf16 v[244:247], a[136:139], a[68:71], v[244:247]\n\t" "v_mfma_f32_16x16x32_bf16 v[244:247], a[140:143], a[84:87], v[244:247]\n\t"
    "v_mfma_f32_16x16x32_bf16 v[248:251], a[136:139], a[72:75], v[248:251]\n\t" "ds_read_b64_tr_b16 a[156:157], %[vr1] offset:14368\n\t"
    "v_mfma_f32_16x16x32_bf16 v[248:251], a[140:143], a[88:91], v[248:251]\n\t" "ds_read_b64_tr_b16 a[158:159], %[vr1] offset:17952\n\t"
    "v_mfma_f32_16x16x32_bf16 v[252:255], a[136:139], a[76:79], v[252:255]\n\t" "v_mfma_f32_16x16x32_bf16 v[252:255], a[140:143], a[92:95], v[252:255]\n\t" "s_waitcnt lgkmcnt(4) vmcnt(18)\n\t"
    "v_mfma_f32_16x16x32_bf16 v[32:35], a[144:147], a[96:99], v[32:35]\n\t" "s_barrier\n\t" "ds_read_b64_tr_b16 a[128:129], %[vr3] offset:0\n\t"
    "v_mfma_f32_16x16x32_bf16 v[32:35], a[148:151], a[112:115], v[32:35]\n\t" "ds_read_b64_tr_b16 a[130:131], %[vr3] offset:3584\n\t"
    "v_mfma_f32_16x16x32_bf16 v[36:39], a[144:147], a[100:103], v[36:39]\n\t" "global_load_dwordx4 a[32:35], %[va], s[76:77] offset:0 sc1\n\t"
    "v_mfma_f32_16x16x32_bf16 v[36:39], a[148:151], a[116:119], v[36:39]\n\t" "v_mfma_f32_16x16x32_bf16 v[40:43], a[144:147], a[104:107], v[40:43]\n\t"
    "ds_read_b64_tr_b16 a[132:133], %[vr3] offset:14336\n\t" "v_mfma_f32_16x16x32_bf16 v[40:43], a[148:151], a[120:123], v[40:43]\n\t" "ds_read_b64_tr_b16 a[134:135], %[vr3] offset:17920\n\t"
    "s_add_u32 m0, %[sl], 14336\n\t" "v_mfma_f32_16x16x32_bf16 v[44:47], a[144:147], a[108:111], v[44:47]\n\t" "global_load_lds_dwordx4 %[vb0], s[78:79]\n\t"
    "v_mfma_f32_16x16x32_bf16 v[44:47], a[148:151], a[124:127], v[44:47]\n\t" "s_waitcnt lgkmcnt(4)\n\t" "v_mfma_f32_16x16x32_bf16 v[48:51], a[152:155], a[96:99], v[48:51]\n\t"
    "ds_read_b64_tr_b16 a[136:137], %[vr3] offset:32\n\t" "v_mfma_f32_16x16x32_bf16 v[48:51], a[156:159], a[112:115], v[48:51]\n\t" "ds_read_b64_tr_b16 a[138:139], %[vr3] offset:3616\n\t"
    "v_mfma_f32_16x16x32_bf16 v[52:55], a[152:155], a[100:103], v[52:55]\n\t" "v_mfma_f32_16x16x32_bf16 v[52:55], a[156:159], a[116:119], v[52:55]\n\t"
    "v_mfma_f32_16x16x32_bf16 v[56:59], a[152:155], a[104:107], v[56:59]\n\t" "ds_read_b64_tr_b16 a[140:141], %[vr3] offset:14368\n\t"
    "v_mfma_f32_16x16x32_bf16 v[56:59], a[156:159], a[120:123], v[56:59]\n\t" "ds_read_b64_tr_b16 a[142:143], %[vr3] offset:17952\n\t"
    "v_mfma_f32_16x16x32_bf16 v[60:63], a[152:155], a[108:111], v[60:63]\n\t" "global_load_dwordx4 a[36:39], %[va], s[76:77] offset:1024 sc1\n\t"
    "v_mfma_f32_16x16x32_bf16 v[60:63], a[156:159], a[124:127], v[60:63]\n\t" "s_waitcnt lgkmcnt(4)\n\t" "v_mfma_f32_16x16x32_bf16 v[64:67], a[128:131], a[96:99], v[64:67]\n\t"
    "ds_read_b64_tr_b16 a[144:145], %[vr5] offset:0\n\t" "v_mfma_f32_16x16x32_bf16 v[64:67], a[132:135], a[112:115], v[64:67]\n\t" "ds_read_b64_tr_b16 a[146:147], %[vr5] offset:3584\n\t"
    "s_add_u32 m0, %[sl], 15360\n\t" "v_mfma_f32_16x16x32_bf16 v[68:71], a[128:131], a[100:103], v[68:71]\n\t" "global_load_lds_dwordx4 %[vb1], s[78:79]\n\t"
    "v_mfma_f32_16x16x32_bf16 v[68:71], a[132:135], a[116:119], v[68:71]\n\t" "v_mfma_f32_16x16x32_bf16 v[72:75], a[128:131], a[104:107], v[72:75]\n\t"
    "ds_read_b64_tr_b16 a[148:149], %[vr5] offset:14336\n\t" "v_mfma_f32_16x16x32_bf16 v[72:75], a[132:135], a[120:123], v[72:75]\n\t" "ds_read_b64_tr_b16 a[150:151], %[vr5] offset:17920\n\t"
    "v_mfma_f32_16x16x32_bf16 v[76:79], a[128:131], a[108:111], v[76:79]\n\t" "v_mfma_f32_16x16x32_bf16 v[76:79], a[132:135], a[124:127], v[76:79]\n\t" "s_waitcnt lgkmcnt(4)\n\t"
    "v_mfma_f32_16x16x32_bf16 v[80:83], a[136:139], a[96:99], v[80:83]\n\t" "ds_read_b64_tr_b16 a[152:153], %[vr5] offset:32\n\t" "v_mfma_f32_16x16x32_bf16 v[80:83], a[140:143], a[112:115], v[80:83]\n\t"
    "ds_read_b64_tr_b16 a[154:155], %[vr5] offset:3616\n\t" "v_mfma_f32_16x16x32_bf16 v[84:87], a[136:139], a[100:103], v[84:87]\n\t" "global_load_dwordx4 a[40:43], %[va], s[76:77] offset:2048 sc1\n\t"
    "v_mfma_f32_16x16x32_bf16 v[84:87], a[140:143], a[116:119], v[84:87]\n\t" "v_mfma_f32_16x16x32_bf16 v[88:91], a[136:139], a[104:107], v[88:91]\n\t"
    "ds_read_b64_tr_b16 a[156:157], %[vr5] offset:14368\n\t" "v_mfma_f32_16x16x32_bf16 v[88:91], a[140:143], a[120:123], v[88:91]\n\t" "ds_read_b64_tr_b16 a[158:159], %[vr5] offset:17952\n\t"
    "v_mfma_f32_16x16x32_bf16 v[92:95], a[136:139], a[108:111], v[92:95]\n\t" "v_mfma_f32_16x16x32_bf16 v[92:95], a[140:143], a[124:127], v[92:95]\n\t" "s_waitcnt lgkmcnt(4)\n\t"
    "v_mfma_f32_16x16x32_bf16 v[96:99], a[144:147], a[96:99], v[96:99]\n\t" "ds_read_b64_tr_b16 a[128:129], %[vr7] offset:0\n\t" "v_mfma_f32_16x16x32_bf16 v[96:99], a[148:151], a[112:115], v[96:99]\n\t"
    "ds_read_b64_tr_b16 a[130:131], %[vr7] offset:3584\n\t" "s_add_u32 m0, %[sl], 16384\n\t" "v_mfma_f32_16x16x32_bf16 v[100:103], a[144:147], a[100:103], v[100:103]\n\t"
    "global_load_lds_dwordx4 %[vb2], s[78:79]\n\t" "v_mfma_f32_16x16x32_bf16 v[100:103], a[148:151], a[116:119], v[100:103]\n\t"
    "v_mfma_f32_16x16x32_bf16 v[104:107], a[144:147], a[104:107], v[104:107]\n\t" "ds_read_b64_tr_b16 a[132:133], %[vr7] offset:14336\n\t"
    "v_mfma_f32_16x16x32_bf16 v[104:107], a[148:151], a[120:123], v[104:107]\n\t" "ds_read_b64_tr_b16 a[134:135], %[vr7] offset:17920\n\t"
    "v_mfma_f32_16x16x32_bf16 v[108:111], a[144:147], a[108:111], v[108:111]\n\t" "global_load_dwordx4 a[44:47], %[va], s[76:77] offset:3072 sc1\n\t"
    "v_mfma_f32_16x16x32_bf16 v[108:111], a[148:151], a[124:127], v[108:111]\n\t" "s_add_u32 s76, s76, 16384\n\t" "s_addc_u32 s77, s77, 0\n\t" "s_waitcnt lgkmcnt(4)\n\t"
    "v_mfma_f32_16x16x32_bf16 v[112:115], a[152:155], a[96:99], v[112:115]\n\t" "ds_read_b64_tr_b16 a[136:137], %[vr1] offset:7168\n\t"
    "v_mfma_f32_16x16x32_bf16 v[112:115], a[156:159], a[112:115], v[112:115]\n\t" "ds_read_b64_tr_b16 a[138:139], %[vr1] offset:10752\n\t"
    "v_mfma_f32_16x16x32_bf16 v[116:119], a[152:155], a[100:103], v[116:119]\n\t" "v_mfma_f32_16x16x32_bf16 v[116:119], a[156:159], a[116:119], v[116:119]\n\t"
    "v_mfma_f32_16x16x32_bf16 v[120:123], a[152:155], a[104:107], v[120:123]\n\t" "ds_read_b64_tr_b16 a[140:141], %[vr1] offset:21504\n\t"
    "v_mfma_f32_16x16x32_bf16 v[120:123], a[156:159], a[120:123], v[120:123]\n\t" "ds_read_b64_tr_b16 a[142:143], %[vr1] offset:25088\n\t" "s_add_u32 m0, %[sl], 17408\n\t"
    "v_mfma_f32_16x16x32_bf16 v[124:127], a[152:155], a[108:111], v[124:127]\n\t" "s_mov_b64 exec, s[82:83]\n\t" "global_load_lds_dwordx4 %[vb3], s[78:79]\n\t" "s_mov_b64 exec, -1\n\t"
    "v_mfma_f32_16x16x32_bf16 v[124:127], a[156:159], a[124:127], v[124:127]\n\t" "s_add_u32 s78, s78, 229376\n\t" "s_addc_u32 s79, s79, 0\n\t" "s_waitcnt lgkmcnt(4)\n\t"
    "v_mfma_f32_16x16x32_bf16 v[128:131], a[128:131], a[96:99], v[128:131]\n\t" "ds_read_b64_tr_b16 a[144:145], %[vr1] offset:7200\n\t"
    "v_mfma_f32_16x16x32_bf16 v[128:131], a[132:135], a[112:115], v[128:131]\n\t" "ds_read_b64_tr_b16 a[146:147], %[vr1] offset:10784\n\t"
    "v_mfma_f32_16x16x32_bf16 v[132:135], a[128:131], a[100:103], v[132:135]\n\t" "v_mfma_f32_16x16x32_bf16 v[132:135], a[132:135], a[116:119], v[132:135]\n\t"
    "v_mfma_f32_16x16x32_bf16 v[136:139], a[128:131], a[104:107], v[136:139]\n\t" "ds_read_b64_tr_b16 a[148:149], %[vr1] offset:21536\n\t"
    "v_mfma_f32_16x16x32_bf16 v[136:139], a[132:135], a[120:123], v[136:139]\n\t" "ds_read_b64_tr_b16 a[150:151], %[vr1] offset:25120\n\t"
    "v_mfma_f32_16x16x32_bf16 v[140:143], a[128:131], a[108:111], v[140:143]\n\t" "v_mfma_f32_16x16x32_bf16 v[140:143], a[132:135], a[124:127], v[140:143]\n\t" "s_waitcnt lgkmcnt(4)\n\t"
    "v_mfma_f32_16x16x32_bf16 v[144:147], a[136:139], a[96:99], v[144:147]\n\t" "ds_read_b64_tr_b16 a[152:153], %[vr3] offset:7168\n\t"
    "v_mfma_f32_16x16x32_bf16 v[144:147], a[140:143], a[112:115], v[144:147]\n\t" "ds_read_b64_tr_b16 a[154:155], %[vr3] offset:10752\n\t"
    "v_mfma_f32_16x16x32_bf16 v[148:151], a[136:139], a[100:103], v[148:151]\n\t" "global_load_dwordx4 a[48:51], %[va], s[76:77] offset:0 sc1\n\t"
    "v_mfma_f32_16x16x32_bf16 v[148:151], a[140:143], a[116:119], v[148:151]\n\t" "v_mfma_f32_16x16x32_bf16 v[152:155], a[136:139], a[104:107], v[152:155]\n\t"
    "ds_read_b64_tr_b16 a[156:157], %[vr3] offset:21504\n\t" "v_mfma_f32_16x16x32_bf16 v[152:155], a[140:143], a[120:123], v[152:155]\n\t" "ds_read_b64_tr_b16 a[158:159], %[vr3] offset:25088\n\t"
    "s_add_u32 m0, %[sl], 28672\n\t" "v_mfma_f32_16x16x32_bf16 v[156:159], a[136:139], a[108:111], v[156:159]\n\t" "global_load_lds_dwordx4 %[vb0], s[78:79]\n\t"
    "v_mfma_f32_16x16x32_bf16 v[156:159], a[140:143], a[124:127], v[156:159]\n\t" "s_waitcnt lgkmcnt(4)\n\t" "v_mfma_f32_16x16x32_bf16 v[160:163], a[144:147], a[96:99], v[160:163]\n\t"
    "ds_read_b64_tr_b16 a[128:129], %[vr3] offset:7200\n\t" "v_mfma_f32_16x16x32_bf16 v[160:163], a[148:151], a[112:115], v[160:163]\n\t" "ds_read_b64_tr_b16 a[130:131], %[vr3] offset:10784\n\t"
    "v_mfma_f32_16x16x32_bf16 v[164:167], a[144:147], a[100:103], v[164:167]\n\t" "v_mfma_f32_16x16x32_bf16 v[164:167], a[148:151], a[116:119], v[164:167]\n\t"
    "v_mfma_f32_16x16x32_bf16 v[168:171], a[144:147], a[104:107], v[168:171]\n\t" "ds_read_b64_tr_b16 a[132:133], %[vr3] offset:21536\n\t"
    "v_mfma_f32_16x16x32_bf16 v[168:171], a[148:151], a[120:123], v[168:171]\n\t" "ds_read_b64_tr_b16 a[134:135], %[vr3] offset:25120\n\t"
    "v_mfma_f32_16x16x32_bf16 v[172:175], a[144:147], a[108:111], v[172:175]\n\t" "global_load_dwordx4 a[52:55], %[va], s[76:77] offset:1024 sc1\n\t"
    "v_mfma_f32_16x16x32_bf16 v[172:175], a[148:151], a[124:127], v[172:175]\n\t" "s_waitcnt lgkmcnt(4)\n\t" "v_mfma_f32_16x16x32_bf16 v[176:179], a[152:155], a[96:99], v[176:179]\n\t"
    "ds_read_b64_tr_b16 a[136:137], %[vr5] offset:7168\n\t" "v_mfma_f32_16x16x32_bf16 v[176:179], a[156:159], a[112:115], v[176:179]\n\t" "ds_read_b64_tr_b16 a[138:139], %[vr5] offset:10752\n\t"
    "s_add_u32 m0, %[sl], 29696\n\t" "v_mfma_f32_16x16x32_bf16 v[180:183], a[152:155], a[100:103], v[180:183]\n\t" "global_load_lds_dwordx4 %[vb1], s[78:79]\n\t"
    "v_mfma_f32_16x16x32_bf16 v[180:183], a[156:159], a[116:119], v[180:183]\n\t" "v_mfma_f32_16x16x32_bf16 v[184:187], a[152:155], a[104:107], v[184:187]\n\t"
    "ds_read_b64_tr_b16 a[140:141], %[vr5] offset:21504\n\t" "v_mfma_f32_16x16x32_bf16 v[184:187], a[156:159], a[120:123], v[184:187]\n\t" "ds_read_b64_tr_b16 a[142:143], %[vr5] offset:25088\n\t"
    "v_mfma_f32_16x16x32_bf16 v[188:191], a[152:155], a[108:111], v[188:191]\n\t" "v_mfma_f32_16x16x32_bf16 v[188:191], a[156:159], a[124:127], v[188:191]\n\t" "s_waitcnt lgkmcnt(4)\n\t"
    "v_mfma_f32_16x16x32_bf16 v[192:195], a[128:131], a[96:99], v[192:195]\n\t" "ds_read_b64_tr_b16 a[144:145], %[vr5] offset:7200\n\t"
    "v_mfma_f32_16x16x32_bf16 v[192:195], a[132:135], a[112:115], v[192:195]\n\t" "ds_read_b64_tr_b16 a[146:147], %[vr5] offset:10784\n\t"
    "v_mfma_f32_16x16x32_bf16 v[196:199], a[128:131], a[100:103], v[196:199]\n\t" "global_load_dwordx4 a[56:59], %[va], s[76:77] offset:2048 sc1\n\t"
    "v_mfma_f32_16x16x32_bf16 v[196:199], a[132:135], a[116:119], v[196:199]\n\t" "v_mfma_f32_16x16x32_bf16 v[200:203], a[128:131], a[104:107], v[200:203]\n\t"
    "ds_read_b64_tr_b16 a[148:149], %[vr5] offset:21536\n\t" "v_mfma_f32_16x16x32_bf16 v[200:203], a[132:135], a[120:123], v[200:203]\n\t" "ds_read_b64_tr_b16 a[150:151], %[vr5] offset:25120\n\t"
    "v_mfma_f32_16x16x32_bf16 v[204:207], a[128:131], a[108:111], v[204:207]\n\t" "v_mfma_f32_16x16x32_bf16 v[204:207], a[132:135], a[124:127], v[204:207]\n\t" "s_waitcnt lgkmcnt(4)\n\t"
    "v_mfma_f32_16x16x32_bf16 v[208:211], a[136:139], a[96:99], v[208:211]\n\t" "ds_read_b64_tr_b16 a[152:153], %[vr7] offset:7168\n\t"
    "v_mfma_f32_16x16x32_bf16 v[208:211], a[140:143], a[112:115], v[208:211]\n\t" "ds_read_b64_tr_b16 a[154:155], %[vr7] offset:10752\n\t" "s_add_u32 m0, %[sl], 30720\n\t"
    "v_mfma_f32_16x16x32_bf16 v[212:215], a[136:139], a[100:103], v[212:215]\n\t" "global_load_lds_dwordx4 %[vb2], s[78:79]\n\t"
    "v_mfma_f32_16x16x32_bf16 v[212:215], a[140:143], a[116:119], v[212:215]\n\t" "v_mfma_f32_16x16x32_bf16 v[216:219], a[136:139], a[104:107], v[216:219]\n\t"
    "ds_read_b64_tr_b16 a[156:157], %[vr7] offset:21504\n\t" "v_mfma_f32_16x16x32_bf16 v[216:219], a[140:143], a[120:123], v[216:219]\n\t" "ds_read_b64_tr_b16 a[158:159], %[vr7] offset:25088\n\t"
    "v_mfma_f32_16x16x32_bf16 v[220:223], a[136:139], a[108:111], v[220:223]\n\t" "global_load_dwordx4 a[60:63], %[va], s[76:77] offset:3072 sc1\n\t"
    "v_mfma_f32_16x16x32_bf16 v[220:223], a[140:143], a[124:127], v[220:223]\n\t" "s_add_u32 s76, s76, 16384\n\t" "s_addc_u32 s77, s77, 0\n\t" "s_waitcnt lgkmcnt(4)\n\t"
    "v_mfma_f32_16x16x32_bf16 v[224:227], a[144:147], a[96:99], v[224:227]\n\t" "s_waitcnt vmcnt(24)\n\t" "s_barrier\n\t" "ds_read_b64_tr_b16 a[128:129], %[vr1] offset:28672\n\t"
    "v_mfma_f32_16x16x32_bf16 v[224:227], a[148:151], a[112:115], v[224:227]\n\t" "ds_read_b64_tr_b16 a[130:131], %[vr1] offset:32256\n\t"
    "v_mfma_f32_16x16x32_bf16 v[228:231], a[144:147], a[100:103], v[228:231]\n\t" "v_mfma_f32_16x16x32_bf16 v[228:231], a[148:151], a[116:119], v[228:231]\n\t"
    "v_mfma_f32_16x16x32_bf16 v[232:235], a[144:147], a[104:107], v[232:235]\n\t" "ds_read_b64_tr_b16 a[132:133], %[vr1] offset:43008\n\t"
    "v_mfma_f32_16x16x32_bf16 v[232:235], a[148:151], a[120:123], v[232:235]\n\t" "ds_read_b64_tr_b16 a[134:135], %[vr1] offset:46592\n\t" "s_add_u32 m0, %[sl], 31744\n\t"
    "v_mfma_f32_16x16x32_bf16 v[236:239], a[144:147], a[108:111], v[236:239]\n\t" "s_mov_b64 exec, s[82:83]\n\t" "global_load_lds_dwordx4 %[vb3], s[78:79]\n\t" "s_mov_b64 exec, -1\n\t"
    "v_mfma_f32_16x16x32_bf16 v[236:239], a[148:151], a[124:127], v[236:239]\n\t" "s_add_u32 s78, s78, 229376\n\t" "s_addc_u32 s79, s79, 0\n\t" "s_waitcnt lgkmcnt(4)\n\t"
    "v_mfma_f32_16x16x32_bf16 v[240:243], a[152:155], a[96:99], v[240:243]\n\t" "ds_read_b64_tr_b16 a[136:137], %[vr1] offset:28704\n\t"
    "v_mfma_f32_16x16x32_bf16 v[240:243], a[156:159], a[112:115], v[240:243]\n\t" "ds_read_b64_tr_b16 a[138:139], %[vr1] offset:32288\n\t"
    "v_mfma_f32_16x16x32_bf16 v[244:247], a[152:155], a[100:103], v[244:247]\n\t" "v_mfma_f32_16x16x32_bf16 v[244:247], a[156:159], a[116:119], v[244:247]\n\t"
    "v_mfma_f32_16x16x32_bf16 v[248:251], a[152:155], a[104:107], v[248:251]\n\t" "ds_read_b64_tr_b16 a[140:141], %[vr1] offset:43040\n\t"
    "v_mfma_f32_16x16x32_bf16 v[248:251], a[156:159], a[120:123], v[248:251]\n\t" "ds_read_b64_tr_b16 a[142:143], %[vr1] offset:46624\n\t"
    "v_mfma_f32_16x16x32_bf16 v[252:255], a[152:155], a[108:111], v[252:255]\n\t" "v_mfma_f32_16x16x32_bf16 v[252:255], a[156:159], a[124:127], v[252:255]\n\t" ".Ld1_%=:\n\t" "s_cmp_eq_u32 s80, 0\n\t"
    "s_cselect_b32 s86, %[wa], 16384\n\t" "s_ashr_i32 s87, s86, 31\n\t" "s_cmp_eq_u32 s80, 0\n\t" "s_cselect_b32 s88, %[wb], 229376\n\t" "s_ashr_i32 s89, s88, 31\n\t" "s_cmp_eq_u32 s80, %[e0]\n\t"
    "s_cbranch_scc1 .LE0_%=\n\t" "s_waitcnt lgkmcnt(4) vmcnt(17)\n\t" "v_mfma_f32_16x16x32_bf16 v[32:35], a[128:131], a[0:3], v[32:35]\n\t" "s_barrier\n\t"
    "ds_read_b64_tr_b16 a[144:145], %[vr3] offset:28672\n\t" "v_mfma_f32_16x16x32_bf16 v[32:35], a[132:135], a[16:19], v[32:35]\n\t" "ds_read_b64_tr_b16 a[146:147], %[vr3] offset:32256\n\t"
    "v_mfma_f32_16x16x32_bf16 v[36:39], a[128:131], a[4:7], v[36:39]\n\t" "global_load_dwordx4 a[64:67], %[va], s[76:77] offset:0 sc1\n\t"
    "v_mfma_f32_16x16x32_bf16 v[36:39], a[132:135], a[20:23], v[36:39]\n\t" "v_mfma_f32_16x16x32_bf16 v[40:43], a[128:131], a[8:11], v[40:43]\n\t" "ds_read_b64_tr_b16 a[148:149], %[vr3] offset:43008\n\t"
    "v_mfma_f32_16x16x32_bf16 v[40:43], a[132:135], a[24:27], v[40:43]\n\t" "ds_read_b64_tr_b16 a[150:151], %[vr3] offset:46592\n\t" "s_add_u32 m0, %[sl], 43008\n\t"
    "v_mfma_f32_16x16x32_bf16 v[44:47], a[128:131], a[12:15], v[44:47]\n\t" "global_load_lds_dwordx4 %[vb0], s[78:79]\n\t" "v_mfma_f32_16x16x32_bf16 v[44:47], a[132:135], a[28:31], v[44:47]\n\t"
    "s_waitcnt lgkmcnt(4)\n\t" "v_mfma_f32_16x16x32_bf16 v[48:51], a[136:139], a[0:3], v[48:51]\n\t" "ds_read_b64_tr_b16 a[152:153], %[vr3] offset:28704\n\t"
    "v_mfma_f32_16x16x32_bf16 v[48:51], a[140:143], a[16:19], v[48:51]\n\t" "ds_read_b64_tr_b16 a[154:155], %[vr3] offset:32288\n\t" "v_mfma_f32_16x16x32_bf16 v[52:55], a[136:139], a[4:7], v[52:55]\n\t"
    "v_mfma_f32_16x16x32_bf16 v[52:55], a[140:143], a[20:23], v[52:55]\n\t" "v_mfma_f32_16x16x32_bf16 v[56:59], a[136:139], a[8:11], v[56:59]\n\t" "ds_read_b64_tr_b16 a[156:157], %[vr3] offset:43040\n\t"
    "v_mfma_f32_16x16x32_bf16 v[56:59], a[140:143], a[24:27], v[56:59]\n\t" "ds_read_b64_tr_b16 a[158:159], %[vr3] offset:46624\n\t" "v_mfma_f32_16x16x32_bf16 v[60:63], a[136:139], a[12:15], v[60:63]\n\t"
    "global_load_dwordx4 a[68:71], %[va], s[76:77] offset:1024 sc1\n\t" "v_mfma_f32_16x16x32_bf16 v[60:63], a[140:143], a[28:31], v[60:63]\n\t" "s_waitcnt lgkmcnt(4)\n\t"
    "v_mfma_f32_16x16x32_bf16 v[64:67], a[144:147], a[0:3], v[64:67]\n\t" "ds_read_b64_tr_b16 a[128:129], %[vr5] offset:28672\n\t" "v_mfma_f32_16x16x32_bf16 v[64:67], a[148:151], a[16:19], v[64:67]\n\t"
    "ds_read_b64_tr_b16 a[130:131], %[vr5] offset:32256\n\t" "s_add_u32 m0, %[sl], 44032\n\t" "v_mfma_f32_16x16x32_bf16 v[68:71], a[144:147], a[4:7], v[68:71]\n\t"
    "global_load_lds_dwordx4 %[vb1], s[78:79]\n\t" "v_mfma_f32_16x16x32_bf16 v[68:71], a[148:151], a[20:23], v[68:71]\n\t" "v_mfma_f32_16x16x32_bf16 v[72:75], a[144:147], a[8:11], v[72:75]\n\t"
    "ds_read_b64_tr_b16 a[132:133], %[vr5] offset:43008\n\t" "v_mfma_f32_16x16x32_bf16 v[72:75], a[148:151], a[24:27], v[72:75]\n\t" "ds_read_b64_tr_b16 a[134:135], %[vr5] offset:46592\n\t"
    "v_mfma_f32_16x16x32_bf16 v[76:79], a[144:147], a[12:15], v[76:79]\n\t" "v_mfma_f32_16x16x32_bf16 v[76:79], a[148:151], a[28:31], v[76:79]\n\t" "s_waitcnt lgkmcnt(4)\n\t"
    "v_mfma_f32_16x16x32_bf16 v[80:83], a[152:155], a[0:3], v[80:83]\n\t" "ds_read_b64_tr_b16 a[136:137], %[vr5] offset:28704\n\t" "v_mfma_f32_16x16x32_bf16 v[80:83], a[156:159], a[16:19], v[80:83]\n\t"
    "ds_read_b64_tr_b16 a[138:139], %[vr5] offset:32288\n\t" "v_mfma_f32_16x16x32_bf16 v[84:87], a[152:155], a[4:7], v[84:87]\n\t" "global_load_dwordx4 a[72:75], %[va], s[76:77] offset:2048 sc1\n\t"
    "v_mfma_f32_16x16x32_bf16 v[84:87], a[156:159], a[20:23], v[84:87]\n\t" "v_mfma_f32_16x16x32_bf16 v[88:91], a[152:155], a[8:11], v[88:91]\n\t" "ds_read_b64_tr_b16 a[140:141], %[vr5] offset:43040\n\t"
    "v_mfma_f32_16x16x32_bf16 v[88:91], a[156:159], a[24:27], v[88:91]\n\t" "ds_read_b64_tr_b16 a[142:143], %[vr5] offset:46624\n\t" "v_mfma_f32_16x16x32_bf16 v[92:95], a[152:155], a[12:15], v[92:95]\n\t"
    "v_mfma_f32_16x16x32_bf16 v[92:95], a[156:159], a[28:31], v[92:95]\n\t" "s_waitcnt lgkmcnt(4)\n\t" "v_mfma_f32_16x16x32_bf16 v[96:99], a[128:131], a[0:3], v[96:99]\n\t"
    "ds_read_b64_tr_b16 a[144:145], %[vr7] offset:28672\n\t" "v_mfma_f32_16x16x32_bf16 v[96:99], a[132:135], a[16:19], v[96:99]\n\t" "ds_read_b64_tr_b16 a[146:147], %[vr7] offset:32256\n\t"
    "s_add_u32 m0, %[sl], 45056\n\t" "v_mfma_f32_16x16x32_bf16 v[100:103], a[128:131], a[4:7], v[100:103]\n\t" "global_load_lds_dwordx4 %[vb2], s[78:79]\n\t"
    "v_mfma_f32_16x16x32_bf16 v[100:103], a[132:135], a[20:23], v[100:103]\n\t" "v_mfma_f32_16x16x32_bf16 v[104:107], a[128:131], a[8:11], v[104:107]\n\t"
    "ds_read_b64_tr_b16 a[148:149], %[vr7] offset:43008\n\t" "v_mfma_f32_16x16x32_bf16 v[104:107], a[132:135], a[24:27], v[104:107]\n\t" "ds_read_b64_tr_b16 a[150:151], %[vr7] offset:46592\n\t"
    "v_mfma_f32_16x16x32_bf16 v[108:111], a[128:131], a[12:15], v[108:111]\n\t" "global_load_dwordx4 a[76:79], %[va], s[76:77] offset:3072 sc1\n\t"
    "v_mfma_f32_16x16x32_bf16 v[108:111], a[132:135], a[28:31], v[108:111]\n\t" "s_add_u32 s76, s76, 16384\n\t" "s_addc_u32 s77, s77, 0\n\t" "s_waitcnt lgkmcnt(4)\n\t"
    "v_mfma_f32_16x16x32_bf16 v[112:115], a[136:139], a[0:3], v[112:115]\n\t" "ds_read_b64_tr_b16 a[152:153], %[vr1] offset:35840\n\t"
    "v_mfma_f32_16x16x32_bf16 v[112:115], a[140:143], a[16:19], v[112:115]\n\t" "ds_read_b64_tr_b16 a[154:155], %[vr1] offset:39424\n\t"
    "v_mfma_f32_16x16x32_bf16 v[116:119], a[136:139], a[4:7], v[116:119]\n\t" "v_mfma_f32_16x16x32_bf16 v[116:119], a[140:143], a[20:23], v[116:119]\n\t"
    "v_mfma_f32_16x16x32_bf16 v[120:123], a[136:139], a[8:11], v[120:123]\n\t" "ds_read_b64_tr_b16 a[156:157], %[vr1] offset:50176\n\t"
    "v_mfma_f32_16x16x32_bf16 v[120:123], a[140:143], a[24:27], v[120:123]\n\t" "ds_read_b64_tr_b16 a[158:159], %[vr1] offset:53760\n\t" "s_add_u32 m0, %[sl], 46080\n\t"
    "v_mfma_f32_16x16x32_bf16 v[124:127], a[136:139], a[12:15], v[124:127]\n\t" "s_mov_b64 exec, s[82:83]\n\t" "global_load_lds_dwordx4 %[vb3], s[78:79]\n\t" "s_mov_b64 exec, -1\n\t"
    "v_mfma_f32_16x16x32_bf16 v[124:127], a[140:143], a[28:31], v[124:127]\n\t" "s_add_u32 s78, s78, 229376\n\t" "s_addc_u32 s79, s79, 0\n\t" "s_waitcnt lgkmcnt(4)\n\t"
    "v_mfma_f32_16x16x32_bf16 v[128:131], a[144:147], a[0:3], v[128:131]\n\t" "ds_read_b64_tr_b16 a[128:129], %[vr1] offset:35872\n\t"
    "v_mfma_f32_16x16x32_bf16 v[128:131], a[148:151], a[16:19], v[128:131]\n\t" "ds_read_b64_tr_b16 a[130:131], %[vr1] offset:39456\n\t"
    "v_mfma_f32_16x16x32_bf16 v[132:135], a[144:147], a[4:7], v[132:135]\n\t" "global_load_dword v29, %[va], %[cd]\n\t" "v_mfma_f32_16x16x32_bf16 v[132:135], a[148:151], a[20:23], v[132:135]\n\t"
    "v_mfma_f32_16x16x32_bf16 v[136:139], a[144:147], a[8:11], v[136:139]\n\t" "ds_read_b64_tr_b16 a[132:133], %[vr1] offset:50208\n\t"
    "v_mfma_f32_16x16x32_bf16 v[136:139], a[148:151], a[24:27], v[136:139]\n\t" "ds_read_b64_tr_b16 a[134:135], %[vr1] offset:53792\n\t"
    "v_mfma_f32_16x16x32_bf16 v[140:143], a[144:147], a[12:15], v[140:143]\n\t" "v_mfma_f32_16x16x32_bf16 v[140:143], a[148:151], a[28:31], v[140:143]\n\t" "s_waitcnt lgkmcnt(4)\n\t"
    "v_mfma_f32_16x16x32_bf16 v[144:147], a[152:155], a[0:3], v[144:147]\n\t" "ds_read_b64_tr_b16 a[136:137], %[vr3] offset:35840\n\t"
    "v_mfma_f32_16x16x32_bf16 v[144:147], a[156:159], a[16:19], v[144:147]\n\t" "ds_read_b64_tr_b16 a[138:139], %[vr3] offset:39424\n\t"
    "v_mfma_f32_16x16x32_bf16 v[148:151], a[152:155], a[4:7], v[148:151]\n\t" "global_load_dwordx4 a[80:83], %[va], s[76:77] offset:0 sc1\n\t"
    "v_mfma_f32_16x16x32_bf16 v[148:151], a[156:159], a[20:23], v[148:151]\n\t" "v_mfma_f32_16x16x32_bf16 v[152:155], a[152:155], a[8:11], v[152:155]\n\t"
    "ds_read_b64_tr_b16 a[140:141], %[vr3] offset:50176\n\t" "v_mfma_f32_16x16x32_bf16 v[152:155], a[156:159], a[24:27], v[152:155]\n\t" "ds_read_b64_tr_b16 a[142:143], %[vr3] offset:53760\n\t"
    "s_add_u32 m0, %[sl], 57344\n\t" "v_mfma_f32_16x16x32_bf16 v[156:159], a[152:155], a[12:15], v[156:159]\n\t" "global_load_lds_dwordx4 %[vb0], s[78:79]\n\t"
    "v_mfma_f32_16x16x32_bf16 v[156:159], a[156:159], a[28:31], v[156:159]\n\t" "s_waitcnt lgkmcnt(4)\n\t" "v_mfma_f32_16x16x32_bf16 v[160:163], a[128:131], a[0:3], v[160:163]\n\t"
    "ds_read_b64_tr_b16 a[144:145], %[vr3] offset:35872\n\t" "v_mfma_f32_16x16x32_bf16 v[160:163], a[132:135], a[16:19], v[160:163]\n\t" "ds_read_b64_tr_b16 a[146:147], %[vr3] offset:39456\n\t"
    "v_mfma_f32_16x16x32_bf16 v[164:167], a[128:131], a[4:7], v[164:167]\n\t" "v_mfma_f32_16x16x32_bf16 v[164:167], a[132:135], a[20:23], v[164:167]\n\t"
    "v_mfma_f32_16x16x32_bf16 v[168:171], a[128:131], a[8:11], v[168:171]\n\t" "ds_read_b64_tr_b16 a[148:149], %[vr3] offset:50208\n\t"
    "v_mfma_f32_16x16x32_bf16 v[168:171], a[132:135], a[24:27], v[168:171]\n\t" "ds_read_b64_tr_b16 a[150:151], %[vr3] offset:53792\n\t"
    "v_mfma_f32_16x16x32_bf16 v[172:175], a[128:131], a[12:15], v[172:175]\n\t" "global_load_dwordx4 a[84:87], %[va], s[76:77] offset:1024 sc1\n\t"
    "v_mfma_f32_16x16x32_bf16 v[172:175], a[132:135], a[28:31], v[172:175]\n\t" "s_waitcnt lgkmcnt(4)\n\t" "v_mfma_f32_16x16x32_bf16 v[176:179], a[136:139], a[0:3], v[176:179]\n\t"
    "ds_read_b64_tr_b16 a[152:153], %[vr5] offset:35840\n\t" "v_mfma_f32_16x16x32_bf16 v[176:179], a[140:143], a[16:19], v[176:179]\n\t" "ds_read_b64_tr_b16 a[154:155], %[vr5] offset:39424\n\t"
    "s_add_u32 m0, %[sl], 58368\n\t" "v_mfma_f32_16x16x32_bf16 v[180:183], a[136:139], a[4:7], v[180:183]\n\t" "global_load_lds_dwordx4 %[vb1], s[78:79]\n\t"
    "v_mfma_f32_16x16x32_bf16 v[180:183], a[140:143], a[20:23], v[180:183]\n\t" "v_mfma_f32_16x16x32_bf16 v[184:187], a[136:139], a[8:11], v[184:187]\n\t"
    "ds_read_b64_tr_b16 a[156:157], %[vr5] offset:50176\n\t" "v_mfma_f32_16x16x32_bf16 v[184:187], a[140:143], a[24:27], v[184:187]\n\t" "ds_read_b64_tr_b16 a[158:159], %[vr5] offset:53760\n\t"
    "v_mfma_f32_16x16x32_bf16 v[188:191], a[136:139], a[12:15], v[188:191]\n\t" "v_mfma_f32_16x16x32_bf16 v[188:191], a[140:143], a[28:31], v[188:191]\n\t" "s_waitcnt lgkmcnt(4)\n\t"
    "v_mfma_f32_16x16x32_bf16 v[192:195], a[144:147], a[0:3], v[192:195]\n\t" "ds_read_b64_tr_b16 a[128:129], %[vr5] offset:35872\n\t"
    "v_mfma_f32_16x16x32_bf16 v[192:195], a[148:151], a[16:19], v[192:195]\n\t" "ds_read_b64_tr_b16 a[130:131], %[vr5] offset:39456\n\t"
    "v_mfma_f32_16x16x32_bf16 v[196:199], a[144:147], a[4:7], v[196:199]\n\t" "global_load_dwordx4 a[88:91], %[va], s[76:77] offset:2048 sc1\n\t"
    "v_mfma_f32_16x16x32_bf16 v[196:199], a[148:151], a[20:23], v[196:199]\n\t" "v_mfma_f32_16x16x32_bf16 v[200:203], a[144:147], a[8:11], v[200:203]\n\t"
    "ds_read_b64_tr_b16 a[132:133], %[vr5] offset:50208\n\t" "v_mfma_f32_16x16x32_bf16 v[200:203], a[148:151], a[24:27], v[200:203]\n\t" "ds_read_b64_tr_b16 a[134:135], %[vr5] offset:53792\n\t"
    "v_mfma_f32_16x16x32_bf16 v[204:207], a[144:147], a[12:15], v[204:207]\n\t" "v_mfma_f32_16x16x32_bf16 v[204:207], a[148:151], a[28:31], v[204:207]\n\t" "s_waitcnt lgkmcnt(4)\n\t"
    "v_mfma_f32_16x16x32_bf16 v[208:211], a[152:155], a[0:3], v[208:211]\n\t" "ds_read_b64_tr_b16 a[136:137], %[vr7] offset:35840\n\t"
    "v_mfma_f32_16x16x32_bf16 v[208:211], a[156:159], a[16:19], v[208:211]\n\t" "ds_read_b64_tr_b16 a[138:139], %[vr7] offset:39424\n\t" "s_add_u32 m0, %[sl], 59392\n\t"
    "v_mfma_f32_16x16x32_bf16 v[212:215], a[152:155], a[4:7], v[212:215]\n\t" "global_load_lds_dwordx4 %[vb2], s[78:79]\n\t" "v_mfma_f32_16x16x32_bf16 v[212:215], a[156:159], a[20:23], v[212:215]\n\t"
    "v_mfma_f32_16x16x32_bf16 v[216:219], a[152:155], a[8:11], v[216:219]\n\t" "ds_read_b64_tr_b16 a[140:141], %[vr7] offset:50176\n\t"
    "v_mfma_f32_16x16x32_bf16 v[216:219], a[156:159], a[24:27], v[216:219]\n\t" "ds_read_b64_tr_b16 a[142:143], %[vr7] offset:53760\n\t"
    "v_mfma_f32_16x16x32_bf16 v[220:223], a[152:155], a[12:15], v[220:223]\n\t" "global_load_dwordx4 a[92:95], %[va], s[76:77] offset:3072 sc1\n\t"
    "v_mfma_f32_16x16x32_bf16 v[220:223], a[156:159], a[28:31], v[220:223]\n\t" "s_add_u32 s76, s76, 16384\n\t" "s_addc_u32 s77, s77, 0\n\t" "s_waitcnt lgkmcnt(4)\n\t"
    "v_mfma_f32_16x16x32_bf16 v[224:227], a[128:131], a[0:3], v[224:227]\n\t" "s_waitcnt vmcnt(24)\n\t" "s_barrier\n\t" "ds_read_b64_tr_b16 a[144:145], %[vr0] offset:0\n\t"
    "v_mfma_f32_16x16x32_bf16 v[224:227], a[132:135], a[16:19], v[224:227]\n\t" "ds_read_b64_tr_b16 a[146:147], %[vr0] offset:3584\n\t"
    "v_mfma_f32_16x16x32_bf16 v[228:231], a[128:131], a[4:7], v[228:231]\n\t" "v_mfma_f32_16x16x32_bf16 v[228:231], a[132:135], a[20:23], v[228:231]\n\t"
    "v_mfma_f32_16x16x32_bf16 v[232:235], a[128:131], a[8:11], v[232:235]\n\t" "ds_read_b64_tr_b16 a[148:149], %[vr0] offset:14336\n\t"
    "v_mfma_f32_16x16x32_bf16 v[232:235], a[132:135], a[24:27], v[232:235]\n\t" "ds_read_b64_tr_b16 a[150:151], %[vr0] offset:17920\n\t" "s_add_u32 m0, %[sl], 60416\n\t"
    "v_mfma_f32_16x16x32_bf16 v[236:239], a[128:131], a[12:15], v[236:239]\n\t" "s_mov_b64 exec, s[82:83]\n\t" "global_load_lds_dwordx4 %[vb3], s[78:79]\n\t" "s_mov_b64 exec, -1\n\t"
    "v_mfma_f32_16x16x32_bf16 v[236:239], a[132:135], a[28:31], v[236:239]\n\t" "s_add_u32 s78, s78, 229376\n\t" "s_addc_u32 s79, s79, 0\n\t" "s_waitcnt lgkmcnt(4)\n\t"
    "v_mfma_f32_16x16x32_bf16 v[240:243], a[136:139], a[0:3], v[240:243]\n\t" "ds_read_b64_tr_b16 a[152:153], %[vr0] offset:32\n\t"
    "v_mfma_f32_16x16x32_bf16 v[240:243], a[140:143], a[16:19], v[240:243]\n\t" "ds_read_b64_tr_b16 a[154:155], %[vr0] offset:3616\n\t"
    "v_mfma_f32_16x16x32_bf16 v[244:247], a[136:139], a[4:7], v[244:247]\n\t" "v_mfma_f32_16x16x32_bf16 v[244:247], a[140:143], a[20:23], v[244:247]\n\t"
    "v_mfma_f32_16x16x32_bf16 v[248:251], a[136:139], a[8:11], v[248:251]\n\t" "ds_read_b64_tr_b16 a[156:157], %[vr0] offset:14368\n\t"
    "v_mfma_f32_16x16x32_bf16 v[248:251], a[140:143], a[24:27], v[248:251]\n\t" "ds_read_b64_tr_b16 a[158:159], %[vr0] offset:17952\n\t"
    "v_mfma_f32_16x16x32_bf16 v[252:255], a[136:139], a[12:15], v[252:255]\n\t" "v_mfma_f32_16x16x32_bf16 v[252:255], a[140:143], a[28:31], v[252:255]\n\t" "s_waitcnt lgkmcnt(4) vmcnt(18)\n\t"
    "v_mfma_f32_16x16x32_bf16 v[32:35], a[144:147], a[32:35], v[32:35]\n\t" "s_barrier\n\t" "ds_read_b64_tr_b16 a[128:129], %[vr2] offset:0\n\t"
    "v_mfma_f32_16x16x32_bf16 v[32:35], a[148:151], a[48:51], v[32:35]\n\t" "ds_read_b64_tr_b16 a[130:131], %[vr2] offset:3584\n\t" "v_mfma_f32_16x16x32_bf16 v[36:39], a[144:147], a[36:39], v[36:39]\n\t"
    "global_load_dwordx4 a[96:99], %[va], s[76:77] offset:0 sc1\n\t" "v_mfma_f32_16x16x32_bf16 v[36:39], a[148:151], a[52:55], v[36:39]\n\t"
    "v_mfma_f32_16x16x32_bf16 v[40:43], a[144:147], a[40:43], v[40:43]\n\t" "ds_read_b64_tr_b16 a[132:133], %[vr2] offset:14336\n\t" "v_mfma_f32_16x16x32_bf16 v[40:43], a[148:151], a[56:59], v[40:43]\n\t"
    "ds_read_b64_tr_b16 a[134:135], %[vr2] offset:17920\n\t" "s_add_u32 m0, %[sl], 71680\n\t" "v_mfma_f32_16x16x32_bf16 v[44:47], a[144:147], a[44:47], v[44:47]\n\t"
    "global_load_lds_dwordx4 %[vb0], s[78:79]\n\t" "v_mfma_f32_16x16x32_bf16 v[44:47], a[148:151], a[60:63], v[44:47]\n\t" "s_waitcnt lgkmcnt(4)\n\t"
    "v_mfma_f32_16x16x32_bf16 v[48:51], a[152:155], a[32:35], v[48:51]\n\t" "ds_read_b64_tr_b16 a[136:137], %[vr2] offset:32\n\t" "v_mfma_f32_16x16x32_bf16 v[48:51], a[156:159], a[48:51], v[48:51]\n\t"
    "ds_read_b64_tr_b16 a[138:139], %[vr2] offset:3616\n\t" "v_mfma_f32_16x16x32_bf16 v[52:55], a[152:155], a[36:39], v[52:55]\n\t" "v_mfma_f32_16x16x32_bf16 v[52:55], a[156:159], a[52:55], v[52:55]\n\t"
    "v_mfma_f32_16x16x32_bf16 v[56:59], a[152:155], a[40:43], v[56:59]\n\t" "ds_read_b64_tr_b16 a[140:141], %[vr2] offset:14368\n\t" "v_mfma_f32_16x16x32_bf16 v[56:59], a[156:159], a[56:59], v[56:59]\n\t"
    "ds_read_b64_tr_b16 a[142:143], %[vr2] offset:17952\n\t" "v_mfma_f32_16x16x32_bf16 v[60:63], a[152:155], a[44:47], v[60:63]\n\t" "global_load_dwordx4 a[100:103], %[va], s[76:77] offset:1024 sc1\n\t"
    "v_mfma_f32_16x16x32_bf16 v[60:63], a[156:159], a[60:63], v[60:63]\n\t" "s_waitcnt lgkmcnt(4)\n\t" "v_mfma_f32_16x16x32_bf16 v[64:67], a[128:131], a[32:35], v[64:67]\n\t"
    "ds_read_b64_tr_b16 a[144:145], %[vr4] offset:0\n\t" "v_mfma_f32_16x16x32_bf16 v[64:67], a[132:135], a[48:51], v[64:67]\n\t" "ds_read_b64_tr_b16 a[146:147], %[vr4] offset:3584\n\t"
    "s_add_u32 m0, %[sl], 72704\n\t" "v_mfma_f32_16x16x32_bf16 v[68:71], a[128:131], a[36:39], v[68:71]\n\t" "global_load_lds_dwordx4 %[vb1], s[78:79]\n\t"
    "v_mfma_f32_16x16x32_bf16 v[68:71], a[132:135], a[52:55], v[68:71]\n\t" "v_mfma_f32_16x16x32_bf16 v[72:75], a[128:131], a[40:43], v[72:75]\n\t" "ds_read_b64_tr_b16 a[148:149], %[vr4] offset:14336\n\t"
    "v_mfma_f32_16x16x32_bf16 v[72:75], a[132:135], a[56:59], v[72:75]\n\t" "ds_read_b64_tr_b16 a[150:151], %[vr4] offset:17920\n\t" "v_mfma_f32_16x16x32_bf16 v[76:79], a[128:131], a[44:47], v[76:79]\n\t"
    "v_mfma_f32_16x16x32_bf16 v[76:79], a[132:135], a[60:63], v[76:79]\n\t" "s_waitcnt lgkmcnt(4)\n\t" "v_mfma_f32_16x16x32_bf16 v[80:83], a[136:139], a[32:35], v[80:83]\n\t"
    "ds_read_b64_tr_b16 a[152:153], %[vr4] offset:32\n\t" "v_mfma_f32_16x16x32_bf16 v[80:83], a[140:143], a[48:51], v[80:83]\n\t" "ds_read_b64_tr_b16 a[154:155], %[vr4] offset:3616\n\t"
    "v_mfma_f32_16x16x32_bf16 v[84:87], a[136:139], a[36:39], v[84:87]\n\t" "global_load_dwordx4 a[104:107], %[va], s[76:77] offset:2048 sc1\n\t"
    "v_mfma_f32_16x16x32_bf16 v[84:87], a[140:143], a[52:55], v[84:87]\n\t" "v_mfma_f32_16x16x32_bf16 v[88:91], a[136:139], a[40:43], v[88:91]\n\t" "ds_read_b64_tr_b16 a[156:157], %[vr4] offset:14368\n\t"
    "v_mfma_f32_16x16x32_bf16 v[88:91], a[140:143], a[56:59], v[88:91]\n\t" "ds_read_b64_tr_b16 a[158:159], %[vr4] offset:17952\n\t" "v_mfma_f32_16x16x32_bf16 v[92:95], a[136:139], a[44:47], v[92:95]\n\t"
    "v_mfma_f32_16x16x32_bf16 v[92:95], a[140:143], a[60:63], v[92:95]\n\t" "s_waitcnt lgkmcnt(4)\n\t" "v_mfma_f32_16x16x32_bf16 v[96:99], a[144:147], a[32:35], v[96:99]\n\t"
    "ds_read_b64_tr_b16 a[128:129], %[vr6] offset:0\n\t" "v_mfma_f32_16x16x32_bf16 v[96:99], a[148:151], a[48:51], v[96:99]\n\t" "ds_read_b64_tr_b16 a[130:131], %[vr6] offset:3584\n\t"
    "s_add_u32 m0, %[sl], 73728\n\t" "v_mfma_f32_16x16x32_bf16 v[100:103], a[144:147], a[36:39], v[100:103]\n\t" "global_load_lds_dwordx4 %[vb2], s[78:79]\n\t"
    "v_mfma_f32_16x16x32_bf16 v[100:103], a[148:151], a[52:55], v[100:103]\n\t" "v_mfma_f32_16x16x32_bf16 v[104:107], a[144:147], a[40:43], v[104:107]\n\t"
    "ds_read_b64_tr_b16 a[132:133], %[vr6] offset:14336\n\t" "v_mfma_f32_16x16x32_bf16 v[104:107], a[148:151], a[56:59], v[104:107]\n\t" "ds_read_b64_tr_b16 a[134:135], %[vr6] offset:17920\n\t"
    "v_mfma_f32_16x16x32_bf16 v[108:111], a[144:147], a[44:47], v[108:111]\n\t" "global_load_dwordx4 a[108:111], %[va], s[76:77] offset:3072 sc1\n\t"
    "v_mfma_f32_16x16x32_bf16 v[108:111], a[148:151], a[60:63], v[108:111]\n\t" "s_add_u32 s76, s76, 16384\n\t" "s_addc_u32 s77, s77, 0\n\t" "s_waitcnt lgkmcnt(4)\n\t"
    "v_mfma_f32_16x16x32_bf16 v[112:115], a[152:155], a[32:35], v[112:115]\n\t" "ds_read_b64_tr_b16 a[136:137], %[vr0] offset:7168\n\t"
    "v_mfma_f32_16x16x32_bf16 v[112:115], a[156:159], a[48:51], v[112:115]\n\t" "ds_read_b64_tr_b16 a[138:139], %[vr0] offset:10752\n\t"
    "v_mfma_f32_16x16x32_bf16 v[116:119], a[152:155], a[36:39], v[116:119]\n\t" "v_mfma_f32_16x16x32_bf16 v[116:119], a[156:159], a[52:55], v[116:119]\n\t"
    "v_mfma_f32_16x16x32_bf16 v[120:123], a[152:155], a[40:43], v[120:123]\n\t" "ds_read_b64_tr_b16 a[140:141], %[vr0] offset:21504\n\t"
    "v_mfma_f32_16x16x32_bf16 v[120:123], a[156:159], a[56:59], v[120:123]\n\t" "ds_read_b64_tr_b16 a[142:143], %[vr0] offset:25088\n\t" "s_add_u32 m0, %[sl], 74752\n\t"
    "v_mfma_f32_16x16x32_bf16 v[124:127], a[152:155], a[44:47], v[124:127]\n\t" "s_mov_b64 exec, s[82:83]\n\t" "global_load_lds_dwordx4 %[vb3], s[78:79]\n\t" "s_mov_b64 exec, -1\n\t"
    "v_mfma_f32_16x16x32_bf16 v[124:127], a[156:159], a[60:63], v[124:127]\n\t" "s_add_u32 s78, s78, s88\n\t" "s_addc_u32 s79, s79, s89\n\t" "s_waitcnt lgkmcnt(4)\n\t"
    "v_mfma_f32_16x16x32_bf16 v[128:131], a[128:131], a[32:35], v[128:131]\n\t" "ds_read_b64_tr_b16 a[144:145], %[vr0] offset:7200\n\t"
    "v_mfma_f32_16x16x32_bf16 v[128:131], a[132:135], a[48:51], v[128:131]\n\t" "ds_read_b64_tr_b16 a[146:147], %[vr0] offset:10784\n\t"
    "v_mfma_f32_16x16x32_bf16 v[132:135], a[128:131], a[36:39], v[132:135]\n\t" "v_mfma_f32_16x16x32_bf16 v[132:135], a[132:135], a[52:55], v[132:135]\n\t"
    "v_mfma_f32_16x16x32_bf16 v[136:139], a[128:131], a[40:43], v[136:139]\n\t" "ds_read_b64_tr_b16 a[148:149], %[vr0] offset:21536\n\t"
    "v_mfma_f32_16x16x32_bf16 v[136:139], a[132:135], a[56:59], v[136:139]\n\t" "ds_read_b64_tr_b16 a[150:151], %[vr0] offset:25120\n\t"
    "v_mfma_f32_16x16x32_bf16 v[140:143], a[128:131], a[44:47], v[140:143]\n\t" "v_mfma_f32_16x16x32_bf16 v[140:143], a[132:135], a[60:63], v[140:143]\n\t" "s_waitcnt lgkmcnt(4)\n\t"
    "v_mfma_f32_16x16x32_bf16 v[144:147], a[136:139], a[32:35], v[144:147]\n\t" "ds_read_b64_tr_b16 a[152:153], %[vr2] offset:7168\n\t"
    "v_mfma_f32_16x16x32_bf16 v[144:147], a[140:143], a[48:51], v[144:147]\n\t" "ds_read_b64_tr_b16 a[154:155], %[vr2] offset:10752\n\t"
    "v_mfma_f32_16x16x32_bf16 v[148:151], a[136:139], a[36:39], v[148:151]\n\t" "global_load_dwordx4 a[112:115], %[va], s[76:77] offset:0 sc1\n\t"
    "v_mfma_f32_16x16x32_bf16 v[148:151], a[140:143], a[52:55], v[148:151]\n\t" "v_mfma_f32_16x16x32_bf16 v[152:155], a[136:139], a[40:43], v[152:155]\n\t"
    "ds_read_b64_tr_b16 a[156:157], %[vr2] offset:21504\n\t" "v_mfma_f32_16x16x32_bf16 v[152:155], a[140:143], a[56:59], v[152:155]\n\t" "ds_read_b64_tr_b16 a[158:159], %[vr2] offset:25088\n\t"
    "s_add_u32 m0, %[sl], 86016\n\t" "v_mfma_f32_16x16x32_bf16 v[156:159], a[136:139], a[44:47], v[156:159]\n\t" "global_load_lds_dwordx4 %[vb0], s[78:79]\n\t"
    "v_mfma_f32_16x16x32_bf16 v[156:159], a[140:143], a[60:63], v[156:159]\n\t" "s_waitcnt lgkmcnt(4)\n\t" "v_mfma_f32_16x16x32_bf16 v[160:163], a[144:147], a[32:35], v[160:163]\n\t"
    "ds_read_b64_tr_b16 a[128:129], %[vr2] offset:7200\n\t" "v_mfma_f32_16x16x32_bf16 v[160:163], a[148:151], a[48:51], v[160:163]\n\t" "ds_read_b64_tr_b16 a[130:131], %[vr2] offset:10784\n\t"
    "v_mfma_f32_16x16x32_bf16 v[164:167], a[144:147], a[36:39], v[164:167]\n\t" "v_mfma_f32_16x16x32_bf16 v[164:167], a[148:151], a[52:55], v[164:167]\n\t"
    "v_mfma_f32_16x16x32_bf16 v[168:171], a[144:147], a[40:43], v[168:171]\n\t" "ds_read_b64_tr_b16 a[132:133], %[vr2] offset:21536\n\t"
    "v_mfma_f32_16x16x32_bf16 v[168:171], a[148:151], a[56:59], v[168:171]\n\t" "ds_read_b64_tr_b16 a[134:135], %[vr2] offset:25120\n\t"
    "v_mfma_f32_16x16x32_bf16 v[172:175], a[144:147], a[44:47], v[172:175]\n\t" "global_load_dwordx4 a[116:119], %[va], s[76:77] offset:1024 sc1\n\t"
    "v_mfma_f32_16x16x32_bf16 v[172:175], a[148:151], a[60:63], v[172:175]\n\t" "s_waitcnt lgkmcnt(4)\n\t" "v_mfma_f32_16x16x32_bf16 v[176:179], a[152:155], a[32:35], v[176:179]\n\t"
    "ds_read_b64_tr_b16 a[136:137], %[vr4] offset:7168\n\t" "v_mfma_f32_16x16x32_bf16 v[176:179], a[156:159], a[48:51], v[176:179]\n\t" "ds_read_b64_tr_b16 a[138:139], %[vr4] offset:10752\n\t"
    "s_add_u32 m0, %[sl], 87040\n\t" "v_mfma_f32_16x16x32_bf16 v[180:183], a[152:155], a[36:39], v[180:183]\n\t" "global_load_lds_dwordx4 %[vb1], s[78:79]\n\t"
    "v_mfma_f32_16x16x32_bf16 v[180:183], a[156:159], a[52:55], v[180:183]\n\t" "v_mfma_f32_16x16x32_bf16 v[184:187], a[152:155], a[40:43], v[184:187]\n\t"
    "ds_read_b64_tr_b16 a[140:141], %[vr4] offset:21504\n\t" "v_mfma_f32_16x16x32_bf16 v[184:187], a[156:159], a[56:59], v[184:187]\n\t" "ds_read_b64_tr_b16 a[142:143], %[vr4] offset:25088\n\t"
    "v_mfma_f32_16x16x32_bf16 v[188:191], a[152:155], a[44:47], v[188:191]\n\t" "v_mfma_f32_16x16x32_bf16 v[188:191], a[156:159], a[60:63], v[188:191]\n\t" "s_waitcnt lgkmcnt(4)\n\t"
    "v_mfma_f32_16x16x32_bf16 v[192:195], a[128:131], a[32:35], v[192:195]\n\t" "ds_read_b64_tr_b16 a[144:145], %[vr4] offset:7200\n\t"
    "v_mfma_f32_16x16x32_bf16 v[192:195], a[132:135], a[48:51], v[192:195]\n\t" "ds_read_b64_tr_b16 a[146:147], %[vr4] offset:10784\n\t"
    "v_mfma_f32_16x16x32_bf16 v[196:199], a[128:131], a[36:39], v[196:199]\n\t" "global_load_dwordx4 a[120:123], %[va], s[76:77] offset:2048 sc1\n\t"
    "v_mfma_f32_16x16x32_bf16 v[196:199], a[132:135], a[52:55], v[196:199]\n\t" "v_mfma_f32_16x16x32_bf16 v[200:203], a[128:131], a[40:43], v[200:203]\n\t"
    "ds_read_b64_tr_b16 a[148:149], %[vr4] offset:21536\n\t" "v_mfma_f32_16x16x32_bf16 v[200:203], a[132:135], a[56:59], v[200:203]\n\t" "ds_read_b64_tr_b16 a[150:151], %[vr4] offset:25120\n\t"
    "v_mfma_f32_16x16x32_bf16 v[204:207], a[128:131], a[44:47], v[204:207]\n\t" "v_mfma_f32_16x16x32_bf16 v[204:207], a[132:135], a[60:63], v[204:207]\n\t" "s_waitcnt lgkmcnt(4)\n\t"
    "v_mfma_f32_16x16x32_bf16 v[208:211], a[136:139], a[32:35], v[208:211]\n\t" "ds_read_b64_tr_b16 a[152:153], %[vr6] offset:7168\n\t"
    "v_mfma_f32_16x16x32_bf16 v[208:211], a[140:143], a[48:51], v[208:211]\n\t" "ds_read_b64_tr_b16 a[154:155], %[vr6] offset:10752\n\t" "s_add_u32 m0, %[sl], 88064\n\t"
    "v_mfma_f32_16x16x32_bf16 v[212:215], a[136:139], a[36:39], v[212:215]\n\t" "global_load_lds_dwordx4 %[vb2], s[78:79]\n\t" "v_mfma_f32_16x16x32_bf16 v[212:215], a[140:143], a[52:55], v[212:215]\n\t"
    "v_mfma_f32_16x16x32_bf16 v[216:219], a[136:139], a[40:43], v[216:219]\n\t" "ds_read_b64_tr_b16 a[156:157], %[vr6] offset:21504\n\t"
    "v_mfma_f32_16x16x32_bf16 v[216:219], a[140:143], a[56:59], v[216:219]\n\t" "ds_read_b64_tr_b16 a[158:159], %[vr6] offset:25088\n\t"
    "v_mfma_f32_16x16x32_bf16 v[220:223], a[136:139], a[44:47], v[220:223]\n\t" "global_load_dwordx4 a[124:127], %[va], s[76:77] offset:3072 sc1\n\t"
    "v_mfma_f32_16x16x32_bf16 v[220:223], a[140:143], a[60:63], v[220:223]\n\t" "s_add_u32 s76, s76, s86\n\t" "s_addc_u32 s77, s77, s87\n\t" "s_waitcnt lgkmcnt(4)\n\t"
    "v_mfma_f32_16x16x32_bf16 v[224:227], a[144:147], a[32:35], v[224:227]\n\t" "s_waitcnt vmcnt(24)\n\t" "s_barrier\n\t" "ds_read_b64_tr_b16 a[128:129], %[vr0] offset:28672\n\t"
    "v_mfma_f32_16x16x32_bf16 v[224:227], a[148:151], a[48:51], v[224:227]\n\t" "ds_read_b64_tr_b16 a[130:131], %[vr0] offset:32256\n\t"
    "v_mfma_f32_16x16x32_bf16 v[228:231], a[144:147], a[36:39], v[228:231]\n\t" "v_mfma_f32_16x16x32_bf16 v[228:231], a[148:151], a[52:55], v[228:231]\n\t"
    "v_mfma_f32_16x16x32_bf16 v[232:235], a[144:147], a[40:43], v[232:235]\n\t" "ds_read_b64_tr_b16 a[132:133], %[vr0] offset:43008\n\t"
    "v_mfma_f32_16x16x32_bf16 v[232:235], a[148:151], a[56:59], v[232:235]\n\t" "ds_read_b64_tr_b16 a[134:135], %[vr0] offset:46592\n\t" "s_add_u32 m0, %[sl], 89088\n\t"
    "v_mfma_f32_16x16x32_bf16 v[236:239], a[144:147], a[44:47], v[236:239]\n\t" "s_mov_b64 exec, s[82:83]\n\t" "global_load_lds_dwordx4 %[vb3], s[78:79]\n\t" "s_mov_b64 exec, -1\n\t"
    "v_mfma_f32_16x16x32_bf16 v[236:239], a[148:151], a[60:63], v[236:239]\n\t" "s_add_u32 s78, s78, 229376\n\t" "s_addc_u32 s79, s79, 0\n\t" "s_waitcnt lgkmcnt(4)\n\t"
    "v_mfma_f32_16x16x32_bf16 v[240:243], a[152:155], a[32:35], v[240:243]\n\t" "ds_read_b64_tr_b16 a[136:137], %[vr0] offset:28704\n\t"
    "v_mfma_f32_16x16x32_bf16 v[240:243], a[156:159], a[48:51], v[240:243]\n\t" "ds_read_b64_tr_b16 a[138:139], %[vr0] offset:32288\n\t"
    "v_mfma_f32_16x16x32_bf16 v[244:247], a[152:155], a[36:39], v[244:247]\n\t" "v_mfma_f32_16x16x32_bf16 v[244:247], a[156:159], a[52:55], v[244:247]\n\t"
    "v_mfma_f32_16x16x32_bf16 v[248:251], a[152:155], a[40:43], v[248:251]\n\t" "ds_read_b64_tr_b16 a[140:141], %[vr0] offset:43040\n\t"
    "v_mfma_f32_16x16x32_bf16 v[248:251], a[156:159], a[56:59], v[248:251]\n\t" "ds_read_b64_tr_b16 a[142:143], %[vr0] offset:46624\n\t"
    "v_mfma_f32_16x16x32_bf16 v[252:255], a[152:155], a[44:47], v[252:255]\n\t" "v_mfma_f32_16x16x32_bf16 v[252:255], a[156:159], a[60:63], v[252:255]\n\t" ".Ld0_%=:\n\t" "s_add_u32 s80, s80, 1\n\t"
    "s_cmp_lt_u32 s80, 2\n\t" "s_cbranch_scc1 .Lloop_%=\n\t" "s_branch .Ltail_%=\n\t" ".LE0_%=:\n\t" "s_waitcnt lgkmcnt(4) vmcnt(17)\n\t"
    "v_mfma_f32_16x16x32_bf16 v[32:35], a[128:131], a[0:3], v[32:35]\n\t" "s_barrier\n\t" "ds_read_b64_tr_b16 a[144:145], %[vr3] offset:28672\n\t"
    "v_mfma_f32_16x16x32_bf16 v[32:35], a[132:135], a[16:19], v[32:35]\n\t" "ds_read_b64_tr_b16 a[146:147], %[vr3] offset:32256\n\t" "v_mfma_f32_16x16x32_bf16 a[160:163], a[128:131], a[184:187], 0\n\t"
    "v_mfma_f32_16x16x32_bf16 v[36:39], a[128:131], a[4:7], v[36:39]\n\t" "global_load_dwordx4 a[64:67], %[va], s[76:77] offset:0 sc1\n\t"
    "v_mfma_f32_16x16x32_bf16 v[36:39], a[132:135], a[20:23], v[36:39]\n\t" "v_mfma_f32_16x16x32_bf16 v[40:43], a[128:131], a[8:11], v[40:43]\n\t" "ds_read_b64_tr_b16 a[148:149], %[vr3] offset:43008\n\t"
    "v_mfma_f32_16x16x32_bf16 v[40:43], a[132:135], a[24:27], v[40:43]\n\t" "ds_read_b64_tr_b16 a[150:151], %[vr3] offset:46592\n\t"
    "v_mfma_f32_16x16x32_bf16 a[160:163], a[132:135], a[188:191], a[160:163]\n\t" "s_add_u32 m0, %[sl], 43008\n\t" "v_mfma_f32_16x16x32_bf16 v[44:47], a[128:131], a[12:15], v[44:47]\n\t"
    "global_load_lds_dwordx4 %[vb0], s[78:79]\n\t" "v_mfma_f32_16x16x32_bf16 v[44:47], a[132:135], a[28:31], v[44:47]\n\t" "s_waitcnt lgkmcnt(4)\n\t"
    "v_mfma_f32_16x16x32_bf16 v[48:51], a[136:139], a[0:3], v[48:51]\n\t" "ds_read_b64_tr_b16 a[152:153], %[vr3] offset:28704\n\t" "v_mfma_f32_16x16x32_bf16 v[48:51], a[140:143], a[16:19], v[48:51]\n\t"
    "ds_read_b64_tr_b16 a[154:155], %[vr3] offset:32288\n\t" "v_mfma_f32_16x16x32_bf16 a[164:167], a[136:139], a[184:187], 0\n\t" "v_mfma_f32_16x16x32_bf16 v[52:55], a[136:139], a[4:7], v[52:55]\n\t"
    "v_mfma_f32_16x16x32_bf16 v[52:55], a[140:143], a[20:23], v[52:55]\n\t" "v_mfma_f32_16x16x32_bf16 v[56:59], a[136:139], a[8:11], v[56:59]\n\t" "ds_read_b64_tr_b16 a[156:157], %[vr3] offset:43040\n\t"
    "v_mfma_f32_16x16x32_bf16 v[56:59], a[140:143], a[24:27], v[56:59]\n\t" "ds_read_b64_tr_b16 a[158:159], %[vr3] offset:46624\n\t"
    "v_mfma_f32_16x16x32_bf16 a[164:167], a[140:143], a[188:191], a[164:167]\n\t" "v_mfma_f32_16x16x32_bf16 v[60:63], a[136:139], a[12:15], v[60:63]\n\t"
    "global_load_dwordx4 a[68:71], %[va], s[76:77] offset:1024 sc1\n\t" "v_mfma_f32_16x16x32_bf16 v[60:63], a[140:143], a[28:31], v[60:63]\n\t" "s_waitcnt lgkmcnt(4)\n\t"
    "v_mfma_f32_16x16x32_bf16 v[64:67], a[144:147], a[0:3], v[64:67]\n\t" "ds_read_b64_tr_b16 a[128:129], %[vr5] offset:28672\n\t" "v_mfma_f32_16x16x32_bf16 v[64:67], a[148:151], a[16:19], v[64:67]\n\t"
    "ds_read_b64_tr_b16 a[130:131], %[vr5] offset:32256\n\t" "s_add_u32 m0, %[sl], 44032\n\t" "v_mfma_f32_16x16x32_bf16 v[68:71], a[144:147], a[4:7], v[68:71]\n\t"
    "global_load_lds_dwordx4 %[vb1], s[78:79]\n\t" "v_mfma_f32_16x16x32_bf16 v[68:71], a[148:151], a[20:23], v[68:71]\n\t" "v_mfma_f32_16x16x32_bf16 v[72:75], a[144:147], a[8:11], v[72:75]\n\t"
    "ds_read_b64_tr_b16 a[132:133], %[vr5] offset:43008\n\t" "v_mfma_f32_16x16x32_bf16 v[72:75], a[148:151], a[24:27], v[72:75]\n\t" "ds_read_b64_tr_b16 a[134:135], %[vr5] offset:46592\n\t"
    "v_mfma_f32_16x16x32_bf16 v[76:79], a[144:147], a[12:15], v[76:79]\n\t" "v_mfma_f32_16x16x32_bf16 v[76:79], a[148:151], a[28:31], v[76:79]\n\t" "s_waitcnt lgkmcnt(4)\n\t"
    "v_mfma_f32_16x16x32_bf16 v[80:83], a[152:155], a[0:3], v[80:83]\n\t" "ds_read_b64_tr_b16 a[136:137], %[vr5] offset:28704\n\t" "v_mfma_f32_16x16x32_bf16 v[80:83], a[156:159], a[16:19], v[80:83]\n\t"
    "ds_read_b64_tr_b16 a[138:139], %[vr5] offset:32288\n\t" "v_mfma_f32_16x16x32_bf16 v[84:87], a[152:155], a[4:7], v[84:87]\n\t" "global_load_dwordx4 a[72:75], %[va], s[76:77] offset:2048 sc1\n\t"
    "v_mfma_f32_16x16x32_bf16 v[84:87], a[156:159], a[20:23], v[84:87]\n\t" "v_mfma_f32_16x16x32_bf16 v[88:91], a[152:155], a[8:11], v[88:91]\n\t" "ds_read_b64_tr_b16 a[140:141], %[vr5] offset:43040\n\t"
    "v_mfma_f32_16x16x32_bf16 v[88:91], a[156:159], a[24:27], v[88:91]\n\t" "ds_read_b64_tr_b16 a[142:143], %[vr5] offset:46624\n\t" "v_mfma_f32_16x16x32_bf16 v[92:95], a[152:155], a[12:15], v[92:95]\n\t"
    "v_mfma_f32_16x16x32_bf16 v[92:95], a[156:159], a[28:31], v[92:95]\n\t" "s_waitcnt lgkmcnt(4)\n\t" "v_mfma_f32_16x16x32_bf16 v[96:99], a[128:131], a[0:3], v[96:99]\n\t"
    "ds_read_b64_tr_b16 a[144:145], %[vr7] offset:28672\n\t" "v_mfma_f32_16x16x32_bf16 v[96:99], a[132:135], a[16:19], v[96:99]\n\t" "ds_read_b64_tr_b16 a[146:147], %[vr7] offset:32256\n\t"
    "s_add_u32 m0, %[sl], 45056\n\t" "v_mfma_f32_16x16x32_bf16 v[100:103], a[128:131], a[4:7], v[100:103]\n\t" "global_load_lds_dwordx4 %[vb2], s[78:79]\n\t"
    "v_mfma_f32_16x16x32_bf16 v[100:103], a[132:135], a[20:23], v[100:103]\n\t" "v_mfma_f32_16x16x32_bf16 v[104:107], a[128:131], a[8:11], v[104:107]\n\t"
    "ds_read_b64_tr_b16 a[148:149], %[vr7] offset:43008\n\t" "v_mfma_f32_16x16x32_bf16 v[104:107], a[132:135], a[24:27], v[104:107]\n\t" "ds_read_b64_tr_b16 a[150:151], %[vr7] offset:46592\n\t"
    "v_mfma_f32_16x16x32_bf16 v[108:111], a[128:131], a[12:15], v[108:111]\n\t" "global_load_dwordx4 a[76:79], %[va], s[76:77] offset:3072 sc1\n\t"
    "v_mfma_f32_16x16x32_bf16 v[108:111], a[132:135], a[28:31], v[108:111]\n\t" "s_add_u32 s76, s76, 16384\n\t" "s_addc_u32 s77, s77, 0\n\t" "s_waitcnt lgkmcnt(4)\n\t"
    "v_mfma_f32_16x16x32_bf16 v[112:115], a[136:139], a[0:3], v[112:115]\n\t" "ds_read_b64_tr_b16 a[152:153], %[vr1] offset:35840\n\t"
    "v_mfma_f32_16x16x32_bf16 v[112:115], a[140:143], a[16:19], v[112:115]\n\t" "ds_read_b64_tr_b16 a[154:155], %[vr1] offset:39424\n\t"
    "v_mfma_f32_16x16x32_bf16 v[116:119], a[136:139], a[4:7], v[116:119]\n\t" "v_mfma_f32_16x16x32_bf16 v[116:119], a[140:143], a[20:23], v[116:119]\n\t"
    "v_mfma_f32_16x16x32_bf16 v[120:123], a[136:139], a[8:11], v[120:123]\n\t" "ds_read_b64_tr_b16 a[156:157], %[vr1] offset:50176\n\t"
    "v_mfma_f32_16x16x32_bf16 v[120:123], a[140:143], a[24:27], v[120:123]\n\t" "ds_read_b64_tr_b16 a[158:159], %[vr1] offset:53760\n\t" "s_add_u32 m0, %[sl], 46080\n\t"
    "v_mfma_f32_16x16x32_bf16 v[124:127], a[136:139], a[12:15], v[124:127]\n\t" "s_mov_b64 exec, s[82:83]\n\t" "global_load_lds_dwordx4 %[vb3], s[78:79]\n\t" "s_mov_b64 exec, -1\n\t"
    "global_load_dword v29, %[va], %[cd]\n\t" "v_mfma_f32_16x16x32_bf16 v[124:127], a[140:143], a[28:31], v[124:127]\n\t" "s_add_u32 s78, s78, 229376\n\t" "s_addc_u32 s79, s79, 0\n\t"
    "s_waitcnt lgkmcnt(4)\n\t" "v_mfma_f32_16x16x32_bf16 v[128:131], a[144:147], a[0:3], v[128:131]\n\t" "ds_read_b64_tr_b16 a[128:129], %[vr1] offset:35872\n\t"
    "v_mfma_f32_16x16x32_bf16 v[128:131], a[148:151], a[16:19], v[128:131]\n\t" "ds_read_b64_tr_b16 a[130:131], %[vr1] offset:39456\n\t"
    "v_mfma_f32_16x16x32_bf16 a[168:171], a[144:147], a[184:187], 0\n\t" "v_mfma_f32_16x16x32_bf16 v[132:135], a[144:147], a[4:7], v[132:135]\n\t"
    "v_mfma_f32_16x16x32_bf16 v[132:135], a[148:151], a[20:23], v[132:135]\n\t" "v_mfma_f32_16x16x32_bf16 v[136:139], a[144:147], a[8:11], v[136:139]\n\t"
    "ds_read_b64_tr_b16 a[132:133], %[vr1] offset:50208\n\t" "v_mfma_f32_16x16x32_bf16 v[136:139], a[148:151], a[24:27], v[136:139]\n\t" "ds_read_b64_tr_b16 a[134:135], %[vr1] offset:53792\n\t"
    "v_mfma_f32_16x16x32_bf16 a[168:171], a[148:151], a[188:191], a[168:171]\n\t" "v_mfma_f32_16x16x32_bf16 v[140:143], a[144:147], a[12:15], v[140:143]\n\t"
    "v_mfma_f32_16x16x32_bf16 v[140:143], a[148:151], a[28:31], v[140:143]\n\t" "s_waitcnt lgkmcnt(4)\n\t" "v_mfma_f32_16x16x32_bf16 v[144:147], a[152:155], a[0:3], v[144:147]\n\t"
    "ds_read_b64_tr_b16 a[136:137], %[vr3] offset:35840\n\t" "v_mfma_f32_16x16x32_bf16 v[144:147], a[156:159], a[16:19], v[144:147]\n\t" "ds_read_b64_tr_b16 a[138:139], %[vr3] offset:39424\n\t"
    "v_mfma_f32_16x16x32_bf16 a[172:175], a[152:155], a[184:187], 0\n\t" "v_mfma_f32_16x16x32_bf16 v[148:151], a[152:155], a[4:7], v[148:151]\n\t"
    "global_load_dwordx4 a[80:83], %[va], s[76:77] offset:0 sc1\n\t" "v_mfma_f32_16x16x32_bf16 v[148:151], a[156:159], a[20:23], v[148:151]\n\t"
    "v_mfma_f32_16x16x32_bf16 v[152:155], a[152:155], a[8:11], v[152:155]\n\t" "ds_read_b64_tr_b16 a[140:141], %[vr3] offset:50176\n\t"
    "v_mfma_f32_16x16x32_bf16 v[152:155], a[156:159], a[24:27], v[152:155]\n\t" "ds_read_b64_tr_b16 a[142:143], %[vr3] offset:53760\n\t"
    "v_mfma_f32_16x16x32_bf16 a[172:175], a[156:159], a[188:191], a[172:175]\n\t" "s_add_u32 m0, %[sl], 57344\n\t" "v_mfma_f32_16x16x32_bf16 v[156:159], a[152:155], a[12:15], v[156:159]\n\t"
    "global_load_lds_dwordx4 %[vb0], s[78:79]\n\t" "v_mfma_f32_16x16x32_bf16 v[156:159], a[156:159], a[28:31], v[156:159]\n\t" "s_waitcnt lgkmcnt(4)\n\t"
    "v_mfma_f32_16x16x32_bf16 v[160:163], a[128:131], a[0:3], v[160:163]\n\t" "ds_read_b64_tr_b16 a[144:145], %[vr3] offset:35872\n\t"
    "v_mfma_f32_16x16x32_bf16 v[160:163], a[132:135], a[16:19], v[160:163]\n\t" "ds_read_b64_tr_b16 a[146:147], %[vr3] offset:39456\n\t"
    "v_mfma_f32_16x16x32_bf16 a[176:179], a[128:131], a[184:187], 0\n\t" "v_mfma_f32_16x16x32_bf16 v[164:167], a[128:131], a[4:7], v[164:167]\n\t"
    "v_mfma_f32_16x16x32_bf16 v[164:167], a[132:135], a[20:23], v[164:167]\n\t" "v_mfma_f32_16x16x32_bf16 v[168:171], a[128:131], a[8:11], v[168:171]\n\t"
    "ds_read_b64_tr_b16 a[148:149], %[vr3] offset:50208\n\t" "v_mfma_f32_16x16x32_bf16 v[168:171], a[132:135], a[24:27], v[168:171]\n\t" "ds_read_b64_tr_b16 a[150:151], %[vr3] offset:53792\n\t"
    "v_mfma_f32_16x16x32_bf16 a[176:179], a[132:135], a[188:191], a[176:179]\n\t" "v_mfma_f32_16x16x32_bf16 v[172:175], a[128:131], a[12:15], v[172:175]\n\t"
    "global_load_dwordx4 a[84:87], %[va], s[76:77] offset:1024 sc1\n\t" "v_mfma_f32_16x16x32_bf16 v[172:175], a[132:135], a[28:31], v[172:175]\n\t" "s_waitcnt lgkmcnt(4)\n\t"
    "v_mfma_f32_16x16x32_bf16 v[176:179], a[136:139], a[0:3], v[176:179]\n\t" "ds_read_b64_tr_b16 a[152:153], %[vr5] offset:35840\n\t"
    "v_mfma_f32_16x16x32_bf16 v[176:179], a[140:143], a[16:19], v[176:179]\n\t" "ds_read_b64_tr_b16 a[154:155], %[vr5] offset:39424\n\t" "s_add_u32 m0, %[sl], 58368\n\t"
    "v_mfma_f32_16x16x32_bf16 v[180:183], a[136:139], a[4:7], v[180:183]\n\t" "global_load_lds_dwordx4 %[vb1], s[78:79]\n\t" "v_mfma_f32_16x16x32_bf16 v[180:183], a[140:143], a[20:23], v[180:183]\n\t"
    "v_mfma_f32_16x16x32_bf16 v[184:187], a[136:139], a[8:11], v[184:187]\n\t" "ds_read_b64_tr_b16 a[156:157], %[vr5] offset:50176\n\t"
    "v_mfma_f32_16x16x32_bf16 v[184:187], a[140:143], a[24:27], v[184:187]\n\t" "ds_read_b64_tr_b16 a[158:159], %[vr5] offset:53760\n\t"
    "v_mfma_f32_16x16x32_bf16 v[188:191], a[136:139], a[12:15], v[188:191]\n\t" "v_mfma_f32_16x16x32_bf16 v[188:191], a[140:143], a[28:31], v[188:191]\n\t" "s_waitcnt lgkmcnt(4)\n\t"
    "v_mfma_f32_16x16x32_bf16 v[192:195], a[144:147], a[0:3], v[192:195]\n\t" "ds_read_b64_tr_b16 a[128:129], %[vr5] offset:35872\n\t"
    "v_mfma_f32_16x16x32_bf16 v[192:195], a[148:151], a[16:19], v[192:195]\n\t" "ds_read_b64_tr_b16 a[130:131], %[vr5] offset:39456\n\t"
    "v_mfma_f32_16x16x32_bf16 v[196:199], a[144:147], a[4:7], v[196:199]\n\t" "global_load_dwordx4 a[88:91], %[va], s[76:77] offset:2048 sc1\n\t"
    "v_mfma_f32_16x16x32_bf16 v[196:199], a[148:151], a[20:23], v[196:199]\n\t" "v_mfma_f32_16x16x32_bf16 v[200:203], a[144:147], a[8:11], v[200:203]\n\t"
    "ds_read_b64_tr_b16 a[132:133], %[vr5] offset:50208\n\t" "v_mfma_f32_16x16x32_bf16 v[200:203], a[148:151], a[24:27], v[200:203]\n\t" "ds_read_b64_tr_b16 a[134:135], %[vr5] offset:53792\n\t"
    "v_mfma_f32_16x16x32_bf16 v[204:207], a[144:147], a[12:15], v[204:207]\n\t" "v_mfma_f32_16x16x32_bf16 v[204:207], a[148:151], a[28:31], v[204:207]\n\t" "s_waitcnt lgkmcnt(4)\n\t"
    "v_mfma_f32_16x16x32_bf16 v[208:211], a[152:155], a[0:3], v[208:211]\n\t" "ds_read_b64_tr_b16 a[136:137], %[vr7] offset:35840\n\t"
    "v_mfma_f32_16x16x32_bf16 v[208:211], a[156:159], a[16:19], v[208:211]\n\t" "ds_read_b64_tr_b16 a[138:139], %[vr7] offset:39424\n\t" "s_add_u32 m0, %[sl], 59392\n\t"
    "v_mfma_f32_16x16x32_bf16 v[212:215], a[152:155], a[4:7], v[212:215]\n\t" "global_load_lds_dwordx4 %[vb2], s[78:79]\n\t" "v_mfma_f32_16x16x32_bf16 v[212:215], a[156:159], a[20:23], v[212:215]\n\t"
    "v_mfma_f32_16x16x32_bf16 v[216:219], a[152:155], a[8:11], v[216:219]\n\t" "ds_read_b64_tr_b16 a[140:141], %[vr7] offset:50176\n\t"
    "v_mfma_f32_16x16x32_bf16 v[216:219], a[156:159], a[24:27], v[216:219]\n\t" "ds_read_b64_tr_b16 a[142:143], %[vr7] offset:53760\n\t"
    "v_mfma_f32_16x16x32_bf16 v[220:223], a[152:155], a[12:15], v[220:223]\n\t" "global_load_dwordx4 a[92:95], %[va], s[76:77] offset:3072 sc1\n\t"
    "v_mfma_f32_16x16x32_bf16 v[220:223], a[156:159], a[28:31], v[220:223]\n\t" "s_add_u32 s76, s76, 16384\n\t" "s_addc_u32 s77, s77, 0\n\t" "s_waitcnt lgkmcnt(4)\n\t"
    "v_mfma_f32_16x16x32_bf16 v[224:227], a[128:131], a[0:3], v[224:227]\n\t" "s_waitcnt vmcnt(24)\n\t" "s_barrier\n\t" "ds_read_b64_tr_b16 a[144:145], %[vr0] offset:0\n\t"
    "v_mfma_f32_16x16x32_bf16 v[224:227], a[132:135], a[16:19], v[224:227]\n\t" "ds_read_b64_tr_b16 a[146:147], %[vr0] offset:3584\n\t"
    "v_mfma_f32_16x16x32_bf16 v[228:231], a[128:131], a[4:7], v[228:231]\n\t" "v_mfma_f32_16x16x32_bf16 v[228:231], a[132:135], a[20:23], v[228:231]\n\t"
    "v_mfma_f32_16x16x32_bf16 v[232:235], a[128:131], a[8:11], v[232:235]\n\t" "ds_read_b64_tr_b16 a[148:149], %[vr0] offset:14336\n\t"
    "v_mfma_f32_16x16x32_bf16 v[232:235], a[132:135], a[24:27], v[232:235]\n\t" "ds_read_b64_tr_b16 a[150:151], %[vr0] offset:17920\n\t" "s_add_u32 m0, %[sl], 60416\n\t"
    "v_mfma_f32_16x16x32_bf16 v[236:239], a[128:131], a[12:15], v[236:239]\n\t" "s_mov_b64 exec, s[82:83]\n\t" "global_load_lds_dwordx4 %[vb3], s[78:79]\n\t" "s_mov_b64 exec, -1\n\t"
    "v_mfma_f32_16x16x32_bf16 v[236:239], a[132:135], a[28:31], v[236:239]\n\t" "s_add_u32 s78, s78, 229376\n\t" "s_addc_u32 s79, s79, 0\n\t" "s_waitcnt lgkmcnt(4)\n\t"
    "v_mfma_f32_16x16x32_bf16 v[240:243], a[136:139], a[0:3], v[240:243]\n\t" "ds_read_b64_tr_b16 a[152:153], %[vr0] offset:32\n\t"
    "v_mfma_f32_16x16x32_bf16 v[240:243], a[140:143], a[16:19], v[240:243]\n\t" "ds_read_b64_tr_b16 a[154:155], %[vr0] offset:3616\n\t" "v_mfma_f32_16x16x32_bf16 a[180:183], a[136:139], a[184:187], 0\n\t"
    "v_mfma_f32_16x16x32_bf16 v[244:247], a[136:139], a[4:7], v[244:247]\n\t" "v_mfma_f32_16x16x32_bf16 v[244:247], a[140:143], a[20:23], v[244:247]\n\t"
    "v_mfma_f32_16x16x32_bf16 v[248:251], a[136:139], a[8:11], v[248:251]\n\t" "ds_read_b64_tr_b16 a[156:157], %[vr0] offset:14368\n\t"
    "v_mfma_f32_16x16x32_bf16 v[248:251], a[140:143], a[24:27], v[248:251]\n\t" "ds_read_b64_tr_b16 a[158:159], %[vr0] offset:17952\n\t"
    "v_mfma_f32_16x16x32_bf16 a[180:183], a[140:143], a[188:191], a[180:183]\n\t" "v_mfma_f32_16x16x32_bf16 v[252:255], a[136:139], a[12:15], v[252:255]\n\t"
    "v_mfma_f32_16x16x32_bf16 v[252:255], a[140:143], a[28:31], v[252:255]\n\t" "s_waitcnt lgkmcnt(4) vmcnt(18)\n\t" "v_mfma_f32_16x16x32_bf16 v[32:35], a[144:147], a[32:35], v[32:35]\n\t" "s_barrier\n\t"
    "ds_read_b64_tr_b16 a[128:129], %[vr2] offset:0\n\t" "v_mfma_f32_16x16x32_bf16 v[32:35], a[148:151], a[48:51], v[32:35]\n\t" "ds_read_b64_tr_b16 a[130:131], %[vr2] offset:3584\n\t"
    "v_mfma_f32_16x16x32_bf16 a[160:163], a[144:147], a[192:195], a[160:163]\n\t" "v_mfma_f32_16x16x32_bf16 v[36:39], a[144:147], a[36:39], v[36:39]\n\t"
    "global_load_dwordx4 a[96:99], %[va], s[76:77] offset:0 sc1\n\t" "v_mfma_f32_16x16x32_bf16 v[36:39], a[148:151], a[52:55], v[36:39]\n\t"
    "v_mfma_f32_16x16x32_bf16 v[40:43], a[144:147], a[40:43], v[40:43]\n\t" "ds_read_b64_tr_b16 a[132:133], %[vr2] offset:14336\n\t" "v_mfma_f32_16x16x32_bf16 v[40:43], a[148:151], a[56:59], v[40:43]\n\t"
    "ds_read_b64_tr_b16 a[134:135], %[vr2] offset:17920\n\t" "v_mfma_f32_16x16x32_bf16 a[160:163], a[148:151], a[196:199], a[160:163]\n\t" "s_add_u32 m0, %[sl], 71680\n\t"
    "v_mfma_f32_16x16x32_bf16 v[44:47], a[144:147], a[44:47], v[44:47]\n\t" "global_load_lds_dwordx4 %[vb0], s[78:79]\n\t" "v_mfma_f32_16x16x32_bf16 v[44:47], a[148:151], a[60:63], v[44:47]\n\t"
    "s_waitcnt lgkmcnt(4)\n\t" "v_mfma_f32_16x16x32_bf16 v[48:51], a[152:155], a[32:35], v[48:51]\n\t" "ds_read_b64_tr_b16 a[136:137], %[vr2] offset:32\n\t"
    "v_mfma_f32_16x16x32_bf16 v[48:51], a[156:159], a[48:51], v[48:51]\n\t" "ds_read_b64_tr_b16 a[138:139], %[vr2] offset:3616\n\t"
    "v_mfma_f32_16x16x32_bf16 a[164:167], a[152:155], a[192:195], a[164:167]\n\t" "v_mfma_f32_16x16x32_bf16 v[52:55], a[152:155], a[36:39], v[52:55]\n\t"
    "v_mfma_f32_16x16x32_bf16 v[52:55], a[156:159], a[52:55], v[52:55]\n\t" "v_mfma_f32_16x16x32_bf16 v[56:59], a[152:155], a[40:43], v[56:59]\n\t" "ds_read_b64_tr_b16 a[140:141], %[vr2] offset:14368\n\t"
    "v_mfma_f32_16x16x32_bf16 v[56:59], a[156:159], a[56:59], v[56:59]\n\t" "ds_read_b64_tr_b16 a[142:143], %[vr2] offset:17952\n\t"
    "v_mfma_f32_16x16x32_bf16 a[164:167], a[156:159], a[196:199], a[164:167]\n\t" "v_mfma_f32_16x16x32_bf16 v[60:63], a[152:155], a[44:47], v[60:63]\n\t"
    "global_load_dwordx4 a[100:103], %[va], s[76:77] offset:1024 sc1\n\t" "v_mfma_f32_16x16x32_bf16 v[60:63], a[156:159], a[60:63], v[60:63]\n\t" "s_waitcnt lgkmcnt(4)\n\t"
    "v_mfma_f32_16x16x32_bf16 v[64:67], a[128:131], a[32:35], v[64:67]\n\t" "ds_read_b64_tr_b16 a[144:145], %[vr4] offset:0\n\t" "v_mfma_f32_16x16x32_bf16 v[64:67], a[132:135], a[48:51], v[64:67]\n\t"
    "ds_read_b64_tr_b16 a[146:147], %[vr4] offset:3584\n\t" "s_add_u32 m0, %[sl], 72704\n\t" "v_mfma_f32_16x16x32_bf16 v[68:71], a[128:131], a[36:39], v[68:71]\n\t"
    "global_load_lds_dwordx4 %[vb1], s[78:79]\n\t" "v_mfma_f32_16x16x32_bf16 v[68:71], a[132:135], a[52:55], v[68:71]\n\t" "v_mfma_f32_16x16x32_bf16 v[72:75], a[128:131], a[40:43], v[72:75]\n\t"
    "ds_read_b64_tr_b16 a[148:149], %[vr4] offset:14336\n\t" "v_mfma_f32_16x16x32_bf16 v[72:75], a[132:135], a[56:59], v[72:75]\n\t" "ds_read_b64_tr_b16 a[150:151], %[vr4] offset:17920\n\t"
    "v_mfma_f32_16x16x32_bf16 v[76:79], a[128:131], a[44:47], v[76:79]\n\t" "v_mfma_f32_16x16x32_bf16 v[76:79], a[132:135], a[60:63], v[76:79]\n\t" "s_waitcnt lgkmcnt(4)\n\t"
    "v_mfma_f32_16x16x32_bf16 v[80:83], a[136:139], a[32:35], v[80:83]\n\t" "ds_read_b64_tr_b16 a[152:153], %[vr4] offset:32\n\t" "v_mfma_f32_16x16x32_bf16 v[80:83], a[140:143], a[48:51], v[80:83]\n\t"
    "ds_read_b64_tr_b16 a[154:155], %[vr4] offset:3616\n\t" "v_mfma_f32_16x16x32_bf16 v[84:87], a[136:139], a[36:39], v[84:87]\n\t" "global_load_dwordx4 a[104:107], %[va], s[76:77] offset:2048 sc1\n\t"
    "v_mfma_f32_16x16x32_bf16 v[84:87], a[140:143], a[52:55], v[84:87]\n\t" "v_mfma_f32_16x16x32_bf16 v[88:91], a[136:139], a[40:43], v[88:91]\n\t" "ds_read_b64_tr_b16 a[156:157], %[vr4] offset:14368\n\t"
    "v_mfma_f32_16x16x32_bf16 v[88:91], a[140:143], a[56:59], v[88:91]\n\t" "ds_read_b64_tr_b16 a[158:159], %[vr4] offset:17952\n\t" "v_mfma_f32_16x16x32_bf16 v[92:95], a[136:139], a[44:47], v[92:95]\n\t"
    "v_mfma_f32_16x16x32_bf16 v[92:95], a[140:143], a[60:63], v[92:95]\n\t" "s_waitcnt lgkmcnt(4)\n\t" "v_mfma_f32_16x16x32_bf16 v[96:99], a[144:147], a[32:35], v[96:99]\n\t"
    "ds_read_b64_tr_b16 a[128:129], %[vr6] offset:0\n\t" "v_mfma_f32_16x16x32_bf16 v[96:99], a[148:151], a[48:51], v[96:99]\n\t" "ds_read_b64_tr_b16 a[130:131], %[vr6] offset:3584\n\t"
    "s_add_u32 m0, %[sl], 73728\n\t" "v_mfma_f32_16x16x32_bf16 v[100:103], a[144:147], a[36:39], v[100:103]\n\t" "global_load_lds_dwordx4 %[vb2], s[78:79]\n\t"
    "v_mfma_f32_16x16x32_bf16 v[100:103], a[148:151], a[52:55], v[100:103]\n\t" "v_mfma_f32_16x16x32_bf16 v[104:107], a[144:147], a[40:43], v[104:107]\n\t"
    "ds_read_b64_tr_b16 a[132:133], %[vr6] offset:14336\n\t" "v_mfma_f32_16x16x32_bf16 v[104:107], a[148:151], a[56:59], v[104:107]\n\t" "ds_read_b64_tr_b16 a[134:135], %[vr6] offset:17920\n\t"
    "v_mfma_f32_16x16x32_bf16 v[108:111], a[144:147], a[44:47], v[108:111]\n\t" "global_load_dwordx4 a[108:111], %[va], s[76:77] offset:3072 sc1\n\t"
    "v_mfma_f32_16x16x32_bf16 v[108:111], a[148:151], a[60:63], v[108:111]\n\t" "s_add_u32 s76, s76, 16384\n\t" "s_addc_u32 s77, s77, 0\n\t" "s_waitcnt lgkmcnt(4)\n\t"
    "v_mfma_f32_16x16x32_bf16 v[112:115], a[152:155], a[32:35], v[112:115]\n\t" "ds_read_b64_tr_b16 a[136:137], %[vr0] offset:7168\n\t"
    "v_mfma_f32_16x16x32_bf16 v[112:115], a[156:159], a[48:51], v[112:115]\n\t" "ds_read_b64_tr_b16 a[138:139], %[vr0] offset:10752\n\t"
    "v_mfma_f32_16x16x32_bf16 v[116:119], a[152:155], a[36:39], v[116:119]\n\t" "v_mfma_f32_16x16x32_bf16 v[116:119], a[156:159], a[52:55], v[116:119]\n\t"
    "v_mfma_f32_16x16x32_bf16 v[120:123], a[152:155], a[40:43], v[120:123]\n\t" "ds_read_b64_tr_b16 a[140:141], %[vr0] offset:21504\n\t"
    "v_mfma_f32_16x16x32_bf16 v[120:123], a[156:159], a[56:59], v[120:123]\n\t" "ds_read_b64_tr_b16 a[142:143], %[vr0] offset:25088\n\t" "s_add_u32 m0, %[sl], 74752\n\t"
    "v_mfma_f32_16x16x32_bf16 v[124:127], a[152:155], a[44:47], v[124:127]\n\t" "s_mov_b64 exec, s[82:83]\n\t" "global_load_lds_dwordx4 %[vb3], s[78:79]\n\t" "s_mov_b64 exec, -1\n\t"
    "v_mfma_f32_16x16x32_bf16 v[124:127], a[156:159], a[60:63], v[124:127]\n\t" "s_add_u32 s78, s78, s88\n\t" "s_addc_u32 s79, s79, s89\n\t" "s_waitcnt lgkmcnt(4)\n\t"
    "v_mfma_f32_16x16x32_bf16 v[128:131], a[128:131], a[32:35], v[128:131]\n\t" "ds_read_b64_tr_b16 a[144:145], %[vr0] offset:7200\n\t"
    "v_mfma_f32_16x16x32_bf16 v[128:131], a[132:135], a[48:51], v[128:131]\n\t" "ds_read_b64_tr_b16 a[146:147], %[vr0] offset:10784\n\t"
    "v_mfma_f32_16x16x32_bf16 a[168:171], a[128:131], a[192:195], a[168:171]\n\t" "v_mfma_f32_16x16x32_bf16 v[132:135], a[128:131], a[36:39], v[132:135]\n\t"
    "v_mfma_f32_16x16x32_bf16 v[132:135], a[132:135], a[52:55], v[132:135]\n\t" "v_mfma_f32_16x16x32_bf16 v[136:139], a[128:131], a[40:43], v[136:139]\n\t"
    "ds_read_b64_tr_b16 a[148:149], %[vr0] offset:21536\n\t" "v_mfma_f32_16x16x32_bf16 v[136:139], a[132:135], a[56:59], v[136:139]\n\t" "ds_read_b64_tr_b16 a[150:151], %[vr0] offset:25120\n\t"
    "v_mfma_f32_16x16x32_bf16 a[168:171], a[132:135], a[196:199], a[168:171]\n\t" "v_mfma_f32_16x16x32_bf16 v[140:143], a[128:131], a[44:47], v[140:143]\n\t"
    "v_mfma_f32_16x16x32_bf16 v[140:143], a[132:135], a[60:63], v[140:143]\n\t" "s_waitcnt lgkmcnt(4)\n\t" "v_mfma_f32_16x16x32_bf16 v[144:147], a[136:139], a[32:35], v[144:147]\n\t"
    "ds_read_b64_tr_b16 a[152:153], %[vr2] offset:7168\n\t" "v_mfma_f32_16x16x32_bf16 v[144:147], a[140:143], a[48:51], v[144:147]\n\t" "ds_read_b64_tr_b16 a[154:155], %[vr2] offset:10752\n\t"
    "v_mfma_f32_16x16x32_bf16 a[172:175], a[136:139], a[192:195], a[172:175]\n\t" "v_mfma_f32_16x16x32_bf16 v[148:151], a[136:139], a[36:39], v[148:151]\n\t"
    "global_load_dwordx4 a[112:115], %[va], s[76:77] offset:0 sc1\n\t" "v_mfma_f32_16x16x32_bf16 v[148:151], a[140:143], a[52:55], v[148:151]\n\t"
    "v_mfma_f32_16x16x32_bf16 v[152:155], a[136:139], a[40:43], v[152:155]\n\t" "ds_read_b64_tr_b16 a[156:157], %[vr2] offset:21504\n\t"
    "v_mfma_f32_16x16x32_bf16 v[152:155], a[140:143], a[56:59], v[152:155]\n\t" "ds_read_b64_tr_b16 a[158:159], %[vr2] offset:25088\n\t"
    "v_mfma_f32_16x16x32_bf16 a[172:175], a[140:143], a[196:199], a[172:175]\n\t" "s_add_u32 m0, %[sl], 86016\n\t" "v_mfma_f32_16x16x32_bf16 v[156:159], a[136:139], a[44:47], v[156:159]\n\t"
    "global_load_lds_dwordx4 %[vb0], s[78:79]\n\t" "v_mfma_f32_16x16x32_bf16 v[156:159], a[140:143], a[60:63], v[156:159]\n\t" "s_waitcnt lgkmcnt(4)\n\t"
    "v_mfma_f32_16x16x32_bf16 v[160:163], a[144:147], a[32:35], v[160:163]\n\t" "ds_read_b64_tr_b16 a[128:129], %[vr2] offset:7200\n\t"
    "v_mfma_f32_16x16x32_bf16 v[160:163], a[148:151], a[48:51], v[160:163]\n\t" "ds_read_b64_tr_b16 a[130:131], %[vr2] offset:10784\n\t"
    "v_mfma_f32_16x16x32_bf16 a[176:179], a[144:147], a[192:195], a[176:179]\n\t" "v_mfma_f32_16x16x32_bf16 v[164:167], a[144:147], a[36:39], v[164:167]\n\t"
    "v_mfma_f32_16x16x32_bf16 v[164:167], a[148:151], a[52:55], v[164:167]\n\t" "v_mfma_f32_16x16x32_bf16 v[168:171], a[144:147], a[40:43], v[168:171]\n\t"
    "ds_read_b64_tr_b16 a[132:133], %[vr2] offset:21536\n\t" "v_mfma_f32_16x16x32_bf16 v[168:171], a[148:151], a[56:59], v[168:171]\n\t" "ds_read_b64_tr_b16 a[134:135], %[vr2] offset:25120\n\t"
    "v_mfma_f32_16x16x32_bf16 a[176:179], a[148:151], a[196:199], a[176:179]\n\t" "v_mfma_f32_16x16x32_bf16 v[172:175], a[144:147], a[44:47], v[172:175]\n\t"
    "global_load_dwordx4 a[116:119], %[va], s[76:77] offset:1024 sc1\n\t" "v_mfma_f32_16x16x32_bf16 v[172:175], a[148:151], a[60:63], v[172:175]\n\t" "s_waitcnt lgkmcnt(4)\n\t"
    "v_mfma_f32_16x16x32_bf16 v[176:179], a[152:155], a[32:35], v[176:179]\n\t" "ds_read_b64_tr_b16 a[136:137], %[vr4] offset:7168\n\t"
    "v_mfma_f32_16x16x32_bf16 v[176:179], a[156:159], a[48:51], v[176:179]\n\t" "ds_read_b64_tr_b16 a[138:139], %[vr4] offset:10752\n\t" "s_add_u32 m0, %[sl], 87040\n\t"
    "v_mfma_f32_16x16x32_bf16 v[180:183], a[152:155], a[36:39], v[180:183]\n\t" "global_load_lds_dwordx4 %[vb1], s[78:79]\n\t" "v_mfma_f32_16x16x32_bf16 v[180:183], a[156:159], a[52:55], v[180:183]\n\t"
    "v_mfma_f32_16x16x32_bf16 v[184:187], a[152:155], a[40:43], v[184:187]\n\t" "ds_read_b64_tr_b16 a[140:141], %[vr4] offset:21504\n\t"
    "v_mfma_f32_16x16x32_bf16 v[184:187], a[156:159], a[56:59], v[184:187]\n\t" "ds_read_b64_tr_b16 a[142:143], %[vr4] offset:25088\n\t"
    "v_mfma_f32_16x16x32_bf16 v[188:191], a[152:155], a[44:47], v[188:191]\n\t" "v_mfma_f32_16x16x32_bf16 v[188:191], a[156:159], a[60:63], v[188:191]\n\t" "s_waitcnt lgkmcnt(4)\n\t"
    "v_mfma_f32_16x16x32_bf16 v[192:195], a[128:131], a[32:35], v[192:195]\n\t" "ds_read_b64_tr_b16 a[144:145], %[vr4] offset:7200\n\t"
    "v_mfma_f32_16x16x32_bf16 v[192:195], a[132:135], a[48:51], v[192:195]\n\t" "ds_read_b64_tr_b16 a[146:147], %[vr4] offset:10784\n\t"
    "v_mfma_f32_16x16x32_bf16 v[196:199], a[128:131], a[36:39], v[196:199]\n\t" "global_load_dwordx4 a[120:123], %[va], s[76:77] offset:2048 sc1\n\t"
    "v_mfma_f32_16x16x32_bf16 v[196:199], a[132:135], a[52:55], v[196:199]\n\t" "v_mfma_f32_16x16x32_bf16 v[200:203], a[128:131], a[40:43], v[200:203]\n\t"
    "ds_read_b64_tr_b16 a[148:149], %[vr4] offset:21536\n\t" "v_mfma_f32_16x16x32_bf16 v[200:203], a[132:135], a[56:59], v[200:203]\n\t" "ds_read_b64_tr_b16 a[150:151], %[vr4] offset:25120\n\t"
    "v_mfma_f32_16x16x32_bf16 v[204:207], a[128:131], a[44:47], v[204:207]\n\t" "v_mfma_f32_16x16x32_bf16 v[204:207], a[132:135], a[60:63], v[204:207]\n\t" "s_waitcnt lgkmcnt(4)\n\t"
    "v_mfma_f32_16x16x32_bf16 v[208:211], a[136:139], a[32:35], v[208:211]\n\t" "ds_read_b64_tr_b16 a[152:153], %[vr6] offset:7168\n\t"
    "v_mfma_f32_16x16x32_bf16 v[208:211], a[140:143], a[48:51], v[208:211]\n\t" "ds_read_b64_tr_b16 a[154:155], %[vr6] offset:10752\n\t" "s_add_u32 m0, %[sl], 88064\n\t"
    "v_mfma_f32_16x16x32_bf16 v[212:215], a[136:139], a[36:39], v[212:215]\n\t" "global_load_lds_dwordx4 %[vb2], s[78:79]\n\t" "v_mfma_f32_16x16x32_bf16 v[212:215], a[140:143], a[52:55], v[212:215]\n\t"
    "v_mfma_f32_16x16x32_bf16 v[216:219], a[136:139], a[40:43], v[216:219]\n\t" "ds_read_b64_tr_b16 a[156:157], %[vr6] offset:21504\n\t"
    "v_mfma_f32_16x16x32_bf16 v[216:219], a[140:143], a[56:59], v[216:219]\n\t" "ds_read_b64_tr_b16 a[158:159], %[vr6] offset:25088\n\t"
    "v_mfma_f32_16x16x32_bf16 v[220:223], a[136:139], a[44:47], v[220:223]\n\t" "global_load_dwordx4 a[124:127], %[va], s[76:77] offset:3072 sc1\n\t"
    "v_mfma_f32_16x16x32_bf16 v[220:223], a[140:143], a[60:63], v[220:223]\n\t" "s_add_u32 s76, s76, s86\n\t" "s_addc_u32 s77, s77, s87\n\t" "s_waitcnt lgkmcnt(4)\n\t"
    "v_mfma_f32_16x16x32_bf16 v[224:227], a[144:147], a[32:35], v[224:227]\n\t" "s_waitcnt vmcnt(24)\n\t" "s_barrier\n\t" "ds_read_b64_tr_b16 a[128:129], %[vr0] offset:28672\n\t"
    "v_mfma_f32_16x16x32_bf16 v[224:227], a[148:151], a[48:51], v[224:227]\n\t" "ds_read_b64_tr_b16 a[130:131], %[vr0] offset:32256\n\t"
    "v_mfma_f32_16x16x32_bf16 v[228:231], a[144:147], a[36:39], v[228:231]\n\t" "v_mfma_f32_16x16x32_bf16 v[228:231], a[148:151], a[52:55], v[228:231]\n\t"
    "v_mfma_f32_16x16x32_bf16 v[232:235], a[144:147], a[40:43], v[232:235]\n\t" "ds_read_b64_tr_b16 a[132:133], %[vr0] offset:43008\n\t"
    "v_mfma_f32_16x16x32_bf16 v[232:235], a[148:151], a[56:59], v[232:235]\n\t" "ds_read_b64_tr_b16 a[134:135], %[vr0] offset:46592\n\t" "s_add_u32 m0, %[sl], 89088\n\t"
    "v_mfma_f32_16x16x32_bf16 v[236:239], a[144:147], a[44:47], v[236:239]\n\t" "s_mov_b64 exec, s[82:83]\n\t" "global_load_lds_dwordx4 %[vb3], s[78:79]\n\t" "s_mov_b64 exec, -1\n\t"
    "v_mfma_f32_16x16x32_bf16 v[236:239], a[148:151], a[60:63], v[236:239]\n\t" "s_add_u32 s78, s78, 229376\n\t" "s_addc_u32 s79, s79, 0\n\t" "s_waitcnt lgkmcnt(4)\n\t"
    "v_mfma_f32_16x16x32_bf16 v[240:243], a[152:155], a[32:35], v[240:243]\n\t" "ds_read_b64_tr_b16 a[136:137], %[vr0] offset:28704\n\t"
    "v_mfma_f32_16x16x32_bf16 v[240:243], a[156:159], a[48:51], v[240:243]\n\t" "ds_read_b64_tr_b16 a[138:139], %[vr0] offset:32288\n\t"
    "v_mfma_f32_16x16x32_bf16 a[180:183], a[152:155], a[192:195], a[180:183]\n\t" "v_mfma_f32_16x16x32_bf16 v[244:247], a[152:155], a[36:39], v[244:247]\n\t"
    "v_mfma_f32_16x16x32_bf16 v[244:247], a[156:159], a[52:55], v[244:247]\n\t" "v_mfma_f32_16x16x32_bf16 v[248:251], a[152:155], a[40:43], v[248:251]\n\t"
    "ds_read_b64_tr_b16 a[140:141], %[vr0] offset:43040\n\t" "v_mfma_f32_16x16x32_bf16 v[248:251], a[156:159], a[56:59], v[248:251]\n\t" "ds_read_b64_tr_b16 a[142:143], %[vr0] offset:46624\n\t"
    "v_mfma_f32_16x16x32_bf16 a[180:183], a[156:159], a[196:199], a[180:183]\n\t" "v_mfma_f32_16x16x32_bf16 v[252:255], a[152:155], a[44:47], v[252:255]\n\t"
    "v_mfma_f32_16x16x32_bf16 v[252:255], a[156:159], a[60:63], v[252:255]\n\t" "v_accvgpr_read_b32 v31, %[cp]\n\t" "s_nop 7\n\t" "s_mov_b64 exec, %[m01]\n\t"
    "global_store_dwordx4 v31, a[160:163], %[su0] offset:0 sc1\n\t" "global_store_dwordx4 v31, a[164:167], %[su0] offset:2304 sc1\n\t" "global_store_dwordx4 v31, a[172:175], %[su0] offset:1152 sc1\n\t"
    "global_store_dwordx4 v31, a[176:179], %[su0] offset:3456 sc1\n\t" "s_mov_b64 exec, %[m6]\n\t" "global_store_dwordx4 v31, a[168:171], %[su6] offset:0 sc1\n\t"
    "global_store_dwordx4 v31, a[180:183], %[su6] offset:1152 sc1\n\t" "s_mov_b64 exec, -1\n\t" "s_branch .Ld0_%=\n\t" ".LE1_%=:\n\t" "s_waitcnt lgkmcnt(4) vmcnt(17)\n\t"
    "v_mfma_f32_16x16x32_bf16 v[32:35], a[128:131], a[64:67], v[32:35]\n\t" "s_barrier\n\t" "ds_read_b64_tr_b16 a[144:145], %[vr2] offset:28672\n\t"
    "v_mfma_f32_16x16x32_bf16 v[32:35], a[132:135], a[80:83], v[32:35]\n\t" "ds_read_b64_tr_b16 a[146:147], %[vr2] offset:32256\n\t" "v_mfma_f32_16x16x32_bf16 a[160:163], a[128:131], a[184:187], 0\n\t"
    "v_mfma_f32_16x16x32_bf16 v[36:39], a[128:131], a[68:71], v[36:39]\n\t" "global_load_dwordx4 a[0:3], %[va], s[76:77] offset:0 sc1\n\t"
    "v_mfma_f32_16x16x32_bf16 v[36:39], a[132:135], a[84:87], v[36:39]\n\t" "v_mfma_f32_16x16x32_bf16 v[40:43], a[128:131], a[72:75], v[40:43]\n\t" "ds_read_b64_tr_b16 a[148:149], %[vr2] offset:43008\n\t"
    "v_mfma_f32_16x16x32_bf16 v[40:43], a[132:135], a[88:91], v[40:43]\n\t" "ds_read_b64_tr_b16 a[150:151], %[vr2] offset:46592\n\t"
    "v_mfma_f32_16x16x32_bf16 a[160:163], a[132:135], a[188:191], a[160:163]\n\t" "s_add_u32 m0, %[sl], 100352\n\t" "v_mfma_f32_16x16x32_bf16 v[44:47], a[128:131], a[76:79], v[44:47]\n\t"
    "global_load_lds_dwordx4 %[vb0], s[78:79]\n\t" "v_mfma_f32_16x16x32_bf16 v[44:47], a[132:135], a[92:95], v[44:47]\n\t" "s_waitcnt lgkmcnt(4)\n\t"
    "v_mfma_f32_16x16x32_bf16 v[48:51], a[136:139], a[64:67], v[48:51]\n\t" "ds_read_b64_tr_b16 a[152:153], %[vr2] offset:28704\n\t" "v_mfma_f32_16x16x32_bf16 v[48:51], a[140:143], a[80:83], v[48:51]\n\t"
    "ds_read_b64_tr_b16 a[154:155], %[vr2] offset:32288\n\t" "v_mfma_f32_16x16x32_bf16 a[164:167], a[136:139], a[184:187], 0\n\t" "v_mfma_f32_16x16x32_bf16 v[52:55], a[136:139], a[68:71], v[52:55]\n\t"
    "v_mfma_f32_16x16x32_bf16 v[52:55], a[140:143], a[84:87], v[52:55]\n\t" "v_mfma_f32_16x16x32_bf16 v[56:59], a[136:139], a[72:75], v[56:59]\n\t" "ds_read_b64_tr_b16 a[156:157], %[vr2] offset:43040\n\t"
    "v_mfma_f32_16x16x32_bf16 v[56:59], a[140:143], a[88:91], v[56:59]\n\t" "ds_read_b64_tr_b16 a[158:159], %[vr2] offset:46624\n\t"
    "v_mfma_f32_16x16x32_bf16 a[164:167], a[140:143], a[188:191], a[164:167]\n\t" "v_mfma_f32_16x16x32_bf16 v[60:63], a[136:139], a[76:79], v[60:63]\n\t"
    "global_load_dwordx4 a[4:7], %[va], s[76:77] offset:1024 sc1\n\t" "v_mfma_f32_16x16x32_bf16 v[60:63], a[140:143], a[92:95], v[60:63]\n\t" "s_waitcnt lgkmcnt(4)\n\t"
    "v_mfma_f32_16x16x32_bf16 v[64:67], a[144:147], a[64:67], v[64:67]\n\t" "ds_read_b64_tr_b16 a[128:129], %[vr4] offset:28672\n\t" "v_mfma_f32_16x16x32_bf16 v[64:67], a[148:151], a[80:83], v[64:67]\n\t"
    "ds_read_b64_tr_b16 a[130:131], %[vr4] offset:32256\n\t" "s_add_u32 m0, %[sl], 101376\n\t" "v_mfma_f32_16x16x32_bf16 v[68:71], a[144:147], a[68:71], v[68:71]\n\t"
    "global_load_lds_dwordx4 %[vb1], s[78:79]\n\t" "v_mfma_f32_16x16x32_bf16 v[68:71], a[148:151], a[84:87], v[68:71]\n\t" "v_mfma_f32_16x16x32_bf16 v[72:75], a[144:147], a[72:75], v[72:75]\n\t"
    "ds_read_b64_tr_b16 a[132:133], %[vr4] offset:43008\n\t" "v_mfma_f32_16x16x32_bf16 v[72:75], a[148:151], a[88:91], v[72:75]\n\t" "ds_read_b64_tr_b16 a[134:135], %[vr4] offset:46592\n\t"
    "v_mfma_f32_16x16x32_bf16 v[76:79], a[144:147], a[76:79], v[76:79]\n\t" "v_mfma_f32_16x16x32_bf16 v[76:79], a[148:151], a[92:95], v[76:79]\n\t" "s_waitcnt lgkmcnt(4)\n\t"
    "v_mfma_f32_16x16x32_bf16 v[80:83], a[152:155], a[64:67], v[80:83]\n\t" "ds_read_b64_tr_b16 a[136:137], %[vr4] offset:28704\n\t" "v_mfma_f32_16x16x32_bf16 v[80:83], a[156:159], a[80:83], v[80:83]\n\t"
    "ds_read_b64_tr_b16 a[138:139], %[vr4] offset:32288\n\t" "v_mfma_f32_16x16x32_bf16 v[84:87], a[152:155], a[68:71], v[84:87]\n\t" "global_load_dwordx4 a[8:11], %[va], s[76:77] offset:2048 sc1\n\t"
    "v_mfma_f32_16x16x32_bf16 v[84:87], a[156:159], a[84:87], v[84:87]\n\t" "v_mfma_f32_16x16x32_bf16 v[88:91], a[152:155], a[72:75], v[88:91]\n\t" "ds_read_b64_tr_b16 a[140:141], %[vr4] offset:43040\n\t"
    "v_mfma_f32_16x16x32_bf16 v[88:91], a[156:159], a[88:91], v[88:91]\n\t" "ds_read_b64_tr_b16 a[142:143], %[vr4] offset:46624\n\t" "v_mfma_f32_16x16x32_bf16 v[92:95], a[152:155], a[76:79], v[92:95]\n\t"
    "v_mfma_f32_16x16x32_bf16 v[92:95], a[156:159], a[92:95], v[92:95]\n\t" "s_waitcnt lgkmcnt(4)\n\t" "v_mfma_f32_16x16x32_bf16 v[96:99], a[128:131], a[64:67], v[96:99]\n\t"
    "ds_read_b64_tr_b16 a[144:145], %[vr6] offset:28672\n\t" "v_mfma_f32_16x16x32_bf16 v[96:99], a[132:135], a[80:83], v[96:99]\n\t" "ds_read_b64_tr_b16 a[146:147], %[vr6] offset:32256\n\t"
    "s_add_u32 m0, %[sl], 102400\n\t" "v_mfma_f32_16x16x32_bf16 v[100:103], a[128:131], a[68:71], v[100:103]\n\t" "global_load_lds_dwordx4 %[vb2], s[78:79]\n\t"
    "v_mfma_f32_16x16x32_bf16 v[100:103], a[132:135], a[84:87], v[100:103]\n\t" "v_mfma_f32_16x16x32_bf16 v[104:107], a[128:131], a[72:75], v[104:107]\n\t"
    "ds_read_b64_tr_b16 a[148:149], %[vr6] offset:43008\n\t" "v_mfma_f32_16x16x32_bf16 v[104:107], a[132:135], a[88:91], v[104:107]\n\t" "ds_read_b64_tr_b16 a[150:151], %[vr6] offset:46592\n\t"
    "v_mfma_f32_16x16x32_bf16 v[108:111], a[128:131], a[76:79], v[108:111]\n\t" "global_load_dwordx4 a[12:15], %[va], s[76:77] offset:3072 sc1\n\t"
    "v_mfma_f32_16x16x32_bf16 v[108:111], a[132:135], a[92:95], v[108:111]\n\t" "s_add_u32 s76, s76, 16384\n\t" "s_addc_u32 s77, s77, 0\n\t" "s_waitcnt lgkmcnt(4)\n\t"
    "v_mfma_f32_16x16x32_bf16 v[112:115], a[136:139], a[64:67], v[112:115]\n\t" "ds_read_b64_tr_b16 a[152:153], %[vr0] offset:35840\n\t"
    "v_mfma_f32_16x16x32_bf16 v[112:115], a[140:143], a[80:83], v[112:115]\n\t" "ds_read_b64_tr_b16 a[154:155], %[vr0] offset:39424\n\t"
    "v_mfma_f32_16x16x32_bf16 v[116:119], a[136:139], a[68:71], v[116:119]\n\t" "v_mfma_f32_16x16x32_bf16 v[116:119], a[140:143], a[84:87], v[116:119]\n\t"
    "v_mfma_f32_16x16x32_bf16 v[120:123], a[136:139], a[72:75], v[120:123]\n\t" "ds_read_b64_tr_b16 a[156:157], %[vr0] offset:50176\n\t"
    "v_mfma_f32_16x16x32_bf16 v[120:123], a[140:143], a[88:91], v[120:123]\n\t" "ds_read_b64_tr_b16 a[158:159], %[vr0] offset:53760\n\t" "s_add_u32 m0, %[sl], 103424\n\t"
    "v_mfma_f32_16x16x32_bf16 v[124:127], a[136:139], a[76:79], v[124:127]\n\t" "s_mov_b64 exec, s[82:83]\n\t" "global_load_lds_dwordx4 %[vb3], s[78:79]\n\t" "s_mov_b64 exec, -1\n\t"
    "global_load_dword v29, %[va], %[cd]\n\t" "v_mfma_f32_16x16x32_bf16 v[124:127], a[140:143], a[92:95], v[124:127]\n\t" "s_add_u32 s78, s78, 229376\n\t" "s_addc_u32 s79, s79, 0\n\t"
    "s_waitcnt lgkmcnt(4)\n\t" "v_mfma_f32_16x16x32_bf16 v[128:131], a[144:147], a[64:67], v[128:131]\n\t" "ds_read_b64_tr_b16 a[128:129], %[vr0] offset:35872\n\t"
    "v_mfma_f32_16x16x32_bf16 v[128:131], a[148:151], a[80:83], v[128:131]\n\t" "ds_read_b64_tr_b16 a[130:131], %[vr0] offset:39456\n\t"
    "v_mfma_f32_16x16x32_bf16 a[168:171], a[144:147], a[184:187], 0\n\t" "v_mfma_f32_16x16x32_bf16 v[132:135], a[144:147], a[68:71], v[132:135]\n\t"
    "v_mfma_f32_16x16x32_bf16 v[132:135], a[148:151], a[84:87], v[132:135]\n\t" "v_mfma_f32_16x16x32_bf16 v[136:139], a[144:147], a[72:75], v[136:139]\n\t"
    "ds_read_b64_tr_b16 a[132:133], %[vr0] offset:50208\n\t" "v_mfma_f32_16x16x32_bf16 v[136:139], a[148:151], a[88:91], v[136:139]\n\t" "ds_read_b64_tr_b16 a[134:135], %[vr0] offset:53792\n\t"
    "v_mfma_f32_16x16x32_bf16 a[168:171], a[148:151], a[188:191], a[168:171]\n\t" "v_mfma_f32_16x16x32_bf16 v[140:143], a[144:147], a[76:79], v[140:143]\n\t"
    "v_mfma_f32_16x16x32_bf16 v[140:143], a[148:151], a[92:95], v[140:143]\n\t" "s_waitcnt lgkmcnt(4)\n\t" "v_mfma_f32_16x16x32_bf16 v[144:147], a[152:155], a[64:67], v[144:147]\n\t"
    "ds_read_b64_tr_b16 a[136:137], %[vr2] offset:35840\n\t" "v_mfma_f32_16x16x32_bf16 v[144:147], a[156:159], a[80:83], v[144:147]\n\t" "ds_read_b64_tr_b16 a[138:139], %[vr2] offset:39424\n\t"
    "v_mfma_f32_16x16x32_bf16 a[172:175], a[152:155], a[184:187], 0\n\t" "v_mfma_f32_16x16x32_bf16 v[148:151], a[152:155], a[68:71], v[148:151]\n\t"
    "global_load_dwordx4 a[16:19], %[va], s[76:77] offset:0 sc1\n\t" "v_mfma_f32_16x16x32_bf16 v[148:151], a[156:159], a[84:87], v[148:151]\n\t"
    "v_mfma_f32_16x16x32_bf16 v[152:155], a[152:155], a[72:75], v[152:155]\n\t" "ds_read_b64_tr_b16 a[140:141], %[vr2] offset:50176\n\t"
    "v_mfma_f32_16x16x32_bf16 v[152:155], a[156:159], a[88:91], v[152:155]\n\t" "ds_read_b64_tr_b16 a[142:143], %[vr2] offset:53760\n\t"
    "v_mfma_f32_16x16x32_bf16 a[172:175], a[156:159], a[188:191], a[172:175]\n\t" "s_add_u32 m0, %[sl], 0\n\t" "v_mfma_f32_16x16x32_bf16 v[156:159], a[152:155], a[76:79], v[156:159]\n\t"
    "global_load_lds_dwordx4 %[vb0], s[78:79]\n\t" "v_mfma_f32_16x16x32_bf16 v[156:159], a[156:159], a[92:95], v[156:159]\n\t" "s_waitcnt lgkmcnt(4)\n\t"
    "v_mfma_f32_16x16x32_bf16 v[160:163], a[128:131], a[64:67], v[160:163]\n\t" "ds_read_b64_tr_b16 a[144:145], %[vr2] offset:35872\n\t"
    "v_mfma_f32_16x16x32_bf16 v[160:163], a[132:135], a[80:83], v[160:163]\n\t" "ds_read_b64_tr_b16 a[146:147], %[vr2] offset:39456\n\t"
    "v_mfma_f32_16x16x32_bf16 a[176:179], a[128:131], a[184:187], 0\n\t" "v_mfma_f32_16x16x32_bf16 v[164:167], a[128:131], a[68:71], v[164:167]\n\t"
    "v_mfma_f32_16x16x32_bf16 v[164:167], a[132:135], a[84:87], v[164:167]\n\t" "v_mfma_f32_16x16x32_bf16 v[168:171], a[128:131], a[72:75], v[168:171]\n\t"
    "ds_read_b64_tr_b16 a[148:149], %[vr2] offset:50208\n\t" "v_mfma_f32_16x16x32_bf16 v[168:171], a[132:135], a[88:91], v[168:171]\n\t" "ds_read_b64_tr_b16 a[150:151], %[vr2] offset:53792\n\t"
    "v_mfma_f32_16x16x32_bf16 a[176:179], a[132:135], a[188:191], a[176:179]\n\t" "v_mfma_f32_16x16x32_bf16 v[172:175], a[128:131], a[76:79], v[172:175]\n\t"
    "global_load_dwordx4 a[20:23], %[va], s[76:77] offset:1024 sc1\n\t" "v_mfma_f32_16x16x32_bf16 v[172:175], a[132:135], a[92:95], v[172:175]\n\t" "s_waitcnt lgkmcnt(4)\n\t"
    "v_mfma_f32_16x16x32_bf16 v[176:179], a[136:139], a[64:67], v[176:179]\n\t" "ds_read_b64_tr_b16 a[152:153], %[vr4] offset:35840\n\t"
    "v_mfma_f32_16x16x32_bf16 v[176:179], a[140:143], a[80:83], v[176:179]\n\t" "ds_read_b64_tr_b16 a[154:155], %[vr4] offset:39424\n\t" "s_add_u32 m0, %[sl], 1024\n\t"
    "v_mfma_f32_16x16x32_bf16 v[180:183], a[136:139], a[68:71], v[180:183]\n\t" "global_load_lds_dwordx4 %[vb1], s[78:79]\n\t" "v_mfma_f32_16x16x32_bf16 v[180:183], a[140:143], a[84:87], v[180:183]\n\t"
    "v_mfma_f32_16x16x32_bf16 v[184:187], a[136:139], a[72:75], v[184:187]\n\t" "ds_read_b64_tr_b16 a[156:157], %[vr4] offset:50176\n\t"
    "v_mfma_f32_16x16x32_bf16 v[184:187], a[140:143], a[88:91], v[184:187]\n\t" "ds_read_b64_tr_b16 a[158:159], %[vr4] offset:53760\n\t"
    "v_mfma_f32_16x16x32_bf16 v[188:191], a[136:139], a[76:79], v[188:191]\n\t" "v_mfma_f32_16x16x32_bf16 v[188:191], a[140:143], a[92:95], v[188:191]\n\t" "s_waitcnt lgkmcnt(4)\n\t"
    "v_mfma_f32_16x16x32_bf16 v[192:195], a[144:147], a[64:67], v[192:195]\n\t" "ds_read_b64_tr_b16 a[128:129], %[vr4] offset:35872\n\t"
    "v_mfma_f32_16x16x32_bf16 v[192:195], a[148:151], a[80:83], v[192:195]\n\t" "ds_read_b64_tr_b16 a[130:131], %[vr4] offset:39456\n\t"
    "v_mfma_f32_16x16x32_bf16 v[196:199], a[144:147], a[68:71], v[196:199]\n\t" "global_load_dwordx4 a[24:27], %[va], s[76:77] offset:2048 sc1\n\t"
    "v_mfma_f32_16x16x32_bf16 v[196:199], a[148:151], a[84:87], v[196:199]\n\t" "v_mfma_f32_16x16x32_bf16 v[200:203], a[144:147], a[72:75], v[200:203]\n\t"
    "ds_read_b64_tr_b16 a[132:133], %[vr4] offset:50208\n\t" "v_mfma_f32_16x16x32_bf16 v[200:203], a[148:151], a[88:91], v[200:203]\n\t" "ds_read_b64_tr_b16 a[134:135], %[vr4] offset:53792\n\t"
    "v_mfma_f32_16x16x32_bf16 v[204:207], a[144:147], a[76:79], v[204:207]\n\t" "v_mfma_f32_16x16x32_bf16 v[204:207], a[148:151], a[92:95], v[204:207]\n\t" "s_waitcnt lgkmcnt(4)\n\t"
    "v_mfma_f32_16x16x32_bf16 v[208:211], a[152:155], a[64:67], v[208:211]\n\t" "ds_read_b64_tr_b16 a[136:137], %[vr6] offset:35840\n\t"
    "v_mfma_f32_16x16x32_bf16 v[208:211], a[156:159], a[80:83], v[208:211]\n\t" "ds_read_b64_tr_b16 a[138:139], %[vr6] offset:39424\n\t" "s_add_u32 m0, %[sl], 2048\n\t"
    "v_mfma_f32_16x16x32_bf16 v[212:215], a[152:155], a[68:71], v[212:215]\n\t" "global_load_lds_dwordx4 %[vb2], s[78:79]\n\t" "v_mfma_f32_16x16x32_bf16 v[212:215], a[156:159], a[84:87], v[212:215]\n\t"
    "v_mfma_f32_16x16x32_bf16 v[216:219], a[152:155], a[72:75], v[216:219]\n\t" "ds_read_b64_tr_b16 a[140:141], %[vr6] offset:50176\n\t"
    "v_mfma_f32_16x16x32_bf16 v[216:219], a[156:159], a[88:91], v[216:219]\n\t" "ds_read_b64_tr_b16 a[142:143], %[vr6] offset:53760\n\t"
    "v_mfma_f32_16x16x32_bf16 v[220:223], a[152:155], a[76:79], v[220:223]\n\t" "global_load_dwordx4 a[28:31], %[va], s[76:77] offset:3072 sc1\n\t"
    "v_mfma_f32_16x16x32_bf16 v[220:223], a[156:159], a[92:95], v[220:223]\n\t" "s_add_u32 s76, s76, 16384\n\t" "s_addc_u32 s77, s77, 0\n\t" "s_waitcnt lgkmcnt(4)\n\t"
    "v_mfma_f32_16x16x32_bf16 v[224:227], a[128:131], a[64:67], v[224:227]\n\t" "s_waitcnt vmcnt(24)\n\t" "s_barrier\n\t" "ds_read_b64_tr_b16 a[144:145], %[vr1] offset:0\n\t"
    "v_mfma_f32_16x16x32_bf16 v[224:227], a[132:135], a[80:83], v[224:227]\n\t" "ds_read_b64_tr_b16 a[146:147], %[vr1] offset:3584\n\t"
    "v_mfma_f32_16x16x32_bf16 v[228:231], a[128:131], a[68:71], v[228:231]\n\t" "v_mfma_f32_16x16x32_bf16 v[228:231], a[132:135], a[84:87], v[228:231]\n\t"
    "v_mfma_f32_16x16x32_bf16 v[232:235], a[128:131], a[72:75], v[232:235]\n\t" "ds_read_b64_tr_b16 a[148:149], %[vr1] offset:14336\n\t"
    "v_mfma_f32_16x16x32_bf16 v[232:235], a[132:135], a[88:91], v[232:235]\n\t" "ds_read_b64_tr_b16 a[150:151], %[vr1] offset:17920\n\t" "s_add_u32 m0, %[sl], 3072\n\t"
    "v_mfma_f32_16x16x32_bf16 v[236:239], a[128:131], a[76:79], v[236:239]\n\t" "s_mov_b64 exec, s[82:83]\n\t" "global_load_lds_dwordx4 %[vb3], s[78:79]\n\t" "s_mov_b64 exec, -1\n\t"
    "v_mfma_f32_16x16x32_bf16 v[236:239], a[132:135], a[92:95], v[236:239]\n\t" "s_add_u32 s78, s78, 229376\n\t" "s_addc_u32 s79, s79, 0\n\t" "s_waitcnt lgkmcnt(4)\n\t"
    "v_mfma_f32_16x16x32_bf16 v[240:243], a[136:139], a[64:67], v[240:243]\n\t" "ds_read_b64_tr_b16 a[152:153], %[vr1] offset:32\n\t"
    "v_mfma_f32_16x16x32_bf16 v[240:243], a[140:143], a[80:83], v[240:243]\n\t" "ds_read_b64_tr_b16 a[154:155], %[vr1] offset:3616\n\t" "v_mfma_f32_16x16x32_bf16 a[180:183], a[136:139], a[184:187], 0\n\t"
    "v_mfma_f32_16x16x32_bf16 v[244:247], a[136:139], a[68:71], v[244:247]\n\t" "v_mfma_f32_16x16x32_bf16 v[244:247], a[140:143], a[84:87], v[244:247]\n\t"
    "v_mfma_f32_16x16x32_bf16 v[248:251], a[136:139], a[72:75], v[248:251]\n\t" "ds_read_b64_tr_b16 a[156:157], %[vr1] offset:14368\n\t"
    "v_mfma_f32_16x16x32_bf16 v[248:251], a[140:143], a[88:91], v[248:251]\n\t" "ds_read_b64_tr_b16 a[158:159], %[vr1] offset:17952\n\t"
    "v_mfma_f32_16x16x32_bf16 a[180:183], a[140:143], a[188:191], a[180:183]\n\t" "v_mfma_f32_16x16x32_bf16 v[252:255], a[136:139], a[76:79], v[252:255]\n\t"
    "v_mfma_f32_16x16x32_bf16 v[252:255], a[140:143], a[92:95], v[252:255]\n\t" "s_waitcnt lgkmcnt(4) vmcnt(18)\n\t" "v_mfma_f32_16x16x32_bf16 v[32:35], a[144:147], a[96:99], v[32:35]\n\t" "s_barrier\n\t"
    "ds_read_b64_tr_b16 a[128:129], %[vr3] offset:0\n\t" "v_mfma_f32_16x16x32_bf16 v[32:35], a[148:151], a[112:115], v[32:35]\n\t" "ds_read_b64_tr_b16 a[130:131], %[vr3] offset:3584\n\t"
    "v_mfma_f32_16x16x32_bf16 a[160:163], a[144:147], a[192:195], a[160:163]\n\t" "v_mfma_f32_16x16x32_bf16 v[36:39], a[144:147], a[100:103], v[36:39]\n\t"
    "global_load_dwordx4 a[32:35], %[va], s[76:77] offset:0 sc1\n\t" "v_mfma_f32_16x16x32_bf16 v[36:39], a[148:151], a[116:119], v[36:39]\n\t"
    "v_mfma_f32_16x16x32_bf16 v[40:43], a[144:147], a[104:107], v[40:43]\n\t" "ds_read_b64_tr_b16 a[132:133], %[vr3] offset:14336\n\t"
    "v_mfma_f32_16x16x32_bf16 v[40:43], a[148:151], a[120:123], v[40:43]\n\t" "ds_read_b64_tr_b16 a[134:135], %[vr3] offset:17920\n\t"
    "v_mfma_f32_16x16x32_bf16 a[160:163], a[148:151], a[196:199], a[160:163]\n\t" "s_add_u32 m0, %[sl], 14336\n\t" "v_mfma_f32_16x16x32_bf16 v[44:47], a[144:147], a[108:111], v[44:47]\n\t"
    "global_load_lds_dwordx4 %[vb0], s[78:79]\n\t" "v_mfma_f32_16x16x32_bf16 v[44:47], a[148:151], a[124:127], v[44:47]\n\t" "s_waitcnt lgkmcnt(4)\n\t"
    "v_mfma_f32_16x16x32_bf16 v[48:51], a[152:155], a[96:99], v[48:51]\n\t" "ds_read_b64_tr_b16 a[136:137], %[vr3] offset:32\n\t" "v_mfma_f32_16x16x32_bf16 v[48:51], a[156:159], a[112:115], v[48:51]\n\t"
    "ds_read_b64_tr_b16 a[138:139], %[vr3] offset:3616\n\t" "v_mfma_f32_16x16x32_bf16 a[164:167], a[152:155], a[192:195], a[164:167]\n\t"
    "v_mfma_f32_16x16x32_bf16 v[52:55], a[152:155], a[100:103], v[52:55]\n\t" "v_mfma_f32_16x16x32_bf16 v[52:55], a[156:159], a[116:119], v[52:55]\n\t"
    "v_mfma_f32_16x16x32_bf16 v[56:59], a[152:155], a[104:107], v[56:59]\n\t" "ds_read_b64_tr_b16 a[140:141], %[vr3] offset:14368\n\t"
    "v_mfma_f32_16x16x32_bf16 v[56:59], a[156:159], a[120:123], v[56:59]\n\t" "ds_read_b64_tr_b16 a[142:143], %[vr3] offset:17952\n\t"
    "v_mfma_f32_16x16x32_bf16 a[164:167], a[156:159], a[196:199], a[164:167]\n\t" "v_mfma_f32_16x16x32_bf16 v[60:63], a[152:155], a[108:111], v[60:63]\n\t"
    "global_load_dwordx4 a[36:39], %[va], s[76:77] offset:1024 sc1\n\t" "v_mfma_f32_16x16x32_bf16 v[60:63], a[156:159], a[124:127], v[60:63]\n\t" "s_waitcnt lgkmcnt(4)\n\t"
    "v_mfma_f32_16x16x32_bf16 v[64:67], a[128:131], a[96:99], v[64:67]\n\t" "ds_read_b64_tr_b16 a[144:145], %[vr5] offset:0\n\t" "v_mfma_f32_16x16x32_bf16 v[64:67], a[132:135], a[112:115], v[64:67]\n\t"
    "ds_read_b64_tr_b16 a[146:147], %[vr5] offset:3584\n\t" "s_add_u32 m0, %[sl], 15360\n\t" "v_mfma_f32_16x16x32_bf16 v[68:71], a[128:131], a[100:103], v[68:71]\n\t"
    "global_load_lds_dwordx4 %[vb1], s[78:79]\n\t" "v_mfma_f32_16x16x32_bf16 v[68:71], a[132:135], a[116:119], v[68:71]\n\t" "v_mfma_f32_16x16x32_bf16 v[72:75], a[128:131], a[104:107], v[72:75]\n\t"
    "ds_read_b64_tr_b16 a[148:149], %[vr5] offset:14336\n\t" "v_mfma_f32_16x16x32_bf16 v[72:75], a[132:135], a[120:123], v[72:75]\n\t" "ds_read_b64_tr_b16 a[150:151], %[vr5] offset:17920\n\t"
    "v_mfma_f32_16x16x32_bf16 v[76:79], a[128:131], a[108:111], v[76:79]\n\t" "v_mfma_f32_16x16x32_bf16 v[76:79], a[132:135], a[124:127], v[76:79]\n\t" "s_waitcnt lgkmcnt(4)\n\t"
    "v_mfma_f32_16x16x32_bf16 v[80:83], a[136:139], a[96:99], v[80:83]\n\t" "ds_read_b64_tr_b16 a[152:153], %[vr5] offset:32\n\t" "v_mfma_f32_16x16x32_bf16 v[80:83], a[140:143], a[112:115], v[80:83]\n\t"
    "ds_read_b64_tr_b16 a[154:155], %[vr5] offset:3616\n\t" "v_mfma_f32_16x16x32_bf16 v[84:87], a[136:139], a[100:103], v[84:87]\n\t" "global_load_dwordx4 a[40:43], %[va], s[76:77] offset:2048 sc1\n\t"
    "v_mfma_f32_16x16x32_bf16 v[84:87], a[140:143], a[116:119], v[84:87]\n\t" "v_mfma_f32_16x16x32_bf16 v[88:91], a[136:139], a[104:107], v[88:91]\n\t"
    "ds_read_b64_tr_b16 a[156:157], %[vr5] offset:14368\n\t" "v_mfma_f32_16x16x32_bf16 v[88:91], a[140:143], a[120:123], v[88:91]\n\t" "ds_read_b64_tr_b16 a[158:159], %[vr5] offset:17952\n\t"
    "v_mfma_f32_16x16x32_bf16 v[92:95], a[136:139], a[108:111], v[92:95]\n\t" "v_mfma_f32_16x16x32_bf16 v[92:95], a[140:143], a[124:127], v[92:95]\n\t" "s_waitcnt lgkmcnt(4)\n\t"
    "v_mfma_f32_16x16x32_bf16 v[96:99], a[144:147], a[96:99], v[96:99]\n\t" "ds_read_b64_tr_b16 a[128:129], %[vr7] offset:0\n\t" "v_mfma_f32_16x16x32_bf16 v[96:99], a[148:151], a[112:115], v[96:99]\n\t"
    "ds_read_b64_tr_b16 a[130:131], %[vr7] offset:3584\n\t" "s_add_u32 m0, %[sl], 16384\n\t" "v_mfma_f32_16x16x32_bf16 v[100:103], a[144:147], a[100:103], v[100:103]\n\t"
    "global_load_lds_dwordx4 %[vb2], s[78:79]\n\t" "v_mfma_f32_16x16x32_bf16 v[100:103], a[148:151], a[116:119], v[100:103]\n\t"
    "v_mfma_f32_16x16x32_bf16 v[104:107], a[144:147], a[104:107], v[104:107]\n\t" "ds_read_b64_tr_b16 a[132:133], %[vr7] offset:14336\n\t"
    "v_mfma_f32_16x16x32_bf16 v[104:107], a[148:151], a[120:123], v[104:107]\n\t" "ds_read_b64_tr_b16 a[134:135], %[vr7] offset:17920\n\t"
    "v_mfma_f32_16x16x32_bf16 v[108:111], a[144:147], a[108:111], v[108:111]\n\t" "global_load_dwordx4 a[44:47], %[va], s[76:77] offset:3072 sc1\n\t"
    "v_mfma_f32_16x16x32_bf16 v[108:111], a[148:151], a[124:127], v[108:111]\n\t" "s_add_u32 s76, s76, 16384\n\t" "s_addc_u32 s77, s77, 0\n\t" "s_waitcnt lgkmcnt(4)\n\t"
    "v_mfma_f32_16x16x32_bf16 v[112:115], a[152:155], a[96:99], v[112:115]\n\t" "ds_read_b64_tr_b16 a[136:137], %[vr1] offset:7168\n\t"
    "v_mfma_f32_16x16x32_bf16 v[112:115], a[156:159], a[112:115], v[112:115]\n\t" "ds_read_b64_tr_b16 a[138:139], %[vr1] offset:10752\n\t"
    "v_mfma_f32_16x16x32_bf16 v[116:119], a[152:155], a[100:103], v[116:119]\n\t" "v_mfma_f32_16x16x32_bf16 v[116:119], a[156:159], a[116:119], v[116:119]\n\t"
    "v_mfma_f32_16x16x32_bf16 v[120:123], a[152:155], a[104:107], v[120:123]\n\t" "ds_read_b64_tr_b16 a[140:141], %[vr1] offset:21504\n\t"
    "v_mfma_f32_16x16x32_bf16 v[120:123], a[156:159], a[120:123], v[120:123]\n\t" "ds_read_b64_tr_b16 a[142:143], %[vr1] offset:25088\n\t" "s_add_u32 m0, %[sl], 17408\n\t"
    "v_mfma_f32_16x16x32_bf16 v[124:127], a[152:155], a[108:111], v[124:127]\n\t" "s_mov_b64 exec, s[82:83]\n\t" "global_load_lds_dwordx4 %[vb3], s[78:79]\n\t" "s_mov_b64 exec, -1\n\t"
    "v_mfma_f32_16x16x32_bf16 v[124:127], a[156:159], a[124:127], v[124:127]\n\t" "s_add_u32 s78, s78, 229376\n\t" "s_addc_u32 s79, s79, 0\n\t" "s_waitcnt lgkmcnt(4)\n\t"
    "v_mfma_f32_16x16x32_bf16 v[128:131], a[128:131], a[96:99], v[128:131]\n\t" "ds_read_b64_tr_b16 a[144:145], %[vr1] offset:7200\n\t"
    "v_mfma_f32_16x16x32_bf16 v[128:131], a[132:135], a[112:115], v[128:131]\n\t" "ds_read_b64_tr_b16 a[146:147], %[vr1] offset:10784\n\t"
    "v_mfma_f32_16x16x32_bf16 a[168:171], a[128:131], a[192:195], a[168:171]\n\t" "v_mfma_f32_16x16x32_bf16 v[132:135], a[128:131], a[100:103], v[132:135]\n\t"
    "v_mfma_f32_16x16x32_bf16 v[132:135], a[132:135], a[116:119], v[132:135]\n\t" "v_mfma_f32_16x16x32_bf16 v[136:139], a[128:131], a[104:107], v[136:139]\n\t"
    "ds_read_b64_tr_b16 a[148:149], %[vr1] offset:21536\n\t" "v_mfma_f32_16x16x32_bf16 v[136:139], a[132:135], a[120:123], v[136:139]\n\t" "ds_read_b64_tr_b16 a[150:151], %[vr1] offset:25120\n\t"
    "v_mfma_f32_16x16x32_bf16 a[168:171], a[132:135], a[196:199], a[168:171]\n\t" "v_mfma_f32_16x16x32_bf16 v[140:143], a[128:131], a[108:111], v[140:143]\n\t"
    "v_mfma_f32_16x16x32_bf16 v[140:143], a[132:135], a[124:127], v[140:143]\n\t" "s_waitcnt lgkmcnt(4)\n\t" "v_mfma_f32_16x16x32_bf16 v[144:147], a[136:139], a[96:99], v[144:147]\n\t"
    "ds_read_b64_tr_b16 a[152:153], %[vr3] offset:7168\n\t" "v_mfma_f32_16x16x32_bf16 v[144:147], a[140:143], a[112:115], v[144:147]\n\t" "ds_read_b64_tr_b16 a[154:155], %[vr3] offset:10752\n\t"
    "v_mfma_f32_16x16x32_bf16 a[172:175], a[136:139], a[192:195], a[172:175]\n\t" "v_mfma_f32_16x16x32_bf16 v[148:151], a[136:139], a[100:103], v[148:151]\n\t"
    "global_load_dwordx4 a[48:51], %[va], s[76:77] offset:0 sc1\n\t" "v_mfma_f32_16x16x32_bf16 v[148:151], a[140:143], a[116:119], v[148:151]\n\t"
    "v_mfma_f32_16x16x32_bf16 v[152:155], a[136:139], a[104:107], v[152:155]\n\t" "ds_read_b64_tr_b16 a[156:157], %[vr3] offset:21504\n\t"
    "v_mfma_f32_16x16x32_bf16 v[152:155], a[140:143], a[120:123], v[152:155]\n\t" "ds_read_b64_tr_b16 a[158:159], %[vr3] offset:25088\n\t"
    "v_mfma_f32_16x16x32_bf16 a[172:175], a[140:143], a[196:199], a[172:175]\n\t" "s_add_u32 m0, %[sl], 28672\n\t" "v_mfma_f32_16x16x32_bf16 v[156:159], a[136:139], a[108:111], v[156:159]\n\t"
    "global_load_lds_dwordx4 %[vb0], s[78:79]\n\t" "v_mfma_f32_16x16x32_bf16 v[156:159], a[140:143], a[124:127], v[156:159]\n\t" "s_waitcnt lgkmcnt(4)\n\t"
    "v_mfma_f32_16x16x32_bf16 v[160:163], a[144:147], a[96:99], v[160:163]\n\t" "ds_read_b64_tr_b16 a[128:129], %[vr3] offset:7200\n\t"
    "v_mfma_f32_16x16x32_bf16 v[160:163], a[148:151], a[112:115], v[160:163]\n\t" "ds_read_b64_tr_b16 a[130:131], %[vr3] offset:10784\n\t"
    "v_mfma_f32_16x16x32_bf16 a[176:179], a[144:147], a[192:195], a[176:179]\n\t" "v_mfma_f32_16x16x32_bf16 v[164:167], a[144:147], a[100:103], v[164:167]\n\t"
    "v_mfma_f32_16x16x32_bf16 v[164:167], a[148:151], a[116:119], v[164:167]\n\t" "v_mfma_f32_16x16x32_bf16 v[168:171], a[144:147], a[104:107], v[168:171]\n\t"
    "ds_read_b64_tr_b16 a[132:133], %[vr3] offset:21536\n\t" "v_mfma_f32_16x16x32_bf16 v[168:171], a[148:151], a[120:123], v[168:171]\n\t" "ds_read_b64_tr_b16 a[134:135], %[vr3] offset:25120\n\t"
    "v_mfma_f32_16x16x32_bf16 a[176:179], a[148:151], a[196:199], a[176:179]\n\t" "v_mfma_f32_16x16x32_bf16 v[172:175], a[144:147], a[108:111], v[172:175]\n\t"
    "global_load_dwordx4 a[52:55], %[va], s[76:77] offset:1024 sc1\n\t" "v_mfma_f32_16x16x32_bf16 v[172:175], a[148:151], a[124:127], v[172:175]\n\t" "s_waitcnt lgkmcnt(4)\n\t"
    "v_mfma_f32_16x16x32_bf16 v[176:179], a[152:155], a[96:99], v[176:179]\n\t" "ds_read_b64_tr_b16 a[136:137], %[vr5] offset:7168\n\t"
    "v_mfma_f32_16x16x32_bf16 v[176:179], a[156:159], a[112:115], v[176:179]\n\t" "ds_read_b64_tr_b16 a[138:139], %[vr5] offset:10752\n\t" "s_add_u32 m0, %[sl], 29696\n\t"
    "v_mfma_f32_16x16x32_bf16 v[180:183], a[152:155], a[100:103], v[180:183]\n\t" "global_load_lds_dwordx4 %[vb1], s[78:79]\n\t"
    "v_mfma_f32_16x16x32_bf16 v[180:183], a[156:159], a[116:119], v[180:183]\n\t" "v_mfma_f32_16x16x32_bf16 v[184:187], a[152:155], a[104:107], v[184:187]\n\t"
    "ds_read_b64_tr_b16 a[140:141], %[vr5] offset:21504\n\t" "v_mfma_f32_16x16x32_bf16 v[184:187], a[156:159], a[120:123], v[184:187]\n\t" "ds_read_b64_tr_b16 a[142:143], %[vr5] offset:25088\n\t"
    "v_mfma_f32_16x16x32_bf16 v[188:191], a[152:155], a[108:111], v[188:191]\n\t" "v_mfma_f32_16x16x32_bf16 v[188:191], a[156:159], a[124:127], v[188:191]\n\t" "s_waitcnt lgkmcnt(4)\n\t"
    "v_mfma_f32_16x16x32_bf16 v[192:195], a[128:131], a[96:99], v[192:195]\n\t" "ds_read_b64_tr_b16 a[144:145], %[vr5] offset:7200\n\t"
    "v_mfma_f32_16x16x32_bf16 v[192:195], a[132:135], a[112:115], v[192:195]\n\t" "ds_read_b64_tr_b16 a[146:147], %[vr5] offset:10784\n\t"
    "v_mfma_f32_16x16x32_bf16 v[196:199], a[128:131], a[100:103], v[196:199]\n\t" "global_load_dwordx4 a[56:59], %[va], s[76:77] offset:2048 sc1\n\t"
    "v_mfma_f32_16x16x32_bf16 v[196:199], a[132:135], a[116:119], v[196:199]\n\t" "v_mfma_f32_16x16x32_bf16 v[200:203], a[128:131], a[104:107], v[200:203]\n\t"
    "ds_read_b64_tr_b16 a[148:149], %[vr5] offset:21536\n\t" "v_mfma_f32_16x16x32_bf16 v[200:203], a[132:135], a[120:123], v[200:203]\n\t" "ds_read_b64_tr_b16 a[150:151], %[vr5] offset:25120\n\t"
    "v_mfma_f32_16x16x32_bf16 v[204:207], a[128:131], a[108:111], v[204:207]\n\t" "v_mfma_f32_16x16x32_bf16 v[204:207], a[132:135], a[124:127], v[204:207]\n\t" "s_waitcnt lgkmcnt(4)\n\t"
    "v_mfma_f32_16x16x32_bf16 v[208:211], a[136:139], a[96:99], v[208:211]\n\t" "ds_read_b64_tr_b16 a[152:153], %[vr7] offset:7168\n\t"
    "v_mfma_f32_16x16x32_bf16 v[208:211], a[140:143], a[112:115], v[208:211]\n\t" "ds_read_b64_tr_b16 a[154:155], %[vr7] offset:10752\n\t" "s_add_u32 m0, %[sl], 30720\n\t"
    "v_mfma_f32_16x16x32_bf16 v[212:215], a[136:139], a[100:103], v[212:215]\n\t" "global_load_lds_dwordx4 %[vb2], s[78:79]\n\t"
    "v_mfma_f32_16x16x32_bf16 v[212:215], a[140:143], a[116:119], v[212:215]\n\t" "v_mfma_f32_16x16x32_bf16 v[216:219], a[136:139], a[104:107], v[216:219]\n\t"
    "ds_read_b64_tr_b16 a[156:157], %[vr7] offset:21504\n\t" "v_mfma_f32_16x16x32_bf16 v[216:219], a[140:143], a[120:123], v[216:219]\n\t" "ds_read_b64_tr_b16 a[158:159], %[vr7] offset:25088\n\t"
    "v_mfma_f32_16x16x32_bf16 v[220:223], a[136:139], a[108:111], v[220:223]\n\t" "global_load_dwordx4 a[60:63], %[va], s[76:77] offset:3072 sc1\n\t"
    "v_mfma_f32_16x16x32_bf16 v[220:223], a[140:143], a[124:127], v[220:223]\n\t" "s_add_u32 s76, s76, 16384\n\t" "s_addc_u32 s77, s77, 0\n\t" "s_waitcnt lgkmcnt(4)\n\t"
    "v_mfma_f32_16x16x32_bf16 v[224:227], a[144:147], a[96:99], v[224:227]\n\t" "s_waitcnt vmcnt(24)\n\t" "s_barrier\n\t" "ds_read_b64_tr_b16 a[128:129], %[vr1] offset:28672\n\t"
    "v_mfma_f32_16x16x32_bf16 v[224:227], a[148:151], a[112:115], v[224:227]\n\t" "ds_read_b64_tr_b16 a[130:131], %[vr1] offset:32256\n\t"
    "v_mfma_f32_16x16x32_bf16 v[228:231], a[144:147], a[100:103], v[228:231]\n\t" "v_mfma_f32_16x16x32_bf16 v[228:231], a[148:151], a[116:119], v[228:231]\n\t"
    "v_mfma_f32_16x16x32_bf16 v[232:235], a[144:147], a[104:107], v[232:235]\n\t" "ds_read_b64_tr_b16 a[132:133], %[vr1] offset:43008\n\t"
    "v_mfma_f32_16x16x32_bf16 v[232:235], a[148:151], a[120:123], v[232:235]\n\t" "ds_read_b64_tr_b16 a[134:135], %[vr1] offset:46592\n\t" "s_add_u32 m0, %[sl], 31744\n\t"
    "v_mfma_f32_16x16x32_bf16 v[236:239], a[144:147], a[108:111], v[236:239]\n\t" "s_mov_b64 exec, s[82:83]\n\t" "global_load_lds_dwordx4 %[vb3], s[78:79]\n\t" "s_mov_b64 exec, -1\n\t"
    "v_mfma_f32_16x16x32_bf16 v[236:239], a[148:151], a[124:127], v[236:239]\n\t" "s_add_u32 s78, s78, 229376\n\t" "s_addc_u32 s79, s79, 0\n\t" "s_waitcnt lgkmcnt(4)\n\t"
    "v_mfma_f32_16x16x32_bf16 v[240:243], a[152:155], a[96:99], v[240:243]\n\t" "ds_read_b64_tr_b16 a[136:137], %[vr1] offset:28704\n\t"
    "v_mfma_f32_16x16x32_bf16 v[240:243], a[156:159], a[112:115], v[240:243]\n\t" "ds_read_b64_tr_b16 a[138:139], %[vr1] offset:32288\n\t"
    "v_mfma_f32_16x16x32_bf16 a[180:183], a[152:155], a[192:195], a[180:183]\n\t" "v_mfma_f32_16x16x32_bf16 v[244:247], a[152:155], a[100:103], v[244:247]\n\t"
    "v_mfma_f32_16x16x32_bf16 v[244:247], a[156:159], a[116:119], v[244:247]\n\t" "v_mfma_f32_16x16x32_bf16 v[248:251], a[152:155], a[104:107], v[248:251]\n\t"
    "ds_read_b64_tr_b16 a[140:141], %[vr1] offset:43040\n\t" "v_mfma_f32_16x16x32_bf16 v[248:251], a[156:159], a[120:123], v[248:251]\n\t" "ds_read_b64_tr_b16 a[142:143], %[vr1] offset:46624\n\t"
    "v_mfma_f32_16x16x32_bf16 a[180:183], a[156:159], a[196:199], a[180:183]\n\t" "v_mfma_f32_16x16x32_bf16 v[252:255], a[152:155], a[108:111], v[252:255]\n\t"
    "v_mfma_f32_16x16x32_bf16 v[252:255], a[156:159], a[124:127], v[252:255]\n\t" "v_accvgpr_read_b32 v31, %[cp]\n\t" "s_nop 7\n\t" "s_mov_b64 exec, %[m01]\n\t"
    "global_store_dwordx4 v31, a[160:163], %[su0] offset:0 sc1\n\t" "global_store_dwordx4 v31, a[164:167], %[su0] offset:2304 sc1\n\t" "global_store_dwordx4 v31, a[172:175], %[su0] offset:1152 sc1\n\t"
    "global_store_dwordx4 v31, a[176:179], %[su0] offset:3456 sc1\n\t" "s_mov_b64 exec, %[m6]\n\t" "global_store_dwordx4 v31, a[168:171], %[su6] offset:0 sc1\n\t"
    "global_store_dwordx4 v31, a[180:183], %[su6] offset:1152 sc1\n\t" "s_mov_b64 exec, -1\n\t" "s_branch .Ld1_%=\n\t" ".Lslow0_%=:\n\t" "s_sleep 2\n\t" "v_accvgpr_read_b32 v31, %[cr0]\n\t"
    "s_mov_b64 s[96:97], 0\n\t" "v_subrev_u32_e64 v31, %[rb], v31\n\t" "global_load_dword v24, v31, %[su] offset:0 sc1\n\t" "global_load_dword v25, v31, %[su] offset:16 sc1\n\t"
    "global_load_dword v26, v31, %[su] offset:32 sc1\n\t" "global_load_dword v27, v31, %[su] offset:48 sc1\n\t" "global_load_dword v28, v31, %[su] offset:64 sc1\n\t"
    "global_load_dword v29, v31, %[su] offset:80 sc1\n\t" "global_load_dword v30, v31, %[su] offset:96 sc1\n\t" "global_load_dword v31, v31, %[su] offset:112 sc1\n\t" "s_waitcnt vmcnt(0)\n\t"
    "v_cmp_eq_u32 vcc, -1, v24\n\t" "s_nop 1\n\t" "s_or_b64 s[96:97], s[96:97], vcc\n\t" "v_cmp_eq_u32 vcc, -1, v25\n\t" "s_nop 1\n\t" "s_or_b64 s[96:97], s[96:97], vcc\n\t"
    "v_cmp_eq_u32 vcc, -1, v26\n\t" "s_nop 1\n\t" "s_or_b64 s[96:97], s[96:97], vcc\n\t" "v_cmp_eq_u32 vcc, -1, v27\n\t" "s_nop 1\n\t" "s_or_b64 s[96:97], s[96:97], vcc\n\t"
    "v_cmp_eq_u32 vcc, -1, v28\n\t" "s_nop 1\n\t" "s_or_b64 s[96:97], s[96:97], vcc\n\t" "v_cmp_eq_u32 vcc, -1, v29\n\t" "s_nop 1\n\t" "s_or_b64 s[96:97], s[96:97], vcc\n\t"
    "v_cmp_eq_u32 vcc, -1, v30\n\t" "s_nop 1\n\t" "s_or_b64 s[96:97], s[96:97], vcc\n\t" "v_cmp_eq_u32 vcc, -1, v31\n\t" "s_nop 1\n\t" "s_or_b64 s[96:97], s[96:97], vcc\n\t" "v_add_f32 v24, v24, v25\n\t"
    "v_add_f32 v26, v26, v27\n\t" "v_add_f32 v28, v28, v29\n\t" "v_add_f32 v30, v30, v31\n\t" "v_add_f32 v24, v24, v26\n\t" "v_add_f32 v28, v28, v30\n\t" "v_add_f32_e64 v24, v24, v28\n\t"
    "s_cbranch_scc0 .Lgo0_%=\n\t" "s_sub_u32 s94, s94, 1\n\t" "s_cbranch_scc0 .Lslow0_%=\n\t" "s_branch .Lgo0_%=\n\t" ".Lslow1_%=:\n\t" "s_sleep 2\n\t" "v_accvgpr_read_b32 v31, %[cr1]\n\t"
    "s_mov_b64 s[96:97], 0\n\t" "v_subrev_u32 v31, %[rb], v31\n\t" "global_load_dword v24, v31, %[su] offset:0 sc1\n\t" "global_load_dword v25, v31, %[su] offset:16 sc1\n\t"
    "global_load_dword v26, v31, %[su] offset:32 sc1\n\t" "global_load_dword v27, v31, %[su] offset:48 sc1\n\t" "global_load_dword v28, v31, %[su] offset:64 sc1\n\t"
    "global_load_dword v29, v31, %[su] offset:80 sc1\n\t" "global_load_dword v30, v31, %[su] offset:96 sc1\n\t" "global_load_dword v31, v31, %[su] offset:112 sc1\n\t" "s_waitcnt vmcnt(0)\n\t"
    "v_cmp_eq_u32 vcc, -1, v24\n\t" "s_nop 1\n\t" "s_or_b64 s[96:97], s[96:97], vcc\n\t" "v_cmp_eq_u32 vcc, -1, v25\n\t" "s_nop 1\n\t" "s_or_b64 s[96:97], s[96:97], vcc\n\t"
    "v_cmp_eq_u32 vcc, -1, v26\n\t" "s_nop 1\n\t" "s_or_b64 s[96:97], s[96:97], vcc\n\t" "v_cmp_eq_u32 vcc, -1, v27\n\t" "s_nop 1\n\t" "s_or_b64 s[96:97], s[96:97], vcc\n\t"
    "v_cmp_eq_u32 vcc, -1, v28\n\t" "s_nop 1\n\t" "s_or_b64 s[96:97], s[96:97], vcc\n\t" "v_cmp_eq_u32 vcc, -1, v29\n\t" "s_nop 1\n\t" "s_or_b64 s[96:97], s[96:97], vcc\n\t"
    "v_cmp_eq_u32 vcc, -1, v30\n\t" "s_nop 1\n\t" "s_or_b64 s[96:97], s[96:97], vcc\n\t" "v_cmp_eq_u32 vcc, -1, v31\n\t" "s_nop 1\n\t" "s_or_b64 s[96:97], s[96:97], vcc\n\t" "v_add_f32 v24, v24, v25\n\t"
    "v_add_f32 v26, v26, v27\n\t" "v_add_f32 v28, v28, v29\n\t" "v_add_f32 v30, v30, v31\n\t" "v_add_f32 v24, v24, v26\n\t" "v_add_f32 v28, v28, v30\n\t" "v_add_f32_e64 v24, v24, v28\n\t"
    "s_cbranch_scc0 .Lgo1_%=\n\t" "s_sub_u32 s94, s94, 1\n\t" "s_cbranch_scc0 .Lslow1_%=\n\t" "s_branch .Lgo1_%=\n\t" ".Ltail_%=:\n\t" "s_cmp_eq_u32 s80, 1\n\t" "s_cselect_b32 s86, %[wa], 16384\n\t"
    "s_ashr_i32 s87, s86, 31\n\t" "s_cmp_eq_u32 s80, 1\n\t" "s_cselect_b32 s88, %[wb], 229376\n\t" "s_ashr_i32 s89, s88, 31\n\t" "s_waitcnt lgkmcnt(4) vmcnt(17)\n\t"
    "v_mfma_f32_16x16x32_bf16 v[32:35], a[128:131], a[64:67], v[32:35]\n\t" "s_barrier\n\t" "ds_read_b64_tr_b16 a[144:145], %[vr2] offset:28672\n\t" "v_accvgpr_write_b32 a200, 0\n\t"
    "v_mfma_f32_16x16x32_bf16 v[32:35], a[132:135], a[80:83], v[32:35]\n\t" "ds_read_b64_tr_b16 a[146:147], %[vr2] offset:32256\n\t" "v_accvgpr_write_b32 a201, 0\n\t"
    "v_mfma_f32_16x16x32_bf16 v[36:39], a[128:131], a[68:71], v[36:39]\n\t" "global_load_dwordx4 a[0:3], %[va], s[76:77] offset:0 sc1\n\t" "v_accvgpr_write_b32 a202, 0\n\t"
    "v_mfma_f32_16x16x32_bf16 v[36:39], a[132:135], a[84:87], v[36:39]\n\t" "v_accvgpr_write_b32 a203, 0\n\t" "v_mfma_f32_16x16x32_bf16 v[40:43], a[128:131], a[72:75], v[40:43]\n\t"
    "ds_read_b64_tr_b16 a[148:149], %[vr2] offset:43008\n\t" "v_accvgpr_write_b32 a204, 0\n\t" "v_mfma_f32_16x16x32_bf16 v[40:43], a[132:135], a[88:91], v[40:43]\n\t"
    "ds_read_b64_tr_b16 a[150:151], %[vr2] offset:46592\n\t" "v_accvgpr_write_b32 a205, 0\n\t" "s_add_u32 m0, %[sl], 100352\n\t" "v_mfma_f32_16x16x32_bf16 v[44:47], a[128:131], a[76:79], v[44:47]\n\t"
    "global_load_lds_dwordx4 %[vb0], s[78:79]\n\t" "v_accvgpr_write_b32 a206, 0\n\t" "v_mfma_f32_16x16x32_bf16 v[44:47], a[132:135], a[92:95], v[44:47]\n\t" "v_accvgpr_write_b32 a207, 0\n\t"
    "s_waitcnt lgkmcnt(4)\n\t" "v_mfma_f32_16x16x32_bf16 v[48:51], a[136:139], a[64:67], v[48:51]\n\t" "ds_read_b64_tr_b16 a[152:153], %[vr2] offset:28704\n\t" "v_accvgpr_write_b32 a208, 0\n\t"
    "v_mfma_f32_16x16x32_bf16 v[48:51], a[140:143], a[80:83], v[48:51]\n\t" "ds_read_b64_tr_b16 a[154:155], %[vr2] offset:32288\n\t" "v_accvgpr_write_b32 a209, 0\n\t"
    "v_mfma_f32_16x16x32_bf16 v[52:55], a[136:139], a[68:71], v[52:55]\n\t" "global_load_dwordx4 a[4:7], %[va], s[76:77] offset:1024 sc1\n\t" "v_accvgpr_write_b32 a210, 0\n\t"
    "v_mfma_f32_16x16x32_bf16 v[52:55], a[140:143], a[84:87], v[52:55]\n\t" "v_accvgpr_write_b32 a211, 0\n\t" "v_mfma_f32_16x16x32_bf16 v[56:59], a[136:139], a[72:75], v[56:59]\n\t"
    "ds_read_b64_tr_b16 a[156:157], %[vr2] offset:43040\n\t" "v_accvgpr_write_b32 a212, 0\n\t" "v_mfma_f32_16x16x32_bf16 v[56:59], a[140:143], a[88:91], v[56:59]\n\t"
    "ds_read_b64_tr_b16 a[158:159], %[vr2] offset:46624\n\t" "v_accvgpr_write_b32 a213, 0\n\t" "s_add_u32 m0, %[sl], 101376\n\t" "v_mfma_f32_16x16x32_bf16 v[60:63], a[136:139], a[76:79], v[60:63]\n\t"
    "global_load_lds_dwordx4 %[vb1], s[78:79]\n\t" "v_accvgpr_write_b32 a214, 0\n\t" "v_mfma_f32_16x16x32_bf16 v[60:63], a[140:143], a[92:95], v[60:63]\n\t" "v_accvgpr_write_b32 a215, 0\n\t"
    "s_waitcnt lgkmcnt(4)\n\t" "v_mfma_f32_16x16x32_bf16 v[64:67], a[144:147], a[64:67], v[64:67]\n\t" "ds_read_b64_tr_b16 a[128:129], %[vr4] offset:28672\n\t" "v_accvgpr_read_b32 v24, a216\n\t"
    "v_mfma_f32_16x16x32_bf16 v[64:67], a[148:151], a[80:83], v[64:67]\n\t" "ds_read_b64_tr_b16 a[130:131], %[vr4] offset:32256\n\t" "v_accvgpr_read_b32 v25, a217\n\t"
    "v_mfma_f32_16x16x32_bf16 v[68:71], a[144:147], a[68:71], v[68:71]\n\t" "v_accvgpr_read_b32 v28, a218\n\t" "v_mfma_f32_16x16x32_bf16 v[68:71], a[148:151], a[84:87], v[68:71]\n\t"
    "v_accvgpr_read_b32 v29, a219\n\t" "v_mfma_f32_16x16x32_bf16 v[72:75], a[144:147], a[72:75], v[72:75]\n\t" "ds_read_b64_tr_b16 a[132:133], %[vr4] offset:43008\n\t" "v_accvgpr_read_b32 v26, a224\n\t"
    "v_mfma_f32_16x16x32_bf16 v[72:75], a[148:151], a[88:91], v[72:75]\n\t" "ds_read_b64_tr_b16 a[134:135], %[vr4] offset:46592\n\t" "v_accvgpr_read_b32 v27, a225\n\t"
    "v_mfma_f32_16x16x32_bf16 v[76:79], a[144:147], a[76:79], v[76:79]\n\t" "global_load_dwordx4 a[8:11], %[va], s[76:77] offset:2048 sc1\n\t" "v_accvgpr_read_b32 v30, a226\n\t"
    "v_mfma_f32_16x16x32_bf16 v[76:79], a[148:151], a[92:95], v[76:79]\n\t" "v_accvgpr_read_b32 v31, a227\n\t" "s_waitcnt lgkmcnt(4)\n\t"
    "v_mfma_f32_16x16x32_bf16 v[80:83], a[152:155], a[64:67], v[80:83]\n\t" "ds_read_b64_tr_b16 a[136:137], %[vr4] offset:28704\n\t" "v_add_f32_e64 v24, v24, v26\n\t"
    "v_mfma_f32_16x16x32_bf16 v[80:83], a[156:159], a[80:83], v[80:83]\n\t" "ds_read_b64_tr_b16 a[138:139], %[vr4] offset:32288\n\t" "v_add_f32_e64 v25, v25, v27\n\t" "s_add_u32 m0, %[sl], 102400\n\t"
    "v_mfma_f32_16x16x32_bf16 v[84:87], a[152:155], a[68:71], v[84:87]\n\t" "global_load_lds_dwordx4 %[vb2], s[78:79]\n\t" "v_add_f32_e64 v28, v28, v30\n\t"
    "v_mfma_f32_16x16x32_bf16 v[84:87], a[156:159], a[84:87], v[84:87]\n\t" "v_add_f32_e64 v29, v29, v31\n\t" "v_mfma_f32_16x16x32_bf16 v[88:91], a[152:155], a[72:75], v[88:91]\n\t"
    "ds_read_b64_tr_b16 a[140:141], %[vr4] offset:43040\n\t" "v_accvgpr_read_b32 v26, a232\n\t" "v_mfma_f32_16x16x32_bf16 v[88:91], a[156:159], a[88:91], v[88:91]\n\t"
    "ds_read_b64_tr_b16 a[142:143], %[vr4] offset:46624\n\t" "v_accvgpr_read_b32 v27, a233\n\t" "v_mfma_f32_16x16x32_bf16 v[92:95], a[152:155], a[76:79], v[92:95]\n\t"
    "global_load_dwordx4 a[12:15], %[va], s[76:77] offset:3072 sc1\n\t" "v_accvgpr_read_b32 v30, a234\n\t" "v_mfma_f32_16x16x32_bf16 v[92:95], a[156:159], a[92:95], v[92:95]\n\t"
    "s_add_u32 s76, s76, 16384\n\t" "s_addc_u32 s77, s77, 0\n\t" "v_accvgpr_read_b32 v31, a235\n\t" "s_waitcnt lgkmcnt(4)\n\t" "v_mfma_f32_16x16x32_bf16 v[96:99], a[128:131], a[64:67], v[96:99]\n\t"
    "ds_read_b64_tr_b16 a[144:145], %[vr6] offset:28672\n\t" "v_add_f32_e64 v24, v24, v26\n\t" "v_mfma_f32_16x16x32_bf16 v[96:99], a[132:135], a[80:83], v[96:99]\n\t"
    "ds_read_b64_tr_b16 a[146:147], %[vr6] offset:32256\n\t" "v_add_f32_e64 v25, v25, v27\n\t" "v_mfma_f32_16x16x32_bf16 v[100:103], a[128:131], a[68:71], v[100:103]\n\t" "v_add_f32_e64 v28, v28, v30\n\t"
    "v_mfma_f32_16x16x32_bf16 v[100:103], a[132:135], a[84:87], v[100:103]\n\t" "v_add_f32_e64 v29, v29, v31\n\t" "v_mfma_f32_16x16x32_bf16 v[104:107], a[128:131], a[72:75], v[104:107]\n\t"
    "ds_read_b64_tr_b16 a[148:149], %[vr6] offset:43008\n\t" "v_accvgpr_read_b32 v26, a240\n\t" "v_mfma_f32_16x16x32_bf16 v[104:107], a[132:135], a[88:91], v[104:107]\n\t"
    "ds_read_b64_tr_b16 a[150:151], %[vr6] offset:46592\n\t" "v_accvgpr_read_b32 v27, a241\n\t" "s_add_u32 m0, %[sl], 103424\n\t"
    "v_mfma_f32_16x16x32_bf16 v[108:111], a[128:131], a[76:79], v[108:111]\n\t" "s_mov_b64 exec, s[82:83]\n\t" "global_load_lds_dwordx4 %[vb3], s[78:79]\n\t" "s_mov_b64 exec, -1\n\t"
    "v_accvgpr_read_b32 v30, a242\n\t" "v_mfma_f32_16x16x32_bf16 v[108:111], a[132:135], a[92:95], v[108:111]\n\t" "s_add_u32 s78, s78, 229376\n\t" "s_addc_u32 s79, s79, 0\n\t"
    "v_accvgpr_read_b32 v31, a243\n\t" "s_waitcnt lgkmcnt(4)\n\t" "v_mfma_f32_16x16x32_bf16 v[112:115], a[136:139], a[64:67], v[112:115]\n\t" "ds_read_b64_tr_b16 a[152:153], %[vr0] offset:35840\n\t"
    "v_add_f32_e64 v24, v24, v26\n\t" "v_mfma_f32_16x16x32_bf16 v[112:115], a[140:143], a[80:83], v[112:115]\n\t" "ds_read_b64_tr_b16 a[154:155], %[vr0] offset:39424\n\t" "v_add_f32_e64 v25, v25, v27\n\t"
    "v_mfma_f32_16x16x32_bf16 v[116:119], a[136:139], a[68:71], v[116:119]\n\t" "global_load_dwordx4 a[16:19], %[va], s[76:77] offset:0 sc1\n\t" "v_add_f32_e64 v28, v28, v30\n\t"
    "v_mfma_f32_16x16x32_bf16 v[116:119], a[140:143], a[84:87], v[116:119]\n\t" "v_add_f32_e64 v29, v29, v31\n\t" "v_mfma_f32_16x16x32_bf16 v[120:123], a[136:139], a[72:75], v[120:123]\n\t"
    "ds_read_b64_tr_b16 a[156:157], %[vr0] offset:50176\n\t" "v_mov_b32_e64 v26, v24\n\t" "v_mfma_f32_16x16x32_bf16 v[120:123], a[140:143], a[88:91], v[120:123]\n\t"
    "ds_read_b64_tr_b16 a[158:159], %[vr0] offset:53760\n\t" "v_mov_b32 v27, v25\n\t" "s_add_u32 m0, %[sl], 0\n\t" "v_mfma_f32_16x16x32_bf16 v[124:127], a[136:139], a[76:79], v[124:127]\n\t"
    "global_load_lds_dwordx4 %[vb0], s[78:79]\n\t" "v_mov_b32_e64 v30, v28\n\t" "v_mfma_f32_16x16x32_bf16 v[124:127], a[140:143], a[92:95], v[124:127]\n\t" "v_mov_b32 v31, v29\n\t"
    "s_waitcnt lgkmcnt(4)\n\t" "v_mfma_f32_16x16x32_bf16 v[128:131], a[144:147], a[64:67], v[128:131]\n\t" "ds_read_b64_tr_b16 a[128:129], %[vr0] offset:35872\n\t" "s_nop 0\n\t"
    "v_mfma_f32_16x16x32_bf16 v[128:131], a[148:151], a[80:83], v[128:131]\n\t" "ds_read_b64_tr_b16 a[130:131], %[vr0] offset:39456\n\t" "s_nop 0\n\t"
    "v_mfma_f32_16x16x32_bf16 v[132:135], a[144:147], a[68:71], v[132:135]\n\t" "v_permlane16_swap_b32 v24, v26\n\t" "v_mfma_f32_16x16x32_bf16 v[132:135], a[148:151], a[84:87], v[132:135]\n\t"
    "v_permlane16_swap_b32 v25, v27\n\t" "v_mfma_f32_16x16x32_bf16 v[136:139], a[144:147], a[72:75], v[136:139]\n\t" "ds_read_b64_tr_b16 a[132:133], %[vr0] offset:50208\n\t"
    "v_permlane16_swap_b32 v28, v30\n\t" "v_mfma_f32_16x16x32_bf16 v[136:139], a[148:151], a[88:91], v[136:139]\n\t" "ds_read_b64_tr_b16 a[134:135], %[vr0] offset:53792\n\t"
    "v_permlane16_swap_b32 v29, v31\n\t" "v_mfma_f32_16x16x32_bf16 v[140:143], a[144:147], a[76:79], v[140:143]\n\t" "v_add_f32_e64 v24, v24, v26\n\t"
    "v_mfma_f32_16x16x32_bf16 v[140:143], a[148:151], a[92:95], v[140:143]\n\t" "v_add_f32 v25, v25, v27\n\t" "s_waitcnt lgkmcnt(4)\n\t"
    "v_mfma_f32_16x16x32_bf16 v[144:147], a[152:155], a[64:67], v[144:147]\n\t" "ds_read_b64_tr_b16 a[136:137], %[vr2] offset:35840\n\t" "v_add_f32_e64 v28, v28, v30\n\t"
    "v_mfma_f32_16x16x32_bf16 v[144:147], a[156:159], a[80:83], v[144:147]\n\t" "ds_read_b64_tr_b16 a[138:139], %[vr2] offset:39424\n\t" "v_add_f32_e64 v29, v29, v31\n\t"
    "v_mfma_f32_16x16x32_bf16 v[148:151], a[152:155], a[68:71], v[148:151]\n\t" "global_load_dwordx4 a[20:23], %[va], s[76:77] offset:1024 sc1\n\t" "v_mov_b32_e64 v26, v24\n\t"
    "v_mfma_f32_16x16x32_bf16 v[148:151], a[156:159], a[84:87], v[148:151]\n\t" "v_mov_b32_e64 v27, v25\n\t" "v_mfma_f32_16x16x32_bf16 v[152:155], a[152:155], a[72:75], v[152:155]\n\t"
    "ds_read_b64_tr_b16 a[140:141], %[vr2] offset:50176\n\t" "v_mov_b32_e64 v30, v28\n\t" "v_mfma_f32_16x16x32_bf16 v[152:155], a[156:159], a[88:91], v[152:155]\n\t"
    "ds_read_b64_tr_b16 a[142:143], %[vr2] offset:53760\n\t" "v_mov_b32_e64 v31, v29\n\t" "s_add_u32 m0, %[sl], 1024\n\t" "v_mfma_f32_16x16x32_bf16 v[156:159], a[152:155], a[76:79], v[156:159]\n\t"
    "global_load_lds_dwordx4 %[vb1], s[78:79]\n\t" "s_nop 0\n\t" "v_mfma_f32_16x16x32_bf16 v[156:159], a[156:159], a[92:95], v[156:159]\n\t" "s_nop 0\n\t" "s_waitcnt lgkmcnt(4)\n\t"
    "v_mfma_f32_16x16x32_bf16 v[160:163], a[128:131], a[64:67], v[160:163]\n\t" "ds_read_b64_tr_b16 a[144:145], %[vr2] offset:35872\n\t" "v_permlane32_swap_b32 v24, v26\n\t"
    "v_mfma_f32_16x16x32_bf16 v[160:163], a[132:135], a[80:83], v[160:163]\n\t" "ds_read_b64_tr_b16 a[146:147], %[vr2] offset:39456\n\t" "v_permlane32_swap_b32 v25, v27\n\t"
    "v_mfma_f32_16x16x32_bf16 v[164:167], a[128:131], a[68:71], v[164:167]\n\t" "global_load_dwordx4 a[24:27], %[va], s[76:77] offset:2048 sc1\n\t" "v_permlane32_swap_b32 v28, v30\n\t"
    "v_mfma_f32_16x16x32_bf16 v[164:167], a[132:135], a[84:87], v[164:167]\n\t" "v_permlane32_swap_b32 v29, v31\n\t" "v_mfma_f32_16x16x32_bf16 v[168:171], a[128:131], a[72:75], v[168:171]\n\t"
    "ds_read_b64_tr_b16 a[148:149], %[vr2] offset:50208\n\t" "v_add_f32 v24, v24, v26\n\t" "v_mfma_f32_16x16x32_bf16 v[168:171], a[132:135], a[88:91], v[168:171]\n\t"
    "ds_read_b64_tr_b16 a[150:151], %[vr2] offset:53792\n\t" "v_add_f32_e64 v25, v25, v27\n\t" "v_mfma_f32_16x16x32_bf16 v[172:175], a[128:131], a[76:79], v[172:175]\n\t"
    "global_load_dwordx4 a[28:31], %[va], s[76:77] offset:3072 sc1\n\t" "v_add_f32_e64 v28, v28, v30\n\t" "v_mfma_f32_16x16x32_bf16 v[172:175], a[132:135], a[92:95], v[172:175]\n\t"
    "s_add_u32 s76, s76, 16384\n\t" "s_addc_u32 s77, s77, 0\n\t" "v_add_f32_e64 v29, v29, v31\n\t" "s_waitcnt lgkmcnt(4)\n\t" "v_mfma_f32_16x16x32_bf16 v[176:179], a[136:139], a[64:67], v[176:179]\n\t"
    "ds_read_b64_tr_b16 a[152:153], %[vr4] offset:35840\n\t" "v_mul_f32 v24, 0xba800000, v24\n\t" "v_mfma_f32_16x16x32_bf16 v[176:179], a[140:143], a[80:83], v[176:179]\n\t"
    "ds_read_b64_tr_b16 a[154:155], %[vr4] offset:39424\n\t" "v_mul_f32 v25, 0x3a800000, v25\n\t" "v_mfma_f32_16x16x32_bf16 v[180:183], a[136:139], a[68:71], v[180:183]\n\t"
    "v_mul_f32 v28, 0xba800000, v28\n\t" "v_mfma_f32_16x16x32_bf16 v[180:183], a[140:143], a[84:87], v[180:183]\n\t" "v_mul_f32 v29, 0x3a800000, v29\n\t"
    "v_mfma_f32_16x16x32_bf16 v[184:187], a[136:139], a[72:75], v[184:187]\n\t" "ds_read_b64_tr_b16 a[156:157], %[vr4] offset:50176\n\t" "v_fma_f32 v25, -v24, v24, v25\n\t"
    "v_mfma_f32_16x16x32_bf16 v[184:187], a[140:143], a[88:91], v[184:187]\n\t" "ds_read_b64_tr_b16 a[158:159], %[vr4] offset:53760\n\t" "v_and_b32 v26, 0xffff0000, v24\n\t"
    "s_add_u32 m0, %[sl], 2048\n\t" "v_mfma_f32_16x16x32_bf16 v[188:191], a[136:139], a[76:79], v[188:191]\n\t" "global_load_lds_dwordx4 %[vb2], s[78:79]\n\t" "v_fma_f32 v29, -v28, v28, v29\n\t"
    "v_mfma_f32_16x16x32_bf16 v[188:191], a[140:143], a[92:95], v[188:191]\n\t" "v_and_b32 v30, 0xffff0000, v28\n\t" "s_waitcnt lgkmcnt(4)\n\t"
    "v_mfma_f32_16x16x32_bf16 v[192:195], a[144:147], a[64:67], v[192:195]\n\t" "ds_read_b64_tr_b16 a[128:129], %[vr4] offset:35872\n\t" "v_max_f32 v25, 0, v25\n\t"
    "v_mfma_f32_16x16x32_bf16 v[192:195], a[148:151], a[80:83], v[192:195]\n\t" "ds_read_b64_tr_b16 a[130:131], %[vr4] offset:39456\n\t" "v_sub_f32_e64 v24, v24, v26\n\t"
    "v_mfma_f32_16x16x32_bf16 v[196:199], a[144:147], a[68:71], v[196:199]\n\t" "global_load_dwordx4 a[32:35], %[va], s[76:77] offset:0 sc1\n\t" "v_max_f32_e64 v29, 0, v29\n\t"
    "v_mfma_f32_16x16x32_bf16 v[196:199], a[148:151], a[84:87], v[196:199]\n\t" "v_sub_f32_e64 v28, v28, v30\n\t" "v_mfma_f32_16x16x32_bf16 v[200:203], a[144:147], a[72:75], v[200:203]\n\t"
    "ds_read_b64_tr_b16 a[132:133], %[vr4] offset:50208\n\t" "v_add_f32 v25, 0x3727c5ac, v25\n\t" "v_mfma_f32_16x16x32_bf16 v[200:203], a[148:151], a[88:91], v[200:203]\n\t"
    "ds_read_b64_tr_b16 a[134:135], %[vr4] offset:53792\n\t" "v_cvt_pk_bf16_f32 v26, v26, v26\n\t" "s_add_u32 m0, %[sl], 3072\n\t"
    "v_mfma_f32_16x16x32_bf16 v[204:207], a[144:147], a[76:79], v[204:207]\n\t" "s_mov_b64 exec, s[82:83]\n\t" "global_load_lds_dwordx4 %[vb3], s[78:79]\n\t" "s_mov_b64 exec, -1\n\t"
    "v_add_f32 v29, 0x3727c5ac, v29\n\t" "v_mfma_f32_16x16x32_bf16 v[204:207], a[148:151], a[92:95], v[204:207]\n\t" "s_add_u32 s78, s78, 229376\n\t" "s_addc_u32 s79, s79, 0\n\t"
    "v_cvt_pk_bf16_f32 v30, v30, v30\n\t" "s_waitcnt lgkmcnt(4)\n\t" "v_mfma_f32_16x16x32_bf16 v[208:211], a[152:155], a[64:67], v[208:211]\n\t" "ds_read_b64_tr_b16 a[136:137], %[vr6] offset:35840\n\t"
    "v_rsq_f32_e64 v27, v25\n\t" "v_mfma_f32_16x16x32_bf16 v[208:211], a[156:159], a[80:83], v[208:211]\n\t" "ds_read_b64_tr_b16 a[138:139], %[vr6] offset:39424\n\t" "v_cvt_pk_bf16_f32 v24, v24, v24\n\t"
    "v_mfma_f32_16x16x32_bf16 v[212:215], a[152:155], a[68:71], v[212:215]\n\t" "v_rsq_f32_e64 v31, v29\n\t" "v_mfma_f32_16x16x32_bf16 v[212:215], a[156:159], a[84:87], v[212:215]\n\t"
    "v_cvt_pk_bf16_f32 v28, v28, v28\n\t" "v_mfma_f32_16x16x32_bf16 v[216:219], a[152:155], a[72:75], v[216:219]\n\t" "ds_read_b64_tr_b16 a[140:141], %[vr6] offset:50176\n\t"
    "v_mul_f32_e64 v20, v25, v27\n\t" "v_mfma_f32_16x16x32_bf16 v[216:219], a[156:159], a[88:91], v[216:219]\n\t" "ds_read_b64_tr_b16 a[142:143], %[vr6] offset:53760\n\t"
    "v_mul_f32 v16, 0xbfb8aa3b, v27\n\t" "v_mfma_f32_16x16x32_bf16 v[220:223], a[152:155], a[76:79], v[220:223]\n\t" "global_load_dwordx4 a[36:39], %[va], s[76:77] offset:1024 sc1\n\t"
    "v_mul_f32_e64 v21, v29, v31\n\t" "v_mfma_f32_16x16x32_bf16 v[220:223], a[156:159], a[92:95], v[220:223]\n\t" "v_mul_f32 v17, 0xbfb8aa3b, v31\n\t" "s_waitcnt lgkmcnt(4)\n\t"
    "v_mfma_f32_16x16x32_bf16 v[224:227], a[128:131], a[64:67], v[224:227]\n\t" "s_waitcnt vmcnt(26)\n\t" "s_barrier\n\t" "ds_read_b64_tr_b16 a[144:145], %[vr1] offset:0\n\t"
    "v_and_b32 v27, 0xffff0000, v20\n\t" "v_mfma_f32_16x16x32_bf16 v[224:227], a[132:135], a[80:83], v[224:227]\n\t" "ds_read_b64_tr_b16 a[146:147], %[vr1] offset:3584\n\t" "s_nop 0\n\t"
    "v_mfma_f32_16x16x32_bf16 v[228:231], a[128:131], a[68:71], v[228:231]\n\t" "global_load_dwordx4 a[40:43], %[va], s[76:77] offset:2048 sc1\n\t" "v_and_b32 v31, 0xffff0000, v21\n\t"
    "v_mfma_f32_16x16x32_bf16 v[228:231], a[132:135], a[84:87], v[228:231]\n\t" "v_sub_f32_e64 v25, v20, v27\n\t" "v_mfma_f32_16x16x32_bf16 v[232:235], a[128:131], a[72:75], v[232:235]\n\t"
    "ds_read_b64_tr_b16 a[148:149], %[vr1] offset:14336\n\t" "v_cvt_pk_bf16_f32 v27, v27, v27\n\t" "v_mfma_f32_16x16x32_bf16 v[232:235], a[132:135], a[88:91], v[232:235]\n\t"
    "ds_read_b64_tr_b16 a[150:151], %[vr1] offset:17920\n\t" "v_sub_f32_e64 v29, v21, v31\n\t" "s_add_u32 m0, %[sl], 14336\n\t" "v_mfma_f32_16x16x32_bf16 v[236:239], a[128:131], a[76:79], v[236:239]\n\t"
    "global_load_lds_dwordx4 %[vb0], s[78:79]\n\t" "v_cvt_pk_bf16_f32 v31, v31, v31\n\t" "v_mfma_f32_16x16x32_bf16 v[236:239], a[132:135], a[92:95], v[236:239]\n\t" "v_cvt_pk_bf16_f32 v25, v25, v25\n\t"
    "s_waitcnt lgkmcnt(4)\n\t" "v_mfma_f32_16x16x32_bf16 v[240:243], a[136:139], a[64:67], v[240:243]\n\t" "ds_read_b64_tr_b16 a[152:153], %[vr1] offset:32\n\t" "s_nop 0\n\t"
    "v_mfma_f32_16x16x32_bf16 v[240:243], a[140:143], a[80:83], v[240:243]\n\t" "ds_read_b64_tr_b16 a[154:155], %[vr1] offset:3616\n\t" "v_cvt_pk_bf16_f32 v29, v29, v29\n\t"
    "v_mfma_f32_16x16x32_bf16 v[244:247], a[136:139], a[68:71], v[244:247]\n\t" "s_mov_b64 exec, 0xffff\n\t" "v_accvgpr_write_b32 a200, v26\n\t" "v_accvgpr_write_b32 a201, v24\n\t"
    "v_accvgpr_write_b32 a202, v27\n\t" "v_accvgpr_write_b32 a203, v25\n\t" "v_accvgpr_write_b32 a204, v30\n\t" "v_accvgpr_write_b32 a205, v28\n\t" "v_accvgpr_write_b32 a206, v31\n\t"
    "v_accvgpr_write_b32 a207, v29\n\t" "s_mov_b64 exec, -1\n\t" "v_mfma_f32_16x16x32_bf16 v[244:247], a[140:143], a[84:87], v[244:247]\n\t" "v_accvgpr_read_b32 v24, a220\n\t"
    "v_mfma_f32_16x16x32_bf16 v[248:251], a[136:139], a[72:75], v[248:251]\n\t" "ds_read_b64_tr_b16 a[156:157], %[vr1] offset:14368\n\t" "v_accvgpr_read_b32 v25, a221\n\t"
    "v_mfma_f32_16x16x32_bf16 v[248:251], a[140:143], a[88:91], v[248:251]\n\t" "ds_read_b64_tr_b16 a[158:159], %[vr1] offset:17952\n\t" "v_accvgpr_read_b32 v28, a222\n\t"
    "v_mfma_f32_16x16x32_bf16 v[252:255], a[136:139], a[76:79], v[252:255]\n\t" "v_accvgpr_read_b32 v29, a223\n\t" "v_mfma_f32_16x16x32_bf16 v[252:255], a[140:143], a[92:95], v[252:255]\n\t"
    "v_accvgpr_read_b32 v26, a228\n\t" "s_waitcnt lgkmcnt(4) vmcnt(21)\n\t" "v_mfma_f32_16x16x32_bf16 v[32:35], a[144:147], a[96:99], v[32:35]\n\t" "s_barrier\n\t"
    "ds_read_b64_tr_b16 a[128:129], %[vr3] offset:0\n\t" "v_accvgpr_read_b32 v27, a229\n\t" "v_mfma_f32_16x16x32_bf16 v[32:35], a[148:151], a[112:115], v[32:35]\n\t"
    "ds_read_b64_tr_b16 a[130:131], %[vr3] offset:3584\n\t" "v_accvgpr_read_b32 v30, a230\n\t" "v_mfma_f32_16x16x32_bf16 v[36:39], a[144:147], a[100:103], v[36:39]\n\t"
    "global_load_dwordx4 a[44:47], %[va], s[76:77] offset:3072 sc1\n\t" "s_mov_b32 m0, %[srl]\n\t" "s_nop 0\n\t" "global_load_lds_dwordx4 %[va], %[sr] offset:0 sc1\n\t" "v_accvgpr_read_b32 v31, a231\n\t"
    "v_mfma_f32_16x16x32_bf16 v[36:39], a[148:151], a[116:119], v[36:39]\n\t" "s_add_u32 s76, s76, 16384\n\t" "s_addc_u32 s77, s77, 0\n\t" "v_add_f32_e64 v24, v24, v26\n\t"
    "v_mfma_f32_16x16x32_bf16 v[40:43], a[144:147], a[104:107], v[40:43]\n\t" "ds_read_b64_tr_b16 a[132:133], %[vr3] offset:14336\n\t" "v_add_f32_e64 v25, v25, v27\n\t"
    "v_mfma_f32_16x16x32_bf16 v[40:43], a[148:151], a[120:123], v[40:43]\n\t" "ds_read_b64_tr_b16 a[134:135], %[vr3] offset:17920\n\t" "v_add_f32_e64 v28, v28, v30\n\t" "s_add_u32 m0, %[sl], 15360\n\t"
    "v_mfma_f32_16x16x32_bf16 v[44:47], a[144:147], a[108:111], v[44:47]\n\t" "global_load_lds_dwordx4 %[vb1], s[78:79]\n\t" "s_mov_b32 m0, %[srl]\n\t" "s_nop 0\n\t"
    "global_load_lds_dwordx4 %[va], %[sr] offset:1024 sc1\n\t" "v_add_f32_e64 v29, v29, v31\n\t" "v_mfma_f32_16x16x32_bf16 v[44:47], a[148:151], a[124:127], v[44:47]\n\t"
    "v_accvgpr_read_b32 v26, a236\n\t" "s_waitcnt lgkmcnt(4)\n\t" "v_mfma_f32_16x16x32_bf16 v[48:51], a[152:155], a[96:99], v[48:51]\n\t" "ds_read_b64_tr_b16 a[136:137], %[vr3] offset:32\n\t"
    "v_accvgpr_read_b32 v27, a237\n\t" "v_mfma_f32_16x16x32_bf16 v[48:51], a[156:159], a[112:115], v[48:51]\n\t" "ds_read_b64_tr_b16 a[138:139], %[vr3] offset:3616\n\t" "v_accvgpr_read_b32 v30, a238\n\t"
    "v_mfma_f32_16x16x32_bf16 v[52:55], a[152:155], a[100:103], v[52:55]\n\t" "global_load_dwordx4 a[48:51], %[va], s[76:77] offset:0 sc1\n\t" "s_mov_b32 m0, %[srl]\n\t" "s_nop 0\n\t"
    "global_load_lds_dwordx4 %[va], %[sr] offset:2048 sc1\n\t" "v_accvgpr_read_b32 v31, a239\n\t" "v_mfma_f32_16x16x32_bf16 v[52:55], a[156:159], a[116:119], v[52:55]\n\t" "v_add_f32 v24, v24, v26\n\t"
    "v_mfma_f32_16x16x32_bf16 v[56:59], a[152:155], a[104:107], v[56:59]\n\t" "ds_read_b64_tr_b16 a[140:141], %[vr3] offset:14368\n\t" "v_add_f32_e64 v25, v25, v27\n\t"
    "v_mfma_f32_16x16x32_bf16 v[56:59], a[156:159], a[120:123], v[56:59]\n\t" "ds_read_b64_tr_b16 a[142:143], %[vr3] offset:17952\n\t" "v_add_f32_e64 v28, v28, v30\n\t" "s_add_u32 m0, %[sl], 16384\n\t"
    "v_mfma_f32_16x16x32_bf16 v[60:63], a[152:155], a[108:111], v[60:63]\n\t" "global_load_lds_dwordx4 %[vb2], s[78:79]\n\t" "s_mov_b32 m0, %[srl]\n\t" "s_nop 0\n\t"
    "global_load_lds_dwordx4 %[va], %[sr] offset:3072 sc1\n\t" "v_add_f32_e64 v29, v29, v31\n\t" "v_mfma_f32_16x16x32_bf16 v[60:63], a[156:159], a[124:127], v[60:63]\n\t"
    "v_accvgpr_read_b32 v26, a244\n\t" "s_waitcnt lgkmcnt(4)\n\t" "v_mfma_f32_16x16x32_bf16 v[64:67], a[128:131], a[96:99], v[64:67]\n\t" "ds_read_b64_tr_b16 a[144:145], %[vr5] offset:0\n\t"
    "v_accvgpr_read_b32 v27, a245\n\t" "v_mfma_f32_16x16x32_bf16 v[64:67], a[132:135], a[112:115], v[64:67]\n\t" "ds_read_b64_tr_b16 a[146:147], %[vr5] offset:3584\n\t" "v_accvgpr_read_b32 v30, a246\n\t"
    "v_mfma_f32_16x16x32_bf16 v[68:71], a[128:131], a[100:103], v[68:71]\n\t" "v_accvgpr_read_b32 v31, a247\n\t" "v_mfma_f32_16x16x32_bf16 v[68:71], a[132:135], a[116:119], v[68:71]\n\t"
    "v_add_f32 v24, v24, v26\n\t" "v_mfma_f32_16x16x32_bf16 v[72:75], a[128:131], a[104:107], v[72:75]\n\t" "ds_read_b64_tr_b16 a[148:149], %[vr5] offset:14336\n\t" "v_add_f32_e64 v25, v25, v27\n\t"
    "v_mfma_f32_16x16x32_bf16 v[72:75], a[132:135], a[120:123], v[72:75]\n\t" "ds_read_b64_tr_b16 a[150:151], %[vr5] offset:17920\n\t" "v_add_f32_e64 v28, v28, v30\n\t"
    "v_mfma_f32_16x16x32_bf16 v[76:79], a[128:131], a[108:111], v[76:79]\n\t" "global_load_dwordx4 a[52:55], %[va], s[76:77] offset:1024 sc1\n\t" "v_add_f32_e64 v29, v29, v31\n\t"
    "v_mfma_f32_16x16x32_bf16 v[76:79], a[132:135], a[124:127], v[76:79]\n\t" "v_mov_b32 v26, v24\n\t" "s_waitcnt lgkmcnt(4)\n\t" "v_mfma_f32_16x16x32_bf16 v[80:83], a[136:139], a[96:99], v[80:83]\n\t"
    "ds_read_b64_tr_b16 a[152:153], %[vr5] offset:32\n\t" "v_mov_b32_e64 v27, v25\n\t" "v_mfma_f32_16x16x32_bf16 v[80:83], a[140:143], a[112:115], v[80:83]\n\t"
    "ds_read_b64_tr_b16 a[154:155], %[vr5] offset:3616\n\t" "v_mov_b32_e64 v30, v28\n\t" "s_add_u32 m0, %[sl], 17408\n\t" "v_mfma_f32_16x16x32_bf16 v[84:87], a[136:139], a[100:103], v[84:87]\n\t"
    "s_mov_b64 exec, s[82:83]\n\t" "global_load_lds_dwordx4 %[vb3], s[78:79]\n\t" "s_mov_b64 exec, -1\n\t" "v_mov_b32_e64 v31, v29\n\t"
    "v_mfma_f32_16x16x32_bf16 v[84:87], a[140:143], a[116:119], v[84:87]\n\t" "s_add_u32 s78, s78, 229376\n\t" "s_addc_u32 s79, s79, 0\n\t" "s_nop 0\n\t"
    "v_mfma_f32_16x16x32_bf16 v[88:91], a[136:139], a[104:107], v[88:91]\n\t" "ds_read_b64_tr_b16 a[156:157], %[vr5] offset:14368\n\t" "s_nop 0\n\t"
    "v_mfma_f32_16x16x32_bf16 v[88:91], a[140:143], a[120:123], v[88:91]\n\t" "ds_read_b64_tr_b16 a[158:159], %[vr5] offset:17952\n\t" "v_permlane16_swap_b32 v24, v26\n\t"
    "v_mfma_f32_16x16x32_bf16 v[92:95], a[136:139], a[108:111], v[92:95]\n\t" "global_load_dwordx4 a[56:59], %[va], s[76:77] offset:2048 sc1\n\t" "v_permlane16_swap_b32 v25, v27\n\t"
    "v_mfma_f32_16x16x32_bf16 v[92:95], a[140:143], a[124:127], v[92:95]\n\t" "v_permlane16_swap_b32 v28, v30\n\t" "s_waitcnt lgkmcnt(4)\n\t"
    "v_mfma_f32_16x16x32_bf16 v[96:99], a[144:147], a[96:99], v[96:99]\n\t" "ds_read_b64_tr_b16 a[128:129], %[vr7] offset:0\n\t" "v_permlane16_swap_b32 v29, v31\n\t"
    "v_mfma_f32_16x16x32_bf16 v[96:99], a[148:151], a[112:115], v[96:99]\n\t" "ds_read_b64_tr_b16 a[130:131], %[vr7] offset:3584\n\t" "v_add_f32_e64 v24, v24, v26\n\t"
    "v_mfma_f32_16x16x32_bf16 v[100:103], a[144:147], a[100:103], v[100:103]\n\t" "v_add_f32_e64 v25, v25, v27\n\t" "v_mfma_f32_16x16x32_bf16 v[100:103], a[148:151], a[116:119], v[100:103]\n\t"
    "v_add_f32_e64 v28, v28, v30\n\t" "v_mfma_f32_16x16x32_bf16 v[104:107], a[144:147], a[104:107], v[104:107]\n\t" "ds_read_b64_tr_b16 a[132:133], %[vr7] offset:14336\n\t"
    "v_add_f32_e64 v29, v29, v31\n\t" "v_mfma_f32_16x16x32_bf16 v[104:107], a[148:151], a[120:123], v[104:107]\n\t" "ds_read_b64_tr_b16 a[134:135], %[vr7] offset:17920\n\t" "v_mov_b32_e64 v26, v24\n\t"
    "s_add_u32 m0, %[sl], 28672\n\t" "v_mfma_f32_16x16x32_bf16 v[108:111], a[144:147], a[108:111], v[108:111]\n\t" "global_load_lds_dwordx4 %[vb0], s[78:79]\n\t" "v_mov_b32_e64 v27, v25\n\t"
    "v_mfma_f32_16x16x32_bf16 v[108:111], a[148:151], a[124:127], v[108:111]\n\t" "v_mov_b32 v30, v28\n\t" "s_waitcnt lgkmcnt(4)\n\t"
    "v_mfma_f32_16x16x32_bf16 v[112:115], a[152:155], a[96:99], v[112:115]\n\t" "ds_read_b64_tr_b16 a[136:137], %[vr1] offset:7168\n\t" "v_mov_b32_e64 v31, v29\n\t"
    "v_mfma_f32_16x16x32_bf16 v[112:115], a[156:159], a[112:115], v[112:115]\n\t" "ds_read_b64_tr_b16 a[138:139], %[vr1] offset:10752\n\t" "s_nop 0\n\t"
    "v_mfma_f32_16x16x32_bf16 v[116:119], a[152:155], a[100:103], v[116:119]\n\t" "global_load_dwordx4 a[60:63], %[va], s[76:77] offset:3072 sc1\n\t" "s_nop 0\n\t"
    "v_mfma_f32_16x16x32_bf16 v[116:119], a[156:159], a[116:119], v[116:119]\n\t" "s_add_u32 s76, s76, 16384\n\t" "s_addc_u32 s77, s77, 0\n\t" "v_permlane32_swap_b32 v24, v26\n\t"
    "v_mfma_f32_16x16x32_bf16 v[120:123], a[152:155], a[104:107], v[120:123]\n\t" "ds_read_b64_tr_b16 a[140:141], %[vr1] offset:21504\n\t" "v_permlane32_swap_b32 v25, v27\n\t"
    "v_mfma_f32_16x16x32_bf16 v[120:123], a[156:159], a[120:123], v[120:123]\n\t" "ds_read_b64_tr_b16 a[142:143], %[vr1] offset:25088\n\t" "v_permlane32_swap_b32 v28, v30\n\t"
    "s_add_u32 m0, %[sl], 29696\n\t" "v_mfma_f32_16x16x32_bf16 v[124:127], a[152:155], a[108:111], v[124:127]\n\t" "global_load_lds_dwordx4 %[vb1], s[78:79]\n\t" "v_permlane32_swap_b32 v29, v31\n\t"
    "v_mfma_f32_16x16x32_bf16 v[124:127], a[156:159], a[124:127], v[124:127]\n\t" "v_add_f32_e64 v24, v24, v26\n\t" "s_waitcnt lgkmcnt(4)\n\t"
    "v_mfma_f32_16x16x32_bf16 v[128:131], a[128:131], a[96:99], v[128:131]\n\t" "ds_read_b64_tr_b16 a[144:145], %[vr1] offset:7200\n\t" "v_add_f32_e64 v25, v25, v27\n\t"
    "v_mfma_f32_16x16x32_bf16 v[128:131], a[132:135], a[112:115], v[128:131]\n\t" "ds_read_b64_tr_b16 a[146:147], %[vr1] offset:10784\n\t" "v_add_f32_e64 v28, v28, v30\n\t"
    "v_mfma_f32_16x16x32_bf16 v[132:135], a[128:131], a[100:103], v[132:135]\n\t" "v_add_f32_e64 v29, v29, v31\n\t" "v_mfma_f32_16x16x32_bf16 v[132:135], a[132:135], a[116:119], v[132:135]\n\t"
    "v_mul_f32 v24, 0xba800000, v24\n\t" "v_mfma_f32_16x16x32_bf16 v[136:139], a[128:131], a[104:107], v[136:139]\n\t" "ds_read_b64_tr_b16 a[148:149], %[vr1] offset:21536\n\t"
    "v_mul_f32 v25, 0x3a800000, v25\n\t" "v_mfma_f32_16x16x32_bf16 v[136:139], a[132:135], a[120:123], v[136:139]\n\t" "ds_read_b64_tr_b16 a[150:151], %[vr1] offset:25120\n\t"
    "v_mul_f32 v28, 0xba800000, v28\n\t" "v_mfma_f32_16x16x32_bf16 v[140:143], a[128:131], a[108:111], v[140:143]\n\t" "v_mul_f32 v29, 0x3a800000, v29\n\t"
    "v_mfma_f32_16x16x32_bf16 v[140:143], a[132:135], a[124:127], v[140:143]\n\t" "v_fma_f32 v25, -v24, v24, v25\n\t" "s_waitcnt lgkmcnt(4)\n\t"
    "v_mfma_f32_16x16x32_bf16 v[144:147], a[136:139], a[96:99], v[144:147]\n\t" "ds_read_b64_tr_b16 a[152:153], %[vr3] offset:7168\n\t" "v_and_b32 v26, 0xffff0000, v24\n\t"
    "v_mfma_f32_16x16x32_bf16 v[144:147], a[140:143], a[112:115], v[144:147]\n\t" "ds_read_b64_tr_b16 a[154:155], %[vr3] offset:10752\n\t" "v_fma_f32 v29, -v28, v28, v29\n\t"
    "v_mfma_f32_16x16x32_bf16 v[148:151], a[136:139], a[100:103], v[148:151]\n\t" "global_load_dwordx4 a[64:67], %[va], s[76:77] offset:0 sc1\n\t" "v_and_b32 v30, 0xffff0000, v28\n\t"
    "v_mfma_f32_16x16x32_bf16 v[148:151], a[140:143], a[116:119], v[148:151]\n\t" "v_max_f32 v25, 0, v25\n\t" "v_mfma_f32_16x16x32_bf16 v[152:155], a[136:139], a[104:107], v[152:155]\n\t"
    "ds_read_b64_tr_b16 a[156:157], %[vr3] offset:21504\n\t" "v_sub_f32_e64 v24, v24, v26\n\t" "v_mfma_f32_16x16x32_bf16 v[152:155], a[140:143], a[120:123], v[152:155]\n\t"
    "ds_read_b64_tr_b16 a[158:159], %[vr3] offset:25088\n\t" "v_max_f32_e64 v29, 0, v29\n\t" "s_add_u32 m0, %[sl], 30720\n\t" "v_mfma_f32_16x16x32_bf16 v[156:159], a[136:139], a[108:111], v[156:159]\n\t"
    "global_load_lds_dwordx4 %[vb2], s[78:79]\n\t" "v_sub_f32_e64 v28, v28, v30\n\t" "v_mfma_f32_16x16x32_bf16 v[156:159], a[140:143], a[124:127], v[156:159]\n\t" "v_add_f32 v25, 0x3727c5ac, v25\n\t"
    "s_waitcnt lgkmcnt(4)\n\t" "v_mfma_f32_16x16x32_bf16 v[160:163], a[144:147], a[96:99], v[160:163]\n\t" "ds_read_b64_tr_b16 a[128:129], %[vr3] offset:7200\n\t" "v_cvt_pk_bf16_f32 v26, v26, v26\n\t"
    "v_mfma_f32_16x16x32_bf16 v[160:163], a[148:151], a[112:115], v[160:163]\n\t" "ds_read_b64_tr_b16 a[130:131], %[vr3] offset:10784\n\t" "v_add_f32 v29, 0x3727c5ac, v29\n\t"
    "v_mfma_f32_16x16x32_bf16 v[164:167], a[144:147], a[100:103], v[164:167]\n\t" "global_load_dwordx4 a[68:71], %[va], s[76:77] offset:1024 sc1\n\t" "v_cvt_pk_bf16_f32 v30, v30, v30\n\t"
    "v_mfma_f32_16x16x32_bf16 v[164:167], a[148:151], a[116:119], v[164:167]\n\t" "v_rsq_f32 v27, v25\n\t" "v_mfma_f32_16x16x32_bf16 v[168:171], a[144:147], a[104:107], v[168:171]\n\t"
    "ds_read_b64_tr_b16 a[132:133], %[vr3] offset:21536\n\t" "v_cvt_pk_bf16_f32 v24, v24, v24\n\t" "v_mfma_f32_16x16x32_bf16 v[168:171], a[148:151], a[120:123], v[168:171]\n\t"
    "ds_read_b64_tr_b16 a[134:135], %[vr3] offset:25120\n\t" "v_rsq_f32_e64 v31, v29\n\t" "s_add_u32 m0, %[sl], 31744\n\t" "v_mfma_f32_16x16x32_bf16 v[172:175], a[144:147], a[108:111], v[172:175]\n\t"
    "s_mov_b64 exec, s[82:83]\n\t" "global_load_lds_dwordx4 %[vb3], s[78:79]\n\t" "s_mov_b64 exec, -1\n\t" "v_cvt_pk_bf16_f32 v28, v28, v28\n\t"
    "v_mfma_f32_16x16x32_bf16 v[172:175], a[148:151], a[124:127], v[172:175]\n\t" "s_add_u32 s78, s78, 229376\n\t" "s_addc_u32 s79, s79, 0\n\t" "v_mul_f32_e64 v22, v25, v27\n\t" "s_waitcnt lgkmcnt(4)\n\t"
    "v_mfma_f32_16x16x32_bf16 v[176:179], a[152:155], a[96:99], v[176:179]\n\t" "ds_read_b64_tr_b16 a[136:137], %[vr5] offset:7168\n\t" "v_mul_f32 v18, 0xbfb8aa3b, v27\n\t"
    "v_mfma_f32_16x16x32_bf16 v[176:179], a[156:159], a[112:115], v[176:179]\n\t" "ds_read_b64_tr_b16 a[138:139], %[vr5] offset:10752\n\t" "v_mul_f32_e64 v23, v29, v31\n\t"
    "v_mfma_f32_16x16x32_bf16 v[180:183], a[152:155], a[100:103], v[180:183]\n\t" "v_mul_f32 v19, 0xbfb8aa3b, v31\n\t" "v_mfma_f32_16x16x32_bf16 v[180:183], a[156:159], a[116:119], v[180:183]\n\t"
    "v_and_b32 v27, 0xffff0000, v22\n\t" "v_mfma_f32_16x16x32_bf16 v[184:187], a[152:155], a[104:107], v[184:187]\n\t" "ds_read_b64_tr_b16 a[140:141], %[vr5] offset:21504\n\t" "s_nop 0\n\t"
    "v_mfma_f32_16x16x32_bf16 v[184:187], a[156:159], a[120:123], v[184:187]\n\t" "ds_read_b64_tr_b16 a[142:143], %[vr5] offset:25088\n\t" "v_and_b32 v31, 0xffff0000, v23\n\t"
    "v_mfma_f32_16x16x32_bf16 v[188:191], a[152:155], a[108:111], v[188:191]\n\t" "global_load_dwordx4 a[72:75], %[va], s[76:77] offset:2048 sc1\n\t" "v_sub_f32 v25, v22, v27\n\t"
    "v_mfma_f32_16x16x32_bf16 v[188:191], a[156:159], a[124:127], v[188:191]\n\t" "v_cvt_pk_bf16_f32 v27, v27, v27\n\t" "s_waitcnt lgkmcnt(4)\n\t"
    "v_mfma_f32_16x16x32_bf16 v[192:195], a[128:131], a[96:99], v[192:195]\n\t" "ds_read_b64_tr_b16 a[144:145], %[vr5] offset:7200\n\t" "v_sub_f32 v29, v23, v31\n\t"
    "v_mfma_f32_16x16x32_bf16 v[192:195], a[132:135], a[112:115], v[192:195]\n\t" "ds_read_b64_tr_b16 a[146:147], %[vr5] offset:10784\n\t" "v_cvt_pk_bf16_f32 v31, v31, v31\n\t"
    "s_add_u32 m0, %[sl], 43008\n\t" "v_mfma_f32_16x16x32_bf16 v[196:199], a[128:131], a[100:103], v[196:199]\n\t" "global_load_lds_dwordx4 %[vb0], s[78:79]\n\t" "v_cvt_pk_bf16_f32 v25, v25, v25\n\t"
    "v_mfma_f32_16x16x32_bf16 v[196:199], a[132:135], a[116:119], v[196:199]\n\t" "s_nop 0\n\t" "v_mfma_f32_16x16x32_bf16 v[200:203], a[128:131], a[104:107], v[200:203]\n\t"
    "ds_read_b64_tr_b16 a[148:149], %[vr5] offset:21536\n\t" "v_cvt_pk_bf16_f32 v29, v29, v29\n\t" "v_mfma_f32_16x16x32_bf16 v[200:203], a[132:135], a[120:123], v[200:203]\n\t"
    "ds_read_b64_tr_b16 a[150:151], %[vr5] offset:25120\n\t" "s_mov_b64 exec, 0xffff\n\t" "v_accvgpr_write_b32 a208, v26\n\t" "v_accvgpr_write_b32 a209, v24\n\t" "v_accvgpr_write_b32 a210, v27\n\t"
    "v_accvgpr_write_b32 a211, v25\n\t" "v_accvgpr_write_b32 a212, v30\n\t" "v_accvgpr_write_b32 a213, v28\n\t" "v_accvgpr_write_b32 a214, v31\n\t" "v_accvgpr_write_b32 a215, v29\n\t"
    "s_mov_b64 exec, -1\n\t" "v_mfma_f32_16x16x32_bf16 v[204:207], a[128:131], a[108:111], v[204:207]\n\t" "global_load_dwordx4 a[76:79], %[va], s[76:77] offset:3072 sc1\n\t"
    "v_mfma_f32_16x16x32_bf16 v[204:207], a[132:135], a[124:127], v[204:207]\n\t" "s_add_u32 s76, s76, 16384\n\t" "s_addc_u32 s77, s77, 0\n\t" "s_waitcnt lgkmcnt(4)\n\t"
    "v_mfma_f32_16x16x32_bf16 v[208:211], a[136:139], a[96:99], v[208:211]\n\t" "ds_read_b64_tr_b16 a[152:153], %[vr7] offset:7168\n\t"
    "v_mfma_f32_16x16x32_bf16 v[208:211], a[140:143], a[112:115], v[208:211]\n\t" "ds_read_b64_tr_b16 a[154:155], %[vr7] offset:10752\n\t"
    "v_mfma_f32_16x16x32_bf16 v[212:215], a[136:139], a[100:103], v[212:215]\n\t" "v_mfma_f32_16x16x32_bf16 v[212:215], a[140:143], a[116:119], v[212:215]\n\t"
    "v_mfma_f32_16x16x32_bf16 v[216:219], a[136:139], a[104:107], v[216:219]\n\t" "ds_read_b64_tr_b16 a[156:157], %[vr7] offset:21504\n\t"
    "v_mfma_f32_16x16x32_bf16 v[216:219], a[140:143], a[120:123], v[216:219]\n\t" "ds_read_b64_tr_b16 a[158:159], %[vr7] offset:25088\n\t" "s_add_u32 m0, %[sl], 44032\n\t"
    "v_mfma_f32_16x16x32_bf16 v[220:223], a[136:139], a[108:111], v[220:223]\n\t" "global_load_lds_dwordx4 %[vb1], s[78:79]\n\t"
    "v_mfma_f32_16x16x32_bf16 v[220:223], a[140:143], a[124:127], v[220:223]\n\t" "s_waitcnt lgkmcnt(4)\n\t" "v_mfma_f32_16x16x32_bf16 v[224:227], a[144:147], a[96:99], v[224:227]\n\t"
    "s_waitcnt vmcnt(14)\n\t" "s_barrier\n\t" "ds_read_b64_tr_b16 a[128:129], %[vr1] offset:28672\n\t" "v_mfma_f32_16x16x32_bf16 v[224:227], a[148:151], a[112:115], v[224:227]\n\t"
    "ds_read_b64_tr_b16 a[130:131], %[vr1] offset:32256\n\t" "v_mfma_f32_16x16x32_bf16 v[228:231], a[144:147], a[100:103], v[228:231]\n\t" "global_load_dwordx4 a[80:83], %[va], s[76:77] offset:0 sc1\n\t"
    "v_mfma_f32_16x16x32_bf16 v[228:231], a[148:151], a[116:119], v[228:231]\n\t" "v_mfma_f32_16x16x32_bf16 v[232:235], a[144:147], a[104:107], v[232:235]\n\t"
    "ds_read_b64_tr_b16 a[132:133], %[vr1] offset:43008\n\t" "v_mfma_f32_16x16x32_bf16 v[232:235], a[148:151], a[120:123], v[232:235]\n\t" "ds_read_b64_tr_b16 a[134:135], %[vr1] offset:46592\n\t"
    "s_add_u32 m0, %[sl], 45056\n\t" "v_mfma_f32_16x16x32_bf16 v[236:239], a[144:147], a[108:111], v[236:239]\n\t" "global_load_lds_dwordx4 %[vb2], s[78:79]\n\t"
    "v_mfma_f32_16x16x32_bf16 v[236:239], a[148:151], a[124:127], v[236:239]\n\t" "s_waitcnt lgkmcnt(4)\n\t" "v_mfma_f32_16x16x32_bf16 v[240:243], a[152:155], a[96:99], v[240:243]\n\t"
    "ds_read_b64_tr_b16 a[136:137], %[vr1] offset:28704\n\t" "v_mfma_f32_16x16x32_bf16 v[240:243], a[156:159], a[112:115], v[240:243]\n\t" "ds_read_b64_tr_b16 a[138:139], %[vr1] offset:32288\n\t"
    "v_mfma_f32_16x16x32_bf16 v[244:247], a[152:155], a[100:103], v[244:247]\n\t" "global_load_dword a231, %[va], %[cd]\n\t" "v_mfma_f32_16x16x32_bf16 v[244:247], a[156:159], a[116:119], v[244:247]\n\t"
    "v_mfma_f32_16x16x32_bf16 v[248:251], a[152:155], a[104:107], v[248:251]\n\t" "ds_read_b64_tr_b16 a[140:141], %[vr1] offset:43040\n\t"
    "v_mfma_f32_16x16x32_bf16 v[248:251], a[156:159], a[120:123], v[248:251]\n\t" "ds_read_b64_tr_b16 a[142:143], %[vr1] offset:46624\n\t"
    "v_mfma_f32_16x16x32_bf16 v[252:255], a[152:155], a[108:111], v[252:255]\n\t" "v_mfma_f32_16x16x32_bf16 v[252:255], a[156:159], a[124:127], v[252:255]\n\t" "s_waitcnt lgkmcnt(4)\n\t"
    "v_mfma_f32_16x16x32_bf16 v[32:35], a[128:131], a[0:3], v[32:35]\n\t" "s_barrier\n\t" "ds_read_b64_tr_b16 a[144:145], %[vr3] offset:28672\n\t" "v_add_u32_e64 v31, %[sld], %[va]\n\t"
    "v_mfma_f32_16x16x32_bf16 v[32:35], a[132:135], a[16:19], v[32:35]\n\t" "ds_read_b64_tr_b16 a[146:147], %[vr3] offset:32256\n\t" "v_mfma_f32_16x16x32_bf16 v[36:39], a[128:131], a[4:7], v[36:39]\n\t"
    "global_load_dwordx4 a[84:87], %[va], s[76:77] offset:1024 sc1\n\t" "ds_read_b128 a[232:235], v31 offset:24576\n\t" "v_mfma_f32_16x16x32_bf16 v[36:39], a[132:135], a[20:23], v[36:39]\n\t"
    "ds_read_b128 a[236:239], v31 offset:28672\n\t" "ds_read_b128 a[240:243], v31 offset:32768\n\t" "v_mfma_f32_16x16x32_bf16 v[40:43], a[128:131], a[8:11], v[40:43]\n\t"
    "ds_read_b64_tr_b16 a[148:149], %[vr3] offset:43008\n\t" "ds_read_b128 a[244:247], v31 offset:36864\n\t" "v_mfma_f32_16x16x32_bf16 v[40:43], a[132:135], a[24:27], v[40:43]\n\t"
    "ds_read_b64_tr_b16 a[150:151], %[vr3] offset:46592\n\t" "v_accvgpr_read_b32 v31, %[cr0]\n\t" "s_add_u32 m0, %[sl], 46080\n\t" "v_mfma_f32_16x16x32_bf16 v[44:47], a[128:131], a[12:15], v[44:47]\n\t"
    "s_mov_b64 exec, s[82:83]\n\t" "global_load_lds_dwordx4 %[vb3], s[78:79]\n\t" "s_mov_b64 exec, -1\n\t" "ds_read2_b32 v[24:25], v31 offset0:0 offset1:4\n\t"
    "v_mfma_f32_16x16x32_bf16 v[44:47], a[132:135], a[28:31], v[44:47]\n\t" "s_add_u32 s78, s78, 229376\n\t" "s_addc_u32 s79, s79, 0\n\t" "ds_read2_b32 v[26:27], v31 offset0:8 offset1:12\n\t"
    "ds_read2_b32 v[28:29], v31 offset0:16 offset1:20\n\t" "s_waitcnt lgkmcnt(11)\n\t" "v_mfma_f32_16x16x32_bf16 v[48:51], a[136:139], a[0:3], v[48:51]\n\t"
    "ds_read_b64_tr_b16 a[152:153], %[vr3] offset:28704\n\t" "ds_read2_b32 v[30:31], v31 offset0:24 offset1:28\n\t" "v_mfma_f32_16x16x32_bf16 v[48:51], a[140:143], a[16:19], v[48:51]\n\t"
    "ds_read_b64_tr_b16 a[154:155], %[vr3] offset:32288\n\t" "v_mfma_f32_16x16x32_bf16 v[52:55], a[136:139], a[4:7], v[52:55]\n\t" "global_load_dwordx4 a[88:91], %[va], s[76:77] offset:2048 sc1\n\t"
    "v_mfma_f32_16x16x32_bf16 v[52:55], a[140:143], a[20:23], v[52:55]\n\t" "v_mfma_f32_16x16x32_bf16 v[56:59], a[136:139], a[8:11], v[56:59]\n\t" "ds_read_b64_tr_b16 a[156:157], %[vr3] offset:43040\n\t"
    "v_mfma_f32_16x16x32_bf16 v[56:59], a[140:143], a[24:27], v[56:59]\n\t" "ds_read_b64_tr_b16 a[158:159], %[vr3] offset:46624\n\t" "v_mfma_f32_16x16x32_bf16 v[60:63], a[136:139], a[12:15], v[60:63]\n\t"
    "global_load_dwordx4 a[92:95], %[va], s[76:77] offset:3072 sc1\n\t" "v_mfma_f32_16x16x32_bf16 v[60:63], a[140:143], a[28:31], v[60:63]\n\t" "s_add_u32 s76, s76, 16384\n\t" "s_addc_u32 s77, s77, 0\n\t"
    "s_waitcnt lgkmcnt(3)\n\t" "v_add_f32_e64 v24, v24, v25\n\t" "v_mfma_f32_16x16x32_bf16 v[64:67], a[144:147], a[0:3], v[64:67]\n\t" "ds_read_b64_tr_b16 a[128:129], %[vr5] offset:28672\n\t"
    "v_add_f32_e64 v26, v26, v27\n\t" "v_mfma_f32_16x16x32_bf16 v[64:67], a[148:151], a[16:19], v[64:67]\n\t" "ds_read_b64_tr_b16 a[130:131], %[vr5] offset:32256\n\t" "v_add_f32 v28, v28, v29\n\t"
    "v_add_f32 v30, v30, v31\n\t" "v_mfma_f32_16x16x32_bf16 v[68:71], a[144:147], a[4:7], v[68:71]\n\t" "v_add_f32_e64 v24, v24, v26\n\t"
    "v_mfma_f32_16x16x32_bf16 v[68:71], a[148:151], a[20:23], v[68:71]\n\t" "v_add_f32_e64 v28, v28, v30\n\t" "v_mfma_f32_16x16x32_bf16 v[72:75], a[144:147], a[8:11], v[72:75]\n\t"
    "ds_read_b64_tr_b16 a[132:133], %[vr5] offset:43008\n\t" "v_add_f32_e64 v24, v24, v28\n\t" "v_mfma_f32_16x16x32_bf16 v[72:75], a[148:151], a[24:27], v[72:75]\n\t"
    "ds_read_b64_tr_b16 a[134:135], %[vr5] offset:46592\n\t" "s_add_u32 m0, %[sl], 57344\n\t" "v_mfma_f32_16x16x32_bf16 v[76:79], a[144:147], a[12:15], v[76:79]\n\t"
    "global_load_lds_dwordx4 %[vb0], s[78:79]\n\t" "v_cmp_u_f32_e64 vcc, v24, v24\n\t" "v_mfma_f32_16x16x32_bf16 v[76:79], a[148:151], a[28:31], v[76:79]\n\t" "s_waitcnt lgkmcnt(4)\n\t"
    "v_mfma_f32_16x16x32_bf16 v[80:83], a[152:155], a[0:3], v[80:83]\n\t" "ds_read_b64_tr_b16 a[136:137], %[vr5] offset:28704\n\t" "v_mfma_f32_16x16x32_bf16 v[80:83], a[156:159], a[16:19], v[80:83]\n\t"
    "ds_read_b64_tr_b16 a[138:139], %[vr5] offset:32288\n\t" "v_mfma_f32_16x16x32_bf16 v[84:87], a[152:155], a[4:7], v[84:87]\n\t" "global_load_dwordx4 a[96:99], %[va], s[76:77] offset:0 sc1\n\t"
    "v_mfma_f32_16x16x32_bf16 v[84:87], a[156:159], a[20:23], v[84:87]\n\t" "s_nop 0\n\t" "s_cbranch_vccnz .Lslow0_%=\n\t" ".Lgo0_%=:\n\t" "v_accvgpr_read_b32 v27, %[cw]\n\t"
    "v_mfma_f32_16x16x32_bf16 v[88:91], a[152:155], a[8:11], v[88:91]\n\t" "ds_read_b64_tr_b16 a[140:141], %[vr5] offset:43040\n\t" "v_and_b32 v25, 0xffff0000, v24\n\t"
    "v_mfma_f32_16x16x32_bf16 v[88:91], a[156:159], a[24:27], v[88:91]\n\t" "ds_read_b64_tr_b16 a[142:143], %[vr5] offset:46624\n\t" "s_add_u32 m0, %[sl], 58368\n\t"
    "v_mfma_f32_16x16x32_bf16 v[92:95], a[152:155], a[12:15], v[92:95]\n\t" "global_load_lds_dwordx4 %[vb1], s[78:79]\n\t" "v_add_u32 v27, 0x0, v27\n\t"
    "v_mfma_f32_16x16x32_bf16 v[92:95], a[156:159], a[28:31], v[92:95]\n\t" "v_sub_f32 v24, v24, v25\n\t" "s_waitcnt lgkmcnt(4)\n\t" "v_mfma_f32_16x16x32_bf16 v[96:99], a[128:131], a[0:3], v[96:99]\n\t"
    "ds_read_b64_tr_b16 a[144:145], %[vr7] offset:28672\n\t" "v_cvt_pk_bf16_f32 v24, v25, v24\n\t" "v_mfma_f32_16x16x32_bf16 v[96:99], a[132:135], a[16:19], v[96:99]\n\t"
    "ds_read_b64_tr_b16 a[146:147], %[vr7] offset:32256\n\t" "v_mfma_f32_16x16x32_bf16 v[100:103], a[128:131], a[4:7], v[100:103]\n\t" "ds_write2_b32 v27, v24, v24 offset0:0 offset1:1\n\t"
    "v_mfma_f32_16x16x32_bf16 v[100:103], a[132:135], a[20:23], v[100:103]\n\t" "v_accvgpr_read_b32 v31, %[cr1]\n\t" "v_mfma_f32_16x16x32_bf16 v[104:107], a[128:131], a[8:11], v[104:107]\n\t"
    "ds_read_b64_tr_b16 a[148:149], %[vr7] offset:43008\n\t" "ds_read2_b32 v[24:25], v31 offset0:0 offset1:4\n\t" "v_mfma_f32_16x16x32_bf16 v[104:107], a[132:135], a[24:27], v[104:107]\n\t"
    "ds_read_b64_tr_b16 a[150:151], %[vr7] offset:46592\n\t" "ds_read2_b32 v[26:27], v31 offset0:8 offset1:12\n\t" "ds_read2_b32 v[28:29], v31 offset0:16 offset1:20\n\t"
    "v_mfma_f32_16x16x32_bf16 v[108:111], a[128:131], a[12:15], v[108:111]\n\t" "global_load_dwordx4 a[100:103], %[va], s[76:77] offset:1024 sc1\n\t" "ds_read2_b32 v[30:31], v31 offset0:24 offset1:28\n\t"
    "v_mfma_f32_16x16x32_bf16 v[108:111], a[132:135], a[28:31], v[108:111]\n\t" "s_waitcnt lgkmcnt(9)\n\t" "v_mfma_f32_16x16x32_bf16 v[112:115], a[136:139], a[0:3], v[112:115]\n\t"
    "ds_read_b64_tr_b16 a[152:153], %[vr1] offset:35840\n\t" "v_mfma_f32_16x16x32_bf16 v[112:115], a[140:143], a[16:19], v[112:115]\n\t" "ds_read_b64_tr_b16 a[154:155], %[vr1] offset:39424\n\t"
    "v_mfma_f32_16x16x32_bf16 v[116:119], a[136:139], a[4:7], v[116:119]\n\t" "global_load_dwordx4 a[104:107], %[va], s[76:77] offset:2048 sc1\n\t"
    "v_mfma_f32_16x16x32_bf16 v[116:119], a[140:143], a[20:23], v[116:119]\n\t" "v_mfma_f32_16x16x32_bf16 v[120:123], a[136:139], a[8:11], v[120:123]\n\t"
    "ds_read_b64_tr_b16 a[156:157], %[vr1] offset:50176\n\t" "v_mfma_f32_16x16x32_bf16 v[120:123], a[140:143], a[24:27], v[120:123]\n\t" "ds_read_b64_tr_b16 a[158:159], %[vr1] offset:53760\n\t"
    "s_waitcnt lgkmcnt(4)\n\t" "v_add_f32_e64 v24, v24, v25\n\t" "s_add_u32 m0, %[sl], 59392\n\t" "v_mfma_f32_16x16x32_bf16 v[124:127], a[136:139], a[12:15], v[124:127]\n\t"
    "global_load_lds_dwordx4 %[vb2], s[78:79]\n\t" "v_add_f32_e64 v26, v26, v27\n\t" "v_mfma_f32_16x16x32_bf16 v[124:127], a[140:143], a[28:31], v[124:127]\n\t" "v_add_f32 v28, v28, v29\n\t"
    "v_add_f32 v30, v30, v31\n\t" "v_mfma_f32_16x16x32_bf16 v[128:131], a[144:147], a[0:3], v[128:131]\n\t" "ds_read_b64_tr_b16 a[128:129], %[vr1] offset:35872\n\t" "v_add_f32_e64 v24, v24, v26\n\t"
    "v_mfma_f32_16x16x32_bf16 v[128:131], a[148:151], a[16:19], v[128:131]\n\t" "ds_read_b64_tr_b16 a[130:131], %[vr1] offset:39456\n\t" "v_add_f32_e64 v28, v28, v30\n\t"
    "v_mfma_f32_16x16x32_bf16 v[132:135], a[144:147], a[4:7], v[132:135]\n\t" "v_add_f32_e64 v24, v24, v28\n\t" "v_mfma_f32_16x16x32_bf16 v[132:135], a[148:151], a[20:23], v[132:135]\n\t"
    "v_mfma_f32_16x16x32_bf16 v[136:139], a[144:147], a[8:11], v[136:139]\n\t" "ds_read_b64_tr_b16 a[132:133], %[vr1] offset:50208\n\t" "v_cmp_u_f32_e64 vcc, v24, v24\n\t"
    "v_mfma_f32_16x16x32_bf16 v[136:139], a[148:151], a[24:27], v[136:139]\n\t" "ds_read_b64_tr_b16 a[134:135], %[vr1] offset:53792\n\t"
    "v_mfma_f32_16x16x32_bf16 v[140:143], a[144:147], a[12:15], v[140:143]\n\t" "v_mfma_f32_16x16x32_bf16 v[140:143], a[148:151], a[28:31], v[140:143]\n\t" "s_waitcnt lgkmcnt(4)\n\t"
    "v_mfma_f32_16x16x32_bf16 v[144:147], a[152:155], a[0:3], v[144:147]\n\t" "ds_read_b64_tr_b16 a[136:137], %[vr3] offset:35840\n\t"
    "v_mfma_f32_16x16x32_bf16 v[144:147], a[156:159], a[16:19], v[144:147]\n\t" "ds_read_b64_tr_b16 a[138:139], %[vr3] offset:39424\n\t" "s_nop 0\n\t" "s_cbranch_vccnz .Lslow1_%=\n\t" ".Lgo1_%=:\n\t"
    "v_accvgpr_read_b32 v27, %[cw]\n\t" "v_mfma_f32_16x16x32_bf16 v[148:151], a[152:155], a[4:7], v[148:151]\n\t" "global_load_dwordx4 a[108:111], %[va], s[76:77] offset:3072 sc1\n\t"
    "v_and_b32 v25, 0xffff0000, v24\n\t" "v_mfma_f32_16x16x32_bf16 v[148:151], a[156:159], a[20:23], v[148:151]\n\t" "s_add_u32 s76, s76, 16384\n\t" "s_addc_u32 s77, s77, 0\n\t"
    "v_mfma_f32_16x16x32_bf16 v[152:155], a[152:155], a[8:11], v[152:155]\n\t" "ds_read_b64_tr_b16 a[140:141], %[vr3] offset:50176\n\t" "v_add_u32 v27, 0x800, v27\n\t"
    "v_mfma_f32_16x16x32_bf16 v[152:155], a[156:159], a[24:27], v[152:155]\n\t" "ds_read_b64_tr_b16 a[142:143], %[vr3] offset:53760\n\t" "v_sub_f32_e64 v24, v24, v25\n\t" "s_add_u32 m0, %[sl], 60416\n\t"
    "v_mfma_f32_16x16x32_bf16 v[156:159], a[152:155], a[12:15], v[156:159]\n\t" "s_mov_b64 exec, s[82:83]\n\t" "global_load_lds_dwordx4 %[vb3], s[78:79]\n\t" "s_mov_b64 exec, -1\n\t"
    "v_cvt_pk_bf16_f32 v24, v25, v24\n\t" "v_mfma_f32_16x16x32_bf16 v[156:159], a[156:159], a[28:31], v[156:159]\n\t" "s_add_u32 s78, s78, 229376\n\t" "s_addc_u32 s79, s79, 0\n\t"
    "s_waitcnt lgkmcnt(4)\n\t" "v_mfma_f32_16x16x32_bf16 v[160:163], a[128:131], a[0:3], v[160:163]\n\t" "ds_read_b64_tr_b16 a[144:145], %[vr3] offset:35872\n\t" "s_cmp_lg_u64 %[m01], 0\n\t"
    "s_cselect_b64 exec, -1, 0\n\t" "ds_write2_b32 v27, v24, v24 offset0:0 offset1:1\n\t" "s_mov_b64 exec, -1\n\t" "v_mfma_f32_16x16x32_bf16 v[160:163], a[132:135], a[16:19], v[160:163]\n\t"
    "ds_read_b64_tr_b16 a[146:147], %[vr3] offset:39456\n\t" "v_mfma_f32_16x16x32_bf16 v[164:167], a[128:131], a[4:7], v[164:167]\n\t" "global_load_dwordx4 a[112:115], %[va], s[76:77] offset:0 sc1\n\t"
    "v_mfma_f32_16x16x32_bf16 v[164:167], a[132:135], a[20:23], v[164:167]\n\t" "v_mfma_f32_16x16x32_bf16 v[168:171], a[128:131], a[8:11], v[168:171]\n\t"
    "ds_read_b64_tr_b16 a[148:149], %[vr3] offset:50208\n\t" "v_mfma_f32_16x16x32_bf16 v[168:171], a[132:135], a[24:27], v[168:171]\n\t" "ds_read_b64_tr_b16 a[150:151], %[vr3] offset:53792\n\t"
    "s_add_u32 m0, %[sl], 71680\n\t" "v_mfma_f32_16x16x32_bf16 v[172:175], a[128:131], a[12:15], v[172:175]\n\t" "global_load_lds_dwordx4 %[vb0], s[78:79]\n\t"
    "v_mfma_f32_16x16x32_bf16 v[172:175], a[132:135], a[28:31], v[172:175]\n\t" "s_waitcnt lgkmcnt(5)\n\t" "v_mfma_f32_16x16x32_bf16 v[176:179], a[136:139], a[0:3], v[176:179]\n\t"
    "ds_read_b64_tr_b16 a[152:153], %[vr5] offset:35840\n\t" "v_mfma_f32_16x16x32_bf16 v[176:179], a[140:143], a[16:19], v[176:179]\n\t" "ds_read_b64_tr_b16 a[154:155], %[vr5] offset:39424\n\t"
    "v_mfma_f32_16x16x32_bf16 v[180:183], a[136:139], a[4:7], v[180:183]\n\t" "v_mfma_f32_16x16x32_bf16 v[180:183], a[140:143], a[20:23], v[180:183]\n\t"
    "v_mfma_f32_16x16x32_bf16 v[184:187], a[136:139], a[8:11], v[184:187]\n\t" "ds_read_b64_tr_b16 a[156:157], %[vr5] offset:50176\n\t"
    "v_mfma_f32_16x16x32_bf16 v[184:187], a[140:143], a[24:27], v[184:187]\n\t" "ds_read_b64_tr_b16 a[158:159], %[vr5] offset:53760\n\t"
    "v_mfma_f32_16x16x32_bf16 v[188:191], a[136:139], a[12:15], v[188:191]\n\t" "global_load_dwordx4 a[116:119], %[va], s[76:77] offset:1024 sc1\n\t"
    "v_mfma_f32_16x16x32_bf16 v[188:191], a[140:143], a[28:31], v[188:191]\n\t" "s_waitcnt lgkmcnt(4)\n\t" "v_mfma_f32_16x16x32_bf16 v[192:195], a[144:147], a[0:3], v[192:195]\n\t"
    "ds_read_b64_tr_b16 a[128:129], %[vr5] offset:35872\n\t" "v_mfma_f32_16x16x32_bf16 v[192:195], a[148:151], a[16:19], v[192:195]\n\t" "ds_read_b64_tr_b16 a[130:131], %[vr5] offset:39456\n\t"
    "s_add_u32 m0, %[sl], 72704\n\t" "v_mfma_f32_16x16x32_bf16 v[196:199], a[144:147], a[4:7], v[196:199]\n\t" "global_load_lds_dwordx4 %[vb1], s[78:79]\n\t"
    "v_mfma_f32_16x16x32_bf16 v[196:199], a[148:151], a[20:23], v[196:199]\n\t" "v_mfma_f32_16x16x32_bf16 v[200:203], a[144:147], a[8:11], v[200:203]\n\t"
    "ds_read_b64_tr_b16 a[132:133], %[vr5] offset:50208\n\t" "v_mfma_f32_16x16x32_bf16 v[200:203], a[148:151], a[24:27], v[200:203]\n\t" "ds_read_b64_tr_b16 a[134:135], %[vr5] offset:53792\n\t"
    "v_mfma_f32_16x16x32_bf16 v[204:207], a[144:147], a[12:15], v[204:207]\n\t" "global_load_dwordx4 a[120:123], %[va], s[76:77] offset:2048 sc1\n\t"
    "v_mfma_f32_16x16x32_bf16 v[204:207], a[148:151], a[28:31], v[204:207]\n\t" "s_waitcnt lgkmcnt(4)\n\t" "v_mfma_f32_16x16x32_bf16 v[208:211], a[152:155], a[0:3], v[208:211]\n\t"
    "ds_read_b64_tr_b16 a[136:137], %[vr7] offset:35840\n\t" "v_mfma_f32_16x16x32_bf16 v[208:211], a[156:159], a[16:19], v[208:211]\n\t" "ds_read_b64_tr_b16 a[138:139], %[vr7] offset:39424\n\t"
    "v_mfma_f32_16x16x32_bf16 v[212:215], a[152:155], a[4:7], v[212:215]\n\t" "v_mfma_f32_16x16x32_bf16 v[212:215], a[156:159], a[20:23], v[212:215]\n\t"
    "v_mfma_f32_16x16x32_bf16 v[216:219], a[152:155], a[8:11], v[216:219]\n\t" "ds_read_b64_tr_b16 a[140:141], %[vr7] offset:50176\n\t"
    "v_mfma_f32_16x16x32_bf16 v[216:219], a[156:159], a[24:27], v[216:219]\n\t" "ds_read_b64_tr_b16 a[142:143], %[vr7] offset:53760\n\t" "s_add_u32 m0, %[sl], 73728\n\t"
    "v_mfma_f32_16x16x32_bf16 v[220:223], a[152:155], a[12:15], v[220:223]\n\t" "global_load_lds_dwordx4 %[vb2], s[78:79]\n\t" "v_mfma_f32_16x16x32_bf16 v[220:223], a[156:159], a[28:31], v[220:223]\n\t"
    "s_waitcnt lgkmcnt(4)\n\t" "v_mfma_f32_16x16x32_bf16 v[224:227], a[128:131], a[0:3], v[224:227]\n\t" "s_waitcnt vmcnt(14)\n\t" "s_barrier\n\t" "v_accvgpr_read_b32 v31, %[ct]\n\t"
    "s_mov_b64 exec, 0xffff\n\t" "v_add_u32_e64 v31, %[gt0], v31\n\t" "s_mov_b64 exec, -1\n\t" "ds_read_b128 a[144:147], v31 offset:0\n\t"
    "v_mfma_f32_16x16x32_bf16 v[224:227], a[132:135], a[16:19], v[224:227]\n\t" "v_mfma_f32_16x16x32_bf16 v[228:231], a[128:131], a[4:7], v[228:231]\n\t"
    "global_load_dwordx4 a[124:127], %[va], s[76:77] offset:3072 sc1\n\t" "v_mfma_f32_16x16x32_bf16 v[228:231], a[132:135], a[20:23], v[228:231]\n\t" "s_add_u32 s76, s76, s86\n\t"
    "s_addc_u32 s77, s77, s87\n\t" "v_mfma_f32_16x16x32_bf16 v[232:235], a[128:131], a[8:11], v[232:235]\n\t" "ds_read_b128 a[148:151], v31 offset:256\n\t"
    "v_mfma_f32_16x16x32_bf16 v[232:235], a[132:135], a[24:27], v[232:235]\n\t" "s_add_u32 m0, %[sl], 74752\n\t" "v_mfma_f32_16x16x32_bf16 v[236:239], a[128:131], a[12:15], v[236:239]\n\t"
    "s_mov_b64 exec, s[82:83]\n\t" "global_load_lds_dwordx4 %[vb3], s[78:79]\n\t" "s_mov_b64 exec, -1\n\t" "v_mfma_f32_16x16x32_bf16 v[236:239], a[132:135], a[28:31], v[236:239]\n\t"
    "s_add_u32 s78, s78, s88\n\t" "s_addc_u32 s79, s79, s89\n\t" "s_waitcnt lgkmcnt(2)\n\t" "v_mfma_f32_16x16x32_bf16 v[240:243], a[136:139], a[0:3], v[240:243]\n\t"
    "ds_read_b64_tr_b16 a[152:153], %[vr0] offset:0\n\t" "v_mfma_f32_16x16x32_bf16 v[240:243], a[140:143], a[16:19], v[240:243]\n\t" "ds_read_b64_tr_b16 a[154:155], %[vr0] offset:3584\n\t"
    "v_mfma_f32_16x16x32_bf16 v[244:247], a[136:139], a[4:7], v[244:247]\n\t" "v_mfma_f32_16x16x32_bf16 v[244:247], a[140:143], a[20:23], v[244:247]\n\t"
    "v_mfma_f32_16x16x32_bf16 v[248:251], a[136:139], a[8:11], v[248:251]\n\t" "ds_read_b64_tr_b16 a[156:157], %[vr0] offset:14336\n\t"
    "v_mfma_f32_16x16x32_bf16 v[248:251], a[140:143], a[24:27], v[248:251]\n\t" "ds_read_b64_tr_b16 a[158:159], %[vr0] offset:17920\n\t"
    "v_mfma_f32_16x16x32_bf16 v[252:255], a[136:139], a[12:15], v[252:255]\n\t" "v_mfma_f32_16x16x32_bf16 v[252:255], a[140:143], a[28:31], v[252:255]\n\t" "s_waitcnt lgkmcnt(4)\n\t"
    "v_mfma_f32_16x16x32_bf16 v[32:35], a[144:147], a[200:203], v[32:35]\n\t" "ds_read_b64_tr_b16 a[128:129], %[vr0] offset:7168\n\t"
    "v_mfma_f32_16x16x32_bf16 v[36:39], a[144:147], a[204:207], v[36:39]\n\t" "ds_read_b64_tr_b16 a[130:131], %[vr0] offset:10752\n\t"
    "v_mfma_f32_16x16x32_bf16 v[40:43], a[144:147], a[208:211], v[40:43]\n\t" "v_mfma_f32_16x16x32_bf16 v[44:47], a[144:147], a[212:215], v[44:47]\n\t"
    "v_mfma_f32_16x16x32_bf16 v[144:147], a[148:151], a[200:203], v[144:147]\n\t" "ds_read_b64_tr_b16 a[132:133], %[vr0] offset:21504\n\t"
    "v_mfma_f32_16x16x32_bf16 v[148:151], a[148:151], a[204:207], v[148:151]\n\t" "ds_read_b64_tr_b16 a[134:135], %[vr0] offset:25088\n\t"
    "v_mfma_f32_16x16x32_bf16 v[152:155], a[148:151], a[208:211], v[152:155]\n\t" "v_mfma_f32_16x16x32_bf16 v[156:159], a[148:151], a[212:215], v[156:159]\n\t" "s_waitcnt lgkmcnt(4)\n\t"
    "v_mfma_f32_16x16x32_bf16 v[32:35], a[152:155], a[32:35], v[32:35]\n\t" "ds_read_b64_tr_b16 a[136:137], %[vr0] offset:28672\n\t" "v_mfma_f32_16x16x32_bf16 v[32:35], a[156:159], a[48:51], v[32:35]\n\t"
    "ds_read_b64_tr_b16 a[138:139], %[vr0] offset:32256\n\t" "v_mfma_f32_16x16x32_bf16 v[36:39], a[152:155], a[36:39], v[36:39]\n\t" "v_mfma_f32_16x16x32_bf16 v[36:39], a[156:159], a[52:55], v[36:39]\n\t"
    "v_mfma_f32_16x16x32_bf16 v[40:43], a[152:155], a[40:43], v[40:43]\n\t" "ds_read_b64_tr_b16 a[140:141], %[vr0] offset:43008\n\t" "v_mfma_f32_16x16x32_bf16 v[40:43], a[156:159], a[56:59], v[40:43]\n\t"
    "ds_read_b64_tr_b16 a[142:143], %[vr0] offset:46592\n\t" "v_mfma_f32_16x16x32_bf16 v[44:47], a[152:155], a[44:47], v[44:47]\n\t" "v_mfma_f32_16x16x32_bf16 v[44:47], a[156:159], a[60:63], v[44:47]\n\t"
    "s_waitcnt lgkmcnt(4)\n\t" "v_mfma_f32_16x16x32_bf16 v[144:147], a[128:131], a[32:35], v[144:147]\n\t" "ds_read_b64_tr_b16 a[144:145], %[vr0] offset:35840\n\t"
    "v_mfma_f32_16x16x32_bf16 v[144:147], a[132:135], a[48:51], v[144:147]\n\t" "ds_read_b64_tr_b16 a[146:147], %[vr0] offset:39424\n\t"
    "v_mfma_f32_16x16x32_bf16 v[148:151], a[128:131], a[36:39], v[148:151]\n\t" "v_mfma_f32_16x16x32_bf16 v[148:151], a[132:135], a[52:55], v[148:151]\n\t"
    "v_mfma_f32_16x16x32_bf16 v[152:155], a[128:131], a[40:43], v[152:155]\n\t" "ds_read_b64_tr_b16 a[148:149], %[vr0] offset:50176\n\t"
    "v_mfma_f32_16x16x32_bf16 v[152:155], a[132:135], a[56:59], v[152:155]\n\t" "ds_read_b64_tr_b16 a[150:151], %[vr0] offset:53760\n\t"
    "v_mfma_f32_16x16x32_bf16 v[156:159], a[128:131], a[44:47], v[156:159]\n\t" "v_mfma_f32_16x16x32_bf16 v[156:159], a[132:135], a[60:63], v[156:159]\n\t" "s_waitcnt lgkmcnt(4)\n\t"
    "v_mfma_f32_16x16x32_bf16 v[32:35], a[136:139], a[64:67], v[32:35]\n\t" "s_waitcnt vmcnt(0)\n\t" "s_barrier\n\t" "v_lshlrev_b32 v30, 3, %[va]\n\t" "s_mov_b32 exec_hi, 0xffffff\n\t"
    "global_load_dword a230, v30, %[tp0]\n\t" "s_mov_b64 exec, -1\n\t" "ds_read_b64_tr_b16 a[152:153], %[vr1] offset:0\n\t" "v_mfma_f32_16x16x32_bf16 v[32:35], a[140:143], a[80:83], v[32:35]\n\t"
    "ds_read_b64_tr_b16 a[154:155], %[vr1] offset:3584\n\t" "v_mfma_f32_16x16x32_bf16 v[36:39], a[136:139], a[68:71], v[36:39]\n\t" "v_mfma_f32_16x16x32_bf16 v[36:39], a[140:143], a[84:87], v[36:39]\n\t"
    "v_mfma_f32_16x16x32_bf16 v[40:43], a[136:139], a[72:75], v[40:43]\n\t" "ds_read_b64_tr_b16 a[156:157], %[vr1] offset:14336\n\t" "v_mfma_f32_16x16x32_bf16 v[40:43], a[140:143], a[88:91], v[40:43]\n\t"
    "ds_read_b64_tr_b16 a[158:159], %[vr1] offset:17920\n\t" "v_mfma_f32_16x16x32_bf16 v[44:47], a[136:139], a[76:79], v[44:47]\n\t" "v_mfma_f32_16x16x32_bf16 v[44:47], a[140:143], a[92:95], v[44:47]\n\t"
    "s_waitcnt lgkmcnt(4)\n\t" "v_mfma_f32_16x16x32_bf16 v[144:147], a[144:147], a[64:67], v[144:147]\n\t" "ds_read_b64_tr_b16 a[128:129], %[vr1] offset:7168\n\t"
    "v_mfma_f32_16x16x32_bf16 v[144:147], a[148:151], a[80:83], v[144:147]\n\t" "ds_read_b64_tr_b16 a[130:131], %[vr1] offset:10752\n\t"
    "v_mfma_f32_16x16x32_bf16 v[148:151], a[144:147], a[68:71], v[148:151]\n\t" "v_mfma_f32_16x16x32_bf16 v[148:151], a[148:151], a[84:87], v[148:151]\n\t"
    "v_mfma_f32_16x16x32_bf16 v[152:155], a[144:147], a[72:75], v[152:155]\n\t" "ds_read_b64_tr_b16 a[132:133], %[vr1] offset:21504\n\t"
    "v_mfma_f32_16x16x32_bf16 v[152:155], a[148:151], a[88:91], v[152:155]\n\t" "ds_read_b64_tr_b16 a[134:135], %[vr1] offset:25088\n\t"
    "v_mfma_f32_16x16x32_bf16 v[156:159], a[144:147], a[76:79], v[156:159]\n\t" "v_mfma_f32_16x16x32_bf16 v[156:159], a[148:151], a[92:95], v[156:159]\n\t" "s_waitcnt lgkmcnt(4)\n\t"
    "v_mfma_f32_16x16x32_bf16 v[32:35], a[152:155], a[96:99], v[32:35]\n\t" "ds_read_b128 a[136:139], v31 offset:512\n\t" "v_mfma_f32_16x16x32_bf16 v[32:35], a[156:159], a[112:115], v[32:35]\n\t"
    "v_mfma_f32_16x16x32_bf16 v[36:39], a[152:155], a[100:103], v[36:39]\n\t" "v_mfma_f32_16x16x32_bf16 v[36:39], a[156:159], a[116:119], v[36:39]\n\t"
    "v_mfma_f32_16x16x32_bf16 v[40:43], a[152:155], a[104:107], v[40:43]\n\t" "ds_read_b128 a[140:143], v31 offset:768\n\t" "v_mfma_f32_16x16x32_bf16 v[40:43], a[156:159], a[120:123], v[40:43]\n\t"
    "v_mfma_f32_16x16x32_bf16 v[44:47], a[152:155], a[108:111], v[44:47]\n\t" "v_mfma_f32_16x16x32_bf16 v[44:47], a[156:159], a[124:127], v[44:47]\n\t" "s_waitcnt lgkmcnt(2)\n\t"
    "v_mfma_f32_16x16x32_bf16 v[144:147], a[128:131], a[96:99], v[144:147]\n\t" "ds_read_b64_tr_b16 a[144:145], %[vr0] offset:32\n\t"
    "v_mfma_f32_16x16x32_bf16 v[144:147], a[132:135], a[112:115], v[144:147]\n\t" "ds_read_b64_tr_b16 a[146:147], %[vr0] offset:3616\n\t"
    "v_mfma_f32_16x16x32_bf16 v[148:151], a[128:131], a[100:103], v[148:151]\n\t" "v_mfma_f32_16x16x32_bf16 v[148:151], a[132:135], a[116:119], v[148:151]\n\t"
    "v_mfma_f32_16x16x32_bf16 v[152:155], a[128:131], a[104:107], v[152:155]\n\t" "ds_read_b64_tr_b16 a[148:149], %[vr0] offset:14368\n\t"
    "v_mfma_f32_16x16x32_bf16 v[152:155], a[132:135], a[120:123], v[152:155]\n\t" "ds_read_b64_tr_b16 a[150:151], %[vr0] offset:17952\n\t"
    "v_mfma_f32_16x16x32_bf16 v[156:159], a[128:131], a[108:111], v[156:159]\n\t" "v_mfma_f32_16x16x32_bf16 v[156:159], a[132:135], a[124:127], v[156:159]\n\t" "s_waitcnt lgkmcnt(4)\n\t"
    "v_mfma_f32_16x16x32_bf16 v[48:51], a[136:139], a[200:203], v[48:51]\n\t" "ds_read_b64_tr_b16 a[152:153], %[vr0] offset:7200\n\t" "v_mul_f32 v32, v32, v16\n\t" "v_mul_f32_e64 v33, v33, v16\n\t"
    "v_mfma_f32_16x16x32_bf16 v[52:55], a[136:139], a[204:207], v[52:55]\n\t" "ds_read_b64_tr_b16 a[154:155], %[vr0] offset:10784\n\t" "v_mul_f32 v34, v34, v16\n\t" "v_mul_f32 v35, v35, v16\n\t"
    "v_mfma_f32_16x16x32_bf16 v[56:59], a[136:139], a[208:211], v[56:59]\n\t" "v_exp_f32_e64 v32, v32\n\t" "v_mfma_f32_16x16x32_bf16 v[60:63], a[136:139], a[212:215], v[60:63]\n\t"
    "v_exp_f32_e64 v33, v33\n\t" "v_mfma_f32_16x16x32_bf16 v[160:163], a[140:143], a[200:203], v[160:163]\n\t" "ds_read_b64_tr_b16 a[156:157], %[vr0] offset:21536\n\t" "v_exp_f32_e64 v34, v34\n\t"
    "v_mfma_f32_16x16x32_bf16 v[164:167], a[140:143], a[204:207], v[164:167]\n\t" "ds_read_b64_tr_b16 a[158:159], %[vr0] offset:25120\n\t" "v_exp_f32_e64 v35, v35\n\t"
    "v_mfma_f32_16x16x32_bf16 v[168:171], a[140:143], a[208:211], v[168:171]\n\t" "v_fma_f32 v32, v32, v20, v20\n\t" "v_fma_f32 v33, v33, v20, v20\n\t"
    "v_mfma_f32_16x16x32_bf16 v[172:175], a[140:143], a[212:215], v[172:175]\n\t" "v_fma_f32 v34, v34, v20, v20\n\t" "v_fma_f32 v35, v35, v20, v20\n\t" "s_waitcnt lgkmcnt(4)\n\t"
    "v_mfma_f32_16x16x32_bf16 v[48:51], a[144:147], a[32:35], v[48:51]\n\t" "ds_read_b64_tr_b16 a[128:129], %[vr0] offset:28704\n\t" "v_rcp_f32 v32, v32\n\t"
    "v_mfma_f32_16x16x32_bf16 v[48:51], a[148:151], a[48:51], v[48:51]\n\t" "ds_read_b64_tr_b16 a[130:131], %[vr0] offset:32288\n\t" "v_rcp_f32_e64 v33, v33\n\t"
    "v_mfma_f32_16x16x32_bf16 v[52:55], a[144:147], a[36:39], v[52:55]\n\t" "v_rcp_f32_e64 v34, v34\n\t" "v_mfma_f32_16x16x32_bf16 v[52:55], a[148:151], a[52:55], v[52:55]\n\t"
    "v_rcp_f32_e64 v35, v35\n\t" "v_mfma_f32_16x16x32_bf16 v[56:59], a[144:147], a[40:43], v[56:59]\n\t" "ds_read_b64_tr_b16 a[132:133], %[vr0] offset:43040\n\t" "v_mul_f32 v144, v144, v32\n\t"
    "v_mul_f32 v145, v145, v33\n\t" "v_mfma_f32_16x16x32_bf16 v[56:59], a[148:151], a[56:59], v[56:59]\n\t" "ds_read_b64_tr_b16 a[134:135], %[vr0] offset:46624\n\t" "v_mul_f32 v146, v146, v34\n\t"
    "v_mul_f32 v147, v147, v35\n\t" "v_mfma_f32_16x16x32_bf16 v[60:63], a[144:147], a[44:47], v[60:63]\n\t" "v_cvt_pk_bf16_f32 v32, v144, v145\n\t" "v_cvt_pk_bf16_f32 v33, v146, v147\n\t"
    "v_mfma_f32_16x16x32_bf16 v[60:63], a[148:151], a[60:63], v[60:63]\n\t" "v_mul_f32 v36, v36, v17\n\t" "v_mul_f32_e64 v37, v37, v17\n\t" "s_waitcnt lgkmcnt(4)\n\t"
    "v_mfma_f32_16x16x32_bf16 v[160:163], a[152:155], a[32:35], v[160:163]\n\t" "ds_read_b64_tr_b16 a[136:137], %[vr0] offset:35872\n\t" "v_mul_f32 v38, v38, v17\n\t" "v_mul_f32 v39, v39, v17\n\t"
    "v_mfma_f32_16x16x32_bf16 v[160:163], a[156:159], a[48:51], v[160:163]\n\t" "ds_read_b64_tr_b16 a[138:139], %[vr0] offset:39456\n\t" "v_exp_f32_e64 v36, v36\n\t"
    "v_mfma_f32_16x16x32_bf16 v[164:167], a[152:155], a[36:39], v[164:167]\n\t" "v_exp_f32_e64 v37, v37\n\t" "v_mfma_f32_16x16x32_bf16 v[164:167], a[156:159], a[52:55], v[164:167]\n\t"
    "v_exp_f32_e64 v38, v38\n\t" "v_mfma_f32_16x16x32_bf16 v[168:171], a[152:155], a[40:43], v[168:171]\n\t" "ds_read_b64_tr_b16 a[140:141], %[vr0] offset:50208\n\t" "v_exp_f32_e64 v39, v39\n\t"
    "v_mfma_f32_16x16x32_bf16 v[168:171], a[156:159], a[56:59], v[168:171]\n\t" "ds_read_b64_tr_b16 a[142:143], %[vr0] offset:53792\n\t" "v_fma_f32 v36, v36, v21, v21\n\t"
    "v_fma_f32 v37, v37, v21, v21\n\t" "v_mfma_f32_16x16x32_bf16 v[172:175], a[152:155], a[44:47], v[172:175]\n\t" "v_fma_f32 v38, v38, v21, v21\n\t" "v_fma_f32 v39, v39, v21, v21\n\t"
    "v_mfma_f32_16x16x32_bf16 v[172:175], a[156:159], a[60:63], v[172:175]\n\t" "v_rcp_f32 v36, v36\n\t" "s_waitcnt lgkmcnt(4)\n\t" "v_mfma_f32_16x16x32_bf16 v[48:51], a[128:131], a[64:67], v[48:51]\n\t"
    "ds_read_b64_tr_b16 a[144:145], %[vr1] offset:32\n\t" "v_rcp_f32_e64 v37, v37\n\t" "v_mfma_f32_16x16x32_bf16 v[48:51], a[132:135], a[80:83], v[48:51]\n\t"
    "ds_read_b64_tr_b16 a[146:147], %[vr1] offset:3616\n\t" "v_rcp_f32_e64 v38, v38\n\t" "v_mfma_f32_16x16x32_bf16 v[52:55], a[128:131], a[68:71], v[52:55]\n\t" "v_rcp_f32_e64 v39, v39\n\t"
    "v_mfma_f32_16x16x32_bf16 v[52:55], a[132:135], a[84:87], v[52:55]\n\t" "v_mul_f32 v148, v148, v36\n\t" "v_mul_f32 v149, v149, v37\n\t"
    "v_mfma_f32_16x16x32_bf16 v[56:59], a[128:131], a[72:75], v[56:59]\n\t" "ds_read_b64_tr_b16 a[148:149], %[vr1] offset:14368\n\t" "v_mul_f32 v150, v150, v38\n\t" "v_mul_f32 v151, v151, v39\n\t"
    "v_mfma_f32_16x16x32_bf16 v[56:59], a[132:135], a[88:91], v[56:59]\n\t" "ds_read_b64_tr_b16 a[150:151], %[vr1] offset:17952\n\t" "v_cvt_pk_bf16_f32 v36, v148, v149\n\t"
    "v_cvt_pk_bf16_f32 v37, v150, v151\n\t" "v_mfma_f32_16x16x32_bf16 v[60:63], a[128:131], a[76:79], v[60:63]\n\t" "v_mul_f32 v40, v40, v18\n\t" "v_mul_f32 v41, v41, v18\n\t"
    "v_mfma_f32_16x16x32_bf16 v[60:63], a[132:135], a[92:95], v[60:63]\n\t" "v_mul_f32 v42, v42, v18\n\t" "v_mul_f32_e64 v43, v43, v18\n\t" "s_waitcnt lgkmcnt(4)\n\t"
    "v_mfma_f32_16x16x32_bf16 v[160:163], a[136:139], a[64:67], v[160:163]\n\t" "ds_read_b64_tr_b16 a[152:153], %[vr1] offset:7200\n\t" "v_exp_f32_e64 v40, v40\n\t"
    "v_mfma_f32_16x16x32_bf16 v[160:163], a[140:143], a[80:83], v[160:163]\n\t" "ds_read_b64_tr_b16 a[154:155], %[vr1] offset:10784\n\t" "v_exp_f32_e64 v41, v41\n\t"
    "v_mfma_f32_16x16x32_bf16 v[164:167], a[136:139], a[68:71], v[164:167]\n\t" "v_exp_f32_e64 v42, v42\n\t" "v_mfma_f32_16x16x32_bf16 v[164:167], a[140:143], a[84:87], v[164:167]\n\t"
    "v_exp_f32_e64 v43, v43\n\t" "v_mfma_f32_16x16x32_bf16 v[168:171], a[136:139], a[72:75], v[168:171]\n\t" "ds_read_b64_tr_b16 a[156:157], %[vr1] offset:21536\n\t" "v_fma_f32 v40, v40, v22, v22\n\t"
    "v_fma_f32 v41, v41, v22, v22\n\t" "v_mfma_f32_16x16x32_bf16 v[168:171], a[140:143], a[88:91], v[168:171]\n\t" "ds_read_b64_tr_b16 a[158:159], %[vr1] offset:25120\n\t"
    "v_fma_f32 v42, v42, v22, v22\n\t" "v_fma_f32 v43, v43, v22, v22\n\t" "v_mfma_f32_16x16x32_bf16 v[172:175], a[136:139], a[76:79], v[172:175]\n\t" "v_rcp_f32_e64 v40, v40\n\t"
    "v_mfma_f32_16x16x32_bf16 v[172:175], a[140:143], a[92:95], v[172:175]\n\t" "v_rcp_f32 v41, v41\n\t" "s_waitcnt lgkmcnt(4)\n\t" "v_mfma_f32_16x16x32_bf16 v[48:51], a[144:147], a[96:99], v[48:51]\n\t"
    "v_accvgpr_read_b32 v31, %[ct]\n\t" "s_mov_b64 exec, 0xffff\n\t" "v_add_u32 v31, %[gt1], v31\n\t" "s_mov_b64 exec, -1\n\t" "ds_read_b128 a[128:131], v31 offset:0\n\t" "v_rcp_f32_e64 v42, v42\n\t"
    "v_mfma_f32_16x16x32_bf16 v[48:51], a[148:151], a[112:115], v[48:51]\n\t" "v_rcp_f32_e64 v43, v43\n\t" "v_mfma_f32_16x16x32_bf16 v[52:55], a[144:147], a[100:103], v[52:55]\n\t"
    "v_mul_f32 v152, v152, v40\n\t" "v_mul_f32 v153, v153, v41\n\t" "v_mfma_f32_16x16x32_bf16 v[52:55], a[148:151], a[116:119], v[52:55]\n\t" "v_mul_f32 v154, v154, v42\n\t"
    "v_mul_f32 v155, v155, v43\n\t" "v_mfma_f32_16x16x32_bf16 v[56:59], a[144:147], a[104:107], v[56:59]\n\t" "ds_read_b128 a[132:135], v31 offset:256\n\t" "v_cvt_pk_bf16_f32 v40, v152, v153\n\t"
    "v_cvt_pk_bf16_f32 v41, v154, v155\n\t" "v_mfma_f32_16x16x32_bf16 v[56:59], a[148:151], a[120:123], v[56:59]\n\t" "v_mul_f32 v44, v44, v19\n\t" "v_mul_f32 v45, v45, v19\n\t"
    "v_mfma_f32_16x16x32_bf16 v[60:63], a[144:147], a[108:111], v[60:63]\n\t" "v_mul_f32 v46, v46, v19\n\t" "v_mul_f32 v47, v47, v19\n\t"
    "v_mfma_f32_16x16x32_bf16 v[60:63], a[148:151], a[124:127], v[60:63]\n\t" "v_exp_f32 v44, v44\n\t" "s_waitcnt lgkmcnt(2)\n\t"
    "v_mfma_f32_16x16x32_bf16 v[160:163], a[152:155], a[96:99], v[160:163]\n\t" "ds_read_b64_tr_b16 a[136:137], %[vr2] offset:0\n\t" "v_exp_f32_e64 v45, v45\n\t"
    "v_mfma_f32_16x16x32_bf16 v[160:163], a[156:159], a[112:115], v[160:163]\n\t" "ds_read_b64_tr_b16 a[138:139], %[vr2] offset:3584\n\t" "v_exp_f32_e64 v46, v46\n\t"
    "v_mfma_f32_16x16x32_bf16 v[164:167], a[152:155], a[100:103], v[164:167]\n\t" "global_load_dword a231, %[va], %[cd]\n\t" "v_exp_f32_e64 v47, v47\n\t"
    "v_mfma_f32_16x16x32_bf16 v[164:167], a[156:159], a[116:119], v[164:167]\n\t" "v_fma_f32 v44, v44, v23, v23\n\t" "v_fma_f32 v45, v45, v23, v23\n\t"
    "v_mfma_f32_16x16x32_bf16 v[168:171], a[152:155], a[104:107], v[168:171]\n\t" "ds_read_b64_tr_b16 a[140:141], %[vr2] offset:14336\n\t" "v_fma_f32 v46, v46, v23, v23\n\t"
    "v_fma_f32 v47, v47, v23, v23\n\t" "v_mfma_f32_16x16x32_bf16 v[168:171], a[156:159], a[120:123], v[168:171]\n\t" "ds_read_b64_tr_b16 a[142:143], %[vr2] offset:17920\n\t" "v_rcp_f32_e64 v44, v44\n\t"
    "v_mfma_f32_16x16x32_bf16 v[172:175], a[152:155], a[108:111], v[172:175]\n\t" "v_rcp_f32_e64 v45, v45\n\t" "v_mfma_f32_16x16x32_bf16 v[172:175], a[156:159], a[124:127], v[172:175]\n\t"
    "v_rcp_f32 v46, v46\n\t" "s_waitcnt lgkmcnt(4)\n\t" "v_mfma_f32_16x16x32_bf16 v[64:67], a[128:131], a[200:203], v[64:67]\n\t" "ds_read_b64_tr_b16 a[144:145], %[vr2] offset:7168\n\t"
    "v_rcp_f32_e64 v47, v47\n\t" "v_mfma_f32_16x16x32_bf16 v[68:71], a[128:131], a[204:207], v[68:71]\n\t" "ds_read_b64_tr_b16 a[146:147], %[vr2] offset:10752\n\t" "v_mul_f32 v156, v156, v44\n\t"
    "v_mul_f32 v157, v157, v45\n\t" "v_mfma_f32_16x16x32_bf16 v[72:75], a[128:131], a[208:211], v[72:75]\n\t" "v_mul_f32 v158, v158, v46\n\t" "v_mul_f32 v159, v159, v47\n\t"
    "v_mfma_f32_16x16x32_bf16 v[76:79], a[128:131], a[212:215], v[76:79]\n\t" "v_cvt_pk_bf16_f32 v44, v156, v157\n\t" "v_cvt_pk_bf16_f32 v45, v158, v159\n\t"
    "v_mfma_f32_16x16x32_bf16 v[176:179], a[132:135], a[200:203], v[176:179]\n\t" "ds_read_b64_tr_b16 a[148:149], %[vr2] offset:21504\n\t" "s_add_u32 s90, %[shl], %[go0]\n\t"
    "s_addc_u32 s91, %[shh], 0\n\t" "s_add_u32 s92, s90, 229376\n\t" "s_addc_u32 s93, s91, 0\n\t" "v_mfma_f32_16x16x32_bf16 v[180:183], a[132:135], a[204:207], v[180:183]\n\t"
    "ds_read_b64_tr_b16 a[150:151], %[vr2] offset:25088\n\t" "v_mul_f32 v48, v48, v16\n\t" "v_mul_f32_e64 v49, v49, v16\n\t" "v_mfma_f32_16x16x32_bf16 v[184:187], a[132:135], a[208:211], v[184:187]\n\t"
    "v_mul_f32 v50, v50, v16\n\t" "v_mul_f32 v51, v51, v16\n\t" "v_mfma_f32_16x16x32_bf16 v[188:191], a[132:135], a[212:215], v[188:191]\n\t" "v_exp_f32 v48, v48\n\t" "s_waitcnt lgkmcnt(4)\n\t"
    "v_mfma_f32_16x16x32_bf16 v[64:67], a[136:139], a[32:35], v[64:67]\n\t" "ds_read_b64_tr_b16 a[152:153], %[vr2] offset:28672\n\t" "v_exp_f32_e64 v49, v49\n\t"
    "v_mfma_f32_16x16x32_bf16 v[64:67], a[140:143], a[48:51], v[64:67]\n\t" "ds_read_b64_tr_b16 a[154:155], %[vr2] offset:32256\n\t" "v_exp_f32_e64 v50, v50\n\t"
    "v_mfma_f32_16x16x32_bf16 v[68:71], a[136:139], a[36:39], v[68:71]\n\t" "v_exp_f32_e64 v51, v51\n\t" "v_mfma_f32_16x16x32_bf16 v[68:71], a[140:143], a[52:55], v[68:71]\n\t"
    "v_fma_f32 v48, v48, v20, v20\n\t" "v_fma_f32 v49, v49, v20, v20\n\t" "v_mfma_f32_16x16x32_bf16 v[72:75], a[136:139], a[40:43], v[72:75]\n\t" "ds_read_b64_tr_b16 a[156:157], %[vr2] offset:43008\n\t"
    "v_fma_f32 v50, v50, v20, v20\n\t" "v_fma_f32 v51, v51, v20, v20\n\t" "v_mfma_f32_16x16x32_bf16 v[72:75], a[140:143], a[56:59], v[72:75]\n\t" "ds_read_b64_tr_b16 a[158:159], %[vr2] offset:46592\n\t"
    "v_rcp_f32_e64 v48, v48\n\t" "v_mfma_f32_16x16x32_bf16 v[76:79], a[136:139], a[44:47], v[76:79]\n\t" "v_rcp_f32_e64 v49, v49\n\t"
    "v_mfma_f32_16x16x32_bf16 v[76:79], a[140:143], a[60:63], v[76:79]\n\t" "v_rcp_f32 v50, v50\n\t" "s_waitcnt lgkmcnt(4)\n\t" "v_mfma_f32_16x16x32_bf16 v[176:179], a[144:147], a[32:35], v[176:179]\n\t"
    "ds_read_b64_tr_b16 a[128:129], %[vr2] offset:35840\n\t" "v_rcp_f32_e64 v51, v51\n\t" "v_mfma_f32_16x16x32_bf16 v[176:179], a[148:151], a[48:51], v[176:179]\n\t"
    "ds_read_b64_tr_b16 a[130:131], %[vr2] offset:39424\n\t" "v_mul_f32 v160, v160, v48\n\t" "v_mul_f32 v161, v161, v49\n\t" "v_mfma_f32_16x16x32_bf16 v[180:183], a[144:147], a[36:39], v[180:183]\n\t"
    "v_mul_f32 v162, v162, v50\n\t" "v_mul_f32 v163, v163, v51\n\t" "v_mfma_f32_16x16x32_bf16 v[180:183], a[148:151], a[52:55], v[180:183]\n\t" "v_cvt_pk_bf16_f32 v34, v160, v161\n\t"
    "v_cvt_pk_bf16_f32 v35, v162, v163\n\t" "v_mfma_f32_16x16x32_bf16 v[184:187], a[144:147], a[40:43], v[184:187]\n\t" "ds_read_b64_tr_b16 a[132:133], %[vr2] offset:50176\n\t"
    "global_store_dwordx4 %[va], v[32:35], s[90:91] offset:0 sc1\n\t" "v_mul_f32_e64 v52, v52, v17\n\t" "v_mfma_f32_16x16x32_bf16 v[184:187], a[148:151], a[56:59], v[184:187]\n\t"
    "ds_read_b64_tr_b16 a[134:135], %[vr2] offset:53760\n\t" "v_mul_f32 v53, v53, v17\n\t" "v_mul_f32 v54, v54, v17\n\t" "v_mfma_f32_16x16x32_bf16 v[188:191], a[144:147], a[44:47], v[188:191]\n\t"
    "v_mul_f32 v55, v55, v17\n\t" "v_exp_f32 v52, v52\n\t" "v_mfma_f32_16x16x32_bf16 v[188:191], a[148:151], a[60:63], v[188:191]\n\t" "v_exp_f32 v53, v53\n\t" "s_waitcnt lgkmcnt(4)\n\t"
    "v_mfma_f32_16x16x32_bf16 v[64:67], a[152:155], a[64:67], v[64:67]\n\t" "ds_read_b64_tr_b16 a[136:137], %[vr3] offset:0\n\t" "v_exp_f32_e64 v54, v54\n\t"
    "v_mfma_f32_16x16x32_bf16 v[64:67], a[156:159], a[80:83], v[64:67]\n\t" "ds_read_b64_tr_b16 a[138:139], %[vr3] offset:3584\n\t" "v_exp_f32_e64 v55, v55\n\t"
    "v_mfma_f32_16x16x32_bf16 v[68:71], a[152:155], a[68:71], v[68:71]\n\t" "v_fma_f32 v52, v52, v21, v21\n\t" "v_fma_f32 v53, v53, v21, v21\n\t"
    "v_mfma_f32_16x16x32_bf16 v[68:71], a[156:159], a[84:87], v[68:71]\n\t" "v_fma_f32 v54, v54, v21, v21\n\t" "v_fma_f32 v55, v55, v21, v21\n\t"
    "v_mfma_f32_16x16x32_bf16 v[72:75], a[152:155], a[72:75], v[72:75]\n\t" "ds_read_b64_tr_b16 a[140:141], %[vr3] offset:14336\n\t" "v_rcp_f32_e64 v52, v52\n\t"
    "v_mfma_f32_16x16x32_bf16 v[72:75], a[156:159], a[88:91], v[72:75]\n\t" "ds_read_b64_tr_b16 a[142:143], %[vr3] offset:17920\n\t" "v_rcp_f32_e64 v53, v53\n\t"
    "v_mfma_f32_16x16x32_bf16 v[76:79], a[152:155], a[76:79], v[76:79]\n\t" "v_rcp_f32_e64 v54, v54\n\t" "v_mfma_f32_16x16x32_bf16 v[76:79], a[156:159], a[92:95], v[76:79]\n\t" "v_rcp_f32 v55, v55\n\t"
    "s_waitcnt lgkmcnt(4)\n\t" "v_mfma_f32_16x16x32_bf16 v[176:179], a[128:131], a[64:67], v[176:179]\n\t" "ds_read_b64_tr_b16 a[144:145], %[vr3] offset:7168\n\t" "v_mul_f32 v164, v164, v52\n\t"
    "v_mul_f32 v165, v165, v53\n\t" "v_mfma_f32_16x16x32_bf16 v[176:179], a[132:135], a[80:83], v[176:179]\n\t" "ds_read_b64_tr_b16 a[146:147], %[vr3] offset:10752\n\t" "v_mul_f32 v166, v166, v54\n\t"
    "v_mul_f32 v167, v167, v55\n\t" "v_mfma_f32_16x16x32_bf16 v[180:183], a[128:131], a[68:71], v[180:183]\n\t" "v_cvt_pk_bf16_f32 v38, v164, v165\n\t" "v_cvt_pk_bf16_f32 v39, v166, v167\n\t"
    "v_mfma_f32_16x16x32_bf16 v[180:183], a[132:135], a[84:87], v[180:183]\n\t" "global_store_dwordx4 %[va], v[36:39], s[90:91] offset:1024 sc1\n\t" "v_mul_f32_e64 v56, v56, v18\n\t"
    "v_mfma_f32_16x16x32_bf16 v[184:187], a[128:131], a[72:75], v[184:187]\n\t" "ds_read_b64_tr_b16 a[148:149], %[vr3] offset:21504\n\t" "v_mul_f32 v57, v57, v18\n\t" "v_mul_f32 v58, v58, v18\n\t"
    "v_mfma_f32_16x16x32_bf16 v[184:187], a[132:135], a[88:91], v[184:187]\n\t" "ds_read_b64_tr_b16 a[150:151], %[vr3] offset:25088\n\t" "v_mul_f32 v59, v59, v18\n\t" "v_exp_f32 v56, v56\n\t"
    "v_mfma_f32_16x16x32_bf16 v[188:191], a[128:131], a[76:79], v[188:191]\n\t" "v_exp_f32_e64 v57, v57\n\t" "v_mfma_f32_16x16x32_bf16 v[188:191], a[132:135], a[92:95], v[188:191]\n\t"
    "v_exp_f32 v58, v58\n\t" "s_waitcnt lgkmcnt(4)\n\t" "v_mfma_f32_16x16x32_bf16 v[64:67], a[136:139], a[96:99], v[64:67]\n\t" "ds_read_b128 a[152:155], v31 offset:512\n\t" "v_exp_f32_e64 v59, v59\n\t"
    "v_mfma_f32_16x16x32_bf16 v[64:67], a[140:143], a[112:115], v[64:67]\n\t" "v_fma_f32 v56, v56, v22, v22\n\t" "v_fma_f32 v57, v57, v22, v22\n\t"
    "v_mfma_f32_16x16x32_bf16 v[68:71], a[136:139], a[100:103], v[68:71]\n\t" "v_fma_f32 v58, v58, v22, v22\n\t" "v_fma_f32 v59, v59, v22, v22\n\t"
    "v_mfma_f32_16x16x32_bf16 v[68:71], a[140:143], a[116:119], v[68:71]\n\t" "v_rcp_f32_e64 v56, v56\n\t" "v_mfma_f32_16x16x32_bf16 v[72:75], a[136:139], a[104:107], v[72:75]\n\t"
    "ds_read_b128 a[156:159], v31 offset:768\n\t" "v_rcp_f32_e64 v57, v57\n\t" "v_mfma_f32_16x16x32_bf16 v[72:75], a[140:143], a[120:123], v[72:75]\n\t" "v_rcp_f32_e64 v58, v58\n\t"
    "v_mfma_f32_16x16x32_bf16 v[76:79], a[136:139], a[108:111], v[76:79]\n\t" "v_rcp_f32_e64 v59, v59\n\t" "v_mfma_f32_16x16x32_bf16 v[76:79], a[140:143], a[124:127], v[76:79]\n\t"
    "v_mul_f32 v168, v168, v56\n\t" "v_mul_f32_e64 v169, v169, v57\n\t" "s_waitcnt lgkmcnt(2)\n\t" "v_mfma_f32_16x16x32_bf16 v[176:179], a[144:147], a[96:99], v[176:179]\n\t"
    "ds_read_b64_tr_b16 a[128:129], %[vr2] offset:32\n\t" "v_mul_f32 v170, v170, v58\n\t" "v_mul_f32 v171, v171, v59\n\t" "v_mfma_f32_16x16x32_bf16 v[176:179], a[148:151], a[112:115], v[176:179]\n\t"
    "ds_read_b64_tr_b16 a[130:131], %[vr2] offset:3616\n\t" "v_cvt_pk_bf16_f32 v42, v168, v169\n\t" "v_cvt_pk_bf16_f32 v43, v170, v171\n\t"
    "v_mfma_f32_16x16x32_bf16 v[180:183], a[144:147], a[100:103], v[180:183]\n\t" "global_store_dwordx4 %[va], v[40:43], s[92:93] offset:0 sc1\n\t" "v_mul_f32_e64 v60, v60, v19\n\t"
    "v_mfma_f32_16x16x32_bf16 v[180:183], a[148:151], a[116:119], v[180:183]\n\t" "v_mul_f32 v61, v61, v19\n\t" "v_mul_f32 v62, v62, v19\n\t"
    "v_mfma_f32_16x16x32_bf16 v[184:187], a[144:147], a[104:107], v[184:187]\n\t" "ds_read_b64_tr_b16 a[132:133], %[vr2] offset:14368\n\t" "v_mul_f32 v63, v63, v19\n\t" "v_exp_f32 v60, v60\n\t"
    "v_mfma_f32_16x16x32_bf16 v[184:187], a[148:151], a[120:123], v[184:187]\n\t" "ds_read_b64_tr_b16 a[134:135], %[vr2] offset:17952\n\t" "v_exp_f32_e64 v61, v61\n\t"
    "v_mfma_f32_16x16x32_bf16 v[188:191], a[144:147], a[108:111], v[188:191]\n\t" "v_exp_f32_e64 v62, v62\n\t" "v_mfma_f32_16x16x32_bf16 v[188:191], a[148:151], a[124:127], v[188:191]\n\t"
    "v_exp_f32 v63, v63\n\t" "s_waitcnt lgkmcnt(4)\n\t" "v_mfma_f32_16x16x32_bf16 v[80:83], a[152:155], a[200:203], v[80:83]\n\t" "ds_read_b64_tr_b16 a[136:137], %[vr2] offset:7200\n\t"
    "v_fma_f32 v60, v60, v23, v23\n\t" "v_fma_f32 v61, v61, v23, v23\n\t" "v_mfma_f32_16x16x32_bf16 v[84:87], a[152:155], a[204:207], v[84:87]\n\t" "ds_read_b64_tr_b16 a[138:139], %[vr2] offset:10784\n\t"
    "v_fma_f32 v62, v62, v23, v23\n\t" "v_fma_f32 v63, v63, v23, v23\n\t" "v_mfma_f32_16x16x32_bf16 v[88:91], a[152:155], a[208:211], v[88:91]\n\t" "v_rcp_f32_e64 v60, v60\n\t"
    "v_mfma_f32_16x16x32_bf16 v[92:95], a[152:155], a[212:215], v[92:95]\n\t" "v_rcp_f32_e64 v61, v61\n\t" "v_mfma_f32_16x16x32_bf16 v[192:195], a[156:159], a[200:203], v[192:195]\n\t"
    "ds_read_b64_tr_b16 a[140:141], %[vr2] offset:21536\n\t" "v_rcp_f32_e64 v62, v62\n\t" "v_mfma_f32_16x16x32_bf16 v[196:199], a[156:159], a[204:207], v[196:199]\n\t"
    "ds_read_b64_tr_b16 a[142:143], %[vr2] offset:25120\n\t" "v_rcp_f32_e64 v63, v63\n\t" "v_mfma_f32_16x16x32_bf16 v[200:203], a[156:159], a[208:211], v[200:203]\n\t" "v_accvgpr_read_b32 v29, %[vbd]\n\t"
    "s_nop 0\n\t" "s_add_u32 m0, %[sld], 24576\n\t" "s_nop 0\n\t" "global_load_lds_dwordx4 v29, %[sbw] offset:0\n\t" "v_mul_f32 v172, v172, v60\n\t" "v_mul_f32 v173, v173, v61\n\t"
    "v_mfma_f32_16x16x32_bf16 v[204:207], a[156:159], a[212:215], v[204:207]\n\t" "v_mul_f32 v174, v174, v62\n\t" "v_mul_f32_e64 v175, v175, v63\n\t" "s_waitcnt lgkmcnt(4)\n\t"
    "v_mfma_f32_16x16x32_bf16 v[80:83], a[128:131], a[32:35], v[80:83]\n\t" "ds_read_b64_tr_b16 a[144:145], %[vr2] offset:28704\n\t" "v_cvt_pk_bf16_f32 v46, v172, v173\n\t"
    "v_cvt_pk_bf16_f32 v47, v174, v175\n\t" "v_mfma_f32_16x16x32_bf16 v[80:83], a[132:135], a[48:51], v[80:83]\n\t" "ds_read_b64_tr_b16 a[146:147], %[vr2] offset:32288\n\t"
    "global_store_dwordx4 %[va], v[44:47], s[92:93] offset:1024 sc1\n\t" "v_mul_f32_e64 v64, v64, v16\n\t" "v_mfma_f32_16x16x32_bf16 v[84:87], a[128:131], a[36:39], v[84:87]\n\t"
    "v_mul_f32 v65, v65, v16\n\t" "v_mul_f32 v66, v66, v16\n\t" "v_mfma_f32_16x16x32_bf16 v[84:87], a[132:135], a[52:55], v[84:87]\n\t" "v_mul_f32 v67, v67, v16\n\t" "v_exp_f32 v64, v64\n\t"
    "v_mfma_f32_16x16x32_bf16 v[88:91], a[128:131], a[40:43], v[88:91]\n\t" "ds_read_b64_tr_b16 a[148:149], %[vr2] offset:43040\n\t" "v_exp_f32_e64 v65, v65\n\t"
    "v_mfma_f32_16x16x32_bf16 v[88:91], a[132:135], a[56:59], v[88:91]\n\t" "ds_read_b64_tr_b16 a[150:151], %[vr2] offset:46624\n\t" "v_exp_f32_e64 v66, v66\n\t"
    "v_mfma_f32_16x16x32_bf16 v[92:95], a[128:131], a[44:47], v[92:95]\n\t" "s_add_u32 m0, %[sld], 28544\n\t" "s_nop 0\n\t" "global_load_lds_dwordx4 v29, %[sbw] offset:128\n\t"
    "v_add_u32 v29, 0x10000, v29\n\t" "v_exp_f32 v67, v67\n\t" "v_mfma_f32_16x16x32_bf16 v[92:95], a[132:135], a[60:63], v[92:95]\n\t" "v_fma_f32 v64, v64, v20, v20\n\t" "v_fma_f32 v65, v65, v20, v20\n\t"
    "s_waitcnt lgkmcnt(4)\n\t" "v_mfma_f32_16x16x32_bf16 v[192:195], a[136:139], a[32:35], v[192:195]\n\t" "ds_read_b64_tr_b16 a[152:153], %[vr2] offset:35872\n\t" "v_fma_f32 v66, v66, v20, v20\n\t"
    "v_fma_f32 v67, v67, v20, v20\n\t" "v_mfma_f32_16x16x32_bf16 v[192:195], a[140:143], a[48:51], v[192:195]\n\t" "ds_read_b64_tr_b16 a[154:155], %[vr2] offset:39456\n\t" "v_rcp_f32 v64, v64\n\t"
    "v_mfma_f32_16x16x32_bf16 v[196:199], a[136:139], a[36:39], v[196:199]\n\t" "v_rcp_f32_e64 v65, v65\n\t" "v_mfma_f32_16x16x32_bf16 v[196:199], a[140:143], a[52:55], v[196:199]\n\t"
    "v_rcp_f32_e64 v66, v66\n\t" "v_mfma_f32_16x16x32_bf16 v[200:203], a[136:139], a[40:43], v[200:203]\n\t" "ds_read_b64_tr_b16 a[156:157], %[vr2] offset:50208\n\t" "v_rcp_f32_e64 v67, v67\n\t"
    "v_mfma_f32_16x16x32_bf16 v[200:203], a[140:143], a[56:59], v[200:203]\n\t" "ds_read_b64_tr_b16 a[158:159], %[vr2] offset:53792\n\t" "v_mul_f32 v176, v176, v64\n\t" "v_mul_f32 v177, v177, v65\n\t"
    "v_mfma_f32_16x16x32_bf16 v[204:207], a[136:139], a[44:47], v[204:207]\n\t" "s_add_u32 m0, %[sld], 32768\n\t" "s_nop 0\n\t" "global_load_lds_dwordx4 v29, %[sbw] offset:0\n\t"
    "v_mul_f32 v178, v178, v66\n\t" "v_mul_f32_e64 v179, v179, v67\n\t" "v_mfma_f32_16x16x32_bf16 v[204:207], a[140:143], a[60:63], v[204:207]\n\t" "v_cvt_pk_bf16_f32 v64, v176, v177\n\t"
    "v_cvt_pk_bf16_f32 v65, v178, v179\n\t" "s_waitcnt lgkmcnt(4)\n\t" "v_mfma_f32_16x16x32_bf16 v[80:83], a[144:147], a[64:67], v[80:83]\n\t" "ds_read_b64_tr_b16 a[128:129], %[vr3] offset:32\n\t"
    "v_mul_f32 v68, v68, v17\n\t" "v_mul_f32_e64 v69, v69, v17\n\t" "v_mfma_f32_16x16x32_bf16 v[80:83], a[148:151], a[80:83], v[80:83]\n\t" "ds_read_b64_tr_b16 a[130:131], %[vr3] offset:3616\n\t"
    "v_mul_f32 v70, v70, v17\n\t" "v_mul_f32 v71, v71, v17\n\t" "v_mfma_f32_16x16x32_bf16 v[84:87], a[144:147], a[68:71], v[84:87]\n\t" "v_exp_f32_e64 v68, v68\n\t"
    "v_mfma_f32_16x16x32_bf16 v[84:87], a[148:151], a[84:87], v[84:87]\n\t" "v_exp_f32_e64 v69, v69\n\t" "v_mfma_f32_16x16x32_bf16 v[88:91], a[144:147], a[72:75], v[88:91]\n\t"
    "ds_read_b64_tr_b16 a[132:133], %[vr3] offset:14368\n\t" "v_exp_f32_e64 v70, v70\n\t" "v_mfma_f32_16x16x32_bf16 v[88:91], a[148:151], a[88:91], v[88:91]\n\t"
    "ds_read_b64_tr_b16 a[134:135], %[vr3] offset:17952\n\t" "v_exp_f32_e64 v71, v71\n\t" "v_mfma_f32_16x16x32_bf16 v[92:95], a[144:147], a[76:79], v[92:95]\n\t" "s_add_u32 m0, %[sld], 36736\n\t"
    "s_nop 0\n\t" "global_load_lds_dwordx4 v29, %[sbw] offset:128\n\t" "v_add_u32 v29, 0x10000, v29\n\t" "v_fma_f32 v68, v68, v21, v21\n\t" "v_fma_f32 v69, v69, v21, v21\n\t"
    "v_mfma_f32_16x16x32_bf16 v[92:95], a[148:151], a[92:95], v[92:95]\n\t" "v_fma_f32 v70, v70, v21, v21\n\t" "v_fma_f32 v71, v71, v21, v21\n\t" "s_waitcnt lgkmcnt(4)\n\t"
    "v_mfma_f32_16x16x32_bf16 v[192:195], a[152:155], a[64:67], v[192:195]\n\t" "ds_read_b64_tr_b16 a[136:137], %[vr3] offset:7200\n\t" "v_rcp_f32_e64 v68, v68\n\t"
    "v_mfma_f32_16x16x32_bf16 v[192:195], a[156:159], a[80:83], v[192:195]\n\t" "ds_read_b64_tr_b16 a[138:139], %[vr3] offset:10784\n\t" "v_rcp_f32_e64 v69, v69\n\t"
    "v_mfma_f32_16x16x32_bf16 v[196:199], a[152:155], a[68:71], v[196:199]\n\t" "v_rcp_f32_e64 v70, v70\n\t" "v_mfma_f32_16x16x32_bf16 v[196:199], a[156:159], a[84:87], v[196:199]\n\t"
    "v_rcp_f32_e64 v71, v71\n\t" "v_mfma_f32_16x16x32_bf16 v[200:203], a[152:155], a[72:75], v[200:203]\n\t" "ds_read_b64_tr_b16 a[140:141], %[vr3] offset:21536\n\t" "v_mul_f32 v180, v180, v68\n\t"
    "v_mul_f32 v181, v181, v69\n\t" "v_mfma_f32_16x16x32_bf16 v[200:203], a[156:159], a[88:91], v[200:203]\n\t" "ds_read_b64_tr_b16 a[142:143], %[vr3] offset:25120\n\t" "v_mul_f32 v182, v182, v70\n\t"
    "v_mul_f32 v183, v183, v71\n\t" "v_mfma_f32_16x16x32_bf16 v[204:207], a[152:155], a[76:79], v[204:207]\n\t" "s_add_u32 m0, %[sld], 40960\n\t" "s_nop 0\n\t"
    "global_load_lds_dwordx4 v29, %[sbw] offset:0\n\t" "v_cvt_pk_bf16_f32 v68, v180, v181\n\t" "v_cvt_pk_bf16_f32 v69, v182, v183\n\t"
    "v_mfma_f32_16x16x32_bf16 v[204:207], a[156:159], a[92:95], v[204:207]\n\t" "v_mul_f32 v72, v72, v18\n\t" "v_mul_f32 v73, v73, v18\n\t" "s_waitcnt lgkmcnt(4)\n\t"
    "v_mfma_f32_16x16x32_bf16 v[80:83], a[128:131], a[96:99], v[80:83]\n\t" "v_accvgpr_read_b32 v31, %[ct]\n\t" "s_mov_b64 exec, 0xffff\n\t" "v_add_u32 v31, %[gt2], v31\n\t" "s_mov_b64 exec, -1\n\t"
    "ds_read_b128 a[144:147], v31 offset:0\n\t" "v_mul_f32 v74, v74, v18\n\t" "v_mul_f32 v75, v75, v18\n\t" "v_mfma_f32_16x16x32_bf16 v[80:83], a[132:135], a[112:115], v[80:83]\n\t"
    "v_exp_f32_e64 v72, v72\n\t" "v_mfma_f32_16x16x32_bf16 v[84:87], a[128:131], a[100:103], v[84:87]\n\t" "v_exp_f32_e64 v73, v73\n\t"
    "v_mfma_f32_16x16x32_bf16 v[84:87], a[132:135], a[116:119], v[84:87]\n\t" "v_exp_f32_e64 v74, v74\n\t" "v_mfma_f32_16x16x32_bf16 v[88:91], a[128:131], a[104:107], v[88:91]\n\t"
    "ds_read_b128 a[148:151], v31 offset:256\n\t" "v_exp_f32_e64 v75, v75\n\t" "v_mfma_f32_16x16x32_bf16 v[88:91], a[132:135], a[120:123], v[88:91]\n\t" "v_fma_f32 v72, v72, v22, v22\n\t"
    "v_fma_f32 v73, v73, v22, v22\n\t" "v_mfma_f32_16x16x32_bf16 v[92:95], a[128:131], a[108:111], v[92:95]\n\t" "s_add_u32 m0, %[sld], 44928\n\t" "s_nop 0\n\t"
    "global_load_lds_dwordx4 v29, %[sbw] offset:128\n\t" "v_add_u32 v29, 0x10000, v29\n\t" "v_fma_f32 v74, v74, v22, v22\n\t" "v_fma_f32 v75, v75, v22, v22\n\t"
    "v_mfma_f32_16x16x32_bf16 v[92:95], a[132:135], a[124:127], v[92:95]\n\t" "v_rcp_f32_e64 v72, v72\n\t" "s_waitcnt lgkmcnt(2)\n\t"
    "v_mfma_f32_16x16x32_bf16 v[192:195], a[136:139], a[96:99], v[192:195]\n\t" "ds_read_b64_tr_b16 a[152:153], %[vr4] offset:0\n\t" "v_rcp_f32_e64 v73, v73\n\t"
    "v_mfma_f32_16x16x32_bf16 v[192:195], a[140:143], a[112:115], v[192:195]\n\t" "ds_read_b64_tr_b16 a[154:155], %[vr4] offset:3584\n\t" "v_rcp_f32_e64 v74, v74\n\t"
    "v_mfma_f32_16x16x32_bf16 v[196:199], a[136:139], a[100:103], v[196:199]\n\t" "v_rcp_f32_e64 v75, v75\n\t" "v_mfma_f32_16x16x32_bf16 v[196:199], a[140:143], a[116:119], v[196:199]\n\t"
    "v_mul_f32 v184, v184, v72\n\t" "v_mul_f32 v185, v185, v73\n\t" "v_mfma_f32_16x16x32_bf16 v[200:203], a[136:139], a[104:107], v[200:203]\n\t" "ds_read_b64_tr_b16 a[156:157], %[vr4] offset:14336\n\t"
    "v_mul_f32 v186, v186, v74\n\t" "v_mul_f32 v187, v187, v75\n\t" "v_mfma_f32_16x16x32_bf16 v[200:203], a[140:143], a[120:123], v[200:203]\n\t" "ds_read_b64_tr_b16 a[158:159], %[vr4] offset:17920\n\t"
    "v_cvt_pk_bf16_f32 v72, v184, v185\n\t" "v_cvt_pk_bf16_f32 v73, v186, v187\n\t" "v_mfma_f32_16x16x32_bf16 v[204:207], a[136:139], a[108:111], v[204:207]\n\t" "s_add_u32 m0, %[sld], 139264\n\t"
    "s_nop 0\n\t" "global_load_lds_dwordx4 v29, %[sbw] offset:0\n\t" "v_mul_f32 v76, v76, v19\n\t" "v_mul_f32_e64 v77, v77, v19\n\t"
    "v_mfma_f32_16x16x32_bf16 v[204:207], a[140:143], a[124:127], v[204:207]\n\t" "v_mul_f32 v78, v78, v19\n\t" "v_mul_f32_e64 v79, v79, v19\n\t" "s_waitcnt lgkmcnt(4)\n\t"
    "v_mfma_f32_16x16x32_bf16 v[96:99], a[144:147], a[200:203], v[96:99]\n\t" "ds_read_b64_tr_b16 a[128:129], %[vr4] offset:7168\n\t" "v_exp_f32_e64 v76, v76\n\t"
    "v_mfma_f32_16x16x32_bf16 v[100:103], a[144:147], a[204:207], v[100:103]\n\t" "ds_read_b64_tr_b16 a[130:131], %[vr4] offset:10752\n\t" "v_exp_f32_e64 v77, v77\n\t"
    "v_mfma_f32_16x16x32_bf16 v[104:107], a[144:147], a[208:211], v[104:107]\n\t" "v_exp_f32_e64 v78, v78\n\t" "v_mfma_f32_16x16x32_bf16 v[108:111], a[144:147], a[212:215], v[108:111]\n\t"
    "v_exp_f32_e64 v79, v79\n\t" "v_mfma_f32_16x16x32_bf16 v[208:211], a[148:151], a[200:203], v[208:211]\n\t" "ds_read_b64_tr_b16 a[132:133], %[vr4] offset:21504\n\t" "v_fma_f32 v76, v76, v23, v23\n\t"
    "v_fma_f32 v77, v77, v23, v23\n\t" "v_mfma_f32_16x16x32_bf16 v[212:215], a[148:151], a[204:207], v[212:215]\n\t" "ds_read_b64_tr_b16 a[134:135], %[vr4] offset:25088\n\t"
    "v_fma_f32 v78, v78, v23, v23\n\t" "v_fma_f32 v79, v79, v23, v23\n\t" "v_mfma_f32_16x16x32_bf16 v[216:219], a[148:151], a[208:211], v[216:219]\n\t" "s_add_u32 m0, %[sld], 143232\n\t" "s_nop 0\n\t"
    "global_load_lds_dwordx4 v29, %[sbw] offset:128\n\t" "v_add_u32 v29, 0x10000, v29\n\t" "v_rcp_f32 v76, v76\n\t" "v_mfma_f32_16x16x32_bf16 v[220:223], a[148:151], a[212:215], v[220:223]\n\t"
    "v_rcp_f32 v77, v77\n\t" "s_waitcnt lgkmcnt(4)\n\t" "v_mfma_f32_16x16x32_bf16 v[96:99], a[152:155], a[32:35], v[96:99]\n\t" "ds_read_b64_tr_b16 a[136:137], %[vr4] offset:28672\n\t"
    "v_rcp_f32_e64 v78, v78\n\t" "v_mfma_f32_16x16x32_bf16 v[96:99], a[156:159], a[48:51], v[96:99]\n\t" "ds_read_b64_tr_b16 a[138:139], %[vr4] offset:32256\n\t" "v_rcp_f32_e64 v79, v79\n\t"
    "v_mfma_f32_16x16x32_bf16 v[100:103], a[152:155], a[36:39], v[100:103]\n\t" "v_mul_f32 v188, v188, v76\n\t" "v_mul_f32 v189, v189, v77\n\t"
    "v_mfma_f32_16x16x32_bf16 v[100:103], a[156:159], a[52:55], v[100:103]\n\t" "v_mul_f32 v190, v190, v78\n\t" "v_mul_f32 v191, v191, v79\n\t"
    "v_mfma_f32_16x16x32_bf16 v[104:107], a[152:155], a[40:43], v[104:107]\n\t" "ds_read_b64_tr_b16 a[140:141], %[vr4] offset:43008\n\t" "v_cvt_pk_bf16_f32 v76, v188, v189\n\t"
    "v_cvt_pk_bf16_f32 v77, v190, v191\n\t" "v_mfma_f32_16x16x32_bf16 v[104:107], a[156:159], a[56:59], v[104:107]\n\t" "ds_read_b64_tr_b16 a[142:143], %[vr4] offset:46592\n\t"
    "s_add_u32 s90, %[shl], %[go1]\n\t" "s_addc_u32 s91, %[shh], 0\n\t" "s_add_u32 s92, s90, 229376\n\t" "s_addc_u32 s93, s91, 0\n\t"
    "v_mfma_f32_16x16x32_bf16 v[108:111], a[152:155], a[44:47], v[108:111]\n\t" "s_add_u32 m0, %[sld], 147456\n\t" "s_nop 0\n\t" "global_load_lds_dwordx4 v29, %[sbw] offset:0\n\t"
    "v_mul_f32 v80, v80, v16\n\t" "v_mul_f32 v81, v81, v16\n\t" "v_mfma_f32_16x16x32_bf16 v[108:111], a[156:159], a[60:63], v[108:111]\n\t" "v_mul_f32 v82, v82, v16\n\t" "v_mul_f32_e64 v83, v83, v16\n\t"
    "s_waitcnt lgkmcnt(4)\n\t" "v_mfma_f32_16x16x32_bf16 v[208:211], a[128:131], a[32:35], v[208:211]\n\t" "ds_read_b64_tr_b16 a[144:145], %[vr4] offset:35840\n\t" "v_exp_f32_e64 v80, v80\n\t"
    "v_mfma_f32_16x16x32_bf16 v[208:211], a[132:135], a[48:51], v[208:211]\n\t" "ds_read_b64_tr_b16 a[146:147], %[vr4] offset:39424\n\t" "v_exp_f32_e64 v81, v81\n\t"
    "v_mfma_f32_16x16x32_bf16 v[212:215], a[128:131], a[36:39], v[212:215]\n\t" "v_exp_f32_e64 v82, v82\n\t" "v_mfma_f32_16x16x32_bf16 v[212:215], a[132:135], a[52:55], v[212:215]\n\t"
    "v_exp_f32_e64 v83, v83\n\t" "v_mfma_f32_16x16x32_bf16 v[216:219], a[128:131], a[40:43], v[216:219]\n\t" "ds_read_b64_tr_b16 a[148:149], %[vr4] offset:50176\n\t" "v_fma_f32 v80, v80, v20, v20\n\t"
    "v_fma_f32 v81, v81, v20, v20\n\t" "v_mfma_f32_16x16x32_bf16 v[216:219], a[132:135], a[56:59], v[216:219]\n\t" "ds_read_b64_tr_b16 a[150:151], %[vr4] offset:53760\n\t"
    "v_fma_f32 v82, v82, v20, v20\n\t" "v_fma_f32 v83, v83, v20, v20\n\t" "v_mfma_f32_16x16x32_bf16 v[220:223], a[128:131], a[44:47], v[220:223]\n\t" "s_add_u32 m0, %[sld], 151424\n\t" "s_nop 0\n\t"
    "global_load_lds_dwordx4 v29, %[sbw] offset:128\n\t" "v_add_u32 v29, 0x10000, v29\n\t" "v_rcp_f32 v80, v80\n\t" "v_mfma_f32_16x16x32_bf16 v[220:223], a[132:135], a[60:63], v[220:223]\n\t"
    "v_rcp_f32 v81, v81\n\t" "s_waitcnt lgkmcnt(4)\n\t" "v_mfma_f32_16x16x32_bf16 v[96:99], a[136:139], a[64:67], v[96:99]\n\t" "ds_read_b64_tr_b16 a[152:153], %[vr5] offset:0\n\t"
    "v_rcp_f32_e64 v82, v82\n\t" "v_mfma_f32_16x16x32_bf16 v[96:99], a[140:143], a[80:83], v[96:99]\n\t" "ds_read_b64_tr_b16 a[154:155], %[vr5] offset:3584\n\t" "v_rcp_f32_e64 v83, v83\n\t"
    "v_mfma_f32_16x16x32_bf16 v[100:103], a[136:139], a[68:71], v[100:103]\n\t" "v_mul_f32 v192, v192, v80\n\t" "v_mul_f32 v193, v193, v81\n\t"
    "v_mfma_f32_16x16x32_bf16 v[100:103], a[140:143], a[84:87], v[100:103]\n\t" "v_mul_f32 v194, v194, v82\n\t" "v_mul_f32 v195, v195, v83\n\t"
    "v_mfma_f32_16x16x32_bf16 v[104:107], a[136:139], a[72:75], v[104:107]\n\t" "ds_read_b64_tr_b16 a[156:157], %[vr5] offset:14336\n\t" "v_cvt_pk_bf16_f32 v66, v192, v193\n\t"
    "v_cvt_pk_bf16_f32 v67, v194, v195\n\t" "v_mfma_f32_16x16x32_bf16 v[104:107], a[140:143], a[88:91], v[104:107]\n\t" "ds_read_b64_tr_b16 a[158:159], %[vr5] offset:17920\n\t"
    "global_store_dwordx4 %[va], v[64:67], s[90:91] offset:0 sc1\n\t" "v_mul_f32_e64 v84, v84, v17\n\t" "v_mfma_f32_16x16x32_bf16 v[108:111], a[136:139], a[76:79], v[108:111]\n\t"
    "s_add_u32 m0, %[sld], 155648\n\t" "s_nop 0\n\t" "global_load_lds_dwordx4 v29, %[sbw] offset:0\n\t" "v_mul_f32 v85, v85, v17\n\t" "v_mul_f32_e64 v86, v86, v17\n\t"
    "v_mfma_f32_16x16x32_bf16 v[108:111], a[140:143], a[92:95], v[108:111]\n\t" "v_mul_f32 v87, v87, v17\n\t" "v_exp_f32_e64 v84, v84\n\t" "s_waitcnt lgkmcnt(4)\n\t"
    "v_mfma_f32_16x16x32_bf16 v[208:211], a[144:147], a[64:67], v[208:211]\n\t" "ds_read_b64_tr_b16 a[128:129], %[vr5] offset:7168\n\t" "v_exp_f32_e64 v85, v85\n\t"
    "v_mfma_f32_16x16x32_bf16 v[208:211], a[148:151], a[80:83], v[208:211]\n\t" "ds_read_b64_tr_b16 a[130:131], %[vr5] offset:10752\n\t" "v_exp_f32_e64 v86, v86\n\t"
    "v_mfma_f32_16x16x32_bf16 v[212:215], a[144:147], a[68:71], v[212:215]\n\t" "v_exp_f32_e64 v87, v87\n\t" "v_mfma_f32_16x16x32_bf16 v[212:215], a[148:151], a[84:87], v[212:215]\n\t"
    "v_fma_f32 v84, v84, v21, v21\n\t" "v_fma_f32 v85, v85, v21, v21\n\t" "v_mfma_f32_16x16x32_bf16 v[216:219], a[144:147], a[72:75], v[216:219]\n\t"
    "ds_read_b64_tr_b16 a[132:133], %[vr5] offset:21504\n\t" "v_fma_f32 v86, v86, v21, v21\n\t" "v_fma_f32 v87, v87, v21, v21\n\t"
    "v_mfma_f32_16x16x32_bf16 v[216:219], a[148:151], a[88:91], v[216:219]\n\t" "ds_read_b64_tr_b16 a[134:135], %[vr5] offset:25088\n\t" "v_rcp_f32_e64 v84, v84\n\t"
    "v_mfma_f32_16x16x32_bf16 v[220:223], a[144:147], a[76:79], v[220:223]\n\t" "s_add_u32 m0, %[sld], 159616\n\t" "s_nop 0\n\t" "global_load_lds_dwordx4 v29, %[sbw] offset:128\n\t"
    "v_add_u32 v29, 0x10000, v29\n\t" "v_rcp_f32 v85, v85\n\t" "v_mfma_f32_16x16x32_bf16 v[220:223], a[148:151], a[92:95], v[220:223]\n\t" "v_rcp_f32 v86, v86\n\t" "s_waitcnt lgkmcnt(4)\n\t"
    "v_mfma_f32_16x16x32_bf16 v[96:99], a[152:155], a[96:99], v[96:99]\n\t" "ds_read_b128 a[136:139], v31 offset:512\n\t" "v_rcp_f32_e64 v87, v87\n\t"
    "v_mfma_f32_16x16x32_bf16 v[96:99], a[156:159], a[112:115], v[96:99]\n\t" "v_mul_f32 v196, v196, v84\n\t" "v_mul_f32 v197, v197, v85\n\t"
    "v_mfma_f32_16x16x32_bf16 v[100:103], a[152:155], a[100:103], v[100:103]\n\t" "v_mul_f32 v198, v198, v86\n\t" "v_mul_f32 v199, v199, v87\n\t"
    "v_mfma_f32_16x16x32_bf16 v[100:103], a[156:159], a[116:119], v[100:103]\n\t" "v_cvt_pk_bf16_f32 v70, v196, v197\n\t" "v_cvt_pk_bf16_f32 v71, v198, v199\n\t"
    "v_mfma_f32_16x16x32_bf16 v[104:107], a[152:155], a[104:107], v[104:107]\n\t" "ds_read_b128 a[140:143], v31 offset:768\n\t" "global_store_dwordx4 %[va], v[68:71], s[90:91] offset:1024 sc1\n\t"
    "v_mul_f32_e64 v88, v88, v18\n\t" "v_mfma_f32_16x16x32_bf16 v[104:107], a[156:159], a[120:123], v[104:107]\n\t" "v_mul_f32 v89, v89, v18\n\t" "v_mul_f32 v90, v90, v18\n\t"
    "v_mfma_f32_16x16x32_bf16 v[108:111], a[152:155], a[108:111], v[108:111]\n\t" "s_add_u32 m0, %[sld], 0\n\t" "s_nop 0\n\t" "global_load_lds_dwordx4 v29, %[sbw] offset:0\n\t"
    "v_mul_f32 v91, v91, v18\n\t" "v_exp_f32 v88, v88\n\t" "v_mfma_f32_16x16x32_bf16 v[108:111], a[156:159], a[124:127], v[108:111]\n\t" "v_exp_f32 v89, v89\n\t" "s_waitcnt lgkmcnt(2)\n\t"
    "v_mfma_f32_16x16x32_bf16 v[208:211], a[128:131], a[96:99], v[208:211]\n\t" "ds_read_b64_tr_b16 a[144:145], %[vr4] offset:32\n\t" "v_exp_f32_e64 v90, v90\n\t"
    "v_mfma_f32_16x16x32_bf16 v[208:211], a[132:135], a[112:115], v[208:211]\n\t" "ds_read_b64_tr_b16 a[146:147], %[vr4] offset:3616\n\t" "v_exp_f32_e64 v91, v91\n\t"
    "v_mfma_f32_16x16x32_bf16 v[212:215], a[128:131], a[100:103], v[212:215]\n\t" "v_fma_f32 v88, v88, v22, v22\n\t" "v_fma_f32 v89, v89, v22, v22\n\t"
    "v_mfma_f32_16x16x32_bf16 v[212:215], a[132:135], a[116:119], v[212:215]\n\t" "v_fma_f32 v90, v90, v22, v22\n\t" "v_fma_f32 v91, v91, v22, v22\n\t"
    "v_mfma_f32_16x16x32_bf16 v[216:219], a[128:131], a[104:107], v[216:219]\n\t" "ds_read_b64_tr_b16 a[148:149], %[vr4] offset:14368\n\t" "v_rcp_f32_e64 v88, v88\n\t"
    "v_mfma_f32_16x16x32_bf16 v[216:219], a[132:135], a[120:123], v[216:219]\n\t" "ds_read_b64_tr_b16 a[150:151], %[vr4] offset:17952\n\t" "v_rcp_f32_e64 v89, v89\n\t"
    "v_mfma_f32_16x16x32_bf16 v[220:223], a[128:131], a[108:111], v[220:223]\n\t" "s_add_u32 m0, %[sld], 3968\n\t" "s_nop 0\n\t" "global_load_lds_dwordx4 v29, %[sbw] offset:128\n\t"
    "v_add_u32 v29, 0x10000, v29\n\t" "v_rcp_f32 v90, v90\n\t" "v_mfma_f32_16x16x32_bf16 v[220:223], a[132:135], a[124:127], v[220:223]\n\t" "v_rcp_f32 v91, v91\n\t" "s_waitcnt lgkmcnt(4)\n\t"
    "v_mfma_f32_16x16x32_bf16 v[112:115], a[136:139], a[200:203], v[112:115]\n\t" "ds_read_b64_tr_b16 a[152:153], %[vr4] offset:7200\n\t" "v_mul_f32 v200, v200, v88\n\t" "v_mul_f32 v201, v201, v89\n\t"
    "v_mfma_f32_16x16x32_bf16 v[116:119], a[136:139], a[204:207], v[116:119]\n\t" "ds_read_b64_tr_b16 a[154:155], %[vr4] offset:10784\n\t" "v_mul_f32 v202, v202, v90\n\t" "v_mul_f32 v203, v203, v91\n\t"
    "v_mfma_f32_16x16x32_bf16 v[120:123], a[136:139], a[208:211], v[120:123]\n\t" "v_cvt_pk_bf16_f32 v74, v200, v201\n\t" "v_cvt_pk_bf16_f32 v75, v202, v203\n\t"
    "v_mfma_f32_16x16x32_bf16 v[124:127], a[136:139], a[212:215], v[124:127]\n\t" "global_store_dwordx4 %[va], v[72:75], s[92:93] offset:0 sc1\n\t" "v_mul_f32_e64 v92, v92, v19\n\t"
    "v_mfma_f32_16x16x32_bf16 v[224:227], a[140:143], a[200:203], v[224:227]\n\t" "ds_read_b64_tr_b16 a[156:157], %[vr4] offset:21536\n\t" "v_mul_f32 v93, v93, v19\n\t" "v_mul_f32 v94, v94, v19\n\t"
    "v_mfma_f32_16x16x32_bf16 v[228:231], a[140:143], a[204:207], v[228:231]\n\t" "ds_read_b64_tr_b16 a[158:159], %[vr4] offset:25120\n\t" "v_mul_f32 v95, v95, v19\n\t" "v_exp_f32 v92, v92\n\t"
    "v_mfma_f32_16x16x32_bf16 v[232:235], a[140:143], a[208:211], v[232:235]\n\t" "s_add_u32 m0, %[sld], 8192\n\t" "s_nop 0\n\t" "global_load_lds_dwordx4 v29, %[sbw] offset:0\n\t" "v_exp_f32 v93, v93\n\t"
    "v_mfma_f32_16x16x32_bf16 v[236:239], a[140:143], a[212:215], v[236:239]\n\t" "v_exp_f32 v94, v94\n\t" "s_waitcnt lgkmcnt(4)\n\t"
    "v_mfma_f32_16x16x32_bf16 v[112:115], a[144:147], a[32:35], v[112:115]\n\t" "ds_read_b64_tr_b16 a[128:129], %[vr4] offset:28704\n\t" "v_exp_f32_e64 v95, v95\n\t"
    "v_mfma_f32_16x16x32_bf16 v[112:115], a[148:151], a[48:51], v[112:115]\n\t" "ds_read_b64_tr_b16 a[130:131], %[vr4] offset:32288\n\t" "v_fma_f32 v92, v92, v23, v23\n\t"
    "v_fma_f32 v93, v93, v23, v23\n\t" "v_mfma_f32_16x16x32_bf16 v[116:119], a[144:147], a[36:39], v[116:119]\n\t" "v_fma_f32 v94, v94, v23, v23\n\t" "v_fma_f32 v95, v95, v23, v23\n\t"
    "v_mfma_f32_16x16x32_bf16 v[116:119], a[148:151], a[52:55], v[116:119]\n\t" "v_rcp_f32_e64 v92, v92\n\t" "v_mfma_f32_16x16x32_bf16 v[120:123], a[144:147], a[40:43], v[120:123]\n\t"
    "ds_read_b64_tr_b16 a[132:133], %[vr4] offset:43040\n\t" "v_rcp_f32_e64 v93, v93\n\t" "v_mfma_f32_16x16x32_bf16 v[120:123], a[148:151], a[56:59], v[120:123]\n\t"
    "ds_read_b64_tr_b16 a[134:135], %[vr4] offset:46624\n\t" "v_rcp_f32_e64 v94, v94\n\t" "v_mfma_f32_16x16x32_bf16 v[124:127], a[144:147], a[44:47], v[124:127]\n\t" "s_add_u32 m0, %[sld], 12160\n\t"
    "s_nop 0\n\t" "global_load_lds_dwordx4 v29, %[sbw] offset:128\n\t" "v_add_u32 v29, 0x10000, v29\n\t" "v_rcp_f32 v95, v95\n\t"
    "v_mfma_f32_16x16x32_bf16 v[124:127], a[148:151], a[60:63], v[124:127]\n\t" "v_mul_f32 v204, v204, v92\n\t" "v_mul_f32_e64 v205, v205, v93\n\t" "s_waitcnt lgkmcnt(4)\n\t"
    "v_mfma_f32_16x16x32_bf16 v[224:227], a[152:155], a[32:35], v[224:227]\n\t" "ds_read_b64_tr_b16 a[136:137], %[vr4] offset:35872\n\t" "v_mul_f32 v206, v206, v94\n\t" "v_mul_f32 v207, v207, v95\n\t"
    "v_mfma_f32_16x16x32_bf16 v[224:227], a[156:159], a[48:51], v[224:227]\n\t" "ds_read_b64_tr_b16 a[138:139], %[vr4] offset:39456\n\t" "v_cvt_pk_bf16_f32 v78, v204, v205\n\t"
    "v_cvt_pk_bf16_f32 v79, v206, v207\n\t" "v_mfma_f32_16x16x32_bf16 v[228:231], a[152:155], a[36:39], v[228:231]\n\t" "global_store_dwordx4 %[va], v[76:79], s[92:93] offset:1024 sc1\n\t"
    "v_mul_f32_e64 v96, v96, v16\n\t" "v_mfma_f32_16x16x32_bf16 v[228:231], a[156:159], a[52:55], v[228:231]\n\t" "v_mul_f32 v97, v97, v16\n\t" "v_mul_f32 v98, v98, v16\n\t"
    "v_mfma_f32_16x16x32_bf16 v[232:235], a[152:155], a[40:43], v[232:235]\n\t" "ds_read_b64_tr_b16 a[140:141], %[vr4] offset:50208\n\t" "v_mul_f32 v99, v99, v16\n\t" "v_exp_f32 v96, v96\n\t"
    "v_mfma_f32_16x16x32_bf16 v[232:235], a[156:159], a[56:59], v[232:235]\n\t" "ds_read_b64_tr_b16 a[142:143], %[vr4] offset:53792\n\t" "v_exp_f32_e64 v97, v97\n\t"
    "v_mfma_f32_16x16x32_bf16 v[236:239], a[152:155], a[44:47], v[236:239]\n\t" "v_exp_f32_e64 v98, v98\n\t" "v_mfma_f32_16x16x32_bf16 v[236:239], a[156:159], a[60:63], v[236:239]\n\t"
    "v_exp_f32 v99, v99\n\t" "s_waitcnt lgkmcnt(4)\n\t" "v_mfma_f32_16x16x32_bf16 v[112:115], a[128:131], a[64:67], v[112:115]\n\t" "ds_read_b64_tr_b16 a[144:145], %[vr5] offset:32\n\t"
    "v_fma_f32 v96, v96, v20, v20\n\t" "v_fma_f32 v97, v97, v20, v20\n\t" "v_mfma_f32_16x16x32_bf16 v[112:115], a[132:135], a[80:83], v[112:115]\n\t"
    "ds_read_b64_tr_b16 a[146:147], %[vr5] offset:3616\n\t" "v_fma_f32 v98, v98, v20, v20\n\t" "v_fma_f32 v99, v99, v20, v20\n\t"
    "v_mfma_f32_16x16x32_bf16 v[116:119], a[128:131], a[68:71], v[116:119]\n\t" "v_rcp_f32_e64 v96, v96\n\t" "v_mfma_f32_16x16x32_bf16 v[116:119], a[132:135], a[84:87], v[116:119]\n\t"
    "v_rcp_f32_e64 v97, v97\n\t" "v_mfma_f32_16x16x32_bf16 v[120:123], a[128:131], a[72:75], v[120:123]\n\t" "ds_read_b64_tr_b16 a[148:149], %[vr5] offset:14368\n\t" "v_rcp_f32_e64 v98, v98\n\t"
    "v_mfma_f32_16x16x32_bf16 v[120:123], a[132:135], a[88:91], v[120:123]\n\t" "ds_read_b64_tr_b16 a[150:151], %[vr5] offset:17952\n\t" "v_rcp_f32_e64 v99, v99\n\t"
    "v_mfma_f32_16x16x32_bf16 v[124:127], a[128:131], a[76:79], v[124:127]\n\t" "v_mul_f32 v208, v208, v96\n\t" "v_mul_f32 v209, v209, v97\n\t"
    "v_mfma_f32_16x16x32_bf16 v[124:127], a[132:135], a[92:95], v[124:127]\n\t" "v_mul_f32 v210, v210, v98\n\t" "v_mul_f32_e64 v211, v211, v99\n\t" "s_waitcnt lgkmcnt(4)\n\t"
    "v_mfma_f32_16x16x32_bf16 v[224:227], a[136:139], a[64:67], v[224:227]\n\t" "ds_read_b64_tr_b16 a[152:153], %[vr5] offset:7200\n\t" "v_cvt_pk_bf16_f32 v96, v208, v209\n\t"
    "v_cvt_pk_bf16_f32 v97, v210, v211\n\t" "v_mfma_f32_16x16x32_bf16 v[224:227], a[140:143], a[80:83], v[224:227]\n\t" "ds_read_b64_tr_b16 a[154:155], %[vr5] offset:10784\n\t"
    "v_mul_f32 v100, v100, v17\n\t" "v_mul_f32 v101, v101, v17\n\t" "v_mfma_f32_16x16x32_bf16 v[228:231], a[136:139], a[68:71], v[228:231]\n\t" "v_mul_f32 v102, v102, v17\n\t"
    "v_mul_f32 v103, v103, v17\n\t" "v_mfma_f32_16x16x32_bf16 v[228:231], a[140:143], a[84:87], v[228:231]\n\t" "v_exp_f32_e64 v100, v100\n\t"
    "v_mfma_f32_16x16x32_bf16 v[232:235], a[136:139], a[72:75], v[232:235]\n\t" "ds_read_b64_tr_b16 a[156:157], %[vr5] offset:21536\n\t" "v_exp_f32_e64 v101, v101\n\t"
    "v_mfma_f32_16x16x32_bf16 v[232:235], a[140:143], a[88:91], v[232:235]\n\t" "ds_read_b64_tr_b16 a[158:159], %[vr5] offset:25120\n\t" "v_exp_f32_e64 v102, v102\n\t"
    "v_mfma_f32_16x16x32_bf16 v[236:239], a[136:139], a[76:79], v[236:239]\n\t" "v_exp_f32_e64 v103, v103\n\t" "v_mfma_f32_16x16x32_bf16 v[236:239], a[140:143], a[92:95], v[236:239]\n\t"
    "v_fma_f32 v100, v100, v21, v21\n\t" "v_fma_f32 v101, v101, v21, v21\n\t" "s_waitcnt lgkmcnt(4)\n\t" "v_mfma_f32_16x16x32_bf16 v[112:115], a[144:147], a[96:99], v[112:115]\n\t"
    "v_accvgpr_read_b32 v31, %[ct]\n\t" "s_mov_b64 exec, 0xffff\n\t" "v_add_u32_e64 v31, %[gt3], v31\n\t" "s_mov_b64 exec, -1\n\t" "ds_read_b128 a[128:131], v31 offset:0\n\t"
    "v_fma_f32 v102, v102, v21, v21\n\t" "v_fma_f32 v103, v103, v21, v21\n\t" "v_mfma_f32_16x16x32_bf16 v[112:115], a[148:151], a[112:115], v[112:115]\n\t" "v_rcp_f32_e64 v100, v100\n\t"
    "v_mfma_f32_16x16x32_bf16 v[116:119], a[144:147], a[100:103], v[116:119]\n\t" "v_rcp_f32_e64 v101, v101\n\t" "v_mfma_f32_16x16x32_bf16 v[116:119], a[148:151], a[116:119], v[116:119]\n\t"
    "v_rcp_f32_e64 v102, v102\n\t" "v_mfma_f32_16x16x32_bf16 v[120:123], a[144:147], a[104:107], v[120:123]\n\t" "ds_read_b128 a[132:135], v31 offset:256\n\t" "v_rcp_f32_e64 v103, v103\n\t"
    "v_mfma_f32_16x16x32_bf16 v[120:123], a[148:151], a[120:123], v[120:123]\n\t" "v_mul_f32 v212, v212, v100\n\t" "v_mul_f32 v213, v213, v101\n\t"
    "v_mfma_f32_16x16x32_bf16 v[124:127], a[144:147], a[108:111], v[124:127]\n\t" "v_mul_f32 v214, v214, v102\n\t" "v_mul_f32 v215, v215, v103\n\t"
    "v_mfma_f32_16x16x32_bf16 v[124:127], a[148:151], a[124:127], v[124:127]\n\t" "v_cvt_pk_bf16_f32 v100, v212, v213\n\t" "v_cvt_pk_bf16_f32 v101, v214, v215\n\t" "s_waitcnt lgkmcnt(2)\n\t"
    "v_mfma_f32_16x16x32_bf16 v[224:227], a[152:155], a[96:99], v[224:227]\n\t" "ds_read_b64_tr_b16 a[136:137], %[vr6] offset:0\n\t" "v_mul_f32 v104, v104, v18\n\t" "v_mul_f32_e64 v105, v105, v18\n\t"
    "v_mfma_f32_16x16x32_bf16 v[224:227], a[156:159], a[112:115], v[224:227]\n\t" "ds_read_b64_tr_b16 a[138:139], %[vr6] offset:3584\n\t" "v_mul_f32 v106, v106, v18\n\t" "v_mul_f32 v107, v107, v18\n\t"
    "v_mfma_f32_16x16x32_bf16 v[228:231], a[152:155], a[100:103], v[228:231]\n\t" "global_load_dword a231, %[va], %[cd]\n\t" "v_exp_f32_e64 v104, v104\n\t"
    "v_mfma_f32_16x16x32_bf16 v[228:231], a[156:159], a[116:119], v[228:231]\n\t" "v_exp_f32_e64 v105, v105\n\t" "v_mfma_f32_16x16x32_bf16 v[232:235], a[152:155], a[104:107], v[232:235]\n\t"
    "ds_read_b64_tr_b16 a[140:141], %[vr6] offset:14336\n\t" "v_exp_f32_e64 v106, v106\n\t" "v_mfma_f32_16x16x32_bf16 v[232:235], a[156:159], a[120:123], v[232:235]\n\t"
    "ds_read_b64_tr_b16 a[142:143], %[vr6] offset:17920\n\t" "v_exp_f32_e64 v107, v107\n\t" "v_mfma_f32_16x16x32_bf16 v[236:239], a[152:155], a[108:111], v[236:239]\n\t"
    "v_fma_f32 v104, v104, v22, v22\n\t" "v_fma_f32 v105, v105, v22, v22\n\t" "v_mfma_f32_16x16x32_bf16 v[236:239], a[156:159], a[124:127], v[236:239]\n\t" "v_fma_f32 v106, v106, v22, v22\n\t"
    "v_fma_f32 v107, v107, v22, v22\n\t" "s_waitcnt lgkmcnt(4)\n\t" "v_mfma_f32_16x16x32_bf16 v[128:131], a[128:131], a[200:203], v[128:131]\n\t" "ds_read_b64_tr_b16 a[144:145], %[vr6] offset:7168\n\t"
    "v_rcp_f32 v104, v104\n\t" "v_mfma_f32_16x16x32_bf16 v[132:135], a[128:131], a[204:207], v[132:135]\n\t" "ds_read_b64_tr_b16 a[146:147], %[vr6] offset:10752\n\t" "v_rcp_f32_e64 v105, v105\n\t"
    "v_mfma_f32_16x16x32_bf16 v[136:139], a[128:131], a[208:211], v[136:139]\n\t" "v_rcp_f32_e64 v106, v106\n\t" "v_mfma_f32_16x16x32_bf16 v[140:143], a[128:131], a[212:215], v[140:143]\n\t"
    "v_rcp_f32_e64 v107, v107\n\t" "v_mfma_f32_16x16x32_bf16 v[240:243], a[132:135], a[200:203], v[240:243]\n\t" "ds_read_b64_tr_b16 a[148:149], %[vr6] offset:21504\n\t" "v_mul_f32 v216, v216, v104\n\t"
    "v_mul_f32 v217, v217, v105\n\t" "v_mfma_f32_16x16x32_bf16 v[244:247], a[132:135], a[204:207], v[244:247]\n\t" "ds_read_b64_tr_b16 a[150:151], %[vr6] offset:25088\n\t" "v_mul_f32 v218, v218, v106\n\t"
    "v_mul_f32 v219, v219, v107\n\t" "v_mfma_f32_16x16x32_bf16 v[248:251], a[132:135], a[208:211], v[248:251]\n\t" "v_cvt_pk_bf16_f32 v104, v216, v217\n\t" "v_cvt_pk_bf16_f32 v105, v218, v219\n\t"
    "v_mfma_f32_16x16x32_bf16 v[252:255], a[132:135], a[212:215], v[252:255]\n\t" "v_mul_f32 v108, v108, v19\n\t" "v_mul_f32_e64 v109, v109, v19\n\t" "s_waitcnt lgkmcnt(4)\n\t"
    "v_mfma_f32_16x16x32_bf16 v[128:131], a[136:139], a[32:35], v[128:131]\n\t" "ds_read_b64_tr_b16 a[152:153], %[vr6] offset:28672\n\t" "v_mul_f32 v110, v110, v19\n\t" "v_mul_f32 v111, v111, v19\n\t"
    "v_mfma_f32_16x16x32_bf16 v[128:131], a[140:143], a[48:51], v[128:131]\n\t" "ds_read_b64_tr_b16 a[154:155], %[vr6] offset:32256\n\t" "v_exp_f32_e64 v108, v108\n\t"
    "v_mfma_f32_16x16x32_bf16 v[132:135], a[136:139], a[36:39], v[132:135]\n\t" "v_exp_f32_e64 v109, v109\n\t" "v_mfma_f32_16x16x32_bf16 v[132:135], a[140:143], a[52:55], v[132:135]\n\t"
    "v_exp_f32_e64 v110, v110\n\t" "v_mfma_f32_16x16x32_bf16 v[136:139], a[136:139], a[40:43], v[136:139]\n\t" "ds_read_b64_tr_b16 a[156:157], %[vr6] offset:43008\n\t" "v_exp_f32_e64 v111, v111\n\t"
    "v_mfma_f32_16x16x32_bf16 v[136:139], a[140:143], a[56:59], v[136:139]\n\t" "ds_read_b64_tr_b16 a[158:159], %[vr6] offset:46592\n\t" "v_fma_f32 v108, v108, v23, v23\n\t"
    "v_fma_f32 v109, v109, v23, v23\n\t" "v_mfma_f32_16x16x32_bf16 v[140:143], a[136:139], a[44:47], v[140:143]\n\t" "v_fma_f32 v110, v110, v23, v23\n\t" "v_fma_f32 v111, v111, v23, v23\n\t"
    "v_mfma_f32_16x16x32_bf16 v[140:143], a[140:143], a[60:63], v[140:143]\n\t" "v_rcp_f32 v108, v108\n\t" "s_waitcnt lgkmcnt(4)\n\t"
    "v_mfma_f32_16x16x32_bf16 v[240:243], a[144:147], a[32:35], v[240:243]\n\t" "ds_read_b64_tr_b16 a[128:129], %[vr6] offset:35840\n\t" "v_rcp_f32_e64 v109, v109\n\t"
    "v_mfma_f32_16x16x32_bf16 v[240:243], a[148:151], a[48:51], v[240:243]\n\t" "ds_read_b64_tr_b16 a[130:131], %[vr6] offset:39424\n\t" "v_rcp_f32_e64 v110, v110\n\t"
    "v_mfma_f32_16x16x32_bf16 v[244:247], a[144:147], a[36:39], v[244:247]\n\t" "v_rcp_f32_e64 v111, v111\n\t" "v_mfma_f32_16x16x32_bf16 v[244:247], a[148:151], a[52:55], v[244:247]\n\t"
    "v_mul_f32 v220, v220, v108\n\t" "v_mul_f32 v221, v221, v109\n\t" "v_mfma_f32_16x16x32_bf16 v[248:251], a[144:147], a[40:43], v[248:251]\n\t" "ds_read_b64_tr_b16 a[132:133], %[vr6] offset:50176\n\t"
    "v_mul_f32 v222, v222, v110\n\t" "v_mul_f32 v223, v223, v111\n\t" "v_mfma_f32_16x16x32_bf16 v[248:251], a[148:151], a[56:59], v[248:251]\n\t" "ds_read_b64_tr_b16 a[134:135], %[vr6] offset:53760\n\t"
    "v_cvt_pk_bf16_f32 v108, v220, v221\n\t" "v_cvt_pk_bf16_f32 v109, v222, v223\n\t" "v_mfma_f32_16x16x32_bf16 v[252:255], a[144:147], a[44:47], v[252:255]\n\t" "s_add_u32 s90, %[shl], %[go2]\n\t"
    "s_addc_u32 s91, %[shh], 0\n\t" "s_add_u32 s92, s90, 229376\n\t" "s_addc_u32 s93, s91, 0\n\t" "v_mfma_f32_16x16x32_bf16 v[252:255], a[148:151], a[60:63], v[252:255]\n\t"
    "v_mul_f32 v112, v112, v16\n\t" "v_mul_f32 v113, v113, v16\n\t" "s_waitcnt lgkmcnt(4)\n\t" "v_mfma_f32_16x16x32_bf16 v[128:131], a[152:155], a[64:67], v[128:131]\n\t"
    "ds_read_b64_tr_b16 a[136:137], %[vr7] offset:0\n\t" "v_mul_f32 v114, v114, v16\n\t" "v_mul_f32 v115, v115, v16\n\t" "v_mfma_f32_16x16x32_bf16 v[128:131], a[156:159], a[80:83], v[128:131]\n\t"
    "ds_read_b64_tr_b16 a[138:139], %[vr7] offset:3584\n\t" "v_exp_f32_e64 v112, v112\n\t" "v_mfma_f32_16x16x32_bf16 v[132:135], a[152:155], a[68:71], v[132:135]\n\t" "v_exp_f32_e64 v113, v113\n\t"
    "v_mfma_f32_16x16x32_bf16 v[132:135], a[156:159], a[84:87], v[132:135]\n\t" "v_exp_f32_e64 v114, v114\n\t" "v_mfma_f32_16x16x32_bf16 v[136:139], a[152:155], a[72:75], v[136:139]\n\t"
    "ds_read_b64_tr_b16 a[140:141], %[vr7] offset:14336\n\t" "v_exp_f32_e64 v115, v115\n\t" "v_mfma_f32_16x16x32_bf16 v[136:139], a[156:159], a[88:91], v[136:139]\n\t"
    "ds_read_b64_tr_b16 a[142:143], %[vr7] offset:17920\n\t" "v_fma_f32 v112, v112, v20, v20\n\t" "v_fma_f32 v113, v113, v20, v20\n\t"
    "v_mfma_f32_16x16x32_bf16 v[140:143], a[152:155], a[76:79], v[140:143]\n\t" "v_fma_f32 v114, v114, v20, v20\n\t" "v_fma_f32 v115, v115, v20, v20\n\t"
    "v_mfma_f32_16x16x32_bf16 v[140:143], a[156:159], a[92:95], v[140:143]\n\t" "v_rcp_f32 v112, v112\n\t" "s_waitcnt lgkmcnt(4)\n\t"
    "v_mfma_f32_16x16x32_bf16 v[240:243], a[128:131], a[64:67], v[240:243]\n\t" "ds_read_b64_tr_b16 a[144:145], %[vr7] offset:7168\n\t" "v_rcp_f32_e64 v113, v113\n\t"
    "v_mfma_f32_16x16x32_bf16 v[240:243], a[132:135], a[80:83], v[240:243]\n\t" "ds_read_b64_tr_b16 a[146:147], %[vr7] offset:10752\n\t" "v_rcp_f32_e64 v114, v114\n\t"
    "v_mfma_f32_16x16x32_bf16 v[244:247], a[128:131], a[68:71], v[244:247]\n\t" "v_rcp_f32_e64 v115, v115\n\t" "v_mfma_f32_16x16x32_bf16 v[244:247], a[132:135], a[84:87], v[244:247]\n\t"
    "v_mul_f32 v224, v224, v112\n\t" "v_mul_f32 v225, v225, v113\n\t" "v_mfma_f32_16x16x32_bf16 v[248:251], a[128:131], a[72:75], v[248:251]\n\t" "ds_read_b64_tr_b16 a[148:149], %[vr7] offset:21504\n\t"
    "v_mul_f32 v226, v226, v114\n\t" "v_mul_f32 v227, v227, v115\n\t" "v_mfma_f32_16x16x32_bf16 v[248:251], a[132:135], a[88:91], v[248:251]\n\t" "ds_read_b64_tr_b16 a[150:151], %[vr7] offset:25088\n\t"
    "v_cvt_pk_bf16_f32 v98, v224, v225\n\t" "v_cvt_pk_bf16_f32 v99, v226, v227\n\t" "v_mfma_f32_16x16x32_bf16 v[252:255], a[128:131], a[76:79], v[252:255]\n\t"
    "global_store_dwordx4 %[va], v[96:99], s[90:91] offset:0 sc1\n\t" "v_mul_f32_e64 v116, v116, v17\n\t" "v_mfma_f32_16x16x32_bf16 v[252:255], a[132:135], a[92:95], v[252:255]\n\t"
    "v_mul_f32 v117, v117, v17\n\t" "v_mul_f32_e64 v118, v118, v17\n\t" "s_waitcnt lgkmcnt(4)\n\t" "v_mfma_f32_16x16x32_bf16 v[128:131], a[136:139], a[96:99], v[128:131]\n\t"
    "v_mul_f32 v119, v119, v17\n\t" "v_exp_f32 v116, v116\n\t" "v_mfma_f32_16x16x32_bf16 v[128:131], a[140:143], a[112:115], v[128:131]\n\t" "v_exp_f32_e64 v117, v117\n\t"
    "v_mfma_f32_16x16x32_bf16 v[132:135], a[136:139], a[100:103], v[132:135]\n\t" "v_exp_f32_e64 v118, v118\n\t" "v_mfma_f32_16x16x32_bf16 v[132:135], a[140:143], a[116:119], v[132:135]\n\t"
    "v_exp_f32_e64 v119, v119\n\t" "v_mfma_f32_16x16x32_bf16 v[136:139], a[136:139], a[104:107], v[136:139]\n\t" "v_fma_f32 v116, v116, v21, v21\n\t" "v_fma_f32 v117, v117, v21, v21\n\t"
    "v_mfma_f32_16x16x32_bf16 v[136:139], a[140:143], a[120:123], v[136:139]\n\t" "v_fma_f32 v118, v118, v21, v21\n\t" "v_fma_f32 v119, v119, v21, v21\n\t"
    "v_mfma_f32_16x16x32_bf16 v[140:143], a[136:139], a[108:111], v[140:143]\n\t" "v_rcp_f32_e64 v116, v116\n\t" "v_mfma_f32_16x16x32_bf16 v[140:143], a[140:143], a[124:127], v[140:143]\n\t"
    "v_rcp_f32 v117, v117\n\t" "s_waitcnt lgkmcnt(0)\n\t" "v_mfma_f32_16x16x32_bf16 v[240:243], a[144:147], a[96:99], v[240:243]\n\t" "v_rcp_f32_e64 v118, v118\n\t"
    "v_mfma_f32_16x16x32_bf16 v[240:243], a[148:151], a[112:115], v[240:243]\n\t" "v_rcp_f32_e64 v119, v119\n\t" "v_mfma_f32_16x16x32_bf16 v[244:247], a[144:147], a[100:103], v[244:247]\n\t"
    "v_mul_f32 v228, v228, v116\n\t" "v_mul_f32 v229, v229, v117\n\t" "v_mfma_f32_16x16x32_bf16 v[244:247], a[148:151], a[116:119], v[244:247]\n\t" "v_mul_f32 v230, v230, v118\n\t"
    "v_mul_f32 v231, v231, v119\n\t" "v_mfma_f32_16x16x32_bf16 v[248:251], a[144:147], a[104:107], v[248:251]\n\t" "v_cvt_pk_bf16_f32 v102, v228, v229\n\t" "v_cvt_pk_bf16_f32 v103, v230, v231\n\t"
    "v_mfma_f32_16x16x32_bf16 v[248:251], a[148:151], a[120:123], v[248:251]\n\t" "global_store_dwordx4 %[va], v[100:103], s[90:91] offset:1024 sc1\n\t" "v_mul_f32_e64 v120, v120, v18\n\t"
    "v_mfma_f32_16x16x32_bf16 v[252:255], a[144:147], a[108:111], v[252:255]\n\t" "v_mul_f32 v121, v121, v18\n\t" "v_mul_f32 v122, v122, v18\n\t"
    "v_mfma_f32_16x16x32_bf16 v[252:255], a[148:151], a[124:127], v[252:255]\n\t" "v_mul_f32 v123, v123, v18\n\t" "v_exp_f32 v120, v120\n\t" "v_exp_f32 v121, v121\n\t" "v_exp_f32 v122, v122\n\t"
    "v_exp_f32_e64 v123, v123\n\t" "v_fma_f32 v120, v120, v22, v22\n\t" "v_fma_f32 v121, v121, v22, v22\n\t" "v_fma_f32 v122, v122, v22, v22\n\t" "v_fma_f32 v123, v123, v22, v22\n\t"
    "v_rcp_f32 v120, v120\n\t" "v_rcp_f32 v121, v121\n\t" "v_rcp_f32 v122, v122\n\t" "v_rcp_f32 v123, v123\n\t" "v_mul_f32 v232, v232, v120\n\t" "v_mul_f32 v233, v233, v121\n\t"
    "v_mul_f32 v234, v234, v122\n\t" "v_mul_f32 v235, v235, v123\n\t" "v_cvt_pk_bf16_f32 v106, v232, v233\n\t" "v_cvt_pk_bf16_f32 v107, v234, v235\n\t"
    "global_store_dwordx4 %[va], v[104:107], s[92:93] offset:0 sc1\n\t" "v_mul_f32 v124, v124, v19\n\t" "v_mul_f32 v125, v125, v19\n\t" "v_mul_f32 v126, v126, v19\n\t" "v_mul_f32 v127, v127, v19\n\t"
    "v_exp_f32 v124, v124\n\t" "v_exp_f32 v125, v125\n\t" "v_exp_f32 v126, v126\n\t" "v_exp_f32 v127, v127\n\t" "v_fma_f32 v124, v124, v23, v23\n\t" "v_fma_f32 v125, v125, v23, v23\n\t"
    "v_fma_f32 v126, v126, v23, v23\n\t" "v_fma_f32 v127, v127, v23, v23\n\t" "v_rcp_f32 v124, v124\n\t" "v_rcp_f32 v125, v125\n\t" "v_rcp_f32 v126, v126\n\t" "v_rcp_f32 v127, v127\n\t"
    "v_mul_f32 v236, v236, v124\n\t" "v_mul_f32 v237, v237, v125\n\t" "v_mul_f32 v238, v238, v126\n\t" "v_mul_f32 v239, v239, v127\n\t" "v_cvt_pk_bf16_f32 v110, v236, v237\n\t"
    "v_cvt_pk_bf16_f32 v111, v238, v239\n\t" "global_store_dwordx4 %[va], v[108:111], s[92:93] offset:1024 sc1\n\t" "s_nop 7\n\t" "s_add_u32 s90, %[shl], %[go3]\n\t" "s_addc_u32 s91, %[shh], 0\n\t"
    "s_add_u32 s92, s90, 229376\n\t" "s_addc_u32 s93, s91, 0\n\t" "v_pk_mul_f32 v[128:129], v[128:129], v[16:17] op_sel:[0,0] op_sel_hi:[1,0]\n\t"
    "v_pk_mul_f32 v[130:131], v[130:131], v[16:17] op_sel:[0,0] op_sel_hi:[1,0]\n\t" "v_exp_f32 v128, v128\n\t" "v_exp_f32 v129, v129\n\t" "v_exp_f32 v130, v130\n\t" "v_exp_f32 v131, v131\n\t"
    "v_pk_fma_f32 v[128:129], v[128:129], v[20:21], v[20:21] op_sel:[0,0,0] op_sel_hi:[1,0,0]\n\t" "v_pk_fma_f32 v[130:131], v[130:131], v[20:21], v[20:21] op_sel:[0,0,0] op_sel_hi:[1,0,0]\n\t"
    "v_rcp_f32 v128, v128\n\t" "v_rcp_f32 v129, v129\n\t" "v_rcp_f32 v130, v130\n\t" "v_rcp_f32 v131, v131\n\t" "v_pk_mul_f32 v[240:241], v[240:241], v[128:129]\n\t"
    "v_pk_mul_f32 v[242:243], v[242:243], v[130:131]\n\t" "v_cvt_pk_bf16_f32 v128, v240, v241\n\t" "v_cvt_pk_bf16_f32 v129, v242, v243\n\t" "v_lshrrev_b32_e64 v31, 1, %[va]\n\t"
    "global_store_dwordx2 v31, v[128:129], s[90:91] offset:0 sc1\n\t" "v_pk_mul_f32 v[132:133], v[132:133], v[16:17] op_sel:[0,1] op_sel_hi:[1,1]\n\t"
    "v_pk_mul_f32 v[134:135], v[134:135], v[16:17] op_sel:[0,1] op_sel_hi:[1,1]\n\t" "v_exp_f32 v132, v132\n\t" "v_exp_f32 v133, v133\n\t" "v_exp_f32 v134, v134\n\t" "v_exp_f32 v135, v135\n\t"
    "v_pk_fma_f32 v[132:133], v[132:133], v[20:21], v[20:21] op_sel:[0,1,1] op_sel_hi:[1,1,1]\n\t" "v_pk_fma_f32 v[134:135], v[134:135], v[20:21], v[20:21] op_sel:[0,1,1] op_sel_hi:[1,1,1]\n\t"
    "v_rcp_f32 v132, v132\n\t" "v_rcp_f32 v133, v133\n\t" "v_rcp_f32 v134, v134\n\t" "v_rcp_f32 v135, v135\n\t" "v_pk_mul_f32 v[244:245], v[244:245], v[132:133]\n\t"
    "v_pk_mul_f32 v[246:247], v[246:247], v[134:135]\n\t" "v_cvt_pk_bf16_f32 v132, v244, v245\n\t" "v_cvt_pk_bf16_f32 v133, v246, v247\n\t"
    "global_store_dwordx2 v31, v[132:133], s[90:91] offset:512 sc1\n\t" "v_pk_mul_f32 v[136:137], v[136:137], v[18:19] op_sel:[0,0] op_sel_hi:[1,0]\n\t"
    "v_pk_mul_f32 v[138:139], v[138:139], v[18:19] op_sel:[0,0] op_sel_hi:[1,0]\n\t" "v_exp_f32 v136, v136\n\t" "v_exp_f32 v137, v137\n\t" "v_exp_f32 v138, v138\n\t" "v_exp_f32 v139, v139\n\t"
    "v_pk_fma_f32 v[136:137], v[136:137], v[22:23], v[22:23] op_sel:[0,0,0] op_sel_hi:[1,0,0]\n\t" "v_pk_fma_f32 v[138:139], v[138:139], v[22:23], v[22:23] op_sel:[0,0,0] op_sel_hi:[1,0,0]\n\t"
    "v_rcp_f32 v136, v136\n\t" "v_rcp_f32 v137, v137\n\t" "v_rcp_f32 v138, v138\n\t" "v_rcp_f32 v139, v139\n\t" "v_pk_mul_f32 v[248:249], v[248:249], v[136:137]\n\t"
    "v_pk_mul_f32 v[250:251], v[250:251], v[138:139]\n\t" "v_cvt_pk_bf16_f32 v136, v248, v249\n\t" "v_cvt_pk_bf16_f32 v137, v250, v251\n\t"
    "global_store_dwordx2 v31, v[136:137], s[92:93] offset:0 sc1\n\t" "v_pk_mul_f32 v[140:141], v[140:141], v[18:19] op_sel:[0,1] op_sel_hi:[1,1]\n\t"
    "v_pk_mul_f32 v[142:143], v[142:143], v[18:19] op_sel:[0,1] op_sel_hi:[1,1]\n\t" "v_exp_f32 v140, v140\n\t" "v_exp_f32 v141, v141\n\t" "v_exp_f32 v142, v142\n\t" "v_exp_f32 v143, v143\n\t"
    "v_pk_fma_f32 v[140:141], v[140:141], v[22:23], v[22:23] op_sel:[0,1,1] op_sel_hi:[1,1,1]\n\t" "v_pk_fma_f32 v[142:143], v[142:143], v[22:23], v[22:23] op_sel:[0,1,1] op_sel_hi:[1,1,1]\n\t"
    "v_rcp_f32 v140, v140\n\t" "v_rcp_f32 v141, v141\n\t" "v_rcp_f32 v142, v142\n\t" "v_rcp_f32 v143, v143\n\t" "v_pk_mul_f32 v[252:253], v[252:253], v[140:141]\n\t"
    "v_pk_mul_f32 v[254:255], v[254:255], v[142:143]\n\t" "v_cvt_pk_bf16_f32 v140, v252, v253\n\t" "v_cvt_pk_bf16_f32 v141, v254, v255\n\t"
    "global_store_dwordx2 v31, v[140:141], s[92:93] offset:512 sc1\n\t" "v_mov_b32 v28, 0\n\t" "v_mov_b64 v[30:31], %[tag]\n\t" "s_waitcnt vmcnt(0)\n\t" "s_barrier\n\t"
    "s_cmp_eq_u32 %[sld], %[sl0]\n\t" "s_cselect_b64 exec, 1, 0\n\t" "global_store_dwordx2 v28, v[30:31], %[sc] sc1\n\t" "s_mov_b64 exec, -1\n\t" "s_add_u32 m0, %[sld], 49152\n\t" "s_nop 0\n\t"
    "global_load_lds_dwordx4 v29, %[sbw] offset:0\n\t" "s_add_u32 m0, %[sld], 53120\n\t" "s_nop 0\n\t" "global_load_lds_dwordx4 v29, %[sbw] offset:128\n\t" "v_add_u32 v29, 0x10000, v29\n\t"
    "s_add_u32 m0, %[sld], 57344\n\t" "s_nop 0\n\t" "global_load_lds_dwordx4 v29, %[sbw] offset:0\n\t" "s_add_u32 m0, %[sld], 61312\n\t" "s_nop 0\n\t" "global_load_lds_dwordx4 v29, %[sbw] offset:128\n\t"
    "v_add_u32 v29, 0x10000, v29\n\t" "s_add_u32 m0, %[sld], 65536\n\t" "s_nop 0\n\t" "global_load_lds_dwordx4 v29, %[sbw] offset:0\n\t" "s_add_u32 m0, %[sld], 69504\n\t" "s_nop 0\n\t"
    "global_load_lds_dwordx4 v29, %[sbw] offset:128\n\t" "v_add_u32 v29, 0x10000, v29\n\t" "s_add_u32 m0, %[sld], 73728\n\t" "s_nop 0\n\t" "global_load_lds_dwordx4 v29, %[sbw] offset:0\n\t"
    "s_add_u32 m0, %[sld], 77696\n\t" "s_nop 0\n\t" "global_load_lds_dwordx4 v29, %[sbw] offset:128\n\t" "v_add_u32 v29, 0x10000, v29\n\t"
    : "=&{v[24:25]}"(fl)
    : [va] "{v0}"(va), [ve] "{a248}"(ve), [vb0] "{v1}"(vb[0]), [vb1] "{v2}"(vb[1]), [vb2] "{v3}"(vb[2]), [vb3] "{v4}"(vb[3]), [vr0] "{v5}"(vr[0]), [vr1] "{v6}"(vr[1]), [vr2] "{v7}"(vr[2]), [vr3] "{v8}"(vr[3]), [vr4] "{v9}"(vr[4]), [vr5] "{v10}"(vr[5]), [vr6] "{v11}"(vr[6]), [vr7] "{v12}"(vr[7]), [gt0] "s"(gt[0]), [go0] "s"(go[0]), [gt1] "s"(gt[1]), [go1] "s"(go[1]), [gt2] "s"(gt[2]), [go2] "s"(go[2]), [gt3] "s"(gt[3]), [go3] "s"(go[3]), [ct] "{a249}"(ct), [cr0] "{a250}"(cr0), [cr1] "{a251}"(cr1), [cw] "{a252}"(cw), [cp] "{a253}"(vp), [vs] "{a254}"(vs), [vf] "{v13}"(vf), [sq] "s"(sq), [wid] "s"(wid), [tag] "s"(tag), [cd] "s"(cd), [sa] "s"(sa), [sb] "s"(sb + 917504), [se] "s"(se), [ss] "s"(ss), [sl] "s"(sl), [su0] "s"(su0), [su6] "s"(su6), [sr] "s"(sr), [su] "s"(su), [srl] "s"(srl), [rb] "s"(ldsb + RAW), [shl] "s"(shl), [shh] "s"(shh), [wa] "s"(wa), [wb] "s"(wb), [e0] "s"(e0), [e1] "s"(e1), [m01] "s"(m01), [m6] "s"(m6), [tp0] "s"(tp0), [vbd] "{a255}"(vbd), [sbw] "s"(sbd), [sld] "s"(sld), [sl0] "s"(ldsb), [sc] "s"(scnt)
    : "v16", "v17", "v18", "v19", "v20", "v21", "v22", "v23", "v26", "v27", "v28", "v29", "v30", "v31", "v32", "v33", "v34", "v35", "v36", "v37", "v38", "v39", "v40", "v41", "v42", "v43", "v44", "v45", "v46", "v47", "v48", "v49", "v50", "v51", "v52", "v53", "v54", "v55", "v56", "v57", "v58", "v59", "v60", "v61", "v62", "v63", "v64", "v65", "v66", "v67", "v68", "v69", "v70", "v71", "v72", "v73", "v74", "v75", "v76", "v77", "v78", "v79", "v80", "v81", "v82", "v83", "v84", "v85", "v86", "v87", "v88", "v89", "v90", "v91", "v92", "v93", "v94", "v95", "v96", "v97", "v98", "v99", "v100", "v101", "v102", "v103", "v104", "v105", "v106", "v107", "v108", "v109", "v110", "v111", "v112", "v113", "v114", "v115", "v116", "v117", "v118", "v119", "v120", "v121", "v122", "v123", "v124", "v125", "v126", "v127", "v128", "v129", "v130", "v131", "v132", "v133", "v134", "v135", "v136", "v137", "v138", "v139", "v140", "v141", "v142", "v143", "v144", "v145", "v146", "v147", "v148", "v149", "v150", "v151", "v152", "v153", "v154", "v155", "v156", "v157", "v158", "v159", "v160", "v161", "v162", "v163", "v164", "v165", "v166", "v167", "v168", "v169", "v170", "v171", "v172", "v173", "v174", "v175", "v176", "v177", "v178", "v179", "v180", "v181", "v182", "v183", "v184", "v185", "v186", "v187", "v188", "v189", "v190", "v191", "v192", "v193", "v194", "v195", "v196", "v197", "v198", "v199", "v200", "v201", "v202", "v203", "v204", "v205", "v206", "v207", "v208", "v209", "v210", "v211", "v212", "v213", "v214", "v215", "v216", "v217", "v218", "v219", "v220", "v221", "v222", "v223", "v224", "v225", "v226", "v227", "v228", "v229", "v230", "v231", "v232", "v233", "v234", "v235", "v236", "v237", "v238", "v239", "v240", "v241", "v242", "v243", "v244", "v245", "v246", "v247", "v248", "v249", "v250", "v251", "v252", "v253", "v254", "v255", "vcc", "a0", "a1", "a2", "a3", "a4", "a5", "a6", "a7", "a8", "a9", "a10", "a11", "a12", "a13", "a14", "a15", "a16", "a17", "a18", "a19", "a20", "a21", "a22", "a23", "a24", "a25", "a26", "a27", "a28", "a29", "a30", "a31", "a32", "a33", "a34", "a35", "a36", "a37", "a38", "a39", "a40", "a41", "a42", "a43", "a44", "a45", "a46", "a47", "a48", "a49", "a50", "a51", "a52", "a53", "a54", "a55", "a56", "a57", "a58", "a59", "a60", "a61", "a62", "a63", "a64", "a65", "a66", "a67", "a68", "a69", "a70", "a71", "a72", "a73", "a74", "a75", "a76", "a77", "a78", "a79", "a80", "a81", "a82", "a83", "a84", "a85", "a86", "a87", "a88", "a89", "a90", "a91", "a92", "a93", "a94", "a95", "a96", "a97", "a98", "a99", "a100", "a101", "a102", "a103", "a104", "a105", "a106", "a107", "a108", "a109", "a110", "a111", "a112", "a113", "a114", "a115", "a116", "a117", "a118", "a119", "a120", "a121", "a122", "a123", "a124", "a125", "a126", "a127", "a128", "a129", "a130", "a131", "a132", "a133", "a134", "a135", "a136", "a137", "a138", "a139", "a140", "a141", "a142", "a143", "a144", "a145", "a146", "a147", "a148", "a149", "a150", "a151", "a152", "a153", "a154", "a155", "a156", "a157", "a158", "a159", "a160", "a161", "a162", "a163", "a164", "a165", "a166", "a167", "a168", "a169", "a170", "a171", "a172", "a173", "a174", "a175", "a176", "a177", "a178", "a179", "a180", "a181", "a182", "a183", "a184", "a185", "a186", "a187", "a188", "a189", "a190", "a191", "a192", "a193", "a194", "a195", "a196", "a197", "a198", "a199", "a200", "a201", "a202", "a203", "a204", "a205", "a206", "a207", "a208", "a209", "a210", "a211", "a212", "a213", "a214", "a215", "a216", "a217", "a218", "a219", "a220", "a221", "a222", "a223", "a224", "a225", "a226", "a227", "a228", "a229", "a230", "a231", "a232", "a233", "a234", "a235", "a236", "a237", "a238", "a239", "a240", "a241", "a242", "a243", "a244", "a245", "a246", "a247", "s76", "s77", "s78", "s79", "s80", "s81", "s82", "s83", "s84", "s85", "s86", "s87", "s88", "s89", "s90", "s91", "s92", "s93", "s94", "s95", "s96", "s97", "s98", "s99", "scc", "memory");
  } } }
  {
  asm volatile("" : "+s"(bx), "+s"(wid), "+s"(ws)); asm volatile("" : "+s"(tag));
  const int xcd = bx & 7, loc = bx >> 3, lane = __builtin_amdgcn_mbcnt_hi(~0u, __builtin_amdgcn_mbcnt_lo(~0u, 0u));
  const int kh = xcd & 1, rb = (xcd >> 1) * 4 + (loc & 3), cb = ROT, fr = lane & 15, fq = lane >> 4;
  unsigned long long fl;
  const unsigned vx = lane * 16, vo = lane * 8, vg = fq * 16, vf = fr * FS, vc = lane * 4 + 4096, sl0 = ldsb + wid * 1024 + (kh ^ 1) * 4096, sl1 = sl0 ^ 4096; const int k0 = KD, wr = 56 - k0;
  const unsigned ww0 = ldsb + wid * 1024 + vx, ww1 = ww0 + 65536, ww2 = ww0 + 131072;
  unsigned vb0 = f_vb(wid, lane) + (kh ^ 1) * 128 + (k0 + 12) * 65536, vb1 = vb0 ^ 128, vr[24];
#pragma unroll
  for (int j = 0; j < 12; ++j) { vr[j] = ldsb + f_vr(lane, j & 3) + (kh ^ 1) * 4096 + (j >> 2) * 65536; vr[12 + j] = vr[j] ^ 4096; }
  const unsigned one = 0x3f80u << (lane & 1) * 16, tp = ldsb + 114688 + wid * 4096; const u32x2 id = {(lane & 2) ? 0u : one, (lane & 2) ? one : 0u};
  const unsigned tw0 = tp + fr * 128 + (((fq >> 1) ^ (fr & 7)) << 4) + (fq & 1) * 8, tw1 = tw0 ^ 32, tw2 = tw0 ^ 64, tw3 = tw0 ^ 96, tr = tp + (lane >> 3) * 128 + (((lane & 7) ^ (lane >> 3)) << 4), vs = (lane >> 3) * 2048 + (lane & 7) * 16;
  const char* sw = ws + (unsigned)(W_TAGS + HF * 8 + FA((rb >> 1) * 32 + kh * 16));
  const char* sa = ws + (unsigned)(W_HH + ((rb * 4 + wid) * 112 + kh * 56 + k0) * 2048); const char* sb = (const char*)pfw + (unsigned)(kh * 1792 * 2048 + cb * 256); const char* sg = ws + (unsigned)(W_GB + bx * 256);
  const char* sm = ws + (unsigned)(W_XCH + (bx * 4 + wid) * 4352); const char* st = ws + (unsigned)(W_XCH + ((bx ^ 1) * 4 + wid) * 4352); const u16* so = out + (unsigned)((rb * 128 + wid * 32) * D_ + cb * 128 + kh * 64);
  asm volatile(".p2align 3\n\t" "s_mov_b64 s[40:41], %[sa]\n\t" "s_mov_b32 s45, 0xffc90000\n\t" "s_add_u32 m0, %[sl0], 81920\n\t" "s_nop 0\n\t" "global_load_lds_dwordx4 %[vb0], %[sb]\n\t"
    "v_add_u32 %[vb0], 0x10000, %[vb0]\n\t" "s_add_u32 m0, %[sl1], 81920\n\t" "s_nop 0\n\t" "global_load_lds_dwordx4 %[vb1], %[sb]\n\t" "v_add_u32 %[vb1], 0x10000, %[vb1]\n\t"
    "s_add_u32 m0, %[sl0], 90112\n\t" "s_nop 0\n\t" "global_load_lds_dwordx4 %[vb0], %[sb]\n\t" "v_add_u32 %[vb0], 0x10000, %[vb0]\n\t" "s_add_u32 m0, %[sl1], 90112\n\t" "s_nop 0\n\t"
    "global_load_lds_dwordx4 %[vb1], %[sb]\n\t" "v_add_u32 %[vb1], 0x10000, %[vb1]\n\t" "s_add_u32 m0, %[sl0], 98304\n\t" "s_nop 0\n\t" "global_load_lds_dwordx4 %[vb0], %[sb]\n\t"
    "v_add_u32 %[vb0], 0x10000, %[vb0]\n\t" "s_add_u32 m0, %[sl1], 98304\n\t" "s_nop 0\n\t" "global_load_lds_dwordx4 %[vb1], %[sb]\n\t" "v_add_u32 %[vb1], 0x10000, %[vb1]\n\t"
    "s_add_u32 m0, %[sl0], 106496\n\t" "s_nop 0\n\t" "global_load_lds_dwordx4 %[vb0], %[sb]\n\t" "v_add_u32 %[vb0], 0x10000, %[vb0]\n\t" "s_add_u32 m0, %[sl1], 106496\n\t" "s_nop 0\n\t"
    "global_load_lds_dwordx4 %[vb1], %[sb]\n\t" "v_add_u32 %[vb1], 0x10000, %[vb1]\n\t" "ds_read_b64_tr_b16 a[176:177], %[vr0] offset:24576\n\t" "ds_read_b64_tr_b16 a[178:179], %[vr0] offset:26624\n\t"
    "ds_read_b64_tr_b16 a[180:181], %[vr0] offset:32768\n\t" "ds_read_b64_tr_b16 a[182:183], %[vr0] offset:34816\n\t" "ds_read_b64_tr_b16 a[184:185], %[vr1] offset:24576\n\t"
    "ds_read_b64_tr_b16 a[186:187], %[vr1] offset:26624\n\t" "ds_read_b64_tr_b16 a[188:189], %[vr1] offset:32768\n\t" "ds_read_b64_tr_b16 a[190:191], %[vr1] offset:34816\n\t"
    "ds_read_b64_tr_b16 a[192:193], %[vr2] offset:24576\n\t" "ds_read_b64_tr_b16 a[194:195], %[vr2] offset:26624\n\t" "ds_read_b64_tr_b16 a[196:197], %[vr2] offset:32768\n\t"
    "ds_read_b64_tr_b16 a[198:199], %[vr2] offset:34816\n\t" "s_mov_b32 s44, 0x400000\n\t" "s_cmp_eq_u32 %[wid], 0\n\t" "s_cselect_b64 exec, 0xffff, 0\n\t"
    "global_load_dwordx2 %[fl], %[vf], %[sw] sc1\n\t" "s_waitcnt vmcnt(0)\n\t" ".Lwait_%=:\n\t" "v_cmp_ne_u64 vcc, %[tag], %[fl]\n\t" "s_nop 4\n\t" "s_cbranch_vccz .Lgo_%=\n\t" "s_mov_b64 exec, vcc\n\t"
    "s_sleep 1\n\t" "global_load_dwordx2 %[fl], %[vf], %[sw] sc1\n\t" "s_sub_u32 s44, s44, 1\n\t" "s_waitcnt vmcnt(0)\n\t" "s_cbranch_scc0 .Lwait_%=\n\t" ".Lgo_%=:\n\t" "s_mov_b64 exec, -1\n\t"
    "s_barrier\n\t" "global_load_dwordx4 a[0:3], %[vx], s[40:41] offset:-4096 sc1\n\t" "global_load_dwordx4 a[4:7], %[vx], s[40:41] offset:-3072 sc1\n\t"
    "global_load_dwordx4 a[8:11], %[vx], s[40:41] offset:-2048 sc1\n\t" "global_load_dwordx4 a[12:15], %[vx], s[40:41] offset:-1024 sc1\n\t"
    "global_load_dwordx4 a[16:19], %[vx], s[40:41] offset:0 sc1\n\t" "global_load_dwordx4 a[20:23], %[vx], s[40:41] offset:1024 sc1\n\t" "global_load_dwordx2 a[24:25], %[vo], s[40:41] offset:2048 sc1\n\t"
    "global_load_dwordx2 a[26:27], %[vo], s[40:41] offset:3072 sc1\n\t" "global_load_dwordx2 a[28:29], %[vo], s[40:41] offset:2560 sc1\n\t"
    "global_load_dwordx2 a[30:31], %[vo], s[40:41] offset:3584 sc1\n\t" "s_waitcnt vmcnt(8)\n\t" "s_waitcnt lgkmcnt(4) vmcnt(6)\n\t" "v_mfma_f32_16x16x32_bf16 v[224:227], a[176:179], a[0:3], 0\n\t"
    "ds_read_b64_tr_b16 a[200:201], %[vr3] offset:24576\n\t" "s_add_u32 s40, s40, 8192\n\t" "s_addc_u32 s41, s41, 0\n\t" "v_mfma_f32_16x16x32_bf16 v[224:227], a[180:183], a[8:11], v[224:227]\n\t"
    "ds_read_b64_tr_b16 a[202:203], %[vr3] offset:26624\n\t" "global_load_dwordx4 a[32:35], %[vx], s[40:41] offset:-4096 sc1\n\t" "v_mfma_f32_16x16x32_bf16 v[240:243], a[176:179], a[4:7], 0\n\t"
    "ds_read_b64_tr_b16 a[204:205], %[vr3] offset:32768\n\t" "v_mfma_f32_16x16x32_bf16 v[240:243], a[180:183], a[12:15], v[240:243]\n\t" "ds_read_b64_tr_b16 a[206:207], %[vr3] offset:34816\n\t"
    "v_mfma_f32_16x16x32_bf16 v[228:231], a[184:187], a[0:3], 0\n\t" "ds_read_b64_tr_b16 a[176:177], %[vr12] offset:24576\n\t" "v_mfma_f32_16x16x32_bf16 v[228:231], a[188:191], a[8:11], v[228:231]\n\t"
    "ds_read_b64_tr_b16 a[178:179], %[vr12] offset:26624\n\t" "global_load_dwordx4 a[36:39], %[vx], s[40:41] offset:-3072 sc1\n\t" "v_mfma_f32_16x16x32_bf16 v[244:247], a[184:187], a[4:7], 0\n\t"
    "ds_read_b64_tr_b16 a[180:181], %[vr12] offset:32768\n\t" "v_mfma_f32_16x16x32_bf16 v[244:247], a[188:191], a[12:15], v[244:247]\n\t" "ds_read_b64_tr_b16 a[182:183], %[vr12] offset:34816\n\t"
    "s_waitcnt lgkmcnt(4)\n\t" "v_mfma_f32_16x16x32_bf16 v[232:235], a[192:195], a[0:3], 0\n\t" "ds_read_b64_tr_b16 a[184:185], %[vr13] offset:24576\n\t"
    "v_mfma_f32_16x16x32_bf16 v[232:235], a[196:199], a[8:11], v[232:235]\n\t" "ds_read_b64_tr_b16 a[186:187], %[vr13] offset:26624\n\t"
    "global_load_dwordx4 a[40:43], %[vx], s[40:41] offset:-2048 sc1\n\t" "v_mfma_f32_16x16x32_bf16 v[248:251], a[192:195], a[4:7], 0\n\t" "ds_read_b64_tr_b16 a[188:189], %[vr13] offset:32768\n\t"
    "v_mfma_f32_16x16x32_bf16 v[248:251], a[196:199], a[12:15], v[248:251]\n\t" "ds_read_b64_tr_b16 a[190:191], %[vr13] offset:34816\n\t" "v_mfma_f32_16x16x32_bf16 v[236:239], a[200:203], a[0:3], 0\n\t"
    "ds_read_b64_tr_b16 a[192:193], %[vr14] offset:24576\n\t" "v_mfma_f32_16x16x32_bf16 v[236:239], a[204:207], a[8:11], v[236:239]\n\t" "ds_read_b64_tr_b16 a[194:195], %[vr14] offset:26624\n\t"
    "global_load_dwordx4 a[44:47], %[vx], s[40:41] offset:-1024 sc1\n\t" "v_mfma_f32_16x16x32_bf16 v[252:255], a[200:203], a[4:7], 0\n\t" "ds_read_b64_tr_b16 a[196:197], %[vr14] offset:32768\n\t"
    "v_mfma_f32_16x16x32_bf16 v[252:255], a[204:207], a[12:15], v[252:255]\n\t" "ds_read_b64_tr_b16 a[198:199], %[vr14] offset:34816\n\t" "s_waitcnt lgkmcnt(4)\n\t"
    "v_mfma_f32_16x16x32_bf16 v[96:99], a[176:179], a[0:3], 0\n\t" "ds_read_b64_tr_b16 a[200:201], %[vr15] offset:24576\n\t" "v_mfma_f32_16x16x32_bf16 v[96:99], a[180:183], a[8:11], v[96:99]\n\t"
    "ds_read_b64_tr_b16 a[202:203], %[vr15] offset:26624\n\t" "global_load_dwordx4 a[48:51], %[vx], s[40:41] offset:0 sc1\n\t" "v_mfma_f32_16x16x32_bf16 v[100:103], a[176:179], a[4:7], 0\n\t"
    "ds_read_b64_tr_b16 a[204:205], %[vr15] offset:32768\n\t" "v_mfma_f32_16x16x32_bf16 v[100:103], a[180:183], a[12:15], v[100:103]\n\t" "ds_read_b64_tr_b16 a[206:207], %[vr15] offset:34816\n\t"
    "v_mfma_f32_16x16x32_bf16 v[104:107], a[184:187], a[0:3], 0\n\t" "s_barrier\n\t" "ds_read_b64_tr_b16 a[176:177], %[vr0] offset:40960\n\t"
    "v_mfma_f32_16x16x32_bf16 v[104:107], a[188:191], a[8:11], v[104:107]\n\t" "ds_read_b64_tr_b16 a[178:179], %[vr0] offset:43008\n\t" "global_load_dwordx4 a[52:55], %[vx], s[40:41] offset:1024 sc1\n\t"
    "v_mfma_f32_16x16x32_bf16 v[108:111], a[184:187], a[4:7], 0\n\t" "ds_read_b64_tr_b16 a[180:181], %[vr8] offset:8192\n\t" "v_mfma_f32_16x16x32_bf16 v[108:111], a[188:191], a[12:15], v[108:111]\n\t"
    "ds_read_b64_tr_b16 a[182:183], %[vr8] offset:10240\n\t" "s_waitcnt lgkmcnt(4)\n\t" "v_mfma_f32_16x16x32_bf16 v[112:115], a[192:195], a[0:3], 0\n\t"
    "ds_read_b64_tr_b16 a[184:185], %[vr1] offset:40960\n\t" "v_mfma_f32_16x16x32_bf16 v[112:115], a[196:199], a[8:11], v[112:115]\n\t" "ds_read_b64_tr_b16 a[186:187], %[vr1] offset:43008\n\t"
    "global_load_dwordx4 a[56:59], %[vx], s[40:41] offset:2048 sc1\n\t" "v_mfma_f32_16x16x32_bf16 v[116:119], a[192:195], a[4:7], 0\n\t" "ds_read_b64_tr_b16 a[188:189], %[vr9] offset:8192\n\t"
    "v_mfma_f32_16x16x32_bf16 v[116:119], a[196:199], a[12:15], v[116:119]\n\t" "ds_read_b64_tr_b16 a[190:191], %[vr9] offset:10240\n\t" "v_mfma_f32_16x16x32_bf16 v[120:123], a[200:203], a[0:3], 0\n\t"
    "ds_read_b64_tr_b16 a[192:193], %[vr2] offset:40960\n\t" "v_mfma_f32_16x16x32_bf16 v[120:123], a[204:207], a[8:11], v[120:123]\n\t" "ds_read_b64_tr_b16 a[194:195], %[vr2] offset:43008\n\t"
    "global_load_dwordx4 a[60:63], %[vx], s[40:41] offset:3072 sc1\n\t" "v_mfma_f32_16x16x32_bf16 v[124:127], a[200:203], a[4:7], 0\n\t" "ds_read_b64_tr_b16 a[196:197], %[vr10] offset:8192\n\t"
    "v_mfma_f32_16x16x32_bf16 v[124:127], a[204:207], a[12:15], v[124:127]\n\t" "ds_read_b64_tr_b16 a[198:199], %[vr10] offset:10240\n\t" "s_waitcnt lgkmcnt(4) vmcnt(8)\n\t"
    "v_mfma_f32_16x16x32_bf16 v[224:227], a[176:179], a[16:19], v[224:227]\n\t" "ds_read_b64_tr_b16 a[200:201], %[vr3] offset:40960\n\t" "s_add_u32 s40, s40, 8192\n\t" "s_addc_u32 s41, s41, 0\n\t"
    "v_mfma_f32_16x16x32_bf16 v[224:227], a[180:183], a[24:27], v[224:227]\n\t" "ds_read_b64_tr_b16 a[202:203], %[vr3] offset:43008\n\t"
    "global_load_dwordx4 a[64:67], %[vx], s[40:41] offset:-4096 sc1\n\t" "v_mfma_f32_16x16x32_bf16 v[240:243], a[176:179], a[20:23], v[240:243]\n\t"
    "ds_read_b64_tr_b16 a[204:205], %[vr11] offset:8192\n\t" "v_mfma_f32_16x16x32_bf16 v[240:243], a[180:183], a[28:31], v[240:243]\n\t" "ds_read_b64_tr_b16 a[206:207], %[vr11] offset:10240\n\t"
    "v_mfma_f32_16x16x32_bf16 v[228:231], a[184:187], a[16:19], v[228:231]\n\t" "ds_read_b64_tr_b16 a[176:177], %[vr12] offset:40960\n\t"
    "v_mfma_f32_16x16x32_bf16 v[228:231], a[188:191], a[24:27], v[228:231]\n\t" "ds_read_b64_tr_b16 a[178:179], %[vr12] offset:43008\n\t" "global_load_dword a255, %[vx], %[cd]\n\t"
    "v_mfma_f32_16x16x32_bf16 v[244:247], a[184:187], a[20:23], v[244:247]\n\t" "ds_read_b64_tr_b16 a[180:181], %[vr20] offset:8192\n\t"
    "v_mfma_f32_16x16x32_bf16 v[244:247], a[188:191], a[28:31], v[244:247]\n\t" "ds_read_b64_tr_b16 a[182:183], %[vr20] offset:10240\n\t" "s_waitcnt lgkmcnt(4)\n\t"
    "v_mfma_f32_16x16x32_bf16 v[232:235], a[192:195], a[16:19], v[232:235]\n\t" "ds_read_b64_tr_b16 a[184:185], %[vr13] offset:40960\n\t"
    "global_load_dwordx4 a[68:71], %[vx], s[40:41] offset:-3072 sc1\n\t" "v_mfma_f32_16x16x32_bf16 v[232:235], a[196:199], a[24:27], v[232:235]\n\t"
    "ds_read_b64_tr_b16 a[186:187], %[vr13] offset:43008\n\t" "v_mfma_f32_16x16x32_bf16 v[248:251], a[192:195], a[20:23], v[248:251]\n\t" "ds_read_b64_tr_b16 a[188:189], %[vr21] offset:8192\n\t"
    "v_mfma_f32_16x16x32_bf16 v[248:251], a[196:199], a[28:31], v[248:251]\n\t" "ds_read_b64_tr_b16 a[190:191], %[vr21] offset:10240\n\t"
    "global_load_dwordx4 a[72:75], %[vx], s[40:41] offset:-2048 sc1\n\t" "v_mfma_f32_16x16x32_bf16 v[236:239], a[200:203], a[16:19], v[236:239]\n\t"
    "ds_read_b64_tr_b16 a[192:193], %[vr14] offset:40960\n\t" "v_mfma_f32_16x16x32_bf16 v[236:239], a[204:207], a[24:27], v[236:239]\n\t" "ds_read_b64_tr_b16 a[194:195], %[vr14] offset:43008\n\t"
    "v_mfma_f32_16x16x32_bf16 v[252:255], a[200:203], a[20:23], v[252:255]\n\t" "ds_read_b64_tr_b16 a[196:197], %[vr22] offset:8192\n\t"
    "v_mfma_f32_16x16x32_bf16 v[252:255], a[204:207], a[28:31], v[252:255]\n\t" "ds_read_b64_tr_b16 a[198:199], %[vr22] offset:10240\n\t" "s_waitcnt lgkmcnt(4)\n\t"
    "v_mfma_f32_16x16x32_bf16 v[96:99], a[176:179], a[16:19], v[96:99]\n\t" "ds_read_b64_tr_b16 a[200:201], %[vr15] offset:40960\n\t" "global_load_dwordx4 a[76:79], %[vx], s[40:41] offset:-1024 sc1\n\t"
    "v_mfma_f32_16x16x32_bf16 v[96:99], a[180:183], a[24:27], v[96:99]\n\t" "ds_read_b64_tr_b16 a[202:203], %[vr15] offset:43008\n\t"
    "v_mfma_f32_16x16x32_bf16 v[100:103], a[176:179], a[20:23], v[100:103]\n\t" "ds_read_b64_tr_b16 a[204:205], %[vr23] offset:8192\n\t"
    "v_mfma_f32_16x16x32_bf16 v[100:103], a[180:183], a[28:31], v[100:103]\n\t" "ds_read_b64_tr_b16 a[206:207], %[vr23] offset:10240\n\t" "global_load_dwordx2 a[0:1], %[vo], s[40:41] offset:0 sc1\n\t"
    "v_mfma_f32_16x16x32_bf16 v[104:107], a[184:187], a[16:19], v[104:107]\n\t" "s_barrier\n\t" "ds_read_b64_tr_b16 a[176:177], %[vr8] offset:16384\n\t"
    "v_mfma_f32_16x16x32_bf16 v[104:107], a[188:191], a[24:27], v[104:107]\n\t" "ds_read_b64_tr_b16 a[178:179], %[vr8] offset:18432\n\t" "global_load_dwordx2 a[2:3], %[vo], s[40:41] offset:1024 sc1\n\t"
    "v_mfma_f32_16x16x32_bf16 v[108:111], a[184:187], a[20:23], v[108:111]\n\t" "ds_read_b64_tr_b16 a[180:181], %[vr8] offset:24576\n\t"
    "v_mfma_f32_16x16x32_bf16 v[108:111], a[188:191], a[28:31], v[108:111]\n\t" "ds_read_b64_tr_b16 a[182:183], %[vr8] offset:26624\n\t" "s_waitcnt lgkmcnt(4)\n\t"
    "v_mfma_f32_16x16x32_bf16 v[112:115], a[192:195], a[16:19], v[112:115]\n\t" "ds_read_b64_tr_b16 a[184:185], %[vr9] offset:16384\n\t" "global_load_dwordx2 a[4:5], %[vo], s[40:41] offset:512 sc1\n\t"
    "v_mfma_f32_16x16x32_bf16 v[112:115], a[196:199], a[24:27], v[112:115]\n\t" "ds_read_b64_tr_b16 a[186:187], %[vr9] offset:18432\n\t" "global_load_dwordx2 a[6:7], %[vo], s[40:41] offset:1536 sc1\n\t"
    "v_mfma_f32_16x16x32_bf16 v[116:119], a[192:195], a[20:23], v[116:119]\n\t" "ds_read_b64_tr_b16 a[188:189], %[vr9] offset:24576\n\t"
    "v_mfma_f32_16x16x32_bf16 v[116:119], a[196:199], a[28:31], v[116:119]\n\t" "ds_read_b64_tr_b16 a[190:191], %[vr9] offset:26624\n\t" "global_load_dwordx4 a[8:11], %[vx], s[40:41] offset:2048 sc1\n\t"
    "v_mfma_f32_16x16x32_bf16 v[120:123], a[200:203], a[16:19], v[120:123]\n\t" "ds_read_b64_tr_b16 a[192:193], %[vr10] offset:16384\n\t"
    "v_mfma_f32_16x16x32_bf16 v[120:123], a[204:207], a[24:27], v[120:123]\n\t" "ds_read_b64_tr_b16 a[194:195], %[vr10] offset:18432\n\t"
    "v_mfma_f32_16x16x32_bf16 v[124:127], a[200:203], a[20:23], v[124:127]\n\t" "ds_read_b64_tr_b16 a[196:197], %[vr10] offset:24576\n\t"
    "v_mfma_f32_16x16x32_bf16 v[124:127], a[204:207], a[28:31], v[124:127]\n\t" "ds_read_b64_tr_b16 a[198:199], %[vr10] offset:26624\n\t" "s_waitcnt lgkmcnt(4) vmcnt(14)\n\t"
    "v_mfma_f32_16x16x32_bf16 v[224:227], a[176:179], a[32:35], v[224:227]\n\t" "ds_read_b64_tr_b16 a[200:201], %[vr11] offset:16384\n\t"
    "global_load_dwordx4 a[12:15], %[vx], s[40:41] offset:3072 sc1\n\t" "v_mfma_f32_16x16x32_bf16 v[224:227], a[180:183], a[40:43], v[224:227]\n\t"
    "ds_read_b64_tr_b16 a[202:203], %[vr11] offset:18432\n\t" "s_add_u32 s40, s40, 8192\n\t" "s_addc_u32 s41, s41, 0\n\t" "v_mfma_f32_16x16x32_bf16 v[240:243], a[176:179], a[36:39], v[240:243]\n\t"
    "ds_read_b64_tr_b16 a[204:205], %[vr11] offset:24576\n\t" "v_mfma_f32_16x16x32_bf16 v[240:243], a[180:183], a[44:47], v[240:243]\n\t" "ds_read_b64_tr_b16 a[206:207], %[vr11] offset:26624\n\t"
    "global_load_dwordx4 a[16:19], %[vx], s[40:41] offset:-4096 sc1\n\t" "v_mfma_f32_16x16x32_bf16 v[228:231], a[184:187], a[32:35], v[228:231]\n\t"
    "ds_read_b64_tr_b16 a[176:177], %[vr20] offset:16384\n\t" "v_mfma_f32_16x16x32_bf16 v[228:231], a[188:191], a[40:43], v[228:231]\n\t" "ds_read_b64_tr_b16 a[178:179], %[vr20] offset:18432\n\t"
    "v_mfma_f32_16x16x32_bf16 v[244:247], a[184:187], a[36:39], v[244:247]\n\t" "ds_read_b64_tr_b16 a[180:181], %[vr20] offset:24576\n\t"
    "v_mfma_f32_16x16x32_bf16 v[244:247], a[188:191], a[44:47], v[244:247]\n\t" "ds_read_b64_tr_b16 a[182:183], %[vr20] offset:26624\n\t" "s_waitcnt lgkmcnt(4)\n\t"
    "v_mfma_f32_16x16x32_bf16 v[232:235], a[192:195], a[32:35], v[232:235]\n\t" "ds_read_b64_tr_b16 a[184:185], %[vr21] offset:16384\n\t"
    "global_load_dwordx4 a[20:23], %[vx], s[40:41] offset:-3072 sc1\n\t" "v_mfma_f32_16x16x32_bf16 v[232:235], a[196:199], a[40:43], v[232:235]\n\t"
    "ds_read_b64_tr_b16 a[186:187], %[vr21] offset:18432\n\t" "v_mfma_f32_16x16x32_bf16 v[248:251], a[192:195], a[36:39], v[248:251]\n\t" "ds_read_b64_tr_b16 a[188:189], %[vr21] offset:24576\n\t"
    "v_mfma_f32_16x16x32_bf16 v[248:251], a[196:199], a[44:47], v[248:251]\n\t" "ds_read_b64_tr_b16 a[190:191], %[vr21] offset:26624\n\t"
    "global_load_dwordx4 a[24:27], %[vx], s[40:41] offset:-2048 sc1\n\t" "v_mfma_f32_16x16x32_bf16 v[236:239], a[200:203], a[32:35], v[236:239]\n\t"
    "ds_read_b64_tr_b16 a[192:193], %[vr22] offset:16384\n\t" "v_mfma_f32_16x16x32_bf16 v[236:239], a[204:207], a[40:43], v[236:239]\n\t" "ds_read_b64_tr_b16 a[194:195], %[vr22] offset:18432\n\t"
    "v_mfma_f32_16x16x32_bf16 v[252:255], a[200:203], a[36:39], v[252:255]\n\t" "ds_read_b64_tr_b16 a[196:197], %[vr22] offset:24576\n\t"
    "v_mfma_f32_16x16x32_bf16 v[252:255], a[204:207], a[44:47], v[252:255]\n\t" "ds_read_b64_tr_b16 a[198:199], %[vr22] offset:26624\n\t" "s_waitcnt lgkmcnt(4)\n\t"
    "v_mfma_f32_16x16x32_bf16 v[96:99], a[176:179], a[32:35], v[96:99]\n\t" "ds_read_b64_tr_b16 a[200:201], %[vr23] offset:16384\n\t" "global_load_dwordx4 a[28:31], %[vx], s[40:41] offset:-1024 sc1\n\t"
    "v_mfma_f32_16x16x32_bf16 v[96:99], a[180:183], a[40:43], v[96:99]\n\t" "ds_read_b64_tr_b16 a[202:203], %[vr23] offset:18432\n\t" "s_add_u32 m0, %[sl0], 114688\n\t"
    "v_mfma_f32_16x16x32_bf16 v[100:103], a[176:179], a[36:39], v[100:103]\n\t" "ds_read_b64_tr_b16 a[204:205], %[vr23] offset:24576\n\t"
    "v_mfma_f32_16x16x32_bf16 v[100:103], a[180:183], a[44:47], v[100:103]\n\t" "ds_read_b64_tr_b16 a[206:207], %[vr23] offset:26624\n\t" "global_load_lds_dwordx4 %[vb0], %[sb]\n\t"
    "v_mfma_f32_16x16x32_bf16 v[104:107], a[184:187], a[32:35], v[104:107]\n\t" "s_barrier\n\t" "ds_read_b64_tr_b16 a[176:177], %[vr0] offset:0\n\t"
    "v_mfma_f32_16x16x32_bf16 v[104:107], a[188:191], a[40:43], v[104:107]\n\t" "ds_read_b64_tr_b16 a[178:179], %[vr0] offset:2048\n\t" "v_add_u32 %[vb0], 0x10000, %[vb0]\n\t"
    "v_mfma_f32_16x16x32_bf16 v[108:111], a[184:187], a[36:39], v[108:111]\n\t" "ds_read_b64_tr_b16 a[180:181], %[vr0] offset:8192\n\t" "s_add_u32 m0, %[sl1], 114688\n\t"
    "v_mfma_f32_16x16x32_bf16 v[108:111], a[188:191], a[44:47], v[108:111]\n\t" "ds_read_b64_tr_b16 a[182:183], %[vr0] offset:10240\n\t" "s_waitcnt lgkmcnt(4)\n\t"
    "v_mfma_f32_16x16x32_bf16 v[112:115], a[192:195], a[32:35], v[112:115]\n\t" "ds_read_b64_tr_b16 a[184:185], %[vr1] offset:0\n\t" "global_load_lds_dwordx4 %[vb1], %[sb]\n\t"
    "v_mfma_f32_16x16x32_bf16 v[112:115], a[196:199], a[40:43], v[112:115]\n\t" "ds_read_b64_tr_b16 a[186:187], %[vr1] offset:2048\n\t" "v_add_u32 %[vb1], 0x10000, %[vb1]\n\t"
    "v_mfma_f32_16x16x32_bf16 v[116:119], a[192:195], a[36:39], v[116:119]\n\t" "ds_read_b64_tr_b16 a[188:189], %[vr1] offset:8192\n\t" "s_add_u32 m0, %[sl0], 122880\n\t"
    "v_mfma_f32_16x16x32_bf16 v[116:119], a[196:199], a[44:47], v[116:119]\n\t" "ds_read_b64_tr_b16 a[190:191], %[vr1] offset:10240\n\t" "global_load_lds_dwordx4 %[vb0], %[sb]\n\t"
    "v_mfma_f32_16x16x32_bf16 v[120:123], a[200:203], a[32:35], v[120:123]\n\t" "ds_read_b64_tr_b16 a[192:193], %[vr2] offset:0\n\t" "v_add_u32 %[vb0], 0x10000, %[vb0]\n\t"
    "v_mfma_f32_16x16x32_bf16 v[120:123], a[204:207], a[40:43], v[120:123]\n\t" "ds_read_b64_tr_b16 a[194:195], %[vr2] offset:2048\n\t" "s_add_u32 m0, %[sl1], 122880\n\t"
    "v_mfma_f32_16x16x32_bf16 v[124:127], a[200:203], a[36:39], v[124:127]\n\t" "ds_read_b64_tr_b16 a[196:197], %[vr2] offset:8192\n\t"
    "v_mfma_f32_16x16x32_bf16 v[124:127], a[204:207], a[44:47], v[124:127]\n\t" "ds_read_b64_tr_b16 a[198:199], %[vr2] offset:10240\n\t" "s_waitcnt lgkmcnt(4) vmcnt(18)\n\t"
    "v_mfma_f32_16x16x32_bf16 v[224:227], a[176:179], a[48:51], v[224:227]\n\t" "ds_read_b64_tr_b16 a[200:201], %[vr3] offset:0\n\t" "global_load_lds_dwordx4 %[vb1], %[sb]\n\t"
    "v_mfma_f32_16x16x32_bf16 v[224:227], a[180:183], a[56:59], v[224:227]\n\t" "ds_read_b64_tr_b16 a[202:203], %[vr3] offset:2048\n\t" "v_add_u32 %[vb1], 0x10000, %[vb1]\n\t"
    "v_mfma_f32_16x16x32_bf16 v[240:243], a[176:179], a[52:55], v[240:243]\n\t" "ds_read_b64_tr_b16 a[204:205], %[vr3] offset:8192\n\t"
    "v_mfma_f32_16x16x32_bf16 v[240:243], a[180:183], a[60:63], v[240:243]\n\t" "ds_read_b64_tr_b16 a[206:207], %[vr3] offset:10240\n\t" "global_load_dwordx4 a[32:35], %[vx], s[40:41] offset:0 sc1\n\t"
    "v_mfma_f32_16x16x32_bf16 v[228:231], a[184:187], a[48:51], v[228:231]\n\t" "ds_read_b64_tr_b16 a[176:177], %[vr12] offset:0\n\t"
    "v_mfma_f32_16x16x32_bf16 v[228:231], a[188:191], a[56:59], v[228:231]\n\t" "ds_read_b64_tr_b16 a[178:179], %[vr12] offset:2048\n\t"
    "v_mfma_f32_16x16x32_bf16 v[244:247], a[184:187], a[52:55], v[244:247]\n\t" "ds_read_b64_tr_b16 a[180:181], %[vr12] offset:8192\n\t"
    "v_mfma_f32_16x16x32_bf16 v[244:247], a[188:191], a[60:63], v[244:247]\n\t" "ds_read_b64_tr_b16 a[182:183], %[vr12] offset:10240\n\t" "s_waitcnt lgkmcnt(4)\n\t"
    "v_mfma_f32_16x16x32_bf16 v[232:235], a[192:195], a[48:51], v[232:235]\n\t" "ds_read_b64_tr_b16 a[184:185], %[vr13] offset:0\n\t" "global_load_dwordx4 a[36:39], %[vx], s[40:41] offset:1024 sc1\n\t"
    "v_mfma_f32_16x16x32_bf16 v[232:235], a[196:199], a[56:59], v[232:235]\n\t" "ds_read_b64_tr_b16 a[186:187], %[vr13] offset:2048\n\t"
    "v_mfma_f32_16x16x32_bf16 v[248:251], a[192:195], a[52:55], v[248:251]\n\t" "ds_read_b64_tr_b16 a[188:189], %[vr13] offset:8192\n\t"
    "v_mfma_f32_16x16x32_bf16 v[248:251], a[196:199], a[60:63], v[248:251]\n\t" "ds_read_b64_tr_b16 a[190:191], %[vr13] offset:10240\n\t"
    "global_load_dwordx4 a[40:43], %[vx], s[40:41] offset:2048 sc1\n\t" "v_mfma_f32_16x16x32_bf16 v[236:239], a[200:203], a[48:51], v[236:239]\n\t" "ds_read_b64_tr_b16 a[192:193], %[vr14] offset:0\n\t"
    "v_mfma_f32_16x16x32_bf16 v[236:239], a[204:207], a[56:59], v[236:239]\n\t" "ds_read_b64_tr_b16 a[194:195], %[vr14] offset:2048\n\t"
    "v_mfma_f32_16x16x32_bf16 v[252:255], a[200:203], a[52:55], v[252:255]\n\t" "ds_read_b64_tr_b16 a[196:197], %[vr14] offset:8192\n\t"
    "v_mfma_f32_16x16x32_bf16 v[252:255], a[204:207], a[60:63], v[252:255]\n\t" "ds_read_b64_tr_b16 a[198:199], %[vr14] offset:10240\n\t" "s_waitcnt lgkmcnt(4)\n\t"
    "v_mfma_f32_16x16x32_bf16 v[96:99], a[176:179], a[48:51], v[96:99]\n\t" "ds_read_b64_tr_b16 a[200:201], %[vr15] offset:0\n\t" "global_load_dwordx4 a[44:47], %[vx], s[40:41] offset:3072 sc1\n\t"
    "v_mfma_f32_16x16x32_bf16 v[96:99], a[180:183], a[56:59], v[96:99]\n\t" "ds_read_b64_tr_b16 a[202:203], %[vr15] offset:2048\n\t" "s_add_u32 m0, %[sl0], 131072\n\t"
    "v_mfma_f32_16x16x32_bf16 v[100:103], a[176:179], a[52:55], v[100:103]\n\t" "ds_read_b64_tr_b16 a[204:205], %[vr15] offset:8192\n\t"
    "v_mfma_f32_16x16x32_bf16 v[100:103], a[180:183], a[60:63], v[100:103]\n\t" "ds_read_b64_tr_b16 a[206:207], %[vr15] offset:10240\n\t" "global_load_lds_dwordx4 %[vb0], %[sb]\n\t"
    "v_mfma_f32_16x16x32_bf16 v[104:107], a[184:187], a[48:51], v[104:107]\n\t" "s_barrier\n\t" "ds_read_b64_tr_b16 a[176:177], %[vr0] offset:49152\n\t"
    "v_mfma_f32_16x16x32_bf16 v[104:107], a[188:191], a[56:59], v[104:107]\n\t" "ds_read_b64_tr_b16 a[178:179], %[vr0] offset:51200\n\t" "v_add_u32 %[vb0], 0x10000, %[vb0]\n\t"
    "v_mfma_f32_16x16x32_bf16 v[108:111], a[184:187], a[52:55], v[108:111]\n\t" "ds_read_b64_tr_b16 a[180:181], %[vr0] offset:57344\n\t" "s_add_u32 m0, %[sl1], 131072\n\t"
    "v_mfma_f32_16x16x32_bf16 v[108:111], a[188:191], a[60:63], v[108:111]\n\t" "ds_read_b64_tr_b16 a[182:183], %[vr0] offset:59392\n\t" "s_waitcnt lgkmcnt(4)\n\t"
    "v_mfma_f32_16x16x32_bf16 v[112:115], a[192:195], a[48:51], v[112:115]\n\t" "ds_read_b64_tr_b16 a[184:185], %[vr1] offset:49152\n\t" "global_load_lds_dwordx4 %[vb1], %[sb]\n\t"
    "v_mfma_f32_16x16x32_bf16 v[112:115], a[196:199], a[56:59], v[112:115]\n\t" "ds_read_b64_tr_b16 a[186:187], %[vr1] offset:51200\n\t" "v_add_u32 %[vb1], 0x10000, %[vb1]\n\t"
    "v_mfma_f32_16x16x32_bf16 v[116:119], a[192:195], a[52:55], v[116:119]\n\t" "ds_read_b64_tr_b16 a[188:189], %[vr1] offset:57344\n\t" "s_add_u32 m0, %[sl0], 16384\n\t"
    "v_mfma_f32_16x16x32_bf16 v[116:119], a[196:199], a[60:63], v[116:119]\n\t" "ds_read_b64_tr_b16 a[190:191], %[vr1] offset:59392\n\t" "global_load_lds_dwordx4 %[vb0], %[sb]\n\t"
    "v_mfma_f32_16x16x32_bf16 v[120:123], a[200:203], a[48:51], v[120:123]\n\t" "ds_read_b64_tr_b16 a[192:193], %[vr2] offset:49152\n\t" "v_add_u32 %[vb0], 0x10000, %[vb0]\n\t"
    "v_mfma_f32_16x16x32_bf16 v[120:123], a[204:207], a[56:59], v[120:123]\n\t" "ds_read_b64_tr_b16 a[194:195], %[vr2] offset:51200\n\t" "s_add_u32 m0, %[sl1], 16384\n\t"
    "v_mfma_f32_16x16x32_bf16 v[124:127], a[200:203], a[52:55], v[124:127]\n\t" "ds_read_b64_tr_b16 a[196:197], %[vr2] offset:57344\n\t"
    "v_mfma_f32_16x16x32_bf16 v[124:127], a[204:207], a[60:63], v[124:127]\n\t" "ds_read_b64_tr_b16 a[198:199], %[vr2] offset:59392\n\t" "s_waitcnt lgkmcnt(4) vmcnt(21)\n\t"
    "v_mfma_f32_16x16x32_bf16 v[224:227], a[176:179], a[64:67], v[224:227]\n\t" "ds_read_b64_tr_b16 a[200:201], %[vr3] offset:49152\n\t" "global_load_lds_dwordx4 %[vb1], %[sb]\n\t"
    "v_mfma_f32_16x16x32_bf16 v[224:227], a[180:183], a[72:75], v[224:227]\n\t" "ds_read_b64_tr_b16 a[202:203], %[vr3] offset:51200\n\t" "v_add_u32 %[vb1], 0x10000, %[vb1]\n\t"
    "v_mfma_f32_16x16x32_bf16 v[240:243], a[176:179], a[68:71], v[240:243]\n\t" "ds_read_b64_tr_b16 a[204:205], %[vr3] offset:57344\n\t" "s_add_u32 s40, s40, 8192\n\t" "s_addc_u32 s41, s41, 0\n\t"
    "v_mfma_f32_16x16x32_bf16 v[240:243], a[180:183], a[76:79], v[240:243]\n\t" "ds_read_b64_tr_b16 a[206:207], %[vr3] offset:59392\n\t"
    "global_load_dwordx4 a[48:51], %[vx], s[40:41] offset:-4096 sc1\n\t" "v_mfma_f32_16x16x32_bf16 v[228:231], a[184:187], a[64:67], v[228:231]\n\t"
    "ds_read_b64_tr_b16 a[176:177], %[vr12] offset:49152\n\t" "v_mfma_f32_16x16x32_bf16 v[228:231], a[188:191], a[72:75], v[228:231]\n\t" "ds_read_b64_tr_b16 a[178:179], %[vr12] offset:51200\n\t"
    "v_mfma_f32_16x16x32_bf16 v[244:247], a[184:187], a[68:71], v[244:247]\n\t" "ds_read_b64_tr_b16 a[180:181], %[vr12] offset:57344\n\t"
    "v_mfma_f32_16x16x32_bf16 v[244:247], a[188:191], a[76:79], v[244:247]\n\t" "ds_read_b64_tr_b16 a[182:183], %[vr12] offset:59392\n\t" "s_waitcnt lgkmcnt(4)\n\t"
    "v_mfma_f32_16x16x32_bf16 v[232:235], a[192:195], a[64:67], v[232:235]\n\t" "ds_read_b64_tr_b16 a[184:185], %[vr13] offset:49152\n\t"
    "global_load_dwordx4 a[52:55], %[vx], s[40:41] offset:-3072 sc1\n\t" "v_mfma_f32_16x16x32_bf16 v[232:235], a[196:199], a[72:75], v[232:235]\n\t"
    "ds_read_b64_tr_b16 a[186:187], %[vr13] offset:51200\n\t" "v_mfma_f32_16x16x32_bf16 v[248:251], a[192:195], a[68:71], v[248:251]\n\t" "ds_read_b64_tr_b16 a[188:189], %[vr13] offset:57344\n\t"
    "v_mfma_f32_16x16x32_bf16 v[248:251], a[196:199], a[76:79], v[248:251]\n\t" "ds_read_b64_tr_b16 a[190:191], %[vr13] offset:59392\n\t"
    "global_load_dwordx2 a[56:57], %[vo], s[40:41] offset:-2048 sc1\n\t" "v_mfma_f32_16x16x32_bf16 v[236:239], a[200:203], a[64:67], v[236:239]\n\t"
    "ds_read_b64_tr_b16 a[192:193], %[vr14] offset:49152\n\t" "v_mfma_f32_16x16x32_bf16 v[236:239], a[204:207], a[72:75], v[236:239]\n\t" "ds_read_b64_tr_b16 a[194:195], %[vr14] offset:51200\n\t"
    "global_load_dwordx2 a[58:59], %[vo], s[40:41] offset:-1024 sc1\n\t" "v_mfma_f32_16x16x32_bf16 v[252:255], a[200:203], a[68:71], v[252:255]\n\t"
    "ds_read_b64_tr_b16 a[196:197], %[vr14] offset:57344\n\t" "v_mfma_f32_16x16x32_bf16 v[252:255], a[204:207], a[76:79], v[252:255]\n\t" "ds_read_b64_tr_b16 a[198:199], %[vr14] offset:59392\n\t"
    "s_waitcnt lgkmcnt(4)\n\t" "v_mfma_f32_16x16x32_bf16 v[96:99], a[176:179], a[64:67], v[96:99]\n\t" "ds_read_b64_tr_b16 a[200:201], %[vr15] offset:49152\n\t"
    "global_load_dwordx2 a[60:61], %[vo], s[40:41] offset:-1536 sc1\n\t" "v_mfma_f32_16x16x32_bf16 v[96:99], a[180:183], a[72:75], v[96:99]\n\t" "ds_read_b64_tr_b16 a[202:203], %[vr15] offset:51200\n\t"
    "global_load_dwordx2 a[62:63], %[vo], s[40:41] offset:-512 sc1\n\t" "v_mfma_f32_16x16x32_bf16 v[100:103], a[176:179], a[68:71], v[100:103]\n\t"
    "ds_read_b64_tr_b16 a[204:205], %[vr15] offset:57344\n\t" "s_add_u32 m0, %[sl0], 24576\n\t" "v_mfma_f32_16x16x32_bf16 v[100:103], a[180:183], a[76:79], v[100:103]\n\t"
    "ds_read_b64_tr_b16 a[206:207], %[vr15] offset:59392\n\t" "global_load_lds_dwordx4 %[vb0], %[sb]\n\t" "v_mfma_f32_16x16x32_bf16 v[104:107], a[184:187], a[64:67], v[104:107]\n\t" "s_barrier\n\t"
    "ds_read_b64_tr_b16 a[176:177], %[vr4] offset:0\n\t" "v_mfma_f32_16x16x32_bf16 v[104:107], a[188:191], a[72:75], v[104:107]\n\t" "ds_read_b64_tr_b16 a[178:179], %[vr4] offset:2048\n\t"
    "v_add_u32 %[vb0], 0x10000, %[vb0]\n\t" "v_mfma_f32_16x16x32_bf16 v[108:111], a[184:187], a[68:71], v[108:111]\n\t" "ds_read_b64_tr_b16 a[180:181], %[vr4] offset:8192\n\t"
    "s_add_u32 m0, %[sl1], 24576\n\t" "v_mfma_f32_16x16x32_bf16 v[108:111], a[188:191], a[76:79], v[108:111]\n\t" "ds_read_b64_tr_b16 a[182:183], %[vr4] offset:10240\n\t" "s_waitcnt lgkmcnt(4)\n\t"
    "v_mfma_f32_16x16x32_bf16 v[112:115], a[192:195], a[64:67], v[112:115]\n\t" "ds_read_b64_tr_b16 a[184:185], %[vr5] offset:0\n\t" "global_load_lds_dwordx4 %[vb1], %[sb]\n\t"
    "v_mfma_f32_16x16x32_bf16 v[112:115], a[196:199], a[72:75], v[112:115]\n\t" "ds_read_b64_tr_b16 a[186:187], %[vr5] offset:2048\n\t" "v_add_u32 %[vb1], 0x10000, %[vb1]\n\t"
    "v_mfma_f32_16x16x32_bf16 v[116:119], a[192:195], a[68:71], v[116:119]\n\t" "ds_read_b64_tr_b16 a[188:189], %[vr5] offset:8192\n\t" "s_add_u32 m0, %[sl0], 32768\n\t"
    "v_mfma_f32_16x16x32_bf16 v[116:119], a[196:199], a[76:79], v[116:119]\n\t" "ds_read_b64_tr_b16 a[190:191], %[vr5] offset:10240\n\t" "global_load_lds_dwordx4 %[vb0], %[sb]\n\t"
    "v_mfma_f32_16x16x32_bf16 v[120:123], a[200:203], a[64:67], v[120:123]\n\t" "ds_read_b64_tr_b16 a[192:193], %[vr6] offset:0\n\t" "v_add_u32 %[vb0], 0x10000, %[vb0]\n\t"
    "v_mfma_f32_16x16x32_bf16 v[120:123], a[204:207], a[72:75], v[120:123]\n\t" "ds_read_b64_tr_b16 a[194:195], %[vr6] offset:2048\n\t" "s_add_u32 m0, %[sl1], 32768\n\t"
    "v_mfma_f32_16x16x32_bf16 v[124:127], a[200:203], a[68:71], v[124:127]\n\t" "ds_read_b64_tr_b16 a[196:197], %[vr6] offset:8192\n\t"
    "v_mfma_f32_16x16x32_bf16 v[124:127], a[204:207], a[76:79], v[124:127]\n\t" "ds_read_b64_tr_b16 a[198:199], %[vr6] offset:10240\n\t" "s_waitcnt lgkmcnt(4) vmcnt(25)\n\t"
    "v_mfma_f32_16x16x32_bf16 v[224:227], a[176:179], a[0:3], v[224:227]\n\t" "ds_read_b64_tr_b16 a[200:201], %[vr7] offset:0\n\t" "global_load_lds_dwordx4 %[vb1], %[sb]\n\t"
    "v_mfma_f32_16x16x32_bf16 v[224:227], a[180:183], a[8:11], v[224:227]\n\t" "ds_read_b64_tr_b16 a[202:203], %[vr7] offset:2048\n\t" "v_add_u32 %[vb1], 0x10000, %[vb1]\n\t"
    "v_mfma_f32_16x16x32_bf16 v[240:243], a[176:179], a[4:7], v[240:243]\n\t" "ds_read_b64_tr_b16 a[204:205], %[vr7] offset:8192\n\t"
    "v_mfma_f32_16x16x32_bf16 v[240:243], a[180:183], a[12:15], v[240:243]\n\t" "ds_read_b64_tr_b16 a[206:207], %[vr7] offset:10240\n\t" "global_load_dwordx4 a[64:67], %[vx], s[40:41] offset:0 sc1\n\t"
    "v_mfma_f32_16x16x32_bf16 v[228:231], a[184:187], a[0:3], v[228:231]\n\t" "ds_read_b64_tr_b16 a[176:177], %[vr16] offset:0\n\t"
    "v_mfma_f32_16x16x32_bf16 v[228:231], a[188:191], a[8:11], v[228:231]\n\t" "ds_read_b64_tr_b16 a[178:179], %[vr16] offset:2048\n\t"
    "v_mfma_f32_16x16x32_bf16 v[244:247], a[184:187], a[4:7], v[244:247]\n\t" "ds_read_b64_tr_b16 a[180:181], %[vr16] offset:8192\n\t"
    "v_mfma_f32_16x16x32_bf16 v[244:247], a[188:191], a[12:15], v[244:247]\n\t" "ds_read_b64_tr_b16 a[182:183], %[vr16] offset:10240\n\t" "s_waitcnt lgkmcnt(4)\n\t"
    "v_mfma_f32_16x16x32_bf16 v[232:235], a[192:195], a[0:3], v[232:235]\n\t" "ds_read_b64_tr_b16 a[184:185], %[vr17] offset:0\n\t" "global_load_dwordx4 a[68:71], %[vx], s[40:41] offset:1024 sc1\n\t"
    "v_mfma_f32_16x16x32_bf16 v[232:235], a[196:199], a[8:11], v[232:235]\n\t" "ds_read_b64_tr_b16 a[186:187], %[vr17] offset:2048\n\t"
    "v_mfma_f32_16x16x32_bf16 v[248:251], a[192:195], a[4:7], v[248:251]\n\t" "ds_read_b64_tr_b16 a[188:189], %[vr17] offset:8192\n\t"
    "v_mfma_f32_16x16x32_bf16 v[248:251], a[196:199], a[12:15], v[248:251]\n\t" "ds_read_b64_tr_b16 a[190:191], %[vr17] offset:10240\n\t"
    "global_load_dwordx4 a[72:75], %[vx], s[40:41] offset:2048 sc1\n\t" "v_mfma_f32_16x16x32_bf16 v[236:239], a[200:203], a[0:3], v[236:239]\n\t" "ds_read_b64_tr_b16 a[192:193], %[vr18] offset:0\n\t"
    "v_mfma_f32_16x16x32_bf16 v[236:239], a[204:207], a[8:11], v[236:239]\n\t" "ds_read_b64_tr_b16 a[194:195], %[vr18] offset:2048\n\t"
    "v_mfma_f32_16x16x32_bf16 v[252:255], a[200:203], a[4:7], v[252:255]\n\t" "ds_read_b64_tr_b16 a[196:197], %[vr18] offset:8192\n\t"
    "v_mfma_f32_16x16x32_bf16 v[252:255], a[204:207], a[12:15], v[252:255]\n\t" "ds_read_b64_tr_b16 a[198:199], %[vr18] offset:10240\n\t" "s_waitcnt lgkmcnt(4)\n\t"
    "v_mfma_f32_16x16x32_bf16 v[96:99], a[176:179], a[0:3], v[96:99]\n\t" "ds_read_b64_tr_b16 a[200:201], %[vr19] offset:0\n\t" "global_load_dwordx4 a[76:79], %[vx], s[40:41] offset:3072 sc1\n\t"
    "v_mfma_f32_16x16x32_bf16 v[96:99], a[180:183], a[8:11], v[96:99]\n\t" "ds_read_b64_tr_b16 a[202:203], %[vr19] offset:2048\n\t" "s_add_u32 m0, %[sl0], 40960\n\t"
    "v_mfma_f32_16x16x32_bf16 v[100:103], a[176:179], a[4:7], v[100:103]\n\t" "ds_read_b64_tr_b16 a[204:205], %[vr19] offset:8192\n\t"
    "v_mfma_f32_16x16x32_bf16 v[100:103], a[180:183], a[12:15], v[100:103]\n\t" "ds_read_b64_tr_b16 a[206:207], %[vr19] offset:10240\n\t" "global_load_lds_dwordx4 %[vb0], %[sb]\n\t"
    "v_mfma_f32_16x16x32_bf16 v[104:107], a[184:187], a[0:3], v[104:107]\n\t" "s_barrier\n\t" "ds_read_b64_tr_b16 a[176:177], %[vr4] offset:16384\n\t"
    "v_mfma_f32_16x16x32_bf16 v[104:107], a[188:191], a[8:11], v[104:107]\n\t" "ds_read_b64_tr_b16 a[178:179], %[vr4] offset:18432\n\t" "v_add_u32 %[vb0], 0x10000, %[vb0]\n\t"
    "v_mfma_f32_16x16x32_bf16 v[108:111], a[184:187], a[4:7], v[108:111]\n\t" "ds_read_b64_tr_b16 a[180:181], %[vr4] offset:24576\n\t" "s_add_u32 m0, %[sl1], 40960\n\t"
    "v_mfma_f32_16x16x32_bf16 v[108:111], a[188:191], a[12:15], v[108:111]\n\t" "ds_read_b64_tr_b16 a[182:183], %[vr4] offset:26624\n\t" "s_waitcnt lgkmcnt(4)\n\t"
    "v_mfma_f32_16x16x32_bf16 v[112:115], a[192:195], a[0:3], v[112:115]\n\t" "ds_read_b64_tr_b16 a[184:185], %[vr5] offset:16384\n\t" "global_load_lds_dwordx4 %[vb1], %[sb]\n\t"
    "v_mfma_f32_16x16x32_bf16 v[112:115], a[196:199], a[8:11], v[112:115]\n\t" "ds_read_b64_tr_b16 a[186:187], %[vr5] offset:18432\n\t" "v_add_u32 %[vb1], 0x10000, %[vb1]\n\t"
    "v_mfma_f32_16x16x32_bf16 v[116:119], a[192:195], a[4:7], v[116:119]\n\t" "ds_read_b64_tr_b16 a[188:189], %[vr5] offset:24576\n\t" "s_add_u32 m0, %[sl0], 139264\n\t"
    "v_mfma_f32_16x16x32_bf16 v[116:119], a[196:199], a[12:15], v[116:119]\n\t" "ds_read_b64_tr_b16 a[190:191], %[vr5] offset:26624\n\t" "global_load_lds_dwordx4 %[vb0], %[sb]\n\t"
    "v_mfma_f32_16x16x32_bf16 v[120:123], a[200:203], a[0:3], v[120:123]\n\t" "ds_read_b64_tr_b16 a[192:193], %[vr6] offset:16384\n\t" "v_add_u32 %[vb0], 0x10000, %[vb0]\n\t"
    "v_mfma_f32_16x16x32_bf16 v[120:123], a[204:207], a[8:11], v[120:123]\n\t" "ds_read_b64_tr_b16 a[194:195], %[vr6] offset:18432\n\t" "s_add_u32 m0, %[sl1], 139264\n\t"
    "v_mfma_f32_16x16x32_bf16 v[124:127], a[200:203], a[4:7], v[124:127]\n\t" "ds_read_b64_tr_b16 a[196:197], %[vr6] offset:24576\n\t"
    "v_mfma_f32_16x16x32_bf16 v[124:127], a[204:207], a[12:15], v[124:127]\n\t" "ds_read_b64_tr_b16 a[198:199], %[vr6] offset:26624\n\t" "s_waitcnt lgkmcnt(4) vmcnt(29)\n\t"
    "v_mfma_f32_16x16x32_bf16 v[224:227], a[176:179], a[16:19], v[224:227]\n\t" "ds_read_b64_tr_b16 a[200:201], %[vr7] offset:16384\n\t" "global_load_lds_dwordx4 %[vb1], %[sb]\n\t"
    "v_mfma_f32_16x16x32_bf16 v[224:227], a[180:183], a[24:27], v[224:227]\n\t" "ds_read_b64_tr_b16 a[202:203], %[vr7] offset:18432\n\t" "v_add_u32 %[vb1], 0x10000, %[vb1]\n\t"
    "v_mfma_f32_16x16x32_bf16 v[240:243], a[176:179], a[20:23], v[240:243]\n\t" "ds_read_b64_tr_b16 a[204:205], %[vr7] offset:24576\n\t" "s_add_u32 s40, s40, 8192\n\t" "s_addc_u32 s41, s41, 0\n\t"
    "v_mfma_f32_16x16x32_bf16 v[240:243], a[180:183], a[28:31], v[240:243]\n\t" "ds_read_b64_tr_b16 a[206:207], %[vr7] offset:26624\n\t" "global_load_dwordx4 a[0:3], %[vx], s[40:41] offset:-4096 sc1\n\t"
    "v_mfma_f32_16x16x32_bf16 v[228:231], a[184:187], a[16:19], v[228:231]\n\t" "ds_read_b64_tr_b16 a[176:177], %[vr16] offset:16384\n\t"
    "v_mfma_f32_16x16x32_bf16 v[228:231], a[188:191], a[24:27], v[228:231]\n\t" "ds_read_b64_tr_b16 a[178:179], %[vr16] offset:18432\n\t"
    "v_mfma_f32_16x16x32_bf16 v[244:247], a[184:187], a[20:23], v[244:247]\n\t" "ds_read_b64_tr_b16 a[180:181], %[vr16] offset:24576\n\t"
    "v_mfma_f32_16x16x32_bf16 v[244:247], a[188:191], a[28:31], v[244:247]\n\t" "ds_read_b64_tr_b16 a[182:183], %[vr16] offset:26624\n\t" "s_waitcnt lgkmcnt(4)\n\t"
    "v_mfma_f32_16x16x32_bf16 v[232:235], a[192:195], a[16:19], v[232:235]\n\t" "ds_read_b64_tr_b16 a[184:185], %[vr17] offset:16384\n\t" "global_load_dwordx4 a[4:7], %[vx], s[40:41] offset:-3072 sc1\n\t"
    "v_mfma_f32_16x16x32_bf16 v[232:235], a[196:199], a[24:27], v[232:235]\n\t" "ds_read_b64_tr_b16 a[186:187], %[vr17] offset:18432\n\t"
    "v_mfma_f32_16x16x32_bf16 v[248:251], a[192:195], a[20:23], v[248:251]\n\t" "ds_read_b64_tr_b16 a[188:189], %[vr17] offset:24576\n\t"
    "v_mfma_f32_16x16x32_bf16 v[248:251], a[196:199], a[28:31], v[248:251]\n\t" "ds_read_b64_tr_b16 a[190:191], %[vr17] offset:26624\n\t"
    "global_load_dwordx4 a[8:11], %[vx], s[40:41] offset:-2048 sc1\n\t" "v_mfma_f32_16x16x32_bf16 v[236:239], a[200:203], a[16:19], v[236:239]\n\t"
    "ds_read_b64_tr_b16 a[192:193], %[vr18] offset:16384\n\t" "v_mfma_f32_16x16x32_bf16 v[236:239], a[204:207], a[24:27], v[236:239]\n\t" "ds_read_b64_tr_b16 a[194:195], %[vr18] offset:18432\n\t"
    "v_mfma_f32_16x16x32_bf16 v[252:255], a[200:203], a[20:23], v[252:255]\n\t" "ds_read_b64_tr_b16 a[196:197], %[vr18] offset:24576\n\t"
    "v_mfma_f32_16x16x32_bf16 v[252:255], a[204:207], a[28:31], v[252:255]\n\t" "ds_read_b64_tr_b16 a[198:199], %[vr18] offset:26624\n\t" "s_waitcnt lgkmcnt(4)\n\t"
    "v_mfma_f32_16x16x32_bf16 v[96:99], a[176:179], a[16:19], v[96:99]\n\t" "ds_read_b64_tr_b16 a[200:201], %[vr19] offset:16384\n\t" "global_load_dwordx4 a[12:15], %[vx], s[40:41] offset:-1024 sc1\n\t"
    "v_mfma_f32_16x16x32_bf16 v[96:99], a[180:183], a[24:27], v[96:99]\n\t" "ds_read_b64_tr_b16 a[202:203], %[vr19] offset:18432\n\t" "s_add_u32 m0, %[sl0], 147456\n\t"
    "v_mfma_f32_16x16x32_bf16 v[100:103], a[176:179], a[20:23], v[100:103]\n\t" "ds_read_b64_tr_b16 a[204:205], %[vr19] offset:24576\n\t"
    "v_mfma_f32_16x16x32_bf16 v[100:103], a[180:183], a[28:31], v[100:103]\n\t" "ds_read_b64_tr_b16 a[206:207], %[vr19] offset:26624\n\t" "global_load_lds_dwordx4 %[vb0], %[sb]\n\t"
    "v_mfma_f32_16x16x32_bf16 v[104:107], a[184:187], a[16:19], v[104:107]\n\t" "s_barrier\n\t" "ds_read_b64_tr_b16 a[176:177], %[vr4] offset:32768\n\t"
    "v_mfma_f32_16x16x32_bf16 v[104:107], a[188:191], a[24:27], v[104:107]\n\t" "ds_read_b64_tr_b16 a[178:179], %[vr4] offset:34816\n\t" "v_add_u32 %[vb0], 0x10000, %[vb0]\n\t"
    "v_mfma_f32_16x16x32_bf16 v[108:111], a[184:187], a[20:23], v[108:111]\n\t" "ds_read_b64_tr_b16 a[180:181], %[vr4] offset:40960\n\t" "s_add_u32 m0, %[sl1], 147456\n\t"
    "v_mfma_f32_16x16x32_bf16 v[108:111], a[188:191], a[28:31], v[108:111]\n\t" "ds_read_b64_tr_b16 a[182:183], %[vr4] offset:43008\n\t" "s_waitcnt lgkmcnt(4)\n\t"
    "v_mfma_f32_16x16x32_bf16 v[112:115], a[192:195], a[16:19], v[112:115]\n\t" "ds_read_b64_tr_b16 a[184:185], %[vr5] offset:32768\n\t" "global_load_lds_dwordx4 %[vb1], %[sb]\n\t"
    "v_mfma_f32_16x16x32_bf16 v[112:115], a[196:199], a[24:27], v[112:115]\n\t" "ds_read_b64_tr_b16 a[186:187], %[vr5] offset:34816\n\t" "v_add_u32 %[vb1], 0x10000, %[vb1]\n\t"
    "v_mfma_f32_16x16x32_bf16 v[116:119], a[192:195], a[20:23], v[116:119]\n\t" "ds_read_b64_tr_b16 a[188:189], %[vr5] offset:40960\n\t" "s_add_u32 m0, %[sl0], 155648\n\t"
    "v_mfma_f32_16x16x32_bf16 v[116:119], a[196:199], a[28:31], v[116:119]\n\t" "ds_read_b64_tr_b16 a[190:191], %[vr5] offset:43008\n\t" "global_load_lds_dwordx4 %[vb0], %[sb]\n\t"
    "v_mfma_f32_16x16x32_bf16 v[120:123], a[200:203], a[16:19], v[120:123]\n\t" "ds_read_b64_tr_b16 a[192:193], %[vr6] offset:32768\n\t" "v_add_u32 %[vb0], 0x10000, %[vb0]\n\t"
    "v_mfma_f32_16x16x32_bf16 v[120:123], a[204:207], a[24:27], v[120:123]\n\t" "ds_read_b64_tr_b16 a[194:195], %[vr6] offset:34816\n\t" "s_add_u32 m0, %[sl1], 155648\n\t"
    "v_mfma_f32_16x16x32_bf16 v[124:127], a[200:203], a[20:23], v[124:127]\n\t" "ds_read_b64_tr_b16 a[196:197], %[vr6] offset:40960\n\t"
    "v_mfma_f32_16x16x32_bf16 v[124:127], a[204:207], a[28:31], v[124:127]\n\t" "ds_read_b64_tr_b16 a[198:199], %[vr6] offset:43008\n\t" "s_waitcnt lgkmcnt(4) vmcnt(29)\n\t"
    "v_mfma_f32_16x16x32_bf16 v[224:227], a[176:179], a[32:35], v[224:227]\n\t" "ds_read_b64_tr_b16 a[200:201], %[vr7] offset:32768\n\t" "global_load_lds_dwordx4 %[vb1], %[sb]\n\t"
    "v_mfma_f32_16x16x32_bf16 v[224:227], a[180:183], a[40:43], v[224:227]\n\t" "ds_read_b64_tr_b16 a[202:203], %[vr7] offset:34816\n\t" "v_add_u32 %[vb1], 0x10000, %[vb1]\n\t"
    "v_mfma_f32_16x16x32_bf16 v[240:243], a[176:179], a[36:39], v[240:243]\n\t" "ds_read_b64_tr_b16 a[204:205], %[vr7] offset:40960\n\t"
    "v_mfma_f32_16x16x32_bf16 v[240:243], a[180:183], a[44:47], v[240:243]\n\t" "ds_read_b64_tr_b16 a[206:207], %[vr7] offset:43008\n\t" "global_load_dword a255, %[vx], %[cd]\n\t"
    "v_mfma_f32_16x16x32_bf16 v[228:231], a[184:187], a[32:35], v[228:231]\n\t" "ds_read_b64_tr_b16 a[176:177], %[vr16] offset:32768\n\t"
    "v_mfma_f32_16x16x32_bf16 v[228:231], a[188:191], a[40:43], v[228:231]\n\t" "ds_read_b64_tr_b16 a[178:179], %[vr16] offset:34816\n\t" "global_load_dwordx4 a[16:19], %[vx], s[40:41] offset:0 sc1\n\t"
    "v_mfma_f32_16x16x32_bf16 v[244:247], a[184:187], a[36:39], v[244:247]\n\t" "ds_read_b64_tr_b16 a[180:181], %[vr16] offset:40960\n\t"
    "v_mfma_f32_16x16x32_bf16 v[244:247], a[188:191], a[44:47], v[244:247]\n\t" "ds_read_b64_tr_b16 a[182:183], %[vr16] offset:43008\n\t" "s_waitcnt lgkmcnt(4)\n\t"
    "v_mfma_f32_16x16x32_bf16 v[232:235], a[192:195], a[32:35], v[232:235]\n\t" "ds_read_b64_tr_b16 a[184:185], %[vr17] offset:32768\n\t"
    "v_mfma_f32_16x16x32_bf16 v[232:235], a[196:199], a[40:43], v[232:235]\n\t" "ds_read_b64_tr_b16 a[186:187], %[vr17] offset:34816\n\t"
    "global_load_dwordx4 a[20:23], %[vx], s[40:41] offset:1024 sc1\n\t" "v_mfma_f32_16x16x32_bf16 v[248:251], a[192:195], a[36:39], v[248:251]\n\t"
    "ds_read_b64_tr_b16 a[188:189], %[vr17] offset:40960\n\t" "v_mfma_f32_16x16x32_bf16 v[248:251], a[196:199], a[44:47], v[248:251]\n\t" "ds_read_b64_tr_b16 a[190:191], %[vr17] offset:43008\n\t"
    "v_mfma_f32_16x16x32_bf16 v[236:239], a[200:203], a[32:35], v[236:239]\n\t" "ds_read_b64_tr_b16 a[192:193], %[vr18] offset:32768\n\t"
    "v_mfma_f32_16x16x32_bf16 v[236:239], a[204:207], a[40:43], v[236:239]\n\t" "ds_read_b64_tr_b16 a[194:195], %[vr18] offset:34816\n\t"
    "global_load_dwordx4 a[24:27], %[vx], s[40:41] offset:2048 sc1\n\t" "v_mfma_f32_16x16x32_bf16 v[252:255], a[200:203], a[36:39], v[252:255]\n\t"
    "ds_read_b64_tr_b16 a[196:197], %[vr18] offset:40960\n\t" "v_mfma_f32_16x16x32_bf16 v[252:255], a[204:207], a[44:47], v[252:255]\n\t" "ds_read_b64_tr_b16 a[198:199], %[vr18] offset:43008\n\t"
    "s_waitcnt lgkmcnt(4)\n\t" "v_mfma_f32_16x16x32_bf16 v[96:99], a[176:179], a[32:35], v[96:99]\n\t" "ds_read_b64_tr_b16 a[200:201], %[vr19] offset:32768\n\t"
    "v_mfma_f32_16x16x32_bf16 v[96:99], a[180:183], a[40:43], v[96:99]\n\t" "ds_read_b64_tr_b16 a[202:203], %[vr19] offset:34816\n\t" "global_load_dwordx4 a[28:31], %[vx], s[40:41] offset:3072 sc1\n\t"
    "v_mfma_f32_16x16x32_bf16 v[100:103], a[176:179], a[36:39], v[100:103]\n\t" "ds_read_b64_tr_b16 a[204:205], %[vr19] offset:40960\n\t"
    "v_mfma_f32_16x16x32_bf16 v[100:103], a[180:183], a[44:47], v[100:103]\n\t" "ds_read_b64_tr_b16 a[206:207], %[vr19] offset:43008\n\t" "s_add_u32 m0, %[sl0], 0\n\t"
    "v_mfma_f32_16x16x32_bf16 v[104:107], a[184:187], a[32:35], v[104:107]\n\t" "s_barrier\n\t" "ds_read_b64_tr_b16 a[176:177], %[vr4] offset:49152\n\t"
    "v_mfma_f32_16x16x32_bf16 v[104:107], a[188:191], a[40:43], v[104:107]\n\t" "ds_read_b64_tr_b16 a[178:179], %[vr4] offset:51200\n\t" "global_load_lds_dwordx4 %[vb0], %[sb]\n\t"
    "v_mfma_f32_16x16x32_bf16 v[108:111], a[184:187], a[36:39], v[108:111]\n\t" "ds_read_b64_tr_b16 a[180:181], %[vr4] offset:57344\n\t" "v_add_u32 %[vb0], 0x10000, %[vb0]\n\t"
    "v_mfma_f32_16x16x32_bf16 v[108:111], a[188:191], a[44:47], v[108:111]\n\t" "ds_read_b64_tr_b16 a[182:183], %[vr4] offset:59392\n\t" "s_waitcnt lgkmcnt(4)\n\t"
    "v_mfma_f32_16x16x32_bf16 v[112:115], a[192:195], a[32:35], v[112:115]\n\t" "ds_read_b64_tr_b16 a[184:185], %[vr5] offset:49152\n\t" "s_add_u32 m0, %[sl1], 0\n\t"
    "v_mfma_f32_16x16x32_bf16 v[112:115], a[196:199], a[40:43], v[112:115]\n\t" "ds_read_b64_tr_b16 a[186:187], %[vr5] offset:51200\n\t" "global_load_lds_dwordx4 %[vb1], %[sb]\n\t"
    "v_mfma_f32_16x16x32_bf16 v[116:119], a[192:195], a[36:39], v[116:119]\n\t" "ds_read_b64_tr_b16 a[188:189], %[vr5] offset:57344\n\t" "v_add_u32 %[vb1], 0x10000, %[vb1]\n\t"
    "v_mfma_f32_16x16x32_bf16 v[116:119], a[196:199], a[44:47], v[116:119]\n\t" "ds_read_b64_tr_b16 a[190:191], %[vr5] offset:59392\n\t" "s_add_u32 m0, %[sl0], 8192\n\t"
    "v_mfma_f32_16x16x32_bf16 v[120:123], a[200:203], a[32:35], v[120:123]\n\t" "ds_read_b64_tr_b16 a[192:193], %[vr6] offset:49152\n\t"
    "v_mfma_f32_16x16x32_bf16 v[120:123], a[204:207], a[40:43], v[120:123]\n\t" "ds_read_b64_tr_b16 a[194:195], %[vr6] offset:51200\n\t" "global_load_lds_dwordx4 %[vb0], %[sb]\n\t"
    "v_mfma_f32_16x16x32_bf16 v[124:127], a[200:203], a[36:39], v[124:127]\n\t" "ds_read_b64_tr_b16 a[196:197], %[vr6] offset:57344\n\t" "v_add_u32 %[vb0], 0x10000, %[vb0]\n\t"
    "v_mfma_f32_16x16x32_bf16 v[124:127], a[204:207], a[44:47], v[124:127]\n\t" "ds_read_b64_tr_b16 a[198:199], %[vr6] offset:59392\n\t" "s_waitcnt lgkmcnt(4) vmcnt(28)\n\t"
    "v_mfma_f32_16x16x32_bf16 v[224:227], a[176:179], a[48:51], v[224:227]\n\t" "ds_read_b64_tr_b16 a[200:201], %[vr7] offset:49152\n\t" "s_add_u32 m0, %[sl1], 8192\n\t"
    "v_mfma_f32_16x16x32_bf16 v[224:227], a[180:183], a[56:59], v[224:227]\n\t" "ds_read_b64_tr_b16 a[202:203], %[vr7] offset:51200\n\t" "global_load_lds_dwordx4 %[vb1], %[sb]\n\t"
    "v_mfma_f32_16x16x32_bf16 v[240:243], a[176:179], a[52:55], v[240:243]\n\t" "ds_read_b64_tr_b16 a[204:205], %[vr7] offset:57344\n\t" "v_add_u32 %[vb1], 0x10000, %[vb1]\n\t"
    "v_mfma_f32_16x16x32_bf16 v[240:243], a[180:183], a[60:63], v[240:243]\n\t" "ds_read_b64_tr_b16 a[206:207], %[vr7] offset:59392\n\t" "s_add_u32 s40, s40, 8192\n\t" "s_addc_u32 s41, s41, 0\n\t"
    "v_mfma_f32_16x16x32_bf16 v[228:231], a[184:187], a[48:51], v[228:231]\n\t" "ds_read_b64_tr_b16 a[176:177], %[vr16] offset:49152\n\t"
    "v_mfma_f32_16x16x32_bf16 v[228:231], a[188:191], a[56:59], v[228:231]\n\t" "ds_read_b64_tr_b16 a[178:179], %[vr16] offset:51200\n\t"
    "global_load_dwordx2 a[32:33], %[vo], s[40:41] offset:-4096 sc1\n\t" "v_mfma_f32_16x16x32_bf16 v[244:247], a[184:187], a[52:55], v[244:247]\n\t"
    "ds_read_b64_tr_b16 a[180:181], %[vr16] offset:57344\n\t" "v_mfma_f32_16x16x32_bf16 v[244:247], a[188:191], a[60:63], v[244:247]\n\t" "ds_read_b64_tr_b16 a[182:183], %[vr16] offset:59392\n\t"
    "s_waitcnt lgkmcnt(4)\n\t" "v_mfma_f32_16x16x32_bf16 v[232:235], a[192:195], a[48:51], v[232:235]\n\t" "ds_read_b64_tr_b16 a[184:185], %[vr17] offset:49152\n\t"
    "global_load_dwordx2 a[34:35], %[vo], s[40:41] offset:-3072 sc1\n\t" "v_mfma_f32_16x16x32_bf16 v[232:235], a[196:199], a[56:59], v[232:235]\n\t"
    "ds_read_b64_tr_b16 a[186:187], %[vr17] offset:51200\n\t" "global_load_dwordx2 a[36:37], %[vo], s[40:41] offset:-3584 sc1\n\t"
    "v_mfma_f32_16x16x32_bf16 v[248:251], a[192:195], a[52:55], v[248:251]\n\t" "ds_read_b64_tr_b16 a[188:189], %[vr17] offset:57344\n\t"
    "v_mfma_f32_16x16x32_bf16 v[248:251], a[196:199], a[60:63], v[248:251]\n\t" "ds_read_b64_tr_b16 a[190:191], %[vr17] offset:59392\n\t"
    "global_load_dwordx2 a[38:39], %[vo], s[40:41] offset:-2560 sc1\n\t" "v_mfma_f32_16x16x32_bf16 v[236:239], a[200:203], a[48:51], v[236:239]\n\t"
    "ds_read_b64_tr_b16 a[192:193], %[vr18] offset:49152\n\t" "v_mfma_f32_16x16x32_bf16 v[236:239], a[204:207], a[56:59], v[236:239]\n\t" "ds_read_b64_tr_b16 a[194:195], %[vr18] offset:51200\n\t"
    "global_load_dwordx4 a[40:43], %[vx], s[40:41] offset:-2048 sc1\n\t" "v_mfma_f32_16x16x32_bf16 v[252:255], a[200:203], a[52:55], v[252:255]\n\t"
    "ds_read_b64_tr_b16 a[196:197], %[vr18] offset:57344\n\t" "v_mfma_f32_16x16x32_bf16 v[252:255], a[204:207], a[60:63], v[252:255]\n\t" "ds_read_b64_tr_b16 a[198:199], %[vr18] offset:59392\n\t"
    "s_waitcnt lgkmcnt(4)\n\t" "v_mfma_f32_16x16x32_bf16 v[96:99], a[176:179], a[48:51], v[96:99]\n\t" "ds_read_b64_tr_b16 a[200:201], %[vr19] offset:49152\n\t"
    "v_mfma_f32_16x16x32_bf16 v[96:99], a[180:183], a[56:59], v[96:99]\n\t" "ds_read_b64_tr_b16 a[202:203], %[vr19] offset:51200\n\t" "global_load_dwordx4 a[44:47], %[vx], s[40:41] offset:-1024 sc1\n\t"
    "v_mfma_f32_16x16x32_bf16 v[100:103], a[176:179], a[52:55], v[100:103]\n\t" "ds_read_b64_tr_b16 a[204:205], %[vr19] offset:57344\n\t"
    "v_mfma_f32_16x16x32_bf16 v[100:103], a[180:183], a[60:63], v[100:103]\n\t" "ds_read_b64_tr_b16 a[206:207], %[vr19] offset:59392\n\t" "s_add_u32 m0, %[sl0], 49152\n\t"
    "v_mfma_f32_16x16x32_bf16 v[104:107], a[184:187], a[48:51], v[104:107]\n\t" "s_barrier\n\t" "ds_read_b64_tr_b16 a[176:177], %[vr8] offset:0\n\t"
    "v_mfma_f32_16x16x32_bf16 v[104:107], a[188:191], a[56:59], v[104:107]\n\t" "ds_read_b64_tr_b16 a[178:179], %[vr8] offset:2048\n\t" "global_load_lds_dwordx4 %[vb0], %[sb]\n\t"
    "v_mfma_f32_16x16x32_bf16 v[108:111], a[184:187], a[52:55], v[108:111]\n\t" "ds_read_b64_tr_b16 a[180:181], %[vr0] offset:16384\n\t" "v_add_u32 %[vb0], 0x10000, %[vb0]\n\t"
    "v_mfma_f32_16x16x32_bf16 v[108:111], a[188:191], a[60:63], v[108:111]\n\t" "ds_read_b64_tr_b16 a[182:183], %[vr0] offset:18432\n\t" "s_waitcnt lgkmcnt(4)\n\t"
    "v_mfma_f32_16x16x32_bf16 v[112:115], a[192:195], a[48:51], v[112:115]\n\t" "ds_read_b64_tr_b16 a[184:185], %[vr9] offset:0\n\t" "s_add_u32 m0, %[sl1], 49152\n\t"
    "v_mfma_f32_16x16x32_bf16 v[112:115], a[196:199], a[56:59], v[112:115]\n\t" "ds_read_b64_tr_b16 a[186:187], %[vr9] offset:2048\n\t" "global_load_lds_dwordx4 %[vb1], %[sb]\n\t"
    "v_mfma_f32_16x16x32_bf16 v[116:119], a[192:195], a[52:55], v[116:119]\n\t" "ds_read_b64_tr_b16 a[188:189], %[vr1] offset:16384\n\t" "v_add_u32 %[vb1], 0x10000, %[vb1]\n\t"
    "v_mfma_f32_16x16x32_bf16 v[116:119], a[196:199], a[60:63], v[116:119]\n\t" "ds_read_b64_tr_b16 a[190:191], %[vr1] offset:18432\n\t" "s_add_u32 m0, %[sl0], 57344\n\t"
    "v_mfma_f32_16x16x32_bf16 v[120:123], a[200:203], a[48:51], v[120:123]\n\t" "ds_read_b64_tr_b16 a[192:193], %[vr10] offset:0\n\t"
    "v_mfma_f32_16x16x32_bf16 v[120:123], a[204:207], a[56:59], v[120:123]\n\t" "ds_read_b64_tr_b16 a[194:195], %[vr10] offset:2048\n\t" "global_load_lds_dwordx4 %[vb0], %[sb]\n\t"
    "v_mfma_f32_16x16x32_bf16 v[124:127], a[200:203], a[52:55], v[124:127]\n\t" "ds_read_b64_tr_b16 a[196:197], %[vr2] offset:16384\n\t" "v_add_u32 %[vb0], 0x10000, %[vb0]\n\t"
    "v_mfma_f32_16x16x32_bf16 v[124:127], a[204:207], a[60:63], v[124:127]\n\t" "ds_read_b64_tr_b16 a[198:199], %[vr2] offset:18432\n\t" "s_waitcnt lgkmcnt(4) vmcnt(30)\n\t"
    "v_mfma_f32_16x16x32_bf16 v[224:227], a[176:179], a[64:67], v[224:227]\n\t" "ds_read_b64_tr_b16 a[200:201], %[vr11] offset:0\n\t" "s_add_u32 m0, %[sl1], 57344\n\t"
    "v_mfma_f32_16x16x32_bf16 v[224:227], a[180:183], a[72:75], v[224:227]\n\t" "ds_read_b64_tr_b16 a[202:203], %[vr11] offset:2048\n\t" "global_load_lds_dwordx4 %[vb1], %[sb]\n\t"
    "v_mfma_f32_16x16x32_bf16 v[240:243], a[176:179], a[68:71], v[240:243]\n\t" "ds_read_b64_tr_b16 a[204:205], %[vr3] offset:16384\n\t" "v_add_u32 %[vb1], 0x10000, %[vb1]\n\t"
    "v_mfma_f32_16x16x32_bf16 v[240:243], a[180:183], a[76:79], v[240:243]\n\t" "ds_read_b64_tr_b16 a[206:207], %[vr3] offset:18432\n\t"
    "v_mfma_f32_16x16x32_bf16 v[228:231], a[184:187], a[64:67], v[228:231]\n\t" "ds_read_b64_tr_b16 a[176:177], %[vr20] offset:0\n\t"
    "v_mfma_f32_16x16x32_bf16 v[228:231], a[188:191], a[72:75], v[228:231]\n\t" "ds_read_b64_tr_b16 a[178:179], %[vr20] offset:2048\n\t" "global_load_dwordx4 a[48:51], %[vx], s[40:41] offset:0 sc1\n\t"
    "v_mfma_f32_16x16x32_bf16 v[244:247], a[184:187], a[68:71], v[244:247]\n\t" "ds_read_b64_tr_b16 a[180:181], %[vr12] offset:16384\n\t"
    "v_mfma_f32_16x16x32_bf16 v[244:247], a[188:191], a[76:79], v[244:247]\n\t" "ds_read_b64_tr_b16 a[182:183], %[vr12] offset:18432\n\t" "s_waitcnt lgkmcnt(4)\n\t"
    "v_mfma_f32_16x16x32_bf16 v[232:235], a[192:195], a[64:67], v[232:235]\n\t" "ds_read_b64_tr_b16 a[184:185], %[vr21] offset:0\n\t"
    "v_mfma_f32_16x16x32_bf16 v[232:235], a[196:199], a[72:75], v[232:235]\n\t" "ds_read_b64_tr_b16 a[186:187], %[vr21] offset:2048\n\t" "global_load_dwordx4 a[52:55], %[vx], s[40:41] offset:1024 sc1\n\t"
    "v_mfma_f32_16x16x32_bf16 v[248:251], a[192:195], a[68:71], v[248:251]\n\t" "ds_read_b64_tr_b16 a[188:189], %[vr13] offset:16384\n\t"
    "v_mfma_f32_16x16x32_bf16 v[248:251], a[196:199], a[76:79], v[248:251]\n\t" "ds_read_b64_tr_b16 a[190:191], %[vr13] offset:18432\n\t"
    "v_mfma_f32_16x16x32_bf16 v[236:239], a[200:203], a[64:67], v[236:239]\n\t" "ds_read_b64_tr_b16 a[192:193], %[vr22] offset:0\n\t"
    "v_mfma_f32_16x16x32_bf16 v[236:239], a[204:207], a[72:75], v[236:239]\n\t" "ds_read_b64_tr_b16 a[194:195], %[vr22] offset:2048\n\t" "global_load_dwordx4 a[56:59], %[vx], s[40:41] offset:2048 sc1\n\t"
    "v_mfma_f32_16x16x32_bf16 v[252:255], a[200:203], a[68:71], v[252:255]\n\t" "ds_read_b64_tr_b16 a[196:197], %[vr14] offset:16384\n\t"
    "v_mfma_f32_16x16x32_bf16 v[252:255], a[204:207], a[76:79], v[252:255]\n\t" "ds_read_b64_tr_b16 a[198:199], %[vr14] offset:18432\n\t" "s_waitcnt lgkmcnt(4)\n\t"
    "v_mfma_f32_16x16x32_bf16 v[96:99], a[176:179], a[64:67], v[96:99]\n\t" "ds_read_b64_tr_b16 a[200:201], %[vr23] offset:0\n\t" "v_mfma_f32_16x16x32_bf16 v[96:99], a[180:183], a[72:75], v[96:99]\n\t"
    "ds_read_b64_tr_b16 a[202:203], %[vr23] offset:2048\n\t" "global_load_dwordx4 a[60:63], %[vx], s[40:41] offset:3072 sc1\n\t" "v_mfma_f32_16x16x32_bf16 v[100:103], a[176:179], a[68:71], v[100:103]\n\t"
    "ds_read_b64_tr_b16 a[204:205], %[vr15] offset:16384\n\t" "v_mfma_f32_16x16x32_bf16 v[100:103], a[180:183], a[76:79], v[100:103]\n\t" "ds_read_b64_tr_b16 a[206:207], %[vr15] offset:18432\n\t"
    "s_add_u32 m0, %[sl0], 65536\n\t" "v_mfma_f32_16x16x32_bf16 v[104:107], a[184:187], a[64:67], v[104:107]\n\t" "s_barrier\n\t" "ds_read_b64_tr_b16 a[176:177], %[vr0] offset:24576\n\t"
    "v_mfma_f32_16x16x32_bf16 v[104:107], a[188:191], a[72:75], v[104:107]\n\t" "ds_read_b64_tr_b16 a[178:179], %[vr0] offset:26624\n\t" "global_load_lds_dwordx4 %[vb0], %[sb]\n\t"
    "v_mfma_f32_16x16x32_bf16 v[108:111], a[184:187], a[68:71], v[108:111]\n\t" "ds_read_b64_tr_b16 a[180:181], %[vr0] offset:32768\n\t" "v_add_u32 %[vb0], 0x10000, %[vb0]\n\t"
    "v_mfma_f32_16x16x32_bf16 v[108:111], a[188:191], a[76:79], v[108:111]\n\t" "ds_read_b64_tr_b16 a[182:183], %[vr0] offset:34816\n\t" "s_waitcnt lgkmcnt(4)\n\t"
    "v_mfma_f32_16x16x32_bf16 v[112:115], a[192:195], a[64:67], v[112:115]\n\t" "ds_read_b64_tr_b16 a[184:185], %[vr1] offset:24576\n\t" "s_add_u32 m0, %[sl1], 65536\n\t"
    "v_mfma_f32_16x16x32_bf16 v[112:115], a[196:199], a[72:75], v[112:115]\n\t" "ds_read_b64_tr_b16 a[186:187], %[vr1] offset:26624\n\t" "global_load_lds_dwordx4 %[vb1], %[sb]\n\t"
    "v_mfma_f32_16x16x32_bf16 v[116:119], a[192:195], a[68:71], v[116:119]\n\t" "ds_read_b64_tr_b16 a[188:189], %[vr1] offset:32768\n\t" "v_add_u32 %[vb1], 0x10000, %[vb1]\n\t"
    "v_mfma_f32_16x16x32_bf16 v[116:119], a[196:199], a[76:79], v[116:119]\n\t" "ds_read_b64_tr_b16 a[190:191], %[vr1] offset:34816\n\t" "s_add_u32 m0, %[sl0], 73728\n\t"
    "v_mfma_f32_16x16x32_bf16 v[120:123], a[200:203], a[64:67], v[120:123]\n\t" "ds_read_b64_tr_b16 a[192:193], %[vr2] offset:24576\n\t"
    "v_mfma_f32_16x16x32_bf16 v[120:123], a[204:207], a[72:75], v[120:123]\n\t" "ds_read_b64_tr_b16 a[194:195], %[vr2] offset:26624\n\t" "global_load_lds_dwordx4 %[vb0], %[sb]\n\t"
    "v_mfma_f32_16x16x32_bf16 v[124:127], a[200:203], a[68:71], v[124:127]\n\t" "ds_read_b64_tr_b16 a[196:197], %[vr2] offset:32768\n\t" "v_add_u32 %[vb0], 0x10000, %[vb0]\n\t"
    "v_mfma_f32_16x16x32_bf16 v[124:127], a[204:207], a[76:79], v[124:127]\n\t" "ds_read_b64_tr_b16 a[198:199], %[vr2] offset:34816\n\t" "s_waitcnt lgkmcnt(4) vmcnt(30)\n\t"
    "v_mfma_f32_16x16x32_bf16 v[224:227], a[176:179], a[0:3], v[224:227]\n\t" "ds_read_b64_tr_b16 a[200:201], %[vr3] offset:24576\n\t" "s_add_u32 m0, %[sl1], 73728\n\t"
    "v_mfma_f32_16x16x32_bf16 v[224:227], a[180:183], a[8:11], v[224:227]\n\t" "ds_read_b64_tr_b16 a[202:203], %[vr3] offset:26624\n\t" "global_load_lds_dwordx4 %[vb1], %[sb]\n\t"
    "v_mfma_f32_16x16x32_bf16 v[240:243], a[176:179], a[4:7], v[240:243]\n\t" "ds_read_b64_tr_b16 a[204:205], %[vr3] offset:32768\n\t" "v_add_u32 %[vb1], 0x10000, %[vb1]\n\t"
    "v_mfma_f32_16x16x32_bf16 v[240:243], a[180:183], a[12:15], v[240:243]\n\t" "ds_read_b64_tr_b16 a[206:207], %[vr3] offset:34816\n\t" "s_add_u32 s40, s40, 8192\n\t" "s_addc_u32 s41, s41, 0\n\t"
    "v_mfma_f32_16x16x32_bf16 v[228:231], a[184:187], a[0:3], v[228:231]\n\t" "ds_read_b64_tr_b16 a[176:177], %[vr12] offset:24576\n\t"
    "v_mfma_f32_16x16x32_bf16 v[228:231], a[188:191], a[8:11], v[228:231]\n\t" "ds_read_b64_tr_b16 a[178:179], %[vr12] offset:26624\n\t"
    "global_load_dwordx4 a[64:67], %[vx], s[40:41] offset:-4096 sc1\n\t" "v_mfma_f32_16x16x32_bf16 v[244:247], a[184:187], a[4:7], v[244:247]\n\t" "ds_read_b64_tr_b16 a[180:181], %[vr12] offset:32768\n\t"
    "v_mfma_f32_16x16x32_bf16 v[244:247], a[188:191], a[12:15], v[244:247]\n\t" "ds_read_b64_tr_b16 a[182:183], %[vr12] offset:34816\n\t" "s_waitcnt lgkmcnt(4)\n\t"
    "v_mfma_f32_16x16x32_bf16 v[232:235], a[192:195], a[0:3], v[232:235]\n\t" "ds_read_b64_tr_b16 a[184:185], %[vr13] offset:24576\n\t"
    "v_mfma_f32_16x16x32_bf16 v[232:235], a[196:199], a[8:11], v[232:235]\n\t" "ds_read_b64_tr_b16 a[186:187], %[vr13] offset:26624\n\t"
    "global_load_dwordx4 a[68:71], %[vx], s[40:41] offset:-3072 sc1\n\t" "v_mfma_f32_16x16x32_bf16 v[248:251], a[192:195], a[4:7], v[248:251]\n\t" "ds_read_b64_tr_b16 a[188:189], %[vr13] offset:32768\n\t"
    "v_mfma_f32_16x16x32_bf16 v[248:251], a[196:199], a[12:15], v[248:251]\n\t" "ds_read_b64_tr_b16 a[190:191], %[vr13] offset:34816\n\t"
    "v_mfma_f32_16x16x32_bf16 v[236:239], a[200:203], a[0:3], v[236:239]\n\t" "ds_read_b64_tr_b16 a[192:193], %[vr14] offset:24576\n\t"
    "v_mfma_f32_16x16x32_bf16 v[236:239], a[204:207], a[8:11], v[236:239]\n\t" "ds_read_b64_tr_b16 a[194:195], %[vr14] offset:26624\n\t"
    "global_load_dwordx4 a[72:75], %[vx], s[40:41] offset:-2048 sc1\n\t" "v_mfma_f32_16x16x32_bf16 v[252:255], a[200:203], a[4:7], v[252:255]\n\t" "ds_read_b64_tr_b16 a[196:197], %[vr14] offset:32768\n\t"
    "v_mfma_f32_16x16x32_bf16 v[252:255], a[204:207], a[12:15], v[252:255]\n\t" "ds_read_b64_tr_b16 a[198:199], %[vr14] offset:34816\n\t" "s_waitcnt lgkmcnt(4)\n\t"
    "v_mfma_f32_16x16x32_bf16 v[96:99], a[176:179], a[0:3], v[96:99]\n\t" "ds_read_b64_tr_b16 a[200:201], %[vr15] offset:24576\n\t" "v_mfma_f32_16x16x32_bf16 v[96:99], a[180:183], a[8:11], v[96:99]\n\t"
    "ds_read_b64_tr_b16 a[202:203], %[vr15] offset:26624\n\t" "global_load_dwordx4 a[76:79], %[vx], s[40:41] offset:-1024 sc1\n\t" "v_mfma_f32_16x16x32_bf16 v[100:103], a[176:179], a[4:7], v[100:103]\n\t"
    "ds_read_b64_tr_b16 a[204:205], %[vr15] offset:32768\n\t" "v_mfma_f32_16x16x32_bf16 v[100:103], a[180:183], a[12:15], v[100:103]\n\t" "ds_read_b64_tr_b16 a[206:207], %[vr15] offset:34816\n\t"
    "s_add_u32 m0, %[sl0], 81920\n\t" "v_mfma_f32_16x16x32_bf16 v[104:107], a[184:187], a[0:3], v[104:107]\n\t" "s_barrier\n\t" "ds_read_b64_tr_b16 a[176:177], %[vr0] offset:40960\n\t"
    "v_mfma_f32_16x16x32_bf16 v[104:107], a[188:191], a[8:11], v[104:107]\n\t" "ds_read_b64_tr_b16 a[178:179], %[vr0] offset:43008\n\t" "global_load_lds_dwordx4 %[vb0], %[sb]\n\t"
    "v_mfma_f32_16x16x32_bf16 v[108:111], a[184:187], a[4:7], v[108:111]\n\t" "ds_read_b64_tr_b16 a[180:181], %[vr8] offset:8192\n\t" "v_add_u32 %[vb0], 0x10000, %[vb0]\n\t"
    "v_mfma_f32_16x16x32_bf16 v[108:111], a[188:191], a[12:15], v[108:111]\n\t" "ds_read_b64_tr_b16 a[182:183], %[vr8] offset:10240\n\t" "s_waitcnt lgkmcnt(4)\n\t"
    "v_mfma_f32_16x16x32_bf16 v[112:115], a[192:195], a[0:3], v[112:115]\n\t" "ds_read_b64_tr_b16 a[184:185], %[vr1] offset:40960\n\t" "s_add_u32 m0, %[sl1], 81920\n\t"
    "v_mfma_f32_16x16x32_bf16 v[112:115], a[196:199], a[8:11], v[112:115]\n\t" "ds_read_b64_tr_b16 a[186:187], %[vr1] offset:43008\n\t" "global_load_lds_dwordx4 %[vb1], %[sb]\n\t"
    "v_mfma_f32_16x16x32_bf16 v[116:119], a[192:195], a[4:7], v[116:119]\n\t" "ds_read_b64_tr_b16 a[188:189], %[vr9] offset:8192\n\t" "v_add_u32 %[vb1], 0x10000, %[vb1]\n\t"
    "v_mfma_f32_16x16x32_bf16 v[116:119], a[196:199], a[12:15], v[116:119]\n\t" "ds_read_b64_tr_b16 a[190:191], %[vr9] offset:10240\n\t" "s_add_u32 m0, %[sl0], 90112\n\t"
    "v_mfma_f32_16x16x32_bf16 v[120:123], a[200:203], a[0:3], v[120:123]\n\t" "ds_read_b64_tr_b16 a[192:193], %[vr2] offset:40960\n\t"
    "v_mfma_f32_16x16x32_bf16 v[120:123], a[204:207], a[8:11], v[120:123]\n\t" "ds_read_b64_tr_b16 a[194:195], %[vr2] offset:43008\n\t" "global_load_lds_dwordx4 %[vb0], %[sb]\n\t"
    "v_mfma_f32_16x16x32_bf16 v[124:127], a[200:203], a[4:7], v[124:127]\n\t" "ds_read_b64_tr_b16 a[196:197], %[vr10] offset:8192\n\t" "v_add_u32 %[vb0], 0x10000, %[vb0]\n\t"
    "v_mfma_f32_16x16x32_bf16 v[124:127], a[204:207], a[12:15], v[124:127]\n\t" "ds_read_b64_tr_b16 a[198:199], %[vr10] offset:10240\n\t" "s_waitcnt lgkmcnt(4) vmcnt(29)\n\t"
    "v_mfma_f32_16x16x32_bf16 v[224:227], a[176:179], a[16:19], v[224:227]\n\t" "ds_read_b64_tr_b16 a[200:201], %[vr3] offset:40960\n\t" "s_add_u32 m0, %[sl1], 90112\n\t"
    "v_mfma_f32_16x16x32_bf16 v[224:227], a[180:183], a[24:27], v[224:227]\n\t" "ds_read_b64_tr_b16 a[202:203], %[vr3] offset:43008\n\t" "global_load_lds_dwordx4 %[vb1], %[sb]\n\t"
    "v_mfma_f32_16x16x32_bf16 v[240:243], a[176:179], a[20:23], v[240:243]\n\t" "ds_read_b64_tr_b16 a[204:205], %[vr11] offset:8192\n\t" "v_add_u32 %[vb1], 0x10000, %[vb1]\n\t"
    "v_mfma_f32_16x16x32_bf16 v[240:243], a[180:183], a[28:31], v[240:243]\n\t" "ds_read_b64_tr_b16 a[206:207], %[vr11] offset:10240\n\t"
    "v_mfma_f32_16x16x32_bf16 v[228:231], a[184:187], a[16:19], v[228:231]\n\t" "ds_read_b64_tr_b16 a[176:177], %[vr12] offset:40960\n\t"
    "v_mfma_f32_16x16x32_bf16 v[228:231], a[188:191], a[24:27], v[228:231]\n\t" "ds_read_b64_tr_b16 a[178:179], %[vr12] offset:43008\n\t" "global_load_dwordx4 a[0:3], %[vx], s[40:41] offset:0 sc1\n\t"
    "v_mfma_f32_16x16x32_bf16 v[244:247], a[184:187], a[20:23], v[244:247]\n\t" "ds_read_b64_tr_b16 a[180:181], %[vr20] offset:8192\n\t"
    "v_mfma_f32_16x16x32_bf16 v[244:247], a[188:191], a[28:31], v[244:247]\n\t" "ds_read_b64_tr_b16 a[182:183], %[vr20] offset:10240\n\t" "s_waitcnt lgkmcnt(4)\n\t"
    "v_mfma_f32_16x16x32_bf16 v[232:235], a[192:195], a[16:19], v[232:235]\n\t" "ds_read_b64_tr_b16 a[184:185], %[vr13] offset:40960\n\t"
    "v_mfma_f32_16x16x32_bf16 v[232:235], a[196:199], a[24:27], v[232:235]\n\t" "ds_read_b64_tr_b16 a[186:187], %[vr13] offset:43008\n\t" "global_load_dwordx4 a[4:7], %[vx], s[40:41] offset:1024 sc1\n\t"
    "v_mfma_f32_16x16x32_bf16 v[248:251], a[192:195], a[20:23], v[248:251]\n\t" "ds_read_b64_tr_b16 a[188:189], %[vr21] offset:8192\n\t"
    "v_mfma_f32_16x16x32_bf16 v[248:251], a[196:199], a[28:31], v[248:251]\n\t" "ds_read_b64_tr_b16 a[190:191], %[vr21] offset:10240\n\t"
    "v_mfma_f32_16x16x32_bf16 v[236:239], a[200:203], a[16:19], v[236:239]\n\t" "ds_read_b64_tr_b16 a[192:193], %[vr14] offset:40960\n\t"
    "v_mfma_f32_16x16x32_bf16 v[236:239], a[204:207], a[24:27], v[236:239]\n\t" "ds_read_b64_tr_b16 a[194:195], %[vr14] offset:43008\n\t" "global_load_dwordx2 a[8:9], %[vo], s[40:41] offset:2048 sc1\n\t"
    "v_mfma_f32_16x16x32_bf16 v[252:255], a[200:203], a[20:23], v[252:255]\n\t" "ds_read_b64_tr_b16 a[196:197], %[vr22] offset:8192\n\t"
    "v_mfma_f32_16x16x32_bf16 v[252:255], a[204:207], a[28:31], v[252:255]\n\t" "ds_read_b64_tr_b16 a[198:199], %[vr22] offset:10240\n\t" "s_waitcnt lgkmcnt(4)\n\t"
    "v_mfma_f32_16x16x32_bf16 v[96:99], a[176:179], a[16:19], v[96:99]\n\t" "ds_read_b64_tr_b16 a[200:201], %[vr15] offset:40960\n\t" "global_load_dwordx2 a[10:11], %[vo], s[40:41] offset:3072 sc1\n\t"
    "v_mfma_f32_16x16x32_bf16 v[96:99], a[180:183], a[24:27], v[96:99]\n\t" "ds_read_b64_tr_b16 a[202:203], %[vr15] offset:43008\n\t" "global_load_dwordx2 a[12:13], %[vo], s[40:41] offset:2560 sc1\n\t"
    "v_mfma_f32_16x16x32_bf16 v[100:103], a[176:179], a[20:23], v[100:103]\n\t" "ds_read_b64_tr_b16 a[204:205], %[vr23] offset:8192\n\t"
    "v_mfma_f32_16x16x32_bf16 v[100:103], a[180:183], a[28:31], v[100:103]\n\t" "ds_read_b64_tr_b16 a[206:207], %[vr23] offset:10240\n\t"
    "global_load_dwordx2 a[14:15], %[vo], s[40:41] offset:3584 sc1\n\t" "v_mfma_f32_16x16x32_bf16 v[104:107], a[184:187], a[16:19], v[104:107]\n\t" "s_barrier\n\t"
    "ds_read_b64_tr_b16 a[176:177], %[vr8] offset:16384\n\t" "v_mfma_f32_16x16x32_bf16 v[104:107], a[188:191], a[24:27], v[104:107]\n\t" "ds_read_b64_tr_b16 a[178:179], %[vr8] offset:18432\n\t"
    "s_add_u32 m0, %[sl0], 98304\n\t" "v_mfma_f32_16x16x32_bf16 v[108:111], a[184:187], a[20:23], v[108:111]\n\t" "ds_read_b64_tr_b16 a[180:181], %[vr8] offset:24576\n\t"
    "global_load_lds_dwordx4 %[vb0], %[sb]\n\t" "v_mfma_f32_16x16x32_bf16 v[108:111], a[188:191], a[28:31], v[108:111]\n\t" "ds_read_b64_tr_b16 a[182:183], %[vr8] offset:26624\n\t"
    "s_waitcnt lgkmcnt(4)\n\t" "v_mfma_f32_16x16x32_bf16 v[112:115], a[192:195], a[16:19], v[112:115]\n\t" "ds_read_b64_tr_b16 a[184:185], %[vr9] offset:16384\n\t" "v_add_u32 %[vb0], 0x10000, %[vb0]\n\t"
    "v_mfma_f32_16x16x32_bf16 v[112:115], a[196:199], a[24:27], v[112:115]\n\t" "ds_read_b64_tr_b16 a[186:187], %[vr9] offset:18432\n\t" "s_add_u32 m0, %[sl1], 98304\n\t"
    "v_mfma_f32_16x16x32_bf16 v[116:119], a[192:195], a[20:23], v[116:119]\n\t" "ds_read_b64_tr_b16 a[188:189], %[vr9] offset:24576\n\t" "global_load_lds_dwordx4 %[vb1], %[sb]\n\t"
    "v_mfma_f32_16x16x32_bf16 v[116:119], a[196:199], a[28:31], v[116:119]\n\t" "ds_read_b64_tr_b16 a[190:191], %[vr9] offset:26624\n\t" "v_add_u32 %[vb1], 0x10000, %[vb1]\n\t"
    "v_mfma_f32_16x16x32_bf16 v[120:123], a[200:203], a[16:19], v[120:123]\n\t" "ds_read_b64_tr_b16 a[192:193], %[vr10] offset:16384\n\t" "s_add_u32 m0, %[sl0], 106496\n\t"
    "v_mfma_f32_16x16x32_bf16 v[120:123], a[204:207], a[24:27], v[120:123]\n\t" "ds_read_b64_tr_b16 a[194:195], %[vr10] offset:18432\n\t" "global_load_lds_dwordx4 %[vb0], %[sb]\n\t"
    "v_mfma_f32_16x16x32_bf16 v[124:127], a[200:203], a[20:23], v[124:127]\n\t" "ds_read_b64_tr_b16 a[196:197], %[vr10] offset:24576\n\t" "v_add_u32 %[vb0], 0x10000, %[vb0]\n\t"
    "v_mfma_f32_16x16x32_bf16 v[124:127], a[204:207], a[28:31], v[124:127]\n\t" "ds_read_b64_tr_b16 a[198:199], %[vr10] offset:26624\n\t" "s_waitcnt lgkmcnt(4) vmcnt(29)\n\t"
    "v_mfma_f32_16x16x32_bf16 v[224:227], a[176:179], a[32:35], v[224:227]\n\t" "ds_read_b64_tr_b16 a[200:201], %[vr11] offset:16384\n\t" "s_add_u32 m0, %[sl1], 106496\n\t"
    "v_mfma_f32_16x16x32_bf16 v[224:227], a[180:183], a[40:43], v[224:227]\n\t" "ds_read_b64_tr_b16 a[202:203], %[vr11] offset:18432\n\t" "global_load_lds_dwordx4 %[vb1], %[sb]\n\t"
    "v_mfma_f32_16x16x32_bf16 v[240:243], a[176:179], a[36:39], v[240:243]\n\t" "ds_read_b64_tr_b16 a[204:205], %[vr11] offset:24576\n\t" "v_add_u32 %[vb1], 0x10000, %[vb1]\n\t"
    "v_mfma_f32_16x16x32_bf16 v[240:243], a[180:183], a[44:47], v[240:243]\n\t" "ds_read_b64_tr_b16 a[206:207], %[vr11] offset:26624\n\t" "s_add_u32 s40, s40, 8192\n\t" "s_addc_u32 s41, s41, 0\n\t"
    "v_mfma_f32_16x16x32_bf16 v[228:231], a[184:187], a[32:35], v[228:231]\n\t" "ds_read_b64_tr_b16 a[176:177], %[vr20] offset:16384\n\t"
    "v_mfma_f32_16x16x32_bf16 v[228:231], a[188:191], a[40:43], v[228:231]\n\t" "ds_read_b64_tr_b16 a[178:179], %[vr20] offset:18432\n\t"
    "global_load_dwordx4 a[16:19], %[vx], s[40:41] offset:-4096 sc1\n\t" "v_mfma_f32_16x16x32_bf16 v[244:247], a[184:187], a[36:39], v[244:247]\n\t"
    "ds_read_b64_tr_b16 a[180:181], %[vr20] offset:24576\n\t" "v_mfma_f32_16x16x32_bf16 v[244:247], a[188:191], a[44:47], v[244:247]\n\t" "ds_read_b64_tr_b16 a[182:183], %[vr20] offset:26624\n\t"
    "s_waitcnt lgkmcnt(4)\n\t" "v_mfma_f32_16x16x32_bf16 v[232:235], a[192:195], a[32:35], v[232:235]\n\t" "ds_read_b64_tr_b16 a[184:185], %[vr21] offset:16384\n\t"
    "v_mfma_f32_16x16x32_bf16 v[232:235], a[196:199], a[40:43], v[232:235]\n\t" "ds_read_b64_tr_b16 a[186:187], %[vr21] offset:18432\n\t"
    "global_load_dwordx4 a[20:23], %[vx], s[40:41] offset:-3072 sc1\n\t" "v_mfma_f32_16x16x32_bf16 v[248:251], a[192:195], a[36:39], v[248:251]\n\t"
    "ds_read_b64_tr_b16 a[188:189], %[vr21] offset:24576\n\t" "v_mfma_f32_16x16x32_bf16 v[248:251], a[196:199], a[44:47], v[248:251]\n\t" "ds_read_b64_tr_b16 a[190:191], %[vr21] offset:26624\n\t"
    "v_mfma_f32_16x16x32_bf16 v[236:239], a[200:203], a[32:35], v[236:239]\n\t" "ds_read_b64_tr_b16 a[192:193], %[vr22] offset:16384\n\t"
    "v_mfma_f32_16x16x32_bf16 v[236:239], a[204:207], a[40:43], v[236:239]\n\t" "ds_read_b64_tr_b16 a[194:195], %[vr22] offset:18432\n\t"
    "global_load_dwordx4 a[24:27], %[vx], s[40:41] offset:-2048 sc1\n\t" "v_mfma_f32_16x16x32_bf16 v[252:255], a[200:203], a[36:39], v[252:255]\n\t"
    "ds_read_b64_tr_b16 a[196:197], %[vr22] offset:24576\n\t" "v_mfma_f32_16x16x32_bf16 v[252:255], a[204:207], a[44:47], v[252:255]\n\t" "ds_read_b64_tr_b16 a[198:199], %[vr22] offset:26624\n\t"
    "s_waitcnt lgkmcnt(4)\n\t" "v_mfma_f32_16x16x32_bf16 v[96:99], a[176:179], a[32:35], v[96:99]\n\t" "ds_read_b64_tr_b16 a[200:201], %[vr23] offset:16384\n\t"
    "v_mfma_f32_16x16x32_bf16 v[96:99], a[180:183], a[40:43], v[96:99]\n\t" "ds_read_b64_tr_b16 a[202:203], %[vr23] offset:18432\n\t" "global_load_dwordx4 a[28:31], %[vx], s[40:41] offset:-1024 sc1\n\t"
    "v_mfma_f32_16x16x32_bf16 v[100:103], a[176:179], a[36:39], v[100:103]\n\t" "ds_read_b64_tr_b16 a[204:205], %[vr23] offset:24576\n\t"
    "v_mfma_f32_16x16x32_bf16 v[100:103], a[180:183], a[44:47], v[100:103]\n\t" "ds_read_b64_tr_b16 a[206:207], %[vr23] offset:26624\n\t" "s_add_u32 m0, %[sl0], 114688\n\t"
    "v_mfma_f32_16x16x32_bf16 v[104:107], a[184:187], a[32:35], v[104:107]\n\t" "s_barrier\n\t" "ds_read_b64_tr_b16 a[176:177], %[vr0] offset:0\n\t"
    "v_mfma_f32_16x16x32_bf16 v[104:107], a[188:191], a[40:43], v[104:107]\n\t" "ds_read_b64_tr_b16 a[178:179], %[vr0] offset:2048\n\t" "global_load_lds_dwordx4 %[vb0], %[sb]\n\t"
    "v_mfma_f32_16x16x32_bf16 v[108:111], a[184:187], a[36:39], v[108:111]\n\t" "ds_read_b64_tr_b16 a[180:181], %[vr0] offset:8192\n\t" "v_add_u32 %[vb0], 0x10000, %[vb0]\n\t"
    "v_mfma_f32_16x16x32_bf16 v[108:111], a[188:191], a[44:47], v[108:111]\n\t" "ds_read_b64_tr_b16 a[182:183], %[vr0] offset:10240\n\t" "s_waitcnt lgkmcnt(4)\n\t"
    "v_mfma_f32_16x16x32_bf16 v[112:115], a[192:195], a[32:35], v[112:115]\n\t" "ds_read_b64_tr_b16 a[184:185], %[vr1] offset:0\n\t" "s_add_u32 m0, %[sl1], 114688\n\t"
    "v_mfma_f32_16x16x32_bf16 v[112:115], a[196:199], a[40:43], v[112:115]\n\t" "ds_read_b64_tr_b16 a[186:187], %[vr1] offset:2048\n\t" "global_load_lds_dwordx4 %[vb1], %[sb]\n\t"
    "v_mfma_f32_16x16x32_bf16 v[116:119], a[192:195], a[36:39], v[116:119]\n\t" "ds_read_b64_tr_b16 a[188:189], %[vr1] offset:8192\n\t" "v_add_u32 %[vb1], 0x10000, %[vb1]\n\t"
    "v_mfma_f32_16x16x32_bf16 v[116:119], a[196:199], a[44:47], v[116:119]\n\t" "ds_read_b64_tr_b16 a[190:191], %[vr1] offset:10240\n\t" "s_add_u32 m0, %[sl0], 122880\n\t"
    "v_mfma_f32_16x16x32_bf16 v[120:123], a[200:203], a[32:35], v[120:123]\n\t" "ds_read_b64_tr_b16 a[192:193], %[vr2] offset:0\n\t"
    "v_mfma_f32_16x16x32_bf16 v[120:123], a[204:207], a[40:43], v[120:123]\n\t" "ds_read_b64_tr_b16 a[194:195], %[vr2] offset:2048\n\t" "global_load_lds_dwordx4 %[vb0], %[sb]\n\t"
    "v_mfma_f32_16x16x32_bf16 v[124:127], a[200:203], a[36:39], v[124:127]\n\t" "ds_read_b64_tr_b16 a[196:197], %[vr2] offset:8192\n\t" "v_add_u32 %[vb0], 0x10000, %[vb0]\n\t"
    "v_mfma_f32_16x16x32_bf16 v[124:127], a[204:207], a[44:47], v[124:127]\n\t" "ds_read_b64_tr_b16 a[198:199], %[vr2] offset:10240\n\t" "s_waitcnt lgkmcnt(4) vmcnt(29)\n\t"
    "v_mfma_f32_16x16x32_bf16 v[224:227], a[176:179], a[48:51], v[224:227]\n\t" "ds_read_b64_tr_b16 a[200:201], %[vr3] offset:0\n\t" "s_add_u32 m0, %[sl1], 122880\n\t"
    "v_mfma_f32_16x16x32_bf16 v[224:227], a[180:183], a[56:59], v[224:227]\n\t" "ds_read_b64_tr_b16 a[202:203], %[vr3] offset:2048\n\t" "global_load_lds_dwordx4 %[vb1], %[sb]\n\t"
    "v_mfma_f32_16x16x32_bf16 v[240:243], a[176:179], a[52:55], v[240:243]\n\t" "ds_read_b64_tr_b16 a[204:205], %[vr3] offset:8192\n\t" "v_add_u32 %[vb1], 0x10000, %[vb1]\n\t"
    "v_mfma_f32_16x16x32_bf16 v[240:243], a[180:183], a[60:63], v[240:243]\n\t" "ds_read_b64_tr_b16 a[206:207], %[vr3] offset:10240\n\t"
    "v_mfma_f32_16x16x32_bf16 v[228:231], a[184:187], a[48:51], v[228:231]\n\t" "ds_read_b64_tr_b16 a[176:177], %[vr12] offset:0\n\t"
    "v_mfma_f32_16x16x32_bf16 v[228:231], a[188:191], a[56:59], v[228:231]\n\t" "ds_read_b64_tr_b16 a[178:179], %[vr12] offset:2048\n\t" "global_load_dwordx4 v[176:179], %[vg], %[sg] offset:0 sc1\n\t"
    "v_mfma_f32_16x16x32_bf16 v[244:247], a[184:187], a[52:55], v[244:247]\n\t" "ds_read_b64_tr_b16 a[180:181], %[vr12] offset:8192\n\t"
    "v_mfma_f32_16x16x32_bf16 v[244:247], a[188:191], a[60:63], v[244:247]\n\t" "ds_read_b64_tr_b16 a[182:183], %[vr12] offset:10240\n\t" "s_waitcnt lgkmcnt(4)\n\t"
    "v_mfma_f32_16x16x32_bf16 v[232:235], a[192:195], a[48:51], v[232:235]\n\t" "ds_read_b64_tr_b16 a[184:185], %[vr13] offset:0\n\t" "global_load_dwordx4 a[32:35], %[vx], s[40:41] offset:0 sc1\n\t"
    "v_mfma_f32_16x16x32_bf16 v[232:235], a[196:199], a[56:59], v[232:235]\n\t" "ds_read_b64_tr_b16 a[186:187], %[vr13] offset:2048\n\t"
    "v_mfma_f32_16x16x32_bf16 v[248:251], a[192:195], a[52:55], v[248:251]\n\t" "ds_read_b64_tr_b16 a[188:189], %[vr13] offset:8192\n\t"
    "v_mfma_f32_16x16x32_bf16 v[248:251], a[196:199], a[60:63], v[248:251]\n\t" "ds_read_b64_tr_b16 a[190:191], %[vr13] offset:10240\n\t"
    "global_load_dwordx4 a[36:39], %[vx], s[40:41] offset:1024 sc1\n\t" "v_mfma_f32_16x16x32_bf16 v[236:239], a[200:203], a[48:51], v[236:239]\n\t" "ds_read_b64_tr_b16 a[192:193], %[vr14] offset:0\n\t"
    "v_mfma_f32_16x16x32_bf16 v[236:239], a[204:207], a[56:59], v[236:239]\n\t" "ds_read_b64_tr_b16 a[194:195], %[vr14] offset:2048\n\t"
    "v_mfma_f32_16x16x32_bf16 v[252:255], a[200:203], a[52:55], v[252:255]\n\t" "ds_read_b64_tr_b16 a[196:197], %[vr14] offset:8192\n\t"
    "v_mfma_f32_16x16x32_bf16 v[252:255], a[204:207], a[60:63], v[252:255]\n\t" "ds_read_b64_tr_b16 a[198:199], %[vr14] offset:10240\n\t" "s_waitcnt lgkmcnt(4)\n\t"
    "v_mfma_f32_16x16x32_bf16 v[96:99], a[176:179], a[48:51], v[96:99]\n\t" "ds_read_b64_tr_b16 a[200:201], %[vr15] offset:0\n\t" "global_load_dwordx4 a[40:43], %[vx], s[40:41] offset:2048 sc1\n\t"
    "v_mfma_f32_16x16x32_bf16 v[96:99], a[180:183], a[56:59], v[96:99]\n\t" "ds_read_b64_tr_b16 a[202:203], %[vr15] offset:2048\n\t"
    "v_mfma_f32_16x16x32_bf16 v[100:103], a[176:179], a[52:55], v[100:103]\n\t" "ds_read_b64_tr_b16 a[204:205], %[vr15] offset:8192\n\t"
    "v_mfma_f32_16x16x32_bf16 v[100:103], a[180:183], a[60:63], v[100:103]\n\t" "ds_read_b64_tr_b16 a[206:207], %[vr15] offset:10240\n\t" "global_load_dwordx4 v[180:183], %[vg], %[sg] offset:64 sc1\n\t"
    "v_mfma_f32_16x16x32_bf16 v[104:107], a[184:187], a[48:51], v[104:107]\n\t" "s_barrier\n\t" "ds_read_b64_tr_b16 a[176:177], %[vr0] offset:49152\n\t"
    "v_mfma_f32_16x16x32_bf16 v[104:107], a[188:191], a[56:59], v[104:107]\n\t" "ds_read_b64_tr_b16 a[178:179], %[vr0] offset:51200\n\t" "global_load_dwordx4 a[44:47], %[vx], s[40:41] offset:3072 sc1\n\t"
    "v_mfma_f32_16x16x32_bf16 v[108:111], a[184:187], a[52:55], v[108:111]\n\t" "ds_read_b64_tr_b16 a[180:181], %[vr0] offset:57344\n\t"
    "v_mfma_f32_16x16x32_bf16 v[108:111], a[188:191], a[60:63], v[108:111]\n\t" "ds_read_b64_tr_b16 a[182:183], %[vr0] offset:59392\n\t" "s_waitcnt lgkmcnt(4)\n\t"
    "v_mfma_f32_16x16x32_bf16 v[112:115], a[192:195], a[48:51], v[112:115]\n\t" "ds_read_b64_tr_b16 a[184:185], %[vr1] offset:49152\n\t" "s_add_u32 m0, %[sl0], 131072\n\t"
    "v_mfma_f32_16x16x32_bf16 v[112:115], a[196:199], a[56:59], v[112:115]\n\t" "ds_read_b64_tr_b16 a[186:187], %[vr1] offset:51200\n\t" "global_load_lds_dwordx4 %[vb0], %[sb]\n\t"
    "v_mfma_f32_16x16x32_bf16 v[116:119], a[192:195], a[52:55], v[116:119]\n\t" "ds_read_b64_tr_b16 a[188:189], %[vr1] offset:57344\n\t" "v_add_u32 %[vb0], 0x10000, %[vb0]\n\t"
    "v_mfma_f32_16x16x32_bf16 v[116:119], a[196:199], a[60:63], v[116:119]\n\t" "ds_read_b64_tr_b16 a[190:191], %[vr1] offset:59392\n\t" "s_add_u32 m0, %[sl1], 131072\n\t"
    "v_mfma_f32_16x16x32_bf16 v[120:123], a[200:203], a[48:51], v[120:123]\n\t" "ds_read_b64_tr_b16 a[192:193], %[vr2] offset:49152\n\t"
    "v_mfma_f32_16x16x32_bf16 v[120:123], a[204:207], a[56:59], v[120:123]\n\t" "ds_read_b64_tr_b16 a[194:195], %[vr2] offset:51200\n\t" "global_load_lds_dwordx4 %[vb1], %[sb]\n\t"
    "v_mfma_f32_16x16x32_bf16 v[124:127], a[200:203], a[52:55], v[124:127]\n\t" "ds_read_b64_tr_b16 a[196:197], %[vr2] offset:57344\n\t" "v_add_u32 %[vb1], 0x10000, %[vb1]\n\t"
    "v_mfma_f32_16x16x32_bf16 v[124:127], a[204:207], a[60:63], v[124:127]\n\t" "ds_read_b64_tr_b16 a[198:199], %[vr2] offset:59392\n\t" "s_waitcnt lgkmcnt(4) vmcnt(30)\n\t"
    "v_mfma_f32_16x16x32_bf16 v[224:227], a[176:179], a[64:67], v[224:227]\n\t" "ds_read_b64_tr_b16 a[200:201], %[vr3] offset:49152\n\t" "s_add_u32 m0, %[sl0], 16384\n\t"
    "v_mfma_f32_16x16x32_bf16 v[224:227], a[180:183], a[72:75], v[224:227]\n\t" "ds_read_b64_tr_b16 a[202:203], %[vr3] offset:51200\n\t" "global_load_lds_dwordx4 %[vb0], %[sb]\n\t"
    "v_mfma_f32_16x16x32_bf16 v[240:243], a[176:179], a[68:71], v[240:243]\n\t" "ds_read_b64_tr_b16 a[204:205], %[vr3] offset:57344\n\t" "v_add_u32 %[vb0], 0x10000, %[vb0]\n\t"
    "v_mfma_f32_16x16x32_bf16 v[240:243], a[180:183], a[76:79], v[240:243]\n\t" "ds_read_b64_tr_b16 a[206:207], %[vr3] offset:59392\n\t"
    "v_mfma_f32_16x16x32_bf16 v[228:231], a[184:187], a[64:67], v[228:231]\n\t" "ds_read_b64_tr_b16 a[176:177], %[vr12] offset:49152\n\t"
    "v_mfma_f32_16x16x32_bf16 v[228:231], a[188:191], a[72:75], v[228:231]\n\t" "ds_read_b64_tr_b16 a[178:179], %[vr12] offset:51200\n\t" "global_load_dwordx4 v[184:187], %[vg], %[sg] offset:128 sc1\n\t"
    "v_mfma_f32_16x16x32_bf16 v[244:247], a[184:187], a[68:71], v[244:247]\n\t" "ds_read_b64_tr_b16 a[180:181], %[vr12] offset:57344\n\t"
    "v_mfma_f32_16x16x32_bf16 v[244:247], a[188:191], a[76:79], v[244:247]\n\t" "ds_read_b64_tr_b16 a[182:183], %[vr12] offset:59392\n\t" "s_waitcnt lgkmcnt(4)\n\t"
    "v_mfma_f32_16x16x32_bf16 v[232:235], a[192:195], a[64:67], v[232:235]\n\t" "ds_read_b64_tr_b16 a[184:185], %[vr13] offset:49152\n\t" "global_load_dword a255, %[vx], %[cd]\n\t"
    "v_mfma_f32_16x16x32_bf16 v[232:235], a[196:199], a[72:75], v[232:235]\n\t" "ds_read_b64_tr_b16 a[186:187], %[vr13] offset:51200\n\t" "s_add_u32 s40, s40, 8192\n\t" "s_addc_u32 s41, s41, 0\n\t"
    "v_mfma_f32_16x16x32_bf16 v[248:251], a[192:195], a[68:71], v[248:251]\n\t" "ds_read_b64_tr_b16 a[188:189], %[vr13] offset:57344\n\t"
    "global_load_dwordx4 a[48:51], %[vx], s[40:41] offset:-4096 sc1\n\t" "v_mfma_f32_16x16x32_bf16 v[248:251], a[196:199], a[76:79], v[248:251]\n\t"
    "ds_read_b64_tr_b16 a[190:191], %[vr13] offset:59392\n\t" "v_mfma_f32_16x16x32_bf16 v[236:239], a[200:203], a[64:67], v[236:239]\n\t" "ds_read_b64_tr_b16 a[192:193], %[vr14] offset:49152\n\t"
    "v_mfma_f32_16x16x32_bf16 v[236:239], a[204:207], a[72:75], v[236:239]\n\t" "ds_read_b64_tr_b16 a[194:195], %[vr14] offset:51200\n\t"
    "global_load_dwordx4 a[52:55], %[vx], s[40:41] offset:-3072 sc1\n\t" "v_mfma_f32_16x16x32_bf16 v[252:255], a[200:203], a[68:71], v[252:255]\n\t"
    "ds_read_b64_tr_b16 a[196:197], %[vr14] offset:57344\n\t" "v_mfma_f32_16x16x32_bf16 v[252:255], a[204:207], a[76:79], v[252:255]\n\t" "ds_read_b64_tr_b16 a[198:199], %[vr14] offset:59392\n\t"
    "s_waitcnt lgkmcnt(4)\n\t" "v_mfma_f32_16x16x32_bf16 v[96:99], a[176:179], a[64:67], v[96:99]\n\t" "ds_read_b64_tr_b16 a[200:201], %[vr15] offset:49152\n\t"
    "v_mfma_f32_16x16x32_bf16 v[96:99], a[180:183], a[72:75], v[96:99]\n\t" "ds_read_b64_tr_b16 a[202:203], %[vr15] offset:51200\n\t" "global_load_dwordx4 a[56:59], %[vx], s[40:41] offset:-2048 sc1\n\t"
    "v_mfma_f32_16x16x32_bf16 v[100:103], a[176:179], a[68:71], v[100:103]\n\t" "ds_read_b64_tr_b16 a[204:205], %[vr15] offset:57344\n\t"
    "v_mfma_f32_16x16x32_bf16 v[100:103], a[180:183], a[76:79], v[100:103]\n\t" "ds_read_b64_tr_b16 a[206:207], %[vr15] offset:59392\n\t"
    "v_mfma_f32_16x16x32_bf16 v[104:107], a[184:187], a[64:67], v[104:107]\n\t" "s_barrier\n\t" "ds_read_b64_tr_b16 a[176:177], %[vr4] offset:0\n\t"
    "v_mfma_f32_16x16x32_bf16 v[104:107], a[188:191], a[72:75], v[104:107]\n\t" "ds_read_b64_tr_b16 a[178:179], %[vr4] offset:2048\n\t" "global_load_dwordx4 v[188:191], %[vg], %[sg] offset:192 sc1\n\t"
    "v_mfma_f32_16x16x32_bf16 v[108:111], a[184:187], a[68:71], v[108:111]\n\t" "ds_read_b64_tr_b16 a[180:181], %[vr4] offset:8192\n\t"
    "v_mfma_f32_16x16x32_bf16 v[108:111], a[188:191], a[76:79], v[108:111]\n\t" "ds_read_b64_tr_b16 a[182:183], %[vr4] offset:10240\n\t" "s_waitcnt lgkmcnt(4)\n\t"
    "v_mfma_f32_16x16x32_bf16 v[112:115], a[192:195], a[64:67], v[112:115]\n\t" "ds_read_b64_tr_b16 a[184:185], %[vr5] offset:0\n\t" "global_load_dwordx4 a[60:63], %[vx], s[40:41] offset:-1024 sc1\n\t"
    "v_mfma_f32_16x16x32_bf16 v[112:115], a[196:199], a[72:75], v[112:115]\n\t" "ds_read_b64_tr_b16 a[186:187], %[vr5] offset:2048\n\t" "s_add_u32 m0, %[sl1], 16384\n\t"
    "v_mfma_f32_16x16x32_bf16 v[116:119], a[192:195], a[68:71], v[116:119]\n\t" "ds_read_b64_tr_b16 a[188:189], %[vr5] offset:8192\n\t"
    "v_mfma_f32_16x16x32_bf16 v[116:119], a[196:199], a[76:79], v[116:119]\n\t" "ds_read_b64_tr_b16 a[190:191], %[vr5] offset:10240\n\t" "global_load_lds_dwordx4 %[vb1], %[sb]\n\t"
    "v_mfma_f32_16x16x32_bf16 v[120:123], a[200:203], a[64:67], v[120:123]\n\t" "ds_read_b64_tr_b16 a[192:193], %[vr6] offset:0\n\t" "v_add_u32 %[vb1], 0x10000, %[vb1]\n\t"
    "v_mfma_f32_16x16x32_bf16 v[120:123], a[204:207], a[72:75], v[120:123]\n\t" "ds_read_b64_tr_b16 a[194:195], %[vr6] offset:2048\n\t" "s_add_u32 m0, %[sl0], 24576\n\t"
    "v_mfma_f32_16x16x32_bf16 v[124:127], a[200:203], a[68:71], v[124:127]\n\t" "ds_read_b64_tr_b16 a[196:197], %[vr6] offset:8192\n\t"
    "v_mfma_f32_16x16x32_bf16 v[124:127], a[204:207], a[76:79], v[124:127]\n\t" "ds_read_b64_tr_b16 a[198:199], %[vr6] offset:10240\n\t" "s_waitcnt lgkmcnt(4) vmcnt(29)\n\t"
    "v_mfma_f32_16x16x32_bf16 v[224:227], a[176:179], a[0:3], v[224:227]\n\t" "ds_read_b64_tr_b16 a[200:201], %[vr7] offset:0\n\t" "global_load_lds_dwordx4 %[vb0], %[sb]\n\t"
    "v_mfma_f32_16x16x32_bf16 v[224:227], a[180:183], a[8:11], v[224:227]\n\t" "ds_read_b64_tr_b16 a[202:203], %[vr7] offset:2048\n\t" "v_add_u32 %[vb0], 0x10000, %[vb0]\n\t"
    "v_mfma_f32_4x4x4_16b_bf16 v[144:147], %[id], a[232:233], 0\n\t" "v_mfma_f32_4x4x4_16b_bf16 v[152:155], %[id], a[234:235], 0\n\t" "v_mfma_f32_4x4x4_16b_bf16 v[160:163], %[id], a[236:237], 0\n\t"
    "v_mfma_f32_4x4x4_16b_bf16 v[168:171], %[id], a[238:239], 0\n\t" "v_mfma_f32_4x4x4_16b_bf16 v[148:151], %[id], a[240:241], 0\n\t" "v_mfma_f32_4x4x4_16b_bf16 v[156:159], %[id], a[242:243], 0\n\t"
    "v_mfma_f32_4x4x4_16b_bf16 v[164:167], %[id], a[244:245], 0\n\t" "v_mfma_f32_4x4x4_16b_bf16 v[172:175], %[id], a[246:247], 0\n\t"
    "v_mfma_f32_16x16x32_bf16 v[240:243], a[176:179], a[4:7], v[240:243]\n\t" "ds_read_b64_tr_b16 a[204:205], %[vr7] offset:8192\n\t"
    "v_mfma_f32_16x16x32_bf16 v[240:243], a[180:183], a[12:15], v[240:243]\n\t" "ds_read_b64_tr_b16 a[206:207], %[vr7] offset:10240\n\t" "global_load_dwordx2 a[64:65], %[vo], s[40:41] offset:0 sc1\n\t"
    "v_mfma_f32_16x16x32_bf16 v[228:231], a[184:187], a[0:3], v[228:231]\n\t" "ds_read_b64_tr_b16 a[176:177], %[vr16] offset:0\n\t"
    "v_mfma_f32_16x16x32_bf16 v[228:231], a[188:191], a[8:11], v[228:231]\n\t" "ds_read_b64_tr_b16 a[178:179], %[vr16] offset:2048\n\t" "global_load_dwordx2 a[66:67], %[vo], s[40:41] offset:1024 sc1\n\t"
    "v_mfma_f32_16x16x32_bf16 v[244:247], a[184:187], a[4:7], v[244:247]\n\t" "ds_read_b64_tr_b16 a[180:181], %[vr16] offset:8192\n\t"
    "v_mfma_f32_16x16x32_bf16 v[244:247], a[188:191], a[12:15], v[244:247]\n\t" "ds_read_b64_tr_b16 a[182:183], %[vr16] offset:10240\n\t" "s_waitcnt lgkmcnt(4)\n\t"
    "v_mfma_f32_16x16x32_bf16 v[232:235], a[192:195], a[0:3], v[232:235]\n\t" "ds_read_b64_tr_b16 a[184:185], %[vr17] offset:0\n\t" "global_load_dwordx2 a[68:69], %[vo], s[40:41] offset:512 sc1\n\t"
    "v_mfma_f32_16x16x32_bf16 v[232:235], a[196:199], a[8:11], v[232:235]\n\t" "ds_read_b64_tr_b16 a[186:187], %[vr17] offset:2048\n\t" "global_load_dwordx2 a[70:71], %[vo], s[40:41] offset:1536 sc1\n\t"
    "v_mfma_f32_16x16x32_bf16 v[248:251], a[192:195], a[4:7], v[248:251]\n\t" "ds_read_b64_tr_b16 a[188:189], %[vr17] offset:8192\n\t" "s_add_u32 m0, %[sl1], 24576\n\t"
    "v_mfma_f32_16x16x32_bf16 v[248:251], a[196:199], a[12:15], v[248:251]\n\t" "ds_read_b64_tr_b16 a[190:191], %[vr17] offset:10240\n\t" "global_load_lds_dwordx4 %[vb1], %[sb]\n\t"
    "v_mfma_f32_16x16x32_bf16 v[236:239], a[200:203], a[0:3], v[236:239]\n\t" "ds_read_b64_tr_b16 a[192:193], %[vr18] offset:0\n\t" "v_add_u32 %[vb1], 0x10000, %[vb1]\n\t"
    "v_mfma_f32_16x16x32_bf16 v[236:239], a[204:207], a[8:11], v[236:239]\n\t" "ds_read_b64_tr_b16 a[194:195], %[vr18] offset:2048\n\t"
    "v_mfma_f32_16x16x32_bf16 v[252:255], a[200:203], a[4:7], v[252:255]\n\t" "ds_read_b64_tr_b16 a[196:197], %[vr18] offset:8192\n\t"
    "v_mfma_f32_16x16x32_bf16 v[252:255], a[204:207], a[12:15], v[252:255]\n\t" "ds_read_b64_tr_b16 a[198:199], %[vr18] offset:10240\n\t" "s_waitcnt lgkmcnt(4)\n\t"
    "v_mfma_f32_16x16x32_bf16 v[96:99], a[176:179], a[0:3], v[96:99]\n\t" "ds_read_b64_tr_b16 a[200:201], %[vr19] offset:0\n\t" "global_load_dwordx4 a[72:75], %[vx], s[40:41] offset:2048 sc1\n\t"
    "v_mfma_f32_16x16x32_bf16 v[96:99], a[180:183], a[8:11], v[96:99]\n\t" "ds_read_b64_tr_b16 a[202:203], %[vr19] offset:2048\n\t"
    "v_mfma_f32_16x16x32_bf16 v[100:103], a[176:179], a[4:7], v[100:103]\n\t" "ds_read_b64_tr_b16 a[204:205], %[vr19] offset:8192\n\t"
    "v_mfma_f32_16x16x32_bf16 v[100:103], a[180:183], a[12:15], v[100:103]\n\t" "ds_read_b64_tr_b16 a[206:207], %[vr19] offset:10240\n\t"
    "global_load_dwordx4 a[76:79], %[vx], s[40:41] offset:3072 sc1\n\t" "v_mfma_f32_16x16x32_bf16 v[104:107], a[184:187], a[0:3], v[104:107]\n\t" "s_barrier\n\t"
    "ds_read_b64_tr_b16 a[176:177], %[vr4] offset:16384\n\t" "v_mfma_f32_16x16x32_bf16 v[104:107], a[188:191], a[8:11], v[104:107]\n\t" "ds_read_b64_tr_b16 a[178:179], %[vr4] offset:18432\n\t"
    "s_add_u32 m0, %[sl0], 32768\n\t" "v_mfma_f32_16x16x32_bf16 v[108:111], a[184:187], a[4:7], v[108:111]\n\t" "ds_read_b64_tr_b16 a[180:181], %[vr4] offset:24576\n\t"
    "v_mfma_f32_16x16x32_bf16 v[108:111], a[188:191], a[12:15], v[108:111]\n\t" "ds_read_b64_tr_b16 a[182:183], %[vr4] offset:26624\n\t" "s_waitcnt lgkmcnt(4)\n\t"
    "v_mfma_f32_16x16x32_bf16 v[112:115], a[192:195], a[0:3], v[112:115]\n\t" "ds_read_b64_tr_b16 a[184:185], %[vr5] offset:16384\n\t" "global_load_lds_dwordx4 %[vb0], %[sb]\n\t"
    "v_mfma_f32_16x16x32_bf16 v[112:115], a[196:199], a[8:11], v[112:115]\n\t" "ds_read_b64_tr_b16 a[186:187], %[vr5] offset:18432\n\t" "v_add_u32 %[vb0], 0x10000, %[vb0]\n\t"
    "v_mfma_f32_16x16x32_bf16 v[116:119], a[192:195], a[4:7], v[116:119]\n\t" "ds_read_b64_tr_b16 a[188:189], %[vr5] offset:24576\n\t" "s_add_u32 m0, %[sl1], 32768\n\t"
    "v_mfma_f32_16x16x32_bf16 v[116:119], a[196:199], a[12:15], v[116:119]\n\t" "ds_read_b64_tr_b16 a[190:191], %[vr5] offset:26624\n\t" "global_load_lds_dwordx4 %[vb1], %[sb]\n\t"
    "v_mfma_f32_16x16x32_bf16 v[120:123], a[200:203], a[0:3], v[120:123]\n\t" "ds_read_b64_tr_b16 a[192:193], %[vr6] offset:16384\n\t" "v_add_u32 %[vb1], 0x10000, %[vb1]\n\t"
    "v_mfma_f32_16x16x32_bf16 v[120:123], a[204:207], a[8:11], v[120:123]\n\t" "ds_read_b64_tr_b16 a[194:195], %[vr6] offset:18432\n\t" "s_add_u32 m0, %[sl0], 40960\n\t"
    "v_mfma_f32_16x16x32_bf16 v[124:127], a[200:203], a[4:7], v[124:127]\n\t" "ds_read_b64_tr_b16 a[196:197], %[vr6] offset:24576\n\t"
    "v_mfma_f32_16x16x32_bf16 v[124:127], a[204:207], a[12:15], v[124:127]\n\t" "ds_read_b64_tr_b16 a[198:199], %[vr6] offset:26624\n\t" "s_waitcnt lgkmcnt(4) vmcnt(31)\n\t"
    "v_mfma_f32_16x16x32_bf16 v[224:227], a[176:179], a[16:19], v[224:227]\n\t" "ds_read_b64_tr_b16 a[200:201], %[vr7] offset:16384\n\t" "global_load_lds_dwordx4 %[vb0], %[sb]\n\t"
    "v_mfma_f32_16x16x32_bf16 v[224:227], a[180:183], a[24:27], v[224:227]\n\t" "ds_read_b64_tr_b16 a[202:203], %[vr7] offset:18432\n\t" "v_add_u32 %[vb0], 0x10000, %[vb0]\n\t"
    "v_mfma_f32_16x16x32_bf16 v[240:243], a[176:179], a[20:23], v[240:243]\n\t" "ds_read_b64_tr_b16 a[204:205], %[vr7] offset:24576\n\t" "s_add_u32 s40, s40, 8192\n\t" "s_addc_u32 s41, s41, 0\n\t"
    "v_mfma_f32_16x16x32_bf16 v[240:243], a[180:183], a[28:31], v[240:243]\n\t" "ds_read_b64_tr_b16 a[206:207], %[vr7] offset:26624\n\t" "global_load_dwordx4 a[0:3], %[vx], s[40:41] offset:-4096 sc1\n\t"
    "v_mfma_f32_16x16x32_bf16 v[228:231], a[184:187], a[16:19], v[228:231]\n\t" "ds_read_b64_tr_b16 a[176:177], %[vr16] offset:16384\n\t"
    "v_mfma_f32_16x16x32_bf16 v[228:231], a[188:191], a[24:27], v[228:231]\n\t" "ds_read_b64_tr_b16 a[178:179], %[vr16] offset:18432\n\t"
    "v_mfma_f32_16x16x32_bf16 v[244:247], a[184:187], a[20:23], v[244:247]\n\t" "ds_read_b64_tr_b16 a[180:181], %[vr16] offset:24576\n\t"
    "v_mfma_f32_16x16x32_bf16 v[244:247], a[188:191], a[28:31], v[244:247]\n\t" "ds_read_b64_tr_b16 a[182:183], %[vr16] offset:26624\n\t" "s_waitcnt lgkmcnt(4)\n\t"
    "v_mfma_f32_16x16x32_bf16 v[232:235], a[192:195], a[16:19], v[232:235]\n\t" "ds_read_b64_tr_b16 a[184:185], %[vr17] offset:16384\n\t" "global_load_dwordx4 a[4:7], %[vx], s[40:41] offset:-3072 sc1\n\t"
    "v_mfma_f32_16x16x32_bf16 v[232:235], a[196:199], a[24:27], v[232:235]\n\t" "ds_read_b64_tr_b16 a[186:187], %[vr17] offset:18432\n\t" "s_add_u32 m0, %[sl1], 40960\n\t"
    "v_mfma_f32_16x16x32_bf16 v[248:251], a[192:195], a[20:23], v[248:251]\n\t" "ds_read_b64_tr_b16 a[188:189], %[vr17] offset:24576\n\t"
    "v_mfma_f32_16x16x32_bf16 v[248:251], a[196:199], a[28:31], v[248:251]\n\t" "ds_read_b64_tr_b16 a[190:191], %[vr17] offset:26624\n\t" "global_load_lds_dwordx4 %[vb1], %[sb]\n\t"
    "v_mfma_f32_16x16x32_bf16 v[236:239], a[200:203], a[16:19], v[236:239]\n\t" "ds_read_b64_tr_b16 a[192:193], %[vr18] offset:16384\n\t" "v_add_u32 %[vb1], 0x10000, %[vb1]\n\t"
    "v_mfma_f32_16x16x32_bf16 v[236:239], a[204:207], a[24:27], v[236:239]\n\t" "ds_read_b64_tr_b16 a[194:195], %[vr18] offset:18432\n\t"
    "v_mfma_f32_16x16x32_bf16 v[252:255], a[200:203], a[20:23], v[252:255]\n\t" "ds_read_b64_tr_b16 a[196:197], %[vr18] offset:24576\n\t"
    "v_mfma_f32_16x16x32_bf16 v[252:255], a[204:207], a[28:31], v[252:255]\n\t" "ds_read_b64_tr_b16 a[198:199], %[vr18] offset:26624\n\t" "s_waitcnt lgkmcnt(4)\n\t"
    "v_mfma_f32_16x16x32_bf16 v[96:99], a[176:179], a[16:19], v[96:99]\n\t" "ds_read_b64_tr_b16 a[200:201], %[vr19] offset:16384\n\t" "global_load_dwordx4 a[8:11], %[vx], s[40:41] offset:-2048 sc1\n\t"
    "v_mfma_f32_16x16x32_bf16 v[96:99], a[180:183], a[24:27], v[96:99]\n\t" "ds_read_b64_tr_b16 a[202:203], %[vr19] offset:18432\n\t"
    "v_mfma_f32_16x16x32_bf16 v[100:103], a[176:179], a[20:23], v[100:103]\n\t" "ds_read_b64_tr_b16 a[204:205], %[vr19] offset:24576\n\t"
    "v_mfma_f32_16x16x32_bf16 v[100:103], a[180:183], a[28:31], v[100:103]\n\t" "ds_read_b64_tr_b16 a[206:207], %[vr19] offset:26624\n\t"
    "global_load_dwordx4 a[12:15], %[vx], s[40:41] offset:-1024 sc1\n\t" "v_mfma_f32_16x16x32_bf16 v[104:107], a[184:187], a[16:19], v[104:107]\n\t" "s_barrier\n\t"
    "ds_read_b64_tr_b16 a[176:177], %[vr4] offset:32768\n\t" "v_mfma_f32_16x16x32_bf16 v[104:107], a[188:191], a[24:27], v[104:107]\n\t" "ds_read_b64_tr_b16 a[178:179], %[vr4] offset:34816\n\t"
    "s_add_u32 m0, %[sl0], 139264\n\t" "v_mfma_f32_16x16x32_bf16 v[108:111], a[184:187], a[20:23], v[108:111]\n\t" "ds_read_b64_tr_b16 a[180:181], %[vr4] offset:40960\n\t"
    "v_mfma_f32_16x16x32_bf16 v[108:111], a[188:191], a[28:31], v[108:111]\n\t" "ds_read_b64_tr_b16 a[182:183], %[vr4] offset:43008\n\t" "s_waitcnt lgkmcnt(4)\n\t"
    "v_mfma_f32_16x16x32_bf16 v[112:115], a[192:195], a[16:19], v[112:115]\n\t" "ds_read_b64_tr_b16 a[184:185], %[vr5] offset:32768\n\t" "global_load_lds_dwordx4 %[vb0], %[sb]\n\t"
    "v_mfma_f32_16x16x32_bf16 v[112:115], a[196:199], a[24:27], v[112:115]\n\t" "ds_read_b64_tr_b16 a[186:187], %[vr5] offset:34816\n\t" "v_add_u32 %[vb0], 0x10000, %[vb0]\n\t"
    "v_mfma_f32_16x16x32_bf16 v[116:119], a[192:195], a[20:23], v[116:119]\n\t" "ds_read_b64_tr_b16 a[188:189], %[vr5] offset:40960\n\t" "s_add_u32 m0, %[sl1], 139264\n\t"
    "v_mfma_f32_16x16x32_bf16 v[116:119], a[196:199], a[28:31], v[116:119]\n\t" "ds_read_b64_tr_b16 a[190:191], %[vr5] offset:43008\n\t" "global_load_lds_dwordx4 %[vb1], %[sb]\n\t"
    "v_mfma_f32_16x16x32_bf16 v[120:123], a[200:203], a[16:19], v[120:123]\n\t" "ds_read_b64_tr_b16 a[192:193], %[vr6] offset:32768\n\t" "v_add_u32 %[vb1], 0x10000, %[vb1]\n\t"
    "v_mfma_f32_16x16x32_bf16 v[120:123], a[204:207], a[24:27], v[120:123]\n\t" "ds_read_b64_tr_b16 a[194:195], %[vr6] offset:34816\n\t" "s_add_u32 m0, %[sl0], 147456\n\t"
    "v_mfma_f32_16x16x32_bf16 v[124:127], a[200:203], a[20:23], v[124:127]\n\t" "ds_read_b64_tr_b16 a[196:197], %[vr6] offset:40960\n\t"
    "v_mfma_f32_16x16x32_bf16 v[124:127], a[204:207], a[28:31], v[124:127]\n\t" "ds_read_b64_tr_b16 a[198:199], %[vr6] offset:43008\n\t" "s_waitcnt lgkmcnt(4) vmcnt(29)\n\t"
    "v_mfma_f32_16x16x32_bf16 v[224:227], a[176:179], a[32:35], v[224:227]\n\t" "ds_read_b64_tr_b16 a[200:201], %[vr7] offset:32768\n\t" "global_load_lds_dwordx4 %[vb0], %[sb]\n\t"
    "v_mfma_f32_16x16x32_bf16 v[224:227], a[180:183], a[40:43], v[224:227]\n\t" "ds_read_b64_tr_b16 a[202:203], %[vr7] offset:34816\n\t" "v_add_u32 %[vb0], 0x10000, %[vb0]\n\t"
    "v_mfma_f32_16x16x32_bf16 v[240:243], a[176:179], a[36:39], v[240:243]\n\t" "ds_read_b64_tr_b16 a[204:205], %[vr7] offset:40960\n\t"
    "v_mfma_f32_16x16x32_bf16 v[240:243], a[180:183], a[44:47], v[240:243]\n\t" "ds_read_b64_tr_b16 a[206:207], %[vr7] offset:43008\n\t" "global_load_dwordx4 a[16:19], %[vx], s[40:41] offset:0 sc1\n\t"
    "v_mfma_f32_16x16x32_bf16 v[228:231], a[184:187], a[32:35], v[228:231]\n\t" "ds_read_b64_tr_b16 a[176:177], %[vr16] offset:32768\n\t"
    "v_mfma_f32_16x16x32_bf16 v[228:231], a[188:191], a[40:43], v[228:231]\n\t" "ds_read_b64_tr_b16 a[178:179], %[vr16] offset:34816\n\t"
    "v_mfma_f32_16x16x32_bf16 v[244:247], a[184:187], a[36:39], v[244:247]\n\t" "ds_read_b64_tr_b16 a[180:181], %[vr16] offset:40960\n\t"
    "v_mfma_f32_16x16x32_bf16 v[244:247], a[188:191], a[44:47], v[244:247]\n\t" "ds_read_b64_tr_b16 a[182:183], %[vr16] offset:43008\n\t" "s_waitcnt lgkmcnt(4)\n\t"
    "v_mfma_f32_16x16x32_bf16 v[232:235], a[192:195], a[32:35], v[232:235]\n\t" "ds_read_b64_tr_b16 a[184:185], %[vr17] offset:32768\n\t"
    "global_load_dwordx4 a[20:23], %[vx], s[40:41] offset:1024 sc1\n\t" "v_mfma_f32_16x16x32_bf16 v[232:235], a[196:199], a[40:43], v[232:235]\n\t"
    "ds_read_b64_tr_b16 a[186:187], %[vr17] offset:34816\n\t" "v_mfma_f32_16x16x32_bf16 v[248:251], a[192:195], a[36:39], v[248:251]\n\t" "ds_read_b64_tr_b16 a[188:189], %[vr17] offset:40960\n\t"
    "v_mfma_f32_16x16x32_bf16 v[248:251], a[196:199], a[44:47], v[248:251]\n\t" "ds_read_b64_tr_b16 a[190:191], %[vr17] offset:43008\n\t"
    "global_load_dwordx4 a[24:27], %[vx], s[40:41] offset:2048 sc1\n\t" "v_mfma_f32_16x16x32_bf16 v[236:239], a[200:203], a[32:35], v[236:239]\n\t"
    "ds_read_b64_tr_b16 a[192:193], %[vr18] offset:32768\n\t" "v_mfma_f32_16x16x32_bf16 v[236:239], a[204:207], a[40:43], v[236:239]\n\t" "ds_read_b64_tr_b16 a[194:195], %[vr18] offset:34816\n\t"
    "v_mfma_f32_16x16x32_bf16 v[252:255], a[200:203], a[36:39], v[252:255]\n\t" "ds_read_b64_tr_b16 a[196:197], %[vr18] offset:40960\n\t"
    "v_mfma_f32_16x16x32_bf16 v[252:255], a[204:207], a[44:47], v[252:255]\n\t" "ds_read_b64_tr_b16 a[198:199], %[vr18] offset:43008\n\t" "s_waitcnt lgkmcnt(4)\n\t"
    "v_mfma_f32_16x16x32_bf16 v[96:99], a[176:179], a[32:35], v[96:99]\n\t" "ds_read_b64_tr_b16 a[200:201], %[vr19] offset:32768\n\t" "global_load_dwordx4 a[28:31], %[vx], s[40:41] offset:3072 sc1\n\t"
    "v_mfma_f32_16x16x32_bf16 v[96:99], a[180:183], a[40:43], v[96:99]\n\t" "ds_read_b64_tr_b16 a[202:203], %[vr19] offset:34816\n\t" "s_add_u32 m0, %[sl0], 155648\n\t"
    "v_mfma_f32_16x16x32_bf16 v[100:103], a[176:179], a[36:39], v[100:103]\n\t" "ds_read_b64_tr_b16 a[204:205], %[vr19] offset:40960\n\t"
    "v_mfma_f32_16x16x32_bf16 v[100:103], a[180:183], a[44:47], v[100:103]\n\t" "ds_read_b64_tr_b16 a[206:207], %[vr19] offset:43008\n\t" "global_load_lds_dwordx4 %[vb0], %[sb]\n\t"
    "v_mfma_f32_16x16x32_bf16 v[104:107], a[184:187], a[32:35], v[104:107]\n\t" "s_barrier\n\t" "ds_read_b64_tr_b16 a[176:177], %[vr4] offset:49152\n\t"
    "v_mfma_f32_16x16x32_bf16 v[104:107], a[188:191], a[40:43], v[104:107]\n\t" "ds_read_b64_tr_b16 a[178:179], %[vr4] offset:51200\n\t" "v_add_u32 %[vb0], 0x10000, %[vb0]\n\t"
    "v_mfma_f32_16x16x32_bf16 v[108:111], a[184:187], a[36:39], v[108:111]\n\t" "ds_read_b64_tr_b16 a[180:181], %[vr4] offset:57344\n\t" "s_add_u32 s40, s40, 8192\n\t" "s_addc_u32 s41, s41, 0\n\t"
    "v_mfma_f32_16x16x32_bf16 v[108:111], a[188:191], a[44:47], v[108:111]\n\t" "ds_read_b64_tr_b16 a[182:183], %[vr4] offset:59392\n\t" "s_waitcnt lgkmcnt(4)\n\t"
    "v_mfma_f32_16x16x32_bf16 v[112:115], a[192:195], a[32:35], v[112:115]\n\t" "ds_read_b64_tr_b16 a[184:185], %[vr5] offset:49152\n\t"
    "global_load_dwordx4 a[80:83], %[vx], s[40:41] offset:-4096 sc1\n\t" "v_mfma_f32_16x16x32_bf16 v[112:115], a[196:199], a[40:43], v[112:115]\n\t"
    "ds_read_b64_tr_b16 a[186:187], %[vr5] offset:51200\n\t" "v_mfma_f32_16x16x32_bf16 v[116:119], a[192:195], a[36:39], v[116:119]\n\t" "ds_read_b64_tr_b16 a[188:189], %[vr5] offset:57344\n\t"
    "v_mfma_f32_16x16x32_bf16 v[116:119], a[196:199], a[44:47], v[116:119]\n\t" "ds_read_b64_tr_b16 a[190:191], %[vr5] offset:59392\n\t"
    "global_load_dwordx4 a[84:87], %[vx], s[40:41] offset:-3072 sc1\n\t" "v_mfma_f32_16x16x32_bf16 v[120:123], a[200:203], a[32:35], v[120:123]\n\t"
    "ds_read_b64_tr_b16 a[192:193], %[vr6] offset:49152\n\t" "v_mfma_f32_16x16x32_bf16 v[120:123], a[204:207], a[40:43], v[120:123]\n\t" "ds_read_b64_tr_b16 a[194:195], %[vr6] offset:51200\n\t"
    "s_add_u32 m0, %[sl0], 0\n\t" "v_mfma_f32_16x16x32_bf16 v[124:127], a[200:203], a[36:39], v[124:127]\n\t" "ds_read_b64_tr_b16 a[196:197], %[vr6] offset:57344\n\t"
    "v_mfma_f32_16x16x32_bf16 v[124:127], a[204:207], a[44:47], v[124:127]\n\t" "ds_read_b64_tr_b16 a[198:199], %[vr6] offset:59392\n\t" "s_waitcnt lgkmcnt(4) vmcnt(27)\n\t"
    "v_mfma_f32_16x16x32_bf16 v[224:227], a[176:179], a[48:51], v[224:227]\n\t" "ds_read_b64_tr_b16 a[200:201], %[vr7] offset:49152\n\t" "global_load_lds_dwordx4 %[vb0], %[sb]\n\t"
    "v_mfma_f32_16x16x32_bf16 v[224:227], a[180:183], a[56:59], v[224:227]\n\t" "ds_read_b64_tr_b16 a[202:203], %[vr7] offset:51200\n\t" "v_add_u32 %[vb0], 0x10000, %[vb0]\n\t"
    "v_mfma_f32_16x16x32_bf16 v[240:243], a[176:179], a[52:55], v[240:243]\n\t" "ds_read_b64_tr_b16 a[204:205], %[vr7] offset:57344\n\t"
    "v_mfma_f32_16x16x32_bf16 v[240:243], a[180:183], a[60:63], v[240:243]\n\t" "ds_read_b64_tr_b16 a[206:207], %[vr7] offset:59392\n\t"
    "global_load_dwordx2 a[88:89], %[vo], s[40:41] offset:-2048 sc1\n\t" "v_mfma_f32_16x16x32_bf16 v[228:231], a[184:187], a[48:51], v[228:231]\n\t"
    "ds_read_b64_tr_b16 a[176:177], %[vr16] offset:49152\n\t" "v_mfma_f32_16x16x32_bf16 v[228:231], a[188:191], a[56:59], v[228:231]\n\t" "ds_read_b64_tr_b16 a[178:179], %[vr16] offset:51200\n\t"
    "global_load_dwordx2 a[90:91], %[vo], s[40:41] offset:-1024 sc1\n\t" "v_mfma_f32_16x16x32_bf16 v[244:247], a[184:187], a[52:55], v[244:247]\n\t"
    "ds_read_b64_tr_b16 a[180:181], %[vr16] offset:57344\n\t" "v_mfma_f32_16x16x32_bf16 v[244:247], a[188:191], a[60:63], v[244:247]\n\t" "ds_read_b64_tr_b16 a[182:183], %[vr16] offset:59392\n\t"
    "s_waitcnt lgkmcnt(4)\n\t" "v_mfma_f32_16x16x32_bf16 v[232:235], a[192:195], a[48:51], v[232:235]\n\t" "ds_read_b64_tr_b16 a[184:185], %[vr17] offset:49152\n\t"
    "global_load_dwordx2 a[92:93], %[vo], s[40:41] offset:-1536 sc1\n\t" "v_mfma_f32_16x16x32_bf16 v[232:235], a[196:199], a[56:59], v[232:235]\n\t"
    "ds_read_b64_tr_b16 a[186:187], %[vr17] offset:51200\n\t" "global_load_dwordx2 a[94:95], %[vo], s[40:41] offset:-512 sc1\n\t"
    "v_mfma_f32_16x16x32_bf16 v[248:251], a[192:195], a[52:55], v[248:251]\n\t" "ds_read_b64_tr_b16 a[188:189], %[vr17] offset:57344\n\t" "s_add_u32 m0, %[sl0], 8192\n\t"
    "v_mfma_f32_16x16x32_bf16 v[248:251], a[196:199], a[60:63], v[248:251]\n\t" "ds_read_b64_tr_b16 a[190:191], %[vr17] offset:59392\n\t" "global_load_lds_dwordx4 %[vb0], %[sb]\n\t"
    "v_mfma_f32_16x16x32_bf16 v[236:239], a[200:203], a[48:51], v[236:239]\n\t" "ds_read_b64_tr_b16 a[192:193], %[vr18] offset:49152\n\t" "v_add_u32 %[vb0], 0x10000, %[vb0]\n\t"
    "v_mfma_f32_16x16x32_bf16 v[236:239], a[204:207], a[56:59], v[236:239]\n\t" "ds_read_b64_tr_b16 a[194:195], %[vr18] offset:51200\n\t"
    "v_mfma_f32_16x16x32_bf16 v[252:255], a[200:203], a[52:55], v[252:255]\n\t" "ds_read_b64_tr_b16 a[196:197], %[vr18] offset:57344\n\t"
    "v_mfma_f32_16x16x32_bf16 v[252:255], a[204:207], a[60:63], v[252:255]\n\t" "ds_read_b64_tr_b16 a[198:199], %[vr18] offset:59392\n\t" "s_waitcnt lgkmcnt(4)\n\t"
    "v_mfma_f32_16x16x32_bf16 v[96:99], a[176:179], a[48:51], v[96:99]\n\t" "ds_read_b64_tr_b16 a[200:201], %[vr19] offset:49152\n\t" "global_load_dwordx4 a[96:99], %[vx], s[40:41] offset:0 sc1\n\t"
    "v_mfma_f32_16x16x32_bf16 v[96:99], a[180:183], a[56:59], v[96:99]\n\t" "ds_read_b64_tr_b16 a[202:203], %[vr19] offset:51200\n\t"
    "v_mfma_f32_16x16x32_bf16 v[100:103], a[176:179], a[52:55], v[100:103]\n\t" "ds_read_b64_tr_b16 a[204:205], %[vr19] offset:57344\n\t"
    "v_mfma_f32_16x16x32_bf16 v[100:103], a[180:183], a[60:63], v[100:103]\n\t" "ds_read_b64_tr_b16 a[206:207], %[vr19] offset:59392\n\t"
    "global_load_dwordx4 a[100:103], %[vx], s[40:41] offset:1024 sc1\n\t" "v_mfma_f32_16x16x32_bf16 v[104:107], a[184:187], a[48:51], v[104:107]\n\t" "s_waitcnt vmcnt(34)\n\t" "s_barrier\n\t"
    "ds_read_b64_tr_b16 a[176:177], %[vr8] offset:0\n\t" "v_mfma_f32_16x16x32_bf16 v[104:107], a[188:191], a[56:59], v[104:107]\n\t" "ds_read_b64_tr_b16 a[178:179], %[vr8] offset:2048\n\t"
    "s_add_u32 m0, %[sl0], 49152\n\t" "v_mfma_f32_16x16x32_bf16 v[108:111], a[184:187], a[52:55], v[108:111]\n\t" "ds_read_b64_tr_b16 a[180:181], %[vr0] offset:16384\n\t"
    "v_mfma_f32_16x16x32_bf16 v[108:111], a[188:191], a[60:63], v[108:111]\n\t" "ds_read_b64_tr_b16 a[182:183], %[vr0] offset:18432\n\t" "s_waitcnt lgkmcnt(4)\n\t"
    "v_mfma_f32_16x16x32_bf16 v[112:115], a[192:195], a[48:51], v[112:115]\n\t" "ds_read_b64_tr_b16 a[184:185], %[vr9] offset:0\n\t" "global_load_lds_dwordx4 %[vb0], %[sb]\n\t"
    "v_mfma_f32_16x16x32_bf16 v[112:115], a[196:199], a[56:59], v[112:115]\n\t" "ds_read_b64_tr_b16 a[186:187], %[vr9] offset:2048\n\t" "v_add_u32 %[vb0], 0x10000, %[vb0]\n\t"
    "v_mfma_f32_16x16x32_bf16 v[116:119], a[192:195], a[52:55], v[116:119]\n\t" "ds_read_b64_tr_b16 a[188:189], %[vr1] offset:16384\n\t"
    "v_mfma_f32_16x16x32_bf16 v[116:119], a[196:199], a[60:63], v[116:119]\n\t" "ds_read_b64_tr_b16 a[190:191], %[vr1] offset:18432\n\t"
    "global_load_dwordx4 a[104:107], %[vx], s[40:41] offset:2048 sc1\n\t" "v_mfma_f32_16x16x32_bf16 v[120:123], a[200:203], a[48:51], v[120:123]\n\t" "ds_read_b64_tr_b16 a[192:193], %[vr10] offset:0\n\t"
    "v_mfma_f32_16x16x32_bf16 v[120:123], a[204:207], a[56:59], v[120:123]\n\t" "ds_read_b64_tr_b16 a[194:195], %[vr10] offset:2048\n\t"
    "v_mfma_f32_16x16x32_bf16 v[124:127], a[200:203], a[52:55], v[124:127]\n\t" "ds_read_b64_tr_b16 a[196:197], %[vr2] offset:16384\n\t"
    "v_mfma_f32_16x16x32_bf16 v[124:127], a[204:207], a[60:63], v[124:127]\n\t" "ds_read_b64_tr_b16 a[198:199], %[vr2] offset:18432\n\t" "s_waitcnt lgkmcnt(4) vmcnt(28)\n\t"
    "v_mfma_f32_16x16x32_bf16 v[224:227], a[176:179], a[64:67], v[224:227]\n\t" "ds_read_b64_tr_b16 a[200:201], %[vr11] offset:0\n\t" "global_load_dwordx4 a[108:111], %[vx], s[40:41] offset:3072 sc1\n\t"
    "v_mfma_f32_16x16x32_bf16 v[224:227], a[180:183], a[72:75], v[224:227]\n\t" "ds_read_b64_tr_b16 a[202:203], %[vr11] offset:2048\n\t" "s_add_u32 m0, %[sl0], 57344\n\t"
    "v_mfma_f32_16x16x32_bf16 v[240:243], a[176:179], a[68:71], v[240:243]\n\t" "ds_read_b64_tr_b16 a[204:205], %[vr3] offset:16384\n\t"
    "v_mfma_f32_16x16x32_bf16 v[240:243], a[180:183], a[76:79], v[240:243]\n\t" "ds_read_b64_tr_b16 a[206:207], %[vr3] offset:18432\n\t" "global_load_lds_dwordx4 %[vb0], %[sb]\n\t"
    "v_mfma_f32_16x16x32_bf16 v[228:231], a[184:187], a[64:67], v[228:231]\n\t" "ds_read_b64_tr_b16 a[176:177], %[vr20] offset:0\n\t" "v_add_u32 %[vb0], 0x10000, %[vb0]\n\t"
    "v_mfma_f32_16x16x32_bf16 v[228:231], a[188:191], a[72:75], v[228:231]\n\t" "ds_read_b64_tr_b16 a[178:179], %[vr20] offset:2048\n\t" "s_add_u32 s40, s40, 8192\n\t" "s_addc_u32 s41, s41, 0\n\t"
    "v_mfma_f32_16x16x32_bf16 v[244:247], a[184:187], a[68:71], v[244:247]\n\t" "ds_read_b64_tr_b16 a[180:181], %[vr12] offset:16384\n\t"
    "v_mfma_f32_16x16x32_bf16 v[244:247], a[188:191], a[76:79], v[244:247]\n\t" "ds_read_b64_tr_b16 a[182:183], %[vr12] offset:18432\n\t" "s_waitcnt lgkmcnt(4)\n\t"
    "v_mfma_f32_16x16x32_bf16 v[232:235], a[192:195], a[64:67], v[232:235]\n\t" "ds_read_b64_tr_b16 a[184:185], %[vr21] offset:0\n\t" "global_load_dwordx4 a[112:115], %[vx], s[40:41] offset:-4096 sc1\n\t"
    "v_mfma_f32_16x16x32_bf16 v[232:235], a[196:199], a[72:75], v[232:235]\n\t" "ds_read_b64_tr_b16 a[186:187], %[vr21] offset:2048\n\t"
    "v_mfma_f32_16x16x32_bf16 v[248:251], a[192:195], a[68:71], v[248:251]\n\t" "ds_read_b64_tr_b16 a[188:189], %[vr13] offset:16384\n\t"
    "v_mfma_f32_16x16x32_bf16 v[248:251], a[196:199], a[76:79], v[248:251]\n\t" "ds_read_b64_tr_b16 a[190:191], %[vr13] offset:18432\n\t"
    "global_load_dwordx4 a[116:119], %[vx], s[40:41] offset:-3072 sc1\n\t" "v_mfma_f32_16x16x32_bf16 v[236:239], a[200:203], a[64:67], v[236:239]\n\t" "ds_read_b64_tr_b16 a[192:193], %[vr22] offset:0\n\t"
    "v_mfma_f32_16x16x32_bf16 v[236:239], a[204:207], a[72:75], v[236:239]\n\t" "ds_read_b64_tr_b16 a[194:195], %[vr22] offset:2048\n\t" "s_add_u32 m0, %[sl0], 65536\n\t"
    "v_mfma_f32_16x16x32_bf16 v[252:255], a[200:203], a[68:71], v[252:255]\n\t" "ds_read_b64_tr_b16 a[196:197], %[vr14] offset:16384\n\t"
    "v_mfma_f32_16x16x32_bf16 v[252:255], a[204:207], a[76:79], v[252:255]\n\t" "ds_read_b64_tr_b16 a[198:199], %[vr14] offset:18432\n\t" "s_waitcnt lgkmcnt(4)\n\t"
    "v_mfma_f32_16x16x32_bf16 v[96:99], a[176:179], a[64:67], v[96:99]\n\t" "ds_read_b64_tr_b16 a[200:201], %[vr23] offset:0\n\t" "global_load_lds_dwordx4 %[vb0], %[sb]\n\t"
    "v_mfma_f32_16x16x32_bf16 v[96:99], a[180:183], a[72:75], v[96:99]\n\t" "ds_read_b64_tr_b16 a[202:203], %[vr23] offset:2048\n\t" "v_add_u32 %[vb0], 0x10000, %[vb0]\n\t"
    "v_mfma_f32_16x16x32_bf16 v[100:103], a[176:179], a[68:71], v[100:103]\n\t" "ds_read_b64_tr_b16 a[204:205], %[vr15] offset:16384\n\t"
    "v_mfma_f32_16x16x32_bf16 v[100:103], a[180:183], a[76:79], v[100:103]\n\t" "ds_read_b64_tr_b16 a[206:207], %[vr15] offset:18432\n\t"
    "global_load_dwordx4 a[120:123], %[vx], s[40:41] offset:-2048 sc1\n\t" "v_mfma_f32_16x16x32_bf16 v[104:107], a[184:187], a[64:67], v[104:107]\n\t" "s_waitcnt vmcnt(32)\n\t" "s_barrier\n\t"
    "ds_read_b64_tr_b16 a[176:177], %[vr0] offset:24576\n\t" "v_mfma_f32_16x16x32_bf16 v[104:107], a[188:191], a[72:75], v[104:107]\n\t" "ds_read_b64_tr_b16 a[178:179], %[vr0] offset:26624\n\t"
    "v_mfma_f32_16x16x32_bf16 v[108:111], a[184:187], a[68:71], v[108:111]\n\t" "ds_read_b64_tr_b16 a[180:181], %[vr0] offset:32768\n\t"
    "v_mfma_f32_16x16x32_bf16 v[108:111], a[188:191], a[76:79], v[108:111]\n\t" "ds_read_b64_tr_b16 a[182:183], %[vr0] offset:34816\n\t" "s_waitcnt lgkmcnt(4)\n\t"
    "v_mfma_f32_16x16x32_bf16 v[112:115], a[192:195], a[64:67], v[112:115]\n\t" "ds_read_b64_tr_b16 a[184:185], %[vr1] offset:24576\n\t"
    "global_load_dwordx4 a[124:127], %[vx], s[40:41] offset:-1024 sc1\n\t" "v_mfma_f32_16x16x32_bf16 v[112:115], a[196:199], a[72:75], v[112:115]\n\t"
    "ds_read_b64_tr_b16 a[186:187], %[vr1] offset:26624\n\t" "s_add_u32 m0, %[sl0], 73728\n\t" "v_mfma_f32_16x16x32_bf16 v[116:119], a[192:195], a[68:71], v[116:119]\n\t"
    "ds_read_b64_tr_b16 a[188:189], %[vr1] offset:32768\n\t" "v_mfma_f32_16x16x32_bf16 v[116:119], a[196:199], a[76:79], v[116:119]\n\t" "ds_read_b64_tr_b16 a[190:191], %[vr1] offset:34816\n\t"
    "global_load_lds_dwordx4 %[vb0], %[sb]\n\t" "v_mfma_f32_16x16x32_bf16 v[120:123], a[200:203], a[64:67], v[120:123]\n\t" "ds_read_b64_tr_b16 a[192:193], %[vr2] offset:24576\n\t"
    "v_add_u32 %[vb0], 0x10000, %[vb0]\n\t" "v_mfma_f32_16x16x32_bf16 v[120:123], a[204:207], a[72:75], v[120:123]\n\t" "ds_read_b64_tr_b16 a[194:195], %[vr2] offset:26624\n\t"
    "v_mfma_f32_16x16x32_bf16 v[124:127], a[200:203], a[68:71], v[124:127]\n\t" "ds_read_b64_tr_b16 a[196:197], %[vr2] offset:32768\n\t"
    "v_mfma_f32_16x16x32_bf16 v[124:127], a[204:207], a[76:79], v[124:127]\n\t" "ds_read_b64_tr_b16 a[198:199], %[vr2] offset:34816\n\t" "s_waitcnt lgkmcnt(4) vmcnt(28)\n\t"
    "v_mfma_f32_16x16x32_bf16 v[224:227], a[176:179], a[0:3], v[224:227]\n\t" "ds_read_b64_tr_b16 a[200:201], %[vr3] offset:24576\n\t" "global_load_dwordx4 a[128:131], %[vx], s[40:41] offset:0 sc1\n\t"
    "v_mfma_f32_16x16x32_bf16 v[224:227], a[180:183], a[8:11], v[224:227]\n\t" "ds_read_b64_tr_b16 a[202:203], %[vr3] offset:26624\n\t"
    "v_mfma_f32_16x16x32_bf16 v[240:243], a[176:179], a[4:7], v[240:243]\n\t" "ds_read_b64_tr_b16 a[204:205], %[vr3] offset:32768\n\t"
    "v_mfma_f32_16x16x32_bf16 v[240:243], a[180:183], a[12:15], v[240:243]\n\t" "ds_read_b64_tr_b16 a[206:207], %[vr3] offset:34816\n\t"
    "global_load_dwordx4 a[132:135], %[vx], s[40:41] offset:1024 sc1\n\t" "v_mfma_f32_16x16x32_bf16 v[228:231], a[184:187], a[0:3], v[228:231]\n\t"
    "ds_read_b64_tr_b16 a[176:177], %[vr12] offset:24576\n\t" "v_mfma_f32_16x16x32_bf16 v[228:231], a[188:191], a[8:11], v[228:231]\n\t" "ds_read_b64_tr_b16 a[178:179], %[vr12] offset:26624\n\t"
    "s_add_u32 m0, %[sl0], 81920\n\t" "v_mfma_f32_16x16x32_bf16 v[244:247], a[184:187], a[4:7], v[244:247]\n\t" "ds_read_b64_tr_b16 a[180:181], %[vr12] offset:32768\n\t"
    "v_mfma_f32_16x16x32_bf16 v[244:247], a[188:191], a[12:15], v[244:247]\n\t" "ds_read_b64_tr_b16 a[182:183], %[vr12] offset:34816\n\t" "s_waitcnt lgkmcnt(4)\n\t"
    "v_mfma_f32_16x16x32_bf16 v[232:235], a[192:195], a[0:3], v[232:235]\n\t" "ds_read_b64_tr_b16 a[184:185], %[vr13] offset:24576\n\t" "global_load_lds_dwordx4 %[vb0], %[sb]\n\t"
    "v_mfma_f32_16x16x32_bf16 v[232:235], a[196:199], a[8:11], v[232:235]\n\t" "ds_read_b64_tr_b16 a[186:187], %[vr13] offset:26624\n\t" "v_add_u32 %[vb0], 0x10000, %[vb0]\n\t"
    "v_mfma_f32_16x16x32_bf16 v[248:251], a[192:195], a[4:7], v[248:251]\n\t" "ds_read_b64_tr_b16 a[188:189], %[vr13] offset:32768\n\t"
    "v_mfma_f32_16x16x32_bf16 v[248:251], a[196:199], a[12:15], v[248:251]\n\t" "ds_read_b64_tr_b16 a[190:191], %[vr13] offset:34816\n\t"
    "global_load_dwordx4 a[136:139], %[vx], s[40:41] offset:2048 sc1\n\t" "v_mfma_f32_16x16x32_bf16 v[236:239], a[200:203], a[0:3], v[236:239]\n\t"
    "ds_read_b64_tr_b16 a[192:193], %[vr14] offset:24576\n\t" "v_mfma_f32_16x16x32_bf16 v[236:239], a[204:207], a[8:11], v[236:239]\n\t" "ds_read_b64_tr_b16 a[194:195], %[vr14] offset:26624\n\t"
    "v_mfma_f32_16x16x32_bf16 v[252:255], a[200:203], a[4:7], v[252:255]\n\t" "ds_read_b64_tr_b16 a[196:197], %[vr14] offset:32768\n\t"
    "v_mfma_f32_16x16x32_bf16 v[252:255], a[204:207], a[12:15], v[252:255]\n\t" "ds_read_b64_tr_b16 a[198:199], %[vr14] offset:34816\n\t" "s_waitcnt lgkmcnt(4)\n\t"
    "v_mfma_f32_16x16x32_bf16 v[96:99], a[176:179], a[0:3], v[96:99]\n\t" "ds_read_b64_tr_b16 a[200:201], %[vr15] offset:24576\n\t" "global_load_dwordx4 a[140:143], %[vx], s[40:41] offset:3072 sc1\n\t"
    "v_mfma_f32_16x16x32_bf16 v[96:99], a[180:183], a[8:11], v[96:99]\n\t" "ds_read_b64_tr_b16 a[202:203], %[vr15] offset:26624\n\t" "s_add_u32 m0, %[sl0], 90112\n\t"
    "v_mfma_f32_16x16x32_bf16 v[100:103], a[176:179], a[4:7], v[100:103]\n\t" "ds_read_b64_tr_b16 a[204:205], %[vr15] offset:32768\n\t"
    "v_mfma_f32_16x16x32_bf16 v[100:103], a[180:183], a[12:15], v[100:103]\n\t" "ds_read_b64_tr_b16 a[206:207], %[vr15] offset:34816\n\t" "global_load_lds_dwordx4 %[vb0], %[sb]\n\t"
    "v_mfma_f32_16x16x32_bf16 v[104:107], a[184:187], a[0:3], v[104:107]\n\t" "s_waitcnt vmcnt(32)\n\t" "s_barrier\n\t" "ds_read_b64_tr_b16 a[176:177], %[vr0] offset:40960\n\t"
    "v_mfma_f32_16x16x32_bf16 v[104:107], a[188:191], a[8:11], v[104:107]\n\t" "ds_read_b64_tr_b16 a[178:179], %[vr0] offset:43008\n\t" "v_add_u32 %[vb0], 0x10000, %[vb0]\n\t"
    "v_mfma_f32_16x16x32_bf16 v[108:111], a[184:187], a[4:7], v[108:111]\n\t" "ds_read_b64_tr_b16 a[180:181], %[vr8] offset:8192\n\t" "s_add_u32 s40, s40, 8192\n\t" "s_addc_u32 s41, s41, 0\n\t"
    "v_mfma_f32_16x16x32_bf16 v[108:111], a[188:191], a[12:15], v[108:111]\n\t" "ds_read_b64_tr_b16 a[182:183], %[vr8] offset:10240\n\t" "s_waitcnt lgkmcnt(4)\n\t"
    "v_mfma_f32_16x16x32_bf16 v[112:115], a[192:195], a[0:3], v[112:115]\n\t" "ds_read_b64_tr_b16 a[184:185], %[vr1] offset:40960\n\t"
    "global_load_dwordx2 a[144:145], %[vo], s[40:41] offset:-4096 sc1\n\t" "v_mfma_f32_16x16x32_bf16 v[112:115], a[196:199], a[8:11], v[112:115]\n\t"
    "ds_read_b64_tr_b16 a[186:187], %[vr1] offset:43008\n\t" "global_load_dwordx2 a[146:147], %[vo], s[40:41] offset:-3072 sc1\n\t"
    "v_mfma_f32_16x16x32_bf16 v[116:119], a[192:195], a[4:7], v[116:119]\n\t" "ds_read_b64_tr_b16 a[188:189], %[vr9] offset:8192\n\t"
    "v_mfma_f32_16x16x32_bf16 v[116:119], a[196:199], a[12:15], v[116:119]\n\t" "ds_read_b64_tr_b16 a[190:191], %[vr9] offset:10240\n\t"
    "global_load_dwordx2 a[148:149], %[vo], s[40:41] offset:-3584 sc1\n\t" "v_mfma_f32_16x16x32_bf16 v[120:123], a[200:203], a[0:3], v[120:123]\n\t"
    "ds_read_b64_tr_b16 a[192:193], %[vr2] offset:40960\n\t" "v_mfma_f32_16x16x32_bf16 v[120:123], a[204:207], a[8:11], v[120:123]\n\t" "ds_read_b64_tr_b16 a[194:195], %[vr2] offset:43008\n\t"
    "global_load_dwordx2 a[150:151], %[vo], s[40:41] offset:-2560 sc1\n\t" "v_mfma_f32_16x16x32_bf16 v[124:127], a[200:203], a[4:7], v[124:127]\n\t"
    "ds_read_b64_tr_b16 a[196:197], %[vr10] offset:8192\n\t" "s_add_u32 m0, %[sl0], 98304\n\t" "v_mfma_f32_16x16x32_bf16 v[124:127], a[204:207], a[12:15], v[124:127]\n\t"
    "ds_read_b64_tr_b16 a[198:199], %[vr10] offset:10240\n\t" "s_waitcnt lgkmcnt(4) vmcnt(31)\n\t" "v_mfma_f32_16x16x32_bf16 v[224:227], a[176:179], a[16:19], v[224:227]\n\t"
    "ds_read_b64_tr_b16 a[200:201], %[vr3] offset:40960\n\t" "global_load_lds_dwordx4 %[vb0], %[sb]\n\t" "v_mfma_f32_16x16x32_bf16 v[224:227], a[180:183], a[24:27], v[224:227]\n\t"
    "ds_read_b64_tr_b16 a[202:203], %[vr3] offset:43008\n\t" "v_add_u32 %[vb0], 0x10000, %[vb0]\n\t" "v_mfma_f32_16x16x32_bf16 v[240:243], a[176:179], a[20:23], v[240:243]\n\t"
    "ds_read_b64_tr_b16 a[204:205], %[vr11] offset:8192\n\t" "v_mfma_f32_16x16x32_bf16 v[240:243], a[180:183], a[28:31], v[240:243]\n\t" "ds_read_b64_tr_b16 a[206:207], %[vr11] offset:10240\n\t"
    "global_load_dwordx4 a[152:155], %[vx], s[40:41] offset:-2048 sc1\n\t" "v_mfma_f32_16x16x32_bf16 v[228:231], a[184:187], a[16:19], v[228:231]\n\t"
    "ds_read_b64_tr_b16 a[176:177], %[vr12] offset:40960\n\t" "v_mfma_f32_16x16x32_bf16 v[228:231], a[188:191], a[24:27], v[228:231]\n\t" "ds_read_b64_tr_b16 a[178:179], %[vr12] offset:43008\n\t"
    "v_mfma_f32_16x16x32_bf16 v[244:247], a[184:187], a[20:23], v[244:247]\n\t" "ds_read_b64_tr_b16 a[180:181], %[vr20] offset:8192\n\t"
    "v_mfma_f32_16x16x32_bf16 v[244:247], a[188:191], a[28:31], v[244:247]\n\t" "ds_read_b64_tr_b16 a[182:183], %[vr20] offset:10240\n\t" "s_waitcnt lgkmcnt(4)\n\t"
    "v_mfma_f32_16x16x32_bf16 v[232:235], a[192:195], a[16:19], v[232:235]\n\t" "ds_read_b64_tr_b16 a[184:185], %[vr13] offset:40960\n\t"
    "global_load_dwordx4 a[156:159], %[vx], s[40:41] offset:-1024 sc1\n\t" "v_mfma_f32_16x16x32_bf16 v[232:235], a[196:199], a[24:27], v[232:235]\n\t"
    "ds_read_b64_tr_b16 a[186:187], %[vr13] offset:43008\n\t" "s_add_u32 m0, %[sl0], 106496\n\t" "v_mfma_f32_16x16x32_bf16 v[248:251], a[192:195], a[20:23], v[248:251]\n\t"
    "ds_read_b64_tr_b16 a[188:189], %[vr21] offset:8192\n\t" "v_mfma_f32_16x16x32_bf16 v[248:251], a[196:199], a[28:31], v[248:251]\n\t" "ds_read_b64_tr_b16 a[190:191], %[vr21] offset:10240\n\t"
    "global_load_lds_dwordx4 %[vb0], %[sb]\n\t" "v_mfma_f32_16x16x32_bf16 v[236:239], a[200:203], a[16:19], v[236:239]\n\t" "ds_read_b64_tr_b16 a[192:193], %[vr14] offset:40960\n\t"
    "v_mfma_f32_16x16x32_bf16 v[236:239], a[204:207], a[24:27], v[236:239]\n\t" "ds_read_b64_tr_b16 a[194:195], %[vr14] offset:43008\n\t"
    "v_mfma_f32_16x16x32_bf16 v[252:255], a[200:203], a[20:23], v[252:255]\n\t" "ds_read_b64_tr_b16 a[196:197], %[vr22] offset:8192\n\t"
    "v_mfma_f32_16x16x32_bf16 v[252:255], a[204:207], a[28:31], v[252:255]\n\t" "ds_read_b64_tr_b16 a[198:199], %[vr22] offset:10240\n\t" "s_waitcnt lgkmcnt(4)\n\t"
    "v_mfma_f32_16x16x32_bf16 v[96:99], a[176:179], a[16:19], v[96:99]\n\t" "ds_read_b64_tr_b16 a[200:201], %[vr15] offset:40960\n\t" "global_load_dwordx4 a[160:163], %[vx], s[40:41] offset:0 sc1\n\t"
    "v_mfma_f32_16x16x32_bf16 v[96:99], a[180:183], a[24:27], v[96:99]\n\t" "ds_read_b64_tr_b16 a[202:203], %[vr15] offset:43008\n\t"
    "v_mfma_f32_16x16x32_bf16 v[100:103], a[176:179], a[20:23], v[100:103]\n\t" "ds_read_b64_tr_b16 a[204:205], %[vr23] offset:8192\n\t"
    "v_mfma_f32_16x16x32_bf16 v[100:103], a[180:183], a[28:31], v[100:103]\n\t" "ds_read_b64_tr_b16 a[206:207], %[vr23] offset:10240\n\t"
    "global_load_dwordx4 a[164:167], %[vx], s[40:41] offset:1024 sc1\n\t" "v_mfma_f32_16x16x32_bf16 v[104:107], a[184:187], a[16:19], v[104:107]\n\t" "s_waitcnt vmcnt(36)\n\t" "s_barrier\n\t"
    "ds_read_b64_tr_b16 a[176:177], %[vr8] offset:16384\n\t" "v_mfma_f32_16x16x32_bf16 v[104:107], a[188:191], a[24:27], v[104:107]\n\t" "ds_read_b64_tr_b16 a[178:179], %[vr8] offset:18432\n\t"
    "s_add_u32 m0, %[sl1], 147456\n\t" "v_mfma_f32_16x16x32_bf16 v[108:111], a[184:187], a[20:23], v[108:111]\n\t" "ds_read_b64_tr_b16 a[180:181], %[vr8] offset:24576\n\t"
    "v_mfma_f32_16x16x32_bf16 v[108:111], a[188:191], a[28:31], v[108:111]\n\t" "ds_read_b64_tr_b16 a[182:183], %[vr8] offset:26624\n\t" "s_waitcnt lgkmcnt(4)\n\t"
    "v_mfma_f32_16x16x32_bf16 v[112:115], a[192:195], a[16:19], v[112:115]\n\t" "ds_read_b64_tr_b16 a[184:185], %[vr9] offset:16384\n\t" "global_load_lds_dwordx4 %[vb1], %[sb]\n\t"
    "v_mfma_f32_16x16x32_bf16 v[112:115], a[196:199], a[24:27], v[112:115]\n\t" "ds_read_b64_tr_b16 a[186:187], %[vr9] offset:18432\n\t" "v_add_u32 %[vb1], 0x10000, %[vb1]\n\t"
    "v_mfma_f32_16x16x32_bf16 v[116:119], a[192:195], a[20:23], v[116:119]\n\t" "ds_read_b64_tr_b16 a[188:189], %[vr9] offset:24576\n\t"
    "v_mfma_f32_16x16x32_bf16 v[116:119], a[196:199], a[28:31], v[116:119]\n\t" "ds_read_b64_tr_b16 a[190:191], %[vr9] offset:26624\n\t"
    "global_load_dwordx4 a[168:171], %[vx], s[40:41] offset:2048 sc1\n\t" "v_mfma_f32_16x16x32_bf16 v[120:123], a[200:203], a[16:19], v[120:123]\n\t"
    "ds_read_b64_tr_b16 a[192:193], %[vr10] offset:16384\n\t" "v_mfma_f32_16x16x32_bf16 v[120:123], a[204:207], a[24:27], v[120:123]\n\t" "ds_read_b64_tr_b16 a[194:195], %[vr10] offset:18432\n\t"
    "v_mfma_f32_16x16x32_bf16 v[124:127], a[200:203], a[20:23], v[124:127]\n\t" "ds_read_b64_tr_b16 a[196:197], %[vr10] offset:24576\n\t"
    "v_mfma_f32_16x16x32_bf16 v[124:127], a[204:207], a[28:31], v[124:127]\n\t" "ds_read_b64_tr_b16 a[198:199], %[vr10] offset:26624\n\t" "s_waitcnt lgkmcnt(4) vmcnt(31)\n\t"
    "v_mfma_f32_16x16x32_bf16 v[224:227], a[176:179], a[80:83], v[224:227]\n\t" "ds_read_b64_tr_b16 a[200:201], %[vr11] offset:16384\n\t"
    "global_load_dwordx4 a[172:175], %[vx], s[40:41] offset:3072 sc1\n\t" "v_mfma_f32_16x16x32_bf16 v[224:227], a[180:183], a[88:91], v[224:227]\n\t"
    "ds_read_b64_tr_b16 a[202:203], %[vr11] offset:18432\n\t" "s_add_u32 m0, %[sl1], 155648\n\t" "v_mfma_f32_16x16x32_bf16 v[240:243], a[176:179], a[84:87], v[240:243]\n\t"
    "ds_read_b64_tr_b16 a[204:205], %[vr11] offset:24576\n\t" "global_load_lds_dwordx4 %[vb1], %[sb]\n\t" "v_mfma_f32_16x16x32_bf16 v[240:243], a[180:183], a[92:95], v[240:243]\n\t"
    "ds_read_b64_tr_b16 a[206:207], %[vr11] offset:26624\n\t" "v_add_u32 %[vb1], 0x10000, %[vb1]\n\t" "v_mfma_f32_16x16x32_bf16 v[228:231], a[184:187], a[80:83], v[228:231]\n\t" "s_waitcnt vmcnt(32)\n\t"
    "s_barrier\n\t" "ds_read_b64_tr_b16 a[176:177], %[vr0] offset:0\n\t" "v_mfma_f32_16x16x32_bf16 v[228:231], a[188:191], a[88:91], v[228:231]\n\t" "ds_read_b64_tr_b16 a[178:179], %[vr0] offset:2048\n\t"
    "s_add_u32 m0, %[sl1], 0\n\t" "v_mfma_f32_16x16x32_bf16 v[244:247], a[184:187], a[84:87], v[244:247]\n\t" "ds_read_b64_tr_b16 a[180:181], %[vr0] offset:8192\n\t"
    "global_load_lds_dwordx4 %[vb1], %[sb]\n\t" "v_mfma_f32_16x16x32_bf16 v[244:247], a[188:191], a[92:95], v[244:247]\n\t" "ds_read_b64_tr_b16 a[182:183], %[vr0] offset:10240\n\t"
    "s_waitcnt lgkmcnt(4)\n\t" "v_mfma_f32_16x16x32_bf16 v[232:235], a[192:195], a[80:83], v[232:235]\n\t" "ds_read_b64_tr_b16 a[184:185], %[vr1] offset:0\n\t" "v_add_u32 %[vb1], 0x10000, %[vb1]\n\t"
    "v_mfma_f32_16x16x32_bf16 v[232:235], a[196:199], a[88:91], v[232:235]\n\t" "ds_read_b64_tr_b16 a[186:187], %[vr1] offset:2048\n\t" "s_add_u32 m0, %[sl1], 8192\n\t"
    "v_mfma_f32_16x16x32_bf16 v[248:251], a[192:195], a[84:87], v[248:251]\n\t" "ds_read_b64_tr_b16 a[188:189], %[vr1] offset:8192\n\t" "global_load_lds_dwordx4 %[vb1], %[sb]\n\t"
    "v_mfma_f32_16x16x32_bf16 v[248:251], a[196:199], a[92:95], v[248:251]\n\t" "ds_read_b64_tr_b16 a[190:191], %[vr1] offset:10240\n\t" "v_add_u32 %[vb1], 0x10000, %[vb1]\n\t"
    "v_mfma_f32_16x16x32_bf16 v[236:239], a[200:203], a[80:83], v[236:239]\n\t" "ds_read_b64_tr_b16 a[192:193], %[vr2] offset:0\n\t" "s_add_u32 m0, %[sl1], 49152\n\t"
    "v_mfma_f32_16x16x32_bf16 v[236:239], a[204:207], a[88:91], v[236:239]\n\t" "ds_read_b64_tr_b16 a[194:195], %[vr2] offset:2048\n\t" "global_load_lds_dwordx4 %[vb1], %[sb]\n\t"
    "v_mfma_f32_16x16x32_bf16 v[252:255], a[200:203], a[84:87], v[252:255]\n\t" "ds_read_b64_tr_b16 a[196:197], %[vr2] offset:8192\n\t" "v_add_u32 %[vb1], 0x10000, %[vb1]\n\t"
    "v_mfma_f32_16x16x32_bf16 v[252:255], a[204:207], a[92:95], v[252:255]\n\t" "ds_read_b64_tr_b16 a[198:199], %[vr2] offset:10240\n\t" "s_waitcnt lgkmcnt(4) vmcnt(30)\n\t"
    "v_mfma_f32_16x16x32_bf16 v[224:227], a[176:179], a[96:99], v[224:227]\n\t" "ds_read_b64_tr_b16 a[200:201], %[vr3] offset:0\n\t" "s_add_u32 m0, %[sl1], 57344\n\t"
    "v_mfma_f32_16x16x32_bf16 v[224:227], a[180:183], a[104:107], v[224:227]\n\t" "ds_read_b64_tr_b16 a[202:203], %[vr3] offset:2048\n\t" "global_load_lds_dwordx4 %[vb1], %[sb]\n\t"
    "v_mfma_f32_16x16x32_bf16 v[240:243], a[176:179], a[100:103], v[240:243]\n\t" "ds_read_b64_tr_b16 a[204:205], %[vr3] offset:8192\n\t" "v_add_u32 %[vb1], 0x10000, %[vb1]\n\t"
    "v_mfma_f32_16x16x32_bf16 v[240:243], a[180:183], a[108:111], v[240:243]\n\t" "ds_read_b64_tr_b16 a[206:207], %[vr3] offset:10240\n\t" "s_add_u32 m0, %[sl1], 65536\n\t"
    "v_mfma_f32_16x16x32_bf16 v[228:231], a[184:187], a[96:99], v[228:231]\n\t" "s_waitcnt vmcnt(30)\n\t" "s_barrier\n\t" "ds_read_b64_tr_b16 a[176:177], %[vr0] offset:49152\n\t"
    "v_mfma_f32_16x16x32_bf16 v[228:231], a[188:191], a[104:107], v[228:231]\n\t" "ds_read_b64_tr_b16 a[178:179], %[vr0] offset:51200\n\t" "global_load_lds_dwordx4 %[vb1], %[sb]\n\t"
    "v_mfma_f32_16x16x32_bf16 v[244:247], a[184:187], a[100:103], v[244:247]\n\t" "ds_read_b64_tr_b16 a[180:181], %[vr0] offset:57344\n\t" "v_add_u32 %[vb1], 0x10000, %[vb1]\n\t"
    "v_mfma_f32_16x16x32_bf16 v[244:247], a[188:191], a[108:111], v[244:247]\n\t" "ds_read_b64_tr_b16 a[182:183], %[vr0] offset:59392\n\t" "s_waitcnt lgkmcnt(4)\n\t"
    "v_mfma_f32_16x16x32_bf16 v[232:235], a[192:195], a[96:99], v[232:235]\n\t" "ds_read_b64_tr_b16 a[184:185], %[vr1] offset:49152\n\t" "s_add_u32 m0, %[sl1], 73728\n\t"
    "v_mfma_f32_16x16x32_bf16 v[232:235], a[196:199], a[104:107], v[232:235]\n\t" "ds_read_b64_tr_b16 a[186:187], %[vr1] offset:51200\n\t" "global_load_lds_dwordx4 %[vb1], %[sb]\n\t"
    "v_mfma_f32_16x16x32_bf16 v[248:251], a[192:195], a[100:103], v[248:251]\n\t" "ds_read_b64_tr_b16 a[188:189], %[vr1] offset:57344\n\t" "v_add_u32 %[vb1], 0x10000, %[vb1]\n\t"
    "v_mfma_f32_16x16x32_bf16 v[248:251], a[196:199], a[108:111], v[248:251]\n\t" "ds_read_b64_tr_b16 a[190:191], %[vr1] offset:59392\n\t"
    "v_mfma_f32_16x16x32_bf16 v[236:239], a[200:203], a[96:99], v[236:239]\n\t" "ds_read_b64_tr_b16 a[192:193], %[vr2] offset:49152\n\t"
    "v_mfma_f32_16x16x32_bf16 v[236:239], a[204:207], a[104:107], v[236:239]\n\t" "ds_read_b64_tr_b16 a[194:195], %[vr2] offset:51200\n\t"
    "v_mfma_f32_16x16x32_bf16 v[252:255], a[200:203], a[100:103], v[252:255]\n\t" "ds_read_b64_tr_b16 a[196:197], %[vr2] offset:57344\n\t"
    "v_mfma_f32_16x16x32_bf16 v[252:255], a[204:207], a[108:111], v[252:255]\n\t" "ds_read_b64_tr_b16 a[198:199], %[vr2] offset:59392\n\t" "s_waitcnt lgkmcnt(4) vmcnt(27)\n\t"
    "v_mfma_f32_16x16x32_bf16 v[224:227], a[176:179], a[112:115], v[224:227]\n\t" "ds_read_b64_tr_b16 a[200:201], %[vr3] offset:49152\n\t"
    "v_mfma_f32_16x16x32_bf16 v[224:227], a[180:183], a[120:123], v[224:227]\n\t" "ds_read_b64_tr_b16 a[202:203], %[vr3] offset:51200\n\t"
    "v_mfma_f32_16x16x32_bf16 v[240:243], a[176:179], a[116:119], v[240:243]\n\t" "ds_read_b64_tr_b16 a[204:205], %[vr3] offset:57344\n\t"
    "v_mfma_f32_16x16x32_bf16 v[240:243], a[180:183], a[124:127], v[240:243]\n\t" "ds_read_b64_tr_b16 a[206:207], %[vr3] offset:59392\n\t"
    "v_mfma_f32_16x16x32_bf16 v[228:231], a[184:187], a[112:115], v[228:231]\n\t" "s_waitcnt vmcnt(26)\n\t" "s_barrier\n\t" "ds_read_b64_tr_b16 a[176:177], %[vr4] offset:0\n\t"
    "v_mfma_f32_16x16x32_bf16 v[228:231], a[188:191], a[120:123], v[228:231]\n\t" "ds_read_b64_tr_b16 a[178:179], %[vr4] offset:2048\n\t"
    "v_mfma_f32_16x16x32_bf16 v[244:247], a[184:187], a[116:119], v[244:247]\n\t" "ds_read_b64_tr_b16 a[180:181], %[vr4] offset:8192\n\t"
    "v_mfma_f32_16x16x32_bf16 v[244:247], a[188:191], a[124:127], v[244:247]\n\t" "ds_read_b64_tr_b16 a[182:183], %[vr4] offset:10240\n\t" "s_waitcnt lgkmcnt(4)\n\t"
    "v_mfma_f32_16x16x32_bf16 v[232:235], a[192:195], a[112:115], v[232:235]\n\t" "ds_read_b64_tr_b16 a[184:185], %[vr5] offset:0\n\t"
    "v_mfma_f32_16x16x32_bf16 v[232:235], a[196:199], a[120:123], v[232:235]\n\t" "ds_read_b64_tr_b16 a[186:187], %[vr5] offset:2048\n\t"
    "v_mfma_f32_16x16x32_bf16 v[248:251], a[192:195], a[116:119], v[248:251]\n\t" "ds_read_b64_tr_b16 a[188:189], %[vr5] offset:8192\n\t"
    "v_mfma_f32_16x16x32_bf16 v[248:251], a[196:199], a[124:127], v[248:251]\n\t" "ds_read_b64_tr_b16 a[190:191], %[vr5] offset:10240\n\t"
    "v_mfma_f32_16x16x32_bf16 v[236:239], a[200:203], a[112:115], v[236:239]\n\t" "ds_read_b64_tr_b16 a[192:193], %[vr6] offset:0\n\t"
    "v_mfma_f32_16x16x32_bf16 v[236:239], a[204:207], a[120:123], v[236:239]\n\t" "ds_read_b64_tr_b16 a[194:195], %[vr6] offset:2048\n\t"
    "v_mfma_f32_16x16x32_bf16 v[252:255], a[200:203], a[116:119], v[252:255]\n\t" "ds_read_b64_tr_b16 a[196:197], %[vr6] offset:8192\n\t"
    "v_mfma_f32_16x16x32_bf16 v[252:255], a[204:207], a[124:127], v[252:255]\n\t" "ds_read_b64_tr_b16 a[198:199], %[vr6] offset:10240\n\t" "s_waitcnt lgkmcnt(4) vmcnt(21)\n\t"
    "v_mfma_f32_16x16x32_bf16 v[224:227], a[176:179], a[128:131], v[224:227]\n\t" "ds_read_b64_tr_b16 a[200:201], %[vr7] offset:0\n\t"
    "v_mfma_f32_16x16x32_bf16 v[224:227], a[180:183], a[136:139], v[224:227]\n\t" "ds_read_b64_tr_b16 a[202:203], %[vr7] offset:2048\n\t"
    "v_mfma_f32_16x16x32_bf16 v[240:243], a[176:179], a[132:135], v[240:243]\n\t" "ds_read_b64_tr_b16 a[204:205], %[vr7] offset:8192\n\t"
    "v_mfma_f32_16x16x32_bf16 v[240:243], a[180:183], a[140:143], v[240:243]\n\t" "ds_read_b64_tr_b16 a[206:207], %[vr7] offset:10240\n\t"
    "v_mfma_f32_16x16x32_bf16 v[228:231], a[184:187], a[128:131], v[228:231]\n\t" "s_waitcnt vmcnt(20)\n\t" "s_barrier\n\t" "ds_read_b64_tr_b16 a[176:177], %[vr4] offset:16384\n\t"
    "v_mfma_f32_16x16x32_bf16 v[228:231], a[188:191], a[136:139], v[228:231]\n\t" "ds_read_b64_tr_b16 a[178:179], %[vr4] offset:18432\n\t"
    "v_mfma_f32_16x16x32_bf16 v[244:247], a[184:187], a[132:135], v[244:247]\n\t" "ds_read_b64_tr_b16 a[180:181], %[vr4] offset:24576\n\t"
    "v_mfma_f32_16x16x32_bf16 v[244:247], a[188:191], a[140:143], v[244:247]\n\t" "ds_read_b64_tr_b16 a[182:183], %[vr4] offset:26624\n\t" "s_waitcnt lgkmcnt(4)\n\t"
    "v_mfma_f32_16x16x32_bf16 v[232:235], a[192:195], a[128:131], v[232:235]\n\t" "ds_read_b64_tr_b16 a[184:185], %[vr5] offset:16384\n\t"
    "v_mfma_f32_16x16x32_bf16 v[232:235], a[196:199], a[136:139], v[232:235]\n\t" "ds_read_b64_tr_b16 a[186:187], %[vr5] offset:18432\n\t"
    "v_mfma_f32_16x16x32_bf16 v[248:251], a[192:195], a[132:135], v[248:251]\n\t" "ds_read_b64_tr_b16 a[188:189], %[vr5] offset:24576\n\t"
    "v_mfma_f32_16x16x32_bf16 v[248:251], a[196:199], a[140:143], v[248:251]\n\t" "ds_read_b64_tr_b16 a[190:191], %[vr5] offset:26624\n\t"
    "v_mfma_f32_16x16x32_bf16 v[236:239], a[200:203], a[128:131], v[236:239]\n\t" "ds_read_b64_tr_b16 a[192:193], %[vr6] offset:16384\n\t"
    "v_mfma_f32_16x16x32_bf16 v[236:239], a[204:207], a[136:139], v[236:239]\n\t" "ds_read_b64_tr_b16 a[194:195], %[vr6] offset:18432\n\t"
    "v_mfma_f32_16x16x32_bf16 v[252:255], a[200:203], a[132:135], v[252:255]\n\t" "ds_read_b64_tr_b16 a[196:197], %[vr6] offset:24576\n\t"
    "v_mfma_f32_16x16x32_bf16 v[252:255], a[204:207], a[140:143], v[252:255]\n\t" "ds_read_b64_tr_b16 a[198:199], %[vr6] offset:26624\n\t" "s_waitcnt lgkmcnt(4) vmcnt(13)\n\t"
    "v_mfma_f32_16x16x32_bf16 v[224:227], a[176:179], a[144:147], v[224:227]\n\t" "ds_read_b64_tr_b16 a[200:201], %[vr7] offset:16384\n\t"
    "v_mfma_f32_16x16x32_bf16 v[224:227], a[180:183], a[152:155], v[224:227]\n\t" "ds_read_b64_tr_b16 a[202:203], %[vr7] offset:18432\n\t"
    "v_mfma_f32_16x16x32_bf16 v[240:243], a[176:179], a[148:151], v[240:243]\n\t" "ds_read_b64_tr_b16 a[204:205], %[vr7] offset:24576\n\t"
    "v_mfma_f32_16x16x32_bf16 v[240:243], a[180:183], a[156:159], v[240:243]\n\t" "ds_read_b64_tr_b16 a[206:207], %[vr7] offset:26624\n\t"
    "v_mfma_f32_16x16x32_bf16 v[228:231], a[184:187], a[144:147], v[228:231]\n\t" "s_waitcnt vmcnt(12)\n\t" "s_barrier\n\t" "ds_read_b64_tr_b16 a[176:177], %[vr4] offset:32768\n\t"
    "v_mfma_f32_16x16x32_bf16 v[228:231], a[188:191], a[152:155], v[228:231]\n\t" "ds_read_b64_tr_b16 a[178:179], %[vr4] offset:34816\n\t"
    "v_mfma_f32_16x16x32_bf16 v[244:247], a[184:187], a[148:151], v[244:247]\n\t" "ds_read_b64_tr_b16 a[180:181], %[vr4] offset:40960\n\t"
    "v_mfma_f32_16x16x32_bf16 v[244:247], a[188:191], a[156:159], v[244:247]\n\t" "ds_read_b64_tr_b16 a[182:183], %[vr4] offset:43008\n\t" "s_waitcnt lgkmcnt(4)\n\t"
    "v_mfma_f32_16x16x32_bf16 v[232:235], a[192:195], a[144:147], v[232:235]\n\t" "ds_read_b64_tr_b16 a[184:185], %[vr5] offset:32768\n\t"
    "v_mfma_f32_16x16x32_bf16 v[232:235], a[196:199], a[152:155], v[232:235]\n\t" "ds_read_b64_tr_b16 a[186:187], %[vr5] offset:34816\n\t"
    "v_mfma_f32_16x16x32_bf16 v[248:251], a[192:195], a[148:151], v[248:251]\n\t" "ds_read_b64_tr_b16 a[188:189], %[vr5] offset:40960\n\t"
    "v_mfma_f32_16x16x32_bf16 v[248:251], a[196:199], a[156:159], v[248:251]\n\t" "ds_read_b64_tr_b16 a[190:191], %[vr5] offset:43008\n\t"
    "v_mfma_f32_16x16x32_bf16 v[236:239], a[200:203], a[144:147], v[236:239]\n\t" "ds_read_b64_tr_b16 a[192:193], %[vr6] offset:32768\n\t"
    "v_mfma_f32_16x16x32_bf16 v[236:239], a[204:207], a[152:155], v[236:239]\n\t" "ds_read_b64_tr_b16 a[194:195], %[vr6] offset:34816\n\t"
    "v_mfma_f32_16x16x32_bf16 v[252:255], a[200:203], a[148:151], v[252:255]\n\t" "ds_read_b64_tr_b16 a[196:197], %[vr6] offset:40960\n\t"
    "v_mfma_f32_16x16x32_bf16 v[252:255], a[204:207], a[156:159], v[252:255]\n\t" "ds_read_b64_tr_b16 a[198:199], %[vr6] offset:43008\n\t" "s_waitcnt lgkmcnt(4) vmcnt(7)\n\t"
    "v_mfma_f32_16x16x32_bf16 v[224:227], a[176:179], a[160:163], v[224:227]\n\t" "ds_read_b64_tr_b16 a[200:201], %[vr7] offset:32768\n\t"
    "v_mfma_f32_16x16x32_bf16 v[224:227], a[180:183], a[168:171], v[224:227]\n\t" "ds_read_b64_tr_b16 a[202:203], %[vr7] offset:34816\n\t"
    "v_mfma_f32_16x16x32_bf16 v[240:243], a[176:179], a[164:167], v[240:243]\n\t" "ds_read_b64_tr_b16 a[204:205], %[vr7] offset:40960\n\t"
    "v_mfma_f32_16x16x32_bf16 v[240:243], a[180:183], a[172:175], v[240:243]\n\t" "ds_read_b64_tr_b16 a[206:207], %[vr7] offset:43008\n\t"
    "v_mfma_f32_16x16x32_bf16 v[228:231], a[184:187], a[160:163], v[228:231]\n\t" "s_waitcnt vmcnt(6)\n\t" "s_barrier\n\t" "ds_read_b64_tr_b16 a[176:177], %[vr20] offset:16384\n\t"
    "v_mfma_f32_16x16x32_bf16 v[228:231], a[188:191], a[168:171], v[228:231]\n\t" "ds_read_b64_tr_b16 a[178:179], %[vr20] offset:18432\n\t"
    "v_mfma_f32_16x16x32_bf16 v[244:247], a[184:187], a[164:167], v[244:247]\n\t" "ds_read_b64_tr_b16 a[180:181], %[vr20] offset:24576\n\t"
    "v_mfma_f32_16x16x32_bf16 v[244:247], a[188:191], a[172:175], v[244:247]\n\t" "ds_read_b64_tr_b16 a[182:183], %[vr20] offset:26624\n\t" "s_waitcnt lgkmcnt(4)\n\t"
    "v_mfma_f32_16x16x32_bf16 v[232:235], a[192:195], a[160:163], v[232:235]\n\t" "ds_read_b64_tr_b16 a[184:185], %[vr21] offset:16384\n\t"
    "v_mfma_f32_16x16x32_bf16 v[232:235], a[196:199], a[168:171], v[232:235]\n\t" "ds_read_b64_tr_b16 a[186:187], %[vr21] offset:18432\n\t"
    "v_mfma_f32_16x16x32_bf16 v[248:251], a[192:195], a[164:167], v[248:251]\n\t" "ds_read_b64_tr_b16 a[188:189], %[vr21] offset:24576\n\t"
    "v_mfma_f32_16x16x32_bf16 v[248:251], a[196:199], a[172:175], v[248:251]\n\t" "ds_read_b64_tr_b16 a[190:191], %[vr21] offset:26624\n\t"
    "v_mfma_f32_16x16x32_bf16 v[236:239], a[200:203], a[160:163], v[236:239]\n\t" "ds_read_b64_tr_b16 a[192:193], %[vr22] offset:16384\n\t"
    "v_mfma_f32_16x16x32_bf16 v[236:239], a[204:207], a[168:171], v[236:239]\n\t" "ds_read_b64_tr_b16 a[194:195], %[vr22] offset:18432\n\t"
    "v_mfma_f32_16x16x32_bf16 v[252:255], a[200:203], a[164:167], v[252:255]\n\t" "ds_read_b64_tr_b16 a[196:197], %[vr22] offset:24576\n\t"
    "v_mfma_f32_16x16x32_bf16 v[252:255], a[204:207], a[172:175], v[252:255]\n\t" "ds_read_b64_tr_b16 a[198:199], %[vr22] offset:26624\n\t" "s_waitcnt lgkmcnt(4)\n\t"
    "v_mfma_f32_16x16x32_bf16 v[96:99], a[176:179], a[80:83], v[96:99]\n\t" "ds_read_b64_tr_b16 a[200:201], %[vr23] offset:16384\n\t"
    "v_mfma_f32_16x16x32_bf16 v[96:99], a[180:183], a[88:91], v[96:99]\n\t" "ds_read_b64_tr_b16 a[202:203], %[vr23] offset:18432\n\t"
    "v_mfma_f32_16x16x32_bf16 v[100:103], a[176:179], a[84:87], v[100:103]\n\t" "ds_read_b64_tr_b16 a[204:205], %[vr23] offset:24576\n\t"
    "v_mfma_f32_16x16x32_bf16 v[100:103], a[180:183], a[92:95], v[100:103]\n\t" "ds_read_b64_tr_b16 a[206:207], %[vr23] offset:26624\n\t" "v_cvt_pk_bf16_f32 v224, v224, v225\n\t"
    "v_cvt_pk_bf16_f32 v225, v226, v227\n\t" "v_add3_u32 v207, %[t32], v224, v225\n\t" "v_mfma_f32_16x16x32_bf16 v[104:107], a[184:187], a[80:83], v[104:107]\n\t" "s_waitcnt vmcnt(4)\n\t" "s_barrier\n\t"
    "ds_read_b64_tr_b16 a[176:177], %[vr12] offset:0\n\t" "v_cvt_pk_bf16_f32 v226, v228, v229\n\t" "v_cvt_pk_bf16_f32 v227, v230, v231\n\t" "v_add3_u32 v207, v207, v226, v227\n\t"
    "v_mfma_f32_16x16x32_bf16 v[104:107], a[188:191], a[88:91], v[104:107]\n\t" "ds_read_b64_tr_b16 a[178:179], %[vr12] offset:2048\n\t" "global_store_dwordx4 %[vx], v[224:227], %[sm] offset:0 sc1\n\t"
    "v_cvt_pk_bf16_f32 v232, v232, v233\n\t" "v_cvt_pk_bf16_f32 v233, v234, v235\n\t" "v_add3_u32 v207, v207, v232, v233\n\t" "v_mfma_f32_16x16x32_bf16 v[108:111], a[184:187], a[84:87], v[108:111]\n\t"
    "ds_read_b64_tr_b16 a[180:181], %[vr12] offset:8192\n\t" "v_cvt_pk_bf16_f32 v234, v236, v237\n\t" "v_cvt_pk_bf16_f32 v235, v238, v239\n\t" "v_add3_u32 v207, v207, v234, v235\n\t"
    "v_mfma_f32_16x16x32_bf16 v[108:111], a[188:191], a[92:95], v[108:111]\n\t" "ds_read_b64_tr_b16 a[182:183], %[vr12] offset:10240\n\t"
    "global_store_dwordx4 %[vx], v[232:235], %[sm] offset:1024 sc1\n\t" "v_cvt_pk_bf16_f32 v240, v240, v241\n\t" "v_cvt_pk_bf16_f32 v241, v242, v243\n\t" "v_add3_u32 v207, v207, v240, v241\n\t"
    "s_waitcnt lgkmcnt(4)\n\t" "v_mfma_f32_16x16x32_bf16 v[112:115], a[192:195], a[80:83], v[112:115]\n\t" "ds_read_b64_tr_b16 a[184:185], %[vr13] offset:0\n\t" "v_cvt_pk_bf16_f32 v242, v244, v245\n\t"
    "v_cvt_pk_bf16_f32 v243, v246, v247\n\t" "v_add3_u32 v207, v207, v242, v243\n\t" "v_mfma_f32_16x16x32_bf16 v[112:115], a[196:199], a[88:91], v[112:115]\n\t"
    "ds_read_b64_tr_b16 a[186:187], %[vr13] offset:2048\n\t" "global_store_dwordx4 %[vx], v[240:243], %[sm] offset:2048 sc1\n\t" "v_cvt_pk_bf16_f32 v248, v248, v249\n\t"
    "v_cvt_pk_bf16_f32 v249, v250, v251\n\t" "v_add3_u32 v207, v207, v248, v249\n\t" "v_mfma_f32_16x16x32_bf16 v[116:119], a[192:195], a[84:87], v[116:119]\n\t"
    "ds_read_b64_tr_b16 a[188:189], %[vr13] offset:8192\n\t" "v_cvt_pk_bf16_f32 v250, v252, v253\n\t" "v_cvt_pk_bf16_f32 v251, v254, v255\n\t" "v_add3_u32 v207, v207, v250, v251\n\t"
    "v_mfma_f32_16x16x32_bf16 v[116:119], a[196:199], a[92:95], v[116:119]\n\t" "ds_read_b64_tr_b16 a[190:191], %[vr13] offset:10240\n\t"
    "global_store_dwordx4 %[vx], v[248:251], %[sm] offset:3072 sc1\n\t" "v_mfma_f32_16x16x32_bf16 v[120:123], a[200:203], a[80:83], v[120:123]\n\t" "ds_read_b64_tr_b16 a[192:193], %[vr14] offset:0\n\t"
    "global_store_dword %[vc], v207, %[sm] sc1\n\t" "v_mfma_f32_16x16x32_bf16 v[120:123], a[204:207], a[88:91], v[120:123]\n\t" "ds_read_b64_tr_b16 a[194:195], %[vr14] offset:2048\n\t"
    "s_add_u32 m0, %[sl1], 81920\n\t" "v_mfma_f32_16x16x32_bf16 v[124:127], a[200:203], a[84:87], v[124:127]\n\t" "ds_read_b64_tr_b16 a[196:197], %[vr14] offset:8192\n\t"
    "global_load_lds_dwordx4 %[vb1], %[sb]\n\t" "v_mfma_f32_16x16x32_bf16 v[124:127], a[204:207], a[92:95], v[124:127]\n\t" "ds_read_b64_tr_b16 a[198:199], %[vr14] offset:10240\n\t"
    "s_waitcnt lgkmcnt(4)\n\t" "v_mfma_f32_16x16x32_bf16 v[96:99], a[176:179], a[96:99], v[96:99]\n\t" "ds_read_b64_tr_b16 a[200:201], %[vr15] offset:0\n\t" "v_add_u32 %[vb1], 0x10000, %[vb1]\n\t"
    "v_mfma_f32_16x16x32_bf16 v[96:99], a[180:183], a[104:107], v[96:99]\n\t" "ds_read_b64_tr_b16 a[202:203], %[vr15] offset:2048\n\t" "s_add_u32 m0, %[sl1], 90112\n\t"
    "v_mfma_f32_16x16x32_bf16 v[100:103], a[176:179], a[100:103], v[100:103]\n\t" "ds_read_b64_tr_b16 a[204:205], %[vr15] offset:8192\n\t" "global_load_lds_dwordx4 %[vb1], %[sb]\n\t"
    "v_mfma_f32_16x16x32_bf16 v[100:103], a[180:183], a[108:111], v[100:103]\n\t" "ds_read_b64_tr_b16 a[206:207], %[vr15] offset:10240\n\t" "v_add_u32 %[vb1], 0x10000, %[vb1]\n\t"
    "v_mfma_f32_16x16x32_bf16 v[104:107], a[184:187], a[96:99], v[104:107]\n\t" "s_waitcnt vmcnt(9)\n\t" "s_barrier\n\t" "ds_read_b64_tr_b16 a[176:177], %[vr12] offset:49152\n\t"
    "v_mfma_f32_16x16x32_bf16 v[104:107], a[188:191], a[104:107], v[104:107]\n\t" "ds_read_b64_tr_b16 a[178:179], %[vr12] offset:51200\n\t" "s_add_u32 m0, %[sl1], 98304\n\t"
    "v_mfma_f32_16x16x32_bf16 v[108:111], a[184:187], a[100:103], v[108:111]\n\t" "ds_read_b64_tr_b16 a[180:181], %[vr12] offset:57344\n\t" "global_load_lds_dwordx4 %[vb1], %[sb]\n\t"
    "v_mfma_f32_16x16x32_bf16 v[108:111], a[188:191], a[108:111], v[108:111]\n\t" "ds_read_b64_tr_b16 a[182:183], %[vr12] offset:59392\n\t" "s_waitcnt lgkmcnt(4)\n\t"
    "v_mfma_f32_16x16x32_bf16 v[112:115], a[192:195], a[96:99], v[112:115]\n\t" "ds_read_b64_tr_b16 a[184:185], %[vr13] offset:49152\n\t" "v_add_u32 %[vb1], 0x10000, %[vb1]\n\t"
    "v_mfma_f32_16x16x32_bf16 v[112:115], a[196:199], a[104:107], v[112:115]\n\t" "ds_read_b64_tr_b16 a[186:187], %[vr13] offset:51200\n\t" "s_add_u32 m0, %[sl1], 106496\n\t"
    "v_mfma_f32_16x16x32_bf16 v[116:119], a[192:195], a[100:103], v[116:119]\n\t" "ds_read_b64_tr_b16 a[188:189], %[vr13] offset:57344\n\t" "global_load_lds_dwordx4 %[vb1], %[sb]\n\t"
    "v_mfma_f32_16x16x32_bf16 v[116:119], a[196:199], a[108:111], v[116:119]\n\t" "ds_read_b64_tr_b16 a[190:191], %[vr13] offset:59392\n\t"
    "v_mfma_f32_16x16x32_bf16 v[120:123], a[200:203], a[96:99], v[120:123]\n\t" "ds_read_b64_tr_b16 a[192:193], %[vr14] offset:49152\n\t"
    "v_mfma_f32_16x16x32_bf16 v[120:123], a[204:207], a[104:107], v[120:123]\n\t" "ds_read_b64_tr_b16 a[194:195], %[vr14] offset:51200\n\t"
    "v_mfma_f32_16x16x32_bf16 v[124:127], a[200:203], a[100:103], v[124:127]\n\t" "ds_read_b64_tr_b16 a[196:197], %[vr14] offset:57344\n\t"
    "v_mfma_f32_16x16x32_bf16 v[124:127], a[204:207], a[108:111], v[124:127]\n\t" "ds_read_b64_tr_b16 a[198:199], %[vr14] offset:59392\n\t" "s_waitcnt lgkmcnt(4)\n\t"
    "v_mfma_f32_16x16x32_bf16 v[96:99], a[176:179], a[112:115], v[96:99]\n\t" "ds_read_b64_tr_b16 a[200:201], %[vr15] offset:49152\n\t"
    "v_mfma_f32_16x16x32_bf16 v[96:99], a[180:183], a[120:123], v[96:99]\n\t" "ds_read_b64_tr_b16 a[202:203], %[vr15] offset:51200\n\t"
    "v_mfma_f32_16x16x32_bf16 v[100:103], a[176:179], a[116:119], v[100:103]\n\t" "ds_read_b64_tr_b16 a[204:205], %[vr15] offset:57344\n\t"
    "v_mfma_f32_16x16x32_bf16 v[100:103], a[180:183], a[124:127], v[100:103]\n\t" "ds_read_b64_tr_b16 a[206:207], %[vr15] offset:59392\n\t"
    "v_mfma_f32_16x16x32_bf16 v[104:107], a[184:187], a[112:115], v[104:107]\n\t" "s_waitcnt vmcnt(9)\n\t" "s_barrier\n\t" "ds_read_b64_tr_b16 a[176:177], %[vr16] offset:0\n\t"
    "v_mfma_f32_16x16x32_bf16 v[104:107], a[188:191], a[120:123], v[104:107]\n\t" "ds_read_b64_tr_b16 a[178:179], %[vr16] offset:2048\n\t"
    "v_mfma_f32_16x16x32_bf16 v[108:111], a[184:187], a[116:119], v[108:111]\n\t" "ds_read_b64_tr_b16 a[180:181], %[vr16] offset:8192\n\t"
    "v_mfma_f32_16x16x32_bf16 v[108:111], a[188:191], a[124:127], v[108:111]\n\t" "ds_read_b64_tr_b16 a[182:183], %[vr16] offset:10240\n\t" "s_waitcnt lgkmcnt(4)\n\t"
    "v_mfma_f32_16x16x32_bf16 v[112:115], a[192:195], a[112:115], v[112:115]\n\t" "ds_read_b64_tr_b16 a[184:185], %[vr17] offset:0\n\t"
    "v_mfma_f32_16x16x32_bf16 v[112:115], a[196:199], a[120:123], v[112:115]\n\t" "ds_read_b64_tr_b16 a[186:187], %[vr17] offset:2048\n\t"
    "v_mfma_f32_16x16x32_bf16 v[116:119], a[192:195], a[116:119], v[116:119]\n\t" "ds_read_b64_tr_b16 a[188:189], %[vr17] offset:8192\n\t"
    "v_mfma_f32_16x16x32_bf16 v[116:119], a[196:199], a[124:127], v[116:119]\n\t" "ds_read_b64_tr_b16 a[190:191], %[vr17] offset:10240\n\t"
    "v_mfma_f32_16x16x32_bf16 v[120:123], a[200:203], a[112:115], v[120:123]\n\t" "ds_read_b64_tr_b16 a[192:193], %[vr18] offset:0\n\t"
    "v_mfma_f32_16x16x32_bf16 v[120:123], a[204:207], a[120:123], v[120:123]\n\t" "ds_read_b64_tr_b16 a[194:195], %[vr18] offset:2048\n\t"
    "v_mfma_f32_16x16x32_bf16 v[124:127], a[200:203], a[116:119], v[124:127]\n\t" "ds_read_b64_tr_b16 a[196:197], %[vr18] offset:8192\n\t"
    "v_mfma_f32_16x16x32_bf16 v[124:127], a[204:207], a[124:127], v[124:127]\n\t" "ds_read_b64_tr_b16 a[198:199], %[vr18] offset:10240\n\t" "s_waitcnt lgkmcnt(4)\n\t"
    "v_mfma_f32_16x16x32_bf16 v[96:99], a[176:179], a[128:131], v[96:99]\n\t" "ds_read_b64_tr_b16 a[200:201], %[vr19] offset:0\n\t"
    "v_mfma_f32_16x16x32_bf16 v[96:99], a[180:183], a[136:139], v[96:99]\n\t" "ds_read_b64_tr_b16 a[202:203], %[vr19] offset:2048\n\t"
    "v_mfma_f32_16x16x32_bf16 v[100:103], a[176:179], a[132:135], v[100:103]\n\t" "ds_read_b64_tr_b16 a[204:205], %[vr19] offset:8192\n\t"
    "v_mfma_f32_16x16x32_bf16 v[100:103], a[180:183], a[140:143], v[100:103]\n\t" "ds_read_b64_tr_b16 a[206:207], %[vr19] offset:10240\n\t"
    "v_mfma_f32_16x16x32_bf16 v[104:107], a[184:187], a[128:131], v[104:107]\n\t" "s_waitcnt vmcnt(2)\n\t" "s_barrier\n\t" "ds_read_b64_tr_b16 a[176:177], %[vr16] offset:16384\n\t"
    "v_mfma_f32_16x16x32_bf16 v[104:107], a[188:191], a[136:139], v[104:107]\n\t" "ds_read_b64_tr_b16 a[178:179], %[vr16] offset:18432\n\t"
    "v_mfma_f32_16x16x32_bf16 v[108:111], a[184:187], a[132:135], v[108:111]\n\t" "ds_read_b64_tr_b16 a[180:181], %[vr16] offset:24576\n\t"
    "v_mfma_f32_16x16x32_bf16 v[108:111], a[188:191], a[140:143], v[108:111]\n\t" "ds_read_b64_tr_b16 a[182:183], %[vr16] offset:26624\n\t" "s_waitcnt lgkmcnt(4)\n\t"
    "v_mfma_f32_16x16x32_bf16 v[112:115], a[192:195], a[128:131], v[112:115]\n\t" "ds_read_b64_tr_b16 a[184:185], %[vr17] offset:16384\n\t"
    "v_mfma_f32_16x16x32_bf16 v[112:115], a[196:199], a[136:139], v[112:115]\n\t" "ds_read_b64_tr_b16 a[186:187], %[vr17] offset:18432\n\t"
    "v_mfma_f32_16x16x32_bf16 v[116:119], a[192:195], a[132:135], v[116:119]\n\t" "ds_read_b64_tr_b16 a[188:189], %[vr17] offset:24576\n\t"
    "v_mfma_f32_16x16x32_bf16 v[116:119], a[196:199], a[140:143], v[116:119]\n\t" "ds_read_b64_tr_b16 a[190:191], %[vr17] offset:26624\n\t"
    "v_mfma_f32_16x16x32_bf16 v[120:123], a[200:203], a[128:131], v[120:123]\n\t" "ds_read_b64_tr_b16 a[192:193], %[vr18] offset:16384\n\t"
    "v_mfma_f32_16x16x32_bf16 v[120:123], a[204:207], a[136:139], v[120:123]\n\t" "ds_read_b64_tr_b16 a[194:195], %[vr18] offset:18432\n\t"
    "v_mfma_f32_16x16x32_bf16 v[124:127], a[200:203], a[132:135], v[124:127]\n\t" "ds_read_b64_tr_b16 a[196:197], %[vr18] offset:24576\n\t"
    "v_mfma_f32_16x16x32_bf16 v[124:127], a[204:207], a[140:143], v[124:127]\n\t" "ds_read_b64_tr_b16 a[198:199], %[vr18] offset:26624\n\t" "s_waitcnt lgkmcnt(4)\n\t"
    "v_mfma_f32_16x16x32_bf16 v[96:99], a[176:179], a[144:147], v[96:99]\n\t" "ds_read_b64_tr_b16 a[200:201], %[vr19] offset:16384\n\t" "global_load_dwordx4 v[208:211], %[vx], %[st] offset:0 sc1\n\t"
    "v_mfma_f32_16x16x32_bf16 v[96:99], a[180:183], a[152:155], v[96:99]\n\t" "ds_read_b64_tr_b16 a[202:203], %[vr19] offset:18432\n\t"
    "v_mfma_f32_16x16x32_bf16 v[100:103], a[176:179], a[148:151], v[100:103]\n\t" "ds_read_b64_tr_b16 a[204:205], %[vr19] offset:24576\n\t"
    "global_load_dwordx4 v[212:215], %[vx], %[st] offset:1024 sc1\n\t" "v_mfma_f32_16x16x32_bf16 v[100:103], a[180:183], a[156:159], v[100:103]\n\t"
    "ds_read_b64_tr_b16 a[206:207], %[vr19] offset:26624\n\t" "v_mfma_f32_16x16x32_bf16 v[104:107], a[184:187], a[144:147], v[104:107]\n\t" "s_waitcnt vmcnt(2)\n\t" "s_barrier\n\t"
    "ds_read_b64_tr_b16 a[176:177], %[vr16] offset:32768\n\t" "global_load_dwordx4 v[216:219], %[vx], %[st] offset:2048 sc1\n\t"
    "v_mfma_f32_16x16x32_bf16 v[104:107], a[188:191], a[152:155], v[104:107]\n\t" "ds_read_b64_tr_b16 a[178:179], %[vr16] offset:34816\n\t"
    "v_mfma_f32_16x16x32_bf16 v[108:111], a[184:187], a[148:151], v[108:111]\n\t" "ds_read_b64_tr_b16 a[180:181], %[vr16] offset:40960\n\t"
    "global_load_dwordx4 v[220:223], %[vx], %[st] offset:3072 sc1\n\t" "v_mfma_f32_16x16x32_bf16 v[108:111], a[188:191], a[156:159], v[108:111]\n\t"
    "ds_read_b64_tr_b16 a[182:183], %[vr16] offset:43008\n\t" "global_load_dword v206, %[vc], %[st] sc1\n\t" "s_waitcnt lgkmcnt(4)\n\t"
    "v_mfma_f32_16x16x32_bf16 v[112:115], a[192:195], a[144:147], v[112:115]\n\t" "ds_read_b64_tr_b16 a[184:185], %[vr17] offset:32768\n\t"
    "v_mfma_f32_16x16x32_bf16 v[112:115], a[196:199], a[152:155], v[112:115]\n\t" "ds_read_b64_tr_b16 a[186:187], %[vr17] offset:34816\n\t"
    "v_mfma_f32_16x16x32_bf16 v[116:119], a[192:195], a[148:151], v[116:119]\n\t" "ds_read_b64_tr_b16 a[188:189], %[vr17] offset:40960\n\t"
    "v_mfma_f32_16x16x32_bf16 v[116:119], a[196:199], a[156:159], v[116:119]\n\t" "ds_read_b64_tr_b16 a[190:191], %[vr17] offset:43008\n\t"
    "v_mfma_f32_16x16x32_bf16 v[120:123], a[200:203], a[144:147], v[120:123]\n\t" "ds_read_b64_tr_b16 a[192:193], %[vr18] offset:32768\n\t"
    "v_mfma_f32_16x16x32_bf16 v[120:123], a[204:207], a[152:155], v[120:123]\n\t" "ds_read_b64_tr_b16 a[194:195], %[vr18] offset:34816\n\t"
    "v_mfma_f32_16x16x32_bf16 v[124:127], a[200:203], a[148:151], v[124:127]\n\t" "ds_read_b64_tr_b16 a[196:197], %[vr18] offset:40960\n\t"
    "v_mfma_f32_16x16x32_bf16 v[124:127], a[204:207], a[156:159], v[124:127]\n\t" "ds_read_b64_tr_b16 a[198:199], %[vr18] offset:43008\n\t" "s_waitcnt lgkmcnt(4)\n\t"
    "v_mfma_f32_16x16x32_bf16 v[96:99], a[176:179], a[160:163], v[96:99]\n\t" "ds_read_b64_tr_b16 a[200:201], %[vr19] offset:32768\n\t"
    "v_mfma_f32_16x16x32_bf16 v[96:99], a[180:183], a[168:171], v[96:99]\n\t" "ds_read_b64_tr_b16 a[202:203], %[vr19] offset:34816\n\t"
    "v_mfma_f32_16x16x32_bf16 v[100:103], a[176:179], a[164:167], v[100:103]\n\t" "ds_read_b64_tr_b16 a[204:205], %[vr19] offset:40960\n\t"
    "v_mfma_f32_16x16x32_bf16 v[100:103], a[180:183], a[172:175], v[100:103]\n\t" "ds_read_b64_tr_b16 a[206:207], %[vr19] offset:43008\n\t"
    "v_mfma_f32_16x16x32_bf16 v[104:107], a[184:187], a[160:163], v[104:107]\n\t" "v_mfma_f32_16x16x32_bf16 v[104:107], a[188:191], a[168:171], v[104:107]\n\t"
    "v_mfma_f32_16x16x32_bf16 v[108:111], a[184:187], a[164:167], v[108:111]\n\t" "v_mfma_f32_16x16x32_bf16 v[108:111], a[188:191], a[172:175], v[108:111]\n\t" "s_waitcnt lgkmcnt(0)\n\t"
    "v_mfma_f32_16x16x32_bf16 v[112:115], a[192:195], a[160:163], v[112:115]\n\t" "v_mfma_f32_16x16x32_bf16 v[112:115], a[196:199], a[168:171], v[112:115]\n\t"
    "v_mfma_f32_16x16x32_bf16 v[116:119], a[192:195], a[164:167], v[116:119]\n\t" "v_mfma_f32_16x16x32_bf16 v[116:119], a[196:199], a[172:175], v[116:119]\n\t"
    "v_mfma_f32_16x16x32_bf16 v[120:123], a[200:203], a[160:163], v[120:123]\n\t" "v_mfma_f32_16x16x32_bf16 v[120:123], a[204:207], a[168:171], v[120:123]\n\t"
    "v_mfma_f32_16x16x32_bf16 v[124:127], a[200:203], a[164:167], v[124:127]\n\t" "v_mfma_f32_16x16x32_bf16 v[124:127], a[204:207], a[172:175], v[124:127]\n\t" "s_mov_b32 s44, 0x400000\n\t"
    ".Lpoll_%=:\n\t" "s_waitcnt vmcnt(0)\n\t" "v_add3_u32 v224, %[t32], v208, v209\n\t" "v_add3_u32 v224, v224, v210, v211\n\t" "v_add3_u32 v224, v224, v212, v213\n\t"
    "v_add3_u32 v224, v224, v214, v215\n\t" "v_add3_u32 v224, v224, v216, v217\n\t" "v_add3_u32 v224, v224, v218, v219\n\t" "v_add3_u32 v224, v224, v220, v221\n\t" "v_add3_u32 v224, v224, v222, v223\n\t"
    "v_cmp_ne_u32_e64 vcc, v206, v224\n\t" "s_nop 4\n\t" "s_cbranch_vccz .Lrdy_%=\n\t" "global_load_dwordx4 v[208:211], %[vx], %[st] offset:0 sc1\n\t"
    "global_load_dwordx4 v[212:215], %[vx], %[st] offset:1024 sc1\n\t" "global_load_dwordx4 v[216:219], %[vx], %[st] offset:2048 sc1\n\t" "global_load_dwordx4 v[220:223], %[vx], %[st] offset:3072 sc1\n\t"
    "global_load_dword v206, %[vc], %[st] sc1\n\t" "s_sub_u32 s44, s44, 1\n\t" "s_cbranch_scc0 .Lpoll_%=\n\t" "s_waitcnt vmcnt(0)\n\t" ".Lrdy_%=:\n\t"
    "v_mfma_f32_4x4x4_16b_bf16 v[96:99], %[id], v[208:209], v[96:99]\n\t" "v_mfma_f32_4x4x4_16b_bf16 v[104:107], %[id], v[210:211], v[104:107]\n\t"
    "v_mfma_f32_4x4x4_16b_bf16 v[112:115], %[id], v[212:213], v[112:115]\n\t" "v_mfma_f32_4x4x4_16b_bf16 v[120:123], %[id], v[214:215], v[120:123]\n\t"
    "v_mfma_f32_4x4x4_16b_bf16 v[100:103], %[id], v[216:217], v[100:103]\n\t" "v_mfma_f32_4x4x4_16b_bf16 v[108:111], %[id], v[218:219], v[108:111]\n\t"
    "v_mfma_f32_4x4x4_16b_bf16 v[116:119], %[id], v[220:221], v[116:119]\n\t" "v_mfma_f32_4x4x4_16b_bf16 v[124:127], %[id], v[222:223], v[124:127]\n\t" "s_mov_b64 s[40:41], %[so]\n\t"
    "v_pk_fma_f32 v[96:97], v[176:177], v[96:97], v[144:145]\n\t" "v_pk_fma_f32 v[98:99], v[178:179], v[98:99], v[146:147]\n\t" "v_cvt_pk_bf16_f32 v96, v96, v97\n\t" "v_cvt_pk_bf16_f32 v97, v98, v99\n\t"
    "ds_write_b64 %[tw0], v[96:97] offset:0\n\t" "v_pk_fma_f32 v[104:105], v[180:181], v[104:105], v[152:153]\n\t" "v_pk_fma_f32 v[106:107], v[182:183], v[106:107], v[154:155]\n\t"
    "v_cvt_pk_bf16_f32 v104, v104, v105\n\t" "v_cvt_pk_bf16_f32 v105, v106, v107\n\t" "ds_write_b64 %[tw1], v[104:105] offset:0\n\t" "v_pk_fma_f32 v[112:113], v[184:185], v[112:113], v[160:161]\n\t"
    "v_pk_fma_f32 v[114:115], v[186:187], v[114:115], v[162:163]\n\t" "v_cvt_pk_bf16_f32 v112, v112, v113\n\t" "v_cvt_pk_bf16_f32 v113, v114, v115\n\t" "ds_write_b64 %[tw2], v[112:113] offset:0\n\t"
    "v_pk_fma_f32 v[120:121], v[188:189], v[120:121], v[168:169]\n\t" "v_pk_fma_f32 v[122:123], v[190:191], v[122:123], v[170:171]\n\t" "v_cvt_pk_bf16_f32 v120, v120, v121\n\t"
    "v_cvt_pk_bf16_f32 v121, v122, v123\n\t" "ds_write_b64 %[tw3], v[120:121] offset:0\n\t" "ds_read_b128 v[224:227], %[tr] offset:0\n\t" "ds_read_b128 v[228:231], %[tr] offset:1024\n\t"
    "v_pk_fma_f32 v[100:101], v[176:177], v[100:101], v[148:149]\n\t" "v_pk_fma_f32 v[102:103], v[178:179], v[102:103], v[150:151]\n\t" "v_cvt_pk_bf16_f32 v100, v100, v101\n\t"
    "v_cvt_pk_bf16_f32 v101, v102, v103\n\t" "ds_write_b64 %[tw0], v[100:101] offset:2048\n\t" "v_pk_fma_f32 v[108:109], v[180:181], v[108:109], v[156:157]\n\t"
    "v_pk_fma_f32 v[110:111], v[182:183], v[110:111], v[158:159]\n\t" "v_cvt_pk_bf16_f32 v108, v108, v109\n\t" "v_cvt_pk_bf16_f32 v109, v110, v111\n\t" "ds_write_b64 %[tw1], v[108:109] offset:2048\n\t"
    "v_pk_fma_f32 v[116:117], v[184:185], v[116:117], v[164:165]\n\t" "v_pk_fma_f32 v[118:119], v[186:187], v[118:119], v[166:167]\n\t" "v_cvt_pk_bf16_f32 v116, v116, v117\n\t"
    "v_cvt_pk_bf16_f32 v117, v118, v119\n\t" "ds_write_b64 %[tw2], v[116:117] offset:2048\n\t" "v_pk_fma_f32 v[124:125], v[188:189], v[124:125], v[172:173]\n\t"
    "v_pk_fma_f32 v[126:127], v[190:191], v[126:127], v[174:175]\n\t" "v_cvt_pk_bf16_f32 v124, v124, v125\n\t" "v_cvt_pk_bf16_f32 v125, v126, v127\n\t" "ds_write_b64 %[tw3], v[124:125] offset:2048\n\t"
    "ds_read_b128 v[232:235], %[tr] offset:2048\n\t" "ds_read_b128 v[236:239], %[tr] offset:3072\n\t" "s_waitcnt lgkmcnt(7)\n\t" "global_store_dwordx4 %[vs], v[224:227], s[40:41]\n\t"
    "s_add_u32 s40, s40, 16384\n\t" "s_addc_u32 s41, s41, 0\n\t" "s_waitcnt lgkmcnt(6)\n\t" "global_store_dwordx4 %[vs], v[228:231], s[40:41]\n\t" "s_add_u32 s40, s40, 16384\n\t"
    "s_addc_u32 s41, s41, 0\n\t" "s_waitcnt lgkmcnt(1)\n\t" "global_store_dwordx4 %[vs], v[232:235], s[40:41]\n\t" "s_add_u32 s40, s40, 16384\n\t" "s_addc_u32 s41, s41, 0\n\t" "s_waitcnt lgkmcnt(0)\n\t"
    "global_store_dwordx4 %[vs], v[236:239], s[40:41]\n\t"
    : [fl] "=&v"(fl), [vb0] "+v"(vb0), [vb1] "+v"(vb1)
    : [vr0] "v"(vr[0]), [vr1] "v"(vr[1]), [vr2] "v"(vr[2]), [vr3] "v"(vr[3]), [vr4] "v"(vr[4]), [vr5] "v"(vr[5]), [vr6] "v"(vr[6]), [vr7] "v"(vr[7]), [vr8] "v"(vr[8]), [vr9] "v"(vr[9]), [vr10] "v"(vr[10]), [vr11] "v"(vr[11]), [vr12] "v"(vr[12]), [vr13] "v"(vr[13]), [vr14] "v"(vr[14]), [vr15] "v"(vr[15]), [vr16] "v"(vr[16]), [vr17] "v"(vr[17]), [vr18] "v"(vr[18]), [vr19] "v"(vr[19]), [vr20] "v"(vr[20]), [vr21] "v"(vr[21]), [vr22] "v"(vr[22]), [vr23] "v"(vr[23]), [vo] "v"(vo), [vx] "v"(vx), [vg] "v"(vg), [vf] "v"(vf), [vc] "v"(vc), [id] "v"(id), [tw0] "v"(tw0), [tw1] "v"(tw1), [tw2] "v"(tw2), [tw3] "v"(tw3), [tr] "v"(tr), [vs] "v"(vs), [ww0] "v"(ww0), [ww1] "v"(ww1), [ww2] "v"(ww2), [t32] "s"((unsigned)tag), [sa] "s"(sa + 4096), [tag] "s"(tag), [wid] "s"(wid), [wr] "s"(wr), [sb] "s"(sb), [sl0] "s"(sl0), [sl1] "s"(sl1), [sg] "s"(sg), [sm] "s"(sm), [st] "s"(st), [sw] "s"(sw), [cd] "s"(cd), [so] "s"(so)
    : "v96", "v97", "v98", "v99", "v100", "v101", "v102", "v103", "v104", "v105", "v106", "v107", "v108", "v109", "v110", "v111", "v112", "v113", "v114", "v115", "v116", "v117", "v118", "v119", "v120", "v121", "v122", "v123", "v124", "v125", "v126", "v127", "v144", "v145", "v146", "v147", "v148", "v149", "v150", "v151", "v152", "v153", "v154", "v155", "v156", "v157", "v158", "v159", "v160", "v161", "v162", "v163", "v164", "v165", "v166", "v167", "v168", "v169", "v170", "v171", "v172", "v173", "v174", "v175", "v176", "v177", "v178", "v179", "v180", "v181", "v182", "v183", "v184", "v185", "v186", "v187", "v188", "v189", "v190", "v191", "v192", "v193", "v194", "v195", "v196", "v197", "v198", "v199", "v200", "v201", "v202", "v203", "v204", "v205", "v206", "v207", "v208", "v209", "v210", "v211", "v212", "v213", "v214", "v215", "v216", "v217", "v218", "v219", "v220", "v221", "v222", "v223", "v224", "v225", "v226", "v227", "v228", "v229", "v230", "v231", "v232", "v233", "v234", "v235", "v236", "v237", "v238", "v239", "v240", "v241", "v242", "v243", "v244", "v245", "v246", "v247", "v248", "v249", "v250", "v251", "v252", "v253", "v254", "v255", "vcc", "a0", "a1", "a2", "a3", "a4", "a5", "a6", "a7", "a8", "a9", "a10", "a11", "a12", "a13", "a14", "a15", "a16", "a17", "a18", "a19", "a20", "a21", "a22", "a23", "a24", "a25", "a26", "a27", "a28", "a29", "a30", "a31", "a32", "a33", "a34", "a35", "a36", "a37", "a38", "a39", "a40", "a41", "a42", "a43", "a44", "a45", "a46", "a47", "a48", "a49", "a50", "a51", "a52", "a53", "a54", "a55", "a56", "a57", "a58", "a59", "a60", "a61", "a62", "a63", "a64", "a65", "a66", "a67", "a68", "a69", "a70", "a71", "a72", "a73", "a74", "a75", "a76", "a77", "a78", "a79", "a80", "a81", "a82", "a83", "a84", "a85", "a86", "a87", "a88", "a89", "a90", "a91", "a92", "a93", "a94", "a95", "a96", "a97", "a98", "a99", "a100", "a101", "a102", "a103", "a104", "a105", "a106", "a107", "a108", "a109", "a110", "a111", "a112", "a113", "a114", "a115", "a116", "a117", "a118", "a119", "a120", "a121", "a122", "a123", "a124", "a125", "a126", "a127", "a128", "a129", "a130", "a131", "a132", "a133", "a134", "a135", "a136", "a137", "a138", "a139", "a140", "a141", "a142", "a143", "a144", "a145", "a146", "a147", "a148", "a149", "a150", "a151", "a152", "a153", "a154", "a155", "a156", "a157", "a158", "a159", "a160", "a161", "a162", "a163", "a164", "a165", "a166", "a167", "a168", "a169", "a170", "a171", "a172", "a173", "a174", "a175", "a176", "a177", "a178", "a179", "a180", "a181", "a182", "a183", "a184", "a185", "a186", "a187", "a188", "a189", "a190", "a191", "a192", "a193", "a194", "a195", "a196", "a197", "a198", "a199", "a200", "a201", "a202", "a203", "a204", "a205", "a206", "a207", "a208", "a209", "a210", "a211", "a212", "a213", "a214", "a215", "a216", "a217", "a218", "a219", "a220", "a221", "a222", "a223", "a224", "a225", "a226", "a227", "a228", "a229", "a230", "a231", "a232", "a233", "a234", "a235", "a236", "a237", "a238", "a239", "a240", "a241", "a242", "a243", "a244", "a245", "a246", "a247", "a248", "a249", "a250", "a251", "a252", "a253", "a254", "a255", "s40", "s41", "s42", "s43", "s44", "s45", "scc", "memory");
  }
  asm volatile("k_down_end:");
}

extern "C" void kernel_launch(void* const* d_in, const int* in_sizes, int n_in, void* d_out, int out_size, void* d_ws, size_t ws_size, hipStream_t stream) {
  const u16* x = (const u16*)d_in[0]; const u16* wqkv = (const u16*)d_in[1]; const u16* wo = (const u16*)d_in[2];
  const u16* wg = (const u16*)d_in[4]; const u16* wu = (const u16*)d_in[5]; const u16* wd = (const u16*)d_in[6];
  const u16* c = (const u16*)d_in[7]; const u16* wmod = (const u16*)d_in[8];
  k_main<<<256, 256, 0, stream>>>(c, wmod, x, wqkv, wo, wg, wu, wd, (u16*)d_out, (char*)d_ws);
}
```

```cpp
#include <hip/hip_runtime.h>
typedef unsigned short u16;
typedef float f32x4 __attribute__((ext_vector_type(4)));
typedef float f32x2 __attribute__((ext_vector_type(2)));
typedef unsigned u32x4 __attribute__((ext_vector_type(4)));
typedef unsigned u32x2 __attribute__((ext_vector_type(2)));
#define LAS __attribute__((address_space(3)))
#define DI __device__ __forceinline__

constexpr int S_ = 2048, D_ = 1024, F_ = 3584;

constexpr unsigned W_GB = 0, W_EF = 1 << 16, W_TAGS = W_EF + (1 << 15), W_PART = W_TAGS + (1 << 18), W_MODP = W_PART + (1 << 15), W_UVP = W_MODP + (1 << 16),
                 W_STATS = W_UVP + (1 << 19), W_O = W_STATS + 16 * S_ * 8, W_Y = W_O + S_ * D_ * 2, W_HH = W_Y + S_ * D_ * 2,
                 W_XCH = W_HH + S_ * F_ * 2;

DI unsigned f_vb(int wid, int lane) { const int r = wid * 8 + (lane >> 3), c = lane & 7; return (unsigned)(r * 2048 + (((((c >> 1) ^ ((r >> 1) & 3)) << 1) | (c & 1)) << 4)); }
DI unsigned f_vr(int lane, int j) { const int row = lane >> 2; return (unsigned)(row * 128 + ((j ^ ((row >> 1) & 3)) << 5) + (lane & 3) * 8); }

#define ORD 0
#define EAR 1
#define FS 256
#define FA(i) ((i) * FS)
constexpr int OF = 0, YF = 32 * FS, HF = 64 * FS, EP = 96 * FS;
extern "C" __global__ __launch_bounds__(256, 1) void k_main(const u16* __restrict__ c, const u16* __restrict__ wmod, const u16* __restrict__ x, const u16* __restrict__ w, const u16* __restrict__ wo,
                                                            const u16* __restrict__ wg, const u16* __restrict__ wu, const u16* __restrict__ pfw, u16* __restrict__ out, char* ws) {
  const char* code; int len;
  asm volatile("s_getpc_b64 %0\n.Lpc%=:\n\ts_mov_b32 %1, k_down_end-.Lpc%=\n\ts_mov_b64 exec, 3\n\tglobal_load_dword a255, %2, %0\n\ts_mov_b64 exec, -1"
               : "=&s"(code), "=s"(len) : "v"(((blockIdx.x & ~7u) + (threadIdx.x >> 5) + (threadIdx.x & 1)) * 128) : "a255");
  asm volatile("" :: "s"(c), "s"(wmod), "s"(x), "s"(w), "s"(wo), "s"(wg), "s"(wu), "s"(pfw), "s"(out), "s"(ws));
  constexpr int SCT = 16384, PH = 18944, RING = 20480, HALO = 131072;
  __shared__ __attribute__((aligned(1024))) char smem[163840];
  LAS char* lds = (LAS char*)smem;
  const unsigned ldsb = (unsigned)(size_t)lds;
  int wid = __builtin_amdgcn_readfirstlane(threadIdx.x >> 6);
  int bx = blockIdx.x;
#define ROT (((loc >> 2) + 1 * (xcd & 6)) & 7)
#define KD ((loc >> 2) * 56 % 56)
#define IDX() const int xcd = bx & 7, loc = bx >> 3, gk = loc >> 3, gc = xcd * 8 + (loc & 7), rb = (xcd >> 1) * 4 + (loc & 3), cb = (xcd & 1) * 8 + (loc >> 2); const unsigned sl1 = ldsb + wid * 1024; const int rot = ROT, co = rot * 2 + (xcd & 1); const char* so = (const char*)(wo + co * 64)
  unsigned long long tag; unsigned cv; float mu[2]; unsigned vbo; const char* cd; int tid = threadIdx.x;
  { IDX();
  const unsigned vs = ((threadIdx.x & 63) >> 2) * 12288 + (((threadIdx.x & 3) ^ ((threadIdx.x >> 4) & 2)) << 4), hf = 0 & loc, sl5 = ldsb + wid * 4096 + (hf ^ 1) * 2048;
  const char* sw2 = (const char*)wmod + (unsigned)((gk * 256 + wid * 64 + (hf ^ 1) * 32) * 12288 + gc * 64);
  { const unsigned vc = threadIdx.x * 2, sl4 = ldsb + wid * 4096 + hf * 2048;
    const u16* sc = c + gk * 256; const char* sw = (const char*)wmod + (unsigned)((gk * 256 + wid * 64 + hf * 32) * 12288 + gc * 64); const char* st = ws + W_TAGS + EP * 8;
    asm volatile( "global_load_ushort v204, %[vc], %[sc]\n\t" "s_mov_b64 s[40:41], %[sw]\n\t" "s_add_u32 m0, %[sl4], 0\n\t" "s_nop 0\n\t" "global_load_lds_dwordx4 %[vs], s[40:41] nt\n\t"
    "s_add_u32 s40, s40, 196608\n\t" "s_addc_u32 s41, s41, 0\n\t" "s_add_u32 m0, %[sl4], 1024\n\t" "s_nop 0\n\t" "global_load_lds_dwordx4 %[vs], s[40:41] nt\n\t" "s_add_u32 s40, s40, 196608\n\t"
    "s_addc_u32 s41, s41, 0\n\t" "s_add_u32 m0, %[sl4], 2048\n\t" "s_nop 0\n\t" "global_load_lds_dwordx4 %[vs], s[40:41] nt\n\t" "s_add_u32 s40, s40, 196608\n\t" "s_addc_u32 s41, s41, 0\n\t"
    "s_add_u32 m0, %[sl4], 3072\n\t" "s_nop 0\n\t" "global_load_lds_dwordx4 %[vs], s[40:41] nt\n\t" "s_load_dwordx2 s[62:63], %[st], 0x0\n\t"
    : "=&{v204}"(cv), "=&{s[62:63]}"(tag)
    : [vc] "v"(vc), [vs] "v"(vs), [sc] "s"(sc), [sw] "s"(sw), [sl4] "s"(sl4), [st] "s"(st)
    : "s40", "s41", "scc", "memory");
  }
  { asm volatile("" : "+v"(tid));
    int lane = tid & 63;
    const unsigned sl = ldsb + (wid & 1) * 1024 + (wid >> 1) * 6144;
    const unsigned dx = (lane >> 3) * 2048 + (((lane & 7) ^ (lane >> 3)) << 4); unsigned d0 = dx + ((rb ? 16 : 0) + wid * 32) * 2048, d1 = d0 + 16384, d2 = d0 + 32768, d3 = d0 + 49152;
    const char* xb0 = (const char*)x + (size_t)(rb ? rb * 128 - 16 : 0) * 2048;
    const unsigned xl = (unsigned)(size_t)xb0, xh_ = (unsigned)((size_t)xb0 >> 32), k0 = rot * 256;
    asm volatile("s_add_u32 s45, %[k0], 0\n\t" "s_and_b32 s45, s45, 0x7ff\n\t" "s_add_u32 s64, %[xl], s45\n\t" "s_addc_u32 s65, %[xh_], 0\n\t" "s_add_u32 m0, %[sl], 20480\n\t" "s_nop 0\n\t"
    "global_load_lds_dwordx4 v196, s[64:65] offset:0\n\t" "s_add_u32 m0, %[sl], 32768\n\t" "s_nop 0\n\t" "global_load_lds_dwordx4 v197, s[64:65] offset:0\n\t" "s_add_u32 m0, %[sl], 22528\n\t"
    "s_nop 0\n\t" "global_load_lds_dwordx4 v198, s[64:65] offset:0\n\t" "s_add_u32 m0, %[sl], 34816\n\t" "s_nop 0\n\t" "global_load_lds_dwordx4 v199, s[64:65] offset:0\n\t"
    :: "{v196}"(d0), "{v197}"(d1), "{v198}"(d2), "{v199}"(d3), [sl] "s"(sl), [xl] "s"(xl), [xh_] "s"(xh_), [k0] "s"(k0)
    : "s40", "s41", "s45", "s64", "s65", "scc", "memory");
    asm volatile("" : "+v"(tid)); lane = tid & 63;
    const int ww = loc * 4 + wid; unsigned tv;
    { const int g = lane >> 4;
      const unsigned vsr = ldsb + (8 * g + ((lane >> 2) & 3)) * 64 + (((wid ^ g) & 1) << 5) + (lane & 3) * 8, vsc = ldsb + g * 16, vcw = ldsb + SCT + tid * 2;
      const char* su = ws + W_PART + (gk * 512 + gc * 8 + wid * 4) * 16 - ldsb; const u16* sq = w;
      const unsigned t0 = ((ww * 2 + (lane & 1)) * 4 + (xcd >> 1)) * 6144 + (xcd & 1) * 1024 + ((384 - (unsigned)(size_t)w) & 896) + (lane >> 1) * 2048;
      asm volatile( "s_mov_b32 s54, 0x07060503\n\t" "s_mov_b32 s55, 0x07060502\n\t" "s_mov_b32 s56, 0x07060501\n\t" "s_mov_b32 s60, 0x00010001\n\t" "s_mov_b32 s61, s60\n\t" "s_add_u32 s45, %[k0], 0\n\t"
    "s_and_b32 s45, s45, 0x7ff\n\t" "s_add_u32 s64, %[xl], s45\n\t" "s_addc_u32 s65, %[xh_], 0\n\t" "s_add_u32 m0, %[sl], 24448\n\t" "s_nop 0\n\t" "global_load_lds_dwordx4 v196, s[64:65] offset:128\n\t"
    "s_add_u32 m0, %[sl], 36736\n\t" "s_nop 0\n\t" "global_load_lds_dwordx4 v197, s[64:65] offset:128\n\t" "s_add_u32 m0, %[sl], 44928\n\t" "s_nop 0\n\t"
    "global_load_lds_dwordx4 v198, s[64:65] offset:128\n\t" "s_add_u32 m0, %[sl], 57216\n\t" "s_nop 0\n\t" "global_load_lds_dwordx4 v199, s[64:65] offset:128\n\t" "s_mov_b64 exec, 63\n\t"
    "global_load_dword v205, %[t0], %[sq]\n\t" "s_mov_b64 exec, -1\n\t" "s_waitcnt vmcnt(13)\n\t" "v_lshlrev_b32 v204, 16, v204\n\t" "v_mul_f32 v206, 0xbfb8aa3b, v204\n\t"
    "v_exp_f32 v206, v206\n\t" "s_nop 0\n\t" "v_add_f32 v206, 1.0, v206\n\t" "v_rcp_f32 v206, v206\n\t" "s_nop 0\n\t" "v_mul_f32 v204, v204, v206\n\t" "v_cvt_pk_bf16_f32 v204, v204, v204\n\t"
    "ds_write_b16 v233, v204\n\t" "s_waitcnt vmcnt(9)\n\t" "s_waitcnt lgkmcnt(0)\n\t" "s_barrier\n\t" "s_add_u32 s62, s62, 1\n\t" "s_addc_u32 s63, s63, 0\n\t"
    "s_cmp_gt_u32 %[wid], 1\n\t" "s_cbranch_scc1 .Lns_%=\n\t" "ds_read_b64_tr_b16 v[48:49], v228 offset:0\n\t" "ds_read_b64_tr_b16 v[50:51], v228 offset:256\n\t"
    "ds_read_b128 v[80:83], v229 offset:16384\n\t" "ds_read_b64_tr_b16 v[52:53], v228 offset:2048\n\t" "ds_read_b64_tr_b16 v[54:55], v228 offset:2304\n\t" "ds_read_b128 v[84:87], v229 offset:16448\n\t"
    "ds_read_b64_tr_b16 v[56:57], v228 offset:4096\n\t" "ds_read_b64_tr_b16 v[58:59], v228 offset:4352\n\t" "ds_read_b128 v[88:91], v229 offset:16512\n\t"
    "ds_read_b64_tr_b16 v[60:61], v228 offset:6144\n\t" "ds_read_b64_tr_b16 v[62:63], v228 offset:6400\n\t" "ds_read_b128 v[92:95], v229 offset:16576\n\t" "s_waitcnt lgkmcnt(9)\n\t"
    "v_mfma_f32_16x16x32_bf16 v[112:115], v[48:51], v[80:83], 0\n\t" "ds_read_b64_tr_b16 v[64:65], v228 offset:8192\n\t" "ds_read_b64_tr_b16 v[66:67], v228 offset:8448\n\t"
    "ds_read_b128 v[96:99], v229 offset:16640\n\t" "s_waitcnt lgkmcnt(9)\n\t" "v_mfma_f32_16x16x32_bf16 v[112:115], v[52:55], v[84:87], v[112:115]\n\t" "ds_read_b64_tr_b16 v[68:69], v228 offset:10240\n\t"
    "ds_read_b64_tr_b16 v[70:71], v228 offset:10496\n\t" "ds_read_b128 v[100:103], v229 offset:16704\n\t" "s_waitcnt lgkmcnt(9)\n\t"
    "v_mfma_f32_16x16x32_bf16 v[112:115], v[56:59], v[88:91], v[112:115]\n\t" "ds_read_b64_tr_b16 v[72:73], v228 offset:12288\n\t" "ds_read_b64_tr_b16 v[74:75], v228 offset:12544\n\t"
    "ds_read_b128 v[104:107], v229 offset:16768\n\t" "s_waitcnt lgkmcnt(9)\n\t" "v_mfma_f32_16x16x32_bf16 v[112:115], v[60:63], v[92:95], v[112:115]\n\t"
    "ds_read_b64_tr_b16 v[76:77], v228 offset:14336\n\t" "ds_read_b64_tr_b16 v[78:79], v228 offset:14592\n\t" "ds_read_b128 v[108:111], v229 offset:16832\n\t" "s_waitcnt lgkmcnt(9)\n\t"
    "v_mfma_f32_16x16x32_bf16 v[112:115], v[64:67], v[96:99], v[112:115]\n\t" "s_waitcnt lgkmcnt(6)\n\t" "v_mfma_f32_16x16x32_bf16 v[112:115], v[68:71], v[100:103], v[112:115]\n\t"
    "s_waitcnt lgkmcnt(3)\n\t" "v_mfma_f32_16x16x32_bf16 v[112:115], v[72:75], v[104:107], v[112:115]\n\t" "s_waitcnt lgkmcnt(0)\n\t"
    "v_mfma_f32_16x16x32_bf16 v[112:115], v[76:79], v[108:111], v[112:115]\n\t" "s_nop 7\n\t" "v_perm_b32 v116, v112, v115, s54\n\t" "v_perm_b32 v117, v113, v115, s55\n\t"
    "v_perm_b32 v118, v114, v115, s56\n\t" "v_xad_u32 v119, v116, v117, s62\n\t" "s_mov_b64 exec, s[60:61]\n\t" "v_xad_u32 v119, v118, v117, v119\n\t"
    "global_store_dwordx4 v229, v[116:119], %[su] sc1\n\t" "s_mov_b64 exec, -1\n\t" ".Lns_%=:\n\t"
    : "+{v204}"(cv), "=&{v205}"(tv), "+{s[62:63]}"(tag)
    : "{v196}"(d0), "{v197}"(d1), "{v198}"(d2), "{v199}"(d3), [sl] "s"(sl), [xl] "s"(xl), [xh_] "s"(xh_), [k0] "s"(k0), "{v233}"(vcw), "{v228}"(vsr), "{v229}"(vsc), [t0] "v"(t0), [sq] "s"(sq), [su] "s"(su), [wid] "s"(wid)
    : "v48", "v49", "v50", "v51", "v52", "v53", "v54", "v55", "v56", "v57", "v58", "v59", "v60", "v61", "v62", "v63", "v64", "v65", "v66", "v67", "v68", "v69", "v70", "v71", "v72", "v73", "v74", "v75", "v76", "v77", "v78", "v79", "v80", "v81", "v82", "v83", "v84", "v85", "v86", "v87", "v88", "v89", "v90", "v91", "v92", "v93", "v94", "v95", "v96", "v97", "v98", "v99", "v100", "v101", "v102", "v103", "v104", "v105", "v106", "v107", "v108", "v109", "v110", "v111", "v112", "v113", "v114", "v115", "v116", "v117", "v118", "v119", "v206", "s45", "s54", "s55", "s56", "s60", "s61", "s64", "s65", "scc", "memory");
    }
    asm volatile("" : "+v"(tid)); lane = tid & 63;
    const int fr = lane & 15, g = lane >> 4, q = (lane >> 2) & 3, ls = ((tid >> 3) - 4 * rot) & 31;
    unsigned vb = (unsigned)((rot * 128 + wid * 8 + (lane >> 3)) * 6144 + (((lane & 7) ^ ((((lane >> 4) & 1) | ((wid & 1) << 1)) << 1)) << 4));
    unsigned dh0 = dx, dh1 = dh0 + 16384, xr0 = sl + RING + ((lane >> 3) & 1) * 12288 + (lane & 7) * 128 + ((g ^ (lane & 7)) << 4), xr1 = xr0 ^ 64;
    unsigned vh = ldsb + HALO + (g * 16 + (fr ^ g)) * 16, vdt = ldsb + (lane & 3) * 64 + g * 16, vhd = vdt + wid * 512, vhx = vh + wid * 2048;
    unsigned vg = tid * 16 + W_PART + 4096, vtw = ldsb + ls * 256 + (tid & 7) * 8, vhw = ldsb + HALO + ls * 1024 + ((tid >> 1) & 3) * 272 + (tid & 1) * 8, vs8 = ldsb + SCT + (tid >> 3) * 16;
    unsigned vpr = ldsb + PH + fr * 8, vph = vpr + wid * 128, vm = (tid >> 3) * 98304 + (tid & 7) * 16; vbo = f_vb(wid, lane);
    unsigned vr0 = ldsb + RING + (g & 1) * 1024 + (g >> 1) * 6144 + q * 128 + ((wid ^ ((q >> 1) | ((g & 1) << 1))) << 5) + (lane & 3) * 8, vr1 = vr0 ^ 32, vr2 = vr0 ^ 64, vr3 = vr0 ^ 96;
    unsigned vr4 = vr0 + 65536, vr5 = vr1 + 65536, vr6 = vr2 + 65536, vr7 = vr3 + 65536;
    const unsigned l0 = ldsb, lh = ldsb + HALO, lt = lh + 24576 + wid * 128, lr = lh + 22528 + wid * 256, w128 = wid * 128, w4k = wid * 4096, xw = w4k + 4096 + (wid == 1 ? 2048 : 0), xh = (wid >> 1) * 2048 + (wid & 1) * 8, ox = (wid >> 1) * 2048 + W_O + (rb * 4 + wid) * 65536 + cb * 4096, kh = k0 + w128;
    const int rw = rb * 4 + wid;
    const char* sb = (const char*)(w + 1024 + cb * 64); const u16* sm = wmod + (size_t)gk * 256 * 6144 + 2048 + gc * 64;
    const unsigned sf = W_TAGS + OF * 8 + FA(rb * 16 + cb);
    unsigned o0 = min(32768 + (ww * 8 + lane) * 128, len - 4), o1 = sf + (lane & 1) * YF * 8, o2 = (lane & 1) * 128 + W_TAGS + HF * 8 + FA(((xcd & 1) * 4 + (loc & 3)) * 32 + (xcd >> 1) * 8 + (loc >> 2));
    unsigned o3 = ((ww * 2 + (lane & 1)) * 4 + (xcd >> 1)) * 2048 + (xcd & 1) * 128 + (lane & 14) * 128;
    asm volatile("" : "+v"(o0), "+v"(o1), "+v"(o2), "+v"(o3));
    const char* t2 = (lane < 8 ? code : lane < 12 ? ws : (const char*)wo) + (lane < 8 ? o0 : lane < 10 ? o1 : lane < 12 ? o2 : o3);
    const int c3 = (3 - ((unsigned)(size_t)w >> 7)) & 7, w2 = ww - ((loc >> 2) > c3 ? 16 : 0), ix = (w2 >= 56 ? w2 - 56 : w2) * 16 * 1 + lane, r14 = (ix * 4682) >> 16;
    unsigned t3 = (xcd & 1) * 512 * 7168 + r14 * (7168 - 1792) + (xcd >> 1) * 1792 + ix * 128; const u16* pf = w2 >= 56 ? wu : wg;
    unsigned long long tm = (loc >> 2) == c3 ? 0ull : ~0ull >> (64 - 16 * 1);
    cd = code + min(ww * 1024, len - 1024);
    { const char* cd1 = cd; const char* cd = cd1 - wid * 1024;
    asm volatile(".p2align 3\n\t" "s_mov_b32 s43, 0xffa30000\n\t" "s_mov_b32 s46, 0x00100001\n\t" "s_mov_b32 s47, 0x10000100\n\t" "s_lshl_b64 s[48:49], s[46:47], 1\n\t" "s_lshl_b64 s[50:51], s[46:47], 2\n\t"
    "s_lshl_b64 s[52:53], s[46:47], 3\n\t" "s_mov_b32 s57, 0x04000c0c\n\t" "s_mov_b32 s58, 0x0706000c\n\t" "s_add_u32 m0, %[sl], 96256\n\t" "s_nop 0\n\t"
    "global_load_lds_dwordx4 v0, %[sb] offset:-2048\n\t" "global_load_lds_dwordx4 v0, %[sb] offset:0\n\t" "global_load_lds_dwordx4 v0, %[sb] offset:2048\n\t" "v_add_u32 v0, 0x30000, v0\n\t"
    "s_add_u32 s45, %[k0], 256\n\t" "s_and_b32 s45, s45, 0x7ff\n\t" "s_add_u32 s64, %[xl], s45\n\t" "s_addc_u32 s65, %[xh_], 0\n\t" "s_add_u32 m0, %[sl], 47104\n\t" "s_nop 0\n\t"
    "global_load_lds_dwordx4 v196, s[64:65] offset:0\n\t" "s_add_u32 m0, %[sl], 59392\n\t" "s_nop 0\n\t" "global_load_lds_dwordx4 v197, s[64:65] offset:0\n\t" "s_add_u32 m0, %[sl], 49152\n\t"
    "s_nop 0\n\t" "global_load_lds_dwordx4 v198, s[64:65] offset:0\n\t" "s_add_u32 m0, %[sl], 61440\n\t" "s_nop 0\n\t" "global_load_lds_dwordx4 v199, s[64:65] offset:0\n\t"
    "s_add_u32 m0, %[sl], 69504\n\t" "s_nop 0\n\t" "global_load_lds_dwordx4 v196, s[64:65] offset:128\n\t" "s_add_u32 m0, %[sl], 81792\n\t" "s_nop 0\n\t"
    "global_load_lds_dwordx4 v197, s[64:65] offset:128\n\t" "s_add_u32 m0, %[sl], 71552\n\t" "s_nop 0\n\t" "global_load_lds_dwordx4 v198, s[64:65] offset:128\n\t" "s_add_u32 m0, %[sl], 83840\n\t"
    "s_nop 0\n\t" "global_load_lds_dwordx4 v199, s[64:65] offset:128\n\t" "ds_read_b128 v[252:255], v234\n\t" "v_mov_b32 v112, 0x3f803f80\n\t" "v_mov_b32 v113, 0x3f803f80\n\t"
    "v_mov_b32 v114, 0x3f803f80\n\t" "v_mov_b32 v115, 0x3f803f80\n\t" "s_mov_b32 s60, 0xfffcfffc\n\t" "s_mov_b32 s61, s60\n\t" "s_mov_b32 s44, 0x100000\n\t" "s_barrier\n\t"
    "s_waitcnt vmcnt(16)\n\t" "ds_read_b128 a[0:3], v202 offset:0\n\t" "ds_read_b128 a[4:7], v202 offset:2048\n\t" "ds_read_b128 a[8:11], v203 offset:0\n\t"
    "ds_read_b128 a[12:15], v203 offset:2048\n\t" "s_waitcnt lgkmcnt(0)\n\t" "s_add_u32 s45, %[kh], 0\n\t" "s_and_b32 s45, s45, 0x7ff\n\t" "s_add_u32 s40, %[xl], s45\n\t" "s_addc_u32 s41, %[xh_], 0\n\t"
    "s_add_u32 m0, %[sl], 20480\n\t" "s_nop 0\n\t" "global_load_lds_dwordx4 v200, s[40:41]\n\t" "s_add_u32 m0, %[sl], 32768\n\t" "s_nop 0\n\t" "global_load_lds_dwordx4 v201, s[40:41]\n\t"
    "s_add_u32 s45, %[kh], 512\n\t" "s_and_b32 s45, s45, 0x7ff\n\t" "s_add_u32 s40, %[xl], s45\n\t" "s_addc_u32 s41, %[xh_], 0\n\t" "s_add_u32 m0, %[sl], 22528\n\t" "s_nop 0\n\t"
    "global_load_lds_dwordx4 v200, s[40:41]\n\t" "s_add_u32 m0, %[sl], 34816\n\t" "s_nop 0\n\t" "global_load_lds_dwordx4 v201, s[40:41]\n\t" "s_waitcnt vmcnt(16)\n\t"
    "v_mfma_f32_16x16x32_bf16 v[116:119], v[112:115], a[0:3], 0\n\t" "ds_read_b128 a[16:19], v202 offset:4096\n\t" "ds_read_b128 a[20:23], v202 offset:24576\n\t"
    "v_mfma_f32_16x16x32_bf16 v[128:131], a[0:3], a[0:3], 0\n\t" "ds_read_b128 a[24:27], v203 offset:4096\n\t" "ds_read_b128 a[28:31], v203 offset:24576\n\t"
    "v_mfma_f32_16x16x32_bf16 v[120:123], v[112:115], a[4:7], 0\n\t" "s_add_u32 s45, %[kh], 1024\n\t" "s_and_b32 s45, s45, 0x7ff\n\t" "s_add_u32 s40, %[xl], s45\n\t" "s_addc_u32 s41, %[xh_], 0\n\t"
    "v_mfma_f32_16x16x32_bf16 v[132:135], a[4:7], a[4:7], 0\n\t" "v_mfma_f32_16x16x32_bf16 v[116:119], v[112:115], a[8:11], v[116:119]\n\t"
    "v_mfma_f32_16x16x32_bf16 v[128:131], a[8:11], a[8:11], v[128:131]\n\t" "s_waitcnt lgkmcnt(0)\n\t" "s_add_u32 m0, %[sl], 24576\n\t"
    "v_mfma_f32_16x16x32_bf16 v[120:123], v[112:115], a[12:15], v[120:123]\n\t" "global_load_lds_dwordx4 v200, s[40:41]\n\t" "s_add_u32 m0, %[sl], 36864\n\t"
    "v_mfma_f32_16x16x32_bf16 v[132:135], a[12:15], a[12:15], v[132:135]\n\t" "global_load_lds_dwordx4 v201, s[40:41]\n\t" "s_add_u32 s45, %[kh], 1536\n\t" "s_and_b32 s45, s45, 0x7ff\n\t"
    "s_add_u32 s40, %[xl], s45\n\t" "s_addc_u32 s41, %[xh_], 0\n\t" "s_add_u32 m0, %[sl], 45056\n\t" "s_nop 0\n\t" "global_load_lds_dwordx4 v200, s[40:41]\n\t" "s_add_u32 m0, %[sl], 57344\n\t"
    "s_nop 0\n\t" "global_load_lds_dwordx4 v201, s[40:41]\n\t" "s_waitcnt vmcnt(12)\n\t" "v_mfma_f32_16x16x32_bf16 v[116:119], v[112:115], a[16:19], v[116:119]\n\t"
    "ds_read_b128 a[32:35], v202 offset:26624\n\t" "ds_read_b128 a[36:39], v202 offset:28672\n\t" "v_mfma_f32_16x16x32_bf16 v[128:131], a[16:19], a[16:19], v[128:131]\n\t"
    "ds_read_b128 a[40:43], v203 offset:26624\n\t" "ds_read_b128 a[44:47], v203 offset:28672\n\t" "v_mfma_f32_16x16x32_bf16 v[120:123], v[112:115], a[20:23], v[120:123]\n\t"
    "s_add_u32 s45, %[k0], 512\n\t" "s_and_b32 s45, s45, 0x7ff\n\t" "s_add_u32 s64, %[xl], s45\n\t" "s_addc_u32 s65, %[xh_], 0\n\t"
    "v_mfma_f32_16x16x32_bf16 v[132:135], a[20:23], a[20:23], v[132:135]\n\t" "v_mfma_f32_16x16x32_bf16 v[116:119], v[112:115], a[24:27], v[116:119]\n\t"
    "v_mfma_f32_16x16x32_bf16 v[128:131], a[24:27], a[24:27], v[128:131]\n\t" "s_waitcnt lgkmcnt(0)\n\t" "s_add_u32 m0, %[sl], 47104\n\t"
    "v_mfma_f32_16x16x32_bf16 v[120:123], v[112:115], a[28:31], v[120:123]\n\t" "global_load_lds_dwordx4 v196, s[64:65] offset:0\n\t" "s_add_u32 m0, %[sl], 59392\n\t"
    "v_mfma_f32_16x16x32_bf16 v[132:135], a[28:31], a[28:31], v[132:135]\n\t" "global_load_lds_dwordx4 v197, s[64:65] offset:0\n\t" "s_add_u32 m0, %[sl], 49152\n\t" "s_nop 0\n\t"
    "global_load_lds_dwordx4 v198, s[64:65] offset:0\n\t" "s_add_u32 m0, %[sl], 61440\n\t" "s_nop 0\n\t" "global_load_lds_dwordx4 v199, s[64:65] offset:0\n\t" "s_waitcnt vmcnt(12)\n\t"
    "v_mfma_f32_16x16x32_bf16 v[116:119], v[112:115], a[32:35], v[116:119]\n\t" "ds_read_b128 a[48:51], v202 offset:49152\n\t" "ds_read_b128 a[52:55], v202 offset:51200\n\t"
    "v_mfma_f32_16x16x32_bf16 v[128:131], a[32:35], a[32:35], v[128:131]\n\t" "ds_read_b128 a[56:59], v203 offset:49152\n\t" "ds_read_b128 a[60:63], v203 offset:51200\n\t"
    "v_mfma_f32_16x16x32_bf16 v[120:123], v[112:115], a[36:39], v[120:123]\n\t" "v_mfma_f32_16x16x32_bf16 v[132:135], a[36:39], a[36:39], v[132:135]\n\t"
    "v_mfma_f32_16x16x32_bf16 v[116:119], v[112:115], a[40:43], v[116:119]\n\t" "v_mfma_f32_16x16x32_bf16 v[128:131], a[40:43], a[40:43], v[128:131]\n\t" "s_waitcnt lgkmcnt(0)\n\t"
    "s_add_u32 m0, %[sl], 69504\n\t" "v_mfma_f32_16x16x32_bf16 v[120:123], v[112:115], a[44:47], v[120:123]\n\t" "global_load_lds_dwordx4 v196, s[64:65] offset:128\n\t" "s_add_u32 m0, %[sl], 81792\n\t"
    "v_mfma_f32_16x16x32_bf16 v[132:135], a[44:47], a[44:47], v[132:135]\n\t" "global_load_lds_dwordx4 v197, s[64:65] offset:128\n\t" "s_add_u32 m0, %[sl], 71552\n\t" "s_nop 0\n\t"
    "global_load_lds_dwordx4 v198, s[64:65] offset:128\n\t" "s_add_u32 m0, %[sl], 83840\n\t" "s_nop 0\n\t" "global_load_lds_dwordx4 v199, s[64:65] offset:128\n\t" "s_waitcnt vmcnt(12)\n\t"
    "v_mfma_f32_16x16x32_bf16 v[116:119], v[112:115], a[48:51], v[116:119]\n\t" "ds_read_b128 v[48:51], v202 offset:0\n\t" "ds_read_b128 v[20:23], v202 offset:2048\n\t"
    "v_mfma_f32_16x16x32_bf16 v[128:131], a[48:51], a[48:51], v[128:131]\n\t" "ds_read_b128 v[52:55], v203 offset:0\n\t" "ds_read_b128 v[24:27], v203 offset:2048\n\t"
    "v_mfma_f32_16x16x32_bf16 v[120:123], v[112:115], a[52:55], v[120:123]\n\t" "s_add_u32 s45, %[k0], 768\n\t" "s_and_b32 s45, s45, 0x7ff\n\t" "s_add_u32 s64, %[xl], s45\n\t"
    "s_addc_u32 s65, %[xh_], 0\n\t" "v_mfma_f32_16x16x32_bf16 v[132:135], a[52:55], a[52:55], v[132:135]\n\t" "v_mfma_f32_16x16x32_bf16 v[116:119], v[112:115], a[56:59], v[116:119]\n\t"
    "v_mfma_f32_16x16x32_bf16 v[128:131], a[56:59], a[56:59], v[128:131]\n\t" "s_waitcnt lgkmcnt(0)\n\t" "s_add_u32 m0, %[sl], 20480\n\t"
    "v_mfma_f32_16x16x32_bf16 v[120:123], v[112:115], a[60:63], v[120:123]\n\t" "global_load_lds_dwordx4 v196, s[64:65] offset:0\n\t" "s_add_u32 m0, %[sl], 32768\n\t"
    "v_mfma_f32_16x16x32_bf16 v[132:135], a[60:63], a[60:63], v[132:135]\n\t" "global_load_lds_dwordx4 v197, s[64:65] offset:0\n\t" "s_add_u32 m0, %[sl], 22528\n\t" "s_nop 0\n\t"
    "global_load_lds_dwordx4 v198, s[64:65] offset:0\n\t" "s_add_u32 m0, %[sl], 34816\n\t" "s_nop 0\n\t" "global_load_lds_dwordx4 v199, s[64:65] offset:0\n\t" "s_waitcnt vmcnt(12)\n\t"
    "v_mfma_f32_16x16x32_bf16 v[124:127], v[112:115], v[48:51], 0\n\t" "ds_read_b128 v[28:31], v202 offset:4096\n\t" "ds_read_b128 v[36:39], v202 offset:24576\n\t"
    "v_mfma_f32_16x16x32_bf16 v[136:139], v[48:51], v[48:51], 0\n\t" "ds_read_b128 v[32:35], v203 offset:4096\n\t" "ds_read_b128 v[40:43], v203 offset:24576\n\t"
    "v_mfma_f32_16x16x32_bf16 v[124:127], v[112:115], v[20:23], v[124:127]\n\t" "v_mfma_f32_16x16x32_bf16 v[136:139], v[20:23], v[20:23], v[136:139]\n\t"
    "v_mfma_f32_16x16x32_bf16 v[124:127], v[112:115], v[52:55], v[124:127]\n\t" "v_mfma_f32_16x16x32_bf16 v[136:139], v[52:55], v[52:55], v[136:139]\n\t" "s_waitcnt lgkmcnt(0)\n\t"
    "s_add_u32 m0, %[sl], 24448\n\t" "v_mfma_f32_16x16x32_bf16 v[124:127], v[112:115], v[24:27], v[124:127]\n\t" "global_load_lds_dwordx4 v196, s[64:65] offset:128\n\t" "s_add_u32 m0, %[sl], 36736\n\t"
    "v_mfma_f32_16x16x32_bf16 v[136:139], v[24:27], v[24:27], v[136:139]\n\t" "global_load_lds_dwordx4 v197, s[64:65] offset:128\n\t" "s_add_u32 m0, %[sl], 44928\n\t" "s_nop 0\n\t"
    "global_load_lds_dwordx4 v198, s[64:65] offset:128\n\t" "s_add_u32 m0, %[sl], 57216\n\t" "s_nop 0\n\t" "global_load_lds_dwordx4 v199, s[64:65] offset:128\n\t" "s_mov_b64 s[40:41], %[ws]\n\t"
    "global_load_dwordx4 v[80:83], v230, s[40:41] offset:-4096 sc1\n\t" "global_load_dwordx4 v[96:99], v230, s[40:41] offset:0 sc1\n\t" "s_add_u32 s40, s40, 8192\n\t" "s_addc_u32 s41, s41, 0\n\t"
    "global_load_dwordx4 v[84:87], v230, s[40:41] offset:-4096 sc1\n\t" "global_load_dwordx4 v[100:103], v230, s[40:41] offset:0 sc1\n\t" "s_add_u32 s40, s40, 8192\n\t" "s_addc_u32 s41, s41, 0\n\t"
    "global_load_dwordx4 v[88:91], v230, s[40:41] offset:-4096 sc1\n\t" "global_load_dwordx4 v[104:107], v230, s[40:41] offset:0 sc1\n\t" "s_add_u32 s40, s40, 8192\n\t" "s_addc_u32 s41, s41, 0\n\t"
    "global_load_dwordx4 v[92:95], v230, s[40:41] offset:-4096 sc1\n\t" "global_load_dwordx4 v[108:111], v230, s[40:41] offset:0 sc1\n\t" "s_waitcnt vmcnt(20)\n\t"
    "v_mfma_f32_16x16x32_bf16 v[124:127], v[112:115], v[28:31], v[124:127]\n\t" "ds_read_b128 a[64:67], v202 offset:26624\n\t" "ds_read_b128 a[68:71], v202 offset:28672\n\t"
    "v_mfma_f32_16x16x32_bf16 v[136:139], v[28:31], v[28:31], v[136:139]\n\t" "ds_read_b128 a[72:75], v203 offset:26624\n\t" "ds_read_b128 a[76:79], v203 offset:28672\n\t"
    "v_mfma_f32_16x16x32_bf16 v[124:127], v[112:115], v[36:39], v[124:127]\n\t" "s_add_u32 s45, %[k0], 1024\n\t" "s_and_b32 s45, s45, 0x7ff\n\t" "s_add_u32 s64, %[xl], s45\n\t"
    "s_addc_u32 s65, %[xh_], 0\n\t" "v_mfma_f32_16x16x32_bf16 v[136:139], v[36:39], v[36:39], v[136:139]\n\t" "v_mfma_f32_16x16x32_bf16 v[124:127], v[112:115], v[32:35], v[124:127]\n\t"
    "v_mfma_f32_16x16x32_bf16 v[136:139], v[32:35], v[32:35], v[136:139]\n\t" "s_waitcnt lgkmcnt(0)\n\t" "s_add_u32 m0, %[sl], 47104\n\t"
    "v_mfma_f32_16x16x32_bf16 v[124:127], v[112:115], v[40:43], v[124:127]\n\t" "global_load_lds_dwordx4 v196, s[64:65] offset:0\n\t" "s_add_u32 m0, %[sl], 59392\n\t"
    "v_mfma_f32_16x16x32_bf16 v[136:139], v[40:43], v[40:43], v[136:139]\n\t" "global_load_lds_dwordx4 v197, s[64:65] offset:0\n\t" "s_add_u32 m0, %[sl], 49152\n\t" "s_nop 0\n\t"
    "global_load_lds_dwordx4 v198, s[64:65] offset:0\n\t" "s_add_u32 m0, %[sl], 61440\n\t" "s_nop 0\n\t" "global_load_lds_dwordx4 v199, s[64:65] offset:0\n\t" "s_waitcnt vmcnt(20)\n\t"
    "v_mfma_f32_16x16x32_bf16 v[116:119], v[112:115], a[64:67], v[116:119]\n\t" "ds_read_b128 a[80:83], v202 offset:49152\n\t" "ds_read_b128 a[84:87], v202 offset:51200\n\t"
    "v_mfma_f32_16x16x32_bf16 v[128:131], a[64:67], a[64:67], v[128:131]\n\t" "ds_read_b128 a[88:91], v203 offset:49152\n\t" "ds_read_b128 a[92:95], v203 offset:51200\n\t"
    "v_mfma_f32_16x16x32_bf16 v[120:123], v[112:115], a[68:71], v[120:123]\n\t" "v_cndmask_b32 v142, 0, v136, s[46:47]\n\t" "v_cndmask_b32 v142, v142, v137, s[48:49]\n\t"
    "v_cndmask_b32 v142, v142, v138, s[50:51]\n\t" "v_cndmask_b32 v142, v142, v139, s[52:53]\n\t" "v_mfma_f32_16x16x32_bf16 v[132:135], a[68:71], a[68:71], v[132:135]\n\t" "v_mov_b32 v143, v142\n\t"
    "s_nop 1\n\t" "v_permlane16_swap_b32 v142, v143\n\t" "v_add_f32 v142, v142, v143\n\t" "v_mfma_f32_16x16x32_bf16 v[116:119], v[112:115], a[72:75], v[116:119]\n\t" "v_mov_b32 v143, v142\n\t"
    "s_nop 1\n\t" "v_permlane32_swap_b32 v142, v143\n\t" "v_add_f32 v142, v142, v143\n\t" "v_mfma_f32_16x16x32_bf16 v[128:131], a[72:75], a[72:75], v[128:131]\n\t" "s_waitcnt lgkmcnt(0)\n\t"
    "s_add_u32 m0, %[sl], 69504\n\t" "v_mfma_f32_16x16x32_bf16 v[120:123], v[112:115], a[76:79], v[120:123]\n\t" "global_load_lds_dwordx4 v196, s[64:65] offset:128\n\t" "s_add_u32 m0, %[sl], 81792\n\t"
    "v_mfma_f32_16x16x32_bf16 v[132:135], a[76:79], a[76:79], v[132:135]\n\t" "global_load_lds_dwordx4 v197, s[64:65] offset:128\n\t" "s_add_u32 m0, %[sl], 71552\n\t" "s_nop 0\n\t"
    "global_load_lds_dwordx4 v198, s[64:65] offset:128\n\t" "s_add_u32 m0, %[sl], 83840\n\t" "s_nop 0\n\t" "global_load_lds_dwordx4 v199, s[64:65] offset:128\n\t" "s_waitcnt vmcnt(20)\n\t"
    "v_mfma_f32_16x16x32_bf16 v[116:119], v[112:115], a[80:83], v[116:119]\n\t" "ds_read_b128 a[96:99], v202 offset:0\n\t" "ds_read_b128 a[100:103], v202 offset:2048\n\t"
    "v_mfma_f32_16x16x32_bf16 v[128:131], a[80:83], a[80:83], v[128:131]\n\t" "ds_read_b128 a[104:107], v203 offset:0\n\t" "ds_read_b128 a[108:111], v203 offset:2048\n\t"
    "v_mfma_f32_16x16x32_bf16 v[120:123], v[112:115], a[84:87], v[120:123]\n\t" "s_add_u32 s45, %[k0], 1280\n\t" "s_and_b32 s45, s45, 0x7ff\n\t" "s_add_u32 s64, %[xl], s45\n\t"
    "s_addc_u32 s65, %[xh_], 0\n\t" "v_mfma_f32_16x16x32_bf16 v[132:135], a[84:87], a[84:87], v[132:135]\n\t" "v_mfma_f32_16x16x32_bf16 v[116:119], v[112:115], a[88:91], v[116:119]\n\t"
    "v_mfma_f32_16x16x32_bf16 v[128:131], a[88:91], a[88:91], v[128:131]\n\t" "s_waitcnt lgkmcnt(0)\n\t" "s_add_u32 m0, %[sl], 20480\n\t"
    "v_mfma_f32_16x16x32_bf16 v[120:123], v[112:115], a[92:95], v[120:123]\n\t" "global_load_lds_dwordx4 v196, s[64:65] offset:0\n\t" "s_add_u32 m0, %[sl], 32768\n\t"
    "v_mfma_f32_16x16x32_bf16 v[132:135], a[92:95], a[92:95], v[132:135]\n\t" "global_load_lds_dwordx4 v197, s[64:65] offset:0\n\t" "s_add_u32 m0, %[sl], 22528\n\t" "s_nop 0\n\t"
    "global_load_lds_dwordx4 v198, s[64:65] offset:0\n\t" "s_add_u32 m0, %[sl], 34816\n\t" "s_nop 0\n\t" "global_load_lds_dwordx4 v199, s[64:65] offset:0\n\t" "s_waitcnt vmcnt(20)\n\t"
    "v_mfma_f32_16x16x32_bf16 v[116:119], v[112:115], a[96:99], v[116:119]\n\t" "ds_read_b128 a[112:115], v202 offset:4096\n\t" "ds_read_b128 a[116:119], v202 offset:24576\n\t"
    "v_mfma_f32_16x16x32_bf16 v[128:131], a[96:99], a[96:99], v[128:131]\n\t" "ds_read_b128 a[120:123], v203 offset:4096\n\t" "ds_read_b128 a[124:127], v203 offset:24576\n\t"
    "v_mfma_f32_16x16x32_bf16 v[120:123], v[112:115], a[100:103], v[120:123]\n\t" "v_mfma_f32_16x16x32_bf16 v[132:135], a[100:103], a[100:103], v[132:135]\n\t"
    "v_mfma_f32_16x16x32_bf16 v[116:119], v[112:115], a[104:107], v[116:119]\n\t" "v_mfma_f32_16x16x32_bf16 v[128:131], a[104:107], a[104:107], v[128:131]\n\t" "s_waitcnt lgkmcnt(0)\n\t"
    "s_add_u32 m0, %[sl], 24448\n\t" "v_mfma_f32_16x16x32_bf16 v[120:123], v[112:115], a[108:111], v[120:123]\n\t" "global_load_lds_dwordx4 v196, s[64:65] offset:128\n\t" "s_add_u32 m0, %[sl], 36736\n\t"
    "v_mfma_f32_16x16x32_bf16 v[132:135], a[108:111], a[108:111], v[132:135]\n\t" "global_load_lds_dwordx4 v197, s[64:65] offset:128\n\t" "s_add_u32 m0, %[sl], 44928\n\t" "s_nop 0\n\t"
    "global_load_lds_dwordx4 v198, s[64:65] offset:128\n\t" "s_add_u32 m0, %[sl], 57216\n\t" "s_nop 0\n\t" "global_load_lds_dwordx4 v199, s[64:65] offset:128\n\t" "s_waitcnt vmcnt(12)\n\t"
    "v_mfma_f32_16x16x32_bf16 v[116:119], v[112:115], a[112:115], v[116:119]\n\t" "ds_read_b128 a[128:131], v202 offset:26624\n\t" "ds_read_b128 a[132:135], v202 offset:28672\n\t"
    "v_mfma_f32_16x16x32_bf16 v[128:131], a[112:115], a[112:115], v[128:131]\n\t" "ds_read_b128 a[136:139], v203 offset:26624\n\t" "ds_read_b128 a[140:143], v203 offset:28672\n\t"
    "v_mfma_f32_16x16x32_bf16 v[120:123], v[112:115], a[116:119], v[120:123]\n\t" "s_add_u32 s45, %[k0], 1536\n\t" "s_and_b32 s45, s45, 0x7ff\n\t" "s_add_u32 s64, %[xl], s45\n\t"
    "s_addc_u32 s65, %[xh_], 0\n\t" "v_xad_u32 v144, v80, v81, s62\n\t" "v_xad_u32 v145, v84, v85, s62\n\t" "v_xad_u32 v146, v88, v89, s62\n\t"
    "v_mfma_f32_16x16x32_bf16 v[132:135], a[116:119], a[116:119], v[132:135]\n\t" "v_xad_u32 v147, v92, v93, s62\n\t" "v_xad_u32 v148, v96, v97, s62\n\t" "v_xad_u32 v149, v100, v101, s62\n\t"
    "v_xad_u32 v150, v104, v105, s62\n\t" "v_mfma_f32_16x16x32_bf16 v[116:119], v[112:115], a[120:123], v[116:119]\n\t" "v_xad_u32 v151, v108, v109, s62\n\t" "v_xad_u32 v144, v82, v81, v144\n\t"
    "v_xad_u32 v145, v86, v85, v145\n\t" "v_xad_u32 v146, v90, v89, v146\n\t" "v_mfma_f32_16x16x32_bf16 v[128:131], a[120:123], a[120:123], v[128:131]\n\t" "v_xad_u32 v147, v94, v93, v147\n\t"
    "v_xad_u32 v148, v98, v97, v148\n\t" "v_xad_u32 v149, v102, v101, v149\n\t" "v_xad_u32 v150, v106, v105, v150\n\t" "s_waitcnt lgkmcnt(0)\n\t" "s_add_u32 m0, %[sl], 47104\n\t"
    "v_mfma_f32_16x16x32_bf16 v[120:123], v[112:115], a[124:127], v[120:123]\n\t" "global_load_lds_dwordx4 v196, s[64:65] offset:0\n\t" "s_add_u32 m0, %[sl], 59392\n\t"
    "v_mfma_f32_16x16x32_bf16 v[132:135], a[124:127], a[124:127], v[132:135]\n\t" "global_load_lds_dwordx4 v197, s[64:65] offset:0\n\t" "s_add_u32 m0, %[sl], 49152\n\t" "s_nop 0\n\t"
    "global_load_lds_dwordx4 v198, s[64:65] offset:0\n\t" "s_add_u32 m0, %[sl], 61440\n\t" "s_nop 0\n\t" "global_load_lds_dwordx4 v199, s[64:65] offset:0\n\t" "s_waitcnt vmcnt(12)\n\t"
    "v_mfma_f32_16x16x32_bf16 v[116:119], v[112:115], a[128:131], v[116:119]\n\t" "ds_read_b128 a[144:147], v202 offset:49152\n\t" "ds_read_b128 a[148:151], v202 offset:51200\n\t"
    "v_mfma_f32_16x16x32_bf16 v[128:131], a[128:131], a[128:131], v[128:131]\n\t" "ds_read_b128 a[152:155], v203 offset:49152\n\t" "ds_read_b128 a[156:159], v203 offset:51200\n\t"
    "v_mfma_f32_16x16x32_bf16 v[120:123], v[112:115], a[132:135], v[120:123]\n\t" "v_xad_u32 v151, v110, v109, v151\n\t" "v_xor_b32_e64 v140, v144, v83\n\t"
    "v_bitop3_b32 v140, v140, v145, v87 bitop3:0xf6\n\t" "v_bitop3_b32 v140, v140, v146, v91 bitop3:0xf6\n\t" "v_mfma_f32_16x16x32_bf16 v[132:135], a[132:135], a[132:135], v[132:135]\n\t"
    "v_bitop3_b32 v140, v140, v147, v95 bitop3:0xf6\n\t" "v_bitop3_b32 v140, v140, v148, v99 bitop3:0xf6\n\t" "v_bitop3_b32 v140, v140, v149, v103 bitop3:0xf6\n\t"
    "v_bitop3_b32 v140, v140, v150, v107 bitop3:0xf6\n\t" "v_mfma_f32_16x16x32_bf16 v[116:119], v[112:115], a[136:139], v[116:119]\n\t" "v_bitop3_b32 v140, v140, v151, v111 bitop3:0xf6\n\t"
    "v_cmp_ne_u32_e64 vcc, 0, v140\n\t" "v_perm_b32 v83, v80, v81, s57\n\t" "v_perm_b32 v87, v84, v85, s57\n\t" "v_mfma_f32_16x16x32_bf16 v[128:131], a[136:139], a[136:139], v[128:131]\n\t"
    "v_perm_b32 v91, v88, v89, s57\n\t" "v_perm_b32 v95, v92, v93, s57\n\t" "v_perm_b32 v99, v96, v97, s57\n\t" "v_perm_b32 v103, v100, v101, s57\n\t" "s_waitcnt lgkmcnt(0)\n\t"
    "s_add_u32 m0, %[sl], 69504\n\t" "v_mfma_f32_16x16x32_bf16 v[120:123], v[112:115], a[140:143], v[120:123]\n\t" "global_load_lds_dwordx4 v196, s[64:65] offset:128\n\t" "s_add_u32 m0, %[sl], 81792\n\t"
    "v_mfma_f32_16x16x32_bf16 v[132:135], a[140:143], a[140:143], v[132:135]\n\t" "global_load_lds_dwordx4 v197, s[64:65] offset:128\n\t" "s_add_u32 m0, %[sl], 71552\n\t" "s_nop 0\n\t"
    "global_load_lds_dwordx4 v198, s[64:65] offset:128\n\t" "s_add_u32 m0, %[sl], 83840\n\t" "s_nop 0\n\t" "global_load_lds_dwordx4 v199, s[64:65] offset:128\n\t" "s_waitcnt vmcnt(12)\n\t"
    "v_mfma_f32_16x16x32_bf16 v[116:119], v[112:115], a[144:147], v[116:119]\n\t" "ds_read_b128 a[160:163], v202 offset:0\n\t" "ds_read_b128 a[164:167], v202 offset:2048\n\t"
    "v_mfma_f32_16x16x32_bf16 v[128:131], a[144:147], a[144:147], v[128:131]\n\t" "ds_read_b128 a[168:171], v203 offset:0\n\t" "ds_read_b128 a[172:175], v203 offset:2048\n\t"
    "v_mfma_f32_16x16x32_bf16 v[120:123], v[112:115], a[148:151], v[120:123]\n\t" "s_add_u32 s45, %[k0], 1792\n\t" "s_and_b32 s45, s45, 0x7ff\n\t" "s_add_u32 s64, %[xl], s45\n\t"
    "s_addc_u32 s65, %[xh_], 0\n\t" "v_perm_b32 v107, v104, v105, s57\n\t" "v_perm_b32 v111, v108, v109, s57\n\t" "s_cbranch_vccnz .Lslow_%=\n\t" ".Lgo_%=:\n\t" "v_perm_b32 v83, v83, v82, s58\n\t"
    "v_mfma_f32_16x16x32_bf16 v[132:135], a[148:151], a[148:151], v[132:135]\n\t" "v_perm_b32 v87, v87, v86, s58\n\t" "v_perm_b32 v91, v91, v90, s58\n\t" "v_perm_b32 v95, v95, v94, s58\n\t"
    "v_perm_b32 v99, v99, v98, s58\n\t" "v_mfma_f32_16x16x32_bf16 v[116:119], v[112:115], a[152:155], v[116:119]\n\t" "v_perm_b32 v103, v103, v102, s58\n\t" "v_perm_b32 v107, v107, v106, s58\n\t"
    "v_perm_b32 v111, v111, v110, s58\n\t" "v_add_f32 v96, v96, v100\n\t" "v_mfma_f32_16x16x32_bf16 v[128:131], a[152:155], a[152:155], v[128:131]\n\t" "v_add_f32 v97, v97, v101\n\t"
    "v_add_f32 v98, v98, v102\n\t" "v_add_f32 v99, v99, v103\n\t" "v_add_f32_e64 v80, v80, v84\n\t" "s_waitcnt lgkmcnt(0)\n\t" "s_add_u32 m0, %[sl], 20480\n\t"
    "v_mfma_f32_16x16x32_bf16 v[120:123], v[112:115], a[156:159], v[120:123]\n\t" "global_load_lds_dwordx4 v196, s[64:65] offset:0\n\t" "s_add_u32 m0, %[sl], 32768\n\t"
    "v_mfma_f32_16x16x32_bf16 v[132:135], a[156:159], a[156:159], v[132:135]\n\t" "global_load_lds_dwordx4 v197, s[64:65] offset:0\n\t" "s_add_u32 m0, %[sl], 22528\n\t" "s_nop 0\n\t"
    "global_load_lds_dwordx4 v198, s[64:65] offset:0\n\t" "s_add_u32 m0, %[sl], 34816\n\t" "s_nop 0\n\t" "global_load_lds_dwordx4 v199, s[64:65] offset:0\n\t" "s_waitcnt vmcnt(12)\n\t"
    "v_mfma_f32_16x16x32_bf16 v[116:119], v[112:115], a[160:163], v[116:119]\n\t" "ds_read_b128 a[176:179], v202 offset:4096\n\t" "ds_read_b128 a[180:183], v202 offset:24576\n\t"
    "v_mfma_f32_16x16x32_bf16 v[128:131], a[160:163], a[160:163], v[128:131]\n\t" "ds_read_b128 a[184:187], v203 offset:4096\n\t" "ds_read_b128 a[188:191], v203 offset:24576\n\t"
    "v_mfma_f32_16x16x32_bf16 v[120:123], v[112:115], a[164:167], v[120:123]\n\t" "v_add_f32 v81, v81, v85\n\t" "v_add_f32 v82, v82, v86\n\t" "v_add_f32 v83, v83, v87\n\t"
    "v_add_f32_e64 v104, v104, v108\n\t" "v_mfma_f32_16x16x32_bf16 v[132:135], a[164:167], a[164:167], v[132:135]\n\t" "v_add_f32 v105, v105, v109\n\t" "v_add_f32 v106, v106, v110\n\t"
    "v_add_f32 v107, v107, v111\n\t" "v_add_f32 v88, v88, v92\n\t" "v_mfma_f32_16x16x32_bf16 v[116:119], v[112:115], a[168:171], v[116:119]\n\t" "v_add_f32 v89, v89, v93\n\t" "v_add_f32 v90, v90, v94\n\t"
    "v_add_f32 v91, v91, v95\n\t" "v_add_f32 v96, v96, v104\n\t" "v_mfma_f32_16x16x32_bf16 v[128:131], a[168:171], a[168:171], v[128:131]\n\t" "v_add_f32 v97, v97, v105\n\t" "v_add_f32 v98, v98, v106\n\t"
    "v_add_f32 v99, v99, v107\n\t" "v_add_f32_e64 v80, v80, v88\n\t" "s_waitcnt lgkmcnt(0)\n\t" "s_add_u32 m0, %[sl], 24448\n\t"
    "v_mfma_f32_16x16x32_bf16 v[120:123], v[112:115], a[172:175], v[120:123]\n\t" "global_load_lds_dwordx4 v196, s[64:65] offset:128\n\t" "s_add_u32 m0, %[sl], 36736\n\t"
    "v_mfma_f32_16x16x32_bf16 v[132:135], a[172:175], a[172:175], v[132:135]\n\t" "global_load_lds_dwordx4 v197, s[64:65] offset:128\n\t" "s_add_u32 m0, %[sl], 44928\n\t" "s_nop 0\n\t"
    "global_load_lds_dwordx4 v198, s[64:65] offset:128\n\t" "s_add_u32 m0, %[sl], 57216\n\t" "s_nop 0\n\t" "global_load_lds_dwordx4 v199, s[64:65] offset:128\n\t" "s_waitcnt vmcnt(12)\n\t"
    "v_mfma_f32_16x16x32_bf16 v[116:119], v[112:115], a[176:179], v[116:119]\n\t" "ds_read_b128 a[192:195], v202 offset:26624\n\t" "ds_read_b128 a[196:199], v202 offset:28672\n\t"
    "v_add_f32 v81, v81, v89\n\t" "v_add_f32 v82, v82, v90\n\t" "v_add_f32 v83, v83, v91\n\t" "v_cvt_pk_bf16_f32 v96, v96, v97\n\t"
    "v_mfma_f32_16x16x32_bf16 v[128:131], a[176:179], a[176:179], v[128:131]\n\t" "ds_read_b128 a[200:203], v203 offset:26624\n\t" "ds_read_b128 a[204:207], v203 offset:28672\n\t"
    "v_cvt_pk_bf16_f32 v98, v98, v99\n\t" "v_cvt_pk_bf16_f32 v80, v80, v81\n\t" "v_cvt_pk_bf16_f32 v81, v82, v83\n\t" "v_xor_b32_e64 v82, 16, v232\n\t"
    "v_mfma_f32_16x16x32_bf16 v[120:123], v[112:115], a[180:183], v[120:123]\n\t" "v_and_b32 v97, 0xffff0000, v96\n\t" "v_lshlrev_b32_e64 v96, 16, v96\n\t" "v_and_b32 v99, 0xffff0000, v98\n\t"
    "v_lshlrev_b32_e64 v98, 16, v98\n\t" "v_mfma_f32_16x16x32_bf16 v[132:135], a[180:183], a[180:183], v[132:135]\n\t" "v_add_f32 v96, 1.0, v96\n\t" "v_add_f32 v97, 1.0, v97\n\t"
    "v_add_f32 v98, 1.0, v98\n\t" "v_add_f32 v99, 1.0, v99\n\t" "v_mfma_f32_16x16x32_bf16 v[116:119], v[112:115], a[184:187], v[116:119]\n\t" "v_cvt_pk_bf16_f32 v96, v96, v97\n\t"
    "v_cvt_pk_bf16_f32 v97, v98, v99\n\t" "v_mov_b32 v101, 0\n\t" "v_mov_b32 v103, 0\n\t" "v_mfma_f32_16x16x32_bf16 v[128:131], a[184:187], a[184:187], v[128:131]\n\t" "v_mov_b32 v104, 0\n\t"
    "v_mov_b32 v106, 0\n\t" "v_and_b32 v100, 0xffff, v96\n\t" "v_and_b32 v102, 0xffff0000, v96\n\t" "s_waitcnt lgkmcnt(0)\n\t" "v_mfma_f32_16x16x32_bf16 v[120:123], v[112:115], a[188:191], v[120:123]\n\t"
    "v_and_b32 v105, 0xffff, v97\n\t" "v_and_b32 v107, 0xffff0000, v97\n\t" "v_mfma_f32_16x16x32_bf16 v[132:135], a[188:191], a[188:191], v[132:135]\n\t" "s_waitcnt vmcnt(8)\n\t"
    "v_mfma_f32_16x16x32_bf16 v[116:119], v[112:115], a[192:195], v[116:119]\n\t" "ds_read_b128 a[208:211], v202 offset:49152\n\t" "ds_read_b128 a[212:215], v202 offset:51200\n\t"
    "v_mfma_f32_16x16x32_bf16 v[128:131], a[192:195], a[192:195], v[128:131]\n\t" "ds_read_b128 a[216:219], v203 offset:49152\n\t" "ds_read_b128 a[220:223], v203 offset:51200\n\t"
    "v_mfma_f32_16x16x32_bf16 v[120:123], v[112:115], a[196:199], v[120:123]\n\t" "v_mfma_f32_16x16x32_bf16 v[132:135], a[196:199], a[196:199], v[132:135]\n\t"
    "v_mfma_f32_16x16x32_bf16 v[116:119], v[112:115], a[200:203], v[116:119]\n\t" "v_mfma_f32_16x16x32_bf16 v[128:131], a[200:203], a[200:203], v[128:131]\n\t" "s_waitcnt lgkmcnt(0)\n\t"
    "v_mfma_f32_16x16x32_bf16 v[120:123], v[112:115], a[204:207], v[120:123]\n\t" "v_mfma_f32_16x16x32_bf16 v[132:135], a[204:207], a[204:207], v[132:135]\n\t" "s_waitcnt vmcnt(4)\n\t"
    "s_add_u32 m0, %[sl], 108544\n\t" "s_nop 0\n\t" "global_load_lds_dwordx4 v0, %[sb] offset:-2048\n\t" "global_load_lds_dwordx4 v0, %[sb] offset:0\n\t"
    "global_load_lds_dwordx4 v0, %[sb] offset:2048\n\t" "v_add_u32 v0, 0x30000, v0\n\t" "s_add_u32 m0, %[sl], 120832\n\t" "s_nop 0\n\t" "global_load_lds_dwordx4 v0, %[sb] offset:-2048\n\t"
    "global_load_lds_dwordx4 v0, %[sb] offset:0\n\t" "global_load_lds_dwordx4 v0, %[sb] offset:2048\n\t" "v_add_u32 v0, 0x30000, v0\n\t"
    "v_mfma_f32_16x16x32_bf16 v[116:119], v[112:115], a[208:211], v[116:119]\n\t" "ds_read_b128 a[224:227], v202 offset:0\n\t" "ds_read_b128 a[228:231], v202 offset:2048\n\t"
    "v_mfma_f32_16x16x32_bf16 v[128:131], a[208:211], a[208:211], v[128:131]\n\t" "ds_read_b128 a[232:235], v203 offset:0\n\t" "ds_read_b128 a[236:239], v203 offset:2048\n\t"
    "v_mfma_f32_16x16x32_bf16 v[120:123], v[112:115], a[212:215], v[120:123]\n\t" "v_mfma_f32_16x16x32_bf16 v[132:135], a[212:215], a[212:215], v[132:135]\n\t"
    "v_mfma_f32_16x16x32_bf16 v[116:119], v[112:115], a[216:219], v[116:119]\n\t" "v_mfma_f32_16x16x32_bf16 v[128:131], a[216:219], a[216:219], v[128:131]\n\t" "s_waitcnt lgkmcnt(0)\n\t"
    "v_mfma_f32_16x16x32_bf16 v[120:123], v[112:115], a[220:223], v[120:123]\n\t" "v_mfma_f32_16x16x32_bf16 v[132:135], a[220:223], a[220:223], v[132:135]\n\t" "s_waitcnt vmcnt(6)\n\t"
    "v_mfma_f32_16x16x32_bf16 v[116:119], v[112:115], a[224:227], v[116:119]\n\t" "ds_read_b128 a[240:243], v202 offset:4096\n\t" "ds_read_b128 a[244:247], v202 offset:24576\n\t"
    "v_mfma_f32_16x16x32_bf16 v[128:131], a[224:227], a[224:227], v[128:131]\n\t" "ds_read_b128 a[248:251], v203 offset:4096\n\t" "ds_read_b128 a[252:255], v203 offset:24576\n\t"
    "v_mfma_f32_16x16x32_bf16 v[120:123], v[112:115], a[228:231], v[120:123]\n\t" "ds_write2_b32 v235, v142, v124 offset1:1\n\t"
    "v_mfma_f32_16x16x32_bf16 v[132:135], a[228:231], a[228:231], v[132:135]\n\t" "ds_write2_b64 v231, v[100:101], v[102:103] offset0:0 offset1:8\n\t"
    "v_mfma_f32_16x16x32_bf16 v[116:119], v[112:115], a[232:235], v[116:119]\n\t" "ds_write2_b64 v231, v[104:105], v[106:107] offset0:16 offset1:24\n\t"
    "v_mfma_f32_16x16x32_bf16 v[128:131], a[232:235], a[232:235], v[128:131]\n\t" "ds_write_b64 v82, v[80:81]\n\t" "s_waitcnt lgkmcnt(4)\n\t"
    "v_mfma_f32_16x16x32_bf16 v[120:123], v[112:115], a[236:239], v[120:123]\n\t" "ds_write_b64 v232, v[96:97]\n\t" "v_mfma_f32_16x16x32_bf16 v[132:135], a[236:239], a[236:239], v[132:135]\n\t"
    "s_waitcnt lgkmcnt(0)\n\t" "s_barrier\n\t" "ds_read_b128 v[220:223], v2 offset:0\n\t" "ds_read_b128 v[44:47], v18 offset:0\n\t"
    "v_mfma_f32_16x16x32_bf16 v[116:119], v[112:115], a[240:243], v[116:119]\n\t" "ds_read_b128 v[242:245], v18 offset:256\n\t" "ds_read_b128 v[224:227], v2 offset:256\n\t"
    "v_mfma_f32_16x16x32_bf16 v[128:131], a[240:243], a[240:243], v[128:131]\n\t" "ds_read2_b64 v[148:151], v246 offset0:0 offset1:16\n\t" "ds_read2_b64 v[152:155], v246 offset0:32 offset1:48\n\t"
    "v_mfma_f32_16x16x32_bf16 v[120:123], v[112:115], a[244:247], v[120:123]\n\t" "ds_read_b64_tr_b16 v[156:157], v8 offset:8192\n\t" "ds_read_b64_tr_b16 v[158:159], v8 offset:8704\n\t"
    "v_mfma_f32_16x16x32_bf16 v[132:135], a[244:247], a[244:247], v[132:135]\n\t" "ds_read_b64_tr_b16 v[160:161], v9 offset:8192\n\t" "ds_read_b64_tr_b16 v[162:163], v9 offset:8704\n\t"
    "v_mfma_f32_16x16x32_bf16 v[116:119], v[112:115], a[248:251], v[116:119]\n\t" "ds_read_b64_tr_b16 v[164:165], v10 offset:8192\n\t" "ds_read_b64_tr_b16 v[166:167], v10 offset:8704\n\t"
    "v_mfma_f32_16x16x32_bf16 v[128:131], a[248:251], a[248:251], v[128:131]\n\t" "ds_read_b64_tr_b16 v[168:169], v8 offset:10240\n\t" "ds_read_b64_tr_b16 v[170:171], v8 offset:10752\n\t"
    "v_mfma_f32_16x16x32_bf16 v[120:123], v[112:115], a[252:255], v[120:123]\n\t" "ds_read_b64_tr_b16 v[172:173], v9 offset:10240\n\t" "s_waitcnt lgkmcnt(11)\n\t"
    "ds_read_b64_tr_b16 v[174:175], v9 offset:10752\n\t" "v_mfma_f32_16x16x32_bf16 v[132:135], a[252:255], a[252:255], v[132:135]\n\t" "ds_read_b64_tr_b16 v[176:177], v10 offset:10240\n\t"
    "ds_read_b64_tr_b16 v[178:179], v10 offset:10752\n\t" "v_mfma_f32_4x4x4_16b_bf16 v[204:207], v[220:221], a[0:1], 0\n\t" "v_mfma_f32_4x4x4_16b_bf16 v[208:211], v[222:223], a[2:3], 0\n\t"
    "v_mfma_f32_4x4x4_16b_bf16 v[212:215], v[220:221], a[4:5], 0\n\t" "v_mfma_f32_4x4x4_16b_bf16 v[216:219], v[222:223], a[6:7], 0\n\t" "v_mfma_f32_4x4x4_16b_bf16 v[56:59], v[44:45], v[48:49], 0\n\t"
    "v_mfma_f32_4x4x4_16b_bf16 v[60:63], v[46:47], v[50:51], 0\n\t" "v_mfma_f32_4x4x4_16b_bf16 v[64:67], v[242:243], v[52:53], 0\n\t" "v_mfma_f32_4x4x4_16b_bf16 v[68:71], v[244:245], v[54:55], 0\n\t"
    "s_waitcnt lgkmcnt(12)\n\t" "v_add_f32 v148, v148, v150\n\t" "v_add_f32 v152, v152, v154\n\t" "v_add_f32 v149, v149, v151\n\t" "v_add_f32 v153, v153, v155\n\t" "v_add_f32 v241, v148, v152\n\t"
    "v_add_f32_e64 v238, v149, v153\n\t" "v_cvt_pk_bf16_f32 v48, v56, v57\n\t" "v_cvt_pk_bf16_f32 v49, v58, v59\n\t" "v_cvt_pk_bf16_f32 v50, v60, v61\n\t" "v_cvt_pk_bf16_f32 v51, v62, v63\n\t"
    "v_cvt_pk_bf16_f32 v52, v64, v65\n\t" "v_cvt_pk_bf16_f32 v53, v66, v67\n\t" "v_cvt_pk_bf16_f32 v54, v68, v69\n\t" "v_cvt_pk_bf16_f32 v55, v70, v71\n\t" "s_mov_b64 exec, s[60:61]\n\t"
    "ds_write_b128 v19, v[48:51] offset:0\n\t" "ds_write_b128 v19, v[52:55] offset:1024\n\t" "s_mov_b64 exec, -1\n\t" "v_cvt_pk_bf16_f32 v188, v204, v205\n\t" "v_cvt_pk_bf16_f32 v189, v206, v207\n\t"
    "v_cvt_pk_bf16_f32 v190, v208, v209\n\t" "v_cvt_pk_bf16_f32 v191, v210, v211\n\t" "v_cvt_pk_bf16_f32 v192, v212, v213\n\t" "v_cvt_pk_bf16_f32 v193, v214, v215\n\t"
    "v_cvt_pk_bf16_f32 v194, v216, v217\n\t" "v_cvt_pk_bf16_f32 v195, v218, v219\n\t" "s_nop 1\n\t" "s_waitcnt lgkmcnt(6)\n\t"
    "v_mfma_f32_16x16x32_bf16 v[56:59], v[156:159], v[188:191], 0\n\t" "ds_read_b64_tr_b16 v[180:181], v7 offset:8192\n\t" "s_add_u32 m0, %[sl], 71680\n\t"
    "v_mfma_f32_16x16x32_bf16 v[60:63], v[156:159], v[192:195], 0\n\t" "ds_read_b64_tr_b16 v[182:183], v7 offset:8704\n\t" "v_mfma_f32_16x16x32_bf16 v[68:71], v[160:163], v[192:195], 0\n\t"
    "ds_read_b64_tr_b16 v[184:185], v7 offset:10240\n\t" "global_load_lds_dwordx4 v0, %[sb] offset:-2048\n\t" "v_mfma_f32_16x16x32_bf16 v[64:67], v[160:163], v[188:191], 0\n\t"
    "ds_read_b64_tr_b16 v[186:187], v7 offset:10752\n\t" "v_mfma_f32_4x4x4_16b_bf16 v[204:207], v[224:225], a[8:9], 0\n\t" "v_mfma_f32_4x4x4_16b_bf16 v[208:211], v[226:227], a[10:11], 0\n\t"
    "v_mfma_f32_4x4x4_16b_bf16 v[212:215], v[224:225], a[12:13], 0\n\t" "v_mfma_f32_4x4x4_16b_bf16 v[216:219], v[226:227], a[14:15], 0\n\t" "v_cndmask_b32 v239, 0, v128, s[46:47]\n\t"
    "v_mfma_f32_16x16x32_bf16 v[72:75], v[164:167], v[188:191], 0\n\t" "ds_read_b64_tr_b16 v[156:157], v7 offset:12288\n\t" "v_cndmask_b32 v239, v239, v129, s[48:49]\n\t"
    "v_mfma_f32_16x16x32_bf16 v[76:79], v[164:167], v[192:195], 0\n\t" "ds_read_b64_tr_b16 v[158:159], v7 offset:12800\n\t" "global_load_lds_dwordx4 v0, %[sb] offset:0\n\t"
    "v_mfma_f32_16x16x32_bf16 v[92:95], v[168:171], v[192:195], 0\n\t" "s_waitcnt lgkmcnt(6)\n\t" "s_barrier\n\t" "ds_read_b128 v[228:231], v1 offset:0\n\t"
    "ds_read_b64_tr_b16 v[160:161], v8 offset:12288\n\t" "v_cvt_pk_bf16_f32 v196, v204, v205\n\t" "v_mfma_f32_16x16x32_bf16 v[88:91], v[168:171], v[188:191], 0\n\t"
    "ds_read_b64_tr_b16 v[162:163], v8 offset:12800\n\t" "v_cvt_pk_bf16_f32 v197, v206, v207\n\t" "s_waitcnt lgkmcnt(5)\n\t" "v_mfma_f32_16x16x32_bf16 v[96:99], v[172:175], v[188:191], 0\n\t"
    "ds_read_b64_tr_b16 v[164:165], v9 offset:12288\n\t" "v_cndmask_b32 v239, v239, v130, s[50:51]\n\t" "v_mfma_f32_16x16x32_bf16 v[100:103], v[172:175], v[192:195], 0\n\t"
    "ds_read_b64_tr_b16 v[166:167], v9 offset:12800\n\t" "v_cvt_pk_bf16_f32 v198, v208, v209\n\t" "v_mfma_f32_16x16x32_bf16 v[108:111], v[176:179], v[192:195], 0\n\t"
    "ds_read_b64_tr_b16 v[168:169], v10 offset:12288\n\t" "v_cvt_pk_bf16_f32 v199, v210, v211\n\t" "v_mfma_f32_16x16x32_bf16 v[104:107], v[176:179], v[188:191], 0\n\t"
    "ds_read_b64_tr_b16 v[170:171], v10 offset:12800\n\t" "ds_read_b128 v[220:223], v2 offset:512\n\t" "v_mfma_f32_16x16x32_bf16 v[48:51], v[180:183], v[188:191], 0\n\t" "s_waitcnt vmcnt(5)\n\t"
    "s_barrier\n\t" "ds_read_b64_tr_b16 v[172:173], v7 offset:20480\n\t" "v_cvt_pk_bf16_f32 v200, v212, v213\n\t" "v_mfma_f32_16x16x32_bf16 v[52:55], v[180:183], v[192:195], 0\n\t"
    "ds_read_b64_tr_b16 v[174:175], v7 offset:20992\n\t" "v_cvt_pk_bf16_f32 v201, v214, v215\n\t" "s_waitcnt lgkmcnt(9)\n\t" "v_mfma_f32_16x16x32_bf16 v[152:155], v[228:231], v[180:183], 0\n\t"
    "global_load_lds_dwordx4 v0, %[sb] offset:2048\n\t" "s_cmp_eq_u32 %[rot], 7\n\t" "s_cselect_b32 s42, s43, 0x30000\n\t" "v_add_u32 v0, s42, v0\n\t"
    "v_mfma_f32_16x16x32_bf16 v[84:87], v[184:187], v[192:195], 0\n\t" "ds_read_b64_tr_b16 v[176:177], v8 offset:20480\n\t" "v_cvt_pk_bf16_f32 v202, v216, v217\n\t"
    "v_mfma_f32_16x16x32_bf16 v[80:83], v[184:187], v[188:191], 0\n\t" "ds_read_b64_tr_b16 v[178:179], v8 offset:20992\n\t" "v_cvt_pk_bf16_f32 v203, v218, v219\n\t"
    "v_mfma_f32_16x16x32_bf16 v[144:147], v[184:187], v[228:231], 0\n\t" "v_cndmask_b32 v239, v239, v131, s[52:53]\n\t" "v_mov_b32_e64 v236, v116\n\t" "v_cndmask_b32 v240, 0, v132, s[46:47]\n\t"
    "v_cndmask_b32 v240, v240, v133, s[48:49]\n\t" "v_cndmask_b32 v240, v240, v134, s[50:51]\n\t" "v_cndmask_b32 v240, v240, v135, s[52:53]\n\t" "v_mov_b32 v237, v120\n\t" "s_waitcnt lgkmcnt(5)\n\t"
    "v_mfma_f32_16x16x32_bf16 v[112:115], v[188:191], v[156:159], 0\n\t" "ds_read_b64_tr_b16 v[180:181], v9 offset:20480\n\t" "s_add_u32 m0, %[sl], 83968\n\t"
    "v_mfma_f32_16x16x32_bf16 v[116:119], v[192:195], v[156:159], 0\n\t" "ds_read_b64_tr_b16 v[182:183], v9 offset:20992\n\t" "global_load_lds_dwordx4 v0, %[sb] offset:-2048\n\t"
    "v_mfma_f32_16x16x32_bf16 v[148:151], v[228:231], v[156:159], 0\n\t" "ds_read_b128 v[232:235], v1 offset:1024\n\t" "global_load_lds_dwordx4 v0, %[sb] offset:0\n\t"
    "v_mfma_f32_16x16x32_bf16 v[124:127], v[192:195], v[160:163], 0\n\t" "ds_read_b64_tr_b16 v[184:185], v10 offset:20480\n\t" "v_mfma_f32_16x16x32_bf16 v[120:123], v[188:191], v[160:163], 0\n\t"
    "ds_read_b64_tr_b16 v[186:187], v10 offset:20992\n\t" "v_mfma_f32_16x16x32_bf16 v[128:131], v[188:191], v[164:167], 0\n\t" "ds_read_b64_tr_b16 v[156:157], v7 offset:22528\n\t"
    "v_mfma_f32_16x16x32_bf16 v[132:135], v[192:195], v[164:167], 0\n\t" "ds_read_b64_tr_b16 v[158:159], v7 offset:23040\n\t" "global_load_lds_dwordx4 v0, %[sb] offset:2048\n\t"
    "v_mfma_f32_16x16x32_bf16 v[140:143], v[192:195], v[168:171], 0\n\t" "ds_read_b64_tr_b16 v[160:161], v8 offset:22528\n\t" "v_add_u32 v0, 0x30000, v0\n\t"
    "v_mfma_f32_16x16x32_bf16 v[136:139], v[188:191], v[168:171], 0\n\t" "ds_read_b64_tr_b16 v[162:163], v8 offset:23040\n\t" "s_waitcnt lgkmcnt(4)\n\t"
    "v_mfma_f32_16x16x32_bf16 v[48:51], v[172:175], v[196:199], v[48:51]\n\t" "ds_read_b64_tr_b16 v[164:165], v9 offset:22528\n\t" "s_add_u32 m0, %[sl], 22528\n\t"
    "v_mfma_f32_16x16x32_bf16 v[52:55], v[172:175], v[200:203], v[52:55]\n\t" "ds_read_b64_tr_b16 v[166:167], v9 offset:23040\n\t" "global_load_lds_dwordx4 v0, %[sb] offset:-2048\n\t"
    "v_mfma_f32_16x16x32_bf16 v[152:155], v[232:235], v[172:175], v[152:155]\n\t" "global_load_lds_dwordx4 v0, %[sb] offset:0\n\t" "v_mfma_f32_16x16x32_bf16 v[60:63], v[176:179], v[200:203], v[60:63]\n\t"
    "ds_read_b64_tr_b16 v[168:169], v10 offset:22528\n\t" "v_mfma_f32_4x4x4_16b_bf16 v[204:207], v[220:221], a[16:17], 0\n\t" "v_mfma_f32_4x4x4_16b_bf16 v[208:211], v[222:223], a[18:19], 0\n\t"
    "v_mfma_f32_4x4x4_16b_bf16 v[212:215], v[220:221], a[20:21], 0\n\t" "v_mfma_f32_4x4x4_16b_bf16 v[216:219], v[222:223], a[22:23], 0\n\t"
    "v_mfma_f32_16x16x32_bf16 v[56:59], v[176:179], v[196:199], v[56:59]\n\t" "ds_read_b64_tr_b16 v[170:171], v10 offset:23040\n\t"
    "v_mfma_f32_16x16x32_bf16 v[64:67], v[180:183], v[196:199], v[64:67]\n\t" "ds_read_b64_tr_b16 v[172:173], v7 offset:24576\n\t" "global_load_lds_dwordx4 v0, %[sb] offset:2048\n\t"
    "v_mfma_f32_16x16x32_bf16 v[68:71], v[180:183], v[200:203], v[68:71]\n\t" "ds_read_b64_tr_b16 v[174:175], v7 offset:25088\n\t" "v_cvt_pk_bf16_f32 v188, v204, v205\n\t"
    "v_mfma_f32_16x16x32_bf16 v[76:79], v[184:187], v[200:203], v[76:79]\n\t" "ds_read_b64_tr_b16 v[176:177], v8 offset:24576\n\t" "v_cvt_pk_bf16_f32 v189, v206, v207\n\t"
    "v_mfma_f32_16x16x32_bf16 v[72:75], v[184:187], v[196:199], v[72:75]\n\t" "ds_read_b64_tr_b16 v[178:179], v8 offset:25088\n\t" "s_waitcnt lgkmcnt(4)\n\t"
    "v_mfma_f32_16x16x32_bf16 v[80:83], v[156:159], v[196:199], v[80:83]\n\t" "ds_read_b64_tr_b16 v[180:181], v9 offset:24576\n\t" "v_cvt_pk_bf16_f32 v190, v208, v209\n\t"
    "v_mfma_f32_16x16x32_bf16 v[84:87], v[156:159], v[200:203], v[84:87]\n\t" "ds_read_b64_tr_b16 v[182:183], v9 offset:25088\n\t" "v_cvt_pk_bf16_f32 v191, v210, v211\n\t"
    "v_mfma_f32_16x16x32_bf16 v[144:147], v[156:159], v[232:235], v[144:147]\n\t" "ds_read_b128 v[224:227], v2 offset:768\n\t" "v_add_u32 v0, 0x30000, v0\n\t"
    "v_mfma_f32_16x16x32_bf16 v[92:95], v[160:163], v[200:203], v[92:95]\n\t" "ds_read_b64_tr_b16 v[184:185], v10 offset:24576\n\t" "v_cvt_pk_bf16_f32 v192, v212, v213\n\t"
    "v_mfma_f32_16x16x32_bf16 v[88:91], v[160:163], v[196:199], v[88:91]\n\t" "ds_read_b64_tr_b16 v[186:187], v10 offset:25088\n\t" "v_cvt_pk_bf16_f32 v193, v214, v215\n\t"
    "v_mfma_f32_16x16x32_bf16 v[96:99], v[164:167], v[196:199], v[96:99]\n\t" "s_waitcnt vmcnt(9)\n\t" "s_barrier\n\t" "ds_read_b64_tr_b16 v[156:157], v7 offset:32768\n\t"
    "v_mfma_f32_16x16x32_bf16 v[100:103], v[164:167], v[200:203], v[100:103]\n\t" "ds_read_b64_tr_b16 v[158:159], v7 offset:33280\n\t" "v_cvt_pk_bf16_f32 v194, v216, v217\n\t"
    "v_mfma_f32_16x16x32_bf16 v[108:111], v[168:171], v[200:203], v[108:111]\n\t" "ds_read_b64_tr_b16 v[160:161], v8 offset:32768\n\t" "v_cvt_pk_bf16_f32 v195, v218, v219\n\t"
    "v_mfma_f32_16x16x32_bf16 v[104:107], v[168:171], v[196:199], v[104:107]\n\t" "ds_read_b64_tr_b16 v[162:163], v8 offset:33280\n\t" "s_waitcnt lgkmcnt(4)\n\t"
    "v_mfma_f32_16x16x32_bf16 v[112:115], v[196:199], v[172:175], v[112:115]\n\t" "ds_read_b64_tr_b16 v[164:165], v9 offset:32768\n\t" "s_add_u32 m0, %[sl], 34816\n\t"
    "v_mfma_f32_16x16x32_bf16 v[116:119], v[200:203], v[172:175], v[116:119]\n\t" "ds_read_b64_tr_b16 v[166:167], v9 offset:33280\n\t" "global_load_lds_dwordx4 v0, %[sb] offset:-2048\n\t"
    "v_mfma_f32_16x16x32_bf16 v[148:151], v[232:235], v[172:175], v[148:151]\n\t" "ds_read_b128 v[228:231], v1 offset:2048\n\t" "global_load_lds_dwordx4 v0, %[sb] offset:0\n\t"
    "v_mfma_f32_16x16x32_bf16 v[124:127], v[200:203], v[176:179], v[124:127]\n\t" "ds_read_b64_tr_b16 v[168:169], v10 offset:32768\n\t" "global_load_lds_dwordx4 v0, %[sb] offset:2048\n\t"
    "v_mfma_f32_16x16x32_bf16 v[120:123], v[196:199], v[176:179], v[120:123]\n\t" "ds_read_b64_tr_b16 v[170:171], v10 offset:33280\n\t" "v_add_u32 v0, 0x30000, v0\n\t"
    "v_mfma_f32_16x16x32_bf16 v[128:131], v[196:199], v[180:183], v[128:131]\n\t" "ds_read_b64_tr_b16 v[172:173], v7 offset:34816\n\t"
    "v_mfma_f32_16x16x32_bf16 v[132:135], v[200:203], v[180:183], v[132:135]\n\t" "ds_read_b64_tr_b16 v[174:175], v7 offset:35328\n\t" "s_mov_b64 exec, %[tm]\n\t" "global_load_dword v17, v16, %[pf]\n\t"
    "s_mov_b64 exec, -1\n\t" "v_mfma_f32_16x16x32_bf16 v[140:143], v[200:203], v[184:187], v[140:143]\n\t" "ds_read_b64_tr_b16 v[176:177], v8 offset:34816\n\t"
    "v_mfma_f32_16x16x32_bf16 v[136:139], v[196:199], v[184:187], v[136:139]\n\t" "ds_read_b64_tr_b16 v[178:179], v8 offset:35328\n\t" "s_waitcnt lgkmcnt(4)\n\t"
    "v_mfma_f32_16x16x32_bf16 v[48:51], v[156:159], v[188:191], v[48:51]\n\t" "ds_read_b64_tr_b16 v[180:181], v9 offset:34816\n\t" "s_add_u32 m0, %[sl], 47104\n\t"
    "v_mfma_f32_16x16x32_bf16 v[52:55], v[156:159], v[192:195], v[52:55]\n\t" "ds_read_b64_tr_b16 v[182:183], v9 offset:35328\n\t"
    "v_mfma_f32_16x16x32_bf16 v[152:155], v[228:231], v[156:159], v[152:155]\n\t" "global_load_lds_dwordx4 v0, %[sb] offset:-2048\n\t"
    "v_mfma_f32_16x16x32_bf16 v[60:63], v[160:163], v[192:195], v[60:63]\n\t" "ds_read_b64_tr_b16 v[184:185], v10 offset:34816\n\t" "v_mfma_f32_4x4x4_16b_bf16 v[204:207], v[224:225], a[24:25], 0\n\t"
    "v_mfma_f32_4x4x4_16b_bf16 v[208:211], v[226:227], a[26:27], 0\n\t" "v_mfma_f32_4x4x4_16b_bf16 v[212:215], v[224:225], a[28:29], 0\n\t"
    "v_mfma_f32_4x4x4_16b_bf16 v[216:219], v[226:227], a[30:31], 0\n\t" "v_mfma_f32_16x16x32_bf16 v[56:59], v[160:163], v[188:191], v[56:59]\n\t" "ds_read_b64_tr_b16 v[186:187], v10 offset:35328\n\t"
    "v_mfma_f32_16x16x32_bf16 v[64:67], v[164:167], v[188:191], v[64:67]\n\t" "ds_read_b64_tr_b16 v[156:157], v7 offset:36864\n\t" "global_load_lds_dwordx4 v0, %[sb] offset:0\n\t"
    "v_mfma_f32_16x16x32_bf16 v[68:71], v[164:167], v[192:195], v[68:71]\n\t" "ds_read_b64_tr_b16 v[158:159], v7 offset:37376\n\t" "v_cvt_pk_bf16_f32 v196, v204, v205\n\t"
    "v_mfma_f32_16x16x32_bf16 v[76:79], v[168:171], v[192:195], v[76:79]\n\t" "ds_read_b64_tr_b16 v[160:161], v8 offset:36864\n\t" "v_cvt_pk_bf16_f32 v197, v206, v207\n\t"
    "v_mfma_f32_16x16x32_bf16 v[72:75], v[168:171], v[188:191], v[72:75]\n\t" "ds_read_b64_tr_b16 v[162:163], v8 offset:37376\n\t" "s_waitcnt lgkmcnt(4)\n\t"
    "v_mfma_f32_16x16x32_bf16 v[80:83], v[172:175], v[188:191], v[80:83]\n\t" "ds_read_b64_tr_b16 v[164:165], v9 offset:36864\n\t" "v_cvt_pk_bf16_f32 v198, v208, v209\n\t"
    "v_mfma_f32_16x16x32_bf16 v[84:87], v[172:175], v[192:195], v[84:87]\n\t" "ds_read_b64_tr_b16 v[166:167], v9 offset:37376\n\t" "v_cvt_pk_bf16_f32 v199, v210, v211\n\t"
    "v_mfma_f32_16x16x32_bf16 v[144:147], v[172:175], v[228:231], v[144:147]\n\t" "ds_read_b128 v[220:223], v2 offset:1024\n\t" "global_load_lds_dwordx4 v0, %[sb] offset:2048\n\t"
    "v_mfma_f32_16x16x32_bf16 v[92:95], v[176:179], v[192:195], v[92:95]\n\t" "ds_read_b64_tr_b16 v[168:169], v10 offset:36864\n\t" "v_cvt_pk_bf16_f32 v200, v212, v213\n\t"
    "v_mfma_f32_16x16x32_bf16 v[88:91], v[176:179], v[188:191], v[88:91]\n\t" "ds_read_b64_tr_b16 v[170:171], v10 offset:37376\n\t" "v_cvt_pk_bf16_f32 v201, v214, v215\n\t"
    "v_mfma_f32_16x16x32_bf16 v[96:99], v[180:183], v[188:191], v[96:99]\n\t" "s_waitcnt vmcnt(13)\n\t" "s_barrier\n\t" "ds_read_b64_tr_b16 v[172:173], v3 offset:49152\n\t"
    "v_mfma_f32_16x16x32_bf16 v[100:103], v[180:183], v[192:195], v[100:103]\n\t" "ds_read_b64_tr_b16 v[174:175], v3 offset:49664\n\t" "v_cvt_pk_bf16_f32 v202, v216, v217\n\t"
    "v_mfma_f32_16x16x32_bf16 v[108:111], v[184:187], v[192:195], v[108:111]\n\t" "ds_read_b64_tr_b16 v[176:177], v4 offset:49152\n\t" "v_cvt_pk_bf16_f32 v203, v218, v219\n\t"
    "v_mfma_f32_16x16x32_bf16 v[104:107], v[184:187], v[188:191], v[104:107]\n\t" "ds_read_b64_tr_b16 v[178:179], v4 offset:49664\n\t" "s_waitcnt lgkmcnt(4)\n\t"
    "v_mfma_f32_16x16x32_bf16 v[112:115], v[188:191], v[156:159], v[112:115]\n\t" "ds_read_b64_tr_b16 v[180:181], v5 offset:49152\n\t" "s_cmp_eq_u32 %[rot], 6\n\t" "s_cselect_b32 s42, s43, 0x30000\n\t"
    "v_add_u32 v0, s42, v0\n\t" "v_mfma_f32_16x16x32_bf16 v[116:119], v[192:195], v[156:159], v[116:119]\n\t" "ds_read_b64_tr_b16 v[182:183], v5 offset:49664\n\t" "s_add_u32 m0, %[sl], 59392\n\t"
    "v_mfma_f32_16x16x32_bf16 v[148:151], v[228:231], v[156:159], v[148:151]\n\t" "ds_read_b128 v[232:235], v1 offset:3072\n\t" "global_load_lds_dwordx4 v0, %[sb] offset:-2048\n\t"
    "v_mfma_f32_16x16x32_bf16 v[124:127], v[192:195], v[160:163], v[124:127]\n\t" "ds_read_b64_tr_b16 v[184:185], v6 offset:49152\n\t" "global_load_lds_dwordx4 v0, %[sb] offset:0\n\t"
    "v_mfma_f32_16x16x32_bf16 v[120:123], v[188:191], v[160:163], v[120:123]\n\t" "ds_read_b64_tr_b16 v[186:187], v6 offset:49664\n\t"
    "v_mfma_f32_16x16x32_bf16 v[128:131], v[188:191], v[164:167], v[128:131]\n\t" "ds_read_b64_tr_b16 v[156:157], v3 offset:51200\n\t"
    "v_mfma_f32_16x16x32_bf16 v[132:135], v[192:195], v[164:167], v[132:135]\n\t" "ds_read_b64_tr_b16 v[158:159], v3 offset:51712\n\t" "global_load_lds_dwordx4 v0, %[sb] offset:2048\n\t"
    "v_mfma_f32_16x16x32_bf16 v[140:143], v[192:195], v[168:171], v[140:143]\n\t" "ds_read_b64_tr_b16 v[160:161], v4 offset:51200\n\t" "v_add_u32 v0, 0x30000, v0\n\t"
    "v_mfma_f32_16x16x32_bf16 v[136:139], v[188:191], v[168:171], v[136:139]\n\t" "ds_read_b64_tr_b16 v[162:163], v4 offset:51712\n\t" "s_waitcnt lgkmcnt(4)\n\t"
    "v_mfma_f32_16x16x32_bf16 v[48:51], v[172:175], v[196:199], v[48:51]\n\t" "ds_read_b64_tr_b16 v[164:165], v5 offset:51200\n\t" "s_add_u32 m0, %[sl], 96256\n\t"
    "v_mfma_f32_16x16x32_bf16 v[52:55], v[172:175], v[200:203], v[52:55]\n\t" "ds_read_b64_tr_b16 v[166:167], v5 offset:51712\n\t" "ds_read_b128 v[44:47], v18 offset:2048\n\t"
    "v_mfma_f32_16x16x32_bf16 v[152:155], v[232:235], v[172:175], v[152:155]\n\t" "global_load_lds_dwordx4 v0, %[sb] offset:-2048\n\t" "ds_read_b128 v[242:245], v18 offset:2304\n\t"
    "v_mfma_f32_16x16x32_bf16 v[60:63], v[176:179], v[200:203], v[60:63]\n\t" "ds_read_b64_tr_b16 v[168:169], v6 offset:51200\n\t" "v_mfma_f32_4x4x4_16b_bf16 v[204:207], v[220:221], a[32:33], 0\n\t"
    "v_mfma_f32_4x4x4_16b_bf16 v[208:211], v[222:223], a[34:35], 0\n\t" "v_mfma_f32_4x4x4_16b_bf16 v[212:215], v[220:221], a[36:37], 0\n\t"
    "v_mfma_f32_4x4x4_16b_bf16 v[216:219], v[222:223], a[38:39], 0\n\t" "v_mfma_f32_16x16x32_bf16 v[56:59], v[176:179], v[196:199], v[56:59]\n\t" "ds_read_b64_tr_b16 v[170:171], v6 offset:51712\n\t"
    "v_mfma_f32_16x16x32_bf16 v[64:67], v[180:183], v[196:199], v[64:67]\n\t" "ds_read_b64_tr_b16 v[172:173], v3 offset:53248\n\t" "v_mfma_f32_16x16x32_bf16 v[68:71], v[180:183], v[200:203], v[68:71]\n\t"
    "ds_read_b64_tr_b16 v[174:175], v3 offset:53760\n\t" "v_cvt_pk_bf16_f32 v188, v204, v205\n\t" "v_mfma_f32_16x16x32_bf16 v[76:79], v[184:187], v[200:203], v[76:79]\n\t"
    "ds_read_b64_tr_b16 v[176:177], v4 offset:53248\n\t" "v_cvt_pk_bf16_f32 v189, v206, v207\n\t" "v_mfma_f32_16x16x32_bf16 v[72:75], v[184:187], v[196:199], v[72:75]\n\t"
    "ds_read_b64_tr_b16 v[178:179], v4 offset:53760\n\t" "s_waitcnt lgkmcnt(4)\n\t" "v_mfma_f32_16x16x32_bf16 v[80:83], v[156:159], v[196:199], v[80:83]\n\t"
    "ds_read_b64_tr_b16 v[180:181], v5 offset:53248\n\t" "v_cvt_pk_bf16_f32 v190, v208, v209\n\t" "v_mfma_f32_16x16x32_bf16 v[84:87], v[156:159], v[200:203], v[84:87]\n\t"
    "ds_read_b64_tr_b16 v[182:183], v5 offset:53760\n\t" "v_cvt_pk_bf16_f32 v191, v210, v211\n\t" "v_mfma_f32_16x16x32_bf16 v[144:147], v[156:159], v[232:235], v[144:147]\n\t"
    "ds_read_b128 v[224:227], v2 offset:1280\n\t" "global_load_lds_dwordx4 v0, %[sb] offset:0\n\t" "v_mfma_f32_16x16x32_bf16 v[92:95], v[160:163], v[200:203], v[92:95]\n\t"
    "ds_read_b64_tr_b16 v[184:185], v6 offset:53248\n\t" "v_cvt_pk_bf16_f32 v192, v212, v213\n\t" "v_mfma_f32_16x16x32_bf16 v[88:91], v[160:163], v[196:199], v[88:91]\n\t"
    "ds_read_b64_tr_b16 v[186:187], v6 offset:53760\n\t" "v_cvt_pk_bf16_f32 v193, v214, v215\n\t" "v_mfma_f32_16x16x32_bf16 v[96:99], v[164:167], v[196:199], v[96:99]\n\t" "s_waitcnt vmcnt(12)\n\t"
    "s_barrier\n\t" "ds_read_b64_tr_b16 v[156:157], v3 offset:61440\n\t" "v_mfma_f32_16x16x32_bf16 v[100:103], v[164:167], v[200:203], v[100:103]\n\t" "ds_read_b64_tr_b16 v[158:159], v3 offset:61952\n\t"
    "v_cvt_pk_bf16_f32 v194, v216, v217\n\t" "v_mfma_f32_16x16x32_bf16 v[108:111], v[168:171], v[200:203], v[108:111]\n\t" "ds_read_b64_tr_b16 v[160:161], v4 offset:61440\n\t"
    "v_cvt_pk_bf16_f32 v195, v218, v219\n\t" "v_mfma_f32_16x16x32_bf16 v[104:107], v[168:171], v[196:199], v[104:107]\n\t" "ds_read_b64_tr_b16 v[162:163], v4 offset:61952\n\t"
    "v_mfma_f32_4x4x4_16b_bf16 v[204:207], v[44:45], v[20:21], 0\n\t" "v_mfma_f32_4x4x4_16b_bf16 v[208:211], v[46:47], v[22:23], 0\n\t" "v_mfma_f32_4x4x4_16b_bf16 v[212:215], v[242:243], v[24:25], 0\n\t"
    "v_mfma_f32_4x4x4_16b_bf16 v[216:219], v[244:245], v[26:27], 0\n\t" "s_waitcnt lgkmcnt(4)\n\t" "v_mfma_f32_16x16x32_bf16 v[112:115], v[196:199], v[172:175], v[112:115]\n\t"
    "ds_read_b64_tr_b16 v[164:165], v5 offset:61440\n\t" "v_mfma_f32_16x16x32_bf16 v[116:119], v[200:203], v[172:175], v[116:119]\n\t" "ds_read_b64_tr_b16 v[166:167], v5 offset:61952\n\t"
    "v_mfma_f32_16x16x32_bf16 v[148:151], v[232:235], v[172:175], v[148:151]\n\t" "ds_read_b128 v[228:231], v1 offset:4096\n\t" "global_load_lds_dwordx4 v0, %[sb] offset:2048\n\t"
    "v_mfma_f32_16x16x32_bf16 v[124:127], v[200:203], v[176:179], v[124:127]\n\t" "ds_read_b64_tr_b16 v[168:169], v6 offset:61440\n\t" "v_add_u32 v0, 0x30000, v0\n\t"
    "v_mfma_f32_16x16x32_bf16 v[120:123], v[196:199], v[176:179], v[120:123]\n\t" "ds_read_b64_tr_b16 v[170:171], v6 offset:61952\n\t" "v_cvt_pk_bf16_f32 v20, v204, v205\n\t"
    "v_mfma_f32_16x16x32_bf16 v[128:131], v[196:199], v[180:183], v[128:131]\n\t" "ds_read_b64_tr_b16 v[172:173], v3 offset:63488\n\t" "v_cvt_pk_bf16_f32 v21, v206, v207\n\t"
    "v_mfma_f32_16x16x32_bf16 v[132:135], v[200:203], v[180:183], v[132:135]\n\t" "ds_read_b64_tr_b16 v[174:175], v3 offset:64000\n\t" "v_cvt_pk_bf16_f32 v22, v208, v209\n\t"
    "v_mfma_f32_16x16x32_bf16 v[140:143], v[200:203], v[184:187], v[140:143]\n\t" "ds_read_b64_tr_b16 v[176:177], v4 offset:63488\n\t" "v_cvt_pk_bf16_f32 v23, v210, v211\n\t"
    "v_mfma_f32_16x16x32_bf16 v[136:139], v[196:199], v[184:187], v[136:139]\n\t" "ds_read_b64_tr_b16 v[178:179], v4 offset:64000\n\t" "s_waitcnt lgkmcnt(4)\n\t"
    "v_mfma_f32_16x16x32_bf16 v[48:51], v[156:159], v[188:191], v[48:51]\n\t" "ds_read_b64_tr_b16 v[180:181], v5 offset:63488\n\t" "s_add_u32 m0, %[sl], 108544\n\t"
    "v_mfma_f32_16x16x32_bf16 v[52:55], v[156:159], v[192:195], v[52:55]\n\t" "ds_read_b64_tr_b16 v[182:183], v5 offset:64000\n\t" "v_cvt_pk_bf16_f32 v24, v212, v213\n\t"
    "v_mfma_f32_16x16x32_bf16 v[152:155], v[228:231], v[156:159], v[152:155]\n\t" "v_cvt_pk_bf16_f32 v25, v214, v215\n\t" "v_cvt_pk_bf16_f32 v26, v216, v217\n\t" "v_cvt_pk_bf16_f32 v27, v218, v219\n\t"
    "s_mov_b64 exec, s[60:61]\n\t" "ds_write_b128 v19, v[20:23] offset:8192\n\t" "ds_write_b128 v19, v[24:27] offset:9216\n\t" "s_mov_b64 exec, -1\n\t"
    "v_mfma_f32_16x16x32_bf16 v[60:63], v[160:163], v[192:195], v[60:63]\n\t" "ds_read_b64_tr_b16 v[184:185], v6 offset:63488\n\t" "global_load_lds_dwordx4 v0, %[sb] offset:-2048\n\t"
    "v_mfma_f32_4x4x4_16b_bf16 v[204:207], v[224:225], a[40:41], 0\n\t" "v_mfma_f32_4x4x4_16b_bf16 v[208:211], v[226:227], a[42:43], 0\n\t"
    "v_mfma_f32_4x4x4_16b_bf16 v[212:215], v[224:225], a[44:45], 0\n\t" "v_mfma_f32_4x4x4_16b_bf16 v[216:219], v[226:227], a[46:47], 0\n\t"
    "v_mfma_f32_16x16x32_bf16 v[56:59], v[160:163], v[188:191], v[56:59]\n\t" "ds_read_b64_tr_b16 v[186:187], v6 offset:64000\n\t" "v_mfma_f32_16x16x32_bf16 v[64:67], v[164:167], v[188:191], v[64:67]\n\t"
    "ds_read_b64_tr_b16 v[156:157], v7 offset:0\n\t" "v_mfma_f32_16x16x32_bf16 v[68:71], v[164:167], v[192:195], v[68:71]\n\t" "ds_read_b64_tr_b16 v[158:159], v7 offset:512\n\t"
    "v_cvt_pk_bf16_f32 v196, v204, v205\n\t" "v_mfma_f32_16x16x32_bf16 v[76:79], v[168:171], v[192:195], v[76:79]\n\t" "ds_read_b64_tr_b16 v[160:161], v8 offset:0\n\t"
    "v_cvt_pk_bf16_f32 v197, v206, v207\n\t" "v_mfma_f32_16x16x32_bf16 v[72:75], v[168:171], v[188:191], v[72:75]\n\t" "ds_read_b64_tr_b16 v[162:163], v8 offset:512\n\t" "s_waitcnt lgkmcnt(4)\n\t"
    "v_mfma_f32_16x16x32_bf16 v[80:83], v[172:175], v[188:191], v[80:83]\n\t" "ds_read_b64_tr_b16 v[164:165], v9 offset:0\n\t" "v_cvt_pk_bf16_f32 v198, v208, v209\n\t"
    "v_mfma_f32_16x16x32_bf16 v[84:87], v[172:175], v[192:195], v[84:87]\n\t" "ds_read_b64_tr_b16 v[166:167], v9 offset:512\n\t" "v_cvt_pk_bf16_f32 v199, v210, v211\n\t"
    "v_mfma_f32_16x16x32_bf16 v[144:147], v[172:175], v[228:231], v[144:147]\n\t" "ds_read_b128 v[220:223], v2 offset:1536\n\t" "global_load_lds_dwordx4 v0, %[sb] offset:0\n\t"
    "v_mfma_f32_16x16x32_bf16 v[92:95], v[176:179], v[192:195], v[92:95]\n\t" "ds_read_b64_tr_b16 v[168:169], v10 offset:0\n\t" "v_cvt_pk_bf16_f32 v200, v212, v213\n\t"
    "v_mfma_f32_16x16x32_bf16 v[88:91], v[176:179], v[188:191], v[88:91]\n\t" "ds_read_b64_tr_b16 v[170:171], v10 offset:512\n\t" "v_cvt_pk_bf16_f32 v201, v214, v215\n\t"
    "v_mfma_f32_16x16x32_bf16 v[96:99], v[180:183], v[188:191], v[96:99]\n\t" "ds_read_b64_tr_b16 v[172:173], v3 offset:0\n\t" "v_mfma_f32_16x16x32_bf16 v[100:103], v[180:183], v[192:195], v[100:103]\n\t"
    "ds_read_b64_tr_b16 v[174:175], v3 offset:512\n\t" "v_cvt_pk_bf16_f32 v202, v216, v217\n\t" "v_mfma_f32_16x16x32_bf16 v[108:111], v[184:187], v[192:195], v[108:111]\n\t"
    "ds_read_b64_tr_b16 v[176:177], v4 offset:0\n\t" "v_cvt_pk_bf16_f32 v203, v218, v219\n\t" "v_mfma_f32_16x16x32_bf16 v[104:107], v[184:187], v[188:191], v[104:107]\n\t"
    "ds_read_b64_tr_b16 v[178:179], v4 offset:512\n\t" "s_waitcnt lgkmcnt(4)\n\t" "v_mfma_f32_16x16x32_bf16 v[112:115], v[188:191], v[156:159], v[112:115]\n\t"
    "ds_read_b64_tr_b16 v[180:181], v5 offset:0\n\t" "v_mfma_f32_16x16x32_bf16 v[116:119], v[192:195], v[156:159], v[116:119]\n\t" "ds_read_b64_tr_b16 v[182:183], v5 offset:512\n\t"
    "v_mfma_f32_16x16x32_bf16 v[148:151], v[228:231], v[156:159], v[148:151]\n\t" "ds_read_b128 v[232:235], v1 offset:5120\n\t" "global_load_lds_dwordx4 v0, %[sb] offset:2048\n\t"
    "v_mfma_f32_16x16x32_bf16 v[124:127], v[192:195], v[160:163], v[124:127]\n\t" "ds_read_b64_tr_b16 v[184:185], v6 offset:0\n\t" "v_add_u32 v0, 0x30000, v0\n\t"
    "v_mfma_f32_16x16x32_bf16 v[120:123], v[188:191], v[160:163], v[120:123]\n\t" "ds_read_b64_tr_b16 v[186:187], v6 offset:512\n\t"
    "v_mfma_f32_16x16x32_bf16 v[128:131], v[188:191], v[164:167], v[128:131]\n\t" "ds_read_b64_tr_b16 v[156:157], v3 offset:2048\n\t"
    "v_mfma_f32_16x16x32_bf16 v[132:135], v[192:195], v[164:167], v[132:135]\n\t" "ds_read_b64_tr_b16 v[158:159], v3 offset:2560\n\t" "s_mov_b64 exec, 0xfffffff\n\t"
    "global_load_dword v17, v[14:15], off\n\t" "s_mov_b64 exec, -1\n\t" "v_mfma_f32_16x16x32_bf16 v[140:143], v[192:195], v[168:171], v[140:143]\n\t" "ds_read_b64_tr_b16 v[160:161], v4 offset:2048\n\t"
    "v_mfma_f32_16x16x32_bf16 v[136:139], v[188:191], v[168:171], v[136:139]\n\t" "ds_read_b64_tr_b16 v[162:163], v4 offset:2560\n\t" "s_waitcnt lgkmcnt(4)\n\t"
    "v_mfma_f32_16x16x32_bf16 v[48:51], v[172:175], v[196:199], v[48:51]\n\t" "ds_read_b64_tr_b16 v[164:165], v5 offset:2048\n\t" "s_add_u32 m0, %[sl], 120832\n\t"
    "v_mfma_f32_16x16x32_bf16 v[52:55], v[172:175], v[200:203], v[52:55]\n\t" "ds_read_b64_tr_b16 v[166:167], v5 offset:2560\n\t" "ds_read_b128 v[44:47], v18 offset:4096\n\t"
    "v_mfma_f32_16x16x32_bf16 v[152:155], v[232:235], v[172:175], v[152:155]\n\t" "global_load_lds_dwordx4 v0, %[sb] offset:-2048\n\t" "ds_read_b128 v[242:245], v18 offset:4352\n\t"
    "v_mfma_f32_16x16x32_bf16 v[60:63], v[176:179], v[200:203], v[60:63]\n\t" "ds_read_b64_tr_b16 v[168:169], v6 offset:2048\n\t" "v_mov_b32_e64 v20, v239\n\t"
    "v_mfma_f32_4x4x4_16b_bf16 v[204:207], v[220:221], a[48:49], 0\n\t" "v_mfma_f32_4x4x4_16b_bf16 v[208:211], v[222:223], a[50:51], 0\n\t"
    "v_mfma_f32_4x4x4_16b_bf16 v[212:215], v[220:221], a[52:53], 0\n\t" "v_mfma_f32_4x4x4_16b_bf16 v[216:219], v[222:223], a[54:55], 0\n\t" "v_mov_b32_e64 v21, v240\n\t"
    "v_mfma_f32_16x16x32_bf16 v[56:59], v[176:179], v[196:199], v[56:59]\n\t" "ds_read_b64_tr_b16 v[170:171], v6 offset:2560\n\t" "v_mfma_f32_16x16x32_bf16 v[64:67], v[180:183], v[196:199], v[64:67]\n\t"
    "ds_read_b64_tr_b16 v[172:173], v3 offset:4096\n\t" "v_mfma_f32_16x16x32_bf16 v[68:71], v[180:183], v[200:203], v[68:71]\n\t" "ds_read_b64_tr_b16 v[174:175], v3 offset:4608\n\t"
    "v_cvt_pk_bf16_f32 v188, v204, v205\n\t" "v_mfma_f32_16x16x32_bf16 v[76:79], v[184:187], v[200:203], v[76:79]\n\t" "ds_read_b64_tr_b16 v[176:177], v4 offset:4096\n\t"
    "v_cvt_pk_bf16_f32 v189, v206, v207\n\t" "v_mfma_f32_16x16x32_bf16 v[72:75], v[184:187], v[196:199], v[72:75]\n\t" "ds_read_b64_tr_b16 v[178:179], v4 offset:4608\n\t" "s_waitcnt lgkmcnt(4)\n\t"
    "v_mfma_f32_16x16x32_bf16 v[80:83], v[156:159], v[196:199], v[80:83]\n\t" "ds_read_b64_tr_b16 v[180:181], v5 offset:4096\n\t" "v_cvt_pk_bf16_f32 v190, v208, v209\n\t"
    "v_mfma_f32_16x16x32_bf16 v[84:87], v[156:159], v[200:203], v[84:87]\n\t" "ds_read_b64_tr_b16 v[182:183], v5 offset:4608\n\t" "v_cvt_pk_bf16_f32 v191, v210, v211\n\t"
    "v_mfma_f32_16x16x32_bf16 v[144:147], v[156:159], v[232:235], v[144:147]\n\t" "ds_read_b128 v[224:227], v2 offset:1792\n\t" "global_load_lds_dwordx4 v0, %[sb] offset:0\n\t"
    "v_mfma_f32_16x16x32_bf16 v[92:95], v[160:163], v[200:203], v[92:95]\n\t" "ds_read_b64_tr_b16 v[184:185], v6 offset:4096\n\t" "v_cvt_pk_bf16_f32 v192, v212, v213\n\t"
    "v_mfma_f32_16x16x32_bf16 v[88:91], v[160:163], v[196:199], v[88:91]\n\t" "ds_read_b64_tr_b16 v[186:187], v6 offset:4608\n\t" "v_cvt_pk_bf16_f32 v193, v214, v215\n\t"
    "v_mfma_f32_16x16x32_bf16 v[96:99], v[164:167], v[196:199], v[96:99]\n\t" "s_waitcnt vmcnt(12)\n\t" "s_barrier\n\t" "ds_read_b64_tr_b16 v[156:157], v3 offset:12288\n\t"
    "v_mfma_f32_16x16x32_bf16 v[100:103], v[164:167], v[200:203], v[100:103]\n\t" "ds_read_b64_tr_b16 v[158:159], v3 offset:12800\n\t" "v_cvt_pk_bf16_f32 v194, v216, v217\n\t"
    "v_mfma_f32_16x16x32_bf16 v[108:111], v[168:171], v[200:203], v[108:111]\n\t" "ds_read_b64_tr_b16 v[160:161], v4 offset:12288\n\t" "v_cvt_pk_bf16_f32 v195, v218, v219\n\t"
    "v_mfma_f32_16x16x32_bf16 v[104:107], v[168:171], v[196:199], v[104:107]\n\t" "ds_read_b64_tr_b16 v[162:163], v4 offset:12800\n\t" "v_mfma_f32_4x4x4_16b_bf16 v[204:207], v[44:45], v[28:29], 0\n\t"
    "v_mfma_f32_4x4x4_16b_bf16 v[208:211], v[46:47], v[30:31], 0\n\t" "v_mfma_f32_4x4x4_16b_bf16 v[212:215], v[242:243], v[32:33], 0\n\t"
    "v_mfma_f32_4x4x4_16b_bf16 v[216:219], v[244:245], v[34:35], 0\n\t" "s_waitcnt lgkmcnt(4)\n\t" "v_mfma_f32_16x16x32_bf16 v[112:115], v[196:199], v[172:175], v[112:115]\n\t"
    "ds_read_b64_tr_b16 v[164:165], v5 offset:12288\n\t" "v_mfma_f32_16x16x32_bf16 v[116:119], v[200:203], v[172:175], v[116:119]\n\t" "ds_read_b64_tr_b16 v[166:167], v5 offset:12800\n\t"
    "v_mfma_f32_16x16x32_bf16 v[148:151], v[232:235], v[172:175], v[148:151]\n\t" "ds_read_b128 v[228:231], v1 offset:6144\n\t" "global_load_lds_dwordx4 v0, %[sb] offset:2048\n\t"
    "v_mfma_f32_16x16x32_bf16 v[124:127], v[200:203], v[176:179], v[124:127]\n\t" "ds_read_b64_tr_b16 v[168:169], v6 offset:12288\n\t" "s_cmp_eq_u32 %[rot], 5\n\t" "s_cselect_b32 s42, s43, 0x30000\n\t"
    "v_add_u32 v0, s42, v0\n\t" "v_mfma_f32_16x16x32_bf16 v[120:123], v[196:199], v[176:179], v[120:123]\n\t" "ds_read_b64_tr_b16 v[170:171], v6 offset:12800\n\t" "v_cvt_pk_bf16_f32 v28, v204, v205\n\t"
    "v_mfma_f32_16x16x32_bf16 v[128:131], v[196:199], v[180:183], v[128:131]\n\t" "ds_read_b64_tr_b16 v[172:173], v3 offset:14336\n\t" "v_cvt_pk_bf16_f32 v29, v206, v207\n\t"
    "v_mfma_f32_16x16x32_bf16 v[132:135], v[200:203], v[180:183], v[132:135]\n\t" "ds_read_b64_tr_b16 v[174:175], v3 offset:14848\n\t" "v_cvt_pk_bf16_f32 v30, v208, v209\n\t"
    "v_mfma_f32_16x16x32_bf16 v[140:143], v[200:203], v[184:187], v[140:143]\n\t" "ds_read_b64_tr_b16 v[176:177], v4 offset:14336\n\t" "v_cvt_pk_bf16_f32 v31, v210, v211\n\t"
    "v_mfma_f32_16x16x32_bf16 v[136:139], v[196:199], v[184:187], v[136:139]\n\t" "ds_read_b64_tr_b16 v[178:179], v4 offset:14848\n\t" "s_waitcnt lgkmcnt(4)\n\t"
    "v_mfma_f32_16x16x32_bf16 v[48:51], v[156:159], v[188:191], v[48:51]\n\t" "ds_read_b64_tr_b16 v[180:181], v5 offset:14336\n\t" "s_add_u32 m0, %[sl], 71680\n\t"
    "v_mfma_f32_16x16x32_bf16 v[52:55], v[156:159], v[192:195], v[52:55]\n\t" "ds_read_b64_tr_b16 v[182:183], v5 offset:14848\n\t" "v_cvt_pk_bf16_f32 v32, v212, v213\n\t"
    "v_mfma_f32_16x16x32_bf16 v[152:155], v[228:231], v[156:159], v[152:155]\n\t" "v_cvt_pk_bf16_f32 v33, v214, v215\n\t" "v_cvt_pk_bf16_f32 v34, v216, v217\n\t" "v_cvt_pk_bf16_f32 v35, v218, v219\n\t"
    "s_mov_b64 exec, s[60:61]\n\t" "ds_write_b128 v19, v[28:31] offset:16384\n\t" "ds_write_b128 v19, v[32:35] offset:17408\n\t" "s_mov_b64 exec, -1\n\t"
    "v_mfma_f32_16x16x32_bf16 v[60:63], v[160:163], v[192:195], v[60:63]\n\t" "ds_read_b64_tr_b16 v[184:185], v6 offset:14336\n\t" "global_load_lds_dwordx4 v0, %[sb] offset:-2048\n\t"
    "v_mfma_f32_4x4x4_16b_bf16 v[204:207], v[224:225], a[56:57], 0\n\t" "v_mfma_f32_4x4x4_16b_bf16 v[208:211], v[226:227], a[58:59], 0\n\t"
    "v_mfma_f32_4x4x4_16b_bf16 v[212:215], v[224:225], a[60:61], 0\n\t" "v_mfma_f32_4x4x4_16b_bf16 v[216:219], v[226:227], a[62:63], 0\n\t" "v_permlane16_swap_b32 v239, v20\n\t"
    "v_mfma_f32_16x16x32_bf16 v[56:59], v[160:163], v[188:191], v[56:59]\n\t" "ds_read_b64_tr_b16 v[186:187], v6 offset:14848\n\t" "v_permlane16_swap_b32 v240, v21\n\t"
    "v_mfma_f32_16x16x32_bf16 v[64:67], v[164:167], v[188:191], v[64:67]\n\t" "ds_read_b64_tr_b16 v[156:157], v3 offset:16384\n\t" "v_add_f32 v239, v239, v20\n\t"
    "v_mfma_f32_16x16x32_bf16 v[68:71], v[164:167], v[192:195], v[68:71]\n\t" "ds_read_b64_tr_b16 v[158:159], v3 offset:16896\n\t" "v_cvt_pk_bf16_f32 v196, v204, v205\n\t"
    "v_mfma_f32_16x16x32_bf16 v[76:79], v[168:171], v[192:195], v[76:79]\n\t" "ds_read_b64_tr_b16 v[160:161], v4 offset:16384\n\t" "v_cvt_pk_bf16_f32 v197, v206, v207\n\t"
    "v_mfma_f32_16x16x32_bf16 v[72:75], v[168:171], v[188:191], v[72:75]\n\t" "ds_read_b64_tr_b16 v[162:163], v4 offset:16896\n\t" "s_waitcnt lgkmcnt(4)\n\t"
    "v_mfma_f32_16x16x32_bf16 v[80:83], v[172:175], v[188:191], v[80:83]\n\t" "ds_read_b64_tr_b16 v[164:165], v5 offset:16384\n\t" "v_cvt_pk_bf16_f32 v198, v208, v209\n\t"
    "v_mfma_f32_16x16x32_bf16 v[84:87], v[172:175], v[192:195], v[84:87]\n\t" "ds_read_b64_tr_b16 v[166:167], v5 offset:16896\n\t" "v_cvt_pk_bf16_f32 v199, v210, v211\n\t"
    "v_mfma_f32_16x16x32_bf16 v[144:147], v[172:175], v[228:231], v[144:147]\n\t" "ds_read_b128 v[220:223], v2 offset:2048\n\t" "global_load_lds_dwordx4 v0, %[sb] offset:0\n\t"
    "v_mfma_f32_16x16x32_bf16 v[92:95], v[176:179], v[192:195], v[92:95]\n\t" "ds_read_b64_tr_b16 v[168:169], v6 offset:16384\n\t" "v_cvt_pk_bf16_f32 v200, v212, v213\n\t"
    "v_mfma_f32_16x16x32_bf16 v[88:91], v[176:179], v[188:191], v[88:91]\n\t" "ds_read_b64_tr_b16 v[170:171], v6 offset:16896\n\t" "v_cvt_pk_bf16_f32 v201, v214, v215\n\t"
    "v_mfma_f32_16x16x32_bf16 v[96:99], v[180:183], v[188:191], v[96:99]\n\t" "ds_read_b64_tr_b16 v[172:173], v3 offset:24576\n\t" "v_add_f32 v240, v240, v21\n\t"
    "v_mfma_f32_16x16x32_bf16 v[100:103], v[180:183], v[192:195], v[100:103]\n\t" "ds_read_b64_tr_b16 v[174:175], v3 offset:25088\n\t" "v_cvt_pk_bf16_f32 v202, v216, v217\n\t"
    "v_mfma_f32_16x16x32_bf16 v[108:111], v[184:187], v[192:195], v[108:111]\n\t" "ds_read_b64_tr_b16 v[176:177], v4 offset:24576\n\t" "v_cvt_pk_bf16_f32 v203, v218, v219\n\t"
    "v_mfma_f32_16x16x32_bf16 v[104:107], v[184:187], v[188:191], v[104:107]\n\t" "ds_read_b64_tr_b16 v[178:179], v4 offset:25088\n\t" "s_waitcnt lgkmcnt(4)\n\t"
    "v_mfma_f32_16x16x32_bf16 v[112:115], v[188:191], v[156:159], v[112:115]\n\t" "ds_read_b64_tr_b16 v[180:181], v5 offset:24576\n\t" "v_mov_b32 v20, v239\n\t"
    "v_mfma_f32_16x16x32_bf16 v[116:119], v[192:195], v[156:159], v[116:119]\n\t" "ds_read_b64_tr_b16 v[182:183], v5 offset:25088\n\t" "v_mov_b32_e64 v21, v240\n\t"
    "v_mfma_f32_16x16x32_bf16 v[148:151], v[228:231], v[156:159], v[148:151]\n\t" "ds_read_b128 v[232:235], v1 offset:7168\n\t" "global_load_lds_dwordx4 v0, %[sb] offset:2048\n\t"
    "v_mfma_f32_16x16x32_bf16 v[124:127], v[192:195], v[160:163], v[124:127]\n\t" "ds_read_b64_tr_b16 v[184:185], v6 offset:24576\n\t" "v_add_u32 v0, 0x30000, v0\n\t"
    "v_mfma_f32_16x16x32_bf16 v[120:123], v[188:191], v[160:163], v[120:123]\n\t" "ds_read_b64_tr_b16 v[186:187], v6 offset:25088\n\t"
    "v_mfma_f32_16x16x32_bf16 v[128:131], v[188:191], v[164:167], v[128:131]\n\t" "ds_read_b64_tr_b16 v[156:157], v3 offset:26624\n\t"
    "v_mfma_f32_16x16x32_bf16 v[132:135], v[192:195], v[164:167], v[132:135]\n\t" "ds_read_b64_tr_b16 v[158:159], v3 offset:27136\n\t" "v_permlane32_swap_b32 v239, v20\n\t"
    "v_mfma_f32_16x16x32_bf16 v[140:143], v[192:195], v[168:171], v[140:143]\n\t" "ds_read_b64_tr_b16 v[160:161], v4 offset:26624\n\t" "v_permlane32_swap_b32 v240, v21\n\t"
    "v_mfma_f32_16x16x32_bf16 v[136:139], v[188:191], v[168:171], v[136:139]\n\t" "ds_read_b64_tr_b16 v[162:163], v4 offset:27136\n\t" "s_waitcnt lgkmcnt(4)\n\t"
    "v_mfma_f32_16x16x32_bf16 v[48:51], v[172:175], v[196:199], v[48:51]\n\t" "ds_read_b64_tr_b16 v[164:165], v5 offset:26624\n\t" "s_add_u32 m0, %[sl], 83968\n\t"
    "v_mfma_f32_16x16x32_bf16 v[52:55], v[172:175], v[200:203], v[52:55]\n\t" "ds_read_b64_tr_b16 v[166:167], v5 offset:27136\n\t" "ds_read_b128 v[44:47], v18 offset:6144\n\t"
    "v_mfma_f32_16x16x32_bf16 v[152:155], v[232:235], v[172:175], v[152:155]\n\t" "global_load_lds_dwordx4 v0, %[sb] offset:-2048\n\t" "ds_read_b128 v[242:245], v18 offset:6400\n\t"
    "v_mfma_f32_16x16x32_bf16 v[60:63], v[176:179], v[200:203], v[60:63]\n\t" "ds_read_b64_tr_b16 v[168:169], v6 offset:26624\n\t" "v_add_f32 v239, v239, v20\n\t"
    "v_mfma_f32_4x4x4_16b_bf16 v[204:207], v[220:221], a[64:65], 0\n\t" "v_mfma_f32_4x4x4_16b_bf16 v[208:211], v[222:223], a[66:67], 0\n\t"
    "v_mfma_f32_4x4x4_16b_bf16 v[212:215], v[220:221], a[68:69], 0\n\t" "v_mfma_f32_4x4x4_16b_bf16 v[216:219], v[222:223], a[70:71], 0\n\t" "v_add_f32_e64 v240, v240, v21\n\t"
    "v_mfma_f32_16x16x32_bf16 v[56:59], v[176:179], v[196:199], v[56:59]\n\t" "ds_read_b64_tr_b16 v[170:171], v6 offset:27136\n\t" "v_mul_f32 v236, 0x3a800000, v236\n\t"
    "v_mfma_f32_16x16x32_bf16 v[64:67], v[180:183], v[196:199], v[64:67]\n\t" "ds_read_b64_tr_b16 v[172:173], v3 offset:28672\n\t" "v_mul_f32 v237, 0x3a800000, v237\n\t"
    "v_mfma_f32_16x16x32_bf16 v[68:71], v[180:183], v[200:203], v[68:71]\n\t" "ds_read_b64_tr_b16 v[174:175], v3 offset:29184\n\t" "v_cvt_pk_bf16_f32 v188, v204, v205\n\t"
    "v_mfma_f32_16x16x32_bf16 v[76:79], v[184:187], v[200:203], v[76:79]\n\t" "ds_read_b64_tr_b16 v[176:177], v4 offset:28672\n\t" "v_cvt_pk_bf16_f32 v189, v206, v207\n\t"
    "v_mfma_f32_16x16x32_bf16 v[72:75], v[184:187], v[196:199], v[72:75]\n\t" "ds_read_b64_tr_b16 v[178:179], v4 offset:29184\n\t" "s_waitcnt lgkmcnt(4)\n\t"
    "v_mfma_f32_16x16x32_bf16 v[80:83], v[156:159], v[196:199], v[80:83]\n\t" "ds_read_b64_tr_b16 v[180:181], v5 offset:28672\n\t" "v_cvt_pk_bf16_f32 v190, v208, v209\n\t"
    "v_mfma_f32_16x16x32_bf16 v[84:87], v[156:159], v[200:203], v[84:87]\n\t" "ds_read_b64_tr_b16 v[182:183], v5 offset:29184\n\t" "v_cvt_pk_bf16_f32 v191, v210, v211\n\t"
    "v_mfma_f32_16x16x32_bf16 v[144:147], v[156:159], v[232:235], v[144:147]\n\t" "ds_read_b128 v[224:227], v2 offset:2304\n\t" "global_load_lds_dwordx4 v0, %[sb] offset:0\n\t"
    "v_mfma_f32_16x16x32_bf16 v[92:95], v[160:163], v[200:203], v[92:95]\n\t" "ds_read_b64_tr_b16 v[184:185], v6 offset:28672\n\t" "v_cvt_pk_bf16_f32 v192, v212, v213\n\t"
    "v_mfma_f32_16x16x32_bf16 v[88:91], v[160:163], v[196:199], v[88:91]\n\t" "ds_read_b64_tr_b16 v[186:187], v6 offset:29184\n\t" "v_cvt_pk_bf16_f32 v193, v214, v215\n\t"
    "v_mfma_f32_16x16x32_bf16 v[96:99], v[164:167], v[196:199], v[96:99]\n\t" "s_waitcnt vmcnt(12)\n\t" "s_barrier\n\t" "ds_read_b64_tr_b16 v[156:157], v3 offset:36864\n\t"
    "v_mfma_f32_16x16x32_bf16 v[100:103], v[164:167], v[200:203], v[100:103]\n\t" "ds_read_b64_tr_b16 v[158:159], v3 offset:37376\n\t" "v_cvt_pk_bf16_f32 v194, v216, v217\n\t"
    "v_mfma_f32_16x16x32_bf16 v[108:111], v[168:171], v[200:203], v[108:111]\n\t" "ds_read_b64_tr_b16 v[160:161], v4 offset:36864\n\t" "v_cvt_pk_bf16_f32 v195, v218, v219\n\t"
    "v_mfma_f32_16x16x32_bf16 v[104:107], v[168:171], v[196:199], v[104:107]\n\t" "ds_read_b64_tr_b16 v[162:163], v4 offset:37376\n\t" "v_mfma_f32_4x4x4_16b_bf16 v[204:207], v[44:45], v[36:37], 0\n\t"
    "v_mfma_f32_4x4x4_16b_bf16 v[208:211], v[46:47], v[38:39], 0\n\t" "v_mfma_f32_4x4x4_16b_bf16 v[212:215], v[242:243], v[40:41], 0\n\t"
    "v_mfma_f32_4x4x4_16b_bf16 v[216:219], v[244:245], v[42:43], 0\n\t" "s_waitcnt lgkmcnt(4)\n\t" "v_mfma_f32_16x16x32_bf16 v[112:115], v[196:199], v[172:175], v[112:115]\n\t"
    "ds_read_b64_tr_b16 v[164:165], v5 offset:36864\n\t" "v_mfma_f32_16x16x32_bf16 v[116:119], v[200:203], v[172:175], v[116:119]\n\t" "ds_read_b64_tr_b16 v[166:167], v5 offset:37376\n\t"
    "v_mfma_f32_16x16x32_bf16 v[148:151], v[232:235], v[172:175], v[148:151]\n\t" "ds_read_b128 v[228:231], v1 offset:8192\n\t" "global_load_lds_dwordx4 v0, %[sb] offset:2048\n\t"
    "v_mfma_f32_16x16x32_bf16 v[124:127], v[200:203], v[176:179], v[124:127]\n\t" "ds_read_b64_tr_b16 v[168:169], v6 offset:36864\n\t" "v_add_u32 v0, 0x30000, v0\n\t"
    "v_mfma_f32_16x16x32_bf16 v[120:123], v[196:199], v[176:179], v[120:123]\n\t" "ds_read_b64_tr_b16 v[170:171], v6 offset:37376\n\t" "v_add_u32 v16, 0x70000, v16\n\t"
    "v_mfma_f32_16x16x32_bf16 v[128:131], v[196:199], v[180:183], v[128:131]\n\t" "ds_read_b64_tr_b16 v[172:173], v3 offset:38912\n\t" "v_cvt_pk_bf16_f32 v36, v204, v205\n\t"
    "v_mfma_f32_16x16x32_bf16 v[132:135], v[200:203], v[180:183], v[132:135]\n\t" "ds_read_b64_tr_b16 v[174:175], v3 offset:39424\n\t" "s_mov_b64 exec, %[tm]\n\t" "global_load_dword v17, v16, %[pf]\n\t"
    "s_mov_b64 exec, -1\n\t" "v_mfma_f32_16x16x32_bf16 v[140:143], v[200:203], v[184:187], v[140:143]\n\t" "ds_read_b64_tr_b16 v[176:177], v4 offset:38912\n\t" "v_cvt_pk_bf16_f32 v37, v206, v207\n\t"
    "v_mfma_f32_16x16x32_bf16 v[136:139], v[196:199], v[184:187], v[136:139]\n\t" "ds_read_b64_tr_b16 v[178:179], v4 offset:39424\n\t" "s_waitcnt lgkmcnt(4)\n\t"
    "v_mfma_f32_16x16x32_bf16 v[48:51], v[156:159], v[188:191], v[48:51]\n\t" "ds_read_b64_tr_b16 v[180:181], v5 offset:38912\n\t" "s_add_u32 m0, %[sl], 22528\n\t"
    "v_mfma_f32_16x16x32_bf16 v[52:55], v[156:159], v[192:195], v[52:55]\n\t" "ds_read_b64_tr_b16 v[182:183], v5 offset:39424\n\t" "v_cvt_pk_bf16_f32 v38, v208, v209\n\t"
    "v_mfma_f32_16x16x32_bf16 v[152:155], v[228:231], v[156:159], v[152:155]\n\t" "v_cvt_pk_bf16_f32 v39, v210, v211\n\t" "v_cvt_pk_bf16_f32 v40, v212, v213\n\t" "v_cvt_pk_bf16_f32 v41, v214, v215\n\t"
    "v_cvt_pk_bf16_f32 v42, v216, v217\n\t" "v_cvt_pk_bf16_f32 v43, v218, v219\n\t" "s_mov_b64 exec, s[60:61]\n\t" "ds_write_b128 v19, v[36:39] offset:24576\n\t"
    "ds_write_b128 v19, v[40:43] offset:25600\n\t" "s_mov_b64 exec, -1\n\t" "v_mfma_f32_16x16x32_bf16 v[60:63], v[160:163], v[192:195], v[60:63]\n\t" "ds_read_b64_tr_b16 v[184:185], v6 offset:38912\n\t"
    "global_load_lds_dwordx4 v0, %[sb] offset:-2048\n\t" "v_mfma_f32_4x4x4_16b_bf16 v[204:207], v[224:225], a[72:73], 0\n\t" "v_mfma_f32_4x4x4_16b_bf16 v[208:211], v[226:227], a[74:75], 0\n\t"
    "v_mfma_f32_4x4x4_16b_bf16 v[212:215], v[224:225], a[76:77], 0\n\t" "v_mfma_f32_4x4x4_16b_bf16 v[216:219], v[226:227], a[78:79], 0\n\t" "v_mul_f32 v238, 0x3a800000, v238\n\t"
    "v_mfma_f32_16x16x32_bf16 v[56:59], v[160:163], v[188:191], v[56:59]\n\t" "ds_read_b64_tr_b16 v[186:187], v6 offset:39424\n\t" "v_mul_f32 v239, 0x3a800000, v239\n\t"
    "v_mfma_f32_16x16x32_bf16 v[64:67], v[164:167], v[188:191], v[64:67]\n\t" "ds_read_b64_tr_b16 v[156:157], v3 offset:40960\n\t" "global_load_lds_dwordx4 v0, %[sb] offset:0\n\t"
    "v_mfma_f32_16x16x32_bf16 v[68:71], v[164:167], v[192:195], v[68:71]\n\t" "ds_read_b64_tr_b16 v[158:159], v3 offset:41472\n\t" "v_cvt_pk_bf16_f32 v196, v204, v205\n\t"
    "v_mfma_f32_16x16x32_bf16 v[76:79], v[168:171], v[192:195], v[76:79]\n\t" "ds_read_b64_tr_b16 v[160:161], v4 offset:40960\n\t" "v_cvt_pk_bf16_f32 v197, v206, v207\n\t"
    "v_mfma_f32_16x16x32_bf16 v[72:75], v[168:171], v[188:191], v[72:75]\n\t" "ds_read_b64_tr_b16 v[162:163], v4 offset:41472\n\t" "s_waitcnt lgkmcnt(4)\n\t"
    "v_mfma_f32_16x16x32_bf16 v[80:83], v[172:175], v[188:191], v[80:83]\n\t" "ds_read_b64_tr_b16 v[164:165], v5 offset:40960\n\t" "v_cvt_pk_bf16_f32 v198, v208, v209\n\t"
    "v_mfma_f32_16x16x32_bf16 v[84:87], v[172:175], v[192:195], v[84:87]\n\t" "ds_read_b64_tr_b16 v[166:167], v5 offset:41472\n\t" "v_cvt_pk_bf16_f32 v199, v210, v211\n\t"
    "v_mfma_f32_16x16x32_bf16 v[144:147], v[172:175], v[228:231], v[144:147]\n\t" "ds_read_b128 v[220:223], v2 offset:2560\n\t" "global_load_lds_dwordx4 v0, %[sb] offset:2048\n\t"
    "v_mfma_f32_16x16x32_bf16 v[92:95], v[176:179], v[192:195], v[92:95]\n\t" "ds_read_b64_tr_b16 v[168:169], v6 offset:40960\n\t" "v_cvt_pk_bf16_f32 v200, v212, v213\n\t"
    "v_mfma_f32_16x16x32_bf16 v[88:91], v[176:179], v[188:191], v[88:91]\n\t" "ds_read_b64_tr_b16 v[170:171], v6 offset:41472\n\t" "v_cvt_pk_bf16_f32 v201, v214, v215\n\t"
    "v_mfma_f32_16x16x32_bf16 v[96:99], v[180:183], v[188:191], v[96:99]\n\t" "ds_read_b64_tr_b16 v[172:173], v7 offset:8192\n\t" "v_add_u32 v0, 0x30000, v0\n\t"
    "v_mfma_f32_16x16x32_bf16 v[100:103], v[180:183], v[192:195], v[100:103]\n\t" "ds_read_b64_tr_b16 v[174:175], v7 offset:8704\n\t" "v_cvt_pk_bf16_f32 v202, v216, v217\n\t"
    "v_mfma_f32_16x16x32_bf16 v[108:111], v[184:187], v[192:195], v[108:111]\n\t" "ds_read_b64_tr_b16 v[176:177], v8 offset:8192\n\t" "v_cvt_pk_bf16_f32 v203, v218, v219\n\t"
    "v_mfma_f32_16x16x32_bf16 v[104:107], v[184:187], v[188:191], v[104:107]\n\t" "ds_read_b64_tr_b16 v[178:179], v8 offset:8704\n\t" "s_waitcnt lgkmcnt(4)\n\t"
    "v_mfma_f32_16x16x32_bf16 v[112:115], v[188:191], v[156:159], v[112:115]\n\t" "ds_read_b64_tr_b16 v[180:181], v9 offset:8192\n\t" "v_mul_f32 v240, 0x3a800000, v240\n\t"
    "v_mfma_f32_16x16x32_bf16 v[116:119], v[192:195], v[156:159], v[116:119]\n\t" "ds_read_b64_tr_b16 v[182:183], v9 offset:8704\n\t" "v_mul_f32 v241, 0x3a800000, v241\n\t"
    "v_mfma_f32_16x16x32_bf16 v[148:151], v[228:231], v[156:159], v[148:151]\n\t" "ds_read_b128 v[232:235], v1 offset:9216\n\t" "global_load_dwordx4 a[32:35], v11, %[sm] nt\n\t"
    "v_mfma_f32_16x16x32_bf16 v[124:127], v[192:195], v[160:163], v[124:127]\n\t" "ds_read_b64_tr_b16 v[184:185], v10 offset:8192\n\t" "v_add_u32 v11, 0x3000, v11\n\t"
    "v_mfma_f32_16x16x32_bf16 v[120:123], v[188:191], v[160:163], v[120:123]\n\t" "ds_read_b64_tr_b16 v[186:187], v10 offset:8704\n\t" "v_fma_f32 v239, -v236, v236, v239\n\t"
    "v_mfma_f32_16x16x32_bf16 v[128:131], v[188:191], v[164:167], v[128:131]\n\t" "ds_read_b64_tr_b16 v[156:157], v7 offset:10240\n\t" "v_fma_f32 v240, -v237, v237, v240\n\t"
    "v_mfma_f32_16x16x32_bf16 v[132:135], v[192:195], v[164:167], v[132:135]\n\t" "ds_read_b64_tr_b16 v[158:159], v7 offset:10752\n\t" "global_load_dwordx4 a[36:39], v11, %[sm] nt\n\t"
    "v_mfma_f32_16x16x32_bf16 v[140:143], v[192:195], v[168:171], v[140:143]\n\t" "ds_read_b64_tr_b16 v[160:161], v8 offset:10240\n\t" "v_fma_f32 v241, -v238, v238, v241\n\t"
    "v_mfma_f32_16x16x32_bf16 v[136:139], v[188:191], v[168:171], v[136:139]\n\t" "ds_read_b64_tr_b16 v[162:163], v8 offset:10752\n\t" "s_waitcnt lgkmcnt(4)\n\t"
    "v_mfma_f32_16x16x32_bf16 v[48:51], v[172:175], v[196:199], v[48:51]\n\t" "ds_read_b64_tr_b16 v[164:165], v9 offset:10240\n\t" "s_add_u32 m0, %[sl], 34816\n\t"
    "v_mfma_f32_16x16x32_bf16 v[52:55], v[172:175], v[200:203], v[52:55]\n\t" "ds_read_b64_tr_b16 v[166:167], v9 offset:10752\n\t" "v_max_f32_e64 v239, 0, v239\n\t"
    "v_mfma_f32_16x16x32_bf16 v[152:155], v[232:235], v[172:175], v[152:155]\n\t" "global_load_lds_dwordx4 v0, %[sb] offset:-2048\n\t" "v_max_f32_e64 v240, 0, v240\n\t"
    "v_mfma_f32_16x16x32_bf16 v[60:63], v[176:179], v[200:203], v[60:63]\n\t" "ds_read_b64_tr_b16 v[168:169], v10 offset:10240\n\t" "v_max_f32_e64 v241, 0, v241\n\t"
    "v_mfma_f32_4x4x4_16b_bf16 v[204:207], v[220:221], a[80:81], 0\n\t" "v_mfma_f32_4x4x4_16b_bf16 v[208:211], v[222:223], a[82:83], 0\n\t"
    "v_mfma_f32_4x4x4_16b_bf16 v[212:215], v[220:221], a[84:85], 0\n\t" "v_mfma_f32_4x4x4_16b_bf16 v[216:219], v[222:223], a[86:87], 0\n\t" "v_add_f32 v239, 0x3727c5ac, v239\n\t"
    "v_mfma_f32_16x16x32_bf16 v[56:59], v[176:179], v[196:199], v[56:59]\n\t" "ds_read_b64_tr_b16 v[170:171], v10 offset:10752\n\t" "v_add_f32 v240, 0x3727c5ac, v240\n\t"
    "v_mfma_f32_16x16x32_bf16 v[64:67], v[180:183], v[196:199], v[64:67]\n\t" "ds_read_b64_tr_b16 v[172:173], v7 offset:12288\n\t" "global_load_lds_dwordx4 v0, %[sb] offset:0\n\t"
    "v_mfma_f32_16x16x32_bf16 v[68:71], v[180:183], v[200:203], v[68:71]\n\t" "ds_read_b64_tr_b16 v[174:175], v7 offset:12800\n\t" "v_cvt_pk_bf16_f32 v188, v204, v205\n\t"
    "v_mfma_f32_16x16x32_bf16 v[76:79], v[184:187], v[200:203], v[76:79]\n\t" "ds_read_b64_tr_b16 v[176:177], v8 offset:12288\n\t" "v_cvt_pk_bf16_f32 v189, v206, v207\n\t"
    "v_mfma_f32_16x16x32_bf16 v[72:75], v[184:187], v[196:199], v[72:75]\n\t" "ds_read_b64_tr_b16 v[178:179], v8 offset:12800\n\t" "s_waitcnt lgkmcnt(4)\n\t"
    "v_mfma_f32_16x16x32_bf16 v[80:83], v[156:159], v[196:199], v[80:83]\n\t" "ds_read_b64_tr_b16 v[180:181], v9 offset:12288\n\t" "v_cvt_pk_bf16_f32 v190, v208, v209\n\t"
    "v_mfma_f32_16x16x32_bf16 v[84:87], v[156:159], v[200:203], v[84:87]\n\t" "ds_read_b64_tr_b16 v[182:183], v9 offset:12800\n\t" "v_cvt_pk_bf16_f32 v191, v210, v211\n\t"
    "v_mfma_f32_16x16x32_bf16 v[144:147], v[156:159], v[232:235], v[144:147]\n\t" "ds_read_b128 v[224:227], v2 offset:2816\n\t" "global_load_lds_dwordx4 v0, %[sb] offset:2048\n\t"
    "v_mfma_f32_16x16x32_bf16 v[92:95], v[160:163], v[200:203], v[92:95]\n\t" "ds_read_b64_tr_b16 v[184:185], v10 offset:12288\n\t" "v_cvt_pk_bf16_f32 v192, v212, v213\n\t"
    "v_mfma_f32_16x16x32_bf16 v[88:91], v[160:163], v[196:199], v[88:91]\n\t" "ds_read_b64_tr_b16 v[186:187], v10 offset:12800\n\t" "v_cvt_pk_bf16_f32 v193, v214, v215\n\t"
    "v_mfma_f32_16x16x32_bf16 v[96:99], v[164:167], v[196:199], v[96:99]\n\t" "s_waitcnt vmcnt(15)\n\t" "s_barrier\n\t" "ds_read_b64_tr_b16 v[156:157], v7 offset:20480\n\t"
    "v_mfma_f32_16x16x32_bf16 v[100:103], v[164:167], v[200:203], v[100:103]\n\t" "ds_read_b64_tr_b16 v[158:159], v7 offset:20992\n\t" "v_cvt_pk_bf16_f32 v194, v216, v217\n\t"
    "v_mfma_f32_16x16x32_bf16 v[108:111], v[168:171], v[200:203], v[108:111]\n\t" "ds_read_b64_tr_b16 v[160:161], v8 offset:20480\n\t" "v_cvt_pk_bf16_f32 v195, v218, v219\n\t"
    "v_mfma_f32_16x16x32_bf16 v[104:107], v[168:171], v[196:199], v[104:107]\n\t" "ds_read_b64_tr_b16 v[162:163], v8 offset:20992\n\t" "s_waitcnt lgkmcnt(4)\n\t"
    "v_mfma_f32_16x16x32_bf16 v[112:115], v[196:199], v[172:175], v[112:115]\n\t" "ds_read_b64_tr_b16 v[164:165], v9 offset:20480\n\t" "s_cmp_eq_u32 %[rot], 4\n\t" "s_cselect_b32 s42, s43, 0x30000\n\t"
    "v_add_u32 v0, s42, v0\n\t" "v_mfma_f32_16x16x32_bf16 v[116:119], v[200:203], v[172:175], v[116:119]\n\t" "ds_read_b64_tr_b16 v[166:167], v9 offset:20992\n\t" "v_add_u32 v11, 0x3000, v11\n\t"
    "v_mfma_f32_16x16x32_bf16 v[148:151], v[232:235], v[172:175], v[148:151]\n\t" "ds_read_b128 v[228:231], v1 offset:10240\n\t" "global_load_dwordx4 a[40:43], v11, %[sm] nt\n\t"
    "v_mfma_f32_16x16x32_bf16 v[124:127], v[200:203], v[176:179], v[124:127]\n\t" "ds_read_b64_tr_b16 v[168:169], v10 offset:20480\n\t" "v_add_u32 v11, 0x3000, v11\n\t"
    "v_mfma_f32_16x16x32_bf16 v[120:123], v[196:199], v[176:179], v[120:123]\n\t" "ds_read_b64_tr_b16 v[170:171], v10 offset:20992\n\t" "v_add_f32 v241, 0x3727c5ac, v241\n\t"
    "v_mfma_f32_16x16x32_bf16 v[128:131], v[196:199], v[180:183], v[128:131]\n\t" "ds_read_b64_tr_b16 v[172:173], v7 offset:22528\n\t" "v_rsq_f32_e64 v20, v239\n\t"
    "v_mfma_f32_16x16x32_bf16 v[132:135], v[200:203], v[180:183], v[132:135]\n\t" "ds_read_b64_tr_b16 v[174:175], v7 offset:23040\n\t" "global_load_dwordx4 a[44:47], v11, %[sm] nt\n\t"
    "v_mfma_f32_16x16x32_bf16 v[140:143], v[200:203], v[184:187], v[140:143]\n\t" "ds_read_b64_tr_b16 v[176:177], v8 offset:22528\n\t" "v_rsq_f32_e64 v21, v240\n\t"
    "v_mfma_f32_16x16x32_bf16 v[136:139], v[196:199], v[184:187], v[136:139]\n\t" "ds_read_b64_tr_b16 v[178:179], v8 offset:23040\n\t" "s_waitcnt lgkmcnt(4)\n\t"
    "v_mfma_f32_16x16x32_bf16 v[48:51], v[156:159], v[188:191], v[48:51]\n\t" "ds_read_b64_tr_b16 v[180:181], v9 offset:22528\n\t" "s_add_u32 m0, %[sl], 47104\n\t"
    "v_mfma_f32_16x16x32_bf16 v[52:55], v[156:159], v[192:195], v[52:55]\n\t" "ds_read_b64_tr_b16 v[182:183], v9 offset:23040\n\t" "v_rsq_f32 v22, v241\n\t"
    "v_mfma_f32_16x16x32_bf16 v[152:155], v[228:231], v[156:159], v[152:155]\n\t" "global_load_lds_dwordx4 v0, %[sb] offset:-2048\n\t" "v_and_b32_e64 v26, 15, v13\n\t"
    "v_mfma_f32_16x16x32_bf16 v[60:63], v[160:163], v[192:195], v[60:63]\n\t" "ds_read_b64_tr_b16 v[184:185], v10 offset:22528\n\t" "v_lshl_add_u32 v26, v26, 2, %[l0]\n\t"
    "v_mfma_f32_4x4x4_16b_bf16 v[204:207], v[224:225], a[88:89], 0\n\t" "v_mfma_f32_4x4x4_16b_bf16 v[208:211], v[226:227], a[90:91], 0\n\t"
    "v_mfma_f32_4x4x4_16b_bf16 v[212:215], v[224:225], a[92:93], 0\n\t" "v_mfma_f32_4x4x4_16b_bf16 v[216:219], v[226:227], a[94:95], 0\n\t" "v_add_u32_e64 v27, %[w128], v26\n\t"
    "v_mfma_f32_16x16x32_bf16 v[56:59], v[160:163], v[188:191], v[56:59]\n\t" "ds_read_b64_tr_b16 v[186:187], v10 offset:23040\n\t" "v_mul_f32_e64 v239, v239, v20\n\t"
    "v_mfma_f32_16x16x32_bf16 v[64:67], v[164:167], v[188:191], v[64:67]\n\t" "ds_read_b64_tr_b16 v[156:157], v7 offset:24576\n\t" "global_load_lds_dwordx4 v0, %[sb] offset:0\n\t"
    "v_mfma_f32_16x16x32_bf16 v[68:71], v[164:167], v[192:195], v[68:71]\n\t" "ds_read_b64_tr_b16 v[158:159], v7 offset:25088\n\t" "v_cvt_pk_bf16_f32 v196, v204, v205\n\t"
    "v_mfma_f32_16x16x32_bf16 v[76:79], v[168:171], v[192:195], v[76:79]\n\t" "ds_read_b64_tr_b16 v[160:161], v8 offset:24576\n\t" "v_cvt_pk_bf16_f32 v197, v206, v207\n\t"
    "v_mfma_f32_16x16x32_bf16 v[72:75], v[168:171], v[188:191], v[72:75]\n\t" "ds_read_b64_tr_b16 v[162:163], v8 offset:25088\n\t" "s_waitcnt lgkmcnt(4)\n\t"
    "v_mfma_f32_16x16x32_bf16 v[80:83], v[172:175], v[188:191], v[80:83]\n\t" "ds_read_b64_tr_b16 v[164:165], v9 offset:24576\n\t" "v_cvt_pk_bf16_f32 v198, v208, v209\n\t"
    "v_mfma_f32_16x16x32_bf16 v[84:87], v[172:175], v[192:195], v[84:87]\n\t" "ds_read_b64_tr_b16 v[166:167], v9 offset:25088\n\t" "v_cvt_pk_bf16_f32 v199, v210, v211\n\t"
    "v_mfma_f32_16x16x32_bf16 v[144:147], v[172:175], v[228:231], v[144:147]\n\t" "ds_read_b128 v[220:223], v2 offset:3072\n\t" "global_load_lds_dwordx4 v0, %[sb] offset:2048\n\t"
    "v_mfma_f32_16x16x32_bf16 v[92:95], v[176:179], v[192:195], v[92:95]\n\t" "ds_read_b64_tr_b16 v[168:169], v10 offset:24576\n\t" "v_cvt_pk_bf16_f32 v200, v212, v213\n\t"
    "v_mfma_f32_16x16x32_bf16 v[88:91], v[176:179], v[188:191], v[88:91]\n\t" "ds_read_b64_tr_b16 v[170:171], v10 offset:25088\n\t" "v_cvt_pk_bf16_f32 v201, v214, v215\n\t"
    "v_mfma_f32_16x16x32_bf16 v[96:99], v[180:183], v[188:191], v[96:99]\n\t" "ds_read_b64_tr_b16 v[172:173], v7 offset:32768\n\t" "v_add_u32 v0, 0x30000, v0\n\t"
    "v_mfma_f32_16x16x32_bf16 v[100:103], v[180:183], v[192:195], v[100:103]\n\t" "ds_read_b64_tr_b16 v[174:175], v7 offset:33280\n\t" "v_cvt_pk_bf16_f32 v202, v216, v217\n\t"
    "v_mfma_f32_16x16x32_bf16 v[108:111], v[184:187], v[192:195], v[108:111]\n\t" "ds_read_b64_tr_b16 v[176:177], v8 offset:32768\n\t" "v_cvt_pk_bf16_f32 v203, v218, v219\n\t"
    "v_mfma_f32_16x16x32_bf16 v[104:107], v[184:187], v[188:191], v[104:107]\n\t" "ds_read_b64_tr_b16 v[178:179], v8 offset:33280\n\t" "s_waitcnt lgkmcnt(4)\n\t"
    "v_mfma_f32_16x16x32_bf16 v[112:115], v[188:191], v[156:159], v[112:115]\n\t" "ds_read_b64_tr_b16 v[180:181], v9 offset:32768\n\t" "v_add_u32 v11, 0x3000, v11\n\t"
    "v_mfma_f32_16x16x32_bf16 v[116:119], v[192:195], v[156:159], v[116:119]\n\t" "ds_read_b64_tr_b16 v[182:183], v9 offset:33280\n\t" "v_mul_f32_e64 v240, v240, v21\n\t"
    "v_mfma_f32_16x16x32_bf16 v[148:151], v[228:231], v[156:159], v[148:151]\n\t" "ds_read_b128 v[232:235], v1 offset:11264\n\t" "global_load_dwordx4 a[48:51], v11, %[sm] nt\n\t"
    "v_mfma_f32_16x16x32_bf16 v[124:127], v[192:195], v[160:163], v[124:127]\n\t" "ds_read_b64_tr_b16 v[184:185], v10 offset:32768\n\t" "v_add_u32 v11, 0x3000, v11\n\t"
    "v_mfma_f32_16x16x32_bf16 v[120:123], v[188:191], v[160:163], v[120:123]\n\t" "ds_read_b64_tr_b16 v[186:187], v10 offset:33280\n\t" "v_mul_f32_e64 v241, v241, v22\n\t"
    "v_mfma_f32_16x16x32_bf16 v[128:131], v[188:191], v[164:167], v[128:131]\n\t" "ds_read_b64_tr_b16 v[156:157], v7 offset:34816\n\t" "ds_write_b32 v27, v20 offset:17472\n\t"
    "v_mfma_f32_16x16x32_bf16 v[132:135], v[192:195], v[164:167], v[132:135]\n\t" "ds_read_b64_tr_b16 v[158:159], v7 offset:35328\n\t" "global_load_dwordx4 a[52:55], v11, %[sm] nt\n\t"
    "v_mfma_f32_16x16x32_bf16 v[140:143], v[192:195], v[168:171], v[140:143]\n\t" "ds_read_b64_tr_b16 v[160:161], v8 offset:34816\n\t" "ds_write_b32 v27, v21 offset:17536\n\t"
    "v_mfma_f32_16x16x32_bf16 v[136:139], v[188:191], v[168:171], v[136:139]\n\t" "ds_read_b64_tr_b16 v[162:163], v8 offset:35328\n\t" "s_waitcnt lgkmcnt(6)\n\t"
    "v_mfma_f32_16x16x32_bf16 v[48:51], v[172:175], v[196:199], v[48:51]\n\t" "ds_read_b64_tr_b16 v[164:165], v9 offset:34816\n\t" "s_add_u32 m0, %[sl], 59392\n\t"
    "v_mfma_f32_16x16x32_bf16 v[52:55], v[172:175], v[200:203], v[52:55]\n\t" "ds_read_b64_tr_b16 v[166:167], v9 offset:35328\n\t" "ds_write_b32 v26, v22 offset:17408\n\t"
    "v_mfma_f32_16x16x32_bf16 v[152:155], v[232:235], v[172:175], v[152:155]\n\t" "global_load_lds_dwordx4 v0, %[sb] offset:-2048\n\t" "s_mov_b64 s[46:47], 0xffff\n\t"
    "v_mfma_f32_16x16x32_bf16 v[60:63], v[176:179], v[200:203], v[60:63]\n\t" "ds_read_b64_tr_b16 v[168:169], v10 offset:34816\n\t" "s_mov_b32 s48, -1\n\t"
    "v_mfma_f32_4x4x4_16b_bf16 v[204:207], v[220:221], a[96:97], 0\n\t" "v_mfma_f32_4x4x4_16b_bf16 v[208:211], v[222:223], a[98:99], 0\n\t"
    "v_mfma_f32_4x4x4_16b_bf16 v[212:215], v[220:221], a[100:101], 0\n\t" "v_mfma_f32_4x4x4_16b_bf16 v[216:219], v[222:223], a[102:103], 0\n\t" "s_mov_b32 s49, 0\n\t"
    "v_mfma_f32_16x16x32_bf16 v[56:59], v[176:179], v[196:199], v[56:59]\n\t" "ds_read_b64_tr_b16 v[170:171], v10 offset:35328\n\t" "v_and_b32 v23, 0xffff0000, v236\n\t"
    "v_mfma_f32_16x16x32_bf16 v[64:67], v[180:183], v[196:199], v[64:67]\n\t" "ds_read_b64_tr_b16 v[172:173], v7 offset:36864\n\t" "global_load_lds_dwordx4 v0, %[sb] offset:0\n\t"
    "v_mfma_f32_16x16x32_bf16 v[68:71], v[180:183], v[200:203], v[68:71]\n\t" "ds_read_b64_tr_b16 v[174:175], v7 offset:37376\n\t" "v_cvt_pk_bf16_f32 v188, v204, v205\n\t"
    "v_mfma_f32_16x16x32_bf16 v[76:79], v[184:187], v[200:203], v[76:79]\n\t" "ds_read_b64_tr_b16 v[176:177], v8 offset:36864\n\t" "v_cvt_pk_bf16_f32 v189, v206, v207\n\t"
    "v_mfma_f32_16x16x32_bf16 v[72:75], v[184:187], v[196:199], v[72:75]\n\t" "ds_read_b64_tr_b16 v[178:179], v8 offset:37376\n\t" "s_waitcnt lgkmcnt(4)\n\t"
    "v_mfma_f32_16x16x32_bf16 v[80:83], v[156:159], v[196:199], v[80:83]\n\t" "ds_read_b64_tr_b16 v[180:181], v9 offset:36864\n\t" "v_cvt_pk_bf16_f32 v190, v208, v209\n\t"
    "v_mfma_f32_16x16x32_bf16 v[84:87], v[156:159], v[200:203], v[84:87]\n\t" "ds_read_b64_tr_b16 v[182:183], v9 offset:37376\n\t" "v_cvt_pk_bf16_f32 v191, v210, v211\n\t"
    "v_mfma_f32_16x16x32_bf16 v[144:147], v[156:159], v[232:235], v[144:147]\n\t" "ds_read_b128 v[224:227], v2 offset:3328\n\t" "global_load_lds_dwordx4 v0, %[sb] offset:2048\n\t"
    "v_mfma_f32_16x16x32_bf16 v[92:95], v[160:163], v[200:203], v[92:95]\n\t" "ds_read_b64_tr_b16 v[184:185], v10 offset:36864\n\t" "v_cvt_pk_bf16_f32 v192, v212, v213\n\t"
    "v_mfma_f32_16x16x32_bf16 v[88:91], v[160:163], v[196:199], v[88:91]\n\t" "ds_read_b64_tr_b16 v[186:187], v10 offset:37376\n\t" "v_cvt_pk_bf16_f32 v193, v214, v215\n\t"
    "v_mfma_f32_16x16x32_bf16 v[96:99], v[164:167], v[196:199], v[96:99]\n\t" "s_waitcnt vmcnt(19)\n\t" "s_barrier\n\t" "ds_read_b64_tr_b16 v[156:157], v3 offset:49152\n\t"
    "v_mfma_f32_16x16x32_bf16 v[100:103], v[164:167], v[200:203], v[100:103]\n\t" "ds_read_b64_tr_b16 v[158:159], v3 offset:49664\n\t" "v_cvt_pk_bf16_f32 v194, v216, v217\n\t"
    "v_mfma_f32_16x16x32_bf16 v[108:111], v[168:171], v[200:203], v[108:111]\n\t" "ds_read_b64_tr_b16 v[160:161], v4 offset:49152\n\t" "v_cvt_pk_bf16_f32 v195, v218, v219\n\t"
    "v_mfma_f32_16x16x32_bf16 v[104:107], v[168:171], v[196:199], v[104:107]\n\t" "ds_read_b64_tr_b16 v[162:163], v4 offset:49664\n\t" "s_waitcnt lgkmcnt(4)\n\t"
    "v_mfma_f32_16x16x32_bf16 v[112:115], v[196:199], v[172:175], v[112:115]\n\t" "ds_read_b64_tr_b16 v[164:165], v5 offset:49152\n\t" "v_add_u32 v0, 0x30000, v0\n\t"
    "v_mfma_f32_16x16x32_bf16 v[116:119], v[200:203], v[172:175], v[116:119]\n\t" "ds_read_b64_tr_b16 v[166:167], v5 offset:49664\n\t" "v_add_u32 v11, 0x3000, v11\n\t"
    "v_mfma_f32_16x16x32_bf16 v[148:151], v[232:235], v[172:175], v[148:151]\n\t" "ds_read_b128 v[228:231], v1 offset:12288\n\t" "global_load_dwordx4 a[56:59], v11, %[sm] nt\n\t"
    "v_mfma_f32_16x16x32_bf16 v[124:127], v[200:203], v[176:179], v[124:127]\n\t" "ds_read_b64_tr_b16 v[168:169], v6 offset:49152\n\t" "v_add_u32 v11, 0x3000, v11\n\t"
    "v_mfma_f32_16x16x32_bf16 v[120:123], v[196:199], v[176:179], v[120:123]\n\t" "ds_read_b64_tr_b16 v[170:171], v6 offset:49664\n\t" "v_sub_f32 v24, v236, v23\n\t"
    "v_mfma_f32_16x16x32_bf16 v[128:131], v[196:199], v[180:183], v[128:131]\n\t" "ds_read_b64_tr_b16 v[172:173], v3 offset:51200\n\t" "v_cndmask_b32 v23, v24, v23, s[46:47]\n\t"
    "v_mfma_f32_16x16x32_bf16 v[132:135], v[200:203], v[180:183], v[132:135]\n\t" "ds_read_b64_tr_b16 v[174:175], v3 offset:51712\n\t" "global_load_dwordx4 a[60:63], v11, %[sm] nt\n\t"
    "v_mfma_f32_16x16x32_bf16 v[140:143], v[200:203], v[184:187], v[140:143]\n\t" "ds_read_b64_tr_b16 v[176:177], v4 offset:51200\n\t" "v_cvt_pk_bf16_f32 v23, -v23, -v23\n\t"
    "v_mfma_f32_16x16x32_bf16 v[136:139], v[196:199], v[184:187], v[136:139]\n\t" "ds_read_b64_tr_b16 v[178:179], v4 offset:51712\n\t" "s_waitcnt lgkmcnt(4)\n\t"
    "v_mfma_f32_16x16x32_bf16 v[48:51], v[156:159], v[188:191], v[48:51]\n\t" "ds_read_b64_tr_b16 v[180:181], v5 offset:51200\n\t" "s_add_u32 m0, %[sl], 96256\n\t"
    "v_mfma_f32_16x16x32_bf16 v[52:55], v[156:159], v[192:195], v[52:55]\n\t" "ds_read_b64_tr_b16 v[182:183], v5 offset:51712\n\t" "v_cndmask_b32 v246, 0, v23, s[48:49]\n\t"
    "v_mfma_f32_16x16x32_bf16 v[152:155], v[228:231], v[156:159], v[152:155]\n\t" "global_load_lds_dwordx4 v0, %[sb] offset:-2048\n\t" "v_and_b32 v23, 0xffff0000, v239\n\t"
    "v_mfma_f32_16x16x32_bf16 v[60:63], v[160:163], v[192:195], v[60:63]\n\t" "ds_read_b64_tr_b16 v[184:185], v6 offset:51200\n\t" "v_sub_f32 v24, v239, v23\n\t"
    "v_mfma_f32_4x4x4_16b_bf16 v[204:207], v[224:225], a[104:105], 0\n\t" "v_mfma_f32_4x4x4_16b_bf16 v[208:211], v[226:227], a[106:107], 0\n\t"
    "v_mfma_f32_4x4x4_16b_bf16 v[212:215], v[224:225], a[108:109], 0\n\t" "v_mfma_f32_4x4x4_16b_bf16 v[216:219], v[226:227], a[110:111], 0\n\t" "v_cndmask_b32 v23, v24, v23, s[46:47]\n\t"
    "v_mfma_f32_16x16x32_bf16 v[56:59], v[160:163], v[188:191], v[56:59]\n\t" "ds_read_b64_tr_b16 v[186:187], v6 offset:51712\n\t" "v_cvt_pk_bf16_f32 v23, v23, v23\n\t"
    "v_mfma_f32_16x16x32_bf16 v[64:67], v[164:167], v[188:191], v[64:67]\n\t" "ds_read_b64_tr_b16 v[156:157], v3 offset:53248\n\t" "v_cndmask_b32 v247, 0, v23, s[48:49]\n\t"
    "v_mfma_f32_16x16x32_bf16 v[68:71], v[164:167], v[192:195], v[68:71]\n\t" "ds_read_b64_tr_b16 v[158:159], v3 offset:53760\n\t" "v_cvt_pk_bf16_f32 v196, v204, v205\n\t"
    "v_mfma_f32_16x16x32_bf16 v[76:79], v[168:171], v[192:195], v[76:79]\n\t" "ds_read_b64_tr_b16 v[160:161], v4 offset:53248\n\t" "v_cvt_pk_bf16_f32 v197, v206, v207\n\t"
    "v_mfma_f32_16x16x32_bf16 v[72:75], v[168:171], v[188:191], v[72:75]\n\t" "ds_read_b64_tr_b16 v[162:163], v4 offset:53760\n\t" "s_waitcnt lgkmcnt(4)\n\t"
    "v_mfma_f32_16x16x32_bf16 v[80:83], v[172:175], v[188:191], v[80:83]\n\t" "ds_read_b64_tr_b16 v[164:165], v5 offset:53248\n\t" "v_cvt_pk_bf16_f32 v198, v208, v209\n\t"
    "v_mfma_f32_16x16x32_bf16 v[84:87], v[172:175], v[192:195], v[84:87]\n\t" "ds_read_b64_tr_b16 v[166:167], v5 offset:53760\n\t" "v_cvt_pk_bf16_f32 v199, v210, v211\n\t"
    "v_mfma_f32_16x16x32_bf16 v[144:147], v[172:175], v[228:231], v[144:147]\n\t" "ds_read_b128 v[220:223], v2 offset:3584\n\t" "global_load_lds_dwordx4 v0, %[sb] offset:0\n\t"
    "v_mfma_f32_16x16x32_bf16 v[92:95], v[176:179], v[192:195], v[92:95]\n\t" "ds_read_b64_tr_b16 v[168:169], v6 offset:53248\n\t" "v_cvt_pk_bf16_f32 v200, v212, v213\n\t"
    "v_mfma_f32_16x16x32_bf16 v[88:91], v[176:179], v[188:191], v[88:91]\n\t" "ds_read_b64_tr_b16 v[170:171], v6 offset:53760\n\t" "v_cvt_pk_bf16_f32 v201, v214, v215\n\t"
    "v_mfma_f32_16x16x32_bf16 v[96:99], v[180:183], v[188:191], v[96:99]\n\t" "ds_read_b64_tr_b16 v[172:173], v3 offset:61440\n\t" "v_and_b32 v23, 0xffff0000, v237\n\t"
    "v_mfma_f32_16x16x32_bf16 v[100:103], v[180:183], v[192:195], v[100:103]\n\t" "ds_read_b64_tr_b16 v[174:175], v3 offset:61952\n\t" "v_cvt_pk_bf16_f32 v202, v216, v217\n\t"
    "v_mfma_f32_16x16x32_bf16 v[108:111], v[184:187], v[192:195], v[108:111]\n\t" "ds_read_b64_tr_b16 v[176:177], v4 offset:61440\n\t" "v_cvt_pk_bf16_f32 v203, v218, v219\n\t"
    "v_mfma_f32_16x16x32_bf16 v[104:107], v[184:187], v[188:191], v[104:107]\n\t" "ds_read_b64_tr_b16 v[178:179], v4 offset:61952\n\t" "s_waitcnt lgkmcnt(4)\n\t"
    "v_mfma_f32_16x16x32_bf16 v[112:115], v[188:191], v[156:159], v[112:115]\n\t" "ds_read_b64_tr_b16 v[180:181], v5 offset:61440\n\t" "v_sub_f32_e64 v24, v237, v23\n\t"
    "v_mfma_f32_16x16x32_bf16 v[116:119], v[192:195], v[156:159], v[116:119]\n\t" "ds_read_b64_tr_b16 v[182:183], v5 offset:61952\n\t" "v_cndmask_b32 v23, v24, v23, s[46:47]\n\t"
    "v_mfma_f32_16x16x32_bf16 v[148:151], v[228:231], v[156:159], v[148:151]\n\t" "ds_read_b128 v[232:235], v1 offset:13312\n\t" "global_load_lds_dwordx4 v0, %[sb] offset:2048\n\t"
    "v_mfma_f32_16x16x32_bf16 v[124:127], v[192:195], v[160:163], v[124:127]\n\t" "ds_read_b64_tr_b16 v[184:185], v6 offset:61440\n\t" "v_add_u32 v0, 0x30000, v0\n\t"
    "v_mfma_f32_16x16x32_bf16 v[120:123], v[188:191], v[160:163], v[120:123]\n\t" "ds_read_b64_tr_b16 v[186:187], v6 offset:61952\n\t" "v_cvt_pk_bf16_f32 v23, -v23, -v23\n\t"
    "v_mfma_f32_16x16x32_bf16 v[128:131], v[188:191], v[164:167], v[128:131]\n\t" "ds_read_b64_tr_b16 v[156:157], v3 offset:63488\n\t" "v_cndmask_b32 v248, 0, v23, s[48:49]\n\t"
    "v_mfma_f32_16x16x32_bf16 v[132:135], v[192:195], v[164:167], v[132:135]\n\t" "ds_read_b64_tr_b16 v[158:159], v3 offset:64000\n\t" "v_and_b32 v23, 0xffff0000, v240\n\t"
    "v_mfma_f32_16x16x32_bf16 v[140:143], v[192:195], v[168:171], v[140:143]\n\t" "ds_read_b64_tr_b16 v[160:161], v4 offset:63488\n\t" "v_sub_f32_e64 v24, v240, v23\n\t"
    "v_mfma_f32_16x16x32_bf16 v[136:139], v[188:191], v[168:171], v[136:139]\n\t" "ds_read_b64_tr_b16 v[162:163], v4 offset:64000\n\t" "s_waitcnt lgkmcnt(4)\n\t"
    "v_mfma_f32_16x16x32_bf16 v[48:51], v[172:175], v[196:199], v[48:51]\n\t" "ds_read_b64_tr_b16 v[164:165], v5 offset:63488\n\t" "s_add_u32 m0, %[sl], 108544\n\t"
    "v_mfma_f32_16x16x32_bf16 v[52:55], v[172:175], v[200:203], v[52:55]\n\t" "ds_read_b64_tr_b16 v[166:167], v5 offset:64000\n\t" "v_cndmask_b32 v23, v24, v23, s[46:47]\n\t"
    "v_mfma_f32_16x16x32_bf16 v[152:155], v[232:235], v[172:175], v[152:155]\n\t" "global_load_lds_dwordx4 v0, %[sb] offset:-2048\n\t" "v_cvt_pk_bf16_f32 v23, v23, v23\n\t"
    "v_mfma_f32_16x16x32_bf16 v[60:63], v[176:179], v[200:203], v[60:63]\n\t" "ds_read_b64_tr_b16 v[168:169], v6 offset:63488\n\t" "v_cndmask_b32 v249, 0, v23, s[48:49]\n\t"
    "v_mfma_f32_4x4x4_16b_bf16 v[204:207], v[220:221], a[112:113], 0\n\t" "v_mfma_f32_4x4x4_16b_bf16 v[208:211], v[222:223], a[114:115], 0\n\t"
    "v_mfma_f32_4x4x4_16b_bf16 v[212:215], v[220:221], a[116:117], 0\n\t" "v_mfma_f32_4x4x4_16b_bf16 v[216:219], v[222:223], a[118:119], 0\n\t" "v_and_b32 v23, 0xffff0000, v238\n\t"
    "v_mfma_f32_16x16x32_bf16 v[56:59], v[176:179], v[196:199], v[56:59]\n\t" "ds_read_b64_tr_b16 v[170:171], v6 offset:64000\n\t" "v_sub_f32 v24, v238, v23\n\t"
    "v_mfma_f32_16x16x32_bf16 v[64:67], v[180:183], v[196:199], v[64:67]\n\t" "ds_read_b64_tr_b16 v[172:173], v7 offset:0\n\t" "v_cndmask_b32 v23, v24, v23, s[46:47]\n\t"
    "v_mfma_f32_16x16x32_bf16 v[68:71], v[180:183], v[200:203], v[68:71]\n\t" "ds_read_b64_tr_b16 v[174:175], v7 offset:512\n\t" "v_cvt_pk_bf16_f32 v188, v204, v205\n\t"
    "v_mfma_f32_16x16x32_bf16 v[76:79], v[184:187], v[200:203], v[76:79]\n\t" "ds_read_b64_tr_b16 v[176:177], v8 offset:0\n\t" "v_cvt_pk_bf16_f32 v189, v206, v207\n\t"
    "v_mfma_f32_16x16x32_bf16 v[72:75], v[184:187], v[196:199], v[72:75]\n\t" "ds_read_b64_tr_b16 v[178:179], v8 offset:512\n\t" "s_waitcnt lgkmcnt(4)\n\t"
    "v_mfma_f32_16x16x32_bf16 v[80:83], v[156:159], v[196:199], v[80:83]\n\t" "ds_read_b64_tr_b16 v[180:181], v9 offset:0\n\t" "v_cvt_pk_bf16_f32 v190, v208, v209\n\t"
    "v_mfma_f32_16x16x32_bf16 v[84:87], v[156:159], v[200:203], v[84:87]\n\t" "ds_read_b64_tr_b16 v[182:183], v9 offset:512\n\t" "v_cvt_pk_bf16_f32 v191, v210, v211\n\t"
    "v_mfma_f32_16x16x32_bf16 v[144:147], v[156:159], v[232:235], v[144:147]\n\t" "ds_read_b128 v[224:227], v2 offset:3840\n\t" "global_load_lds_dwordx4 v0, %[sb] offset:0\n\t"
    "v_mfma_f32_16x16x32_bf16 v[92:95], v[160:163], v[200:203], v[92:95]\n\t" "ds_read_b64_tr_b16 v[184:185], v10 offset:0\n\t" "v_cvt_pk_bf16_f32 v192, v212, v213\n\t"
    "v_mfma_f32_16x16x32_bf16 v[88:91], v[160:163], v[196:199], v[88:91]\n\t" "ds_read_b64_tr_b16 v[186:187], v10 offset:512\n\t" "v_cvt_pk_bf16_f32 v193, v214, v215\n\t"
    "v_mfma_f32_16x16x32_bf16 v[96:99], v[164:167], v[196:199], v[96:99]\n\t" "s_waitcnt vmcnt(17)\n\t" "s_barrier\n\t" "ds_read_b64_tr_b16 v[156:157], v3 offset:0\n\t"
    "v_mfma_f32_16x16x32_bf16 v[100:103], v[164:167], v[200:203], v[100:103]\n\t" "ds_read_b64_tr_b16 v[158:159], v3 offset:512\n\t" "v_cvt_pk_bf16_f32 v194, v216, v217\n\t"
    "v_mfma_f32_16x16x32_bf16 v[108:111], v[168:171], v[200:203], v[108:111]\n\t" "ds_read_b64_tr_b16 v[160:161], v4 offset:0\n\t" "v_cvt_pk_bf16_f32 v195, v218, v219\n\t"
    "v_mfma_f32_16x16x32_bf16 v[104:107], v[168:171], v[196:199], v[104:107]\n\t" "ds_read_b64_tr_b16 v[162:163], v4 offset:512\n\t" "s_waitcnt lgkmcnt(4)\n\t"
    "v_mfma_f32_16x16x32_bf16 v[112:115], v[196:199], v[172:175], v[112:115]\n\t" "ds_read_b64_tr_b16 v[164:165], v5 offset:0\n\t" "v_cvt_pk_bf16_f32 v23, -v23, -v23\n\t"
    "v_mfma_f32_16x16x32_bf16 v[116:119], v[200:203], v[172:175], v[116:119]\n\t" "ds_read_b64_tr_b16 v[166:167], v5 offset:512\n\t" "v_cndmask_b32 v250, 0, v23, s[48:49]\n\t"
    "v_mfma_f32_16x16x32_bf16 v[148:151], v[232:235], v[172:175], v[148:151]\n\t" "ds_read_b128 v[228:231], v1 offset:14336\n\t" "global_load_lds_dwordx4 v0, %[sb] offset:2048\n\t"
    "v_mfma_f32_16x16x32_bf16 v[124:127], v[200:203], v[176:179], v[124:127]\n\t" "ds_read_b64_tr_b16 v[168:169], v6 offset:0\n\t" "s_cmp_eq_u32 %[rot], 3\n\t" "s_cselect_b32 s42, s43, 0x30000\n\t"
    "v_add_u32 v0, s42, v0\n\t" "v_mfma_f32_16x16x32_bf16 v[120:123], v[196:199], v[176:179], v[120:123]\n\t" "ds_read_b64_tr_b16 v[170:171], v6 offset:512\n\t" "v_add_u32 v16, 0x70000, v16\n\t"
    "v_mfma_f32_16x16x32_bf16 v[128:131], v[196:199], v[180:183], v[128:131]\n\t" "ds_read_b64_tr_b16 v[172:173], v3 offset:2048\n\t" "v_and_b32 v23, 0xffff0000, v241\n\t"
    "v_mfma_f32_16x16x32_bf16 v[132:135], v[200:203], v[180:183], v[132:135]\n\t" "ds_read_b64_tr_b16 v[174:175], v3 offset:2560\n\t" "s_mov_b64 exec, %[tm]\n\t" "global_load_dword a64, v16, %[pf]\n\t"
    "s_mov_b64 exec, -1\n\t" "v_mfma_f32_16x16x32_bf16 v[140:143], v[200:203], v[184:187], v[140:143]\n\t" "ds_read_b64_tr_b16 v[176:177], v4 offset:2048\n\t" "v_sub_f32_e64 v24, v241, v23\n\t"
    "v_mfma_f32_16x16x32_bf16 v[136:139], v[196:199], v[184:187], v[136:139]\n\t" "ds_read_b64_tr_b16 v[178:179], v4 offset:2560\n\t" "s_waitcnt lgkmcnt(4)\n\t"
    "v_mfma_f32_16x16x32_bf16 v[48:51], v[156:159], v[188:191], v[48:51]\n\t" "ds_read_b64_tr_b16 v[180:181], v5 offset:2048\n\t" "s_add_u32 m0, %[sl], 120832\n\t"
    "v_mfma_f32_16x16x32_bf16 v[52:55], v[156:159], v[192:195], v[52:55]\n\t" "ds_read_b64_tr_b16 v[182:183], v5 offset:2560\n\t" "v_cndmask_b32 v23, v24, v23, s[46:47]\n\t"
    "v_mfma_f32_16x16x32_bf16 v[152:155], v[228:231], v[156:159], v[152:155]\n\t" "global_load_lds_dwordx4 v0, %[sb] offset:-2048\n\t" "v_cvt_pk_bf16_f32 v23, v23, v23\n\t"
    "v_mfma_f32_16x16x32_bf16 v[60:63], v[160:163], v[192:195], v[60:63]\n\t" "ds_read_b64_tr_b16 v[184:185], v6 offset:2048\n\t" "v_cndmask_b32 v251, 0, v23, s[48:49]\n\t"
    "v_mfma_f32_4x4x4_16b_bf16 v[204:207], v[224:225], a[120:121], 0\n\t" "v_mfma_f32_4x4x4_16b_bf16 v[208:211], v[226:227], a[122:123], 0\n\t"
    "v_mfma_f32_4x4x4_16b_bf16 v[212:215], v[224:225], a[124:125], 0\n\t" "v_mfma_f32_4x4x4_16b_bf16 v[216:219], v[226:227], a[126:127], 0\n\t" "v_mul_f32 v240, 0x3e8293ee, v20\n\t"
    "v_mfma_f32_16x16x32_bf16 v[56:59], v[160:163], v[188:191], v[56:59]\n\t" "ds_read_b64_tr_b16 v[186:187], v6 offset:2560\n\t" "v_mul_f32 v241, 0x3e8293ee, v21\n\t"
    "v_mfma_f32_16x16x32_bf16 v[64:67], v[164:167], v[188:191], v[64:67]\n\t" "ds_read_b64_tr_b16 v[156:157], v3 offset:4096\n\t" "v_mfma_f32_16x16x32_bf16 v[68:71], v[164:167], v[192:195], v[68:71]\n\t"
    "ds_read_b64_tr_b16 v[158:159], v3 offset:4608\n\t" "v_cvt_pk_bf16_f32 v196, v204, v205\n\t" "v_mfma_f32_16x16x32_bf16 v[76:79], v[168:171], v[192:195], v[76:79]\n\t"
    "ds_read_b64_tr_b16 v[160:161], v4 offset:4096\n\t" "v_cvt_pk_bf16_f32 v197, v206, v207\n\t" "v_mfma_f32_16x16x32_bf16 v[72:75], v[168:171], v[188:191], v[72:75]\n\t"
    "ds_read_b64_tr_b16 v[162:163], v4 offset:4608\n\t" "s_waitcnt lgkmcnt(4)\n\t" "v_mfma_f32_16x16x32_bf16 v[80:83], v[172:175], v[188:191], v[80:83]\n\t"
    "ds_read_b64_tr_b16 v[164:165], v5 offset:4096\n\t" "v_cvt_pk_bf16_f32 v198, v208, v209\n\t" "v_mfma_f32_16x16x32_bf16 v[84:87], v[172:175], v[192:195], v[84:87]\n\t"
    "ds_read_b64_tr_b16 v[166:167], v5 offset:4608\n\t" "v_cvt_pk_bf16_f32 v199, v210, v211\n\t" "v_mfma_f32_16x16x32_bf16 v[144:147], v[172:175], v[228:231], v[144:147]\n\t"
    "ds_read_b128 v[220:223], v2 offset:4096\n\t" "global_load_lds_dwordx4 v0, %[sb] offset:0\n\t" "v_mfma_f32_16x16x32_bf16 v[92:95], v[176:179], v[192:195], v[92:95]\n\t"
    "ds_read_b64_tr_b16 v[168:169], v6 offset:4096\n\t" "v_cvt_pk_bf16_f32 v200, v212, v213\n\t" "v_mfma_f32_16x16x32_bf16 v[88:91], v[176:179], v[188:191], v[88:91]\n\t"
    "ds_read_b64_tr_b16 v[170:171], v6 offset:4608\n\t" "v_cvt_pk_bf16_f32 v201, v214, v215\n\t" "v_mfma_f32_16x16x32_bf16 v[96:99], v[180:183], v[188:191], v[96:99]\n\t"
    "ds_read_b64_tr_b16 v[172:173], v3 offset:12288\n\t" "v_mfma_f32_16x16x32_bf16 v[100:103], v[180:183], v[192:195], v[100:103]\n\t" "ds_read_b64_tr_b16 v[174:175], v3 offset:12800\n\t"
    "v_cvt_pk_bf16_f32 v202, v216, v217\n\t" "v_mfma_f32_16x16x32_bf16 v[108:111], v[184:187], v[192:195], v[108:111]\n\t" "ds_read_b64_tr_b16 v[176:177], v4 offset:12288\n\t"
    "v_cvt_pk_bf16_f32 v203, v218, v219\n\t" "v_mfma_f32_16x16x32_bf16 v[104:107], v[184:187], v[188:191], v[104:107]\n\t" "ds_read_b64_tr_b16 v[178:179], v4 offset:12800\n\t" "s_waitcnt lgkmcnt(4)\n\t"
    "v_mfma_f32_16x16x32_bf16 v[112:115], v[188:191], v[156:159], v[112:115]\n\t" "ds_read_b64_tr_b16 v[180:181], v5 offset:12288\n\t"
    "v_mfma_f32_16x16x32_bf16 v[116:119], v[192:195], v[156:159], v[116:119]\n\t" "ds_read_b64_tr_b16 v[182:183], v5 offset:12800\n\t"
    "v_mfma_f32_16x16x32_bf16 v[148:151], v[228:231], v[156:159], v[148:151]\n\t" "ds_read_b128 v[232:235], v1 offset:15360\n\t" "global_load_lds_dwordx4 v0, %[sb] offset:2048\n\t"
    "v_mfma_f32_16x16x32_bf16 v[124:127], v[192:195], v[160:163], v[124:127]\n\t" "ds_read_b64_tr_b16 v[184:185], v6 offset:12288\n\t" "v_add_u32 v0, 0x30000, v0\n\t"
    "v_mfma_f32_16x16x32_bf16 v[120:123], v[188:191], v[160:163], v[120:123]\n\t" "ds_read_b64_tr_b16 v[186:187], v6 offset:12800\n\t"
    "v_mfma_f32_16x16x32_bf16 v[128:131], v[188:191], v[164:167], v[128:131]\n\t" "ds_read_b64_tr_b16 v[156:157], v3 offset:14336\n\t"
    "v_mfma_f32_16x16x32_bf16 v[132:135], v[192:195], v[164:167], v[132:135]\n\t" "ds_read_b64_tr_b16 v[158:159], v3 offset:14848\n\t"
    "v_mfma_f32_16x16x32_bf16 v[140:143], v[192:195], v[168:171], v[140:143]\n\t" "ds_read_b64_tr_b16 v[160:161], v4 offset:14336\n\t"
    "v_mfma_f32_16x16x32_bf16 v[136:139], v[188:191], v[168:171], v[136:139]\n\t" "ds_read_b64_tr_b16 v[162:163], v4 offset:14848\n\t" "s_waitcnt lgkmcnt(4)\n\t"
    "v_mfma_f32_16x16x32_bf16 v[48:51], v[172:175], v[196:199], v[48:51]\n\t" "ds_read_b64_tr_b16 v[164:165], v5 offset:14336\n\t" "s_add_u32 m0, %[sl], 71680\n\t"
    "v_mfma_f32_16x16x32_bf16 v[52:55], v[172:175], v[200:203], v[52:55]\n\t" "ds_read_b64_tr_b16 v[166:167], v5 offset:14848\n\t"
    "v_mfma_f32_16x16x32_bf16 v[152:155], v[232:235], v[172:175], v[152:155]\n\t" "global_load_lds_dwordx4 v0, %[sb] offset:-2048\n\t"
    "v_mfma_f32_16x16x32_bf16 v[60:63], v[176:179], v[200:203], v[60:63]\n\t" "ds_read_b64_tr_b16 v[168:169], v6 offset:14336\n\t" "v_mfma_f32_4x4x4_16b_bf16 v[204:207], v[220:221], a[128:129], 0\n\t"
    "v_mfma_f32_4x4x4_16b_bf16 v[208:211], v[222:223], a[130:131], 0\n\t" "v_mfma_f32_4x4x4_16b_bf16 v[212:215], v[220:221], a[132:133], 0\n\t"
    "v_mfma_f32_4x4x4_16b_bf16 v[216:219], v[222:223], a[134:135], 0\n\t" "v_mfma_f32_16x16x32_bf16 v[56:59], v[176:179], v[196:199], v[56:59]\n\t" "ds_read_b64_tr_b16 v[170:171], v6 offset:14848\n\t"
    "v_mfma_f32_16x16x32_bf16 v[64:67], v[180:183], v[196:199], v[64:67]\n\t" "ds_read_b64_tr_b16 v[172:173], v3 offset:16384\n\t" "v_mfma_f32_16x16x32_bf16 v[68:71], v[180:183], v[200:203], v[68:71]\n\t"
    "ds_read_b64_tr_b16 v[174:175], v3 offset:16896\n\t" "v_cvt_pk_bf16_f32 v188, v204, v205\n\t" "v_mfma_f32_16x16x32_bf16 v[76:79], v[184:187], v[200:203], v[76:79]\n\t"
    "ds_read_b64_tr_b16 v[176:177], v4 offset:16384\n\t" "v_cvt_pk_bf16_f32 v189, v206, v207\n\t" "v_mfma_f32_16x16x32_bf16 v[72:75], v[184:187], v[196:199], v[72:75]\n\t"
    "ds_read_b64_tr_b16 v[178:179], v4 offset:16896\n\t" "s_waitcnt lgkmcnt(4)\n\t" "v_mfma_f32_16x16x32_bf16 v[80:83], v[156:159], v[196:199], v[80:83]\n\t"
    "ds_read_b64_tr_b16 v[180:181], v5 offset:16384\n\t" "v_cvt_pk_bf16_f32 v190, v208, v209\n\t" "v_mfma_f32_16x16x32_bf16 v[84:87], v[156:159], v[200:203], v[84:87]\n\t"
    "ds_read_b64_tr_b16 v[182:183], v5 offset:16896\n\t" "v_cvt_pk_bf16_f32 v191, v210, v211\n\t" "v_mfma_f32_16x16x32_bf16 v[144:147], v[156:159], v[232:235], v[144:147]\n\t"
    "ds_read_b128 v[224:227], v2 offset:4352\n\t" "global_load_lds_dwordx4 v0, %[sb] offset:0\n\t" "v_mfma_f32_16x16x32_bf16 v[92:95], v[160:163], v[200:203], v[92:95]\n\t"
    "ds_read_b64_tr_b16 v[184:185], v6 offset:16384\n\t" "v_cvt_pk_bf16_f32 v192, v212, v213\n\t" "v_mfma_f32_16x16x32_bf16 v[88:91], v[160:163], v[196:199], v[88:91]\n\t"
    "ds_read_b64_tr_b16 v[186:187], v6 offset:16896\n\t" "v_cvt_pk_bf16_f32 v193, v214, v215\n\t" "v_mfma_f32_16x16x32_bf16 v[96:99], v[164:167], v[196:199], v[96:99]\n\t" "s_waitcnt vmcnt(14)\n\t"
    "s_barrier\n\t" "ds_read_b64_tr_b16 v[156:157], v3 offset:24576\n\t" "v_mfma_f32_16x16x32_bf16 v[100:103], v[164:167], v[200:203], v[100:103]\n\t" "ds_read_b64_tr_b16 v[158:159], v3 offset:25088\n\t"
    "v_cvt_pk_bf16_f32 v194, v216, v217\n\t" "v_mfma_f32_16x16x32_bf16 v[108:111], v[168:171], v[200:203], v[108:111]\n\t" "ds_read_b64_tr_b16 v[160:161], v4 offset:24576\n\t"
    "v_cvt_pk_bf16_f32 v195, v218, v219\n\t" "v_mfma_f32_16x16x32_bf16 v[104:107], v[168:171], v[196:199], v[104:107]\n\t" "ds_read_b64_tr_b16 v[162:163], v4 offset:25088\n\t" "s_waitcnt lgkmcnt(4)\n\t"
    "v_mfma_f32_16x16x32_bf16 v[112:115], v[196:199], v[172:175], v[112:115]\n\t" "ds_read_b64_tr_b16 v[164:165], v5 offset:24576\n\t"
    "v_mfma_f32_16x16x32_bf16 v[116:119], v[200:203], v[172:175], v[116:119]\n\t" "ds_read_b64_tr_b16 v[166:167], v5 offset:25088\n\t"
    "v_mfma_f32_16x16x32_bf16 v[148:151], v[232:235], v[172:175], v[148:151]\n\t" "ds_read_b128 v[228:231], v1 offset:16384\n\t" "global_load_lds_dwordx4 v0, %[sb] offset:2048\n\t"
    "v_mfma_f32_16x16x32_bf16 v[124:127], v[200:203], v[176:179], v[124:127]\n\t" "ds_read_b64_tr_b16 v[168:169], v6 offset:24576\n\t" "v_add_u32 v0, 0x30000, v0\n\t"
    "v_mfma_f32_16x16x32_bf16 v[120:123], v[196:199], v[176:179], v[120:123]\n\t" "ds_read_b64_tr_b16 v[170:171], v6 offset:25088\n\t"
    "v_mfma_f32_16x16x32_bf16 v[128:131], v[196:199], v[180:183], v[128:131]\n\t" "ds_read_b64_tr_b16 v[172:173], v3 offset:26624\n\t"
    "v_mfma_f32_16x16x32_bf16 v[132:135], v[200:203], v[180:183], v[132:135]\n\t" "ds_read_b64_tr_b16 v[174:175], v3 offset:27136\n\t"
    "v_mfma_f32_16x16x32_bf16 v[140:143], v[200:203], v[184:187], v[140:143]\n\t" "ds_read_b64_tr_b16 v[176:177], v4 offset:26624\n\t"
    "v_mfma_f32_16x16x32_bf16 v[136:139], v[196:199], v[184:187], v[136:139]\n\t" "ds_read_b64_tr_b16 v[178:179], v4 offset:27136\n\t" "s_waitcnt lgkmcnt(4)\n\t"
    "v_mfma_f32_16x16x32_bf16 v[48:51], v[156:159], v[188:191], v[48:51]\n\t" "ds_read_b64_tr_b16 v[180:181], v5 offset:26624\n\t" "s_add_u32 m0, %[sl], 83968\n\t"
    "v_mfma_f32_16x16x32_bf16 v[52:55], v[156:159], v[192:195], v[52:55]\n\t" "ds_read_b64_tr_b16 v[182:183], v5 offset:27136\n\t"
    "v_mfma_f32_16x16x32_bf16 v[152:155], v[228:231], v[156:159], v[152:155]\n\t" "global_load_lds_dwordx4 v0, %[sb] offset:-2048\n\t"
    "v_mfma_f32_16x16x32_bf16 v[60:63], v[160:163], v[192:195], v[60:63]\n\t" "ds_read_b64_tr_b16 v[184:185], v6 offset:26624\n\t" "v_mfma_f32_4x4x4_16b_bf16 v[204:207], v[224:225], a[136:137], 0\n\t"
    "v_mfma_f32_4x4x4_16b_bf16 v[208:211], v[226:227], a[138:139], 0\n\t" "v_mfma_f32_4x4x4_16b_bf16 v[212:215], v[224:225], a[140:141], 0\n\t"
    "v_mfma_f32_4x4x4_16b_bf16 v[216:219], v[226:227], a[142:143], 0\n\t" "v_mfma_f32_16x16x32_bf16 v[56:59], v[160:163], v[188:191], v[56:59]\n\t" "ds_read_b64_tr_b16 v[186:187], v6 offset:27136\n\t"
    "v_mfma_f32_16x16x32_bf16 v[64:67], v[164:167], v[188:191], v[64:67]\n\t" "ds_read_b64_tr_b16 v[156:157], v3 offset:28672\n\t" "v_mfma_f32_16x16x32_bf16 v[68:71], v[164:167], v[192:195], v[68:71]\n\t"
    "ds_read_b64_tr_b16 v[158:159], v3 offset:29184\n\t" "v_cvt_pk_bf16_f32 v196, v204, v205\n\t" "v_mfma_f32_16x16x32_bf16 v[76:79], v[168:171], v[192:195], v[76:79]\n\t"
    "ds_read_b64_tr_b16 v[160:161], v4 offset:28672\n\t" "v_cvt_pk_bf16_f32 v197, v206, v207\n\t" "v_mfma_f32_16x16x32_bf16 v[72:75], v[168:171], v[188:191], v[72:75]\n\t"
    "ds_read_b64_tr_b16 v[162:163], v4 offset:29184\n\t" "s_waitcnt lgkmcnt(4)\n\t" "v_mfma_f32_16x16x32_bf16 v[80:83], v[172:175], v[188:191], v[80:83]\n\t"
    "ds_read_b64_tr_b16 v[164:165], v5 offset:28672\n\t" "v_cvt_pk_bf16_f32 v198, v208, v209\n\t" "v_mfma_f32_16x16x32_bf16 v[84:87], v[172:175], v[192:195], v[84:87]\n\t"
    "ds_read_b64_tr_b16 v[166:167], v5 offset:29184\n\t" "v_cvt_pk_bf16_f32 v199, v210, v211\n\t" "v_mfma_f32_16x16x32_bf16 v[144:147], v[172:175], v[228:231], v[144:147]\n\t"
    "ds_read_b128 v[220:223], v2 offset:4608\n\t" "global_load_lds_dwordx4 v0, %[sb] offset:0\n\t" "v_mfma_f32_16x16x32_bf16 v[92:95], v[176:179], v[192:195], v[92:95]\n\t"
    "ds_read_b64_tr_b16 v[168:169], v6 offset:28672\n\t" "v_cvt_pk_bf16_f32 v200, v212, v213\n\t" "v_mfma_f32_16x16x32_bf16 v[88:91], v[176:179], v[188:191], v[88:91]\n\t"
    "ds_read_b64_tr_b16 v[170:171], v6 offset:29184\n\t" "v_cvt_pk_bf16_f32 v201, v214, v215\n\t" "v_mfma_f32_16x16x32_bf16 v[96:99], v[180:183], v[188:191], v[96:99]\n\t"
    "ds_read_b64_tr_b16 v[172:173], v3 offset:36864\n\t" "v_mfma_f32_16x16x32_bf16 v[100:103], v[180:183], v[192:195], v[100:103]\n\t" "ds_read_b64_tr_b16 v[174:175], v3 offset:37376\n\t"
    "v_cvt_pk_bf16_f32 v202, v216, v217\n\t" "v_mfma_f32_16x16x32_bf16 v[108:111], v[184:187], v[192:195], v[108:111]\n\t" "ds_read_b64_tr_b16 v[176:177], v4 offset:36864\n\t"
    "v_cvt_pk_bf16_f32 v203, v218, v219\n\t" "v_mfma_f32_16x16x32_bf16 v[104:107], v[184:187], v[188:191], v[104:107]\n\t" "ds_read_b64_tr_b16 v[178:179], v4 offset:37376\n\t" "s_waitcnt lgkmcnt(4)\n\t"
    "v_mfma_f32_16x16x32_bf16 v[112:115], v[188:191], v[156:159], v[112:115]\n\t" "ds_read_b64_tr_b16 v[180:181], v5 offset:36864\n\t"
    "v_mfma_f32_16x16x32_bf16 v[116:119], v[192:195], v[156:159], v[116:119]\n\t" "ds_read_b64_tr_b16 v[182:183], v5 offset:37376\n\t"
    "v_mfma_f32_16x16x32_bf16 v[148:151], v[228:231], v[156:159], v[148:151]\n\t" "ds_read_b128 v[232:235], v1 offset:17408\n\t" "global_load_lds_dwordx4 v0, %[sb] offset:2048\n\t"
    "v_mfma_f32_16x16x32_bf16 v[124:127], v[192:195], v[160:163], v[124:127]\n\t" "ds_read_b64_tr_b16 v[184:185], v6 offset:36864\n\t" "v_add_u32 v0, 0x30000, v0\n\t"
    "v_mfma_f32_16x16x32_bf16 v[120:123], v[188:191], v[160:163], v[120:123]\n\t" "ds_read_b64_tr_b16 v[186:187], v6 offset:37376\n\t" "v_add_u32 v16, 0x70000, v16\n\t"
    "v_mfma_f32_16x16x32_bf16 v[128:131], v[188:191], v[164:167], v[128:131]\n\t" "ds_read_b64_tr_b16 v[156:157], v3 offset:38912\n\t"
    "v_mfma_f32_16x16x32_bf16 v[132:135], v[192:195], v[164:167], v[132:135]\n\t" "ds_read_b64_tr_b16 v[158:159], v3 offset:39424\n\t" "s_mov_b64 exec, %[tm]\n\t" "global_load_dword a64, v16, %[pf]\n\t"
    "s_mov_b64 exec, -1\n\t" "v_mfma_f32_16x16x32_bf16 v[140:143], v[192:195], v[168:171], v[140:143]\n\t" "ds_read_b64_tr_b16 v[160:161], v4 offset:38912\n\t"
    "v_mfma_f32_16x16x32_bf16 v[136:139], v[188:191], v[168:171], v[136:139]\n\t" "ds_read_b64_tr_b16 v[162:163], v4 offset:39424\n\t" "s_waitcnt lgkmcnt(4)\n\t"
    "v_mfma_f32_16x16x32_bf16 v[48:51], v[172:175], v[196:199], v[48:51]\n\t" "ds_read_b64_tr_b16 v[164:165], v5 offset:38912\n\t" "s_add_u32 m0, %[sl], 22528\n\t"
    "v_mfma_f32_16x16x32_bf16 v[52:55], v[172:175], v[200:203], v[52:55]\n\t" "ds_read_b64_tr_b16 v[166:167], v5 offset:39424\n\t"
    "v_mfma_f32_16x16x32_bf16 v[152:155], v[232:235], v[172:175], v[152:155]\n\t" "global_load_lds_dwordx4 v0, %[sb] offset:-2048\n\t"
    "v_mfma_f32_16x16x32_bf16 v[60:63], v[176:179], v[200:203], v[60:63]\n\t" "ds_read_b64_tr_b16 v[168:169], v6 offset:38912\n\t" "v_mfma_f32_4x4x4_16b_bf16 v[204:207], v[220:221], a[144:145], 0\n\t"
    "v_mfma_f32_4x4x4_16b_bf16 v[208:211], v[222:223], a[146:147], 0\n\t" "v_mfma_f32_4x4x4_16b_bf16 v[212:215], v[220:221], a[148:149], 0\n\t"
    "v_mfma_f32_4x4x4_16b_bf16 v[216:219], v[222:223], a[150:151], 0\n\t" "v_mfma_f32_16x16x32_bf16 v[56:59], v[176:179], v[196:199], v[56:59]\n\t" "ds_read_b64_tr_b16 v[170:171], v6 offset:39424\n\t"
    "v_mfma_f32_16x16x32_bf16 v[64:67], v[180:183], v[196:199], v[64:67]\n\t" "ds_read_b64_tr_b16 v[172:173], v3 offset:40960\n\t" "v_mfma_f32_16x16x32_bf16 v[68:71], v[180:183], v[200:203], v[68:71]\n\t"
    "ds_read_b64_tr_b16 v[174:175], v3 offset:41472\n\t" "v_cvt_pk_bf16_f32 v188, v204, v205\n\t" "v_mfma_f32_16x16x32_bf16 v[76:79], v[184:187], v[200:203], v[76:79]\n\t"
    "ds_read_b64_tr_b16 v[176:177], v4 offset:40960\n\t" "v_cvt_pk_bf16_f32 v189, v206, v207\n\t" "v_mfma_f32_16x16x32_bf16 v[72:75], v[184:187], v[196:199], v[72:75]\n\t"
    "ds_read_b64_tr_b16 v[178:179], v4 offset:41472\n\t" "s_waitcnt lgkmcnt(4)\n\t" "v_mfma_f32_16x16x32_bf16 v[80:83], v[156:159], v[196:199], v[80:83]\n\t"
    "ds_read_b64_tr_b16 v[180:181], v5 offset:40960\n\t" "v_cvt_pk_bf16_f32 v190, v208, v209\n\t" "v_mfma_f32_16x16x32_bf16 v[84:87], v[156:159], v[200:203], v[84:87]\n\t"
    "ds_read_b64_tr_b16 v[182:183], v5 offset:41472\n\t" "v_cvt_pk_bf16_f32 v191, v210, v211\n\t" "v_mfma_f32_16x16x32_bf16 v[144:147], v[156:159], v[232:235], v[144:147]\n\t"
    "ds_read_b128 v[224:227], v2 offset:4864\n\t" "global_load_lds_dwordx4 v0, %[sb] offset:0\n\t" "v_mfma_f32_16x16x32_bf16 v[92:95], v[160:163], v[200:203], v[92:95]\n\t"
    "ds_read_b64_tr_b16 v[184:185], v6 offset:40960\n\t" "v_cvt_pk_bf16_f32 v192, v212, v213\n\t" "v_mfma_f32_16x16x32_bf16 v[88:91], v[160:163], v[196:199], v[88:91]\n\t"
    "ds_read_b64_tr_b16 v[186:187], v6 offset:41472\n\t" "v_cvt_pk_bf16_f32 v193, v214, v215\n\t" "v_mfma_f32_16x16x32_bf16 v[96:99], v[164:167], v[196:199], v[96:99]\n\t" "s_waitcnt vmcnt(13)\n\t"
    "s_barrier\n\t" "ds_read_b64_tr_b16 v[156:157], v7 offset:8192\n\t" "v_mfma_f32_16x16x32_bf16 v[100:103], v[164:167], v[200:203], v[100:103]\n\t" "ds_read_b64_tr_b16 v[158:159], v7 offset:8704\n\t"
    "v_cvt_pk_bf16_f32 v194, v216, v217\n\t" "v_mfma_f32_16x16x32_bf16 v[108:111], v[168:171], v[200:203], v[108:111]\n\t" "ds_read_b64_tr_b16 v[160:161], v8 offset:8192\n\t"
    "v_cvt_pk_bf16_f32 v195, v218, v219\n\t" "v_mfma_f32_16x16x32_bf16 v[104:107], v[168:171], v[196:199], v[104:107]\n\t" "ds_read_b64_tr_b16 v[162:163], v8 offset:8704\n\t" "s_waitcnt lgkmcnt(4)\n\t"
    "v_mfma_f32_16x16x32_bf16 v[112:115], v[196:199], v[172:175], v[112:115]\n\t" "ds_read_b64_tr_b16 v[164:165], v9 offset:8192\n\t"
    "v_mfma_f32_16x16x32_bf16 v[116:119], v[200:203], v[172:175], v[116:119]\n\t" "ds_read_b64_tr_b16 v[166:167], v9 offset:8704\n\t"
    "v_mfma_f32_16x16x32_bf16 v[148:151], v[232:235], v[172:175], v[148:151]\n\t" "ds_read_b128 v[228:231], v1 offset:18432\n\t" "global_load_lds_dwordx4 v0, %[sb] offset:2048\n\t"
    "v_mfma_f32_16x16x32_bf16 v[124:127], v[200:203], v[176:179], v[124:127]\n\t" "ds_read_b64_tr_b16 v[168:169], v10 offset:8192\n\t" "s_cmp_eq_u32 %[rot], 2\n\t" "s_cselect_b32 s42, s43, 0x30000\n\t"
    "v_add_u32 v0, s42, v0\n\t" "v_mfma_f32_16x16x32_bf16 v[120:123], v[196:199], v[176:179], v[120:123]\n\t" "ds_read_b64_tr_b16 v[170:171], v10 offset:8704\n\t" "v_lshlrev_b32_e64 v17, 4, v13\n\t"
    "v_mfma_f32_16x16x32_bf16 v[128:131], v[196:199], v[180:183], v[128:131]\n\t" "ds_read_b64_tr_b16 v[172:173], v7 offset:10240\n\t"
    "v_mfma_f32_16x16x32_bf16 v[132:135], v[200:203], v[180:183], v[132:135]\n\t" "ds_read_b64_tr_b16 v[174:175], v7 offset:10752\n\t" "global_load_dword a64, v17, %[cd]\n\t"
    "v_mfma_f32_16x16x32_bf16 v[140:143], v[200:203], v[184:187], v[140:143]\n\t" "ds_read_b64_tr_b16 v[176:177], v8 offset:10240\n\t"
    "v_mfma_f32_16x16x32_bf16 v[136:139], v[196:199], v[184:187], v[136:139]\n\t" "ds_read_b64_tr_b16 v[178:179], v8 offset:10752\n\t" "s_waitcnt lgkmcnt(4)\n\t"
    "v_mfma_f32_16x16x32_bf16 v[48:51], v[156:159], v[188:191], v[48:51]\n\t" "ds_read_b64_tr_b16 v[180:181], v9 offset:10240\n\t" "s_add_u32 m0, %[sl], 34816\n\t"
    "v_mfma_f32_16x16x32_bf16 v[52:55], v[156:159], v[192:195], v[52:55]\n\t" "ds_read_b64_tr_b16 v[182:183], v9 offset:10752\n\t" "s_mov_b32 s50, 0xaaaaaaaa\n\t"
    "v_mfma_f32_16x16x32_bf16 v[152:155], v[228:231], v[156:159], v[152:155]\n\t" "global_load_lds_dwordx4 v0, %[sb] offset:-2048\n\t" "s_mov_b32 s51, s50\n\t"
    "v_mfma_f32_16x16x32_bf16 v[60:63], v[160:163], v[192:195], v[60:63]\n\t" "ds_read_b64_tr_b16 v[184:185], v10 offset:10240\n\t" "s_mov_b32 s52, 0xfff0ffff\n\t"
    "v_mfma_f32_4x4x4_16b_bf16 v[204:207], v[224:225], a[152:153], 0\n\t" "v_mfma_f32_4x4x4_16b_bf16 v[208:211], v[226:227], a[154:155], 0\n\t"
    "v_mfma_f32_4x4x4_16b_bf16 v[212:215], v[224:225], a[156:157], 0\n\t" "v_mfma_f32_4x4x4_16b_bf16 v[216:219], v[226:227], a[158:159], 0\n\t" "s_mov_b32 s53, 0xf000ff00\n\t"
    "v_mfma_f32_16x16x32_bf16 v[56:59], v[160:163], v[188:191], v[56:59]\n\t" "ds_read_b64_tr_b16 v[186:187], v10 offset:10752\n\t" "s_mov_b32 s54, 0xffc0fffc\n\t"
    "v_mfma_f32_16x16x32_bf16 v[64:67], v[164:167], v[188:191], v[64:67]\n\t" "ds_read_b64_tr_b16 v[156:157], v7 offset:12288\n\t" "s_mov_b32 s55, 0xc000fc00\n\t"
    "v_mfma_f32_16x16x32_bf16 v[68:71], v[164:167], v[192:195], v[68:71]\n\t" "ds_read_b64_tr_b16 v[158:159], v7 offset:12800\n\t" "v_cvt_pk_bf16_f32 v196, v204, v205\n\t"
    "v_mfma_f32_16x16x32_bf16 v[76:79], v[168:171], v[192:195], v[76:79]\n\t" "ds_read_b64_tr_b16 v[160:161], v8 offset:12288\n\t" "v_cvt_pk_bf16_f32 v197, v206, v207\n\t"
    "v_mfma_f32_16x16x32_bf16 v[72:75], v[168:171], v[188:191], v[72:75]\n\t" "ds_read_b64_tr_b16 v[162:163], v8 offset:12800\n\t" "s_waitcnt lgkmcnt(4)\n\t"
    "v_mfma_f32_16x16x32_bf16 v[80:83], v[172:175], v[188:191], v[80:83]\n\t" "ds_read_b64_tr_b16 v[164:165], v9 offset:12288\n\t" "v_cvt_pk_bf16_f32 v198, v208, v209\n\t"
    "v_mfma_f32_16x16x32_bf16 v[84:87], v[172:175], v[192:195], v[84:87]\n\t" "ds_read_b64_tr_b16 v[166:167], v9 offset:12800\n\t" "v_cvt_pk_bf16_f32 v199, v210, v211\n\t"
    "v_mfma_f32_16x16x32_bf16 v[144:147], v[172:175], v[228:231], v[144:147]\n\t" "ds_read_b128 v[220:223], v2 offset:5120\n\t" "global_load_lds_dwordx4 v0, %[sb] offset:0\n\t"
    "v_mfma_f32_16x16x32_bf16 v[92:95], v[176:179], v[192:195], v[92:95]\n\t" "ds_read_b64_tr_b16 v[168:169], v10 offset:12288\n\t" "v_cvt_pk_bf16_f32 v200, v212, v213\n\t"
    "v_mfma_f32_16x16x32_bf16 v[88:91], v[176:179], v[188:191], v[88:91]\n\t" "ds_read_b64_tr_b16 v[170:171], v10 offset:12800\n\t" "v_cvt_pk_bf16_f32 v201, v214, v215\n\t"
    "v_mfma_f32_16x16x32_bf16 v[96:99], v[180:183], v[188:191], v[96:99]\n\t" "ds_read_b64_tr_b16 v[172:173], v7 offset:20480\n\t" "s_cmp_eq_u32 %[rw], 0\n\t" "s_cselect_b64 s[58:59], -1, 0\n\t"
    "v_mfma_f32_16x16x32_bf16 v[100:103], v[180:183], v[192:195], v[100:103]\n\t" "ds_read_b64_tr_b16 v[174:175], v7 offset:20992\n\t" "v_cvt_pk_bf16_f32 v202, v216, v217\n\t"
    "v_mfma_f32_16x16x32_bf16 v[108:111], v[184:187], v[192:195], v[108:111]\n\t" "ds_read_b64_tr_b16 v[176:177], v8 offset:20480\n\t" "v_cvt_pk_bf16_f32 v203, v218, v219\n\t"
    "v_mfma_f32_16x16x32_bf16 v[104:107], v[184:187], v[188:191], v[104:107]\n\t" "ds_read_b64_tr_b16 v[178:179], v8 offset:20992\n\t" "s_waitcnt lgkmcnt(4)\n\t"
    "v_mfma_f32_16x16x32_bf16 v[112:115], v[188:191], v[156:159], v[112:115]\n\t" "ds_read_b64_tr_b16 v[180:181], v9 offset:20480\n\t" "s_andn2_b64 s[56:57], s[58:59], s[52:53]\n\t"
    "v_mfma_f32_16x16x32_bf16 v[116:119], v[192:195], v[156:159], v[116:119]\n\t" "ds_read_b64_tr_b16 v[182:183], v9 offset:20992\n\t" "s_andn2_b64 s[58:59], s[58:59], s[54:55]\n\t"
    "v_mfma_f32_16x16x32_bf16 v[148:151], v[228:231], v[156:159], v[148:151]\n\t" "ds_read_b128 v[232:235], v1 offset:19456\n\t" "global_load_lds_dwordx4 v0, %[sb] offset:2048\n\t"
    "v_mfma_f32_16x16x32_bf16 v[124:127], v[192:195], v[160:163], v[124:127]\n\t" "ds_read_b64_tr_b16 v[184:185], v10 offset:20480\n\t" "v_add_u32 v0, 0x30000, v0\n\t"
    "v_mfma_f32_16x16x32_bf16 v[120:123], v[188:191], v[160:163], v[120:123]\n\t" "ds_read_b64_tr_b16 v[186:187], v10 offset:20992\n\t" "s_mov_b32 s48, 0x00030003\n\t"
    "v_mfma_f32_16x16x32_bf16 v[128:131], v[188:191], v[164:167], v[128:131]\n\t" "ds_read_b64_tr_b16 v[156:157], v7 offset:22528\n\t" "s_mov_b32 s49, s48\n\t"
    "v_mfma_f32_16x16x32_bf16 v[132:135], v[192:195], v[164:167], v[132:135]\n\t" "ds_read_b64_tr_b16 v[158:159], v7 offset:23040\n\t" "v_and_b32 v46, 63, v13\n\t"
    "v_mfma_f32_16x16x32_bf16 v[140:143], v[192:195], v[168:171], v[140:143]\n\t" "ds_read_b64_tr_b16 v[160:161], v8 offset:22528\n\t" "v_and_b32_e64 v47, 15, v13\n\t"
    "v_mfma_f32_16x16x32_bf16 v[136:139], v[188:191], v[168:171], v[136:139]\n\t" "ds_read_b64_tr_b16 v[162:163], v8 offset:23040\n\t" "s_waitcnt lgkmcnt(4)\n\t"
    "v_mfma_f32_16x16x32_bf16 v[48:51], v[172:175], v[196:199], v[48:51]\n\t" "ds_read_b64_tr_b16 v[164:165], v9 offset:22528\n\t" "s_add_u32 m0, %[sl], 47104\n\t"
    "v_mfma_f32_16x16x32_bf16 v[52:55], v[172:175], v[200:203], v[52:55]\n\t" "ds_read_b64_tr_b16 v[166:167], v9 offset:23040\n\t" "v_and_b32 v45, 48, v13\n\t"
    "v_mfma_f32_16x16x32_bf16 v[152:155], v[232:235], v[172:175], v[152:155]\n\t" "global_load_lds_dwordx4 v0, %[sb] offset:-2048\n\t" "v_and_b32_e64 v44, 1, v13\n\t"
    "v_mfma_f32_16x16x32_bf16 v[60:63], v[176:179], v[200:203], v[60:63]\n\t" "ds_read_b64_tr_b16 v[168:169], v10 offset:22528\n\t" "v_lshl_add_u32 v44, v44, 2, v45\n\t"
    "v_mfma_f32_4x4x4_16b_bf16 v[204:207], v[220:221], a[160:161], 0\n\t" "v_mfma_f32_4x4x4_16b_bf16 v[208:211], v[222:223], a[162:163], 0\n\t"
    "v_mfma_f32_4x4x4_16b_bf16 v[212:215], v[220:221], a[164:165], 0\n\t" "v_mfma_f32_4x4x4_16b_bf16 v[216:219], v[222:223], a[166:167], 0\n\t" "v_add_u32_e64 v44, %[w128], v44\n\t"
    "v_mfma_f32_16x16x32_bf16 v[56:59], v[176:179], v[196:199], v[56:59]\n\t" "ds_read_b64_tr_b16 v[170:171], v10 offset:23040\n\t" "v_add_u32_e64 v44, %[l0], v44\n\t"
    "v_mfma_f32_16x16x32_bf16 v[64:67], v[180:183], v[196:199], v[64:67]\n\t" "ds_read_b64_tr_b16 v[172:173], v7 offset:24576\n\t" "v_add_u32_e64 v43, 64, v44\n\t"
    "v_mfma_f32_16x16x32_bf16 v[68:71], v[180:183], v[200:203], v[68:71]\n\t" "ds_read_b64_tr_b16 v[174:175], v7 offset:25088\n\t" "v_cvt_pk_bf16_f32 v188, v204, v205\n\t"
    "v_mfma_f32_16x16x32_bf16 v[76:79], v[184:187], v[200:203], v[76:79]\n\t" "ds_read_b64_tr_b16 v[176:177], v8 offset:24576\n\t" "v_cvt_pk_bf16_f32 v189, v206, v207\n\t"
    "v_mfma_f32_16x16x32_bf16 v[72:75], v[184:187], v[196:199], v[72:75]\n\t" "ds_read_b64_tr_b16 v[178:179], v8 offset:25088\n\t" "s_waitcnt lgkmcnt(4)\n\t"
    "v_mfma_f32_16x16x32_bf16 v[80:83], v[156:159], v[196:199], v[80:83]\n\t" "ds_read_b64_tr_b16 v[180:181], v9 offset:24576\n\t" "v_cvt_pk_bf16_f32 v190, v208, v209\n\t"
    "v_mfma_f32_16x16x32_bf16 v[84:87], v[156:159], v[200:203], v[84:87]\n\t" "ds_read_b64_tr_b16 v[182:183], v9 offset:25088\n\t" "v_cvt_pk_bf16_f32 v191, v210, v211\n\t"
    "v_mfma_f32_16x16x32_bf16 v[144:147], v[156:159], v[232:235], v[144:147]\n\t" "ds_read_b128 v[224:227], v2 offset:5376\n\t" "global_load_lds_dwordx4 v0, %[sb] offset:0\n\t"
    "v_mfma_f32_16x16x32_bf16 v[92:95], v[160:163], v[200:203], v[92:95]\n\t" "ds_read_b64_tr_b16 v[184:185], v10 offset:24576\n\t" "v_cvt_pk_bf16_f32 v192, v212, v213\n\t"
    "v_mfma_f32_16x16x32_bf16 v[88:91], v[160:163], v[196:199], v[88:91]\n\t" "ds_read_b64_tr_b16 v[186:187], v10 offset:25088\n\t" "v_cvt_pk_bf16_f32 v193, v214, v215\n\t"
    "v_mfma_f32_16x16x32_bf16 v[96:99], v[164:167], v[196:199], v[96:99]\n\t" "s_waitcnt vmcnt(13)\n\t" "s_barrier\n\t" "ds_read_b64_tr_b16 v[156:157], v7 offset:32768\n\t"
    "v_mfma_f32_16x16x32_bf16 v[100:103], v[164:167], v[200:203], v[100:103]\n\t" "ds_read_b64_tr_b16 v[158:159], v7 offset:33280\n\t" "v_cvt_pk_bf16_f32 v194, v216, v217\n\t"
    "v_mfma_f32_16x16x32_bf16 v[108:111], v[168:171], v[200:203], v[108:111]\n\t" "ds_read_b64_tr_b16 v[160:161], v8 offset:32768\n\t" "v_cvt_pk_bf16_f32 v195, v218, v219\n\t"
    "v_mfma_f32_16x16x32_bf16 v[104:107], v[168:171], v[196:199], v[104:107]\n\t" "ds_read_b64_tr_b16 v[162:163], v8 offset:33280\n\t" "s_waitcnt lgkmcnt(4)\n\t"
    "v_mfma_f32_16x16x32_bf16 v[112:115], v[196:199], v[172:175], v[112:115]\n\t" "ds_read_b64_tr_b16 v[164:165], v9 offset:32768\n\t" "v_cndmask_b32 v42, v44, v43, s[52:53]\n\t"
    "v_mfma_f32_16x16x32_bf16 v[116:119], v[200:203], v[172:175], v[116:119]\n\t" "ds_read_b64_tr_b16 v[166:167], v9 offset:33280\n\t" "v_cndmask_b32 v43, v44, v43, s[54:55]\n\t"
    "v_mfma_f32_16x16x32_bf16 v[148:151], v[232:235], v[172:175], v[148:151]\n\t" "ds_read_b128 v[228:231], v1 offset:20480\n\t" "global_load_lds_dwordx4 v0, %[sb] offset:2048\n\t"
    "v_mfma_f32_16x16x32_bf16 v[124:127], v[200:203], v[176:179], v[124:127]\n\t" "ds_read_b64_tr_b16 v[168:169], v10 offset:32768\n\t" "v_add_u32 v0, 0x30000, v0\n\t"
    "v_mfma_f32_16x16x32_bf16 v[120:123], v[196:199], v[176:179], v[120:123]\n\t" "ds_read_b64_tr_b16 v[170:171], v10 offset:33280\n\t" "v_add_u32 v16, 0x70000, v16\n\t"
    "v_mfma_f32_16x16x32_bf16 v[128:131], v[196:199], v[180:183], v[128:131]\n\t" "ds_read_b64_tr_b16 v[172:173], v7 offset:34816\n\t" "ds_read_b32 v24, v42 offset:17408\n\t"
    "v_mfma_f32_16x16x32_bf16 v[132:135], v[200:203], v[180:183], v[132:135]\n\t" "ds_read_b64_tr_b16 v[174:175], v7 offset:35328\n\t" "s_mov_b64 exec, %[tm]\n\t" "global_load_dword a64, v16, %[pf]\n\t"
    "s_mov_b64 exec, -1\n\t" "v_mfma_f32_16x16x32_bf16 v[140:143], v[200:203], v[184:187], v[140:143]\n\t" "ds_read_b64_tr_b16 v[176:177], v8 offset:34816\n\t" "ds_read_b32 v25, v43 offset:17416\n\t"
    "v_mfma_f32_16x16x32_bf16 v[136:139], v[196:199], v[184:187], v[136:139]\n\t" "ds_read_b64_tr_b16 v[178:179], v8 offset:35328\n\t" "s_waitcnt lgkmcnt(6)\n\t"
    "v_mfma_f32_16x16x32_bf16 v[48:51], v[156:159], v[188:191], v[48:51]\n\t" "ds_read_b64_tr_b16 v[180:181], v9 offset:34816\n\t" "s_add_u32 m0, %[sl], 59392\n\t"
    "v_mfma_f32_16x16x32_bf16 v[52:55], v[156:159], v[192:195], v[52:55]\n\t" "ds_read_b64_tr_b16 v[182:183], v9 offset:35328\n\t" "ds_read_b32 v26, v42 offset:17472\n\t"
    "v_mfma_f32_16x16x32_bf16 v[152:155], v[228:231], v[156:159], v[152:155]\n\t" "global_load_lds_dwordx4 v0, %[sb] offset:-2048\n\t" "ds_read_b32 v27, v43 offset:17480\n\t"
    "v_mfma_f32_16x16x32_bf16 v[60:63], v[160:163], v[192:195], v[60:63]\n\t" "ds_read_b64_tr_b16 v[184:185], v10 offset:34816\n\t" "v_mov_b32 v40, 0x0c0c0100\n\t"
    "v_mfma_f32_4x4x4_16b_bf16 v[204:207], v[224:225], a[168:169], 0\n\t" "v_mfma_f32_4x4x4_16b_bf16 v[208:211], v[226:227], a[170:171], 0\n\t"
    "v_mfma_f32_4x4x4_16b_bf16 v[212:215], v[224:225], a[172:173], 0\n\t" "v_mfma_f32_4x4x4_16b_bf16 v[216:219], v[226:227], a[174:175], 0\n\t" "v_mov_b32 v41, 0x01000c0c\n\t"
    "v_mfma_f32_16x16x32_bf16 v[56:59], v[160:163], v[188:191], v[56:59]\n\t" "ds_read_b64_tr_b16 v[186:187], v10 offset:35328\n\t" "v_cndmask_b32 v40, v40, v41, s[50:51]\n\t"
    "v_mfma_f32_16x16x32_bf16 v[64:67], v[164:167], v[188:191], v[64:67]\n\t" "ds_read_b64_tr_b16 v[156:157], v7 offset:36864\n\t" "v_mov_b32 v39, 0x0c0c0302\n\t"
    "v_mfma_f32_16x16x32_bf16 v[68:71], v[164:167], v[192:195], v[68:71]\n\t" "ds_read_b64_tr_b16 v[158:159], v7 offset:37376\n\t" "v_cvt_pk_bf16_f32 v196, v204, v205\n\t"
    "v_mfma_f32_16x16x32_bf16 v[76:79], v[168:171], v[192:195], v[76:79]\n\t" "ds_read_b64_tr_b16 v[160:161], v8 offset:36864\n\t" "v_cvt_pk_bf16_f32 v197, v206, v207\n\t"
    "v_mfma_f32_16x16x32_bf16 v[72:75], v[168:171], v[188:191], v[72:75]\n\t" "s_waitcnt lgkmcnt(11)\n\t" "ds_read_b64_tr_b16 v[162:163], v8 offset:37376\n\t" "s_waitcnt lgkmcnt(4)\n\t"
    "v_mfma_f32_16x16x32_bf16 v[80:83], v[172:175], v[188:191], v[80:83]\n\t" "ds_read_b64_tr_b16 v[164:165], v9 offset:36864\n\t" "v_cvt_pk_bf16_f32 v198, v208, v209\n\t"
    "v_mfma_f32_16x16x32_bf16 v[84:87], v[172:175], v[192:195], v[84:87]\n\t" "ds_read_b64_tr_b16 v[166:167], v9 offset:37376\n\t" "v_cvt_pk_bf16_f32 v199, v210, v211\n\t"
    "v_mfma_f32_16x16x32_bf16 v[144:147], v[172:175], v[228:231], v[144:147]\n\t" "ds_read_b128 v[220:223], v2 offset:5632\n\t" "global_load_lds_dwordx4 v0, %[sb] offset:0\n\t"
    "v_mfma_f32_16x16x32_bf16 v[92:95], v[176:179], v[192:195], v[92:95]\n\t" "ds_read_b64_tr_b16 v[168:169], v10 offset:36864\n\t" "v_cvt_pk_bf16_f32 v200, v212, v213\n\t"
    "v_mfma_f32_16x16x32_bf16 v[88:91], v[176:179], v[188:191], v[88:91]\n\t" "ds_read_b64_tr_b16 v[170:171], v10 offset:37376\n\t" "v_cvt_pk_bf16_f32 v201, v214, v215\n\t"
    "v_mfma_f32_16x16x32_bf16 v[96:99], v[180:183], v[188:191], v[96:99]\n\t" "ds_read_b64_tr_b16 v[172:173], v3 offset:49152\n\t" "v_mov_b32 v41, 0x03020c0c\n\t"
    "v_mfma_f32_16x16x32_bf16 v[100:103], v[180:183], v[192:195], v[100:103]\n\t" "ds_read_b64_tr_b16 v[174:175], v3 offset:49664\n\t" "v_cvt_pk_bf16_f32 v202, v216, v217\n\t"
    "v_mfma_f32_16x16x32_bf16 v[108:111], v[184:187], v[192:195], v[108:111]\n\t" "ds_read_b64_tr_b16 v[176:177], v4 offset:49152\n\t" "v_cvt_pk_bf16_f32 v203, v218, v219\n\t"
    "v_mfma_f32_16x16x32_bf16 v[104:107], v[184:187], v[188:191], v[104:107]\n\t" "ds_read_b64_tr_b16 v[178:179], v4 offset:49664\n\t" "s_waitcnt lgkmcnt(4)\n\t"
    "v_mfma_f32_16x16x32_bf16 v[112:115], v[188:191], v[156:159], v[112:115]\n\t" "ds_read_b64_tr_b16 v[180:181], v5 offset:49152\n\t" "v_cndmask_b32 v41, v39, v41, s[50:51]\n\t"
    "v_mfma_f32_16x16x32_bf16 v[116:119], v[192:195], v[156:159], v[116:119]\n\t" "ds_read_b64_tr_b16 v[182:183], v5 offset:49664\n\t" "v_mov_b32 v39, 0x0c0c0c0c\n\t"
    "v_mfma_f32_16x16x32_bf16 v[148:151], v[228:231], v[156:159], v[148:151]\n\t" "ds_read_b128 v[232:235], v1 offset:21504\n\t" "global_load_lds_dwordx4 v0, %[sb] offset:2048\n\t"
    "v_mfma_f32_16x16x32_bf16 v[124:127], v[192:195], v[160:163], v[124:127]\n\t" "ds_read_b64_tr_b16 v[184:185], v6 offset:49152\n\t" "v_add_u32 v0, 0x30000, v0\n\t"
    "v_mfma_f32_16x16x32_bf16 v[120:123], v[188:191], v[160:163], v[120:123]\n\t" "ds_read_b64_tr_b16 v[186:187], v6 offset:49664\n\t" "s_add_u32 m0, %[sl1], 0\n\t"
    "v_mfma_f32_16x16x32_bf16 v[128:131], v[188:191], v[164:167], v[128:131]\n\t" "ds_read_b64_tr_b16 v[156:157], v3 offset:51200\n\t" "v_cndmask_b32 v20, v40, v39, s[52:53]\n\t"
    "v_mfma_f32_16x16x32_bf16 v[132:135], v[192:195], v[164:167], v[132:135]\n\t" "ds_read_b64_tr_b16 v[158:159], v3 offset:51712\n\t" "global_load_lds_dwordx4 v12, %[so]\n\t"
    "v_mfma_f32_16x16x32_bf16 v[140:143], v[192:195], v[168:171], v[140:143]\n\t" "ds_read_b64_tr_b16 v[160:161], v4 offset:51200\n\t" "v_cndmask_b32 v21, v41, v39, s[54:55]\n\t"
    "v_mfma_f32_16x16x32_bf16 v[136:139], v[188:191], v[168:171], v[136:139]\n\t" "ds_read_b64_tr_b16 v[162:163], v4 offset:51712\n\t" "s_waitcnt lgkmcnt(4)\n\t"
    "v_mfma_f32_16x16x32_bf16 v[48:51], v[172:175], v[196:199], v[48:51]\n\t" "ds_read_b64_tr_b16 v[164:165], v5 offset:51200\n\t" "s_add_u32 m0, %[sl], 96256\n\t"
    "v_mfma_f32_16x16x32_bf16 v[52:55], v[172:175], v[200:203], v[52:55]\n\t" "ds_read_b64_tr_b16 v[166:167], v5 offset:51712\n\t" "v_cndmask_b32 v22, v39, v40, s[52:53]\n\t"
    "v_mfma_f32_16x16x32_bf16 v[152:155], v[232:235], v[172:175], v[152:155]\n\t" "global_load_lds_dwordx4 v0, %[sb] offset:-2048\n\t" "v_cndmask_b32 v23, v39, v41, s[54:55]\n\t"
    "v_mfma_f32_16x16x32_bf16 v[60:63], v[176:179], v[200:203], v[60:63]\n\t" "ds_read_b64_tr_b16 v[168:169], v6 offset:51200\n\t" "s_mov_b32 s60, 0xcccccccc\n\t"
    "v_mfma_f32_4x4x4_16b_bf16 v[204:207], v[220:221], a[176:177], 0\n\t" "v_mfma_f32_4x4x4_16b_bf16 v[208:211], v[222:223], a[178:179], 0\n\t"
    "v_mfma_f32_4x4x4_16b_bf16 v[212:215], v[220:221], a[180:181], 0\n\t" "v_mfma_f32_4x4x4_16b_bf16 v[216:219], v[222:223], a[182:183], 0\n\t" "s_mov_b32 s61, s60\n\t"
    "v_mfma_f32_16x16x32_bf16 v[56:59], v[176:179], v[196:199], v[56:59]\n\t" "ds_read_b64_tr_b16 v[170:171], v6 offset:51712\n\t" "v_cndmask_b32 v14, v40, v39, s[60:61]\n\t"
    "v_mfma_f32_16x16x32_bf16 v[64:67], v[180:183], v[196:199], v[64:67]\n\t" "ds_read_b64_tr_b16 v[172:173], v3 offset:53248\n\t" "v_cndmask_b32 v15, v41, v39, s[60:61]\n\t"
    "v_mfma_f32_16x16x32_bf16 v[68:71], v[180:183], v[200:203], v[68:71]\n\t" "ds_read_b64_tr_b16 v[174:175], v3 offset:53760\n\t" "v_cvt_pk_bf16_f32 v188, v204, v205\n\t"
    "v_mfma_f32_16x16x32_bf16 v[76:79], v[184:187], v[200:203], v[76:79]\n\t" "ds_read_b64_tr_b16 v[176:177], v4 offset:53248\n\t" "v_cvt_pk_bf16_f32 v189, v206, v207\n\t"
    "v_mfma_f32_16x16x32_bf16 v[72:75], v[184:187], v[196:199], v[72:75]\n\t" "ds_read_b64_tr_b16 v[178:179], v4 offset:53760\n\t" "s_waitcnt lgkmcnt(4)\n\t"
    "v_mfma_f32_16x16x32_bf16 v[80:83], v[156:159], v[196:199], v[80:83]\n\t" "ds_read_b64_tr_b16 v[180:181], v5 offset:53248\n\t" "v_cvt_pk_bf16_f32 v190, v208, v209\n\t"
    "v_mfma_f32_16x16x32_bf16 v[84:87], v[156:159], v[200:203], v[84:87]\n\t" "ds_read_b64_tr_b16 v[182:183], v5 offset:53760\n\t" "v_cvt_pk_bf16_f32 v191, v210, v211\n\t"
    "v_mfma_f32_16x16x32_bf16 v[144:147], v[156:159], v[232:235], v[144:147]\n\t" "ds_read_b128 v[224:227], v2 offset:5888\n\t" "global_load_lds_dwordx4 v0, %[sb] offset:0\n\t"
    "v_mfma_f32_16x16x32_bf16 v[92:95], v[160:163], v[200:203], v[92:95]\n\t" "ds_read_b64_tr_b16 v[184:185], v6 offset:53248\n\t" "v_cvt_pk_bf16_f32 v192, v212, v213\n\t"
    "v_mfma_f32_16x16x32_bf16 v[88:91], v[160:163], v[196:199], v[88:91]\n\t" "ds_read_b64_tr_b16 v[186:187], v6 offset:53760\n\t" "v_cvt_pk_bf16_f32 v193, v214, v215\n\t"
    "v_mfma_f32_16x16x32_bf16 v[96:99], v[164:167], v[196:199], v[96:99]\n\t" "s_waitcnt vmcnt(14)\n\t" "s_barrier\n\t" "ds_read_b64_tr_b16 v[156:157], v3 offset:61440\n\t"
    "v_mfma_f32_16x16x32_bf16 v[100:103], v[164:167], v[200:203], v[100:103]\n\t" "ds_read_b64_tr_b16 v[158:159], v3 offset:61952\n\t" "v_cvt_pk_bf16_f32 v194, v216, v217\n\t"
    "v_mfma_f32_16x16x32_bf16 v[108:111], v[168:171], v[200:203], v[108:111]\n\t" "ds_read_b64_tr_b16 v[160:161], v4 offset:61440\n\t" "v_cvt_pk_bf16_f32 v195, v218, v219\n\t"
    "v_mfma_f32_16x16x32_bf16 v[104:107], v[168:171], v[196:199], v[104:107]\n\t" "ds_read_b64_tr_b16 v[162:163], v4 offset:61952\n\t" "s_waitcnt lgkmcnt(4)\n\t"
    "v_mfma_f32_16x16x32_bf16 v[112:115], v[196:199], v[172:175], v[112:115]\n\t" "ds_read_b64_tr_b16 v[164:165], v5 offset:61440\n\t" "v_cndmask_b32 v17, v39, v40, s[60:61]\n\t"
    "v_mfma_f32_16x16x32_bf16 v[116:119], v[200:203], v[172:175], v[116:119]\n\t" "ds_read_b64_tr_b16 v[166:167], v5 offset:61952\n\t" "v_cndmask_b32 v19, v39, v41, s[60:61]\n\t"
    "v_mfma_f32_16x16x32_bf16 v[148:151], v[232:235], v[172:175], v[148:151]\n\t" "ds_read_b128 v[228:231], v1 offset:22528\n\t" "global_load_lds_dwordx4 v0, %[sb] offset:2048\n\t"
    "v_mfma_f32_16x16x32_bf16 v[124:127], v[200:203], v[176:179], v[124:127]\n\t" "ds_read_b64_tr_b16 v[168:169], v6 offset:61440\n\t" "s_cmp_eq_u32 %[rot], 1\n\t" "s_cselect_b32 s42, s43, 0x30000\n\t"
    "v_add_u32_e64 v0, s42, v0\n\t" "v_mfma_f32_16x16x32_bf16 v[120:123], v[196:199], v[176:179], v[120:123]\n\t" "ds_read_b64_tr_b16 v[170:171], v6 offset:61952\n\t" "v_add_u32 v12, 0x20000, v12\n\t"
    "v_mfma_f32_16x16x32_bf16 v[128:131], v[196:199], v[180:183], v[128:131]\n\t" "ds_read_b64_tr_b16 v[172:173], v3 offset:63488\n\t" "s_add_u32 m0, %[sl1], 8192\n\t"
    "v_mfma_f32_16x16x32_bf16 v[132:135], v[200:203], v[180:183], v[132:135]\n\t" "ds_read_b64_tr_b16 v[174:175], v3 offset:64000\n\t" "global_load_lds_dwordx4 v12, %[so]\n\t"
    "v_mfma_f32_16x16x32_bf16 v[140:143], v[200:203], v[184:187], v[140:143]\n\t" "ds_read_b64_tr_b16 v[176:177], v4 offset:63488\n\t" "v_and_b32_e64 v11, 7, v13\n\t"
    "v_mfma_f32_16x16x32_bf16 v[136:139], v[196:199], v[184:187], v[136:139]\n\t" "ds_read_b64_tr_b16 v[178:179], v4 offset:64000\n\t" "s_waitcnt lgkmcnt(4)\n\t"
    "v_mfma_f32_16x16x32_bf16 v[48:51], v[156:159], v[188:191], v[48:51]\n\t" "ds_read_b64_tr_b16 v[180:181], v5 offset:63488\n\t" "s_add_u32 m0, %[sl], 108544\n\t"
    "v_mfma_f32_16x16x32_bf16 v[52:55], v[156:159], v[192:195], v[52:55]\n\t" "ds_read_b64_tr_b16 v[182:183], v5 offset:64000\n\t" "v_lshrrev_b32 v18, 1, v45\n\t"
    "v_mfma_f32_16x16x32_bf16 v[152:155], v[228:231], v[156:159], v[152:155]\n\t" "global_load_lds_dwordx4 v0, %[sb] offset:-2048\n\t" "v_lshl_add_u32 v11, v11, 5, v18\n\t"
    "v_mfma_f32_16x16x32_bf16 v[60:63], v[160:163], v[192:195], v[60:63]\n\t" "ds_read_b64_tr_b16 v[184:185], v6 offset:63488\n\t" "v_add_u32_e64 v11, %[lr], v11\n\t"
    "v_mfma_f32_4x4x4_16b_bf16 v[204:207], v[224:225], a[184:185], 0\n\t" "v_mfma_f32_4x4x4_16b_bf16 v[208:211], v[226:227], a[186:187], 0\n\t"
    "v_mfma_f32_4x4x4_16b_bf16 v[212:215], v[224:225], a[188:189], 0\n\t" "v_mfma_f32_4x4x4_16b_bf16 v[216:219], v[226:227], a[190:191], 0\n\t" "v_mul_f32_e64 v28, v240, v24\n\t"
    "v_mfma_f32_16x16x32_bf16 v[56:59], v[160:163], v[188:191], v[56:59]\n\t" "ds_read_b64_tr_b16 v[186:187], v6 offset:64000\n\t" "v_mul_f32_e64 v29, v240, v25\n\t"
    "v_mfma_f32_16x16x32_bf16 v[64:67], v[164:167], v[188:191], v[64:67]\n\t" "ds_read_b64_tr_b16 v[156:157], v7 offset:0\n\t" "v_mul_f32_e64 v30, v241, v26\n\t"
    "v_mfma_f32_16x16x32_bf16 v[68:71], v[164:167], v[192:195], v[68:71]\n\t" "ds_read_b64_tr_b16 v[158:159], v7 offset:512\n\t" "v_cvt_pk_bf16_f32 v196, v204, v205\n\t"
    "v_mfma_f32_16x16x32_bf16 v[76:79], v[168:171], v[192:195], v[76:79]\n\t" "ds_read_b64_tr_b16 v[160:161], v8 offset:0\n\t" "v_cvt_pk_bf16_f32 v197, v206, v207\n\t"
    "v_mfma_f32_16x16x32_bf16 v[72:75], v[168:171], v[188:191], v[72:75]\n\t" "ds_read_b64_tr_b16 v[162:163], v8 offset:512\n\t" "s_waitcnt lgkmcnt(4)\n\t"
    "v_mfma_f32_16x16x32_bf16 v[80:83], v[172:175], v[188:191], v[80:83]\n\t" "ds_read_b64_tr_b16 v[164:165], v9 offset:0\n\t" "v_cvt_pk_bf16_f32 v198, v208, v209\n\t"
    "v_mfma_f32_16x16x32_bf16 v[84:87], v[172:175], v[192:195], v[84:87]\n\t" "ds_read_b64_tr_b16 v[166:167], v9 offset:512\n\t" "v_cvt_pk_bf16_f32 v199, v210, v211\n\t"
    "v_mfma_f32_16x16x32_bf16 v[144:147], v[172:175], v[228:231], v[144:147]\n\t" "ds_read_b128 v[220:223], v2 offset:6144\n\t" "global_load_lds_dwordx4 v0, %[sb] offset:0\n\t"
    "v_mfma_f32_16x16x32_bf16 v[92:95], v[176:179], v[192:195], v[92:95]\n\t" "ds_read_b64_tr_b16 v[168:169], v10 offset:0\n\t" "v_cvt_pk_bf16_f32 v200, v212, v213\n\t"
    "v_mfma_f32_16x16x32_bf16 v[88:91], v[176:179], v[188:191], v[88:91]\n\t" "ds_read_b64_tr_b16 v[170:171], v10 offset:512\n\t" "v_cvt_pk_bf16_f32 v201, v214, v215\n\t"
    "v_mfma_f32_16x16x32_bf16 v[96:99], v[180:183], v[188:191], v[96:99]\n\t" "ds_read_b64_tr_b16 v[172:173], v3 offset:0\n\t" "v_mul_f32 v31, v241, v27\n\t"
    "v_mfma_f32_16x16x32_bf16 v[100:103], v[180:183], v[192:195], v[100:103]\n\t" "ds_read_b64_tr_b16 v[174:175], v3 offset:512\n\t" "v_cvt_pk_bf16_f32 v202, v216, v217\n\t"
    "v_mfma_f32_16x16x32_bf16 v[108:111], v[184:187], v[192:195], v[108:111]\n\t" "ds_read_b64_tr_b16 v[176:177], v4 offset:0\n\t" "v_cvt_pk_bf16_f32 v203, v218, v219\n\t"
    "v_mfma_f32_16x16x32_bf16 v[104:107], v[184:187], v[188:191], v[104:107]\n\t" "ds_read_b64_tr_b16 v[178:179], v4 offset:512\n\t" "s_waitcnt lgkmcnt(4)\n\t"
    "v_mfma_f32_16x16x32_bf16 v[112:115], v[188:191], v[156:159], v[112:115]\n\t" "ds_read_b64_tr_b16 v[180:181], v5 offset:0\n\t" "v_mov_b32 v39, 0xceabfb70\n\t"
    "v_mfma_f32_16x16x32_bf16 v[116:119], v[192:195], v[156:159], v[116:119]\n\t" "ds_read_b64_tr_b16 v[182:183], v5 offset:512\n\t" "v_cndmask_b32 v28, v28, 0, s[56:57]\n\t"
    "v_mfma_f32_16x16x32_bf16 v[148:151], v[228:231], v[156:159], v[148:151]\n\t" "ds_read_b128 v[232:235], v1 offset:23552\n\t" "global_load_lds_dwordx4 v0, %[sb] offset:2048\n\t"
    "v_mfma_f32_16x16x32_bf16 v[124:127], v[192:195], v[160:163], v[124:127]\n\t" "ds_read_b64_tr_b16 v[184:185], v6 offset:0\n\t" "v_add_u32 v0, 0x30000, v0\n\t"
    "v_mfma_f32_16x16x32_bf16 v[120:123], v[188:191], v[160:163], v[120:123]\n\t" "ds_read_b64_tr_b16 v[186:187], v6 offset:512\n\t" "v_add_u32 v16, 0x70000, v16\n\t"
    "v_mfma_f32_16x16x32_bf16 v[128:131], v[188:191], v[164:167], v[128:131]\n\t" "ds_read_b64_tr_b16 v[156:157], v3 offset:2048\n\t" "v_cndmask_b32 v29, v29, 0, s[58:59]\n\t"
    "v_mfma_f32_16x16x32_bf16 v[132:135], v[192:195], v[164:167], v[132:135]\n\t" "ds_read_b64_tr_b16 v[158:159], v3 offset:2560\n\t" "s_mov_b64 exec, %[tm]\n\t" "global_load_dword a64, v16, %[pf]\n\t"
    "s_mov_b64 exec, -1\n\t" "v_mfma_f32_16x16x32_bf16 v[140:143], v[192:195], v[168:171], v[140:143]\n\t" "ds_read_b64_tr_b16 v[160:161], v4 offset:2048\n\t" "v_cndmask_b32 v32, 0, v39, s[56:57]\n\t"
    "v_mfma_f32_16x16x32_bf16 v[136:139], v[188:191], v[168:171], v[136:139]\n\t" "ds_read_b64_tr_b16 v[162:163], v4 offset:2560\n\t" "s_waitcnt lgkmcnt(4)\n\t"
    "v_mfma_f32_16x16x32_bf16 v[48:51], v[172:175], v[196:199], v[48:51]\n\t" "ds_read_b64_tr_b16 v[164:165], v5 offset:2048\n\t" "s_add_u32 m0, %[sl], 120832\n\t"
    "v_mfma_f32_16x16x32_bf16 v[52:55], v[172:175], v[200:203], v[52:55]\n\t" "ds_read_b64_tr_b16 v[166:167], v5 offset:2560\n\t" "v_cndmask_b32 v33, 0, v39, s[58:59]\n\t"
    "v_mfma_f32_16x16x32_bf16 v[152:155], v[232:235], v[172:175], v[152:155]\n\t" "global_load_lds_dwordx4 v0, %[sb] offset:-2048\n\t" "v_lshl_add_u32 v43, v46, 4, %[lh]\n\t"
    "v_mfma_f32_16x16x32_bf16 v[60:63], v[176:179], v[200:203], v[60:63]\n\t" "ds_read_b64_tr_b16 v[168:169], v6 offset:2048\n\t" "v_lshl_add_u32 v36, v47, 3, %[lt]\n\t"
    "v_mfma_f32_4x4x4_16b_bf16 v[204:207], v[220:221], a[192:193], 0\n\t" "v_mfma_f32_4x4x4_16b_bf16 v[208:211], v[222:223], a[194:195], 0\n\t"
    "v_mfma_f32_4x4x4_16b_bf16 v[212:215], v[220:221], a[196:197], 0\n\t" "v_mfma_f32_4x4x4_16b_bf16 v[216:219], v[222:223], a[198:199], 0\n\t" "v_lshl_add_u32 v35, v46, 3, %[lh]\n\t"
    "v_mfma_f32_16x16x32_bf16 v[56:59], v[176:179], v[196:199], v[56:59]\n\t" "ds_read_b64_tr_b16 v[170:171], v6 offset:2560\n\t" "v_add_u32 v35, 0x6800, v35\n\t"
    "v_mfma_f32_16x16x32_bf16 v[64:67], v[180:183], v[196:199], v[64:67]\n\t" "ds_read_b64_tr_b16 v[172:173], v3 offset:4096\n\t" "v_cndmask_b32 v35, v35, v36, s[46:47]\n\t"
    "v_mfma_f32_16x16x32_bf16 v[68:71], v[180:183], v[200:203], v[68:71]\n\t" "ds_read_b64_tr_b16 v[174:175], v3 offset:4608\n\t" "v_cvt_pk_bf16_f32 v188, v204, v205\n\t"
    "v_mfma_f32_16x16x32_bf16 v[76:79], v[184:187], v[200:203], v[76:79]\n\t" "ds_read_b64_tr_b16 v[176:177], v4 offset:4096\n\t" "v_cvt_pk_bf16_f32 v189, v206, v207\n\t"
    "v_mfma_f32_16x16x32_bf16 v[72:75], v[184:187], v[196:199], v[72:75]\n\t" "ds_read_b64_tr_b16 v[178:179], v4 offset:4608\n\t" "s_waitcnt lgkmcnt(4)\n\t"
    "v_mfma_f32_16x16x32_bf16 v[80:83], v[156:159], v[196:199], v[80:83]\n\t" "ds_read_b64_tr_b16 v[180:181], v5 offset:4096\n\t" "v_cvt_pk_bf16_f32 v190, v208, v209\n\t"
    "v_mfma_f32_16x16x32_bf16 v[84:87], v[156:159], v[200:203], v[84:87]\n\t" "ds_read_b64_tr_b16 v[182:183], v5 offset:4608\n\t" "v_cvt_pk_bf16_f32 v191, v210, v211\n\t"
    "v_mfma_f32_16x16x32_bf16 v[144:147], v[156:159], v[232:235], v[144:147]\n\t" "ds_read_b128 v[224:227], v2 offset:6400\n\t" "global_load_lds_dwordx4 v0, %[sb] offset:0\n\t"
    "v_mfma_f32_16x16x32_bf16 v[92:95], v[160:163], v[200:203], v[92:95]\n\t" "ds_read_b64_tr_b16 v[184:185], v6 offset:4096\n\t" "v_cvt_pk_bf16_f32 v192, v212, v213\n\t"
    "v_mfma_f32_16x16x32_bf16 v[88:91], v[160:163], v[196:199], v[88:91]\n\t" "ds_read_b64_tr_b16 v[186:187], v6 offset:4608\n\t" "v_cvt_pk_bf16_f32 v193, v214, v215\n\t"
    "v_mfma_f32_16x16x32_bf16 v[96:99], v[164:167], v[196:199], v[96:99]\n\t" "s_waitcnt vmcnt(15)\n\t" "s_barrier\n\t" "ds_read_b64_tr_b16 v[156:157], v3 offset:12288\n\t"
    "v_mfma_f32_16x16x32_bf16 v[100:103], v[164:167], v[200:203], v[100:103]\n\t" "ds_read_b64_tr_b16 v[158:159], v3 offset:12800\n\t" "v_cvt_pk_bf16_f32 v194, v216, v217\n\t"
    "v_mfma_f32_16x16x32_bf16 v[108:111], v[168:171], v[200:203], v[108:111]\n\t" "ds_read_b64_tr_b16 v[160:161], v4 offset:12288\n\t" "v_cvt_pk_bf16_f32 v195, v218, v219\n\t"
    "v_mfma_f32_16x16x32_bf16 v[104:107], v[168:171], v[196:199], v[104:107]\n\t" "ds_read_b64_tr_b16 v[162:163], v4 offset:12800\n\t" "s_waitcnt lgkmcnt(4)\n\t"
    "v_mfma_f32_16x16x32_bf16 v[112:115], v[196:199], v[172:175], v[112:115]\n\t" "ds_read_b64_tr_b16 v[164:165], v5 offset:12288\n\t" "v_lshl_add_u32 v34, v45, 1, %[lt]\n\t"
    "v_mfma_f32_16x16x32_bf16 v[116:119], v[200:203], v[172:175], v[116:119]\n\t" "ds_read_b64_tr_b16 v[166:167], v5 offset:12800\n\t" "v_lshl_add_u32 v34, v47, 2, v34\n\t"
    "v_mfma_f32_16x16x32_bf16 v[148:151], v[232:235], v[172:175], v[148:151]\n\t" "ds_read_b128 v[228:231], v1 offset:24576\n\t" "global_load_lds_dwordx4 v0, %[sb] offset:2048\n\t"
    "v_mfma_f32_16x16x32_bf16 v[124:127], v[200:203], v[176:179], v[124:127]\n\t" "ds_read_b64_tr_b16 v[168:169], v6 offset:12288\n\t" "v_add_u32 v0, 0x30000, v0\n\t"
    "v_mfma_f32_16x16x32_bf16 v[120:123], v[196:199], v[176:179], v[120:123]\n\t" "ds_read_b64_tr_b16 v[170:171], v6 offset:12800\n\t" "v_add_u32 v12, 0x10000, v12\n\t"
    "v_mfma_f32_16x16x32_bf16 v[128:131], v[196:199], v[180:183], v[128:131]\n\t" "ds_read_b64_tr_b16 v[172:173], v3 offset:14336\n\t" "s_add_u32 m0, %[sl1], 12288\n\t"
    "v_mfma_f32_16x16x32_bf16 v[132:135], v[200:203], v[180:183], v[132:135]\n\t" "ds_read_b64_tr_b16 v[174:175], v3 offset:14848\n\t" "global_load_lds_dwordx4 v12, %[so]\n\t"
    "v_mfma_f32_16x16x32_bf16 v[140:143], v[200:203], v[184:187], v[140:143]\n\t" "ds_read_b64_tr_b16 v[176:177], v4 offset:14336\n\t" "v_add_u32 v34, 0x200, v34\n\t"
    "v_mfma_f32_16x16x32_bf16 v[136:139], v[196:199], v[184:187], v[136:139]\n\t" "ds_read_b64_tr_b16 v[178:179], v4 offset:14848\n\t" "s_waitcnt lgkmcnt(4)\n\t"
    "v_mfma_f32_16x16x32_bf16 v[48:51], v[156:159], v[188:191], v[48:51]\n\t" "ds_read_b64_tr_b16 v[180:181], v5 offset:14336\n\t" "s_add_u32 m0, %[sl], 71680\n\t"
    "v_mfma_f32_16x16x32_bf16 v[52:55], v[156:159], v[192:195], v[52:55]\n\t" "ds_read_b64_tr_b16 v[182:183], v5 offset:14848\n\t" "v_lshl_add_u32 v37, v46, 2, %[lh]\n\t"
    "v_mfma_f32_16x16x32_bf16 v[152:155], v[228:231], v[156:159], v[152:155]\n\t" "global_load_lds_dwordx4 v0, %[sb] offset:-2048\n\t" "v_add_u32 v37, 0x6800, v37\n\t"
    "v_mfma_f32_16x16x32_bf16 v[60:63], v[160:163], v[192:195], v[60:63]\n\t" "ds_read_b64_tr_b16 v[184:185], v6 offset:14336\n\t" "v_cndmask_b32 v34, v37, v34, s[48:49]\n\t"
    "v_mfma_f32_4x4x4_16b_bf16 v[204:207], v[224:225], a[200:201], 0\n\t" "v_mfma_f32_4x4x4_16b_bf16 v[208:211], v[226:227], a[202:203], 0\n\t"
    "v_mfma_f32_4x4x4_16b_bf16 v[212:215], v[224:225], a[204:205], 0\n\t" "v_mfma_f32_4x4x4_16b_bf16 v[216:219], v[226:227], a[206:207], 0\n\t" "v_xor_b32 v37, 0x80, v36\n\t"
    "v_mfma_f32_16x16x32_bf16 v[56:59], v[160:163], v[188:191], v[56:59]\n\t" "ds_read_b64_tr_b16 v[186:187], v6 offset:14848\n\t" "v_xor_b32 v38, 0x100, v36\n\t"
    "v_mfma_f32_16x16x32_bf16 v[64:67], v[164:167], v[188:191], v[64:67]\n\t" "ds_read_b64_tr_b16 v[156:157], v3 offset:16384\n\t" "v_xor_b32 v39, 0x180, v36\n\t"
    "v_mfma_f32_16x16x32_bf16 v[68:71], v[164:167], v[192:195], v[68:71]\n\t" "ds_read_b64_tr_b16 v[158:159], v3 offset:16896\n\t" "v_cvt_pk_bf16_f32 v196, v204, v205\n\t"
    "v_mfma_f32_16x16x32_bf16 v[76:79], v[168:171], v[192:195], v[76:79]\n\t" "ds_read_b64_tr_b16 v[160:161], v4 offset:16384\n\t" "v_cvt_pk_bf16_f32 v197, v206, v207\n\t"
    "v_mfma_f32_16x16x32_bf16 v[72:75], v[168:171], v[188:191], v[72:75]\n\t" "ds_read_b64_tr_b16 v[162:163], v4 offset:16896\n\t" "s_waitcnt lgkmcnt(4)\n\t"
    "v_mfma_f32_16x16x32_bf16 v[80:83], v[172:175], v[188:191], v[80:83]\n\t" "ds_read_b64_tr_b16 v[164:165], v5 offset:16384\n\t" "v_cvt_pk_bf16_f32 v198, v208, v209\n\t"
    "v_mfma_f32_16x16x32_bf16 v[84:87], v[172:175], v[192:195], v[84:87]\n\t" "ds_read_b64_tr_b16 v[166:167], v5 offset:16896\n\t" "v_cvt_pk_bf16_f32 v199, v210, v211\n\t"
    "v_mfma_f32_16x16x32_bf16 v[144:147], v[172:175], v[228:231], v[144:147]\n\t" "ds_read_b128 v[220:223], v2 offset:6656\n\t" "global_load_lds_dwordx4 v0, %[sb] offset:0\n\t"
    "v_mfma_f32_16x16x32_bf16 v[92:95], v[176:179], v[192:195], v[92:95]\n\t" "ds_read_b64_tr_b16 v[168:169], v6 offset:16384\n\t" "v_cvt_pk_bf16_f32 v200, v212, v213\n\t"
    "v_mfma_f32_16x16x32_bf16 v[88:91], v[176:179], v[188:191], v[88:91]\n\t" "ds_read_b64_tr_b16 v[170:171], v6 offset:16896\n\t" "v_cvt_pk_bf16_f32 v201, v214, v215\n\t"
    "v_mfma_f32_16x16x32_bf16 v[96:99], v[180:183], v[188:191], v[96:99]\n\t" "ds_read_b64_tr_b16 v[172:173], v3 offset:24576\n\t" "v_add_u32_e64 v40, %[xw], v43\n\t"
    "v_mfma_f32_16x16x32_bf16 v[100:103], v[180:183], v[192:195], v[100:103]\n\t" "ds_read_b64_tr_b16 v[174:175], v3 offset:25088\n\t" "v_cvt_pk_bf16_f32 v202, v216, v217\n\t"
    "v_mfma_f32_16x16x32_bf16 v[108:111], v[184:187], v[192:195], v[108:111]\n\t" "ds_read_b64_tr_b16 v[176:177], v4 offset:24576\n\t" "v_cvt_pk_bf16_f32 v203, v218, v219\n\t"
    "v_mfma_f32_16x16x32_bf16 v[104:107], v[184:187], v[188:191], v[104:107]\n\t" "ds_read_b64_tr_b16 v[178:179], v4 offset:25088\n\t" "s_waitcnt lgkmcnt(4)\n\t"
    "v_mfma_f32_16x16x32_bf16 v[112:115], v[188:191], v[156:159], v[112:115]\n\t" "ds_read_b64_tr_b16 v[180:181], v5 offset:24576\n\t" "v_xor_b32 v41, 0x800, v40\n\t"
    "v_mfma_f32_16x16x32_bf16 v[116:119], v[192:195], v[156:159], v[116:119]\n\t" "ds_read_b64_tr_b16 v[182:183], v5 offset:25088\n\t" "v_add_u32 v42, %[xh], v43\n\t"
    "v_mfma_f32_16x16x32_bf16 v[148:151], v[228:231], v[156:159], v[148:151]\n\t" "ds_read_b128 v[232:235], v1 offset:25600\n\t" "global_load_lds_dwordx4 v0, %[sb] offset:2048\n\t"
    "v_mfma_f32_16x16x32_bf16 v[124:127], v[192:195], v[160:163], v[124:127]\n\t" "ds_read_b64_tr_b16 v[184:185], v6 offset:24576\n\t" "v_add_u32 v0, 0x30000, v0\n\t"
    "v_mfma_f32_16x16x32_bf16 v[120:123], v[188:191], v[160:163], v[120:123]\n\t" "ds_read_b64_tr_b16 v[186:187], v6 offset:25088\n\t" "v_add_u32_e64 v43, %[w4k], v43\n\t"
    "v_mfma_f32_16x16x32_bf16 v[128:131], v[188:191], v[164:167], v[128:131]\n\t" "ds_read_b64_tr_b16 v[156:157], v3 offset:26624\n\t" "v_lshl_add_u32 v44, v46, 4, %[ox]\n\t"
    "v_mfma_f32_16x16x32_bf16 v[132:135], v[192:195], v[164:167], v[132:135]\n\t" "ds_read_b64_tr_b16 v[158:159], v3 offset:27136\n\t" "v_xor_b32 v45, 0x800, v44\n\t"
    "v_mfma_f32_16x16x32_bf16 v[140:143], v[192:195], v[168:171], v[140:143]\n\t" "ds_read_b64_tr_b16 v[160:161], v4 offset:26624\n\t"
    "v_mfma_f32_16x16x32_bf16 v[136:139], v[188:191], v[168:171], v[136:139]\n\t" "ds_read_b64_tr_b16 v[162:163], v4 offset:27136\n\t" "s_waitcnt lgkmcnt(4)\n\t"
    "v_mfma_f32_16x16x32_bf16 v[48:51], v[172:175], v[196:199], v[48:51]\n\t" "ds_read_b64_tr_b16 v[164:165], v5 offset:26624\n\t" "s_add_u32 m0, %[sl], 83968\n\t"
    "v_mfma_f32_16x16x32_bf16 v[52:55], v[172:175], v[200:203], v[52:55]\n\t" "ds_read_b64_tr_b16 v[166:167], v5 offset:27136\n\t"
    "v_mfma_f32_16x16x32_bf16 v[152:155], v[232:235], v[172:175], v[152:155]\n\t" "global_load_lds_dwordx4 v0, %[sb] offset:-2048\n\t"
    "v_mfma_f32_16x16x32_bf16 v[60:63], v[176:179], v[200:203], v[60:63]\n\t" "ds_read_b64_tr_b16 v[168:169], v6 offset:26624\n\t" "v_mfma_f32_4x4x4_16b_bf16 v[204:207], v[220:221], a[208:209], 0\n\t"
    "v_mfma_f32_4x4x4_16b_bf16 v[208:211], v[222:223], a[210:211], 0\n\t" "v_mfma_f32_4x4x4_16b_bf16 v[212:215], v[220:221], a[212:213], 0\n\t"
    "v_mfma_f32_4x4x4_16b_bf16 v[216:219], v[222:223], a[214:215], 0\n\t" "v_mfma_f32_16x16x32_bf16 v[56:59], v[176:179], v[196:199], v[56:59]\n\t" "ds_read_b64_tr_b16 v[170:171], v6 offset:27136\n\t"
    "v_mfma_f32_16x16x32_bf16 v[64:67], v[180:183], v[196:199], v[64:67]\n\t" "ds_read_b64_tr_b16 v[172:173], v3 offset:28672\n\t" "v_mfma_f32_16x16x32_bf16 v[68:71], v[180:183], v[200:203], v[68:71]\n\t"
    "ds_read_b64_tr_b16 v[174:175], v3 offset:29184\n\t" "v_cvt_pk_bf16_f32 v188, v204, v205\n\t" "v_mfma_f32_16x16x32_bf16 v[76:79], v[184:187], v[200:203], v[76:79]\n\t"
    "ds_read_b64_tr_b16 v[176:177], v4 offset:28672\n\t" "v_cvt_pk_bf16_f32 v189, v206, v207\n\t" "v_mfma_f32_16x16x32_bf16 v[72:75], v[184:187], v[196:199], v[72:75]\n\t"
    "ds_read_b64_tr_b16 v[178:179], v4 offset:29184\n\t" "s_waitcnt lgkmcnt(4)\n\t" "v_mfma_f32_16x16x32_bf16 v[80:83], v[156:159], v[196:199], v[80:83]\n\t"
    "ds_read_b64_tr_b16 v[180:181], v5 offset:28672\n\t" "v_cvt_pk_bf16_f32 v190, v208, v209\n\t" "v_mfma_f32_16x16x32_bf16 v[84:87], v[156:159], v[200:203], v[84:87]\n\t"
    "ds_read_b64_tr_b16 v[182:183], v5 offset:29184\n\t" "v_cvt_pk_bf16_f32 v191, v210, v211\n\t" "v_mfma_f32_16x16x32_bf16 v[144:147], v[156:159], v[232:235], v[144:147]\n\t"
    "ds_read_b128 v[224:227], v2 offset:6912\n\t" "global_load_lds_dwordx4 v0, %[sb] offset:0\n\t" "v_mfma_f32_16x16x32_bf16 v[92:95], v[160:163], v[200:203], v[92:95]\n\t"
    "ds_read_b64_tr_b16 v[184:185], v6 offset:28672\n\t" "v_cvt_pk_bf16_f32 v192, v212, v213\n\t" "v_mfma_f32_16x16x32_bf16 v[88:91], v[160:163], v[196:199], v[88:91]\n\t"
    "ds_read_b64_tr_b16 v[186:187], v6 offset:29184\n\t" "v_cvt_pk_bf16_f32 v193, v214, v215\n\t" "v_mfma_f32_16x16x32_bf16 v[96:99], v[164:167], v[196:199], v[96:99]\n\t" "s_waitcnt vmcnt(14)\n\t"
    "s_barrier\n\t" "ds_read_b64_tr_b16 v[156:157], v3 offset:36864\n\t" "v_mfma_f32_16x16x32_bf16 v[100:103], v[164:167], v[200:203], v[100:103]\n\t" "ds_read_b64_tr_b16 v[158:159], v3 offset:37376\n\t"
    "v_cvt_pk_bf16_f32 v194, v216, v217\n\t" "v_mfma_f32_16x16x32_bf16 v[108:111], v[168:171], v[200:203], v[108:111]\n\t" "ds_read_b64_tr_b16 v[160:161], v4 offset:36864\n\t"
    "v_cvt_pk_bf16_f32 v195, v218, v219\n\t" "v_mfma_f32_16x16x32_bf16 v[104:107], v[168:171], v[196:199], v[104:107]\n\t" "ds_read_b64_tr_b16 v[162:163], v4 offset:37376\n\t" "s_waitcnt lgkmcnt(4)\n\t"
    "v_mfma_f32_16x16x32_bf16 v[112:115], v[196:199], v[172:175], v[112:115]\n\t" "ds_read_b64_tr_b16 v[164:165], v5 offset:36864\n\t"
    "v_mfma_f32_16x16x32_bf16 v[116:119], v[200:203], v[172:175], v[116:119]\n\t" "ds_read_b64_tr_b16 v[166:167], v5 offset:37376\n\t"
    "v_mfma_f32_16x16x32_bf16 v[148:151], v[232:235], v[172:175], v[148:151]\n\t" "ds_read_b128 v[228:231], v1 offset:26624\n\t" "global_load_lds_dwordx4 v0, %[sb] offset:2048\n\t"
    "v_mfma_f32_16x16x32_bf16 v[124:127], v[200:203], v[176:179], v[124:127]\n\t" "ds_read_b64_tr_b16 v[168:169], v6 offset:36864\n\t" "v_add_u32 v16, 0x70000, v16\n\t"
    "v_mfma_f32_16x16x32_bf16 v[120:123], v[196:199], v[176:179], v[120:123]\n\t" "ds_read_b64_tr_b16 v[170:171], v6 offset:37376\n\t"
    "v_mfma_f32_16x16x32_bf16 v[128:131], v[196:199], v[180:183], v[128:131]\n\t" "ds_read_b64_tr_b16 v[172:173], v3 offset:38912\n\t"
    "v_mfma_f32_16x16x32_bf16 v[132:135], v[200:203], v[180:183], v[132:135]\n\t" "ds_read_b64_tr_b16 v[174:175], v3 offset:39424\n\t" "s_mov_b64 exec, %[tm]\n\t" "global_load_dword a64, v16, %[pf]\n\t"
    "s_mov_b64 exec, -1\n\t" "v_mfma_f32_16x16x32_bf16 v[140:143], v[200:203], v[184:187], v[140:143]\n\t" "ds_read_b64_tr_b16 v[176:177], v4 offset:38912\n\t"
    "v_mfma_f32_16x16x32_bf16 v[136:139], v[196:199], v[184:187], v[136:139]\n\t" "ds_read_b64_tr_b16 v[178:179], v4 offset:39424\n\t" "s_waitcnt lgkmcnt(4)\n\t"
    "v_mfma_f32_16x16x32_bf16 v[48:51], v[156:159], v[188:191], v[48:51]\n\t" "ds_read_b64_tr_b16 v[180:181], v5 offset:38912\n\t" "v_add_u32 v12, 0x20000, v12\n\t"
    "v_mfma_f32_16x16x32_bf16 v[52:55], v[156:159], v[192:195], v[52:55]\n\t" "ds_read_b64_tr_b16 v[182:183], v5 offset:39424\n\t" "s_add_u32 m0, %[sl1], 20480\n\t"
    "v_mfma_f32_16x16x32_bf16 v[152:155], v[228:231], v[156:159], v[152:155]\n\t" "global_load_lds_dwordx4 v12, %[so]\n\t" "v_add_u32 v12, 0x10000, v12\n\t"
    "v_mfma_f32_16x16x32_bf16 v[60:63], v[160:163], v[192:195], v[60:63]\n\t" "ds_read_b64_tr_b16 v[184:185], v6 offset:38912\n\t" "v_mfma_f32_4x4x4_16b_bf16 v[204:207], v[224:225], a[216:217], 0\n\t"
    "v_mfma_f32_4x4x4_16b_bf16 v[208:211], v[226:227], a[218:219], 0\n\t" "v_mfma_f32_4x4x4_16b_bf16 v[212:215], v[224:225], a[220:221], 0\n\t"
    "v_mfma_f32_4x4x4_16b_bf16 v[216:219], v[226:227], a[222:223], 0\n\t" "v_mfma_f32_16x16x32_bf16 v[56:59], v[160:163], v[188:191], v[56:59]\n\t" "ds_read_b64_tr_b16 v[186:187], v6 offset:39424\n\t"
    "v_mfma_f32_16x16x32_bf16 v[64:67], v[164:167], v[188:191], v[64:67]\n\t" "ds_read_b64_tr_b16 v[156:157], v3 offset:40960\n\t" "v_mfma_f32_16x16x32_bf16 v[68:71], v[164:167], v[192:195], v[68:71]\n\t"
    "ds_read_b64_tr_b16 v[158:159], v3 offset:41472\n\t" "v_cvt_pk_bf16_f32 v196, v204, v205\n\t" "v_mfma_f32_16x16x32_bf16 v[76:79], v[168:171], v[192:195], v[76:79]\n\t"
    "ds_read_b64_tr_b16 v[160:161], v4 offset:40960\n\t" "v_cvt_pk_bf16_f32 v197, v206, v207\n\t" "v_mfma_f32_16x16x32_bf16 v[72:75], v[168:171], v[188:191], v[72:75]\n\t"
    "ds_read_b64_tr_b16 v[162:163], v4 offset:41472\n\t" "s_waitcnt lgkmcnt(4)\n\t" "v_mfma_f32_16x16x32_bf16 v[80:83], v[172:175], v[188:191], v[80:83]\n\t"
    "ds_read_b64_tr_b16 v[164:165], v5 offset:40960\n\t" "v_cvt_pk_bf16_f32 v198, v208, v209\n\t" "v_mfma_f32_16x16x32_bf16 v[84:87], v[172:175], v[192:195], v[84:87]\n\t"
    "ds_read_b64_tr_b16 v[166:167], v5 offset:41472\n\t" "v_cvt_pk_bf16_f32 v199, v210, v211\n\t" "v_mfma_f32_16x16x32_bf16 v[144:147], v[172:175], v[228:231], v[144:147]\n\t"
    "ds_read_b128 v[220:223], v2 offset:7168\n\t" "s_add_u32 m0, %[sl1], 24576\n\t" "v_mfma_f32_16x16x32_bf16 v[92:95], v[176:179], v[192:195], v[92:95]\n\t"
    "ds_read_b64_tr_b16 v[168:169], v6 offset:40960\n\t" "v_cvt_pk_bf16_f32 v200, v212, v213\n\t" "v_mfma_f32_16x16x32_bf16 v[88:91], v[176:179], v[188:191], v[88:91]\n\t"
    "ds_read_b64_tr_b16 v[170:171], v6 offset:41472\n\t" "v_cvt_pk_bf16_f32 v201, v214, v215\n\t" "v_mfma_f32_16x16x32_bf16 v[96:99], v[180:183], v[188:191], v[96:99]\n\t"
    "ds_read_b64_tr_b16 v[172:173], v7 offset:8192\n\t" "global_load_lds_dwordx4 v12, %[so]\n\t" "v_mfma_f32_16x16x32_bf16 v[100:103], v[180:183], v[192:195], v[100:103]\n\t"
    "ds_read_b64_tr_b16 v[174:175], v7 offset:8704\n\t" "v_cvt_pk_bf16_f32 v202, v216, v217\n\t" "v_mfma_f32_16x16x32_bf16 v[108:111], v[184:187], v[192:195], v[108:111]\n\t"
    "ds_read_b64_tr_b16 v[176:177], v8 offset:8192\n\t" "v_cvt_pk_bf16_f32 v203, v218, v219\n\t" "v_mfma_f32_16x16x32_bf16 v[104:107], v[184:187], v[188:191], v[104:107]\n\t"
    "ds_read_b64_tr_b16 v[178:179], v8 offset:8704\n\t" "s_waitcnt lgkmcnt(4)\n\t" "v_mfma_f32_16x16x32_bf16 v[112:115], v[188:191], v[156:159], v[112:115]\n\t"
    "ds_read_b64_tr_b16 v[180:181], v9 offset:8192\n\t" "v_add_u32 v12, 0x10000, v12\n\t" "v_mfma_f32_16x16x32_bf16 v[116:119], v[192:195], v[156:159], v[116:119]\n\t"
    "ds_read_b64_tr_b16 v[182:183], v9 offset:8704\n\t" "s_add_u32 m0, %[sl1], 28672\n\t" "v_mfma_f32_16x16x32_bf16 v[148:151], v[228:231], v[156:159], v[148:151]\n\t"
    "ds_read_b128 v[232:235], v1 offset:27648\n\t" "global_load_lds_dwordx4 v12, %[so]\n\t" "v_mfma_f32_16x16x32_bf16 v[124:127], v[192:195], v[160:163], v[124:127]\n\t"
    "ds_read_b64_tr_b16 v[184:185], v10 offset:8192\n\t" "v_mfma_f32_16x16x32_bf16 v[120:123], v[188:191], v[160:163], v[120:123]\n\t" "ds_read_b64_tr_b16 v[186:187], v10 offset:8704\n\t"
    "v_mfma_f32_16x16x32_bf16 v[128:131], v[188:191], v[164:167], v[128:131]\n\t" "ds_read_b64_tr_b16 v[156:157], v7 offset:10240\n\t"
    "v_mfma_f32_16x16x32_bf16 v[132:135], v[192:195], v[164:167], v[132:135]\n\t" "ds_read_b64_tr_b16 v[158:159], v7 offset:10752\n\t"
    "v_mfma_f32_16x16x32_bf16 v[140:143], v[192:195], v[168:171], v[140:143]\n\t" "ds_read_b64_tr_b16 v[160:161], v8 offset:10240\n\t"
    "v_mfma_f32_16x16x32_bf16 v[136:139], v[188:191], v[168:171], v[136:139]\n\t" "ds_read_b64_tr_b16 v[162:163], v8 offset:10752\n\t" "s_waitcnt lgkmcnt(4)\n\t"
    "v_mfma_f32_16x16x32_bf16 v[48:51], v[172:175], v[196:199], v[48:51]\n\t" "ds_read_b64_tr_b16 v[164:165], v9 offset:10240\n\t" "v_add_u32 v12, 0x10000, v12\n\t"
    "v_mfma_f32_16x16x32_bf16 v[52:55], v[172:175], v[200:203], v[52:55]\n\t" "ds_read_b64_tr_b16 v[166:167], v9 offset:10752\n\t" "s_add_u32 m0, %[sl1], 32768\n\t"
    "v_mfma_f32_16x16x32_bf16 v[152:155], v[232:235], v[172:175], v[152:155]\n\t" "global_load_lds_dwordx4 v12, %[so]\n\t" "v_add_u32 v12, 0x10000, v12\n\t"
    "v_mfma_f32_16x16x32_bf16 v[60:63], v[176:179], v[200:203], v[60:63]\n\t" "ds_read_b64_tr_b16 v[168:169], v10 offset:10240\n\t" "v_mfma_f32_4x4x4_16b_bf16 v[204:207], v[220:221], a[224:225], 0\n\t"
    "v_mfma_f32_4x4x4_16b_bf16 v[208:211], v[222:223], a[226:227], 0\n\t" "v_mfma_f32_4x4x4_16b_bf16 v[212:215], v[220:221], a[228:229], 0\n\t"
    "v_mfma_f32_4x4x4_16b_bf16 v[216:219], v[222:223], a[230:231], 0\n\t" "v_mfma_f32_16x16x32_bf16 v[56:59], v[176:179], v[196:199], v[56:59]\n\t" "ds_read_b64_tr_b16 v[170:171], v10 offset:10752\n\t"
    "v_mfma_f32_16x16x32_bf16 v[64:67], v[180:183], v[196:199], v[64:67]\n\t" "ds_read_b64_tr_b16 v[172:173], v7 offset:12288\n\t" "v_mfma_f32_16x16x32_bf16 v[68:71], v[180:183], v[200:203], v[68:71]\n\t"
    "ds_read_b64_tr_b16 v[174:175], v7 offset:12800\n\t" "v_cvt_pk_bf16_f32 v188, v204, v205\n\t" "v_mfma_f32_16x16x32_bf16 v[76:79], v[184:187], v[200:203], v[76:79]\n\t"
    "ds_read_b64_tr_b16 v[176:177], v8 offset:12288\n\t" "v_cvt_pk_bf16_f32 v189, v206, v207\n\t" "v_mfma_f32_16x16x32_bf16 v[72:75], v[184:187], v[196:199], v[72:75]\n\t"
    "ds_read_b64_tr_b16 v[178:179], v8 offset:12800\n\t" "s_waitcnt lgkmcnt(4)\n\t" "v_mfma_f32_16x16x32_bf16 v[80:83], v[156:159], v[196:199], v[80:83]\n\t"
    "ds_read_b64_tr_b16 v[180:181], v9 offset:12288\n\t" "v_cvt_pk_bf16_f32 v190, v208, v209\n\t" "v_mfma_f32_16x16x32_bf16 v[84:87], v[156:159], v[200:203], v[84:87]\n\t"
    "ds_read_b64_tr_b16 v[182:183], v9 offset:12800\n\t" "v_cvt_pk_bf16_f32 v191, v210, v211\n\t" "v_mfma_f32_16x16x32_bf16 v[144:147], v[156:159], v[232:235], v[144:147]\n\t"
    "ds_read_b128 v[224:227], v2 offset:7424\n\t" "s_add_u32 m0, %[sl1], 36864\n\t" "v_mfma_f32_16x16x32_bf16 v[92:95], v[160:163], v[200:203], v[92:95]\n\t"
    "ds_read_b64_tr_b16 v[184:185], v10 offset:12288\n\t" "v_cvt_pk_bf16_f32 v192, v212, v213\n\t" "v_mfma_f32_16x16x32_bf16 v[88:91], v[160:163], v[196:199], v[88:91]\n\t"
    "ds_read_b64_tr_b16 v[186:187], v10 offset:12800\n\t" "v_cvt_pk_bf16_f32 v193, v214, v215\n\t" "v_mfma_f32_16x16x32_bf16 v[96:99], v[164:167], v[196:199], v[96:99]\n\t" "s_waitcnt vmcnt(12)\n\t"
    "s_barrier\n\t" "ds_read_b64_tr_b16 v[156:157], v7 offset:20480\n\t" "v_mfma_f32_16x16x32_bf16 v[100:103], v[164:167], v[200:203], v[100:103]\n\t" "ds_read_b64_tr_b16 v[158:159], v7 offset:20992\n\t"
    "v_cvt_pk_bf16_f32 v194, v216, v217\n\t" "v_mfma_f32_16x16x32_bf16 v[108:111], v[168:171], v[200:203], v[108:111]\n\t" "ds_read_b64_tr_b16 v[160:161], v8 offset:20480\n\t"
    "v_cvt_pk_bf16_f32 v195, v218, v219\n\t" "v_mfma_f32_16x16x32_bf16 v[104:107], v[168:171], v[196:199], v[104:107]\n\t" "ds_read_b64_tr_b16 v[162:163], v8 offset:20992\n\t" "s_waitcnt lgkmcnt(4)\n\t"
    "v_mfma_f32_16x16x32_bf16 v[112:115], v[196:199], v[172:175], v[112:115]\n\t" "ds_read_b64_tr_b16 v[164:165], v9 offset:20480\n\t" "global_load_lds_dwordx4 v12, %[so]\n\t"
    "v_mfma_f32_16x16x32_bf16 v[116:119], v[200:203], v[172:175], v[116:119]\n\t" "ds_read_b64_tr_b16 v[166:167], v9 offset:20992\n\t" "v_add_u32 v12, 0x10000, v12\n\t"
    "v_mfma_f32_16x16x32_bf16 v[148:151], v[232:235], v[172:175], v[148:151]\n\t" "ds_read_b128 v[228:231], v1 offset:28672\n\t" "s_add_u32 m0, %[sl1], 40960\n\t"
    "v_mfma_f32_16x16x32_bf16 v[124:127], v[200:203], v[176:179], v[124:127]\n\t" "ds_read_b64_tr_b16 v[168:169], v10 offset:20480\n\t" "global_load_lds_dwordx4 v12, %[so]\n\t"
    "v_mfma_f32_16x16x32_bf16 v[120:123], v[196:199], v[176:179], v[120:123]\n\t" "ds_read_b64_tr_b16 v[170:171], v10 offset:20992\n\t"
    "v_mfma_f32_16x16x32_bf16 v[128:131], v[196:199], v[180:183], v[128:131]\n\t" "ds_read_b64_tr_b16 v[172:173], v7 offset:22528\n\t"
    "v_mfma_f32_16x16x32_bf16 v[132:135], v[200:203], v[180:183], v[132:135]\n\t" "ds_read_b64_tr_b16 v[174:175], v7 offset:23040\n\t"
    "v_mfma_f32_16x16x32_bf16 v[140:143], v[200:203], v[184:187], v[140:143]\n\t" "ds_read_b64_tr_b16 v[176:177], v8 offset:22528\n\t"
    "v_mfma_f32_16x16x32_bf16 v[136:139], v[196:199], v[184:187], v[136:139]\n\t" "ds_read_b64_tr_b16 v[178:179], v8 offset:23040\n\t" "s_waitcnt lgkmcnt(4)\n\t"
    "v_mfma_f32_16x16x32_bf16 v[48:51], v[156:159], v[188:191], v[48:51]\n\t" "ds_read_b64_tr_b16 v[180:181], v9 offset:22528\n\t" "v_add_u32 v12, 0xfffa0000, v12\n\t"
    "v_mfma_f32_16x16x32_bf16 v[52:55], v[156:159], v[192:195], v[52:55]\n\t" "ds_read_b64_tr_b16 v[182:183], v9 offset:23040\n\t" "s_add_u32 m0, %[sl1], 16384\n\t"
    "v_mfma_f32_16x16x32_bf16 v[152:155], v[228:231], v[156:159], v[152:155]\n\t" "global_load_lds_dwordx4 v12, %[so]\n\t" "v_add_u32 v12, 0x70000, v12\n\t"
    "v_mfma_f32_16x16x32_bf16 v[60:63], v[160:163], v[192:195], v[60:63]\n\t" "ds_read_b64_tr_b16 v[184:185], v10 offset:22528\n\t" "s_add_u32 m0, %[sl1], 45056\n\t"
    "v_mfma_f32_4x4x4_16b_bf16 v[204:207], v[224:225], a[232:233], 0\n\t" "v_mfma_f32_4x4x4_16b_bf16 v[208:211], v[226:227], a[234:235], 0\n\t"
    "v_mfma_f32_4x4x4_16b_bf16 v[212:215], v[224:225], a[236:237], 0\n\t" "v_mfma_f32_4x4x4_16b_bf16 v[216:219], v[226:227], a[238:239], 0\n\t"
    "v_mfma_f32_16x16x32_bf16 v[56:59], v[160:163], v[188:191], v[56:59]\n\t" "ds_read_b64_tr_b16 v[186:187], v10 offset:23040\n\t"
    "v_mfma_f32_16x16x32_bf16 v[64:67], v[164:167], v[188:191], v[64:67]\n\t" "ds_read_b64_tr_b16 v[156:157], v7 offset:24576\n\t" "global_load_lds_dwordx4 v12, %[so]\n\t"
    "v_mfma_f32_16x16x32_bf16 v[68:71], v[164:167], v[192:195], v[68:71]\n\t" "ds_read_b64_tr_b16 v[158:159], v7 offset:25088\n\t" "v_cvt_pk_bf16_f32 v196, v204, v205\n\t"
    "v_mfma_f32_16x16x32_bf16 v[76:79], v[168:171], v[192:195], v[76:79]\n\t" "ds_read_b64_tr_b16 v[160:161], v8 offset:24576\n\t" "v_cvt_pk_bf16_f32 v197, v206, v207\n\t"
    "v_mfma_f32_16x16x32_bf16 v[72:75], v[168:171], v[188:191], v[72:75]\n\t" "ds_read_b64_tr_b16 v[162:163], v8 offset:25088\n\t" "s_waitcnt lgkmcnt(4)\n\t"
    "v_mfma_f32_16x16x32_bf16 v[80:83], v[172:175], v[188:191], v[80:83]\n\t" "ds_read_b64_tr_b16 v[164:165], v9 offset:24576\n\t" "v_cvt_pk_bf16_f32 v198, v208, v209\n\t"
    "v_mfma_f32_16x16x32_bf16 v[84:87], v[172:175], v[192:195], v[84:87]\n\t" "ds_read_b64_tr_b16 v[166:167], v9 offset:25088\n\t" "v_cvt_pk_bf16_f32 v199, v210, v211\n\t"
    "v_mfma_f32_16x16x32_bf16 v[144:147], v[172:175], v[228:231], v[144:147]\n\t" "ds_read_b128 v[220:223], v2 offset:7680\n\t" "v_add_u32 v12, 0x10000, v12\n\t"
    "v_mfma_f32_16x16x32_bf16 v[92:95], v[176:179], v[192:195], v[92:95]\n\t" "ds_read_b64_tr_b16 v[168:169], v10 offset:24576\n\t" "v_cvt_pk_bf16_f32 v200, v212, v213\n\t"
    "v_mfma_f32_16x16x32_bf16 v[88:91], v[176:179], v[188:191], v[88:91]\n\t" "ds_read_b64_tr_b16 v[170:171], v10 offset:25088\n\t" "v_cvt_pk_bf16_f32 v201, v214, v215\n\t"
    "v_mfma_f32_16x16x32_bf16 v[96:99], v[180:183], v[188:191], v[96:99]\n\t" "ds_read_b64_tr_b16 v[172:173], v7 offset:32768\n\t" "s_add_u32 m0, %[sl1], 49152\n\t"
    "v_mfma_f32_16x16x32_bf16 v[100:103], v[180:183], v[192:195], v[100:103]\n\t" "ds_read_b64_tr_b16 v[174:175], v7 offset:33280\n\t" "v_cvt_pk_bf16_f32 v202, v216, v217\n\t"
    "v_mfma_f32_16x16x32_bf16 v[108:111], v[184:187], v[192:195], v[108:111]\n\t" "ds_read_b64_tr_b16 v[176:177], v8 offset:32768\n\t" "v_cvt_pk_bf16_f32 v203, v218, v219\n\t"
    "v_mfma_f32_16x16x32_bf16 v[104:107], v[184:187], v[188:191], v[104:107]\n\t" "ds_read_b64_tr_b16 v[178:179], v8 offset:33280\n\t" "s_waitcnt lgkmcnt(4)\n\t"
    "v_mfma_f32_16x16x32_bf16 v[112:115], v[188:191], v[156:159], v[112:115]\n\t" "ds_read_b64_tr_b16 v[180:181], v9 offset:32768\n\t" "global_load_lds_dwordx4 v12, %[so]\n\t"
    "v_mfma_f32_16x16x32_bf16 v[116:119], v[192:195], v[156:159], v[116:119]\n\t" "ds_read_b64_tr_b16 v[182:183], v9 offset:33280\n\t" "v_add_u32 v12, 0x10000, v12\n\t"
    "v_mfma_f32_16x16x32_bf16 v[148:151], v[228:231], v[156:159], v[148:151]\n\t" "ds_read_b128 v[232:235], v1 offset:29696\n\t" "s_add_u32 m0, %[sl1], 53248\n\t"
    "v_mfma_f32_16x16x32_bf16 v[124:127], v[192:195], v[160:163], v[124:127]\n\t" "ds_read_b64_tr_b16 v[184:185], v10 offset:32768\n\t" "global_load_lds_dwordx4 v12, %[so]\n\t"
    "v_mfma_f32_16x16x32_bf16 v[120:123], v[188:191], v[160:163], v[120:123]\n\t" "ds_read_b64_tr_b16 v[186:187], v10 offset:33280\n\t" "v_add_u32 v16, 0x70000, v16\n\t"
    "v_mfma_f32_16x16x32_bf16 v[128:131], v[188:191], v[164:167], v[128:131]\n\t" "ds_read_b64_tr_b16 v[156:157], v7 offset:34816\n\t"
    "v_mfma_f32_16x16x32_bf16 v[132:135], v[192:195], v[164:167], v[132:135]\n\t" "ds_read_b64_tr_b16 v[158:159], v7 offset:35328\n\t" "s_mov_b64 exec, %[tm]\n\t" "global_load_dword a64, v16, %[pf]\n\t"
    "s_mov_b64 exec, -1\n\t" "v_mfma_f32_16x16x32_bf16 v[140:143], v[192:195], v[168:171], v[140:143]\n\t" "ds_read_b64_tr_b16 v[160:161], v8 offset:34816\n\t"
    "v_mfma_f32_16x16x32_bf16 v[136:139], v[188:191], v[168:171], v[136:139]\n\t" "ds_read_b64_tr_b16 v[162:163], v8 offset:35328\n\t" "s_waitcnt lgkmcnt(4)\n\t"
    "v_mfma_f32_16x16x32_bf16 v[48:51], v[172:175], v[196:199], v[48:51]\n\t" "ds_read_b64_tr_b16 v[164:165], v9 offset:34816\n\t" "v_add_u32 v12, 0x10000, v12\n\t"
    "v_mfma_f32_16x16x32_bf16 v[52:55], v[172:175], v[200:203], v[52:55]\n\t" "ds_read_b64_tr_b16 v[166:167], v9 offset:35328\n\t" "s_add_u32 m0, %[sl1], 57344\n\t"
    "v_mfma_f32_16x16x32_bf16 v[152:155], v[232:235], v[172:175], v[152:155]\n\t" "global_load_lds_dwordx4 v12, %[so]\n\t" "v_add_u32 v12, 0x10000, v12\n\t"
    "v_mfma_f32_16x16x32_bf16 v[60:63], v[176:179], v[200:203], v[60:63]\n\t" "ds_read_b64_tr_b16 v[168:169], v10 offset:34816\n\t" "v_mfma_f32_4x4x4_16b_bf16 v[204:207], v[220:221], a[240:241], 0\n\t"
    "v_mfma_f32_4x4x4_16b_bf16 v[208:211], v[222:223], a[242:243], 0\n\t" "v_mfma_f32_4x4x4_16b_bf16 v[212:215], v[220:221], a[244:245], 0\n\t"
    "v_mfma_f32_4x4x4_16b_bf16 v[216:219], v[222:223], a[246:247], 0\n\t" "v_mfma_f32_16x16x32_bf16 v[56:59], v[176:179], v[196:199], v[56:59]\n\t" "ds_read_b64_tr_b16 v[170:171], v10 offset:35328\n\t"
    "v_mfma_f32_16x16x32_bf16 v[64:67], v[180:183], v[196:199], v[64:67]\n\t" "ds_read_b64_tr_b16 v[172:173], v7 offset:36864\n\t" "v_mfma_f32_16x16x32_bf16 v[68:71], v[180:183], v[200:203], v[68:71]\n\t"
    "ds_read_b64_tr_b16 v[174:175], v7 offset:37376\n\t" "v_cvt_pk_bf16_f32 v188, v204, v205\n\t" "v_mfma_f32_16x16x32_bf16 v[76:79], v[184:187], v[200:203], v[76:79]\n\t"
    "ds_read_b64_tr_b16 v[176:177], v8 offset:36864\n\t" "v_cvt_pk_bf16_f32 v189, v206, v207\n\t" "v_mfma_f32_16x16x32_bf16 v[72:75], v[184:187], v[196:199], v[72:75]\n\t"
    "ds_read_b64_tr_b16 v[178:179], v8 offset:37376\n\t" "s_waitcnt lgkmcnt(4)\n\t" "v_mfma_f32_16x16x32_bf16 v[80:83], v[156:159], v[196:199], v[80:83]\n\t"
    "ds_read_b64_tr_b16 v[180:181], v9 offset:36864\n\t" "v_cvt_pk_bf16_f32 v190, v208, v209\n\t" "v_mfma_f32_16x16x32_bf16 v[84:87], v[156:159], v[200:203], v[84:87]\n\t"
    "ds_read_b64_tr_b16 v[182:183], v9 offset:37376\n\t" "v_cvt_pk_bf16_f32 v191, v210, v211\n\t" "v_mfma_f32_16x16x32_bf16 v[144:147], v[156:159], v[232:235], v[144:147]\n\t"
    "ds_read_b128 v[224:227], v2 offset:7936\n\t" "s_add_u32 m0, %[sl1], 61440\n\t" "v_mfma_f32_16x16x32_bf16 v[92:95], v[160:163], v[200:203], v[92:95]\n\t"
    "ds_read_b64_tr_b16 v[184:185], v10 offset:36864\n\t" "v_cvt_pk_bf16_f32 v192, v212, v213\n\t" "v_mfma_f32_16x16x32_bf16 v[88:91], v[160:163], v[196:199], v[88:91]\n\t"
    "ds_read_b64_tr_b16 v[186:187], v10 offset:37376\n\t" "v_cvt_pk_bf16_f32 v193, v214, v215\n\t" "v_mfma_f32_16x16x32_bf16 v[96:99], v[164:167], v[196:199], v[96:99]\n\t" "s_waitcnt vmcnt(13)\n\t"
    "s_barrier\n\t" "ds_read_b64_tr_b16 v[156:157], v3 offset:51200\n\t" "v_mfma_f32_16x16x32_bf16 v[100:103], v[164:167], v[200:203], v[100:103]\n\t" "ds_read_b64_tr_b16 v[158:159], v3 offset:51712\n\t"
    "v_cvt_pk_bf16_f32 v194, v216, v217\n\t" "v_mfma_f32_16x16x32_bf16 v[108:111], v[168:171], v[200:203], v[108:111]\n\t" "ds_read_b64_tr_b16 v[160:161], v3 offset:53248\n\t"
    "v_cvt_pk_bf16_f32 v195, v218, v219\n\t" "v_mfma_f32_16x16x32_bf16 v[104:107], v[168:171], v[196:199], v[104:107]\n\t" "ds_read_b64_tr_b16 v[162:163], v3 offset:53760\n\t" "s_waitcnt lgkmcnt(4)\n\t"
    "v_mfma_f32_16x16x32_bf16 v[112:115], v[196:199], v[172:175], v[112:115]\n\t" "ds_read_b64_tr_b16 v[164:165], v3 offset:49152\n\t" "global_load_lds_dwordx4 v12, %[so]\n\t"
    "v_mfma_f32_16x16x32_bf16 v[116:119], v[200:203], v[172:175], v[116:119]\n\t" "ds_read_b64_tr_b16 v[166:167], v3 offset:49664\n\t" "v_add_u32 v12, 0x10000, v12\n\t"
    "v_mfma_f32_16x16x32_bf16 v[148:151], v[232:235], v[172:175], v[148:151]\n\t" "ds_read_b128 v[228:231], v1 offset:30720\n\t" "s_add_u32 m0, %[sl1], 65536\n\t"
    "v_mfma_f32_16x16x32_bf16 v[124:127], v[200:203], v[176:179], v[124:127]\n\t" "ds_read_b64_tr_b16 v[168:169], v3 offset:63488\n\t" "ds_read_b128 v[232:235], v1 offset:31744\n\t"
    "v_mfma_f32_16x16x32_bf16 v[120:123], v[196:199], v[176:179], v[120:123]\n\t" "ds_read_b64_tr_b16 v[170:171], v3 offset:64000\n\t" "global_load_lds_dwordx4 v12, %[so]\n\t"
    "v_mfma_f32_16x16x32_bf16 v[128:131], v[196:199], v[180:183], v[128:131]\n\t" "ds_read_b64_tr_b16 v[172:173], v7 offset:0\n\t" "v_lshlrev_b32 v18, 4, v13\n\t"
    "v_mfma_f32_16x16x32_bf16 v[132:135], v[200:203], v[180:183], v[132:135]\n\t" "ds_read_b64_tr_b16 v[174:175], v7 offset:512\n\t" "global_load_dword a64, v18, %[cd]\n\t"
    "v_mfma_f32_16x16x32_bf16 v[140:143], v[200:203], v[184:187], v[140:143]\n\t" "ds_read_b64_tr_b16 v[176:177], v3 offset:61440\n\t"
    "v_mfma_f32_16x16x32_bf16 v[136:139], v[196:199], v[184:187], v[136:139]\n\t" "ds_read_b64_tr_b16 v[178:179], v3 offset:61952\n\t" "s_waitcnt lgkmcnt(4)\n\t"
    "v_mfma_f32_16x16x32_bf16 v[80:83], v[156:159], v[188:191], v[80:83]\n\t" "ds_read_b64_tr_b16 v[180:181], v4 offset:49152\n\t" "v_add_u32 v12, 0x70000, v12\n\t"
    "v_mfma_f32_16x16x32_bf16 v[84:87], v[156:159], v[192:195], v[84:87]\n\t" "ds_read_b64_tr_b16 v[182:183], v4 offset:49664\n\t" "s_add_u32 m0, %[sl1], 94208\n\t"
    "v_mfma_f32_16x16x32_bf16 v[144:147], v[156:159], v[228:231], v[144:147]\n\t" "global_load_lds_dwordx4 v12, %[so]\n\t" "v_add_u32 v12, 0x10000, v12\n\t"
    "v_mfma_f32_16x16x32_bf16 v[116:119], v[192:195], v[160:163], v[116:119]\n\t" "ds_read_b64_tr_b16 v[184:185], v5 offset:49152\n\t" "v_mfma_f32_4x4x4_16b_bf16 v[204:207], v[224:225], a[248:249], 0\n\t"
    "v_mfma_f32_4x4x4_16b_bf16 v[208:211], v[226:227], a[250:251], 0\n\t" "v_mfma_f32_4x4x4_16b_bf16 v[212:215], v[224:225], a[252:253], 0\n\t"
    "v_mfma_f32_4x4x4_16b_bf16 v[216:219], v[226:227], a[254:255], 0\n\t" "v_mfma_f32_16x16x32_bf16 v[112:115], v[188:191], v[160:163], v[112:115]\n\t" "ds_read_b64_tr_b16 v[186:187], v5 offset:49664\n\t"
    "v_mfma_f32_16x16x32_bf16 v[148:151], v[228:231], v[160:163], v[148:151]\n\t" "v_mfma_f32_16x16x32_bf16 v[48:51], v[164:167], v[188:191], v[48:51]\n\t"
    "ds_read_b64_tr_b16 v[156:157], v6 offset:49152\n\t" "v_cvt_pk_bf16_f32 v196, v204, v205\n\t" "v_mfma_f32_16x16x32_bf16 v[52:55], v[164:167], v[192:195], v[52:55]\n\t"
    "ds_read_b64_tr_b16 v[158:159], v6 offset:49664\n\t" "v_cvt_pk_bf16_f32 v197, v206, v207\n\t" "v_mfma_f32_16x16x32_bf16 v[152:155], v[228:231], v[164:167], v[152:155]\n\t"
    "s_add_u32 m0, %[sl1], 98304\n\t" "v_mfma_f32_16x16x32_bf16 v[144:147], v[168:171], v[232:235], v[144:147]\n\t" "ds_read_b64_tr_b16 v[160:161], v4 offset:51200\n\t"
    "ds_read_b64_tr_b16 v[162:163], v4 offset:51712\n\t" "v_cvt_pk_bf16_f32 v198, v208, v209\n\t" "s_waitcnt lgkmcnt(4)\n\t" "v_mfma_f32_16x16x32_bf16 v[148:151], v[232:235], v[172:175], v[148:151]\n\t"
    "ds_read_b64_tr_b16 v[164:165], v5 offset:51200\n\t" "ds_read_b64_tr_b16 v[166:167], v5 offset:51712\n\t" "v_cvt_pk_bf16_f32 v199, v210, v211\n\t"
    "v_mfma_f32_16x16x32_bf16 v[152:155], v[232:235], v[176:179], v[152:155]\n\t" "ds_read_b64_tr_b16 v[168:169], v6 offset:51200\n\t" "ds_read_b64_tr_b16 v[170:171], v6 offset:51712\n\t"
    "v_mfma_f32_16x16x32_bf16 v[60:63], v[180:183], v[192:195], v[60:63]\n\t" "ds_read_b64_tr_b16 v[172:173], v4 offset:53248\n\t" "v_cvt_pk_bf16_f32 v200, v212, v213\n\t"
    "v_mfma_f32_16x16x32_bf16 v[56:59], v[180:183], v[188:191], v[56:59]\n\t" "ds_read_b64_tr_b16 v[174:175], v4 offset:53760\n\t" "v_cvt_pk_bf16_f32 v201, v214, v215\n\t"
    "v_mfma_f32_16x16x32_bf16 v[64:67], v[184:187], v[188:191], v[64:67]\n\t" "ds_read_b64_tr_b16 v[176:177], v5 offset:53248\n\t" "global_load_lds_dwordx4 v12, %[so]\n\t"
    "v_mfma_f32_16x16x32_bf16 v[68:71], v[184:187], v[192:195], v[68:71]\n\t" "ds_read_b64_tr_b16 v[178:179], v5 offset:53760\n\t" "v_cvt_pk_bf16_f32 v202, v216, v217\n\t" "s_waitcnt lgkmcnt(4)\n\t"
    "v_mfma_f32_16x16x32_bf16 v[76:79], v[156:159], v[192:195], v[76:79]\n\t" "ds_read_b64_tr_b16 v[180:181], v6 offset:53248\n\t" "v_cvt_pk_bf16_f32 v203, v218, v219\n\t"
    "v_mfma_f32_16x16x32_bf16 v[72:75], v[156:159], v[188:191], v[72:75]\n\t" "ds_read_b64_tr_b16 v[182:183], v6 offset:53760\n\t" "v_add_u32 v12, 0x10000, v12\n\t"
    "v_mfma_f32_16x16x32_bf16 v[88:91], v[160:163], v[188:191], v[88:91]\n\t" "ds_read_b64_tr_b16 v[184:185], v3 offset:63488\n\t" "s_add_u32 m0, %[sl1], 102400\n\t"
    "v_mfma_f32_16x16x32_bf16 v[92:95], v[160:163], v[192:195], v[92:95]\n\t" "ds_read_b64_tr_b16 v[186:187], v3 offset:64000\n\t" "v_and_b32 v242, 0xffff0000, v144\n\t"
    "v_mfma_f32_16x16x32_bf16 v[100:103], v[164:167], v[192:195], v[100:103]\n\t" "ds_read_b64_tr_b16 v[156:157], v4 offset:63488\n\t" "global_load_lds_dwordx4 v12, %[so]\n\t"
    "v_mfma_f32_16x16x32_bf16 v[96:99], v[164:167], v[188:191], v[96:99]\n\t" "ds_read_b64_tr_b16 v[158:159], v4 offset:64000\n\t" "v_sub_f32 v18, v144, v242\n\t"
    "v_mfma_f32_16x16x32_bf16 v[104:107], v[168:171], v[188:191], v[104:107]\n\t" "ds_read_b64_tr_b16 v[160:161], v5 offset:63488\n\t" "v_cvt_pk_bf16_f32 v242, v242, v18\n\t"
    "v_mfma_f32_16x16x32_bf16 v[108:111], v[168:171], v[192:195], v[108:111]\n\t" "ds_read_b64_tr_b16 v[162:163], v5 offset:64000\n\t" "s_waitcnt lgkmcnt(4)\n\t"
    "v_mfma_f32_16x16x32_bf16 v[124:127], v[192:195], v[172:175], v[124:127]\n\t" "ds_read_b64_tr_b16 v[164:165], v6 offset:63488\n\t" "v_and_b32 v243, 0xffff0000, v145\n\t"
    "v_mfma_f32_16x16x32_bf16 v[120:123], v[188:191], v[172:175], v[120:123]\n\t" "ds_read_b64_tr_b16 v[166:167], v6 offset:64000\n\t" "v_sub_f32 v18, v145, v243\n\t"
    "v_mfma_f32_16x16x32_bf16 v[128:131], v[188:191], v[176:179], v[128:131]\n\t" "ds_read_b64_tr_b16 v[168:169], v7 offset:0\n\t" "v_cvt_pk_bf16_f32 v243, v243, v18\n\t"
    "v_mfma_f32_16x16x32_bf16 v[132:135], v[192:195], v[176:179], v[132:135]\n\t" "ds_read_b64_tr_b16 v[170:171], v7 offset:512\n\t" "ds_write2_b32 v34, v242, v243 offset0:0 offset1:2\n\t"
    "v_mfma_f32_16x16x32_bf16 v[140:143], v[192:195], v[180:183], v[140:143]\n\t" "ds_read_b64_tr_b16 v[172:173], v8 offset:0\n\t" "v_and_b32 v244, 0xffff0000, v146\n\t"
    "v_mfma_f32_16x16x32_bf16 v[136:139], v[188:191], v[180:183], v[136:139]\n\t" "ds_read_b64_tr_b16 v[174:175], v8 offset:512\n\t" "v_sub_f32_e64 v18, v146, v244\n\t"
    "v_mfma_f32_16x16x32_bf16 v[80:83], v[184:187], v[196:199], v[80:83]\n\t" "ds_read_b64_tr_b16 v[176:177], v9 offset:0\n\t" "v_cvt_pk_bf16_f32 v244, v244, v18\n\t"
    "v_mfma_f32_16x16x32_bf16 v[84:87], v[184:187], v[200:203], v[84:87]\n\t" "ds_read_b64_tr_b16 v[178:179], v9 offset:512\n\t" "s_waitcnt lgkmcnt(5)\n\t"
    "v_mfma_f32_16x16x32_bf16 v[92:95], v[156:159], v[200:203], v[92:95]\n\t" "ds_read_b64_tr_b16 v[180:181], v10 offset:0\n\t" "v_and_b32 v245, 0xffff0000, v147\n\t"
    "v_mfma_f32_16x16x32_bf16 v[88:91], v[156:159], v[196:199], v[88:91]\n\t" "ds_read_b64_tr_b16 v[182:183], v10 offset:512\n\t" "v_sub_f32 v18, v147, v245\n\t"
    "v_mfma_f32_16x16x32_bf16 v[96:99], v[160:163], v[196:199], v[96:99]\n\t" "ds_read_b64_tr_b16 v[184:185], v3 offset:61440\n\t" "v_cvt_pk_bf16_f32 v245, v245, v18\n\t"
    "v_mfma_f32_16x16x32_bf16 v[100:103], v[160:163], v[200:203], v[100:103]\n\t" "ds_read_b64_tr_b16 v[186:187], v3 offset:61952\n\t" "ds_write2_b32 v34, v244, v245 offset0:4 offset1:6\n\t"
    "v_mfma_f32_16x16x32_bf16 v[108:111], v[164:167], v[200:203], v[108:111]\n\t" "ds_read_b64_tr_b16 v[156:157], v4 offset:61440\n\t" "v_and_b32 v238, 0xffff0000, v148\n\t"
    "v_mfma_f32_16x16x32_bf16 v[104:107], v[164:167], v[196:199], v[104:107]\n\t" "ds_read_b64_tr_b16 v[158:159], v4 offset:61952\n\t" "v_sub_f32_e64 v18, v148, v238\n\t"
    "v_mfma_f32_16x16x32_bf16 v[112:115], v[196:199], v[168:171], v[112:115]\n\t" "ds_read_b64_tr_b16 v[160:161], v5 offset:61440\n\t" "v_cvt_pk_bf16_f32 v238, v238, v18\n\t"
    "v_mfma_f32_16x16x32_bf16 v[116:119], v[200:203], v[168:171], v[116:119]\n\t" "ds_read_b64_tr_b16 v[162:163], v5 offset:61952\n\t" "s_waitcnt lgkmcnt(5)\n\t"
    "v_mfma_f32_16x16x32_bf16 v[124:127], v[200:203], v[172:175], v[124:127]\n\t" "ds_read_b64_tr_b16 v[164:165], v6 offset:61440\n\t" "v_and_b32 v239, 0xffff0000, v149\n\t"
    "v_mfma_f32_16x16x32_bf16 v[120:123], v[196:199], v[172:175], v[120:123]\n\t" "ds_read_b64_tr_b16 v[166:167], v6 offset:61952\n\t" "v_sub_f32 v18, v149, v239\n\t"
    "v_mfma_f32_16x16x32_bf16 v[128:131], v[196:199], v[176:179], v[128:131]\n\t" "v_cvt_pk_bf16_f32 v239, v239, v18\n\t" "v_and_b32 v240, 0xffff0000, v152\n\t"
    "v_mfma_f32_16x16x32_bf16 v[132:135], v[200:203], v[176:179], v[132:135]\n\t" "v_sub_f32_e64 v18, v152, v240\n\t" "v_cvt_pk_bf16_f32 v240, v240, v18\n\t"
    "v_mfma_f32_16x16x32_bf16 v[140:143], v[200:203], v[180:183], v[140:143]\n\t" "v_and_b32 v241, 0xffff0000, v153\n\t" "v_sub_f32_e64 v18, v153, v241\n\t"
    "v_mfma_f32_16x16x32_bf16 v[136:139], v[196:199], v[180:183], v[136:139]\n\t" "v_cvt_pk_bf16_f32 v241, v241, v18\n\t" "v_mfma_f32_16x16x32_bf16 v[48:51], v[184:187], v[196:199], v[48:51]\n\t"
    "ds_write2st64_b64 v35, v[238:239], v[240:241] offset0:2 offset1:0\n\t" "v_mfma_f32_16x16x32_bf16 v[52:55], v[184:187], v[200:203], v[52:55]\n\t" "s_waitcnt lgkmcnt(1)\n\t"
    "v_mfma_f32_16x16x32_bf16 v[60:63], v[156:159], v[200:203], v[60:63]\n\t" "s_waitcnt lgkmcnt(0)\n\t" "s_barrier\n\t" "v_mfma_f32_16x16x32_bf16 v[56:59], v[156:159], v[196:199], v[56:59]\n\t"
    "ds_read2st64_b64 v[188:191], v36 offset0:1 offset1:2\n\t" "ds_read2st64_b64 v[192:195], v37 offset0:1 offset1:2\n\t" "v_mfma_f32_16x16x32_bf16 v[64:67], v[160:163], v[196:199], v[64:67]\n\t"
    "ds_read2st64_b64 v[220:223], v38 offset0:1 offset1:2\n\t" "ds_read2st64_b64 v[224:227], v39 offset0:1 offset1:2\n\t" "v_mfma_f32_16x16x32_bf16 v[68:71], v[160:163], v[200:203], v[68:71]\n\t"
    "ds_read_b64 v[228:229], v36\n\t" "ds_read_b64 v[230:231], v37\n\t" "v_mfma_f32_16x16x32_bf16 v[76:79], v[164:167], v[200:203], v[76:79]\n\t" "ds_read_b64 v[232:233], v38\n\t"
    "ds_read_b64 v[234:235], v39\n\t" "v_mfma_f32_16x16x32_bf16 v[72:75], v[164:167], v[196:199], v[72:75]\n\t" "s_waitcnt lgkmcnt(4)\n\t"
    "v_mfma_f32_16x16x16_bf16 v[84:87], v[188:189], v[248:249], v[84:87]\n\t" "s_nop 1\n\t" "v_mfma_f32_16x16x16_bf16 v[92:95], v[192:193], v[248:249], v[92:95]\n\t" "s_nop 1\n\t"
    "v_mfma_f32_16x16x16_bf16 v[100:103], v[220:221], v[248:249], v[100:103]\n\t" "s_nop 1\n\t" "v_mfma_f32_16x16x16_bf16 v[108:111], v[224:225], v[248:249], v[108:111]\n\t"
    "v_cvt_pk_bf16_f32 v164, v84, v85\n\t" "v_cvt_pk_bf16_f32 v165, v86, v87\n\t" "v_mfma_f32_16x16x16_bf16 v[144:147], v[188:189], v[250:251], v[144:147]\n\t" "v_cvt_pk_bf16_f32 v166, v92, v93\n\t"
    "v_cvt_pk_bf16_f32 v167, v94, v95\n\t" "v_mfma_f32_16x16x16_bf16 v[116:119], v[248:249], v[190:191], v[116:119]\n\t" "ds_write2_b64 v40, v[164:165], v[166:167] offset0:1 offset1:0\n\t"
    "v_cvt_pk_bf16_f32 v168, v100, v101\n\t" "v_mfma_f32_16x16x16_bf16 v[124:127], v[248:249], v[194:195], v[124:127]\n\t" "v_cvt_pk_bf16_f32 v169, v102, v103\n\t" "v_cvt_pk_bf16_f32 v170, v108, v109\n\t"
    "v_mfma_f32_16x16x16_bf16 v[132:135], v[248:249], v[222:223], v[132:135]\n\t" "v_cvt_pk_bf16_f32 v171, v110, v111\n\t" "v_cvt_pk_bf16_f32 v144, v144, v145\n\t"
    "v_mfma_f32_16x16x16_bf16 v[140:143], v[248:249], v[226:227], v[140:143]\n\t" "ds_write2_b64 v41, v[168:169], v[170:171] offset0:1 offset1:0\n\t" "v_cvt_pk_bf16_f32 v145, v146, v147\n\t"
    "v_mfma_f32_16x16x16_bf16 v[148:151], v[250:251], v[190:191], v[148:151]\n\t" "v_cvt_pk_bf16_f32 v200, v116, v117\n\t" "v_cvt_pk_bf16_f32 v201, v118, v119\n\t"
    "v_mfma_f32_16x16x16_bf16 v[80:83], v[188:189], v[246:247], v[80:83]\n\t" "v_cvt_pk_bf16_f32 v206, v124, v125\n\t" "v_cvt_pk_bf16_f32 v207, v126, v127\n\t"
    "v_mfma_f32_16x16x16_bf16 v[88:91], v[192:193], v[246:247], v[88:91]\n\t" "ds_write2_b64 v40, v[200:201], v[206:207] offset0:129 offset1:128\n\t" "v_cvt_pk_bf16_f32 v212, v132, v133\n\t"
    "v_mfma_f32_16x16x16_bf16 v[96:99], v[220:221], v[246:247], v[96:99]\n\t" "v_cvt_pk_bf16_f32 v213, v134, v135\n\t" "v_cvt_pk_bf16_f32 v218, v140, v141\n\t"
    "v_mfma_f32_16x16x16_bf16 v[104:107], v[224:225], v[246:247], v[104:107]\n\t" "v_cvt_pk_bf16_f32 v219, v142, v143\n\t" "v_cvt_pk_bf16_f32 v148, v148, v149\n\t" "s_waitcnt lgkmcnt(3)\n\t"
    "v_mfma_f32_16x16x16_bf16 v[52:55], v[228:229], v[248:249], v[52:55]\n\t" "ds_write2_b64 v41, v[212:213], v[218:219] offset0:129 offset1:128\n\t" "v_cvt_pk_bf16_f32 v149, v150, v151\n\t"
    "v_mfma_f32_16x16x16_bf16 v[60:63], v[230:231], v[248:249], v[60:63]\n\t" "ds_write2_b64 v42, v[144:145], v[148:149] offset0:0 offset1:128\n\t" "v_cvt_pk_bf16_f32 v156, v80, v81\n\t"
    "v_mfma_f32_16x16x16_bf16 v[68:71], v[232:233], v[248:249], v[68:71]\n\t" "v_cvt_pk_bf16_f32 v157, v82, v83\n\t" "v_cvt_pk_bf16_f32 v158, v88, v89\n\t"
    "v_mfma_f32_16x16x16_bf16 v[76:79], v[234:235], v[248:249], v[76:79]\n\t" "v_cvt_pk_bf16_f32 v159, v90, v91\n\t" "v_cvt_pk_bf16_f32 v160, v96, v97\n\t"
    "v_mfma_f32_16x16x16_bf16 v[48:51], v[228:229], v[246:247], v[48:51]\n\t" "v_cvt_pk_bf16_f32 v161, v98, v99\n\t" "v_cvt_pk_bf16_f32 v162, v104, v105\n\t"
    "v_mfma_f32_16x16x16_bf16 v[56:59], v[230:231], v[246:247], v[56:59]\n\t" "v_cvt_pk_bf16_f32 v163, v106, v107\n\t" "v_cvt_pk_bf16_f32 v180, v52, v53\n\t"
    "v_mfma_f32_16x16x16_bf16 v[64:67], v[232:233], v[246:247], v[64:67]\n\t" "s_waitcnt lgkmcnt(0)\n\t" "s_barrier\n\t" "v_mfma_f32_16x16x16_bf16 v[72:75], v[234:235], v[246:247], v[72:75]\n\t"
    "ds_read_b128 v[144:147], v43 offset:0\n\t" "ds_read_b128 v[148:151], v43 offset:2048\n\t" "v_mfma_f32_16x16x16_bf16 v[112:115], v[246:247], v[190:191], v[112:115]\n\t"
    "ds_read_b64 v[196:197], v43 offset:1024\n\t" "ds_read_b64 v[202:203], v43 offset:1032\n\t" "v_mfma_f32_16x16x16_bf16 v[120:123], v[246:247], v[194:195], v[120:123]\n\t"
    "ds_read_b64 v[208:209], v43 offset:3072\n\t" "ds_read_b64 v[214:215], v43 offset:3080\n\t" "v_mfma_f32_16x16x16_bf16 v[128:131], v[246:247], v[222:223], v[128:131]\n\t"
    "v_cvt_pk_bf16_f32 v181, v54, v55\n\t" "v_cvt_pk_bf16_f32 v182, v60, v61\n\t" "v_mfma_f32_16x16x16_bf16 v[136:139], v[246:247], v[226:227], v[136:139]\n\t" "v_cvt_pk_bf16_f32 v183, v62, v63\n\t"
    "v_cvt_pk_bf16_f32 v184, v68, v69\n\t" "s_nop 1\n\t" "v_mfma_f32_16x16x32_bf16 v[80:83], v[156:159], v[180:183], 0\n\t" "v_cvt_pk_bf16_f32 v185, v70, v71\n\t" "v_cvt_pk_bf16_f32 v186, v76, v77\n\t"
    "v_mfma_f32_16x16x32_bf16 v[84:87], v[164:167], v[180:183], 0\n\t" "v_cvt_pk_bf16_f32 v187, v78, v79\n\t" "v_cvt_pk_bf16_f32 v172, v48, v49\n\t" "s_nop 1\n\t"
    "v_mfma_f32_16x16x32_bf16 v[88:91], v[160:163], v[184:187], 0\n\t" "v_cvt_pk_bf16_f32 v173, v50, v51\n\t" "v_cvt_pk_bf16_f32 v174, v56, v57\n\t"
    "v_mfma_f32_16x16x32_bf16 v[92:95], v[168:171], v[184:187], 0\n\t" "v_cvt_pk_bf16_f32 v175, v58, v59\n\t" "v_cvt_pk_bf16_f32 v176, v64, v65\n\t" "s_nop 1\n\t" "s_waitcnt lgkmcnt(4)\n\t"
    "v_mfma_f32_16x16x32_bf16 v[96:99], v[144:147], v[172:175], 0\n\t" "v_cvt_pk_bf16_f32 v177, v66, v67\n\t" "v_cvt_pk_bf16_f32 v178, v72, v73\n\t"
    "v_mfma_f32_16x16x32_bf16 v[100:103], v[156:159], v[172:175], 0\n\t" "v_cvt_pk_bf16_f32 v179, v74, v75\n\t" "v_cvt_pk_bf16_f32 v198, v112, v113\n\t" "s_nop 1\n\t"
    "v_mfma_f32_16x16x32_bf16 v[104:107], v[148:151], v[176:179], 0\n\t" "v_cvt_pk_bf16_f32 v199, v114, v115\n\t" "v_cvt_pk_bf16_f32 v204, v120, v121\n\t"
    "v_mfma_f32_16x16x32_bf16 v[108:111], v[160:163], v[176:179], 0\n\t" "v_cvt_pk_bf16_f32 v205, v122, v123\n\t" "v_cvt_pk_bf16_f32 v210, v128, v129\n\t" "v_cvt_pk_bf16_f32 v211, v130, v131\n\t"
    "v_cvt_pk_bf16_f32 v216, v136, v137\n\t" "v_cvt_pk_bf16_f32 v217, v138, v139\n\t" "v_cndmask_b32 v48, v80, v81, s[50:51]\n\t" "v_cndmask_b32 v64, v84, v85, s[50:51]\n\t"
    "v_cndmask_b32 v49, v82, v83, s[50:51]\n\t" "v_cndmask_b32 v65, v86, v87, s[50:51]\n\t" "v_cndmask_b32 v48, v48, v64, s[52:53]\n\t" "v_cndmask_b32 v49, v49, v65, s[54:55]\n\t"
    "v_pk_mul_f32 v[48:49], v[48:49], v[30:31]\n\t" "v_max_f32 v56, v48, v49\n\t" "v_cndmask_b32 v50, v88, v89, s[50:51]\n\t" "v_cndmask_b32 v64, v92, v93, s[50:51]\n\t"
    "v_cndmask_b32 v51, v90, v91, s[50:51]\n\t" "v_cndmask_b32 v65, v94, v95, s[50:51]\n\t" "v_cndmask_b32 v50, v50, v64, s[52:53]\n\t" "v_cndmask_b32 v51, v51, v65, s[54:55]\n\t"
    "v_pk_mul_f32 v[50:51], v[50:51], v[30:31]\n\t" "v_max_f32_e64 v57, v50, v51\n\t" "v_cndmask_b32 v52, v96, v97, s[50:51]\n\t" "v_cndmask_b32 v64, v100, v101, s[50:51]\n\t"
    "v_cndmask_b32 v53, v98, v99, s[50:51]\n\t" "v_cndmask_b32 v65, v102, v103, s[50:51]\n\t" "v_cndmask_b32 v52, v52, v64, s[52:53]\n\t" "v_cndmask_b32 v53, v53, v65, s[54:55]\n\t"
    "v_pk_fma_f32 v[52:53], v[52:53], v[28:29], v[32:33]\n\t" "v_max_f32_e64 v58, v52, v53\n\t" "v_cndmask_b32 v54, v104, v105, s[50:51]\n\t" "v_cndmask_b32 v64, v108, v109, s[50:51]\n\t"
    "v_cndmask_b32 v55, v106, v107, s[50:51]\n\t" "v_cndmask_b32 v65, v110, v111, s[50:51]\n\t" "v_cndmask_b32 v54, v54, v64, s[52:53]\n\t" "v_cndmask_b32 v55, v55, v65, s[54:55]\n\t"
    "v_pk_fma_f32 v[54:55], v[54:55], v[28:29], v[32:33]\n\t" "v_max_f32 v59, v54, v55\n\t" "v_permlane16_swap_b32 v56, v57\n\t" "s_nop 0\n\t" "v_permlane16_swap_b32 v58, v59\n\t"
    "v_max_f32 v56, v56, v57\n\t" "v_max_f32 v58, v58, v59\n\t" "s_nop 0\n\t" "s_nop 0\n\t" "v_permlane32_swap_b32 v56, v58\n\t" "v_max_f32 v56, v56, v58\n\t" "v_mov_b32 v58, v56\n\t" "s_nop 0\n\t"
    "s_nop 0\n\t" "v_permlane32_swap_b32 v56, v58\n\t" "v_mov_b32 v57, v56\n\t" "v_mov_b32_e64 v59, v58\n\t" "s_nop 0\n\t" "v_permlane16_swap_b32 v56, v57\n\t" "v_permlane16_swap_b32 v58, v59\n\t"
    "v_pk_add_f32 v[48:49], v[48:49], v[56:57] op_sel:[0,0] op_sel_hi:[1,0] neg_lo:[0,1] neg_hi:[0,1]\n\t"
    "v_pk_add_f32 v[50:51], v[50:51], v[56:57] op_sel:[0,1] op_sel_hi:[1,1] neg_lo:[0,1] neg_hi:[0,1]\n\t"
    "v_pk_add_f32 v[52:53], v[52:53], v[58:59] op_sel:[0,0] op_sel_hi:[1,0] neg_lo:[0,1] neg_hi:[0,1]\n\t"
    "v_pk_add_f32 v[54:55], v[54:55], v[58:59] op_sel:[0,1] op_sel_hi:[1,1] neg_lo:[0,1] neg_hi:[0,1]\n\t" "v_exp_f32 v48, v48\n\t" "v_exp_f32 v49, v49\n\t" "v_exp_f32 v50, v50\n\t"
    "v_exp_f32 v51, v51\n\t" "v_exp_f32 v52, v52\n\t" "v_exp_f32 v53, v53\n\t" "v_exp_f32 v54, v54\n\t" "v_exp_f32 v55, v55\n\t" "v_add_f32 v56, v48, v49\n\t" "v_add_f32 v57, v50, v51\n\t"
    "v_add_f32 v58, v52, v53\n\t" "v_add_f32 v59, v54, v55\n\t" "v_permlane16_swap_b32 v56, v57\n\t" "v_mul_f32 v48, v48, v26\n\t" "v_permlane16_swap_b32 v58, v59\n\t" "v_add_f32 v56, v56, v57\n\t"
    "v_add_f32 v58, v58, v59\n\t" "v_mul_f32 v49, v49, v27\n\t" "v_mul_f32 v50, v50, v26\n\t" "v_permlane32_swap_b32 v56, v58\n\t" "v_add_f32 v56, v56, v58\n\t" "v_rcp_f32 v56, v56\n\t"
    "v_mul_f32 v51, v51, v27\n\t" "v_mov_b32 v58, v56\n\t" "v_mul_f32 v52, v52, v24\n\t" "v_mul_f32 v53, v53, v25\n\t" "v_permlane32_swap_b32 v56, v58\n\t" "v_mov_b32 v57, v56\n\t"
    "v_mov_b32 v59, v58\n\t" "v_mul_f32 v54, v54, v24\n\t" "v_permlane16_swap_b32 v56, v57\n\t" "v_permlane16_swap_b32 v58, v59\n\t" "v_mul_f32_e64 v55, v55, v25\n\t"
    "v_pk_mul_f32 v[48:49], v[48:49], v[56:57] op_sel:[0,0] op_sel_hi:[1,0]\n\t" "v_pk_mul_f32 v[50:51], v[50:51], v[56:57] op_sel:[0,1] op_sel_hi:[1,1]\n\t"
    "v_pk_mul_f32 v[52:53], v[52:53], v[58:59] op_sel:[0,0] op_sel_hi:[1,0]\n\t" "v_pk_mul_f32 v[54:55], v[54:55], v[58:59] op_sel:[0,1] op_sel_hi:[1,1]\n\t" "s_waitcnt lgkmcnt(0)\n\t"
    "v_cvt_pk_bf16_f32 v48, v48, v49\n\t" "v_perm_b32 v112, v48, v48, v20\n\t" "v_perm_b32 v113, v48, v48, v21\n\t" "v_perm_b32 v114, v48, v48, v22\n\t" "v_perm_b32 v115, v48, v48, v23\n\t"
    "v_cvt_pk_bf16_f32 v50, v50, v51\n\t" "v_perm_b32 v116, v50, v50, v20\n\t" "v_perm_b32 v117, v50, v50, v21\n\t" "v_perm_b32 v118, v50, v50, v22\n\t" "v_perm_b32 v119, v50, v50, v23\n\t"
    "v_mfma_f32_16x16x32_bf16 v[80:83], v[198:201], v[112:115], 0\n\t" "v_mfma_f32_16x16x32_bf16 v[84:87], v[204:207], v[112:115], 0\n\t" "v_cvt_pk_bf16_f32 v52, v52, v53\n\t"
    "v_perm_b32 v120, v52, v52, v20\n\t" "v_perm_b32 v121, v52, v52, v21\n\t" "v_perm_b32 v122, v52, v52, v22\n\t" "v_perm_b32 v123, v52, v52, v23\n\t"
    "v_mfma_f32_16x16x32_bf16 v[88:91], v[210:213], v[116:119], 0\n\t" "v_mfma_f32_16x16x32_bf16 v[92:95], v[216:219], v[116:119], 0\n\t" "v_cvt_pk_bf16_f32 v54, v54, v55\n\t"
    "v_perm_b32 v124, v54, v54, v20\n\t" "v_perm_b32 v125, v54, v54, v21\n\t" "v_perm_b32 v126, v54, v54, v22\n\t" "v_perm_b32 v127, v54, v54, v23\n\t"
    "v_mfma_f32_16x16x32_bf16 v[96:99], v[196:199], v[120:123], 0\n\t" "v_mfma_f32_16x16x32_bf16 v[100:103], v[202:205], v[120:123], 0\n\t" "v_cvt_pk_bf16_f32 v128, v80, v81\n\t"
    "v_cvt_pk_bf16_f32 v129, v82, v83\n\t" "v_cvt_pk_bf16_f32 v130, v84, v85\n\t" "v_cvt_pk_bf16_f32 v131, v86, v87\n\t" "global_store_dwordx4 v44, v[128:131], %[ws] offset:1024 sc1\n\t"
    "v_mfma_f32_16x16x32_bf16 v[104:107], v[208:211], v[124:127], 0\n\t" "v_mfma_f32_16x16x32_bf16 v[108:111], v[214:217], v[124:127], 0\n\t" "v_cvt_pk_bf16_f32 v132, v88, v89\n\t"
    "v_cvt_pk_bf16_f32 v133, v90, v91\n\t" "v_cvt_pk_bf16_f32 v134, v92, v93\n\t" "v_cvt_pk_bf16_f32 v135, v94, v95\n\t" "global_store_dwordx4 v45, v[132:135], %[ws] offset:1024 sc1\n\t"
    "v_cvt_pk_bf16_f32 v136, v96, v97\n\t" "v_cvt_pk_bf16_f32 v137, v98, v99\n\t" "v_cvt_pk_bf16_f32 v138, v100, v101\n\t" "v_cvt_pk_bf16_f32 v139, v102, v103\n\t"
    "global_store_dwordx4 v44, v[136:139], %[ws] offset:0 sc1\n\t" "v_cvt_pk_bf16_f32 v140, v104, v105\n\t" "v_cvt_pk_bf16_f32 v141, v106, v107\n\t" "v_cvt_pk_bf16_f32 v142, v108, v109\n\t"
    "v_cvt_pk_bf16_f32 v143, v110, v111\n\t" "global_store_dwordx4 v45, v[140:143], %[ws] offset:0 sc1\n\t" "v_perm_b32 v156, v252, v252, v14\n\t" "v_perm_b32 v157, v252, v252, v17\n\t"
    "v_perm_b32 v158, v252, v252, v15\n\t" "v_perm_b32 v159, v252, v252, v19\n\t" "v_mfma_f32_4x4x4_16b_bf16 v[176:179], v[156:157], a[32:33], 0\n\t"
    "v_mfma_f32_4x4x4_16b_bf16 v[180:183], v[156:157], a[34:35], 0\n\t" "v_perm_b32 v160, v253, v253, v14\n\t" "v_perm_b32 v161, v253, v253, v17\n\t"
    "v_mfma_f32_4x4x4_16b_bf16 v[176:179], v[158:159], a[36:37], v[176:179]\n\t" "v_mfma_f32_4x4x4_16b_bf16 v[180:183], v[158:159], a[38:39], v[180:183]\n\t" "v_perm_b32 v162, v253, v253, v15\n\t"
    "v_perm_b32 v163, v253, v253, v19\n\t" "v_mfma_f32_4x4x4_16b_bf16 v[176:179], v[160:161], a[40:41], v[176:179]\n\t" "v_mfma_f32_4x4x4_16b_bf16 v[180:183], v[160:161], a[42:43], v[180:183]\n\t"
    "v_perm_b32 v164, v254, v254, v14\n\t" "v_perm_b32 v165, v254, v254, v17\n\t" "v_mfma_f32_4x4x4_16b_bf16 v[176:179], v[162:163], a[44:45], v[176:179]\n\t"
    "v_mfma_f32_4x4x4_16b_bf16 v[180:183], v[162:163], a[46:47], v[180:183]\n\t" "v_perm_b32 v166, v254, v254, v15\n\t" "v_perm_b32 v167, v254, v254, v19\n\t"
    "v_mfma_f32_4x4x4_16b_bf16 v[176:179], v[164:165], a[48:49], v[176:179]\n\t" "v_mfma_f32_4x4x4_16b_bf16 v[180:183], v[164:165], a[50:51], v[180:183]\n\t" "v_perm_b32 v168, v255, v255, v14\n\t"
    "v_perm_b32 v169, v255, v255, v17\n\t" "v_mfma_f32_4x4x4_16b_bf16 v[176:179], v[166:167], a[52:53], v[176:179]\n\t" "v_mfma_f32_4x4x4_16b_bf16 v[180:183], v[166:167], a[54:55], v[180:183]\n\t"
    "v_perm_b32 v170, v255, v255, v15\n\t" "v_perm_b32 v171, v255, v255, v19\n\t" "v_mfma_f32_4x4x4_16b_bf16 v[176:179], v[168:169], a[56:57], v[176:179]\n\t"
    "v_mfma_f32_4x4x4_16b_bf16 v[180:183], v[168:169], a[58:59], v[180:183]\n\t" "s_nop 0\n\t" "v_mfma_f32_4x4x4_16b_bf16 v[176:179], v[170:171], a[60:61], v[176:179]\n\t"
    "v_mfma_f32_4x4x4_16b_bf16 v[180:183], v[170:171], a[62:63], v[180:183]\n\t" "s_nop 0\n\t" "s_nop 3\n\t" "v_permlane32_swap_b32 v176, v180\n\t" "v_permlane32_swap_b32 v177, v181\n\t"
    "v_permlane32_swap_b32 v178, v182\n\t" "v_permlane32_swap_b32 v179, v183\n\t" "v_add_f32 v176, v176, v180\n\t" "v_add_f32 v178, v178, v182\n\t" "v_add_f32 v177, v177, v181\n\t"
    "v_add_f32 v179, v179, v183\n\t" "v_permlane16_swap_b32 v176, v178\n\t" "s_nop 0\n\t" "v_permlane16_swap_b32 v177, v179\n\t" "v_add_f32 v176, v176, v178\n\t" "v_add_f32 v177, v177, v179\n\t"
    "s_nop 0\n\t" "v_add_f32_dpp v176, v176, v176 row_ror:8 row_mask:0xf bank_mask:0xf\n\t" "v_add_f32_dpp v177, v177, v177 row_ror:8 row_mask:0xf bank_mask:0xf\n\t" "ds_write_b64 v11, v[176:177]\n\t"
    "v_mov_b64 v[178:179], s[62:63]\n\t" "v_mov_b32 v180, %[sf]\n\t" "s_waitcnt vmcnt(0) lgkmcnt(0)\n\t" "s_barrier\n\t" "s_cmp_eq_u32 %[w128], 0\n\t" "s_cselect_b64 exec, 1, 0\n\t"
    "global_store_dwordx2 v180, v[178:179], %[ws] sc1\n\t" "s_mov_b64 exec, -1\n\t" "v_add_u32 v12, 0xffe80000, v12\n\t" "s_add_u32 m0, %[sl1], 4096\n\t" "s_nop 0\n\t"
    "global_load_lds_dwordx4 v12, %[so]\n\t" "v_add_u32 v12, 0x100000, v12\n\t" "s_add_u32 m0, %[sl1], 69632\n\t" "s_nop 0\n\t" "global_load_lds_dwordx4 v12, %[so]\n\t" "v_add_u32 v12, 0x10000, v12\n\t"
    "s_add_u32 m0, %[sl1], 73728\n\t" "s_nop 0\n\t" "global_load_lds_dwordx4 v12, %[so]\n\t" "v_add_u32 v12, 0x10000, v12\n\t" "s_add_u32 m0, %[sl1], 77824\n\t" "s_nop 0\n\t"
    "global_load_lds_dwordx4 v12, %[so]\n\t" "v_add_u32 v12, 0x10000, v12\n\t" "s_add_u32 m0, %[sl1], 81920\n\t" "s_nop 0\n\t" "global_load_lds_dwordx4 v12, %[so]\n\t" "v_add_u32 v12, 0x10000, v12\n\t"
    "s_add_u32 m0, %[sl1], 86016\n\t" "s_nop 0\n\t" "global_load_lds_dwordx4 v12, %[so]\n\t" "v_add_u32 v12, 0x10000, v12\n\t" "s_add_u32 m0, %[sl1], 90112\n\t" "s_nop 0\n\t"
    "global_load_lds_dwordx4 v12, %[so]\n\t" "v_add_u32 v12, 0x40000, v12\n\t" "s_add_u32 m0, %[sl1], 106496\n\t" "s_nop 0\n\t" "global_load_lds_dwordx4 v12, %[so]\n\t" "s_branch .Lend_%=\n\t"
    ".Lslow_%=:\n\t" "s_sleep 2\n\t" "s_mov_b64 s[40:41], %[ws]\n\t" "global_load_dwordx4 v[80:83], v230, s[40:41] offset:-4096 sc1\n\t"
    "global_load_dwordx4 v[96:99], v230, s[40:41] offset:0 sc1\n\t" "s_add_u32 s40, s40, 8192\n\t" "s_addc_u32 s41, s41, 0\n\t" "global_load_dwordx4 v[84:87], v230, s[40:41] offset:-4096 sc1\n\t"
    "global_load_dwordx4 v[100:103], v230, s[40:41] offset:0 sc1\n\t" "s_add_u32 s40, s40, 8192\n\t" "s_addc_u32 s41, s41, 0\n\t" "global_load_dwordx4 v[88:91], v230, s[40:41] offset:-4096 sc1\n\t"
    "global_load_dwordx4 v[104:107], v230, s[40:41] offset:0 sc1\n\t" "s_add_u32 s40, s40, 8192\n\t" "s_addc_u32 s41, s41, 0\n\t" "global_load_dwordx4 v[92:95], v230, s[40:41] offset:-4096 sc1\n\t"
    "global_load_dwordx4 v[108:111], v230, s[40:41] offset:0 sc1\n\t" "s_waitcnt vmcnt(0)\n\t" "v_xad_u32 v144, v80, v81, s62\n\t" "v_xad_u32 v145, v84, v85, s62\n\t" "v_xad_u32 v146, v88, v89, s62\n\t"
    "v_xad_u32 v147, v92, v93, s62\n\t" "v_xad_u32 v148, v96, v97, s62\n\t" "v_xad_u32 v149, v100, v101, s62\n\t" "v_xad_u32 v150, v104, v105, s62\n\t" "v_xad_u32 v151, v108, v109, s62\n\t"
    "v_xad_u32 v144, v82, v81, v144\n\t" "v_xad_u32 v145, v86, v85, v145\n\t" "v_xad_u32 v146, v90, v89, v146\n\t" "v_xad_u32 v147, v94, v93, v147\n\t" "v_xad_u32 v148, v98, v97, v148\n\t"
    "v_xad_u32 v149, v102, v101, v149\n\t" "v_xad_u32 v150, v106, v105, v150\n\t" "v_xad_u32 v151, v110, v109, v151\n\t" "v_xor_b32_e64 v140, v144, v83\n\t"
    "v_bitop3_b32 v140, v140, v145, v87 bitop3:0xf6\n\t" "v_bitop3_b32 v140, v140, v146, v91 bitop3:0xf6\n\t" "v_bitop3_b32 v140, v140, v147, v95 bitop3:0xf6\n\t"
    "v_bitop3_b32 v140, v140, v148, v99 bitop3:0xf6\n\t" "v_bitop3_b32 v140, v140, v149, v103 bitop3:0xf6\n\t" "v_bitop3_b32 v140, v140, v150, v107 bitop3:0xf6\n\t"
    "v_bitop3_b32 v140, v140, v151, v111 bitop3:0xf6\n\t" "v_cmp_ne_u32_e64 vcc, 0, v140\n\t" "v_perm_b32 v83, v80, v81, s57\n\t" "v_perm_b32 v87, v84, v85, s57\n\t" "v_perm_b32 v91, v88, v89, s57\n\t"
    "v_perm_b32 v95, v92, v93, s57\n\t" "v_perm_b32 v99, v96, v97, s57\n\t" "v_perm_b32 v103, v100, v101, s57\n\t" "v_perm_b32 v107, v104, v105, s57\n\t" "v_perm_b32 v111, v108, v109, s57\n\t"
    "s_cbranch_vccz .Lgo_%=\n\t" "s_sub_u32 s44, s44, 1\n\t" "s_cbranch_scc0 .Lslow_%=\n\t" "s_branch .Lgo_%=\n\t" ".Lend_%=:\n\t"
    : "+{v0}"(vb), "+{v1}"(vh), "+{v2}"(vdt), "+{v11}"(vm), "+{v12}"(vbo), "+{v13}"(tid), "+{v[14:15]}"(t2), "+{v16}"(t3), "+{v18}"(vhd), "+{v19}"(vhx), "+{v196}"(d0), "+{v197}"(d1), "+{v198}"(d2), "+{v199}"(d3), "+{v200}"(dh0), "+{v201}"(dh1), "+{v202}"(xr0), "+{v203}"(xr1), "+{v230}"(vg), "+{v231}"(vtw), "+{v232}"(vhw), "+{v234}"(vs8), "+{v235}"(vph), "+{v246}"(vpr), "+{v3}"(vr0), "+{v4}"(vr1), "+{v5}"(vr2), "+{v6}"(vr3), "+{v7}"(vr4), "+{v8}"(vr5), "+{v9}"(vr6), "+{v10}"(vr7), "+{v205}"(tv), "+{s[62:63]}"(tag), "={v236}"(mu[0]), "={v237}"(mu[1])
    : [l0] "s"(l0), [lh] "s"(lh), [lt] "s"(lt), [lr] "s"(lr), [sl] "s"(sl), [sl1] "s"(sl1), [sb] "s"(sb), [rot] "s"(rot), [xl] "s"(xl), [xh_] "s"(xh_), [k0] "s"(k0), [kh] "s"(kh), [ws] "s"(ws), [sm] "s"(sm), [so] "s"(so), [sf] "s"(sf), [pf] "s"(pf), [tm] "s"(tm), [cd] "s"(cd), [w128] "s"(w128), [w4k] "s"(w4k), [xw] "s"(xw), [xh] "s"(xh), [ox] "s"(ox), [rw] "s"(rw)
    : "v17", "v20", "v21", "v22", "v23", "v24", "v25", "v26", "v27", "v28", "v29", "v30", "v31", "v32", "v33", "v34", "v35", "v36", "v37", "v38", "v39", "v40", "v41", "v42", "v43", "v44", "v45", "v46", "v47", "v48", "v49", "v50", "v51", "v52", "v53", "v54", "v55", "v56", "v57", "v58", "v59", "v60", "v61", "v62", "v63", "v64", "v65", "v66", "v67", "v68", "v69", "v70", "v71", "v72", "v73", "v74", "v75", "v76", "v77", "v78", "v79", "v80", "v81", "v82", "v83", "v84", "v85", "v86", "v87", "v88", "v89", "v90", "v91", "v92", "v93", "v94", "v95", "v96", "v97", "v98", "v99", "v100", "v101", "v102", "v103", "v104", "v105", "v106", "v107", "v108", "v109", "v110", "v111", "v112", "v113", "v114", "v115", "v116", "v117", "v118", "v119", "v120", "v121", "v122", "v123", "v124", "v125", "v126", "v127", "v128", "v129", "v130", "v131", "v132", "v133", "v134", "v135", "v136", "v137", "v138", "v139", "v140", "v141", "v142", "v143", "v144", "v145", "v146", "v147", "v148", "v149", "v150", "v151", "v152", "v153", "v154", "v155", "v156", "v157", "v158", "v159", "v160", "v161", "v162", "v163", "v164", "v165", "v166", "v167", "v168", "v169", "v170", "v171", "v172", "v173", "v174", "v175", "v176", "v177", "v178", "v179", "v180", "v181", "v182", "v183", "v184", "v185", "v186", "v187", "v188", "v189", "v190", "v191", "v192", "v193", "v194", "v195", "v204", "v206", "v207", "v208", "v209", "v210", "v211", "v212", "v213", "v214", "v215", "v216", "v217", "v218", "v219", "v220", "v221", "v222", "v223", "v224", "v225", "v226", "v227", "v228", "v229", "v233", "v238", "v239", "v240", "v241", "v242", "v243", "v244", "v245", "v247", "v248", "v249", "v250", "v251", "v252", "v253", "v254", "v255", "a0", "a1", "a2", "a3", "a4", "a5", "a6", "a7", "a8", "a9", "a10", "a11", "a12", "a13", "a14", "a15", "a16", "a17", "a18", "a19", "a20", "a21", "a22", "a23", "a24", "a25", "a26", "a27", "a28", "a29", "a30", "a31", "a32", "a33", "a34", "a35", "a36", "a37", "a38", "a39", "a40", "a41", "a42", "a43", "a44", "a45", "a46", "a47", "a48", "a49", "a50", "a51", "a52", "a53", "a54", "a55", "a56", "a57", "a58", "a59", "a60", "a61", "a62", "a63", "a64", "a65", "a66", "a67", "a68", "a69", "a70", "a71", "a72", "a73", "a74", "a75", "a76", "a77", "a78", "a79", "a80", "a81", "a82", "a83", "a84", "a85", "a86", "a87", "a88", "a89", "a90", "a91", "a92", "a93", "a94", "a95", "a96", "a97", "a98", "a99", "a100", "a101", "a102", "a103", "a104", "a105", "a106", "a107", "a108", "a109", "a110", "a111", "a112", "a113", "a114", "a115", "a116", "a117", "a118", "a119", "a120", "a121", "a122", "a123", "a124", "a125", "a126", "a127", "a128", "a129", "a130", "a131", "a132", "a133", "a134", "a135", "a136", "a137", "a138", "a139", "a140", "a141", "a142", "a143", "a144", "a145", "a146", "a147", "a148", "a149", "a150", "a151", "a152", "a153", "a154", "a155", "a156", "a157", "a158", "a159", "a160", "a161", "a162", "a163", "a164", "a165", "a166", "a167", "a168", "a169", "a170", "a171", "a172", "a173", "a174", "a175", "a176", "a177", "a178", "a179", "a180", "a181", "a182", "a183", "a184", "a185", "a186", "a187", "a188", "a189", "a190", "a191", "a192", "a193", "a194", "a195", "a196", "a197", "a198", "a199", "a200", "a201", "a202", "a203", "a204", "a205", "a206", "a207", "a208", "a209", "a210", "a211", "a212", "a213", "a214", "a215", "a216", "a217", "a218", "a219", "a220", "a221", "a222", "a223", "a224", "a225", "a226", "a227", "a228", "a229", "a230", "a231", "a232", "a233", "a234", "a235", "a236", "a237", "a238", "a239", "a240", "a241", "a242", "a243", "a244", "a245", "a246", "a247", "a248", "a249", "a250", "a251", "a252", "a253", "a254", "a255", "s40", "s41", "s42", "s43", "s44", "s45", "s46", "s47", "s48", "s49", "s50", "s51", "s52", "s53", "s54", "s55", "s56", "s57", "s58", "s59", "s60", "s61", "s64", "s65", "vcc", "scc", "memory"); }
  } }
  asm volatile("" : "+s"(tag)); asm volatile("" : "+s"(bx), "+s"(wid), "+s"(ws));
  constexpr int YS = 16384, YW = YS == 4096 ? 131072 : 4096;
  constexpr int GB = 49152, RAW = 0, TAB = 16384, ZB = TAB + 3584;
#define GW() const unsigned sl = ldsb + GB + wid * 3584; const char* sb = (const char*)((wid >> 1) ? wu : wg) + (unsigned)(((xcd >> 1) * 8 + (loc >> 2)) * 224 + (xcd & 1) * 16 * 32 * 7168)
  unsigned vb[4]; u32x4 stg[36];
  f32x4 xr[8];
  { unsigned vr[16]; const unsigned va = (tid & 63) * 16; const char* sa;
    { IDX();
    const unsigned vf = (tid & 15) * FS, rr = ldsb + HALO + 22528 + va;
    const char* sq = ws + (unsigned)(W_TAGS + OF * 8 + FA(rb * 16)); sa = ws + (unsigned)(W_O + (rb * 4 + wid) * 65536 + 4096);
    const char* su = ws + (unsigned)(wid == 3 ? W_MODP + gc * 1024 + gk * 256 : W_UVP + ((xcd >> 1) * 8 + (loc >> 2)) * 16384 + ((xcd & 1) * 4 + (loc & 3)) * 2048 + (wid & 1) * 1024);
    const int lane = tid & 63, fr = lane & 15, par = bx & 1; u32x4 id[2];
#pragma unroll
    for (int j = 0; j < 4; ++j) { vr[j] = ldsb + f_vr(lane, j) + (wid & 1) * 2048; vr[4 + j] = vr[j] + 65536; vr[8 + j] = vr[j] ^ 2048; vr[12 + j] = vr[8 + j] + 65536; }
#pragma unroll
    for (int j = 0; j < 8; ++j) id[j >> 2][j & 3] = (lane >> 1) - (fr >> 3) * 12 == (j >> 2) * 16 + (j & 3) ? 0x3f80u << (fr & 1) * 16 : 0u;
    asm volatile( "s_cmp_eq_u32 %[wid], 0\n\t" "s_cselect_b64 s[46:47], 0xffff, 0\n\t" "s_mov_b64 exec, s[46:47]\n\t" "global_load_dwordx2 v[190:191], v14, %[sq] sc1\n\t" "s_mov_b64 exec, -1\n\t"
    "v_sub_f32 v156, 0, v236\n\t" "v_sub_f32 v157, 0, v236\n\t" "v_sub_f32 v158, 0, v236\n\t" "v_sub_f32 v159, 0, v236\n\t" "v_sub_f32 v160, 0, v237\n\t" "v_sub_f32 v161, 0, v237\n\t"
    "v_sub_f32 v162, 0, v237\n\t" "v_sub_f32 v163, 0, v237\n\t" "s_cmp_eq_u32 %[par], 0\n\t" "s_cbranch_scc0 .Lx1_%=\n\t" "s_add_u32 m0, %[sl1], 110592\n\t" "v_add_u32 v12, 0x10000, v12\n\t"
    "v_mfma_f32_16x16x32_bf16 v[80:83], v[40:43], a[0:3], v[156:159]\n\t" "global_load_lds_dwordx4 v12, %[so]\n\t" "s_add_u32 m0, %[sl1], 114688\n\t" "v_add_u32 v12, 0x10000, v12\n\t"
    "v_mfma_f32_16x16x32_bf16 v[84:87], v[40:43], a[4:7], v[160:163]\n\t" "global_load_lds_dwordx4 v12, %[so]\n\t" "s_add_u32 m0, %[sl1], 118784\n\t" "v_add_u32 v12, 0x10000, v12\n\t"
    "v_mfma_f32_16x16x32_bf16 v[88:91], v[44:47], a[0:3], v[156:159]\n\t" "global_load_lds_dwordx4 v12, %[so]\n\t" "s_add_u32 m0, %[sl1], 122880\n\t" "v_add_u32 v12, 0x10000, v12\n\t"
    "v_mfma_f32_16x16x32_bf16 v[92:95], v[44:47], a[4:7], v[160:163]\n\t" "global_load_lds_dwordx4 v12, %[so]\n\t" "s_add_u32 m0, %[sl1], 126976\n\t" "v_add_u32 v12, 0x10000, v12\n\t"
    "v_mfma_f32_16x16x32_bf16 v[96:99], v[40:43], a[8:11], v[156:159]\n\t" "global_load_lds_dwordx4 v12, %[so]\n\t" "v_mfma_f32_16x16x32_bf16 v[100:103], v[40:43], a[12:15], v[160:163]\n\t"
    "v_mfma_f32_16x16x32_bf16 v[104:107], v[44:47], a[8:11], v[156:159]\n\t" "v_mfma_f32_16x16x32_bf16 v[108:111], v[44:47], a[12:15], v[160:163]\n\t" "s_branch .Lx2_%=\n\t" ".Lx1_%=:\n\t"
    "s_add_u32 m0, %[sl1], 110592\n\t" "v_add_u32 v12, 0x10000, v12\n\t" "v_mfma_f32_16x16x32_bf16 v[80:83], v[40:43], a[16:19], v[156:159]\n\t" "global_load_lds_dwordx4 v12, %[so]\n\t"
    "s_add_u32 m0, %[sl1], 114688\n\t" "v_add_u32 v12, 0x10000, v12\n\t" "v_mfma_f32_16x16x32_bf16 v[84:87], v[40:43], a[20:23], v[160:163]\n\t" "global_load_lds_dwordx4 v12, %[so]\n\t"
    "s_add_u32 m0, %[sl1], 118784\n\t" "v_add_u32 v12, 0x10000, v12\n\t" "v_mfma_f32_16x16x32_bf16 v[88:91], v[44:47], a[16:19], v[156:159]\n\t" "global_load_lds_dwordx4 v12, %[so]\n\t"
    "s_add_u32 m0, %[sl1], 122880\n\t" "v_add_u32 v12, 0x10000, v12\n\t" "v_mfma_f32_16x16x32_bf16 v[92:95], v[44:47], a[20:23], v[160:163]\n\t" "global_load_lds_dwordx4 v12, %[so]\n\t"
    "s_add_u32 m0, %[sl1], 126976\n\t" "v_add_u32 v12, 0x10000, v12\n\t" "v_mfma_f32_16x16x32_bf16 v[96:99], v[40:43], a[24:27], v[156:159]\n\t" "global_load_lds_dwordx4 v12, %[so]\n\t"
    "v_mfma_f32_16x16x32_bf16 v[100:103], v[40:43], a[28:31], v[160:163]\n\t" "v_mfma_f32_16x16x32_bf16 v[104:107], v[44:47], a[24:27], v[156:159]\n\t"
    "v_mfma_f32_16x16x32_bf16 v[108:111], v[44:47], a[28:31], v[160:163]\n\t" ".Lx2_%=:\n\t" "v_mov_b64 v[164:165], %[tag]\n\t" "s_cmp_eq_u32 %[wid], 0\n\t" "s_cselect_b64 s[42:43], 0, -1\n\t"
    "v_mov_b32 v168, -1\n\t" "v_mov_b32 v169, -1\n\t" "v_mov_b32 v170, -1\n\t" "v_mov_b32 v171, -1\n\t" "s_cmp_eq_u32 %[wid], 3\n\t" "s_cbranch_scc0 .Lnu_%=\n\t"
    "ds_read_b128 v[172:175], v35 offset:0\n\t" "ds_read_b128 v[176:179], v35 offset:256\n\t" "ds_read_b128 v[180:183], v35 offset:512\n\t" "ds_read_b128 v[184:187], v35 offset:768\n\t"
    "s_mov_b32 s48, 0x07060503\n\t" "s_mov_b32 s49, 0x07060502\n\t" "s_mov_b32 s50, 0x07060501\n\t" "s_waitcnt lgkmcnt(0)\n\t" "v_pk_add_f32 v[172:173], v[172:173], v[176:177]\n\t"
    "v_pk_add_f32 v[174:175], v[174:175], v[178:179]\n\t" "v_pk_add_f32 v[180:181], v[180:181], v[184:185]\n\t" "v_pk_add_f32 v[182:183], v[182:183], v[186:187]\n\t"
    "v_pk_add_f32 v[172:173], v[172:173], v[180:181]\n\t" "v_pk_add_f32 v[174:175], v[174:175], v[182:183]\n\t" "v_perm_b32 v168, v172, v175, s48\n\t" "v_perm_b32 v169, v173, v175, s49\n\t"
    "v_perm_b32 v170, v174, v175, s50\n\t" "v_xad_u32 v171, v168, v169, v164\n\t" "s_mov_b64 s[42:43], 0xffff\n\t" "v_xad_u32 v171, v170, v169, v171\n\t" ".Lnu_%=:\n\t" "s_mov_b64 s[40:41], %[sa]\n\t"
    "s_mov_b32 s44, 0x400000\n\t" "s_mov_b64 exec, s[46:47]\n\t" "s_waitcnt vmcnt(5)\n\t" ".Lpo_%=:\n\t" "v_cmp_ne_u64 vcc, %[tag], v[190:191]\n\t" "s_nop 4\n\t" "s_cbranch_vccz .Lro_%=\n\t"
    "s_mov_b64 exec, vcc\n\t" "s_sleep 1\n\t" "global_load_dwordx2 v[190:191], v14, %[sq] sc1\n\t" "s_sub_u32 s44, s44, 1\n\t" "s_waitcnt vmcnt(0)\n\t" "s_cbranch_scc0 .Lpo_%=\n\t" ".Lro_%=:\n\t"
    "s_mov_b64 exec, -1\n\t" "s_barrier\n\t" "global_load_dwordx4 a[64:67], v0, s[40:41] offset:-4096 sc1\n\t" "global_load_dwordx4 a[68:71], v0, s[40:41] offset:-3072 sc1\n\t"
    "global_load_dwordx4 a[72:75], v0, s[40:41] offset:-2048 sc1\n\t" "global_load_dwordx4 a[76:79], v0, s[40:41] offset:-1024 sc1\n\t" "ds_read_b64_tr_b16 a[160:161], v16 offset:0\n\t"
    "ds_read_b64_tr_b16 a[162:163], v24 offset:0\n\t" "ds_read_b64_tr_b16 a[164:165], v17 offset:0\n\t" "ds_read_b64_tr_b16 a[166:167], v25 offset:0\n\t" "ds_read_b64_tr_b16 a[168:169], v18 offset:0\n\t"
    "ds_read_b64_tr_b16 a[170:171], v26 offset:0\n\t" "ds_read_b64_tr_b16 a[172:173], v19 offset:0\n\t" "ds_read_b64_tr_b16 a[174:175], v27 offset:0\n\t"
    "ds_read_b64_tr_b16 a[176:177], v16 offset:4096\n\t" "ds_read_b64_tr_b16 a[178:179], v24 offset:4096\n\t" "ds_read_b64_tr_b16 a[180:181], v17 offset:4096\n\t"
    "ds_read_b64_tr_b16 a[182:183], v25 offset:4096\n\t" "global_load_dwordx4 a[80:83], v0, s[40:41] offset:0 sc1\n\t" "global_load_dwordx4 a[84:87], v0, s[40:41] offset:1024 sc1\n\t"
    "global_load_dwordx4 a[88:91], v0, s[40:41] offset:2048 sc1\n\t" "global_load_dwordx4 a[92:95], v0, s[40:41] offset:3072 sc1\n\t" "s_add_u32 s40, s40, 8192\n\t" "s_addc_u32 s41, s41, 0\n\t"
    "global_load_dwordx4 a[96:99], v0, s[40:41] offset:-4096 sc1\n\t" "global_load_dwordx4 a[100:103], v0, s[40:41] offset:-3072 sc1\n\t"
    "global_load_dwordx4 a[104:107], v0, s[40:41] offset:-2048 sc1\n\t" "global_load_dwordx4 a[108:111], v0, s[40:41] offset:-1024 sc1\n\t" "s_mov_b64 exec, s[42:43]\n\t"
    "global_store_dwordx4 v0, v[168:171], %[su] sc1\n\t" "s_mov_b64 exec, -1\n\t" "s_nop 1\n\t"
    : "+{v12}"(vbo), "=&{v[80:83]}"(xr[0]), "=&{v[84:87]}"(xr[1]), "=&{v[88:91]}"(xr[2]), "=&{v[92:95]}"(xr[3]), "=&{v[96:99]}"(xr[4]), "=&{v[100:103]}"(xr[5]), "=&{v[104:107]}"(xr[6]), "=&{v[108:111]}"(xr[7])
    : "{v0}"(va), "{v14}"(vf), "{v35}"(rr), "{v236}"(mu[0]), "{v237}"(mu[1]), "{v16}"(vr[0]), "{v17}"(vr[1]), "{v18}"(vr[2]), "{v19}"(vr[3]), "{v20}"(vr[4]), "{v21}"(vr[5]), "{v22}"(vr[6]), "{v23}"(vr[7]), "{v24}"(vr[8]), "{v25}"(vr[9]), "{v26}"(vr[10]), "{v27}"(vr[11]), "{v28}"(vr[12]), "{v29}"(vr[13]), "{v30}"(vr[14]), "{v31}"(vr[15]), "{v[40:43]}"(id[0]), "{v[44:47]}"(id[1]), [tag] "s"(tag), [par] "s"(par), [wid] "s"(wid), [sl1] "s"(sl1), [so] "s"(so), [su] "s"(su), [sa] "s"(sa), [sq] "s"(sq)
    : "v156", "v157", "v158", "v159", "v160", "v161", "v162", "v163", "v164", "v165", "v166", "v167", "v168", "v169", "v170", "v171", "v172", "v173", "v174", "v175", "v176", "v177", "v178", "v179", "v180", "v181", "v182", "v183", "v184", "v185", "v186", "v187", "v188", "v189", "v190", "v191", "v192", "v193", "v194", "v195", "a0", "a1", "a2", "a3", "a4", "a5", "a6", "a7", "a8", "a9", "a10", "a11", "a12", "a13", "a14", "a15", "a16", "a17", "a18", "a19", "a20", "a21", "a22", "a23", "a24", "a25", "a26", "a27", "a28", "a29", "a30", "a31", "a32", "a33", "a34", "a35", "a36", "a37", "a38", "a39", "a40", "a41", "a42", "a43", "a44", "a45", "a46", "a47", "a48", "a49", "a50", "a51", "a52", "a53", "a54", "a55", "a56", "a57", "a58", "a59", "a60", "a61", "a62", "a63", "a64", "a65", "a66", "a67", "a68", "a69", "a70", "a71", "a72", "a73", "a74", "a75", "a76", "a77", "a78", "a79", "a80", "a81", "a82", "a83", "a84", "a85", "a86", "a87", "a88", "a89", "a90", "a91", "a92", "a93", "a94", "a95", "a96", "a97", "a98", "a99", "a100", "a101", "a102", "a103", "a104", "a105", "a106", "a107", "a108", "a109", "a110", "a111", "a112", "a113", "a114", "a115", "a116", "a117", "a118", "a119", "a120", "a121", "a122", "a123", "a124", "a125", "a126", "a127", "a128", "a129", "a130", "a131", "a132", "a133", "a134", "a135", "a136", "a137", "a138", "a139", "a140", "a141", "a142", "a143", "a144", "a145", "a146", "a147", "a148", "a149", "a150", "a151", "a152", "a153", "a154", "a155", "a156", "a157", "a158", "a159", "a160", "a161", "a162", "a163", "a164", "a165", "a166", "a167", "a168", "a169", "a170", "a171", "a172", "a173", "a174", "a175", "a176", "a177", "a178", "a179", "a180", "a181", "a182", "a183", "a184", "a185", "a186", "a187", "a188", "a189", "a190", "a191", "s40", "s41", "s42", "s43", "s44", "s45", "s46", "s47", "s48", "s49", "s50", "s78", "s79", "s82", "s83", "vcc", "scc", "memory");
    }
    asm volatile("" : "+s"(bx), "+s"(wid), "+s"(ws)); asm volatile("" : "+v"(tid));
    IDX(); GW();
    const int lane = tid & 63, fr = lane & 15, fq = lane >> 4, col = fr * 4 + fq, role = wid == 1 ? 1 : (rb & 1) ? 3 : wid + (wid == 3 && bx == 0);
#pragma unroll
    for (int i = 0; i < 4; ++i) { const int idx = min(i * 64 + lane, 223), r = (idx * 293) >> 12; vb[i] = (unsigned)((wid & 1) * 16 * 7168 + r * (7168 - 224) + idx * 16); }
    const unsigned tw = ldsb + HALO + 1024 + wid * 512 + col * 4, tr = ldsb + HALO + 1024 + wid * 512 + fq * 16, vs = (lane & 31) * 8;
    const unsigned ve = wid == 1 ? col * 4 : (fr >> 3) * 128 + (fr & 3) * 16 + (fr & 4) * 2 + fq * 2;
    const char* sg0 = ws + (unsigned)(W_MODP + co * 1024); const char* sg1 = sg0 + 32768; const char* sg2 = sg0 + (unsigned)(16384 + (wid & 1) * 32768);
    const char* se = ws + (unsigned)(wid == 1 ? W_GB + bx * 256 : wid == 3 ? W_TAGS + EP * 8 : W_EF + rb * 2048 + co * 256 + (wid ? 0 : 64));
    const char* sy0 = ws + (unsigned)(W_Y + (rb >> 1) * 524288 + ((rb & 1) * 2 + (wid >> 1)) * YW + 2 * co * YS + (wid & 1) * 2048); const char* sy1 = sy0 + YS; const char* ss = ws + (unsigned)(W_STATS + (cb * S_ + rb * 128 + wid * 32) * 8);
    asm volatile(".p2align 3\n\t" "s_mov_b64 s[40:41], %[sa]\n\t" "s_mov_b32 s49, 0x04000c0c\n\t" "s_mov_b32 s50, 0x0706000c\n\t" "s_mov_b32 s44, 0x400000\n\t" "v_mov_b64 v[164:165], %[tag]\n\t"
    "s_waitcnt vmcnt(10)\n\t" "s_waitcnt lgkmcnt(4)\n\t" "v_mfma_f32_16x16x32_bf16 v[48:51], a[160:163], a[64:67], 0\n\t" "ds_read_b64_tr_b16 a[184:185], v18 offset:4096\n\t"
    "global_load_dwordx4 a[112:115], v0, s[40:41] offset:0 sc1\n\t" "v_mfma_f32_16x16x32_bf16 v[52:55], a[160:163], a[68:71], 0\n\t" "ds_read_b64_tr_b16 a[186:187], v26 offset:4096\n\t"
    "v_mfma_f32_16x16x32_bf16 v[60:63], a[164:167], a[68:71], 0\n\t" "ds_read_b64_tr_b16 a[188:189], v19 offset:4096\n\t" "global_load_dwordx4 a[116:119], v0, s[40:41] offset:1024 sc1\n\t"
    "v_mfma_f32_16x16x32_bf16 v[56:59], a[164:167], a[64:67], 0\n\t" "ds_read_b64_tr_b16 a[190:191], v27 offset:4096\n\t" "v_mfma_f32_16x16x32_bf16 v[64:67], a[168:171], a[64:67], 0\n\t"
    "ds_read_b64_tr_b16 a[160:161], v16 offset:8192\n\t" "global_load_dwordx4 a[120:123], v0, s[40:41] offset:2048 sc1\n\t" "v_mfma_f32_16x16x32_bf16 v[68:71], a[168:171], a[68:71], 0\n\t"
    "ds_read_b64_tr_b16 a[162:163], v24 offset:8192\n\t" "v_mfma_f32_16x16x32_bf16 v[76:79], a[172:175], a[68:71], 0\n\t" "ds_read_b64_tr_b16 a[164:165], v17 offset:8192\n\t"
    "v_mfma_f32_16x16x32_bf16 v[72:75], a[172:175], a[64:67], 0\n\t" "ds_read_b64_tr_b16 a[166:167], v25 offset:8192\n\t" "s_waitcnt lgkmcnt(4) vmcnt(11)\n\t"
    "v_mfma_f32_16x16x32_bf16 v[48:51], a[176:179], a[72:75], v[48:51]\n\t" "s_barrier\n\t" "ds_read_b64_tr_b16 a[168:169], v18 offset:8192\n\t"
    "global_load_dwordx4 a[124:127], v0, s[40:41] offset:3072 sc1\n\t" "v_mfma_f32_16x16x32_bf16 v[52:55], a[176:179], a[76:79], v[52:55]\n\t" "ds_read_b64_tr_b16 a[170:171], v26 offset:8192\n\t"
    "s_add_u32 s40, s40, 8192\n\t" "s_addc_u32 s41, s41, 0\n\t" "v_mfma_f32_16x16x32_bf16 v[60:63], a[180:183], a[76:79], v[60:63]\n\t" "ds_read_b64_tr_b16 a[172:173], v19 offset:8192\n\t"
    "v_mfma_f32_16x16x32_bf16 v[56:59], a[180:183], a[72:75], v[56:59]\n\t" "ds_read_b64_tr_b16 a[174:175], v27 offset:8192\n\t" "v_mfma_f32_16x16x32_bf16 v[64:67], a[184:187], a[72:75], v[64:67]\n\t"
    "ds_read_b64_tr_b16 a[176:177], v16 offset:12288\n\t" "global_load_dwordx4 a[128:131], v0, s[40:41] offset:-4096 sc1\n\t" "v_mfma_f32_16x16x32_bf16 v[68:71], a[184:187], a[76:79], v[68:71]\n\t"
    "ds_read_b64_tr_b16 a[178:179], v24 offset:12288\n\t" "v_mfma_f32_16x16x32_bf16 v[76:79], a[188:191], a[76:79], v[76:79]\n\t" "ds_read_b64_tr_b16 a[180:181], v17 offset:12288\n\t"
    "v_mfma_f32_16x16x32_bf16 v[72:75], a[188:191], a[72:75], v[72:75]\n\t" "ds_read_b64_tr_b16 a[182:183], v25 offset:12288\n\t" "s_waitcnt lgkmcnt(4) vmcnt(11)\n\t"
    "v_mfma_f32_16x16x32_bf16 v[48:51], a[160:163], a[80:83], v[48:51]\n\t" "ds_read_b64_tr_b16 a[184:185], v18 offset:12288\n\t" "global_load_dwordx4 a[132:135], v0, s[40:41] offset:-3072 sc1\n\t"
    "v_mfma_f32_16x16x32_bf16 v[52:55], a[160:163], a[84:87], v[52:55]\n\t" "ds_read_b64_tr_b16 a[186:187], v26 offset:12288\n\t" "v_mfma_f32_16x16x32_bf16 v[60:63], a[164:167], a[84:87], v[60:63]\n\t"
    "ds_read_b64_tr_b16 a[188:189], v19 offset:12288\n\t" "v_mfma_f32_16x16x32_bf16 v[56:59], a[164:167], a[80:83], v[56:59]\n\t" "ds_read_b64_tr_b16 a[190:191], v27 offset:12288\n\t"
    "v_mfma_f32_16x16x32_bf16 v[64:67], a[168:171], a[80:83], v[64:67]\n\t" "ds_read_b64_tr_b16 a[160:161], v16 offset:16384\n\t" "global_load_dwordx4 a[136:139], v0, s[40:41] offset:-2048 sc1\n\t"
    "v_mfma_f32_16x16x32_bf16 v[68:71], a[168:171], a[84:87], v[68:71]\n\t" "ds_read_b64_tr_b16 a[162:163], v24 offset:16384\n\t" "v_mfma_f32_16x16x32_bf16 v[76:79], a[172:175], a[84:87], v[76:79]\n\t"
    "ds_read_b64_tr_b16 a[164:165], v17 offset:16384\n\t" "v_mfma_f32_16x16x32_bf16 v[72:75], a[172:175], a[80:83], v[72:75]\n\t" "ds_read_b64_tr_b16 a[166:167], v25 offset:16384\n\t"
    "s_waitcnt lgkmcnt(4) vmcnt(11)\n\t" "v_mfma_f32_16x16x32_bf16 v[48:51], a[176:179], a[88:91], v[48:51]\n\t" "ds_read_b64_tr_b16 a[168:169], v18 offset:16384\n\t"
    "global_load_dwordx4 a[140:143], v0, s[40:41] offset:-1024 sc1\n\t" "v_mfma_f32_16x16x32_bf16 v[52:55], a[176:179], a[92:95], v[52:55]\n\t" "ds_read_b64_tr_b16 a[170:171], v26 offset:16384\n\t"
    "v_mfma_f32_16x16x32_bf16 v[60:63], a[180:183], a[92:95], v[60:63]\n\t" "ds_read_b64_tr_b16 a[172:173], v19 offset:16384\n\t" "v_mfma_f32_16x16x32_bf16 v[56:59], a[180:183], a[88:91], v[56:59]\n\t"
    "ds_read_b64_tr_b16 a[174:175], v27 offset:16384\n\t" "v_mfma_f32_16x16x32_bf16 v[64:67], a[184:187], a[88:91], v[64:67]\n\t" "ds_read_b64_tr_b16 a[176:177], v16 offset:20480\n\t"
    "global_load_dwordx4 a[144:147], v0, s[40:41] offset:0 sc1\n\t" "v_mfma_f32_16x16x32_bf16 v[68:71], a[184:187], a[92:95], v[68:71]\n\t" "ds_read_b64_tr_b16 a[178:179], v24 offset:20480\n\t"
    "v_mfma_f32_16x16x32_bf16 v[76:79], a[188:191], a[92:95], v[76:79]\n\t" "ds_read_b64_tr_b16 a[180:181], v17 offset:20480\n\t" "v_mfma_f32_16x16x32_bf16 v[72:75], a[188:191], a[88:91], v[72:75]\n\t"
    "ds_read_b64_tr_b16 a[182:183], v25 offset:20480\n\t" "s_waitcnt lgkmcnt(4) vmcnt(11)\n\t" "v_mfma_f32_16x16x32_bf16 v[48:51], a[160:163], a[96:99], v[48:51]\n\t"
    "ds_read_b64_tr_b16 a[184:185], v18 offset:20480\n\t" "global_load_dwordx4 a[148:151], v0, s[40:41] offset:1024 sc1\n\t" "v_mfma_f32_16x16x32_bf16 v[52:55], a[160:163], a[100:103], v[52:55]\n\t"
    "ds_read_b64_tr_b16 a[186:187], v26 offset:20480\n\t" "v_mfma_f32_16x16x32_bf16 v[60:63], a[164:167], a[100:103], v[60:63]\n\t" "ds_read_b64_tr_b16 a[188:189], v19 offset:20480\n\t"
    "v_mfma_f32_16x16x32_bf16 v[56:59], a[164:167], a[96:99], v[56:59]\n\t" "ds_read_b64_tr_b16 a[190:191], v27 offset:20480\n\t" "v_mfma_f32_16x16x32_bf16 v[64:67], a[168:171], a[96:99], v[64:67]\n\t"
    "ds_read_b64_tr_b16 a[160:161], v16 offset:24576\n\t" "global_load_dwordx4 a[152:155], v0, s[40:41] offset:2048 sc1\n\t" "v_mfma_f32_16x16x32_bf16 v[68:71], a[168:171], a[100:103], v[68:71]\n\t"
    "ds_read_b64_tr_b16 a[162:163], v24 offset:24576\n\t" "v_mfma_f32_16x16x32_bf16 v[76:79], a[172:175], a[100:103], v[76:79]\n\t" "ds_read_b64_tr_b16 a[164:165], v17 offset:24576\n\t"
    "v_mfma_f32_16x16x32_bf16 v[72:75], a[172:175], a[96:99], v[72:75]\n\t" "ds_read_b64_tr_b16 a[166:167], v25 offset:24576\n\t" "s_waitcnt lgkmcnt(4) vmcnt(11)\n\t"
    "v_mfma_f32_16x16x32_bf16 v[48:51], a[176:179], a[104:107], v[48:51]\n\t" "ds_read_b64_tr_b16 a[168:169], v18 offset:24576\n\t" "global_load_dwordx4 a[156:159], v0, s[40:41] offset:3072 sc1\n\t"
    "v_mfma_f32_16x16x32_bf16 v[52:55], a[176:179], a[108:111], v[52:55]\n\t" "ds_read_b64_tr_b16 a[170:171], v26 offset:24576\n\t" "s_add_u32 s40, s40, 8192\n\t" "s_addc_u32 s41, s41, 0\n\t"
    "v_mfma_f32_16x16x32_bf16 v[60:63], a[180:183], a[108:111], v[60:63]\n\t" "ds_read_b64_tr_b16 a[172:173], v19 offset:24576\n\t"
    "v_mfma_f32_16x16x32_bf16 v[56:59], a[180:183], a[104:107], v[56:59]\n\t" "ds_read_b64_tr_b16 a[174:175], v27 offset:24576\n\t"
    "v_mfma_f32_16x16x32_bf16 v[64:67], a[184:187], a[104:107], v[64:67]\n\t" "ds_read_b64_tr_b16 a[176:177], v16 offset:28672\n\t" "global_load_dwordx4 a[64:67], v0, s[40:41] offset:-4096 sc1\n\t"
    "v_mfma_f32_16x16x32_bf16 v[68:71], a[184:187], a[108:111], v[68:71]\n\t" "ds_read_b64_tr_b16 a[178:179], v24 offset:28672\n\t"
    "v_mfma_f32_16x16x32_bf16 v[76:79], a[188:191], a[108:111], v[76:79]\n\t" "ds_read_b64_tr_b16 a[180:181], v17 offset:28672\n\t"
    "v_mfma_f32_16x16x32_bf16 v[72:75], a[188:191], a[104:107], v[72:75]\n\t" "ds_read_b64_tr_b16 a[182:183], v25 offset:28672\n\t" "s_waitcnt lgkmcnt(4) vmcnt(11)\n\t"
    "v_mfma_f32_16x16x32_bf16 v[48:51], a[160:163], a[112:115], v[48:51]\n\t" "ds_read_b64_tr_b16 a[184:185], v18 offset:28672\n\t" "global_load_dwordx4 a[68:71], v0, s[40:41] offset:-3072 sc1\n\t"
    "v_mfma_f32_16x16x32_bf16 v[52:55], a[160:163], a[116:119], v[52:55]\n\t" "ds_read_b64_tr_b16 a[186:187], v26 offset:28672\n\t"
    "v_mfma_f32_16x16x32_bf16 v[60:63], a[164:167], a[116:119], v[60:63]\n\t" "ds_read_b64_tr_b16 a[188:189], v19 offset:28672\n\t"
    "v_mfma_f32_16x16x32_bf16 v[56:59], a[164:167], a[112:115], v[56:59]\n\t" "ds_read_b64_tr_b16 a[190:191], v27 offset:28672\n\t"
    "v_mfma_f32_16x16x32_bf16 v[64:67], a[168:171], a[112:115], v[64:67]\n\t" "ds_read_b64_tr_b16 a[160:161], v16 offset:32768\n\t" "global_load_dwordx4 a[72:75], v0, s[40:41] offset:-2048 sc1\n\t"
    "v_mfma_f32_16x16x32_bf16 v[68:71], a[168:171], a[116:119], v[68:71]\n\t" "ds_read_b64_tr_b16 a[162:163], v24 offset:32768\n\t"
    "v_mfma_f32_16x16x32_bf16 v[76:79], a[172:175], a[116:119], v[76:79]\n\t" "ds_read_b64_tr_b16 a[164:165], v17 offset:32768\n\t"
    "v_mfma_f32_16x16x32_bf16 v[72:75], a[172:175], a[112:115], v[72:75]\n\t" "ds_read_b64_tr_b16 a[166:167], v25 offset:32768\n\t" "s_waitcnt lgkmcnt(4) vmcnt(11)\n\t"
    "v_mfma_f32_16x16x32_bf16 v[48:51], a[176:179], a[120:123], v[48:51]\n\t" "ds_read_b64_tr_b16 a[168:169], v18 offset:32768\n\t" "global_load_dwordx4 a[76:79], v0, s[40:41] offset:-1024 sc1\n\t"
    "v_mfma_f32_16x16x32_bf16 v[52:55], a[176:179], a[124:127], v[52:55]\n\t" "ds_read_b64_tr_b16 a[170:171], v26 offset:32768\n\t"
    "v_mfma_f32_16x16x32_bf16 v[60:63], a[180:183], a[124:127], v[60:63]\n\t" "ds_read_b64_tr_b16 a[172:173], v19 offset:32768\n\t"
    "v_mfma_f32_16x16x32_bf16 v[56:59], a[180:183], a[120:123], v[56:59]\n\t" "ds_read_b64_tr_b16 a[174:175], v27 offset:32768\n\t"
    "v_mfma_f32_16x16x32_bf16 v[64:67], a[184:187], a[120:123], v[64:67]\n\t" "ds_read_b64_tr_b16 a[176:177], v16 offset:36864\n\t" "global_load_dwordx4 a[80:83], v0, s[40:41] offset:0 sc1\n\t"
    "v_mfma_f32_16x16x32_bf16 v[68:71], a[184:187], a[124:127], v[68:71]\n\t" "ds_read_b64_tr_b16 a[178:179], v24 offset:36864\n\t"
    "v_mfma_f32_16x16x32_bf16 v[76:79], a[188:191], a[124:127], v[76:79]\n\t" "ds_read_b64_tr_b16 a[180:181], v17 offset:36864\n\t"
    "v_mfma_f32_16x16x32_bf16 v[72:75], a[188:191], a[120:123], v[72:75]\n\t" "ds_read_b64_tr_b16 a[182:183], v25 offset:36864\n\t" "s_waitcnt lgkmcnt(4) vmcnt(11)\n\t"
    "v_mfma_f32_16x16x32_bf16 v[48:51], a[160:163], a[128:131], v[48:51]\n\t" "ds_read_b64_tr_b16 a[184:185], v18 offset:36864\n\t" "global_load_dwordx4 a[84:87], v0, s[40:41] offset:1024 sc1\n\t"
    "v_mfma_f32_16x16x32_bf16 v[52:55], a[160:163], a[132:135], v[52:55]\n\t" "ds_read_b64_tr_b16 a[186:187], v26 offset:36864\n\t"
    "v_mfma_f32_16x16x32_bf16 v[60:63], a[164:167], a[132:135], v[60:63]\n\t" "ds_read_b64_tr_b16 a[188:189], v19 offset:36864\n\t"
    "v_mfma_f32_16x16x32_bf16 v[56:59], a[164:167], a[128:131], v[56:59]\n\t" "ds_read_b64_tr_b16 a[190:191], v27 offset:36864\n\t"
    "v_mfma_f32_16x16x32_bf16 v[64:67], a[168:171], a[128:131], v[64:67]\n\t" "ds_read_b64_tr_b16 a[160:161], v16 offset:40960\n\t" "global_load_dwordx4 a[88:91], v0, s[40:41] offset:2048 sc1\n\t"
    "v_mfma_f32_16x16x32_bf16 v[68:71], a[168:171], a[132:135], v[68:71]\n\t" "ds_read_b64_tr_b16 a[162:163], v24 offset:40960\n\t"
    "v_mfma_f32_16x16x32_bf16 v[76:79], a[172:175], a[132:135], v[76:79]\n\t" "ds_read_b64_tr_b16 a[164:165], v17 offset:40960\n\t"
    "v_mfma_f32_16x16x32_bf16 v[72:75], a[172:175], a[128:131], v[72:75]\n\t" "ds_read_b64_tr_b16 a[166:167], v25 offset:40960\n\t" "s_waitcnt lgkmcnt(4) vmcnt(11)\n\t"
    "v_mfma_f32_16x16x32_bf16 v[48:51], a[176:179], a[136:139], v[48:51]\n\t" "ds_read_b64_tr_b16 a[168:169], v18 offset:40960\n\t" "global_load_dwordx4 a[92:95], v0, s[40:41] offset:3072 sc1\n\t"
    "v_mfma_f32_16x16x32_bf16 v[52:55], a[176:179], a[140:143], v[52:55]\n\t" "ds_read_b64_tr_b16 a[170:171], v26 offset:40960\n\t" "s_add_u32 s40, s40, 8192\n\t" "s_addc_u32 s41, s41, 0\n\t"
    "v_mfma_f32_16x16x32_bf16 v[60:63], a[180:183], a[140:143], v[60:63]\n\t" "ds_read_b64_tr_b16 a[172:173], v19 offset:40960\n\t"
    "v_mfma_f32_16x16x32_bf16 v[56:59], a[180:183], a[136:139], v[56:59]\n\t" "ds_read_b64_tr_b16 a[174:175], v27 offset:40960\n\t"
    "v_mfma_f32_16x16x32_bf16 v[64:67], a[184:187], a[136:139], v[64:67]\n\t" "ds_read_b64_tr_b16 a[176:177], v16 offset:45056\n\t" "global_load_dwordx4 a[96:99], v0, s[40:41] offset:-4096 sc1\n\t"
    "v_mfma_f32_16x16x32_bf16 v[68:71], a[184:187], a[140:143], v[68:71]\n\t" "ds_read_b64_tr_b16 a[178:179], v24 offset:45056\n\t"
    "v_mfma_f32_16x16x32_bf16 v[76:79], a[188:191], a[140:143], v[76:79]\n\t" "ds_read_b64_tr_b16 a[180:181], v17 offset:45056\n\t"
    "v_mfma_f32_16x16x32_bf16 v[72:75], a[188:191], a[136:139], v[72:75]\n\t" "ds_read_b64_tr_b16 a[182:183], v25 offset:45056\n\t" "s_waitcnt lgkmcnt(4) vmcnt(11)\n\t"
    "v_mfma_f32_16x16x32_bf16 v[48:51], a[160:163], a[144:147], v[48:51]\n\t" "ds_read_b64_tr_b16 a[184:185], v18 offset:45056\n\t" "global_load_dwordx4 a[100:103], v0, s[40:41] offset:-3072 sc1\n\t"
    "v_mfma_f32_16x16x32_bf16 v[52:55], a[160:163], a[148:151], v[52:55]\n\t" "ds_read_b64_tr_b16 a[186:187], v26 offset:45056\n\t" "global_load_dwordx4 v[112:115], v0, %[sg0] sc1\n\t"
    "v_mfma_f32_16x16x32_bf16 v[60:63], a[164:167], a[148:151], v[60:63]\n\t" "ds_read_b64_tr_b16 a[188:189], v19 offset:45056\n\t"
    "v_mfma_f32_16x16x32_bf16 v[56:59], a[164:167], a[144:147], v[56:59]\n\t" "ds_read_b64_tr_b16 a[190:191], v27 offset:45056\n\t" "global_load_dwordx4 v[116:119], v0, %[sg1] sc1\n\t"
    "v_mfma_f32_16x16x32_bf16 v[64:67], a[168:171], a[144:147], v[64:67]\n\t" "ds_read_b64_tr_b16 a[160:161], v16 offset:49152\n\t" "global_load_dwordx4 a[104:107], v0, s[40:41] offset:-2048 sc1\n\t"
    "v_mfma_f32_16x16x32_bf16 v[68:71], a[168:171], a[148:151], v[68:71]\n\t" "ds_read_b64_tr_b16 a[162:163], v24 offset:49152\n\t" "global_load_dwordx4 v[120:123], v0, %[sg2] sc1\n\t"
    "v_mfma_f32_16x16x32_bf16 v[76:79], a[172:175], a[148:151], v[76:79]\n\t" "ds_read_b64_tr_b16 a[164:165], v17 offset:49152\n\t"
    "v_mfma_f32_16x16x32_bf16 v[72:75], a[172:175], a[144:147], v[72:75]\n\t" "ds_read_b64_tr_b16 a[166:167], v25 offset:49152\n\t" "s_waitcnt lgkmcnt(4) vmcnt(14)\n\t"
    "v_mfma_f32_16x16x32_bf16 v[48:51], a[176:179], a[152:155], v[48:51]\n\t" "ds_read_b64_tr_b16 a[168:169], v18 offset:49152\n\t" "global_load_dwordx4 a[108:111], v0, s[40:41] offset:-1024 sc1\n\t"
    "v_mfma_f32_16x16x32_bf16 v[52:55], a[176:179], a[156:159], v[52:55]\n\t" "ds_read_b64_tr_b16 a[170:171], v26 offset:49152\n\t"
    "v_mfma_f32_16x16x32_bf16 v[60:63], a[180:183], a[156:159], v[60:63]\n\t" "ds_read_b64_tr_b16 a[172:173], v19 offset:49152\n\t"
    "v_mfma_f32_16x16x32_bf16 v[56:59], a[180:183], a[152:155], v[56:59]\n\t" "ds_read_b64_tr_b16 a[174:175], v27 offset:49152\n\t"
    "v_mfma_f32_16x16x32_bf16 v[64:67], a[184:187], a[152:155], v[64:67]\n\t" "ds_read_b64_tr_b16 a[176:177], v16 offset:53248\n\t" "global_load_dwordx4 a[112:115], v0, s[40:41] offset:0 sc1\n\t"
    "v_mfma_f32_16x16x32_bf16 v[68:71], a[184:187], a[156:159], v[68:71]\n\t" "ds_read_b64_tr_b16 a[178:179], v24 offset:53248\n\t"
    "v_mfma_f32_16x16x32_bf16 v[76:79], a[188:191], a[156:159], v[76:79]\n\t" "ds_read_b64_tr_b16 a[180:181], v17 offset:53248\n\t"
    "v_mfma_f32_16x16x32_bf16 v[72:75], a[188:191], a[152:155], v[72:75]\n\t" "ds_read_b64_tr_b16 a[182:183], v25 offset:53248\n\t" "s_waitcnt lgkmcnt(4) vmcnt(14)\n\t"
    "v_mfma_f32_16x16x32_bf16 v[48:51], a[160:163], a[64:67], v[48:51]\n\t" "ds_read_b64_tr_b16 a[184:185], v18 offset:53248\n\t" "global_load_dwordx4 a[116:119], v0, s[40:41] offset:1024 sc1\n\t"
    "v_mfma_f32_16x16x32_bf16 v[52:55], a[160:163], a[68:71], v[52:55]\n\t" "ds_read_b64_tr_b16 a[186:187], v26 offset:53248\n\t" "v_mfma_f32_16x16x32_bf16 v[60:63], a[164:167], a[68:71], v[60:63]\n\t"
    "ds_read_b64_tr_b16 a[188:189], v19 offset:53248\n\t" "v_mfma_f32_16x16x32_bf16 v[56:59], a[164:167], a[64:67], v[56:59]\n\t" "ds_read_b64_tr_b16 a[190:191], v27 offset:53248\n\t"
    "v_mfma_f32_16x16x32_bf16 v[64:67], a[168:171], a[64:67], v[64:67]\n\t" "ds_read_b64_tr_b16 a[160:161], v16 offset:57344\n\t" "global_load_dwordx4 a[120:123], v0, s[40:41] offset:2048 sc1\n\t"
    "v_mfma_f32_16x16x32_bf16 v[68:71], a[168:171], a[68:71], v[68:71]\n\t" "ds_read_b64_tr_b16 a[162:163], v24 offset:57344\n\t" "v_mfma_f32_16x16x32_bf16 v[76:79], a[172:175], a[68:71], v[76:79]\n\t"
    "ds_read_b64_tr_b16 a[164:165], v17 offset:57344\n\t" "v_mfma_f32_16x16x32_bf16 v[72:75], a[172:175], a[64:67], v[72:75]\n\t" "ds_read_b64_tr_b16 a[166:167], v25 offset:57344\n\t"
    "s_waitcnt lgkmcnt(4) vmcnt(14)\n\t" "v_mfma_f32_16x16x32_bf16 v[48:51], a[176:179], a[72:75], v[48:51]\n\t" "ds_read_b64_tr_b16 a[168:169], v18 offset:57344\n\t"
    "global_load_dwordx4 a[124:127], v0, s[40:41] offset:3072 sc1\n\t" "v_mfma_f32_16x16x32_bf16 v[52:55], a[176:179], a[76:79], v[52:55]\n\t" "ds_read_b64_tr_b16 a[170:171], v26 offset:57344\n\t"
    "s_add_u32 s40, s40, 8192\n\t" "s_addc_u32 s41, s41, 0\n\t" "v_mfma_f32_16x16x32_bf16 v[60:63], a[180:183], a[76:79], v[60:63]\n\t" "ds_read_b64_tr_b16 a[172:173], v19 offset:57344\n\t"
    "v_mfma_f32_16x16x32_bf16 v[56:59], a[180:183], a[72:75], v[56:59]\n\t" "ds_read_b64_tr_b16 a[174:175], v27 offset:57344\n\t" "v_mfma_f32_16x16x32_bf16 v[64:67], a[184:187], a[72:75], v[64:67]\n\t"
    "ds_read_b64_tr_b16 a[176:177], v16 offset:61440\n\t" "global_load_dwordx4 a[128:131], v0, s[40:41] offset:-4096 sc1\n\t" "v_mfma_f32_16x16x32_bf16 v[68:71], a[184:187], a[76:79], v[68:71]\n\t"
    "ds_read_b64_tr_b16 a[178:179], v24 offset:61440\n\t" "v_mfma_f32_16x16x32_bf16 v[76:79], a[188:191], a[76:79], v[76:79]\n\t" "ds_read_b64_tr_b16 a[180:181], v17 offset:61440\n\t"
    "v_mfma_f32_16x16x32_bf16 v[72:75], a[188:191], a[72:75], v[72:75]\n\t" "ds_read_b64_tr_b16 a[182:183], v25 offset:61440\n\t" "s_waitcnt lgkmcnt(4) vmcnt(14)\n\t"
    "v_mfma_f32_16x16x32_bf16 v[48:51], a[160:163], a[80:83], v[48:51]\n\t" "ds_read_b64_tr_b16 a[184:185], v18 offset:61440\n\t" "global_load_dwordx4 a[132:135], v0, s[40:41] offset:-3072 sc1\n\t"
    "v_mfma_f32_16x16x32_bf16 v[52:55], a[160:163], a[84:87], v[52:55]\n\t" "ds_read_b64_tr_b16 a[186:187], v26 offset:61440\n\t" "v_mfma_f32_16x16x32_bf16 v[60:63], a[164:167], a[84:87], v[60:63]\n\t"
    "ds_read_b64_tr_b16 a[188:189], v19 offset:61440\n\t" "v_mfma_f32_16x16x32_bf16 v[56:59], a[164:167], a[80:83], v[56:59]\n\t" "ds_read_b64_tr_b16 a[190:191], v27 offset:61440\n\t"
    "v_mfma_f32_16x16x32_bf16 v[64:67], a[168:171], a[80:83], v[64:67]\n\t" "ds_read_b64_tr_b16 a[160:161], v20 offset:0\n\t" "global_load_dwordx4 a[136:139], v0, s[40:41] offset:-2048 sc1\n\t"
    "v_mfma_f32_16x16x32_bf16 v[68:71], a[168:171], a[84:87], v[68:71]\n\t" "ds_read_b64_tr_b16 a[162:163], v28 offset:0\n\t" "v_mfma_f32_16x16x32_bf16 v[76:79], a[172:175], a[84:87], v[76:79]\n\t"
    "ds_read_b64_tr_b16 a[164:165], v21 offset:0\n\t" "v_mfma_f32_16x16x32_bf16 v[72:75], a[172:175], a[80:83], v[72:75]\n\t" "ds_read_b64_tr_b16 a[166:167], v29 offset:0\n\t"
    "s_waitcnt lgkmcnt(4) vmcnt(14)\n\t" "v_mfma_f32_16x16x32_bf16 v[48:51], a[176:179], a[88:91], v[48:51]\n\t" "ds_read_b64_tr_b16 a[168:169], v22 offset:0\n\t"
    "global_load_dwordx4 a[140:143], v0, s[40:41] offset:-1024 sc1\n\t" "v_mfma_f32_16x16x32_bf16 v[52:55], a[176:179], a[92:95], v[52:55]\n\t" "ds_read_b64_tr_b16 a[170:171], v30 offset:0\n\t"
    "v_mfma_f32_16x16x32_bf16 v[60:63], a[180:183], a[92:95], v[60:63]\n\t" "ds_read_b64_tr_b16 a[172:173], v23 offset:0\n\t" "v_mfma_f32_16x16x32_bf16 v[56:59], a[180:183], a[88:91], v[56:59]\n\t"
    "ds_read_b64_tr_b16 a[174:175], v31 offset:0\n\t" "v_mfma_f32_16x16x32_bf16 v[64:67], a[184:187], a[88:91], v[64:67]\n\t" "ds_read_b64_tr_b16 a[176:177], v20 offset:4096\n\t"
    "global_load_dwordx4 a[144:147], v0, s[40:41] offset:0 sc1\n\t" "v_mfma_f32_16x16x32_bf16 v[68:71], a[184:187], a[92:95], v[68:71]\n\t" "ds_read_b64_tr_b16 a[178:179], v28 offset:4096\n\t"
    "v_mfma_f32_16x16x32_bf16 v[76:79], a[188:191], a[92:95], v[76:79]\n\t" "ds_read_b64_tr_b16 a[180:181], v21 offset:4096\n\t" "v_mfma_f32_16x16x32_bf16 v[72:75], a[188:191], a[88:91], v[72:75]\n\t"
    "ds_read_b64_tr_b16 a[182:183], v29 offset:4096\n\t" "s_waitcnt lgkmcnt(4) vmcnt(14)\n\t" "v_mfma_f32_16x16x32_bf16 v[48:51], a[160:163], a[96:99], v[48:51]\n\t"
    "ds_read_b64_tr_b16 a[184:185], v22 offset:4096\n\t" "global_load_dwordx4 a[148:151], v0, s[40:41] offset:1024 sc1\n\t" "s_waitcnt vmcnt(11)\n\t"
    "v_mfma_f32_16x16x32_bf16 v[52:55], a[160:163], a[100:103], v[52:55]\n\t" "ds_read_b64_tr_b16 a[186:187], v30 offset:4096\n\t" "v_xad_u32 v156, v112, v113, v164\n\t"
    "v_xad_u32 v157, v116, v117, v164\n\t" "v_mfma_f32_16x16x32_bf16 v[60:63], a[164:167], a[100:103], v[60:63]\n\t" "ds_read_b64_tr_b16 a[188:189], v23 offset:4096\n\t"
    "v_xad_u32 v158, v120, v121, v164\n\t" "v_xad_u32 v156, v114, v113, v156\n\t" "v_mfma_f32_16x16x32_bf16 v[56:59], a[164:167], a[96:99], v[56:59]\n\t"
    "ds_read_b64_tr_b16 a[190:191], v31 offset:4096\n\t" "v_xad_u32 v157, v118, v117, v157\n\t" "v_xad_u32 v158, v122, v121, v158\n\t"
    "v_mfma_f32_16x16x32_bf16 v[64:67], a[168:171], a[96:99], v[64:67]\n\t" "ds_read_b64_tr_b16 a[160:161], v20 offset:8192\n\t" "global_load_dwordx4 a[152:155], v0, s[40:41] offset:2048 sc1\n\t"
    "v_xor_b32_e64 v156, v156, v115\n\t" "v_mfma_f32_16x16x32_bf16 v[68:71], a[168:171], a[100:103], v[68:71]\n\t" "ds_read_b64_tr_b16 a[162:163], v28 offset:8192\n\t"
    "v_bitop3_b32 v156, v156, v157, v119 bitop3:0xf6\n\t" "v_bitop3_b32 v156, v156, v158, v123 bitop3:0xf6\n\t" "v_mfma_f32_16x16x32_bf16 v[76:79], a[172:175], a[100:103], v[76:79]\n\t"
    "ds_read_b64_tr_b16 a[164:165], v21 offset:8192\n\t" "v_cmp_ne_u32_e64 vcc, 0, v156\n\t" "v_perm_b32 v115, v112, v113, s49\n\t" "v_mfma_f32_16x16x32_bf16 v[72:75], a[172:175], a[96:99], v[72:75]\n\t"
    "ds_read_b64_tr_b16 a[166:167], v29 offset:8192\n\t" "v_perm_b32 v119, v116, v117, s49\n\t" "v_perm_b32 v123, v120, v121, s49\n\t" "s_waitcnt lgkmcnt(4) vmcnt(11)\n\t"
    "v_mfma_f32_16x16x32_bf16 v[48:51], a[176:179], a[104:107], v[48:51]\n\t" "ds_read_b64_tr_b16 a[168:169], v22 offset:8192\n\t" "global_load_dwordx4 a[156:159], v0, s[40:41] offset:3072 sc1\n\t"
    "s_nop 1\n\t" "v_mfma_f32_16x16x32_bf16 v[52:55], a[176:179], a[108:111], v[52:55]\n\t" "ds_read_b64_tr_b16 a[170:171], v30 offset:8192\n\t" "s_add_u32 s40, s40, 8192\n\t" "s_addc_u32 s41, s41, 0\n\t"
    "v_mfma_f32_16x16x32_bf16 v[60:63], a[180:183], a[108:111], v[60:63]\n\t" "ds_read_b64_tr_b16 a[172:173], v23 offset:8192\n\t" "s_cbranch_vccnz .Lslow_%=\n\t" ".Lgo_%=:\n\t"
    "v_mfma_f32_16x16x32_bf16 v[56:59], a[180:183], a[104:107], v[56:59]\n\t" "ds_read_b64_tr_b16 a[174:175], v31 offset:8192\n\t" "v_perm_b32 v115, v115, v114, s50\n\t"
    "v_perm_b32 v119, v119, v118, s50\n\t" "v_mfma_f32_16x16x32_bf16 v[64:67], a[184:187], a[104:107], v[64:67]\n\t" "ds_read_b64_tr_b16 a[176:177], v20 offset:12288\n\t"
    "global_load_dwordx4 a[64:67], v0, s[40:41] offset:-4096 sc1\n\t" "v_perm_b32 v123, v123, v122, s50\n\t" "v_mfma_f32_16x16x32_bf16 v[68:71], a[184:187], a[108:111], v[68:71]\n\t"
    "ds_read_b64_tr_b16 a[178:179], v28 offset:12288\n\t" "v_permlane32_swap_b32 v112, v114\n\t" "v_permlane32_swap_b32 v116, v118\n\t"
    "v_mfma_f32_16x16x32_bf16 v[76:79], a[188:191], a[108:111], v[76:79]\n\t" "ds_read_b64_tr_b16 a[180:181], v21 offset:12288\n\t" "v_permlane32_swap_b32 v120, v122\n\t"
    "v_permlane32_swap_b32 v113, v115\n\t" "v_mfma_f32_16x16x32_bf16 v[72:75], a[188:191], a[104:107], v[72:75]\n\t" "ds_read_b64_tr_b16 a[182:183], v29 offset:12288\n\t"
    "v_permlane32_swap_b32 v117, v119\n\t" "v_permlane32_swap_b32 v121, v123\n\t" "s_waitcnt lgkmcnt(4) vmcnt(11)\n\t" "v_mfma_f32_16x16x32_bf16 v[48:51], a[160:163], a[112:115], v[48:51]\n\t"
    "ds_read_b64_tr_b16 a[184:185], v22 offset:12288\n\t" "global_load_dwordx4 a[68:71], v0, s[40:41] offset:-3072 sc1\n\t" "v_add_f32 v112, v112, v114\n\t"
    "v_mfma_f32_16x16x32_bf16 v[52:55], a[160:163], a[116:119], v[52:55]\n\t" "ds_read_b64_tr_b16 a[186:187], v30 offset:12288\n\t" "v_add_f32 v116, v116, v118\n\t" "v_add_f32 v120, v120, v122\n\t"
    "v_mfma_f32_16x16x32_bf16 v[60:63], a[164:167], a[116:119], v[60:63]\n\t" "ds_read_b64_tr_b16 a[188:189], v23 offset:12288\n\t" "v_add_f32 v113, v113, v115\n\t" "v_add_f32 v117, v117, v119\n\t"
    "v_mfma_f32_16x16x32_bf16 v[56:59], a[164:167], a[112:115], v[56:59]\n\t" "ds_read_b64_tr_b16 a[190:191], v31 offset:12288\n\t" "v_add_f32 v121, v121, v123\n\t" "v_permlane16_swap_b32 v112, v113\n\t"
    "v_mfma_f32_16x16x32_bf16 v[64:67], a[168:171], a[112:115], v[64:67]\n\t" "ds_read_b64_tr_b16 a[160:161], v20 offset:16384\n\t" "global_load_dwordx4 a[72:75], v0, s[40:41] offset:-2048 sc1\n\t"
    "v_permlane16_swap_b32 v116, v117\n\t" "v_mfma_f32_16x16x32_bf16 v[68:71], a[168:171], a[116:119], v[68:71]\n\t" "ds_read_b64_tr_b16 a[162:163], v28 offset:16384\n\t"
    "v_permlane16_swap_b32 v120, v121\n\t" "v_add_f32_e64 v112, v112, v113\n\t" "v_mfma_f32_16x16x32_bf16 v[76:79], a[172:175], a[116:119], v[76:79]\n\t"
    "ds_read_b64_tr_b16 a[164:165], v21 offset:16384\n\t" "v_add_f32 v116, v116, v117\n\t" "v_add_f32 v120, v120, v121\n\t" "v_mfma_f32_16x16x32_bf16 v[72:75], a[172:175], a[112:115], v[72:75]\n\t"
    "ds_read_b64_tr_b16 a[166:167], v29 offset:16384\n\t" "v_cvt_pk_bf16_f32 v112, v112, v112\n\t" "v_cvt_pk_bf16_f32 v116, v116, v116\n\t" "s_waitcnt lgkmcnt(4) vmcnt(11)\n\t"
    "v_mfma_f32_16x16x32_bf16 v[48:51], a[176:179], a[120:123], v[48:51]\n\t" "ds_read_b64_tr_b16 a[168:169], v22 offset:16384\n\t" "global_load_dwordx4 a[76:79], v0, s[40:41] offset:-1024 sc1\n\t"
    "v_cvt_pk_bf16_f32 v120, v120, v120\n\t" "v_mfma_f32_16x16x32_bf16 v[52:55], a[176:179], a[124:127], v[52:55]\n\t" "ds_read_b64_tr_b16 a[170:171], v30 offset:16384\n\t"
    "v_and_b32 v112, 0xffff0000, v112\n\t" "v_and_b32 v116, 0xffff0000, v116\n\t" "v_mfma_f32_16x16x32_bf16 v[60:63], a[180:183], a[124:127], v[60:63]\n\t"
    "ds_read_b64_tr_b16 a[172:173], v23 offset:16384\n\t" "v_and_b32 v120, 0xffff0000, v120\n\t" "v_add_f32 v116, 1.0, v116\n\t" "v_mfma_f32_16x16x32_bf16 v[56:59], a[180:183], a[120:123], v[56:59]\n\t"
    "ds_read_b64_tr_b16 a[174:175], v31 offset:16384\n\t" "v_cvt_pk_bf16_f32 v116, v116, v116\n\t" "v_and_b32 v116, 0xffff0000, v116\n\t"
    "v_mfma_f32_16x16x32_bf16 v[64:67], a[184:187], a[120:123], v[64:67]\n\t" "ds_read_b64_tr_b16 a[176:177], v20 offset:20480\n\t" "global_load_dwordx4 a[80:83], v0, s[40:41] offset:0 sc1\n\t"
    "ds_write2st64_b32 v15, v112, v116 offset1:1\n\t" "v_mfma_f32_16x16x32_bf16 v[68:71], a[184:187], a[124:127], v[68:71]\n\t" "ds_read_b64_tr_b16 a[178:179], v28 offset:20480\n\t"
    "s_cmp_eq_u32 %[role], 0\n\t" "s_cselect_b64 exec, -1, 0\n\t" "global_store_short_d16_hi v33, v120, %[se] sc1\n\t" "s_mov_b64 exec, -1\n\t"
    "v_mfma_f32_16x16x32_bf16 v[76:79], a[188:191], a[124:127], v[76:79]\n\t" "ds_read_b64_tr_b16 a[180:181], v21 offset:20480\n\t" "s_cmp_eq_u32 %[role], 1\n\t" "s_cselect_b64 exec, -1, 0\n\t"
    "global_store_dword v33, v120, %[se] sc1\n\t" "s_mov_b64 exec, -1\n\t" "v_mfma_f32_16x16x32_bf16 v[72:75], a[188:191], a[120:123], v[72:75]\n\t" "ds_read_b64_tr_b16 a[182:183], v29 offset:20480\n\t"
    "s_cmp_eq_u32 %[role], 2\n\t" "s_cselect_b64 exec, -1, 0\n\t" "global_store_short_d16_hi v33, v116, %[se] sc1\n\t" "s_mov_b64 exec, -1\n\t" "s_waitcnt lgkmcnt(5) vmcnt(11)\n\t"
    "v_mfma_f32_16x16x32_bf16 v[48:51], a[160:163], a[128:131], v[48:51]\n\t" "ds_read_b64_tr_b16 a[184:185], v22 offset:20480\n\t" "global_load_dwordx4 a[84:87], v0, s[40:41] offset:1024 sc1\n\t"
    "s_cmp_eq_u32 %[role], 4\n\t" "s_cselect_b64 exec, 1, 0\n\t" "global_store_dwordx2 v0, v[164:165], %[se]\n\t" "s_mov_b64 exec, -1\n\t"
    "v_mfma_f32_16x16x32_bf16 v[52:55], a[160:163], a[132:135], v[52:55]\n\t" "ds_read_b64_tr_b16 a[186:187], v30 offset:20480\n\t" "ds_read_b128 v[124:127], v32 offset:0\n\t"
    "ds_read_b128 v[128:131], v32 offset:64\n\t" "v_mfma_f32_16x16x32_bf16 v[60:63], a[164:167], a[132:135], v[60:63]\n\t" "ds_read_b64_tr_b16 a[188:189], v23 offset:20480\n\t"
    "ds_read_b128 v[132:135], v32 offset:128\n\t" "ds_read_b128 v[136:139], v32 offset:192\n\t" "v_mfma_f32_16x16x32_bf16 v[56:59], a[164:167], a[128:131], v[56:59]\n\t"
    "ds_read_b64_tr_b16 a[190:191], v31 offset:20480\n\t" "ds_read_b128 v[140:143], v32 offset:256\n\t" "ds_read_b128 v[144:147], v32 offset:320\n\t"
    "v_mfma_f32_16x16x32_bf16 v[64:67], a[168:171], a[128:131], v[64:67]\n\t" "s_waitcnt lgkmcnt(11)\n\t" "ds_read_b64_tr_b16 a[160:161], v20 offset:24576\n\t"
    "global_load_dwordx4 a[88:91], v0, s[40:41] offset:2048 sc1\n\t" "v_mfma_f32_16x16x32_bf16 v[68:71], a[168:171], a[132:135], v[68:71]\n\t" "ds_read_b64_tr_b16 a[162:163], v28 offset:24576\n\t"
    "ds_read_b128 v[148:151], v32 offset:384\n\t" "ds_read_b128 v[152:155], v32 offset:448\n\t" "v_mfma_f32_16x16x32_bf16 v[76:79], a[172:175], a[132:135], v[76:79]\n\t" "s_waitcnt lgkmcnt(11)\n\t"
    "ds_read_b64_tr_b16 a[164:165], v21 offset:24576\n\t" "v_mfma_f32_16x16x32_bf16 v[72:75], a[172:175], a[128:131], v[72:75]\n\t" "ds_read_b64_tr_b16 a[166:167], v29 offset:24576\n\t"
    "s_waitcnt lgkmcnt(8) vmcnt(11)\n\t" "v_mfma_f32_16x16x32_bf16 v[48:51], a[176:179], a[136:139], v[48:51]\n\t" "ds_read_b64_tr_b16 a[168:169], v22 offset:24576\n\t"
    "global_load_dwordx4 a[92:95], v0, s[40:41] offset:3072 sc1\n\t" "v_mfma_f32_16x16x32_bf16 v[52:55], a[176:179], a[140:143], v[52:55]\n\t" "ds_read_b64_tr_b16 a[170:171], v30 offset:24576\n\t"
    "s_add_u32 s40, s40, 8192\n\t" "s_addc_u32 s41, s41, 0\n\t" "v_mfma_f32_16x16x32_bf16 v[60:63], a[180:183], a[140:143], v[60:63]\n\t" "ds_read_b64_tr_b16 a[172:173], v23 offset:24576\n\t"
    "v_mfma_f32_16x16x32_bf16 v[56:59], a[180:183], a[136:139], v[56:59]\n\t" "ds_read_b64_tr_b16 a[174:175], v31 offset:24576\n\t"
    "v_mfma_f32_16x16x32_bf16 v[64:67], a[184:187], a[136:139], v[64:67]\n\t" "ds_read_b64_tr_b16 a[176:177], v20 offset:28672\n\t" "global_load_dwordx4 a[96:99], v0, s[40:41] offset:-4096 sc1\n\t"
    "v_mfma_f32_16x16x32_bf16 v[68:71], a[184:187], a[140:143], v[68:71]\n\t" "ds_read_b64_tr_b16 a[178:179], v28 offset:28672\n\t"
    "v_mfma_f32_16x16x32_bf16 v[76:79], a[188:191], a[140:143], v[76:79]\n\t" "ds_read_b64_tr_b16 a[180:181], v21 offset:28672\n\t"
    "v_mfma_f32_16x16x32_bf16 v[72:75], a[188:191], a[136:139], v[72:75]\n\t" "s_waitcnt lgkmcnt(11)\n\t" "ds_read_b64_tr_b16 a[182:183], v29 offset:28672\n\t" "s_waitcnt lgkmcnt(4) vmcnt(11)\n\t"
    "v_mfma_f32_16x16x32_bf16 v[48:51], a[160:163], a[144:147], v[48:51]\n\t" "ds_read_b64_tr_b16 a[184:185], v22 offset:28672\n\t" "global_load_dwordx4 a[100:103], v0, s[40:41] offset:-3072 sc1\n\t"
    "v_mfma_f32_16x16x32_bf16 v[52:55], a[160:163], a[148:151], v[52:55]\n\t" "ds_read_b64_tr_b16 a[186:187], v30 offset:28672\n\t"
    "v_mfma_f32_16x16x32_bf16 v[60:63], a[164:167], a[148:151], v[60:63]\n\t" "ds_read_b64_tr_b16 a[188:189], v23 offset:28672\n\t"
    "v_mfma_f32_16x16x32_bf16 v[56:59], a[164:167], a[144:147], v[56:59]\n\t" "ds_read_b64_tr_b16 a[190:191], v31 offset:28672\n\t"
    "v_mfma_f32_16x16x32_bf16 v[64:67], a[168:171], a[144:147], v[64:67]\n\t" "ds_read_b64_tr_b16 a[160:161], v20 offset:32768\n\t" "global_load_dwordx4 a[104:107], v0, s[40:41] offset:-2048 sc1\n\t"
    "v_mfma_f32_16x16x32_bf16 v[68:71], a[168:171], a[148:151], v[68:71]\n\t" "ds_read_b64_tr_b16 a[162:163], v28 offset:32768\n\t"
    "v_mfma_f32_16x16x32_bf16 v[76:79], a[172:175], a[148:151], v[76:79]\n\t" "ds_read_b64_tr_b16 a[164:165], v21 offset:32768\n\t"
    "v_mfma_f32_16x16x32_bf16 v[72:75], a[172:175], a[144:147], v[72:75]\n\t" "ds_read_b64_tr_b16 a[166:167], v29 offset:32768\n\t" "s_waitcnt lgkmcnt(4) vmcnt(11)\n\t"
    "v_mfma_f32_16x16x32_bf16 v[48:51], a[176:179], a[152:155], v[48:51]\n\t" "ds_read_b64_tr_b16 a[168:169], v22 offset:32768\n\t" "global_load_dwordx4 a[108:111], v0, s[40:41] offset:-1024 sc1\n\t"
    "v_mfma_f32_16x16x32_bf16 v[52:55], a[176:179], a[156:159], v[52:55]\n\t" "ds_read_b64_tr_b16 a[170:171], v30 offset:32768\n\t"
    "v_mfma_f32_16x16x32_bf16 v[60:63], a[180:183], a[156:159], v[60:63]\n\t" "ds_read_b64_tr_b16 a[172:173], v23 offset:32768\n\t"
    "v_mfma_f32_16x16x32_bf16 v[56:59], a[180:183], a[152:155], v[56:59]\n\t" "ds_read_b64_tr_b16 a[174:175], v31 offset:32768\n\t"
    "v_mfma_f32_16x16x32_bf16 v[64:67], a[184:187], a[152:155], v[64:67]\n\t" "ds_read_b64_tr_b16 a[176:177], v20 offset:36864\n\t" "global_load_dwordx4 a[112:115], v0, s[40:41] offset:0 sc1\n\t"
    "v_mfma_f32_16x16x32_bf16 v[68:71], a[184:187], a[156:159], v[68:71]\n\t" "ds_read_b64_tr_b16 a[178:179], v28 offset:36864\n\t"
    "v_mfma_f32_16x16x32_bf16 v[76:79], a[188:191], a[156:159], v[76:79]\n\t" "ds_read_b64_tr_b16 a[180:181], v21 offset:36864\n\t"
    "v_mfma_f32_16x16x32_bf16 v[72:75], a[188:191], a[152:155], v[72:75]\n\t" "ds_read_b64_tr_b16 a[182:183], v29 offset:36864\n\t" "s_waitcnt lgkmcnt(4) vmcnt(11)\n\t"
    "v_mfma_f32_16x16x32_bf16 v[48:51], a[160:163], a[64:67], v[48:51]\n\t" "ds_read_b64_tr_b16 a[184:185], v22 offset:36864\n\t" "global_load_dwordx4 a[116:119], v0, s[40:41] offset:1024 sc1\n\t"
    "v_mfma_f32_16x16x32_bf16 v[52:55], a[160:163], a[68:71], v[52:55]\n\t" "ds_read_b64_tr_b16 a[186:187], v30 offset:36864\n\t" "v_mfma_f32_16x16x32_bf16 v[60:63], a[164:167], a[68:71], v[60:63]\n\t"
    "ds_read_b64_tr_b16 a[188:189], v23 offset:36864\n\t" "v_mfma_f32_16x16x32_bf16 v[56:59], a[164:167], a[64:67], v[56:59]\n\t" "ds_read_b64_tr_b16 a[190:191], v31 offset:36864\n\t"
    "v_mfma_f32_16x16x32_bf16 v[64:67], a[168:171], a[64:67], v[64:67]\n\t" "ds_read_b64_tr_b16 a[160:161], v20 offset:40960\n\t" "global_load_dwordx4 a[120:123], v0, s[40:41] offset:2048 sc1\n\t"
    "v_mfma_f32_16x16x32_bf16 v[68:71], a[168:171], a[68:71], v[68:71]\n\t" "ds_read_b64_tr_b16 a[162:163], v28 offset:40960\n\t" "v_mfma_f32_16x16x32_bf16 v[76:79], a[172:175], a[68:71], v[76:79]\n\t"
    "ds_read_b64_tr_b16 a[164:165], v21 offset:40960\n\t" "v_mfma_f32_16x16x32_bf16 v[72:75], a[172:175], a[64:67], v[72:75]\n\t" "ds_read_b64_tr_b16 a[166:167], v29 offset:40960\n\t"
    "s_waitcnt lgkmcnt(4) vmcnt(11)\n\t" "v_mfma_f32_16x16x32_bf16 v[48:51], a[176:179], a[72:75], v[48:51]\n\t" "ds_read_b64_tr_b16 a[168:169], v22 offset:40960\n\t"
    "global_load_dwordx4 a[124:127], v0, s[40:41] offset:3072 sc1\n\t" "v_mfma_f32_16x16x32_bf16 v[52:55], a[176:179], a[76:79], v[52:55]\n\t" "ds_read_b64_tr_b16 a[170:171], v30 offset:40960\n\t"
    "s_add_u32 s40, s40, 8192\n\t" "s_addc_u32 s41, s41, 0\n\t" "v_mfma_f32_16x16x32_bf16 v[60:63], a[180:183], a[76:79], v[60:63]\n\t" "ds_read_b64_tr_b16 a[172:173], v23 offset:40960\n\t"
    "s_mov_b64 s[78:79], %[sb]\n\t" "s_mov_b32 s82, -1\n\t" "s_mov_b32 s83, 0\n\t" "s_add_u32 m0, %[sl], 86016\n\t" "s_nop 0\n\t" "global_load_lds_dwordx4 v1, s[78:79]\n\t"
    "v_mfma_f32_16x16x32_bf16 v[56:59], a[180:183], a[72:75], v[56:59]\n\t" "ds_read_b64_tr_b16 a[174:175], v31 offset:40960\n\t" "v_mfma_f32_16x16x32_bf16 v[64:67], a[184:187], a[72:75], v[64:67]\n\t"
    "ds_read_b64_tr_b16 a[176:177], v20 offset:45056\n\t" "v_mfma_f32_16x16x32_bf16 v[68:71], a[184:187], a[76:79], v[68:71]\n\t" "ds_read_b64_tr_b16 a[178:179], v28 offset:45056\n\t"
    "v_mfma_f32_16x16x32_bf16 v[76:79], a[188:191], a[76:79], v[76:79]\n\t" "ds_read_b64_tr_b16 a[180:181], v21 offset:45056\n\t" "s_add_u32 m0, %[sl], 87040\n\t" "s_nop 0\n\t"
    "global_load_lds_dwordx4 v2, s[78:79]\n\t" "v_mfma_f32_16x16x32_bf16 v[72:75], a[188:191], a[72:75], v[72:75]\n\t" "ds_read_b64_tr_b16 a[182:183], v29 offset:45056\n\t"
    "s_waitcnt lgkmcnt(4) vmcnt(12)\n\t" "v_mfma_f32_16x16x32_bf16 v[48:51], a[160:163], a[80:83], v[48:51]\n\t" "ds_read_b64_tr_b16 a[184:185], v22 offset:45056\n\t"
    "v_mfma_f32_16x16x32_bf16 v[52:55], a[160:163], a[84:87], v[52:55]\n\t" "ds_read_b64_tr_b16 a[186:187], v30 offset:45056\n\t" "v_mfma_f32_16x16x32_bf16 v[60:63], a[164:167], a[84:87], v[60:63]\n\t"
    "ds_read_b64_tr_b16 a[188:189], v23 offset:45056\n\t" "s_add_u32 m0, %[sl], 88064\n\t" "s_nop 0\n\t" "global_load_lds_dwordx4 v3, s[78:79]\n\t"
    "v_mfma_f32_16x16x32_bf16 v[56:59], a[164:167], a[80:83], v[56:59]\n\t" "ds_read_b64_tr_b16 a[190:191], v31 offset:45056\n\t" "v_mfma_f32_16x16x32_bf16 v[64:67], a[168:171], a[80:83], v[64:67]\n\t"
    "ds_read_b64_tr_b16 a[160:161], v20 offset:49152\n\t" "v_mfma_f32_16x16x32_bf16 v[68:71], a[168:171], a[84:87], v[68:71]\n\t" "ds_read_b64_tr_b16 a[162:163], v28 offset:49152\n\t"
    "v_mfma_f32_16x16x32_bf16 v[76:79], a[172:175], a[84:87], v[76:79]\n\t" "ds_read_b64_tr_b16 a[164:165], v21 offset:49152\n\t" "s_add_u32 m0, %[sl], 89088\n\t" "s_nop 0\n\t"
    "s_mov_b64 exec, s[82:83]\n\t" "global_load_lds_dwordx4 v4, s[78:79]\n\t" "s_mov_b64 exec, -1\n\t" "s_add_u32 s78, s78, 229376\n\t" "s_addc_u32 s79, s79, 0\n\t"
    "v_mfma_f32_16x16x32_bf16 v[72:75], a[172:175], a[80:83], v[72:75]\n\t" "ds_read_b64_tr_b16 a[166:167], v29 offset:49152\n\t" "s_waitcnt lgkmcnt(4) vmcnt(12)\n\t"
    "v_mfma_f32_16x16x32_bf16 v[48:51], a[176:179], a[88:91], v[48:51]\n\t" "ds_read_b64_tr_b16 a[168:169], v22 offset:49152\n\t" "v_mfma_f32_16x16x32_bf16 v[52:55], a[176:179], a[92:95], v[52:55]\n\t"
    "ds_read_b64_tr_b16 a[170:171], v30 offset:49152\n\t" "v_mfma_f32_16x16x32_bf16 v[60:63], a[180:183], a[92:95], v[60:63]\n\t" "ds_read_b64_tr_b16 a[172:173], v23 offset:49152\n\t"
    "s_add_u32 m0, %[sl], 100352\n\t" "s_nop 0\n\t" "global_load_lds_dwordx4 v1, s[78:79]\n\t" "v_mfma_f32_16x16x32_bf16 v[56:59], a[180:183], a[88:91], v[56:59]\n\t"
    "ds_read_b64_tr_b16 a[174:175], v31 offset:49152\n\t" "v_mfma_f32_16x16x32_bf16 v[64:67], a[184:187], a[88:91], v[64:67]\n\t" "ds_read_b64_tr_b16 a[176:177], v20 offset:53248\n\t"
    "v_mfma_f32_16x16x32_bf16 v[68:71], a[184:187], a[92:95], v[68:71]\n\t" "ds_read_b64_tr_b16 a[178:179], v28 offset:53248\n\t" "v_mfma_f32_16x16x32_bf16 v[76:79], a[188:191], a[92:95], v[76:79]\n\t"
    "ds_read_b64_tr_b16 a[180:181], v21 offset:53248\n\t" "s_add_u32 m0, %[sl], 101376\n\t" "s_nop 0\n\t" "global_load_lds_dwordx4 v2, s[78:79]\n\t"
    "v_mfma_f32_16x16x32_bf16 v[72:75], a[188:191], a[88:91], v[72:75]\n\t" "ds_read_b64_tr_b16 a[182:183], v29 offset:53248\n\t" "s_waitcnt lgkmcnt(4) vmcnt(12)\n\t"
    "v_mfma_f32_16x16x32_bf16 v[48:51], a[160:163], a[96:99], v[48:51]\n\t" "ds_read_b64_tr_b16 a[184:185], v22 offset:53248\n\t" "v_mfma_f32_16x16x32_bf16 v[52:55], a[160:163], a[100:103], v[52:55]\n\t"
    "ds_read_b64_tr_b16 a[186:187], v30 offset:53248\n\t" "v_mfma_f32_16x16x32_bf16 v[60:63], a[164:167], a[100:103], v[60:63]\n\t" "ds_read_b64_tr_b16 a[188:189], v23 offset:53248\n\t"
    "s_add_u32 m0, %[sl], 102400\n\t" "s_nop 0\n\t" "global_load_lds_dwordx4 v3, s[78:79]\n\t" "v_mfma_f32_16x16x32_bf16 v[56:59], a[164:167], a[96:99], v[56:59]\n\t"
    "ds_read_b64_tr_b16 a[190:191], v31 offset:53248\n\t" "v_mfma_f32_16x16x32_bf16 v[64:67], a[168:171], a[96:99], v[64:67]\n\t" "ds_read_b64_tr_b16 a[160:161], v20 offset:57344\n\t"
    "v_mfma_f32_16x16x32_bf16 v[68:71], a[168:171], a[100:103], v[68:71]\n\t" "ds_read_b64_tr_b16 a[162:163], v28 offset:57344\n\t"
    "v_mfma_f32_16x16x32_bf16 v[76:79], a[172:175], a[100:103], v[76:79]\n\t" "ds_read_b64_tr_b16 a[164:165], v21 offset:57344\n\t" "s_add_u32 m0, %[sl], 103424\n\t" "s_nop 0\n\t"
    "s_mov_b64 exec, s[82:83]\n\t" "global_load_lds_dwordx4 v4, s[78:79]\n\t" "s_mov_b64 exec, -1\n\t" "s_add_u32 s78, s78, 229376\n\t" "s_addc_u32 s79, s79, 0\n\t"
    "v_mfma_f32_16x16x32_bf16 v[72:75], a[172:175], a[96:99], v[72:75]\n\t" "ds_read_b64_tr_b16 a[166:167], v29 offset:57344\n\t" "s_waitcnt lgkmcnt(4) vmcnt(12)\n\t"
    "v_mfma_f32_16x16x32_bf16 v[48:51], a[176:179], a[104:107], v[48:51]\n\t" "ds_read_b64_tr_b16 a[168:169], v22 offset:57344\n\t"
    "v_mfma_f32_16x16x32_bf16 v[52:55], a[176:179], a[108:111], v[52:55]\n\t" "ds_read_b64_tr_b16 a[170:171], v30 offset:57344\n\t"
    "v_mfma_f32_16x16x32_bf16 v[60:63], a[180:183], a[108:111], v[60:63]\n\t" "ds_read_b64_tr_b16 a[172:173], v23 offset:57344\n\t"
    "v_mfma_f32_16x16x32_bf16 v[56:59], a[180:183], a[104:107], v[56:59]\n\t" "ds_read_b64_tr_b16 a[174:175], v31 offset:57344\n\t"
    "v_mfma_f32_16x16x32_bf16 v[64:67], a[184:187], a[104:107], v[64:67]\n\t" "ds_read_b64_tr_b16 a[176:177], v20 offset:61440\n\t"
    "v_mfma_f32_16x16x32_bf16 v[68:71], a[184:187], a[108:111], v[68:71]\n\t" "ds_read_b64_tr_b16 a[178:179], v28 offset:61440\n\t"
    "v_mfma_f32_16x16x32_bf16 v[76:79], a[188:191], a[108:111], v[76:79]\n\t" "ds_read_b64_tr_b16 a[180:181], v21 offset:61440\n\t"
    "v_mfma_f32_16x16x32_bf16 v[72:75], a[188:191], a[104:107], v[72:75]\n\t" "ds_read_b64_tr_b16 a[182:183], v29 offset:61440\n\t" "s_waitcnt lgkmcnt(4) vmcnt(10)\n\t"
    "v_mfma_f32_16x16x32_bf16 v[48:51], a[160:163], a[112:115], v[48:51]\n\t" "ds_read_b64_tr_b16 a[184:185], v22 offset:61440\n\t"
    "v_mfma_f32_16x16x32_bf16 v[52:55], a[160:163], a[116:119], v[52:55]\n\t" "ds_read_b64_tr_b16 a[186:187], v30 offset:61440\n\t"
    "v_mfma_f32_16x16x32_bf16 v[60:63], a[164:167], a[116:119], v[60:63]\n\t" "ds_read_b64_tr_b16 a[188:189], v23 offset:61440\n\t"
    "v_mfma_f32_16x16x32_bf16 v[56:59], a[164:167], a[112:115], v[56:59]\n\t" "ds_read_b64_tr_b16 a[190:191], v31 offset:61440\n\t"
    "v_mfma_f32_16x16x32_bf16 v[64:67], a[168:171], a[112:115], v[64:67]\n\t" "v_mfma_f32_16x16x32_bf16 v[68:71], a[168:171], a[116:119], v[68:71]\n\t"
    "v_mfma_f32_16x16x32_bf16 v[76:79], a[172:175], a[116:119], v[76:79]\n\t" "v_mfma_f32_16x16x32_bf16 v[72:75], a[172:175], a[112:115], v[72:75]\n\t" "s_waitcnt lgkmcnt(0) vmcnt(8)\n\t"
    "v_mfma_f32_16x16x32_bf16 v[48:51], a[176:179], a[120:123], v[48:51]\n\t" "v_mfma_f32_16x16x32_bf16 v[52:55], a[176:179], a[124:127], v[52:55]\n\t"
    "v_mfma_f32_16x16x32_bf16 v[60:63], a[180:183], a[124:127], v[60:63]\n\t" "v_mfma_f32_16x16x32_bf16 v[56:59], a[180:183], a[120:123], v[56:59]\n\t"
    "v_mfma_f32_16x16x32_bf16 v[64:67], a[184:187], a[120:123], v[64:67]\n\t" "v_mfma_f32_16x16x32_bf16 v[68:71], a[184:187], a[124:127], v[68:71]\n\t"
    "v_mfma_f32_16x16x32_bf16 v[76:79], a[188:191], a[124:127], v[76:79]\n\t" "v_mfma_f32_16x16x32_bf16 v[72:75], a[188:191], a[120:123], v[72:75]\n\t" "s_nop 1\n\t"
    "v_pk_fma_f32 v[48:49], v[124:125], v[48:49], v[80:81]\n\t" "v_pk_fma_f32 v[50:51], v[126:127], v[50:51], v[82:83]\n\t" "v_pk_fma_f32 v[56:57], v[128:129], v[56:57], v[88:89]\n\t"
    "v_pk_fma_f32 v[58:59], v[130:131], v[58:59], v[90:91]\n\t" "v_pk_mul_f32 v[156:157], v[48:49], v[140:141]\n\t" "v_pk_mul_f32 v[158:159], v[50:51], v[142:143]\n\t"
    "v_pk_mul_f32 v[160:161], v[56:57], v[144:145]\n\t" "v_pk_mul_f32 v[162:163], v[58:59], v[146:147]\n\t" "v_cvt_pk_bf16_f32 v164, v156, v157\n\t" "v_cvt_pk_bf16_f32 v165, v158, v159\n\t"
    "v_cvt_pk_bf16_f32 v166, v160, v161\n\t" "v_cvt_pk_bf16_f32 v167, v162, v163\n\t" "global_store_dwordx4 v0, v[164:167], %[sy0] offset:0 sc1\n\t"
    "v_pk_fma_f32 v[52:53], v[124:125], v[52:53], v[84:85]\n\t" "v_pk_fma_f32 v[54:55], v[126:127], v[54:55], v[86:87]\n\t" "v_pk_fma_f32 v[60:61], v[128:129], v[60:61], v[92:93]\n\t"
    "v_pk_fma_f32 v[62:63], v[130:131], v[62:63], v[94:95]\n\t" "v_pk_mul_f32 v[172:173], v[52:53], v[140:141]\n\t" "v_pk_mul_f32 v[174:175], v[54:55], v[142:143]\n\t"
    "v_pk_mul_f32 v[176:177], v[60:61], v[144:145]\n\t" "v_pk_mul_f32 v[178:179], v[62:63], v[146:147]\n\t" "v_cvt_pk_bf16_f32 v180, v172, v173\n\t" "v_cvt_pk_bf16_f32 v181, v174, v175\n\t"
    "v_cvt_pk_bf16_f32 v182, v176, v177\n\t" "v_cvt_pk_bf16_f32 v183, v178, v179\n\t" "global_store_dwordx4 v0, v[180:183], %[sy0] offset:1024 sc1\n\t"
    "v_pk_fma_f32 v[64:65], v[132:133], v[64:65], v[96:97]\n\t" "v_pk_fma_f32 v[66:67], v[134:135], v[66:67], v[98:99]\n\t" "v_pk_fma_f32 v[72:73], v[136:137], v[72:73], v[104:105]\n\t"
    "v_pk_fma_f32 v[74:75], v[138:139], v[74:75], v[106:107]\n\t" "v_pk_mul_f32 v[156:157], v[64:65], v[148:149]\n\t" "v_pk_mul_f32 v[158:159], v[66:67], v[150:151]\n\t"
    "v_pk_mul_f32 v[160:161], v[72:73], v[152:153]\n\t" "v_pk_mul_f32 v[162:163], v[74:75], v[154:155]\n\t" "v_cvt_pk_bf16_f32 v168, v156, v157\n\t" "v_cvt_pk_bf16_f32 v169, v158, v159\n\t"
    "v_cvt_pk_bf16_f32 v170, v160, v161\n\t" "v_cvt_pk_bf16_f32 v171, v162, v163\n\t" "global_store_dwordx4 v0, v[168:171], %[sy1] offset:0 sc1\n\t"
    "v_pk_fma_f32 v[68:69], v[132:133], v[68:69], v[100:101]\n\t" "v_pk_fma_f32 v[70:71], v[134:135], v[70:71], v[102:103]\n\t" "v_pk_fma_f32 v[76:77], v[136:137], v[76:77], v[108:109]\n\t"
    "v_pk_fma_f32 v[78:79], v[138:139], v[78:79], v[110:111]\n\t" "v_pk_mul_f32 v[172:173], v[68:69], v[148:149]\n\t" "v_pk_mul_f32 v[174:175], v[70:71], v[150:151]\n\t"
    "v_pk_mul_f32 v[176:177], v[76:77], v[152:153]\n\t" "v_pk_mul_f32 v[178:179], v[78:79], v[154:155]\n\t" "v_cvt_pk_bf16_f32 v184, v172, v173\n\t" "v_cvt_pk_bf16_f32 v185, v174, v175\n\t"
    "v_cvt_pk_bf16_f32 v186, v176, v177\n\t" "v_cvt_pk_bf16_f32 v187, v178, v179\n\t" "global_store_dwordx4 v0, v[184:187], %[sy1] offset:1024 sc1\n\t" "v_pk_mul_f32 v[114:115], v[48:49], v[48:49]\n\t"
    "v_pk_mul_f32 v[118:119], v[52:53], v[52:53]\n\t" "v_pk_add_f32 v[112:113], v[50:51], v[48:49]\n\t" "v_pk_fma_f32 v[114:115], v[50:51], v[50:51], v[114:115]\n\t"
    "v_pk_add_f32 v[116:117], v[54:55], v[52:53]\n\t" "v_pk_fma_f32 v[118:119], v[54:55], v[54:55], v[118:119]\n\t" "v_pk_add_f32 v[112:113], v[56:57], v[112:113]\n\t"
    "v_pk_fma_f32 v[114:115], v[56:57], v[56:57], v[114:115]\n\t" "v_pk_add_f32 v[116:117], v[60:61], v[116:117]\n\t" "v_pk_fma_f32 v[118:119], v[60:61], v[60:61], v[118:119]\n\t"
    "v_pk_add_f32 v[112:113], v[58:59], v[112:113]\n\t" "v_pk_fma_f32 v[114:115], v[58:59], v[58:59], v[114:115]\n\t" "v_pk_add_f32 v[116:117], v[62:63], v[116:117]\n\t"
    "v_pk_fma_f32 v[118:119], v[62:63], v[62:63], v[118:119]\n\t" "v_pk_add_f32 v[112:113], v[64:65], v[112:113]\n\t" "v_pk_fma_f32 v[114:115], v[64:65], v[64:65], v[114:115]\n\t"
    "v_pk_add_f32 v[116:117], v[68:69], v[116:117]\n\t" "v_pk_fma_f32 v[118:119], v[68:69], v[68:69], v[118:119]\n\t" "v_pk_add_f32 v[112:113], v[66:67], v[112:113]\n\t"
    "v_pk_fma_f32 v[114:115], v[66:67], v[66:67], v[114:115]\n\t" "v_pk_add_f32 v[116:117], v[70:71], v[116:117]\n\t" "v_pk_fma_f32 v[118:119], v[70:71], v[70:71], v[118:119]\n\t"
    "v_pk_add_f32 v[112:113], v[72:73], v[112:113]\n\t" "v_pk_fma_f32 v[114:115], v[72:73], v[72:73], v[114:115]\n\t" "v_pk_add_f32 v[116:117], v[76:77], v[116:117]\n\t"
    "v_pk_fma_f32 v[118:119], v[76:77], v[76:77], v[118:119]\n\t" "v_pk_add_f32 v[112:113], v[74:75], v[112:113]\n\t" "v_pk_fma_f32 v[114:115], v[74:75], v[74:75], v[114:115]\n\t"
    "v_pk_add_f32 v[116:117], v[78:79], v[116:117]\n\t" "v_pk_fma_f32 v[118:119], v[78:79], v[78:79], v[118:119]\n\t" "v_add_f32 v188, v112, v113\n\t" "v_add_f32 v189, v114, v115\n\t"
    "v_add_f32 v190, v116, v117\n\t" "v_add_f32_e64 v191, v118, v119\n\t" "v_pk_add_f32 v[48:49], v[48:49], v[236:237] op_sel:[0,0] op_sel_hi:[1,0]\n\t"
    "v_pk_add_f32 v[50:51], v[50:51], v[236:237] op_sel:[0,0] op_sel_hi:[1,0]\n\t" "v_permlane16_swap_b32 v188, v190\n\t" "v_permlane16_swap_b32 v189, v191\n\t" "v_add_f32 v188, v188, v190\n\t"
    "v_add_f32 v189, v189, v191\n\t" "v_mov_b32 v190, v188\n\t" "v_mov_b32 v191, v189\n\t" "v_pk_add_f32 v[56:57], v[56:57], v[236:237] op_sel:[0,0] op_sel_hi:[1,0]\n\t"
    "v_pk_add_f32 v[58:59], v[58:59], v[236:237] op_sel:[0,0] op_sel_hi:[1,0]\n\t" "v_permlane32_swap_b32 v188, v190\n\t" "v_permlane32_swap_b32 v189, v191\n\t" "v_add_f32 v188, v188, v190\n\t"
    "v_add_f32_e64 v189, v189, v191\n\t" "s_mov_b32 exec_hi, 0\n\t" "global_store_dwordx2 v34, v[188:189], %[ss] sc1\n\t" "s_mov_b64 exec, -1\n\t"
    "v_pk_add_f32 v[64:65], v[64:65], v[236:237] op_sel:[0,0] op_sel_hi:[1,0]\n\t" "v_pk_add_f32 v[66:67], v[66:67], v[236:237] op_sel:[0,0] op_sel_hi:[1,0]\n\t"
    "v_pk_add_f32 v[72:73], v[72:73], v[236:237] op_sel:[0,0] op_sel_hi:[1,0]\n\t" "v_pk_add_f32 v[74:75], v[74:75], v[236:237] op_sel:[0,0] op_sel_hi:[1,0]\n\t"
    "v_pk_add_f32 v[52:53], v[52:53], v[236:237] op_sel:[0,1] op_sel_hi:[1,1]\n\t" "v_pk_add_f32 v[54:55], v[54:55], v[236:237] op_sel:[0,1] op_sel_hi:[1,1]\n\t"
    "v_pk_add_f32 v[60:61], v[60:61], v[236:237] op_sel:[0,1] op_sel_hi:[1,1]\n\t" "v_pk_add_f32 v[62:63], v[62:63], v[236:237] op_sel:[0,1] op_sel_hi:[1,1]\n\t"
    "v_pk_add_f32 v[68:69], v[68:69], v[236:237] op_sel:[0,1] op_sel_hi:[1,1]\n\t" "v_pk_add_f32 v[70:71], v[70:71], v[236:237] op_sel:[0,1] op_sel_hi:[1,1]\n\t"
    "v_pk_add_f32 v[76:77], v[76:77], v[236:237] op_sel:[0,1] op_sel_hi:[1,1]\n\t" "v_pk_add_f32 v[78:79], v[78:79], v[236:237] op_sel:[0,1] op_sel_hi:[1,1]\n\t" "v_cvt_pk_bf16_f32 v80, v48, v49\n\t"
    "v_cvt_pk_bf16_f32 v81, v50, v51\n\t" "v_cvt_pk_bf16_f32 v82, v56, v57\n\t" "v_cvt_pk_bf16_f32 v83, v58, v59\n\t" "v_cvt_pk_bf16_f32 v84, v64, v65\n\t" "v_cvt_pk_bf16_f32 v85, v66, v67\n\t"
    "v_cvt_pk_bf16_f32 v86, v72, v73\n\t" "v_cvt_pk_bf16_f32 v87, v74, v75\n\t" "v_cvt_pk_bf16_f32 v88, v52, v53\n\t" "v_cvt_pk_bf16_f32 v89, v54, v55\n\t" "v_cvt_pk_bf16_f32 v90, v60, v61\n\t"
    "v_cvt_pk_bf16_f32 v91, v62, v63\n\t" "v_cvt_pk_bf16_f32 v92, v68, v69\n\t" "v_cvt_pk_bf16_f32 v93, v70, v71\n\t" "v_cvt_pk_bf16_f32 v94, v76, v77\n\t" "v_cvt_pk_bf16_f32 v95, v78, v79\n\t"
    "s_branch .Lend_%=\n\t" ".Lslow_%=:\n\t" "s_sleep 2\n\t" "global_load_dwordx4 v[112:115], v0, %[sg0] sc1\n\t" "global_load_dwordx4 v[116:119], v0, %[sg1] sc1\n\t"
    "global_load_dwordx4 v[120:123], v0, %[sg2] sc1\n\t" "s_waitcnt vmcnt(0)\n\t" "v_xad_u32 v156, v112, v113, v164\n\t" "v_xad_u32 v157, v116, v117, v164\n\t" "v_xad_u32 v158, v120, v121, v164\n\t"
    "v_xad_u32 v156, v114, v113, v156\n\t" "v_xad_u32 v157, v118, v117, v157\n\t" "v_xad_u32 v158, v122, v121, v158\n\t" "v_xor_b32_e64 v156, v156, v115\n\t"
    "v_bitop3_b32 v156, v156, v157, v119 bitop3:0xf6\n\t" "v_bitop3_b32 v156, v156, v158, v123 bitop3:0xf6\n\t" "v_cmp_ne_u32_e64 vcc, 0, v156\n\t" "v_perm_b32 v115, v112, v113, s49\n\t"
    "v_perm_b32 v119, v116, v117, s49\n\t" "v_perm_b32 v123, v120, v121, s49\n\t" "s_nop 1\n\t" "s_cbranch_vccz .Lgo_%=\n\t" "s_sub_u32 s44, s44, 1\n\t" "s_cbranch_scc0 .Lslow_%=\n\t"
    "s_branch .Lgo_%=\n\t" ".Lend_%=:\n\t"
    : "+{v[80:83]}"(xr[0]), "+{v[84:87]}"(xr[1]), "+{v[88:91]}"(xr[2]), "+{v[92:95]}"(xr[3]), "+{v[96:99]}"(xr[4]), "+{v[100:103]}"(xr[5]), "+{v[104:107]}"(xr[6]), "+{v[108:111]}"(xr[7])
    : "{v0}"(va), "{v15}"(tw), "{v32}"(tr), "{v33}"(ve), "{v34}"(vs), "{v236}"(mu[0]), "{v237}"(mu[1]), "{v1}"(vb[0]), "{v2}"(vb[1]), "{v3}"(vb[2]), "{v4}"(vb[3]), "{v16}"(vr[0]), "{v17}"(vr[1]), "{v18}"(vr[2]), "{v19}"(vr[3]), "{v20}"(vr[4]), "{v21}"(vr[5]), "{v22}"(vr[6]), "{v23}"(vr[7]), "{v24}"(vr[8]), "{v25}"(vr[9]), "{v26}"(vr[10]), "{v27}"(vr[11]), "{v28}"(vr[12]), "{v29}"(vr[13]), "{v30}"(vr[14]), "{v31}"(vr[15]), [sa] "s"(sa + 8192), [tag] "s"(tag), [role] "s"(role), [sg0] "s"(sg0), [sg1] "s"(sg1), [sg2] "s"(sg2), [se] "s"(se), [sy0] "s"(sy0), [sy1] "s"(sy1), [ss] "s"(ss), [sb] "s"(sb), [sl] "s"(sl), [cd] "s"(cd)
    : "v48", "v49", "v50", "v51", "v52", "v53", "v54", "v55", "v56", "v57", "v58", "v59", "v60", "v61", "v62", "v63", "v64", "v65", "v66", "v67", "v68", "v69", "v70", "v71", "v72", "v73", "v74", "v75", "v76", "v77", "v78", "v79", "v112", "v113", "v114", "v115", "v116", "v117", "v118", "v119", "v120", "v121", "v122", "v123", "v124", "v125", "v126", "v127", "v128", "v129", "v130", "v131", "v132", "v133", "v134", "v135", "v136", "v137", "v138", "v139", "v140", "v141", "v142", "v143", "v144", "v145", "v146", "v147", "v148", "v149", "v150", "v151", "v152", "v153", "v154", "v155", "v156", "v157", "v158", "v159", "v160", "v161", "v162", "v163", "v164", "v165", "v166", "v167", "v168", "v169", "v170", "v171", "v172", "v173", "v174", "v175", "v176", "v177", "v178", "v179", "v180", "v181", "v182", "v183", "v184", "v185", "v186", "v187", "v188", "v189", "v190", "v191", "v192", "v193", "v194", "v195", "a0", "a1", "a2", "a3", "a4", "a5", "a6", "a7", "a8", "a9", "a10", "a11", "a12", "a13", "a14", "a15", "a16", "a17", "a18", "a19", "a20", "a21", "a22", "a23", "a24", "a25", "a26", "a27", "a28", "a29", "a30", "a31", "a32", "a33", "a34", "a35", "a36", "a37", "a38", "a39", "a40", "a41", "a42", "a43", "a44", "a45", "a46", "a47", "a48", "a49", "a50", "a51", "a52", "a53", "a54", "a55", "a56", "a57", "a58", "a59", "a60", "a61", "a62", "a63", "a64", "a65", "a66", "a67", "a68", "a69", "a70", "a71", "a72", "a73", "a74", "a75", "a76", "a77", "a78", "a79", "a80", "a81", "a82", "a83", "a84", "a85", "a86", "a87", "a88", "a89", "a90", "a91", "a92", "a93", "a94", "a95", "a96", "a97", "a98", "a99", "a100", "a101", "a102", "a103", "a104", "a105", "a106", "a107", "a108", "a109", "a110", "a111", "a112", "a113", "a114", "a115", "a116", "a117", "a118", "a119", "a120", "a121", "a122", "a123", "a124", "a125", "a126", "a127", "a128", "a129", "a130", "a131", "a132", "a133", "a134", "a135", "a136", "a137", "a138", "a139", "a140", "a141", "a142", "a143", "a144", "a145", "a146", "a147", "a148", "a149", "a150", "a151", "a152", "a153", "a154", "a155", "a156", "a157", "a158", "a159", "a160", "a161", "a162", "a163", "a164", "a165", "a166", "a167", "a168", "a169", "a170", "a171", "a172", "a173", "a174", "a175", "a176", "a177", "a178", "a179", "a180", "a181", "a182", "a183", "a184", "a185", "a186", "a187", "a188", "a189", "a190", "a191", "s40", "s41", "s42", "s43", "s44", "s45", "s46", "s47", "s48", "s49", "s50", "s78", "s79", "s82", "s83", "vcc", "scc", "memory");
  }
  asm volatile("" : "+s"(bx), "+s"(wid), "+s"(ws)); asm volatile("" : "+s"(tag)); asm volatile("" : "+v"(tid));
  {
  const int xcd = bx & 7, loc = bx >> 3, lane = tid & 63, cb = (xcd >> 1) * 8 + (loc >> 2);
  GW();
  auto grp = [&](int i) { return i == 3 ? 3 : ((i == 0 ? 0x50 : i == 1 ? 0xa9 : 0x06) >> 2 * wid) & 3; };
  unsigned vr[8], gt[4], go[4]; u32x4 tmp;
  const unsigned va = lane * 16, ct = ldsb + (lane < 16 ? TAB + lane * 16 : ZB), zb = ldsb + ZB + tid * 4, xw = ldsb + 24576 + tid * 16;
#pragma unroll
  for (int m = 0; m < 4; ++m) {
    vr[2 * m] = ldsb + GB + (lane >> 2) * 224 + (lane & 3) * 8 + (m < 3 ? grp(m) * 64 + (cb & 1) * 32 : (cb & 1) ? 0 : 192); vr[2 * m + 1] = vr[2 * m] + 4 * 14336;
    gt[m] = grp(m) * 1024;
    const int bk = (cb >> 1) & 7, gm = grp(m);
    go[m] = !EAR ? bk * 7 * 2048 + (m < 3 ? ((cb & 1) * 4 + gm) * 2048 : 3 * 2048 + (cb & 1) * 1024) : m == 3 ? (18 + 5 * bk) * 2048 + (cb & 1) * 1024 : (m == 0 ? 2 * bk + (cb & 1) : 16 + 5 * bk + (cb & 1) * 3 + (gm >> 1)) * 2048;
  }
  const unsigned vs = (unsigned)((lane >> 4) * 4 * S_ + (lane & 15)) * 8, ve = (lane & 1) * 64 + (lane >> 4) * 16, vbd = f_vb(wid, lane);
  const unsigned cr0 = ldsb + RAW + (tid >> 2) * 144 + (tid & 3) * 4, cw = ldsb + TAB + (tid >> 5) * 256 + (tid & 15) * 16 + ((tid >> 4) & 1) * 8;
  const char* sf = ws + (unsigned)(W_TAGS + YF * 8 + FA(((xcd >> 1) * 4 + (loc & 3)) * 16 + (xcd & 1) * 8 + (loc >> 2)));
#define PINS0 "v"(vr[1]), "v"(vr[3]), "v"(vr[5]), "v"(vr[7]), "v"(ct), "v"(vs), "v"(ve), "v"(vbd), "v"(cr0), "v"(cw), "s"(gt[0]), "s"(gt[1]), "s"(gt[2]), "s"(go[0]), "s"(go[1]), "s"(go[2]), "s"(go[3])
  asm volatile("s_mov_b64 s[78:79], %[sb]\n\t" "s_mov_b32 s82, -1\n\t" "s_mov_b32 s83, 0\n\t" "v_mov_b64 v[192:193], %[tag]\n\t" "v_mov_b32 v194, 0\n\t" "s_waitcnt vmcnt(0)\n\t" "s_barrier\n\t"
    "s_cmp_eq_u32 %[wid], 0\n\t" "s_cselect_b64 exec, 1, 0\n\t" "global_store_dwordx2 v194, v[192:193], %[sf] sc1\n\t" "s_mov_b64 exec, -1\n\t" "ds_write_b32 v36, v194\n\t"
    "ds_write_b128 v37, v[80:83] offset:0\n\t" "ds_write_b128 v37, v[84:87] offset:4096\n\t" "ds_write_b128 v37, v[88:91] offset:8192\n\t" "ds_write_b128 v37, v[92:95] offset:12288\n\t"
    "s_add_u32 m0, %[sl], 0\n\t" "s_nop 0\n\t" "global_load_lds_dwordx4 v1, s[78:79]\n\t" "s_add_u32 m0, %[sl], 1024\n\t" "s_nop 0\n\t" "global_load_lds_dwordx4 v2, s[78:79]\n\t"
    "s_add_u32 m0, %[sl], 2048\n\t" "s_nop 0\n\t" "global_load_lds_dwordx4 v3, s[78:79]\n\t" "s_add_u32 m0, %[sl], 3072\n\t" "s_nop 0\n\t" "s_mov_b64 exec, s[82:83]\n\t"
    "global_load_lds_dwordx4 v4, s[78:79]\n\t" "s_mov_b64 exec, -1\n\t" "s_add_u32 s78, s78, 229376\n\t" "s_addc_u32 s79, s79, 0\n\t" "s_add_u32 m0, %[sl], 14336\n\t" "s_nop 0\n\t"
    "global_load_lds_dwordx4 v1, s[78:79]\n\t" "s_add_u32 m0, %[sl], 15360\n\t" "s_nop 0\n\t" "global_load_lds_dwordx4 v2, s[78:79]\n\t" "s_add_u32 m0, %[sl], 16384\n\t" "s_nop 0\n\t"
    "global_load_lds_dwordx4 v3, s[78:79]\n\t" "s_add_u32 m0, %[sl], 17408\n\t" "s_nop 0\n\t" "s_mov_b64 exec, s[82:83]\n\t" "global_load_lds_dwordx4 v4, s[78:79]\n\t" "s_mov_b64 exec, -1\n\t"
    "s_add_u32 s78, s78, 229376\n\t" "s_addc_u32 s79, s79, 0\n\t"
    : "=&{v[192:195]}"(tmp)
    : "{v[80:83]}"(xr[0]), "{v[84:87]}"(xr[1]), "{v[88:91]}"(xr[2]), "{v[92:95]}"(xr[3]), "{v36}"(zb), "{v37}"(xw), "{v1}"(vb[0]), "{v2}"(vb[1]), "{v3}"(vb[2]), "{v4}"(vb[3]), [sb] "s"(sb + 458752), [tag] "s"(tag), [wid] "s"(wid), [sf] "s"(sf), [sl] "s"(sl), PINS0
    : "s78", "s79", "s82", "s83", "scc", "memory");
  asm volatile("" : "+s"(bx), "+s"(wid), "+s"(ws)); asm volatile("" : "+v"(tid));
  { { const int xcd = bx & 7, loc = bx >> 3, lane = tid & 63, rot = xcd & 1, rb = (xcd & 1) * 4 + (loc & 3), cb = (xcd >> 1) * 8 + (loc >> 2);
    unsigned f0 = rb * 32 + lane, f1 = ((xcd >> 1) * 4 + (lane & 3)) * 16 + (lane & 4) * 2 + (loc >> 2); asm volatile("" : "+v"(f0), "+v"(f1));
    const unsigned vf = FA(lane < 32 ? f0 : f1) + W_TAGS + YF * 8, vp = ((lane & 1) * 4 + (lane >> 4)) * 144 + rb * 16, cr1 = cr0 + (wid < 3 ? 64 * 144 : 0), srl = ldsb + RAW + wid * 4096, sld = ldsb + wid * 1024;
    const char* sq = ws; const char* sa = ws + (unsigned)(W_Y + rb * 524288 + wid * YW + rot * 16 * YS); const char* scnt = ws + (unsigned)(W_TAGS + HF * 8 + FA(rb * 32 + cb));
    const char* se = ws + (unsigned)(W_EF + rb * 4096 + ((rb + 1) & 7) * 512);
    const char* ss = ws + (unsigned)(W_STATS + rb * 2048 + wid * 512); const char* su = ws + (unsigned)(W_UVP + cb * 16384);
    const size_t sh = (size_t)(ws + (unsigned)(W_HH + ((rb * 8 + wid * 2) * 112 + (cb >> 4) * 56) * 2048));
    const unsigned shl = (unsigned)sh, shh = (unsigned)(sh >> 32);
    const char* su0 = su + (unsigned)(grp(2) * 4608); const char* su6 = su + 6 * 2304; const char* sr = su + (unsigned)(wid * 4096);
    const unsigned long long m01 = wid < 3 ? 0x0003000300030003ull : 0ull, m6 = wid < 3 ? 0ull : 0x0003000300030003ull;
    const char* tp0 = (const char*)pfw + (unsigned)((bx * 4 + wid) * 7168); const char* sbd = (const char*)pfw + (unsigned)((xcd & 1) * 1792 * 2048 + ROT * 256 + EAR * (loc & 1) * 65536);
    const int rd = (xcd >> 1) * 4 + (loc & 3); const char* sw = ws + (unsigned)(W_TAGS + HF * 8 + FA((rd >> 1) * 32 + rot * 16)); const char* sd = ws + (unsigned)(W_HH + ((rd * 4 + wid) * 112 + rot * 56) * 2048 + 4096);
    const int wa = rot ? -31 * YS : YS, wb = rot ? -31 * 32 * 7168 : 32 * 7168, e1 = (rb & 1) ? -1 : (rb >> 1) & 1, e0 = (rb & 1) ? (rb >> 1) & 1 : -1;
    unsigned long long fl;
    asm volatile(".p2align 3\n\t" "s_mov_b32 s80, 0\n\t" "s_cmp_eq_u32 s80, 1\n\t" "s_cselect_b32 s86, %[wa], 16384\n\t" "s_ashr_i32 s87, s86, 31\n\t" "s_cmp_eq_u32 s80, 1\n\t" "s_cselect_b32 s88, %[wb], 229376\n\t"
    "s_ashr_i32 s89, s88, 31\n\t" "s_mov_b64 s[78:79], %[sb]\n\t" "s_mov_b32 s82, -1\n\t" "s_mov_b32 s83, 0\n\t" "s_mov_b32 s94, 0x10000\n\t" "s_mov_b64 s[84:85], %[tp0]\n\t"
    "s_mov_b64 s[76:77], %[sa]\n\t" "s_mov_b32 s81, 0x400000\n\t" "s_cmp_eq_u32 %[wid], 0\n\t" "s_cselect_b64 exec, -1, 0\n\t" "global_load_dwordx2 v[24:25], %[vf], %[sq] sc1\n\t" ".Lpy_%=:\n\t"
    "s_waitcnt vmcnt(0)\n\t" "v_cmp_ne_u64 vcc, %[tag], v[24:25]\n\t" "s_nop 4\n\t" "s_cbranch_vccz .Lry_%=\n\t" "s_mov_b64 exec, vcc\n\t" "s_sleep 1\n\t"
    "global_load_dwordx2 v[24:25], %[vf], %[sq] sc1\n\t" "s_sub_u32 s81, s81, 1\n\t" "s_cbranch_scc0 .Lpy_%=\n\t" ".Lry_%=:\n\t" "s_mov_b64 exec, -1\n\t" "s_barrier\n\t"
    "global_load_dwordx4 a[0:3], %[va], s[76:77] offset:0 sc1\n\t" "global_load_dwordx4 a[4:7], %[va], s[76:77] offset:1024 sc1\n\t" "global_load_dwordx4 a[8:11], %[va], s[76:77] offset:2048 sc1\n\t"
    "global_load_dwordx4 a[12:15], %[va], s[76:77] offset:3072 sc1\n\t" "s_add_u32 s76, s76, 16384\n\t" "s_addc_u32 s77, s77, 0\n\t" "ds_read_b64_tr_b16 a[128:129], %[vr1] offset:28672\n\t"
    "ds_read_b64_tr_b16 a[130:131], %[vr1] offset:32256\n\t" "ds_read_b64_tr_b16 a[132:133], %[vr1] offset:28704\n\t" "ds_read_b64_tr_b16 a[134:135], %[vr1] offset:32288\n\t"
    "ds_read_b64_tr_b16 a[136:137], %[vr3] offset:28672\n\t" "ds_read_b64_tr_b16 a[138:139], %[vr3] offset:32256\n\t" "ds_read_b64_tr_b16 a[140:141], %[vr3] offset:28704\n\t"
    "ds_read_b64_tr_b16 a[142:143], %[vr3] offset:32288\n\t" "global_load_dwordx4 a[16:19], %[va], s[76:77] offset:0 sc1\n\t" "global_load_dwordx4 a[20:23], %[va], s[76:77] offset:1024 sc1\n\t"
    "global_load_dwordx4 a[24:27], %[va], s[76:77] offset:2048 sc1\n\t" "global_load_dwordx4 a[28:31], %[va], s[76:77] offset:3072 sc1\n\t" "s_add_u32 s76, s76, 16384\n\t" "s_addc_u32 s77, s77, 0\n\t"
    "global_load_dwordx4 a[32:35], %[va], s[76:77] offset:0 sc1\n\t" "global_load_dwordx4 a[36:39], %[va], s[76:77] offset:1024 sc1\n\t" "global_load_dwordx4 a[40:43], %[va], s[76:77] offset:2048 sc1\n\t"
    "global_load_dwordx4 a[44:47], %[va], s[76:77] offset:3072 sc1\n\t" "s_add_u32 s76, s76, 16384\n\t" "s_addc_u32 s77, s77, 0\n\t" "s_waitcnt vmcnt(8)\n\t" "s_waitcnt lgkmcnt(4)\n\t"
    "v_mfma_f32_16x16x32_bf16 v[32:35], a[128:131], a[0:3], 0\n\t" "ds_read_b64_tr_b16 a[144:145], %[vr5] offset:28672\n\t" "s_mov_b64 s[90:91], %[ss]\n\t"
    "v_mfma_f32_16x16x32_bf16 v[36:39], a[128:131], a[4:7], 0\n\t" "ds_read_b64_tr_b16 a[146:147], %[vr5] offset:32256\n\t" "v_accvgpr_read_b32 v30, %[vs]\n\t"
    "v_mfma_f32_16x16x32_bf16 v[40:43], a[128:131], a[8:11], 0\n\t" "global_load_dwordx4 a[48:51], %[va], s[76:77] offset:0 sc1\n\t" "v_accvgpr_read_b32 v31, %[ve]\n\t"
    "v_mfma_f32_16x16x32_bf16 v[44:47], a[128:131], a[12:15], 0\n\t" "v_mfma_f32_16x16x32_bf16 v[48:51], a[132:135], a[0:3], 0\n\t" "ds_read_b64_tr_b16 a[148:149], %[vr5] offset:28704\n\t"
    "global_load_dwordx4 a[184:187], v31, %[se] offset:0 sc1\n\t" "v_mfma_f32_16x16x32_bf16 v[52:55], a[132:135], a[4:7], 0\n\t" "ds_read_b64_tr_b16 a[150:151], %[vr5] offset:32288\n\t"
    "s_add_u32 m0, %[sl], 28672\n\t" "v_mfma_f32_16x16x32_bf16 v[56:59], a[132:135], a[8:11], 0\n\t" "global_load_lds_dwordx4 %[vb0], s[78:79]\n\t"
    "global_load_dwordx4 a[188:191], v31, %[se] offset:128 sc1\n\t" "v_mfma_f32_16x16x32_bf16 v[60:63], a[132:135], a[12:15], 0\n\t" "s_waitcnt lgkmcnt(4)\n\t"
    "v_mfma_f32_16x16x32_bf16 v[64:67], a[136:139], a[0:3], 0\n\t" "ds_read_b64_tr_b16 a[152:153], %[vr7] offset:28672\n\t" "global_load_dwordx4 a[192:195], v31, %[se] offset:256 sc1\n\t"
    "v_mfma_f32_16x16x32_bf16 v[68:71], a[136:139], a[4:7], 0\n\t" "ds_read_b64_tr_b16 a[154:155], %[vr7] offset:32256\n\t" "v_mfma_f32_16x16x32_bf16 v[72:75], a[136:139], a[8:11], 0\n\t"
    "global_load_dwordx4 a[52:55], %[va], s[76:77] offset:1024 sc1\n\t" "global_load_dwordx4 a[196:199], v31, %[se] offset:384 sc1\n\t" "v_mfma_f32_16x16x32_bf16 v[76:79], a[136:139], a[12:15], 0\n\t"
    "v_mfma_f32_16x16x32_bf16 v[80:83], a[140:143], a[0:3], 0\n\t" "ds_read_b64_tr_b16 a[156:157], %[vr1] offset:35840\n\t" "global_load_dwordx2 a[216:217], v30, s[90:91] offset:0 sc1\n\t"
    "v_mfma_f32_16x16x32_bf16 v[84:87], a[140:143], a[4:7], 0\n\t" "ds_read_b64_tr_b16 a[158:159], %[vr1] offset:39424\n\t" "s_add_u32 m0, %[sl], 29696\n\t"
    "v_mfma_f32_16x16x32_bf16 v[88:91], a[140:143], a[8:11], 0\n\t" "global_load_lds_dwordx4 %[vb1], s[78:79]\n\t" "v_mfma_f32_16x16x32_bf16 v[92:95], a[140:143], a[12:15], 0\n\t"
    "s_waitcnt lgkmcnt(4)\n\t" "v_mfma_f32_16x16x32_bf16 v[96:99], a[144:147], a[0:3], 0\n\t" "ds_read_b64_tr_b16 a[128:129], %[vr1] offset:35872\n\t"
    "v_mfma_f32_16x16x32_bf16 v[100:103], a[144:147], a[4:7], 0\n\t" "ds_read_b64_tr_b16 a[130:131], %[vr1] offset:39456\n\t" "v_mfma_f32_16x16x32_bf16 v[104:107], a[144:147], a[8:11], 0\n\t"
    "v_mfma_f32_16x16x32_bf16 v[108:111], a[144:147], a[12:15], 0\n\t" "v_mfma_f32_16x16x32_bf16 v[112:115], a[148:151], a[0:3], 0\n\t" "ds_read_b64_tr_b16 a[132:133], %[vr3] offset:35840\n\t"
    "v_mfma_f32_16x16x32_bf16 v[116:119], a[148:151], a[4:7], 0\n\t" "ds_read_b64_tr_b16 a[134:135], %[vr3] offset:39424\n\t" "v_mfma_f32_16x16x32_bf16 v[120:123], a[148:151], a[8:11], 0\n\t"
    "global_load_dwordx4 a[56:59], %[va], s[76:77] offset:2048 sc1\n\t" "v_mfma_f32_16x16x32_bf16 v[124:127], a[148:151], a[12:15], 0\n\t" "s_waitcnt lgkmcnt(4)\n\t"
    "v_mfma_f32_16x16x32_bf16 v[128:131], a[152:155], a[0:3], 0\n\t" "ds_read_b64_tr_b16 a[136:137], %[vr3] offset:35872\n\t" "global_load_dwordx2 a[218:219], v30, s[90:91] offset:128 sc1\n\t"
    "v_mfma_f32_16x16x32_bf16 v[132:135], a[152:155], a[4:7], 0\n\t" "ds_read_b64_tr_b16 a[138:139], %[vr3] offset:39456\n\t" "s_add_u32 m0, %[sl], 30720\n\t"
    "v_mfma_f32_16x16x32_bf16 v[136:139], a[152:155], a[8:11], 0\n\t" "global_load_lds_dwordx4 %[vb2], s[78:79]\n\t" "v_mfma_f32_16x16x32_bf16 v[140:143], a[152:155], a[12:15], 0\n\t"
    "v_mfma_f32_16x16x32_bf16 v[144:147], a[156:159], a[0:3], 0\n\t" "ds_read_b64_tr_b16 a[140:141], %[vr5] offset:35840\n\t" "v_mfma_f32_16x16x32_bf16 v[148:151], a[156:159], a[4:7], 0\n\t"
    "ds_read_b64_tr_b16 a[142:143], %[vr5] offset:39424\n\t" "v_mfma_f32_16x16x32_bf16 v[152:155], a[156:159], a[8:11], 0\n\t" "global_load_dwordx4 a[60:63], %[va], s[76:77] offset:3072 sc1\n\t"
    "v_mfma_f32_16x16x32_bf16 v[156:159], a[156:159], a[12:15], 0\n\t" "s_add_u32 s76, s76, 16384\n\t" "s_addc_u32 s77, s77, 0\n\t" "s_waitcnt lgkmcnt(4)\n\t"
    "v_mfma_f32_16x16x32_bf16 v[160:163], a[128:131], a[0:3], 0\n\t" "ds_read_b64_tr_b16 a[144:145], %[vr5] offset:35872\n\t" "v_mfma_f32_16x16x32_bf16 v[164:167], a[128:131], a[4:7], 0\n\t"
    "ds_read_b64_tr_b16 a[146:147], %[vr5] offset:39456\n\t" "v_mfma_f32_16x16x32_bf16 v[168:171], a[128:131], a[8:11], 0\n\t" "v_mfma_f32_16x16x32_bf16 v[172:175], a[128:131], a[12:15], 0\n\t"
    "v_mfma_f32_16x16x32_bf16 v[176:179], a[132:135], a[0:3], 0\n\t" "ds_read_b64_tr_b16 a[148:149], %[vr7] offset:35840\n\t" "global_load_dwordx2 a[220:221], v30, s[90:91] offset:256 sc1\n\t"
    "v_mfma_f32_16x16x32_bf16 v[180:183], a[132:135], a[4:7], 0\n\t" "ds_read_b64_tr_b16 a[150:151], %[vr7] offset:39424\n\t" "s_add_u32 m0, %[sl], 31744\n\t"
    "v_mfma_f32_16x16x32_bf16 v[184:187], a[132:135], a[8:11], 0\n\t" "s_mov_b64 exec, s[82:83]\n\t" "global_load_lds_dwordx4 %[vb3], s[78:79]\n\t" "s_mov_b64 exec, -1\n\t"
    "v_mfma_f32_16x16x32_bf16 v[188:191], a[132:135], a[12:15], 0\n\t" "s_add_u32 s78, s78, 229376\n\t" "s_addc_u32 s79, s79, 0\n\t" "s_waitcnt lgkmcnt(4)\n\t"
    "v_mfma_f32_16x16x32_bf16 v[192:195], a[136:139], a[0:3], 0\n\t" "s_waitcnt vmcnt(19)\n\t" "s_barrier\n\t" "ds_read_b64_tr_b16 a[152:153], %[vr1] offset:43008\n\t"
    "v_mfma_f32_16x16x32_bf16 v[196:199], a[136:139], a[4:7], 0\n\t" "ds_read_b64_tr_b16 a[154:155], %[vr1] offset:46592\n\t" "v_mfma_f32_16x16x32_bf16 v[200:203], a[136:139], a[8:11], 0\n\t"
    "global_load_dwordx4 a[64:67], %[va], s[76:77] offset:0 sc1\n\t" "v_mfma_f32_16x16x32_bf16 v[204:207], a[136:139], a[12:15], 0\n\t" "v_mfma_f32_16x16x32_bf16 v[208:211], a[140:143], a[0:3], 0\n\t"
    "ds_read_b64_tr_b16 a[156:157], %[vr1] offset:43040\n\t" "v_mfma_f32_16x16x32_bf16 v[212:215], a[140:143], a[4:7], 0\n\t" "ds_read_b64_tr_b16 a[158:159], %[vr1] offset:46624\n\t"
    "s_add_u32 m0, %[sl], 43008\n\t" "v_mfma_f32_16x16x32_bf16 v[216:219], a[140:143], a[8:11], 0\n\t" "global_load_lds_dwordx4 %[vb0], s[78:79]\n\t"
    "v_mfma_f32_16x16x32_bf16 v[220:223], a[140:143], a[12:15], 0\n\t" "s_waitcnt lgkmcnt(4)\n\t" "v_mfma_f32_16x16x32_bf16 v[224:227], a[144:147], a[0:3], 0\n\t"
    "ds_read_b64_tr_b16 a[128:129], %[vr3] offset:43008\n\t" "global_load_dwordx2 a[222:223], v30, s[90:91] offset:384 sc1\n\t" "v_mfma_f32_16x16x32_bf16 v[228:231], a[144:147], a[4:7], 0\n\t"
    "ds_read_b64_tr_b16 a[130:131], %[vr3] offset:46592\n\t" "v_mfma_f32_16x16x32_bf16 v[232:235], a[144:147], a[8:11], 0\n\t" "global_load_dword v29, %[va], %[cd]\n\t"
    "v_mfma_f32_16x16x32_bf16 v[236:239], a[144:147], a[12:15], 0\n\t" "v_mfma_f32_16x16x32_bf16 v[240:243], a[148:151], a[0:3], 0\n\t" "ds_read_b64_tr_b16 a[132:133], %[vr3] offset:43040\n\t"
    "v_mfma_f32_16x16x32_bf16 v[244:247], a[148:151], a[4:7], 0\n\t" "ds_read_b64_tr_b16 a[134:135], %[vr3] offset:46624\n\t" "v_mfma_f32_16x16x32_bf16 v[248:251], a[148:151], a[8:11], 0\n\t"
    "v_mfma_f32_16x16x32_bf16 v[252:255], a[148:151], a[12:15], 0\n\t" "s_waitcnt lgkmcnt(4)\n\t" "v_mfma_f32_16x16x32_bf16 v[32:35], a[152:155], a[16:19], v[32:35]\n\t"
    "ds_read_b64_tr_b16 a[136:137], %[vr5] offset:43008\n\t" "v_mfma_f32_16x16x32_bf16 v[36:39], a[152:155], a[20:23], v[36:39]\n\t" "ds_read_b64_tr_b16 a[138:139], %[vr5] offset:46592\n\t"
    "v_mfma_f32_16x16x32_bf16 v[40:43], a[152:155], a[24:27], v[40:43]\n\t" "global_load_dwordx4 a[68:71], %[va], s[76:77] offset:1024 sc1\n\t"
    "v_mfma_f32_16x16x32_bf16 v[44:47], a[152:155], a[28:31], v[44:47]\n\t" "v_mfma_f32_16x16x32_bf16 v[48:51], a[156:159], a[16:19], v[48:51]\n\t" "ds_read_b64_tr_b16 a[140:141], %[vr5] offset:43040\n\t"
    "s_add_u32 s90, s90, 16384\n\t" "s_addc_u32 s91, s91, 0\n\t" "v_mfma_f32_16x16x32_bf16 v[52:55], a[156:159], a[20:23], v[52:55]\n\t" "ds_read_b64_tr_b16 a[142:143], %[vr5] offset:46624\n\t"
    "global_load_dwordx2 a[224:225], v30, s[90:91] offset:0 sc1\n\t" "s_add_u32 m0, %[sl], 44032\n\t" "v_mfma_f32_16x16x32_bf16 v[56:59], a[156:159], a[24:27], v[56:59]\n\t"
    "global_load_lds_dwordx4 %[vb1], s[78:79]\n\t" "v_mfma_f32_16x16x32_bf16 v[60:63], a[156:159], a[28:31], v[60:63]\n\t" "s_waitcnt lgkmcnt(4)\n\t"
    "v_mfma_f32_16x16x32_bf16 v[64:67], a[128:131], a[16:19], v[64:67]\n\t" "ds_read_b64_tr_b16 a[144:145], %[vr7] offset:43008\n\t" "v_mfma_f32_16x16x32_bf16 v[68:71], a[128:131], a[20:23], v[68:71]\n\t"
    "ds_read_b64_tr_b16 a[146:147], %[vr7] offset:46592\n\t" "v_mfma_f32_16x16x32_bf16 v[72:75], a[128:131], a[24:27], v[72:75]\n\t" "global_load_dwordx4 a[72:75], %[va], s[76:77] offset:2048 sc1\n\t"
    "v_mfma_f32_16x16x32_bf16 v[76:79], a[128:131], a[28:31], v[76:79]\n\t" "v_mfma_f32_16x16x32_bf16 v[80:83], a[132:135], a[16:19], v[80:83]\n\t" "ds_read_b64_tr_b16 a[148:149], %[vr1] offset:50176\n\t"
    "v_mfma_f32_16x16x32_bf16 v[84:87], a[132:135], a[20:23], v[84:87]\n\t" "ds_read_b64_tr_b16 a[150:151], %[vr1] offset:53760\n\t" "s_add_u32 m0, %[sl], 45056\n\t"
    "v_mfma_f32_16x16x32_bf16 v[88:91], a[132:135], a[24:27], v[88:91]\n\t" "global_load_lds_dwordx4 %[vb2], s[78:79]\n\t" "v_mfma_f32_16x16x32_bf16 v[92:95], a[132:135], a[28:31], v[92:95]\n\t"
    "s_waitcnt lgkmcnt(4)\n\t" "v_mfma_f32_16x16x32_bf16 v[96:99], a[136:139], a[16:19], v[96:99]\n\t" "ds_read_b64_tr_b16 a[152:153], %[vr1] offset:50208\n\t"
    "v_mfma_f32_16x16x32_bf16 v[100:103], a[136:139], a[20:23], v[100:103]\n\t" "ds_read_b64_tr_b16 a[154:155], %[vr1] offset:53792\n\t" "global_load_dwordx2 a[226:227], v30, s[90:91] offset:128 sc1\n\t"
    "v_mfma_f32_16x16x32_bf16 v[104:107], a[136:139], a[24:27], v[104:107]\n\t" "v_mfma_f32_16x16x32_bf16 v[108:111], a[136:139], a[28:31], v[108:111]\n\t"
    "v_mfma_f32_16x16x32_bf16 v[112:115], a[140:143], a[16:19], v[112:115]\n\t" "ds_read_b64_tr_b16 a[156:157], %[vr3] offset:50176\n\t"
    "v_mfma_f32_16x16x32_bf16 v[116:119], a[140:143], a[20:23], v[116:119]\n\t" "ds_read_b64_tr_b16 a[158:159], %[vr3] offset:53760\n\t"
    "v_mfma_f32_16x16x32_bf16 v[120:123], a[140:143], a[24:27], v[120:123]\n\t" "global_load_dwordx4 a[76:79], %[va], s[76:77] offset:3072 sc1\n\t"
    "v_mfma_f32_16x16x32_bf16 v[124:127], a[140:143], a[28:31], v[124:127]\n\t" "s_add_u32 s76, s76, 16384\n\t" "s_addc_u32 s77, s77, 0\n\t" "s_waitcnt lgkmcnt(4)\n\t"
    "v_mfma_f32_16x16x32_bf16 v[128:131], a[144:147], a[16:19], v[128:131]\n\t" "ds_read_b64_tr_b16 a[128:129], %[vr3] offset:50208\n\t"
    "v_mfma_f32_16x16x32_bf16 v[132:135], a[144:147], a[20:23], v[132:135]\n\t" "ds_read_b64_tr_b16 a[130:131], %[vr3] offset:53792\n\t" "s_add_u32 m0, %[sl], 46080\n\t"
    "v_mfma_f32_16x16x32_bf16 v[136:139], a[144:147], a[24:27], v[136:139]\n\t" "s_mov_b64 exec, s[82:83]\n\t" "global_load_lds_dwordx4 %[vb3], s[78:79]\n\t" "s_mov_b64 exec, -1\n\t"
    "v_mfma_f32_16x16x32_bf16 v[140:143], a[144:147], a[28:31], v[140:143]\n\t" "s_add_u32 s78, s78, 229376\n\t" "s_addc_u32 s79, s79, 0\n\t"
    "v_mfma_f32_16x16x32_bf16 v[144:147], a[148:151], a[16:19], v[144:147]\n\t" "ds_read_b64_tr_b16 a[132:133], %[vr5] offset:50176\n\t"
    "v_mfma_f32_16x16x32_bf16 v[148:151], a[148:151], a[20:23], v[148:151]\n\t" "ds_read_b64_tr_b16 a[134:135], %[vr5] offset:53760\n\t" "global_load_dwordx2 a[228:229], v30, s[90:91] offset:256 sc1\n\t"
    "v_mfma_f32_16x16x32_bf16 v[152:155], a[148:151], a[24:27], v[152:155]\n\t" "global_load_dwordx4 a[80:83], %[va], s[76:77] offset:0 sc1\n\t"
    "v_mfma_f32_16x16x32_bf16 v[156:159], a[148:151], a[28:31], v[156:159]\n\t" "s_waitcnt lgkmcnt(4)\n\t" "v_mfma_f32_16x16x32_bf16 v[160:163], a[152:155], a[16:19], v[160:163]\n\t"
    "ds_read_b64_tr_b16 a[136:137], %[vr5] offset:50208\n\t" "v_mfma_f32_16x16x32_bf16 v[164:167], a[152:155], a[20:23], v[164:167]\n\t" "ds_read_b64_tr_b16 a[138:139], %[vr5] offset:53792\n\t"
    "v_mfma_f32_16x16x32_bf16 v[168:171], a[152:155], a[24:27], v[168:171]\n\t" "v_mfma_f32_16x16x32_bf16 v[172:175], a[152:155], a[28:31], v[172:175]\n\t"
    "v_mfma_f32_16x16x32_bf16 v[176:179], a[156:159], a[16:19], v[176:179]\n\t" "ds_read_b64_tr_b16 a[140:141], %[vr7] offset:50176\n\t"
    "v_mfma_f32_16x16x32_bf16 v[180:183], a[156:159], a[20:23], v[180:183]\n\t" "ds_read_b64_tr_b16 a[142:143], %[vr7] offset:53760\n\t" "s_add_u32 m0, %[sl], 57344\n\t"
    "v_mfma_f32_16x16x32_bf16 v[184:187], a[156:159], a[24:27], v[184:187]\n\t" "global_load_lds_dwordx4 %[vb0], s[78:79]\n\t" "v_mfma_f32_16x16x32_bf16 v[188:191], a[156:159], a[28:31], v[188:191]\n\t"
    "s_waitcnt lgkmcnt(4)\n\t" "v_mfma_f32_16x16x32_bf16 v[192:195], a[128:131], a[16:19], v[192:195]\n\t" "s_barrier\n\t" "ds_read_b64_tr_b16 a[144:145], %[vr0] offset:0\n\t"
    "v_mfma_f32_16x16x32_bf16 v[196:199], a[128:131], a[20:23], v[196:199]\n\t" "ds_read_b64_tr_b16 a[146:147], %[vr0] offset:3584\n\t" "global_load_dwordx2 a[230:231], v30, s[90:91] offset:384 sc1\n\t"
    "v_mfma_f32_16x16x32_bf16 v[200:203], a[128:131], a[24:27], v[200:203]\n\t" "global_load_dwordx4 a[84:87], %[va], s[76:77] offset:1024 sc1\n\t"
    "v_mfma_f32_16x16x32_bf16 v[204:207], a[128:131], a[28:31], v[204:207]\n\t" "v_mfma_f32_16x16x32_bf16 v[208:211], a[132:135], a[16:19], v[208:211]\n\t"
    "ds_read_b64_tr_b16 a[148:149], %[vr0] offset:14336\n\t" "v_mfma_f32_16x16x32_bf16 v[212:215], a[132:135], a[20:23], v[212:215]\n\t" "ds_read_b64_tr_b16 a[150:151], %[vr0] offset:17920\n\t"
    "s_add_u32 m0, %[sl], 58368\n\t" "v_mfma_f32_16x16x32_bf16 v[216:219], a[132:135], a[24:27], v[216:219]\n\t" "global_load_lds_dwordx4 %[vb1], s[78:79]\n\t"
    "v_mfma_f32_16x16x32_bf16 v[220:223], a[132:135], a[28:31], v[220:223]\n\t" "s_waitcnt lgkmcnt(4)\n\t" "v_mfma_f32_16x16x32_bf16 v[224:227], a[136:139], a[16:19], v[224:227]\n\t"
    "ds_read_b64_tr_b16 a[152:153], %[vr0] offset:32\n\t" "v_mfma_f32_16x16x32_bf16 v[228:231], a[136:139], a[20:23], v[228:231]\n\t" "ds_read_b64_tr_b16 a[154:155], %[vr0] offset:3616\n\t"
    "v_mfma_f32_16x16x32_bf16 v[232:235], a[136:139], a[24:27], v[232:235]\n\t" "v_mfma_f32_16x16x32_bf16 v[236:239], a[136:139], a[28:31], v[236:239]\n\t"
    "v_mfma_f32_16x16x32_bf16 v[240:243], a[140:143], a[16:19], v[240:243]\n\t" "ds_read_b64_tr_b16 a[156:157], %[vr0] offset:14368\n\t"
    "v_mfma_f32_16x16x32_bf16 v[244:247], a[140:143], a[20:23], v[244:247]\n\t" "ds_read_b64_tr_b16 a[158:159], %[vr0] offset:17952\n\t" "s_add_u32 s90, s90, 16384\n\t" "s_addc_u32 s91, s91, 0\n\t"
    "v_mfma_f32_16x16x32_bf16 v[248:251], a[140:143], a[24:27], v[248:251]\n\t" "global_load_dwordx2 a[232:233], v30, s[90:91] offset:0 sc1\n\t"
    "v_mfma_f32_16x16x32_bf16 v[252:255], a[140:143], a[28:31], v[252:255]\n\t" "s_waitcnt lgkmcnt(4) vmcnt(21)\n\t" "v_mfma_f32_16x16x32_bf16 v[32:35], a[144:147], a[32:35], v[32:35]\n\t" "s_barrier\n\t"
    "ds_read_b64_tr_b16 a[128:129], %[vr2] offset:0\n\t" "v_mfma_f32_16x16x32_bf16 v[32:35], a[148:151], a[48:51], v[32:35]\n\t" "ds_read_b64_tr_b16 a[130:131], %[vr2] offset:3584\n\t"
    "v_mfma_f32_16x16x32_bf16 v[36:39], a[144:147], a[36:39], v[36:39]\n\t" "global_load_dwordx4 a[88:91], %[va], s[76:77] offset:2048 sc1\n\t"
    "v_mfma_f32_16x16x32_bf16 v[36:39], a[148:151], a[52:55], v[36:39]\n\t" "v_mfma_f32_16x16x32_bf16 v[40:43], a[144:147], a[40:43], v[40:43]\n\t" "ds_read_b64_tr_b16 a[132:133], %[vr2] offset:14336\n\t"
    "v_mfma_f32_16x16x32_bf16 v[40:43], a[148:151], a[56:59], v[40:43]\n\t" "ds_read_b64_tr_b16 a[134:135], %[vr2] offset:17920\n\t" "s_add_u32 m0, %[sl], 59392\n\t"
    "v_mfma_f32_16x16x32_bf16 v[44:47], a[144:147], a[44:47], v[44:47]\n\t" "global_load_lds_dwordx4 %[vb2], s[78:79]\n\t" "v_mfma_f32_16x16x32_bf16 v[44:47], a[148:151], a[60:63], v[44:47]\n\t"
    "s_waitcnt lgkmcnt(4)\n\t" "v_mfma_f32_16x16x32_bf16 v[48:51], a[152:155], a[32:35], v[48:51]\n\t" "ds_read_b64_tr_b16 a[136:137], %[vr2] offset:32\n\t"
    "v_mfma_f32_16x16x32_bf16 v[48:51], a[156:159], a[48:51], v[48:51]\n\t" "ds_read_b64_tr_b16 a[138:139], %[vr2] offset:3616\n\t" "v_mfma_f32_16x16x32_bf16 v[52:55], a[152:155], a[36:39], v[52:55]\n\t"
    "global_load_dwordx4 a[92:95], %[va], s[76:77] offset:3072 sc1\n\t" "global_load_dwordx2 a[234:235], v30, s[90:91] offset:128 sc1\n\t"
    "v_mfma_f32_16x16x32_bf16 v[52:55], a[156:159], a[52:55], v[52:55]\n\t" "s_add_u32 s76, s76, 16384\n\t" "s_addc_u32 s77, s77, 0\n\t"
    "v_mfma_f32_16x16x32_bf16 v[56:59], a[152:155], a[40:43], v[56:59]\n\t" "ds_read_b64_tr_b16 a[140:141], %[vr2] offset:14368\n\t" "v_mfma_f32_16x16x32_bf16 v[56:59], a[156:159], a[56:59], v[56:59]\n\t"
    "ds_read_b64_tr_b16 a[142:143], %[vr2] offset:17952\n\t" "s_add_u32 m0, %[sl], 60416\n\t" "v_mfma_f32_16x16x32_bf16 v[60:63], a[152:155], a[44:47], v[60:63]\n\t" "s_mov_b64 exec, s[82:83]\n\t"
    "global_load_lds_dwordx4 %[vb3], s[78:79]\n\t" "s_mov_b64 exec, -1\n\t" "v_mfma_f32_16x16x32_bf16 v[60:63], a[156:159], a[60:63], v[60:63]\n\t" "s_add_u32 s78, s78, 229376\n\t"
    "s_addc_u32 s79, s79, 0\n\t" "s_waitcnt lgkmcnt(4)\n\t" "v_mfma_f32_16x16x32_bf16 v[64:67], a[128:131], a[32:35], v[64:67]\n\t" "ds_read_b64_tr_b16 a[144:145], %[vr4] offset:0\n\t"
    "v_mfma_f32_16x16x32_bf16 v[64:67], a[132:135], a[48:51], v[64:67]\n\t" "ds_read_b64_tr_b16 a[146:147], %[vr4] offset:3584\n\t" "v_mfma_f32_16x16x32_bf16 v[68:71], a[128:131], a[36:39], v[68:71]\n\t"
    "v_mfma_f32_16x16x32_bf16 v[68:71], a[132:135], a[52:55], v[68:71]\n\t" "v_mfma_f32_16x16x32_bf16 v[72:75], a[128:131], a[40:43], v[72:75]\n\t" "ds_read_b64_tr_b16 a[148:149], %[vr4] offset:14336\n\t"
    "v_mfma_f32_16x16x32_bf16 v[72:75], a[132:135], a[56:59], v[72:75]\n\t" "ds_read_b64_tr_b16 a[150:151], %[vr4] offset:17920\n\t" "v_mfma_f32_16x16x32_bf16 v[76:79], a[128:131], a[44:47], v[76:79]\n\t"
    "global_load_dwordx4 a[96:99], %[va], s[76:77] offset:0 sc1\n\t" "global_load_dwordx2 a[236:237], v30, s[90:91] offset:256 sc1\n\t"
    "v_mfma_f32_16x16x32_bf16 v[76:79], a[132:135], a[60:63], v[76:79]\n\t" "s_waitcnt lgkmcnt(4)\n\t" "v_mfma_f32_16x16x32_bf16 v[80:83], a[136:139], a[32:35], v[80:83]\n\t"
    "ds_read_b64_tr_b16 a[152:153], %[vr4] offset:32\n\t" "v_mfma_f32_16x16x32_bf16 v[80:83], a[140:143], a[48:51], v[80:83]\n\t" "ds_read_b64_tr_b16 a[154:155], %[vr4] offset:3616\n\t"
    "s_add_u32 m0, %[sl], 71680\n\t" "v_mfma_f32_16x16x32_bf16 v[84:87], a[136:139], a[36:39], v[84:87]\n\t" "global_load_lds_dwordx4 %[vb0], s[78:79]\n\t"
    "v_mfma_f32_16x16x32_bf16 v[84:87], a[140:143], a[52:55], v[84:87]\n\t" "v_mfma_f32_16x16x32_bf16 v[88:91], a[136:139], a[40:43], v[88:91]\n\t" "ds_read_b64_tr_b16 a[156:157], %[vr4] offset:14368\n\t"
    "v_mfma_f32_16x16x32_bf16 v[88:91], a[140:143], a[56:59], v[88:91]\n\t" "ds_read_b64_tr_b16 a[158:159], %[vr4] offset:17952\n\t" "v_mfma_f32_16x16x32_bf16 v[92:95], a[136:139], a[44:47], v[92:95]\n\t"
    "global_load_dwordx4 a[100:103], %[va], s[76:77] offset:1024 sc1\n\t" "v_mfma_f32_16x16x32_bf16 v[92:95], a[140:143], a[60:63], v[92:95]\n\t" "s_waitcnt lgkmcnt(4)\n\t"
    "v_mfma_f32_16x16x32_bf16 v[96:99], a[144:147], a[32:35], v[96:99]\n\t" "ds_read_b64_tr_b16 a[128:129], %[vr6] offset:0\n\t" "v_mfma_f32_16x16x32_bf16 v[96:99], a[148:151], a[48:51], v[96:99]\n\t"
    "ds_read_b64_tr_b16 a[130:131], %[vr6] offset:3584\n\t" "v_mfma_f32_16x16x32_bf16 v[100:103], a[144:147], a[36:39], v[100:103]\n\t" "global_load_dwordx2 a[238:239], v30, s[90:91] offset:384 sc1\n\t"
    "v_mfma_f32_16x16x32_bf16 v[100:103], a[148:151], a[52:55], v[100:103]\n\t" "v_mfma_f32_16x16x32_bf16 v[104:107], a[144:147], a[40:43], v[104:107]\n\t"
    "ds_read_b64_tr_b16 a[132:133], %[vr6] offset:14336\n\t" "v_mfma_f32_16x16x32_bf16 v[104:107], a[148:151], a[56:59], v[104:107]\n\t" "ds_read_b64_tr_b16 a[134:135], %[vr6] offset:17920\n\t"
    "s_add_u32 m0, %[sl], 72704\n\t" "v_mfma_f32_16x16x32_bf16 v[108:111], a[144:147], a[44:47], v[108:111]\n\t" "global_load_lds_dwordx4 %[vb1], s[78:79]\n\t"
    "v_mfma_f32_16x16x32_bf16 v[108:111], a[148:151], a[60:63], v[108:111]\n\t" "s_waitcnt lgkmcnt(4)\n\t" "v_mfma_f32_16x16x32_bf16 v[112:115], a[152:155], a[32:35], v[112:115]\n\t"
    "ds_read_b64_tr_b16 a[136:137], %[vr0] offset:7168\n\t" "v_mfma_f32_16x16x32_bf16 v[112:115], a[156:159], a[48:51], v[112:115]\n\t" "ds_read_b64_tr_b16 a[138:139], %[vr0] offset:10752\n\t"
    "v_mfma_f32_16x16x32_bf16 v[116:119], a[152:155], a[36:39], v[116:119]\n\t" "global_load_dwordx4 a[104:107], %[va], s[76:77] offset:2048 sc1\n\t"
    "v_mfma_f32_16x16x32_bf16 v[116:119], a[156:159], a[52:55], v[116:119]\n\t" "v_mfma_f32_16x16x32_bf16 v[120:123], a[152:155], a[40:43], v[120:123]\n\t"
    "ds_read_b64_tr_b16 a[140:141], %[vr0] offset:21504\n\t" "v_mfma_f32_16x16x32_bf16 v[120:123], a[156:159], a[56:59], v[120:123]\n\t" "ds_read_b64_tr_b16 a[142:143], %[vr0] offset:25088\n\t"
    "s_add_u32 m0, %[sl], 73728\n\t" "v_mfma_f32_16x16x32_bf16 v[124:127], a[152:155], a[44:47], v[124:127]\n\t" "global_load_lds_dwordx4 %[vb2], s[78:79]\n\t" "s_add_u32 s90, s90, 16384\n\t"
    "s_addc_u32 s91, s91, 0\n\t" "v_mfma_f32_16x16x32_bf16 v[124:127], a[156:159], a[60:63], v[124:127]\n\t" "global_load_dwordx2 a[240:241], v30, s[90:91] offset:0 sc1\n\t" "s_waitcnt lgkmcnt(4)\n\t"
    "v_mfma_f32_16x16x32_bf16 v[128:131], a[128:131], a[32:35], v[128:131]\n\t" "ds_read_b64_tr_b16 a[144:145], %[vr0] offset:7200\n\t"
    "v_mfma_f32_16x16x32_bf16 v[128:131], a[132:135], a[48:51], v[128:131]\n\t" "ds_read_b64_tr_b16 a[146:147], %[vr0] offset:10784\n\t"
    "v_mfma_f32_16x16x32_bf16 v[132:135], a[128:131], a[36:39], v[132:135]\n\t" "v_mfma_f32_16x16x32_bf16 v[132:135], a[132:135], a[52:55], v[132:135]\n\t"
    "v_mfma_f32_16x16x32_bf16 v[136:139], a[128:131], a[40:43], v[136:139]\n\t" "ds_read_b64_tr_b16 a[148:149], %[vr0] offset:21536\n\t"
    "v_mfma_f32_16x16x32_bf16 v[136:139], a[132:135], a[56:59], v[136:139]\n\t" "ds_read_b64_tr_b16 a[150:151], %[vr0] offset:25120\n\t"
    "v_mfma_f32_16x16x32_bf16 v[140:143], a[128:131], a[44:47], v[140:143]\n\t" "v_mfma_f32_16x16x32_bf16 v[140:143], a[132:135], a[60:63], v[140:143]\n\t" "s_waitcnt lgkmcnt(4)\n\t"
    "v_mfma_f32_16x16x32_bf16 v[144:147], a[136:139], a[32:35], v[144:147]\n\t" "ds_read_b64_tr_b16 a[152:153], %[vr2] offset:7168\n\t"
    "v_mfma_f32_16x16x32_bf16 v[144:147], a[140:143], a[48:51], v[144:147]\n\t" "ds_read_b64_tr_b16 a[154:155], %[vr2] offset:10752\n\t"
    "v_mfma_f32_16x16x32_bf16 v[148:151], a[136:139], a[36:39], v[148:151]\n\t" "global_load_dwordx4 a[108:111], %[va], s[76:77] offset:3072 sc1\n\t"
    "v_mfma_f32_16x16x32_bf16 v[148:151], a[140:143], a[52:55], v[148:151]\n\t" "s_add_u32 s76, s76, 16384\n\t" "s_addc_u32 s77, s77, 0\n\t"
    "global_load_dwordx2 a[242:243], v30, s[90:91] offset:128 sc1\n\t" "v_mfma_f32_16x16x32_bf16 v[152:155], a[136:139], a[40:43], v[152:155]\n\t" "ds_read_b64_tr_b16 a[156:157], %[vr2] offset:21504\n\t"
    "v_mfma_f32_16x16x32_bf16 v[152:155], a[140:143], a[56:59], v[152:155]\n\t" "ds_read_b64_tr_b16 a[158:159], %[vr2] offset:25088\n\t" "s_add_u32 m0, %[sl], 74752\n\t"
    "v_mfma_f32_16x16x32_bf16 v[156:159], a[136:139], a[44:47], v[156:159]\n\t" "s_mov_b64 exec, s[82:83]\n\t" "global_load_lds_dwordx4 %[vb3], s[78:79]\n\t" "s_mov_b64 exec, -1\n\t"
    "v_mfma_f32_16x16x32_bf16 v[156:159], a[140:143], a[60:63], v[156:159]\n\t" "s_add_u32 s78, s78, s88\n\t" "s_addc_u32 s79, s79, s89\n\t" "s_waitcnt lgkmcnt(4)\n\t"
    "v_mfma_f32_16x16x32_bf16 v[160:163], a[144:147], a[32:35], v[160:163]\n\t" "ds_read_b64_tr_b16 a[128:129], %[vr2] offset:7200\n\t"
    "v_mfma_f32_16x16x32_bf16 v[160:163], a[148:151], a[48:51], v[160:163]\n\t" "ds_read_b64_tr_b16 a[130:131], %[vr2] offset:10784\n\t"
    "v_mfma_f32_16x16x32_bf16 v[164:167], a[144:147], a[36:39], v[164:167]\n\t" "global_load_dwordx4 a[112:115], %[va], s[76:77] offset:0 sc1\n\t"
    "v_mfma_f32_16x16x32_bf16 v[164:167], a[148:151], a[52:55], v[164:167]\n\t" "v_mfma_f32_16x16x32_bf16 v[168:171], a[144:147], a[40:43], v[168:171]\n\t"
    "ds_read_b64_tr_b16 a[132:133], %[vr2] offset:21536\n\t" "v_mfma_f32_16x16x32_bf16 v[168:171], a[148:151], a[56:59], v[168:171]\n\t" "ds_read_b64_tr_b16 a[134:135], %[vr2] offset:25120\n\t"
    "s_add_u32 m0, %[sl], 86016\n\t" "v_mfma_f32_16x16x32_bf16 v[172:175], a[144:147], a[44:47], v[172:175]\n\t" "global_load_lds_dwordx4 %[vb0], s[78:79]\n\t"
    "v_mfma_f32_16x16x32_bf16 v[172:175], a[148:151], a[60:63], v[172:175]\n\t" "global_load_dwordx2 a[244:245], v30, s[90:91] offset:256 sc1\n\t" "s_waitcnt lgkmcnt(4)\n\t"
    "v_mfma_f32_16x16x32_bf16 v[176:179], a[152:155], a[32:35], v[176:179]\n\t" "ds_read_b64_tr_b16 a[136:137], %[vr4] offset:7168\n\t"
    "v_mfma_f32_16x16x32_bf16 v[176:179], a[156:159], a[48:51], v[176:179]\n\t" "ds_read_b64_tr_b16 a[138:139], %[vr4] offset:10752\n\t"
    "v_mfma_f32_16x16x32_bf16 v[180:183], a[152:155], a[36:39], v[180:183]\n\t" "v_mfma_f32_16x16x32_bf16 v[180:183], a[156:159], a[52:55], v[180:183]\n\t"
    "v_mfma_f32_16x16x32_bf16 v[184:187], a[152:155], a[40:43], v[184:187]\n\t" "ds_read_b64_tr_b16 a[140:141], %[vr4] offset:21504\n\t"
    "v_mfma_f32_16x16x32_bf16 v[184:187], a[156:159], a[56:59], v[184:187]\n\t" "ds_read_b64_tr_b16 a[142:143], %[vr4] offset:25088\n\t"
    "v_mfma_f32_16x16x32_bf16 v[188:191], a[152:155], a[44:47], v[188:191]\n\t" "global_load_dwordx4 a[116:119], %[va], s[76:77] offset:1024 sc1\n\t"
    "v_mfma_f32_16x16x32_bf16 v[188:191], a[156:159], a[60:63], v[188:191]\n\t" "s_waitcnt lgkmcnt(4)\n\t" "v_mfma_f32_16x16x32_bf16 v[192:195], a[128:131], a[32:35], v[192:195]\n\t"
    "ds_read_b64_tr_b16 a[144:145], %[vr4] offset:7200\n\t" "v_mfma_f32_16x16x32_bf16 v[192:195], a[132:135], a[48:51], v[192:195]\n\t" "ds_read_b64_tr_b16 a[146:147], %[vr4] offset:10784\n\t"
    "s_add_u32 m0, %[sl], 87040\n\t" "v_mfma_f32_16x16x32_bf16 v[196:199], a[128:131], a[36:39], v[196:199]\n\t" "global_load_lds_dwordx4 %[vb1], s[78:79]\n\t"
    "v_mfma_f32_16x16x32_bf16 v[196:199], a[132:135], a[52:55], v[196:199]\n\t" "global_load_dwordx2 a[246:247], v30, s[90:91] offset:384 sc1\n\t"
    "v_mfma_f32_16x16x32_bf16 v[200:203], a[128:131], a[40:43], v[200:203]\n\t" "ds_read_b64_tr_b16 a[148:149], %[vr4] offset:21536\n\t"
    "v_mfma_f32_16x16x32_bf16 v[200:203], a[132:135], a[56:59], v[200:203]\n\t" "ds_read_b64_tr_b16 a[150:151], %[vr4] offset:25120\n\t"
    "v_mfma_f32_16x16x32_bf16 v[204:207], a[128:131], a[44:47], v[204:207]\n\t" "global_load_dwordx4 a[120:123], %[va], s[76:77] offset:2048 sc1\n\t"
    "v_mfma_f32_16x16x32_bf16 v[204:207], a[132:135], a[60:63], v[204:207]\n\t" "s_waitcnt lgkmcnt(4)\n\t" "v_mfma_f32_16x16x32_bf16 v[208:211], a[136:139], a[32:35], v[208:211]\n\t"
    "ds_read_b64_tr_b16 a[152:153], %[vr6] offset:7168\n\t" "v_mfma_f32_16x16x32_bf16 v[208:211], a[140:143], a[48:51], v[208:211]\n\t" "ds_read_b64_tr_b16 a[154:155], %[vr6] offset:10752\n\t"
    "v_mfma_f32_16x16x32_bf16 v[212:215], a[136:139], a[36:39], v[212:215]\n\t" "v_mfma_f32_16x16x32_bf16 v[212:215], a[140:143], a[52:55], v[212:215]\n\t"
    "v_mfma_f32_16x16x32_bf16 v[216:219], a[136:139], a[40:43], v[216:219]\n\t" "ds_read_b64_tr_b16 a[156:157], %[vr6] offset:21504\n\t"
    "v_mfma_f32_16x16x32_bf16 v[216:219], a[140:143], a[56:59], v[216:219]\n\t" "ds_read_b64_tr_b16 a[158:159], %[vr6] offset:25088\n\t" "s_add_u32 m0, %[sl], 88064\n\t"
    "v_mfma_f32_16x16x32_bf16 v[220:223], a[136:139], a[44:47], v[220:223]\n\t" "global_load_lds_dwordx4 %[vb2], s[78:79]\n\t" "v_mfma_f32_16x16x32_bf16 v[220:223], a[140:143], a[60:63], v[220:223]\n\t"
    "s_waitcnt lgkmcnt(4)\n\t" "v_mfma_f32_16x16x32_bf16 v[224:227], a[144:147], a[32:35], v[224:227]\n\t" "s_waitcnt vmcnt(32)\n\t" "s_barrier\n\t"
    "ds_read_b64_tr_b16 a[128:129], %[vr0] offset:28672\n\t" "v_mfma_f32_16x16x32_bf16 v[224:227], a[148:151], a[48:51], v[224:227]\n\t" "ds_read_b64_tr_b16 a[130:131], %[vr0] offset:32256\n\t"
    "v_mfma_f32_16x16x32_bf16 v[228:231], a[144:147], a[36:39], v[228:231]\n\t" "global_load_dwordx4 a[124:127], %[va], s[76:77] offset:3072 sc1\n\t"
    "v_mfma_f32_16x16x32_bf16 v[228:231], a[148:151], a[52:55], v[228:231]\n\t" "s_add_u32 s76, s76, s86\n\t" "s_addc_u32 s77, s77, s87\n\t"
    "v_mfma_f32_16x16x32_bf16 v[232:235], a[144:147], a[40:43], v[232:235]\n\t" "ds_read_b64_tr_b16 a[132:133], %[vr0] offset:43008\n\t"
    "v_mfma_f32_16x16x32_bf16 v[232:235], a[148:151], a[56:59], v[232:235]\n\t" "ds_read_b64_tr_b16 a[134:135], %[vr0] offset:46592\n\t" "s_add_u32 m0, %[sl], 89088\n\t"
    "v_mfma_f32_16x16x32_bf16 v[236:239], a[144:147], a[44:47], v[236:239]\n\t" "s_mov_b64 exec, s[82:83]\n\t" "global_load_lds_dwordx4 %[vb3], s[78:79]\n\t" "s_mov_b64 exec, -1\n\t"
    "v_mfma_f32_16x16x32_bf16 v[236:239], a[148:151], a[60:63], v[236:239]\n\t" "s_add_u32 s78, s78, 229376\n\t" "s_addc_u32 s79, s79, 0\n\t" "s_waitcnt lgkmcnt(4)\n\t"
    "v_mfma_f32_16x16x32_bf16 v[240:243], a[152:155], a[32:35], v[240:243]\n\t" "ds_read_b64_tr_b16 a[136:137], %[vr0] offset:28704\n\t"
    "v_mfma_f32_16x16x32_bf16 v[240:243], a[156:159], a[48:51], v[240:243]\n\t" "ds_read_b64_tr_b16 a[138:139], %[vr0] offset:32288\n\t"
    "v_mfma_f32_16x16x32_bf16 v[244:247], a[152:155], a[36:39], v[244:247]\n\t" "v_mfma_f32_16x16x32_bf16 v[244:247], a[156:159], a[52:55], v[244:247]\n\t"
    "v_mfma_f32_16x16x32_bf16 v[248:251], a[152:155], a[40:43], v[248:251]\n\t" "ds_read_b64_tr_b16 a[140:141], %[vr0] offset:43040\n\t"
    "v_mfma_f32_16x16x32_bf16 v[248:251], a[156:159], a[56:59], v[248:251]\n\t" "ds_read_b64_tr_b16 a[142:143], %[vr0] offset:46624\n\t"
    "v_mfma_f32_16x16x32_bf16 v[252:255], a[152:155], a[44:47], v[252:255]\n\t" "v_mfma_f32_16x16x32_bf16 v[252:255], a[156:159], a[60:63], v[252:255]\n\t" ".Lloop_%=:\n\t" "s_cmp_eq_u32 s80, %[e1]\n\t"
    "s_cbranch_scc1 .LE1_%=\n\t" "s_waitcnt lgkmcnt(4) vmcnt(17)\n\t" "v_mfma_f32_16x16x32_bf16 v[32:35], a[128:131], a[64:67], v[32:35]\n\t" "s_barrier\n\t"
    "ds_read_b64_tr_b16 a[144:145], %[vr2] offset:28672\n\t" "v_mfma_f32_16x16x32_bf16 v[32:35], a[132:135], a[80:83], v[32:35]\n\t" "ds_read_b64_tr_b16 a[146:147], %[vr2] offset:32256\n\t"
    "v_mfma_f32_16x16x32_bf16 v[36:39], a[128:131], a[68:71], v[36:39]\n\t" "global_load_dwordx4 a[0:3], %[va], s[76:77] offset:0 sc1\n\t"
    "v_mfma_f32_16x16x32_bf16 v[36:39], a[132:135], a[84:87], v[36:39]\n\t" "v_mfma_f32_16x16x32_bf16 v[40:43], a[128:131], a[72:75], v[40:43]\n\t" "ds_read_b64_tr_b16 a[148:149], %[vr2] offset:43008\n\t"
    "v_mfma_f32_16x16x32_bf16 v[40:43], a[132:135], a[88:91], v[40:43]\n\t" "ds_read_b64_tr_b16 a[150:151], %[vr2] offset:46592\n\t" "s_add_u32 m0, %[sl], 100352\n\t"
    "v_mfma_f32_16x16x32_bf16 v[44:47], a[128:131], a[76:79], v[44:47]\n\t" "global_load_lds_dwordx4 %[vb0], s[78:79]\n\t" "v_mfma_f32_16x16x32_bf16 v[44:47], a[132:135], a[92:95], v[44:47]\n\t"
    "s_waitcnt lgkmcnt(4)\n\t" "v_mfma_f32_16x16x32_bf16 v[48:51], a[136:139], a[64:67], v[48:51]\n\t" "ds_read_b64_tr_b16 a[152:153], %[vr2] offset:28704\n\t"
    "v_mfma_f32_16x16x32_bf16 v[48:51], a[140:143], a[80:83], v[48:51]\n\t" "ds_read_b64_tr_b16 a[154:155], %[vr2] offset:32288\n\t" "v_mfma_f32_16x16x32_bf16 v[52:55], a[136:139], a[68:71], v[52:55]\n\t"
    "v_mfma_f32_16x16x32_bf16 v[52:55], a[140:143], a[84:87], v[52:55]\n\t" "v_mfma_f32_16x16x32_bf16 v[56:59], a[136:139], a[72:75], v[56:59]\n\t" "ds_read_b64_tr_b16 a[156:157], %[vr2] offset:43040\n\t"
    "v_mfma_f32_16x16x32_bf16 v[56:59], a[140:143], a[88:91], v[56:59]\n\t" "ds_read_b64_tr_b16 a[158:159], %[vr2] offset:46624\n\t" "v_mfma_f32_16x16x32_bf16 v[60:63], a[136:139], a[76:79], v[60:63]\n\t"
    "global_load_dwordx4 a[4:7], %[va], s[76:77] offset:1024 sc1\n\t" "v_mfma_f32_16x16x32_bf16 v[60:63], a[140:143], a[92:95], v[60:63]\n\t" "s_waitcnt lgkmcnt(4)\n\t"
    "v_mfma_f32_16x16x32_bf16 v[64:67], a[144:147], a[64:67], v[64:67]\n\t" "ds_read_b64_tr_b16 a[128:129], %[vr4] offset:28672\n\t" "v_mfma_f32_16x16x32_bf16 v[64:67], a[148:151], a[80:83], v[64:67]\n\t"
    "ds_read_b64_tr_b16 a[130:131], %[vr4] offset:32256\n\t" "s_add_u32 m0, %[sl], 101376\n\t" "v_mfma_f32_16x16x32_bf16 v[68:71], a[144:147], a[68:71], v[68:71]\n\t"
    "global_load_lds_dwordx4 %[vb1], s[78:79]\n\t" "v_mfma_f32_16x16x32_bf16 v[68:71], a[148:151], a[84:87], v[68:71]\n\t" "v_mfma_f32_16x16x32_bf16 v[72:75], a[144:147], a[72:75], v[72:75]\n\t"
    "ds_read_b64_tr_b16 a[132:133], %[vr4] offset:43008\n\t" "v_mfma_f32_16x16x32_bf16 v[72:75], a[148:151], a[88:91], v[72:75]\n\t" "ds_read_b64_tr_b16 a[134:135], %[vr4] offset:46592\n\t"
    "v_mfma_f32_16x16x32_bf16 v[76:79], a[144:147], a[76:79], v[76:79]\n\t" "v_mfma_f32_16x16x32_bf16 v[76:79], a[148:151], a[92:95], v[76:79]\n\t" "s_waitcnt lgkmcnt(4)\n\t"
    "v_mfma_f32_16x16x32_bf16 v[80:83], a[152:155], a[64:67], v[80:83]\n\t" "ds_read_b64_tr_b16 a[136:137], %[vr4] offset:28704\n\t" "v_mfma_f32_16x16x32_bf16 v[80:83], a[156:159], a[80:83], v[80:83]\n\t"
    "ds_read_b64_tr_b16 a[138:139], %[vr4] offset:32288\n\t" "v_mfma_f32_16x16x32_bf16 v[84:87], a[152:155], a[68:71], v[84:87]\n\t" "global_load_dwordx4 a[8:11], %[va], s[76:77] offset:2048 sc1\n\t"
    "v_mfma_f32_16x16x32_bf16 v[84:87], a[156:159], a[84:87], v[84:87]\n\t" "v_mfma_f32_16x16x32_bf16 v[88:91], a[152:155], a[72:75], v[88:91]\n\t" "ds_read_b64_tr_b16 a[140:141], %[vr4] offset:43040\n\t"
    "v_mfma_f32_16x16x32_bf16 v[88:91], a[156:159], a[88:91], v[88:91]\n\t" "ds_read_b64_tr_b16 a[142:143], %[vr4] offset:46624\n\t" "v_mfma_f32_16x16x32_bf16 v[92:95], a[152:155], a[76:79], v[92:95]\n\t"
    "v_mfma_f32_16x16x32_bf16 v[92:95], a[156:159], a[92:95], v[92:95]\n\t" "s_waitcnt lgkmcnt(4)\n\t" "v_mfma_f32_16x16x32_bf16 v[96:99], a[128:131], a[64:67], v[96:99]\n\t"
    "ds_read_b64_tr_b16 a[144:145], %[vr6] offset:28672\n\t" "v_mfma_f32_16x16x32_bf16 v[96:99], a[132:135], a[80:83], v[96:99]\n\t" "ds_read_b64_tr_b16 a[146:147], %[vr6] offset:32256\n\t"
    "s_add_u32 m0, %[sl], 102400\n\t" "v_mfma_f32_16x16x32_bf16 v[100:103], a[128:131], a[68:71], v[100:103]\n\t" "global_load_lds_dwordx4 %[vb2], s[78:79]\n\t"
    "v_mfma_f32_16x16x32_bf16 v[100:103], a[132:135], a[84:87], v[100:103]\n\t" "v_mfma_f32_16x16x32_bf16 v[104:107], a[128:131], a[72:75], v[104:107]\n\t"
    "ds_read_b64_tr_b16 a[148:149], %[vr6] offset:43008\n\t" "v_mfma_f32_16x16x32_bf16 v[104:107], a[132:135], a[88:91], v[104:107]\n\t" "ds_read_b64_tr_b16 a[150:151], %[vr6] offset:46592\n\t"
    "v_mfma_f32_16x16x32_bf16 v[108:111], a[128:131], a[76:79], v[108:111]\n\t" "global_load_dwordx4 a[12:15], %[va], s[76:77] offset:3072 sc1\n\t"
    "v_mfma_f32_16x16x32_bf16 v[108:111], a[132:135], a[92:95], v[108:111]\n\t" "s_add_u32 s76, s76, 16384\n\t" "s_addc_u32 s77, s77, 0\n\t" "s_waitcnt lgkmcnt(4)\n\t"
    "v_mfma_f32_16x16x32_bf16 v[112:115], a[136:139], a[64:67], v[112:115]\n\t" "ds_read_b64_tr_b16 a[152:153], %[vr0] offset:35840\n\t"
    "v_mfma_f32_16x16x32_bf16 v[112:115], a[140:143], a[80:83], v[112:115]\n\t" "ds_read_b64_tr_b16 a[154:155], %[vr0] offset:39424\n\t"
    "v_mfma_f32_16x16x32_bf16 v[116:119], a[136:139], a[68:71], v[116:119]\n\t" "v_mfma_f32_16x16x32_bf16 v[116:119], a[140:143], a[84:87], v[116:119]\n\t"
    "v_mfma_f32_16x16x32_bf16 v[120:123], a[136:139], a[72:75], v[120:123]\n\t" "ds_read_b64_tr_b16 a[156:157], %[vr0] offset:50176\n\t"
    "v_mfma_f32_16x16x32_bf16 v[120:123], a[140:143], a[88:91], v[120:123]\n\t" "ds_read_b64_tr_b16 a[158:159], %[vr0] offset:53760\n\t" "s_add_u32 m0, %[sl], 103424\n\t"
    "v_mfma_f32_16x16x32_bf16 v[124:127], a[136:139], a[76:79], v[124:127]\n\t" "s_mov_b64 exec, s[82:83]\n\t" "global_load_lds_dwordx4 %[vb3], s[78:79]\n\t" "s_mov_b64 exec, -1\n\t"
    "v_mfma_f32_16x16x32_bf16 v[124:127], a[140:143], a[92:95], v[124:127]\n\t" "s_add_u32 s78, s78, 229376\n\t" "s_addc_u32 s79, s79, 0\n\t" "s_waitcnt lgkmcnt(4)\n\t"
    "v_mfma_f32_16x16x32_bf16 v[128:131], a[144:147], a[64:67], v[128:131]\n\t" "ds_read_b64_tr_b16 a[128:129], %[vr0] offset:35872\n\t"
    "v_mfma_f32_16x16x32_bf16 v[128:131], a[148:151], a[80:83], v[128:131]\n\t" "ds_read_b64_tr_b16 a[130:131], %[vr0] offset:39456\n\t"
    "v_mfma_f32_16x16x32_bf16 v[132:135], a[144:147], a[68:71], v[132:135]\n\t" "global_load_dword v29, %[va], %[cd]\n\t" "v_mfma_f32_16x16x32_bf16 v[132:135], a[148:151], a[84:87], v[132:135]\n\t"
    "v_mfma_f32_16x16x32_bf16 v[136:139], a[144:147], a[72:75], v[136:139]\n\t" "ds_read_b64_tr_b16 a[132:133], %[vr0] offset:50208\n\t"
    "v_mfma_f32_16x16x32_bf16 v[136:139], a[148:151], a[88:91], v[136:139]\n\t" "ds_read_b64_tr_b16 a[134:135], %[vr0] offset:53792\n\t"
    "v_mfma_f32_16x16x32_bf16 v[140:143], a[144:147], a[76:79], v[140:143]\n\t" "v_mfma_f32_16x16x32_bf16 v[140:143], a[148:151], a[92:95], v[140:143]\n\t" "s_waitcnt lgkmcnt(4)\n\t"
    "v_mfma_f32_16x16x32_bf16 v[144:147], a[152:155], a[64:67], v[144:147]\n\t" "ds_read_b64_tr_b16 a[136:137], %[vr2] offset:35840\n\t"
    "v_mfma_f32_16x16x32_bf16 v[144:147], a[156:159], a[80:83], v[144:147]\n\t" "ds_read_b64_tr_b16 a[138:139], %[vr2] offset:39424\n\t"
    "v_mfma_f32_16x16x32_bf16 v[148:151], a[152:155], a[68:71], v[148:151]\n\t" "global_load_dwordx4 a[16:19], %[va], s[76:77] offset:0 sc1\n\t"
    "v_mfma_f32_16x16x32_bf16 v[148:151], a[156:159], a[84:87], v[148:151]\n\t" "v_mfma_f32_16x16x32_bf16 v[152:155], a[152:155], a[72:75], v[152:155]\n\t"
    "ds_read_b64_tr_b16 a[140:141], %[vr2] offset:50176\n\t" "v_mfma_f32_16x16x32_bf16 v[152:155], a[156:159], a[88:91], v[152:155]\n\t" "ds_read_b64_tr_b16 a[142:143], %[vr2] offset:53760\n\t"
    "s_add_u32 m0, %[sl], 0\n\t" "v_mfma_f32_16x16x32_bf16 v[156:159], a[152:155], a[76:79], v[156:159]\n\t" "global_load_lds_dwordx4 %[vb0], s[78:79]\n\t"
    "v_mfma_f32_16x16x32_bf16 v[156:159], a[156:159], a[92:95], v[156:159]\n\t" "s_waitcnt lgkmcnt(4)\n\t" "v_mfma_f32_16x16x32_bf16 v[160:163], a[128:131], a[64:67], v[160:163]\n\t"
    "ds_read_b64_tr_b16 a[144:145], %[vr2] offset:35872\n\t" "v_mfma_f32_16x16x32_bf16 v[160:163], a[132:135], a[80:83], v[160:163]\n\t" "ds_read_b64_tr_b16 a[146:147], %[vr2] offset:39456\n\t"
    "v_mfma_f32_16x16x32_bf16 v[164:167], a[128:131], a[68:71], v[164:167]\n\t" "v_mfma_f32_16x16x32_bf16 v[164:167], a[132:135], a[84:87], v[164:167]\n\t"
    "v_mfma_f32_16x16x32_bf16 v[168:171], a[128:131], a[72:75], v[168:171]\n\t" "ds_read_b64_tr_b16 a[148:149], %[vr2] offset:50208\n\t"
    "v_mfma_f32_16x16x32_bf16 v[168:171], a[132:135], a[88:91], v[168:171]\n\t" "ds_read_b64_tr_b16 a[150:151], %[vr2] offset:53792\n\t"
    "v_mfma_f32_16x16x32_bf16 v[172:175], a[128:131], a[76:79], v[172:175]\n\t" "global_load_dwordx4 a[20:23], %[va], s[76:77] offset:1024 sc1\n\t"
    "v_mfma_f32_16x16x32_bf16 v[172:175], a[132:135], a[92:95], v[172:175]\n\t" "s_waitcnt lgkmcnt(4)\n\t" "v_mfma_f32_16x16x32_bf16 v[176:179], a[136:139], a[64:67], v[176:179]\n\t"
    "ds_read_b64_tr_b16 a[152:153], %[vr4] offset:35840\n\t" "v_mfma_f32_16x16x32_bf16 v[176:179], a[140:143], a[80:83], v[176:179]\n\t" "ds_read_b64_tr_b16 a[154:155], %[vr4] offset:39424\n\t"
    "s_add_u32 m0, %[sl], 1024\n\t" "v_mfma_f32_16x16x32_bf16 v[180:183], a[136:139], a[68:71], v[180:183]\n\t" "global_load_lds_dwordx4 %[vb1], s[78:79]\n\t"
    "v_mfma_f32_16x16x32_bf16 v[180:183], a[140:143], a[84:87], v[180:183]\n\t" "v_mfma_f32_16x16x32_bf16 v[184:187], a[136:139], a[72:75], v[184:187]\n\t"
    "ds_read_b64_tr_b16 a[156:157], %[vr4] offset:50176\n\t" "v_mfma_f32_16x16x32_bf16 v[184:187], a[140:143], a[88:91], v[184:187]\n\t" "ds_read_b64_tr_b16 a[158:159], %[vr4] offset:53760\n\t"
    "v_mfma_f32_16x16x32_bf16 v[188:191], a[136:139], a[76:79], v[188:191]\n\t" "v_mfma_f32_16x16x32_bf16 v[188:191], a[140:143], a[92:95], v[188:191]\n\t" "s_waitcnt lgkmcnt(4)\n\t"
    "v_mfma_f32_16x16x32_bf16 v[192:195], a[144:147], a[64:67], v[192:195]\n\t" "ds_read_b64_tr_b16 a[128:129], %[vr4] offset:35872\n\t"
    "v_mfma_f32_16x16x32_bf16 v[192:195], a[148:151], a[80:83], v[192:195]\n\t" "ds_read_b64_tr_b16 a[130:131], %[vr4] offset:39456\n\t"
    "v_mfma_f32_16x16x32_bf16 v[196:199], a[144:147], a[68:71], v[196:199]\n\t" "global_load_dwordx4 a[24:27], %[va], s[76:77] offset:2048 sc1\n\t"
    "v_mfma_f32_16x16x32_bf16 v[196:199], a[148:151], a[84:87], v[196:199]\n\t" "v_mfma_f32_16x16x32_bf16 v[200:203], a[144:147], a[72:75], v[200:203]\n\t"
    "ds_read_b64_tr_b16 a[132:133], %[vr4] offset:50208\n\t" "v_mfma_f32_16x16x32_bf16 v[200:203], a[148:151], a[88:91], v[200:203]\n\t" "ds_read_b64_tr_b16 a[134:135], %[vr4] offset:53792\n\t"
    "v_mfma_f32_16x16x32_bf16 v[204:207], a[144:147], a[76:79], v[204:207]\n\t" "v_mfma_f32_16x16x32_bf16 v[204:207], a[148:151], a[92:95], v[204:207]\n\t" "s_waitcnt lgkmcnt(4)\n\t"
    "v_mfma_f32_16x16x32_bf16 v[208:211], a[152:155], a[64:67], v[208:211]\n\t" "ds_read_b64_tr_b16 a[136:137], %[vr6] offset:35840\n\t"
    "v_mfma_f32_16x16x32_bf16 v[208:211], a[156:159], a[80:83], v[208:211]\n\t" "ds_read_b64_tr_b16 a[138:139], %[vr6] offset:39424\n\t" "s_add_u32 m0, %[sl], 2048\n\t"
    "v_mfma_f32_16x16x32_bf16 v[212:215], a[152:155], a[68:71], v[212:215]\n\t" "global_load_lds_dwordx4 %[vb2], s[78:79]\n\t" "v_mfma_f32_16x16x32_bf16 v[212:215], a[156:159], a[84:87], v[212:215]\n\t"
    "v_mfma_f32_16x16x32_bf16 v[216:219], a[152:155], a[72:75], v[216:219]\n\t" "ds_read_b64_tr_b16 a[140:141], %[vr6] offset:50176\n\t"
    "v_mfma_f32_16x16x32_bf16 v[216:219], a[156:159], a[88:91], v[216:219]\n\t" "ds_read_b64_tr_b16 a[142:143], %[vr6] offset:53760\n\t"
    "v_mfma_f32_16x16x32_bf16 v[220:223], a[152:155], a[76:79], v[220:223]\n\t" "global_load_dwordx4 a[28:31], %[va], s[76:77] offset:3072 sc1\n\t"
    "v_mfma_f32_16x16x32_bf16 v[220:223], a[156:159], a[92:95], v[220:223]\n\t" "s_add_u32 s76, s76, 16384\n\t" "s_addc_u32 s77, s77, 0\n\t" "s_waitcnt lgkmcnt(4)\n\t"
    "v_mfma_f32_16x16x32_bf16 v[224:227], a[128:131], a[64:67], v[224:227]\n\t" "s_waitcnt vmcnt(24)\n\t" "s_barrier\n\t" "ds_read_b64_tr_b16 a[144:145], %[vr1] offset:0\n\t"
    "v_mfma_f32_16x16x32_bf16 v[224:227], a[132:135], a[80:83], v[224:227]\n\t" "ds_read_b64_tr_b16 a[146:147], %[vr1] offset:3584\n\t"
    "v_mfma_f32_16x16x32_bf16 v[228:231], a[128:131], a[68:71], v[228:231]\n\t" "v_mfma_f32_16x16x32_bf16 v[228:231], a[132:135], a[84:87], v[228:231]\n\t"
    "v_mfma_f32_16x16x32_bf16 v[232:235], a[128:131], a[72:75], v[232:235]\n\t" "ds_read_b64_tr_b16 a[148:149], %[vr1] offset:14336\n\t"
    "v_mfma_f32_16x16x32_bf16 v[232:235], a[132:135], a[88:91], v[232:235]\n\t" "ds_read_b64_tr_b16 a[150:151], %[vr1] offset:17920\n\t" "s_add_u32 m0, %[sl], 3072\n\t"
    "v_mfma_f32_16x16x32_bf16 v[236:239], a[128:131], a[76:79], v[236:239]\n\t" "s_mov_b64 exec, s[82:83]\n\t" "global_load_lds_dwordx4 %[vb3], s[78:79]\n\t" "s_mov_b64 exec, -1\n\t"
    "v_mfma_f32_16x16x32_bf16 v[236:239], a[132:135], a[92:95], v[236:239]\n\t" "s_add_u32 s78, s78, 229376\n\t" "s_addc_u32 s79, s79, 0\n\t" "s_waitcnt lgkmcnt(4)\n\t"
    "v_mfma_f32_16x16x32_bf16 v[240:243], a[136:139], a[64:67], v[240:243]\n\t" "ds_read_b64_tr_b16 a[152:153], %[vr1] offset:32\n\t"
    "v_mfma_f32_16x16x32_bf16 v[240:243], a[140:143], a[80:83], v[240:243]\n\t" "ds_read_b64_tr_b16 a[154:155], %[vr1] offset:3616\n\t"
    "v_mfma_f32_16x16x32_bf16 v[244:247], a[136:139], a[68:71], v[244:247]\n\t" "v_mfma_f32_16x16x32_bf16 v[244:247], a[140:143], a[84:87], v[244:247]\n\t"
    "v_mfma_f32_16x16x32_bf16 v[248:251], a[136:139], a[72:75], v[248:251]\n\t" "ds_read_b64_tr_b16 a[156:157], %[vr1] offset:14368\n\t"
    "v_mfma_f32_16x16x32_bf16 v[248:251], a[140:143], a[88:91], v[248:251]\n\t" "ds_read_b64_tr_b16 a[158:159], %[vr1] offset:17952\n\t"
    "v_mfma_f32_16x16x32_bf16 v[252:255], a[136:139], a[76:79], v[252:255]\n\t" "v_mfma_f32_16x16x32_bf16 v[252:255], a[140:143], a[92:95], v[252:255]\n\t" "s_waitcnt lgkmcnt(4) vmcnt(18)\n\t"
    "v_mfma_f32_16x16x32_bf16 v[32:35], a[144:147], a[96:99], v[32:35]\n\t" "s_barrier\n\t" "ds_read_b64_tr_b16 a[128:129], %[vr3] offset:0\n\t"
    "v_mfma_f32_16x16x32_bf16 v[32:35], a[148:151], a[112:115], v[32:35]\n\t" "ds_read_b64_tr_b16 a[130:131], %[vr3] offset:3584\n\t"
    "v_mfma_f32_16x16x32_bf16 v[36:39], a[144:147], a[100:103], v[36:39]\n\t" "global_load_dwordx4 a[32:35], %[va], s[76:77] offset:0 sc1\n\t"
    "v_mfma_f32_16x16x32_bf16 v[36:39], a[148:151], a[116:119], v[36:39]\n\t" "v_mfma_f32_16x16x32_bf16 v[40:43], a[144:147], a[104:107], v[40:43]\n\t"
    "ds_read_b64_tr_b16 a[132:133], %[vr3] offset:14336\n\t" "v_mfma_f32_16x16x32_bf16 v[40:43], a[148:151], a[120:123], v[40:43]\n\t" "ds_read_b64_tr_b16 a[134:135], %[vr3] offset:17920\n\t"
    "s_add_u32 m0, %[sl], 14336\n\t" "v_mfma_f32_16x16x32_bf16 v[44:47], a[144:147], a[108:111], v[44:47]\n\t" "global_load_lds_dwordx4 %[vb0], s[78:79]\n\t"
    "v_mfma_f32_16x16x32_bf16 v[44:47], a[148:151], a[124:127], v[44:47]\n\t" "s_waitcnt lgkmcnt(4)\n\t" "v_mfma_f32_16x16x32_bf16 v[48:51], a[152:155], a[96:99], v[48:51]\n\t"
    "ds_read_b64_tr_b16 a[136:137], %[vr3] offset:32\n\t" "v_mfma_f32_16x16x32_bf16 v[48:51], a[156:159], a[112:115], v[48:51]\n\t" "ds_read_b64_tr_b16 a[138:139], %[vr3] offset:3616\n\t"
    "v_mfma_f32_16x16x32_bf16 v[52:55], a[152:155], a[100:103], v[52:55]\n\t" "v_mfma_f32_16x16x32_bf16 v[52:55], a[156:159], a[116:119], v[52:55]\n\t"
    "v_mfma_f32_16x16x32_bf16 v[56:59], a[152:155], a[104:107], v[56:59]\n\t" "ds_read_b64_tr_b16 a[140:141], %[vr3] offset:14368\n\t"
    "v_mfma_f32_16x16x32_bf16 v[56:59], a[156:159], a[120:123], v[56:59]\n\t" "ds_read_b64_tr_b16 a[142:143], %[vr3] offset:17952\n\t"
    "v_mfma_f32_16x16x32_bf16 v[60:63], a[152:155], a[108:111], v[60:63]\n\t" "global_load_dwordx4 a[36:39], %[va], s[76:77] offset:1024 sc1\n\t"
    "v_mfma_f32_16x16x32_bf16 v[60:63], a[156:159], a[124:127], v[60:63]\n\t" "s_waitcnt lgkmcnt(4)\n\t" "v_mfma_f32_16x16x32_bf16 v[64:67], a[128:131], a[96:99], v[64:67]\n\t"
    "ds_read_b64_tr_b16 a[144:145], %[vr5] offset:0\n\t" "v_mfma_f32_16x16x32_bf16 v[64:67], a[132:135], a[112:115], v[64:67]\n\t" "ds_read_b64_tr_b16 a[146:147], %[vr5] offset:3584\n\t"
    "s_add_u32 m0, %[sl], 15360\n\t" "v_mfma_f32_16x16x32_bf16 v[68:71], a[128:131], a[100:103], v[68:71]\n\t" "global_load_lds_dwordx4 %[vb1], s[78:79]\n\t"
    "v_mfma_f32_16x16x32_bf16 v[68:71], a[132:135], a[116:119], v[68:71]\n\t" "v_mfma_f32_16x16x32_bf16 v[72:75], a[128:131], a[104:107], v[72:75]\n\t"
    "ds_read_b64_tr_b16 a[148:149], %[vr5] offset:14336\n\t" "v_mfma_f32_16x16x32_bf16 v[72:75], a[132:135], a[120:123], v[72:75]\n\t" "ds_read_b64_tr_b16 a[150:151], %[vr5] offset:17920\n\t"
    "v_mfma_f32_16x16x32_bf16 v[76:79], a[128:131], a[108:111], v[76:79]\n\t" "v_mfma_f32_16x16x32_bf16 v[76:79], a[132:135], a[124:127], v[76:79]\n\t" "s_waitcnt lgkmcnt(4)\n\t"
    "v_mfma_f32_16x16x32_bf16 v[80:83], a[136:139], a[96:99], v[80:83]\n\t" "ds_read_b64_tr_b16 a[152:153], %[vr5] offset:32\n\t" "v_mfma_f32_16x16x32_bf16 v[80:83], a[140:143], a[112:115], v[80:83]\n\t"
    "ds_read_b64_tr_b16 a[154:155], %[vr5] offset:3616\n\t" "v_mfma_f32_16x16x32_bf16 v[84:87], a[136:139], a[100:103], v[84:87]\n\t" "global_load_dwordx4 a[40:43], %[va], s[76:77] offset:2048 sc1\n\t"
    "v_mfma_f32_16x16x32_bf16 v[84:87], a[140:143], a[116:119], v[84:87]\n\t" "v_mfma_f32_16x16x32_bf16 v[88:91], a[136:139], a[104:107], v[88:91]\n\t"
    "ds_read_b64_tr_b16 a[156:157], %[vr5] offset:14368\n\t" "v_mfma_f32_16x16x32_bf16 v[88:91], a[140:143], a[120:123], v[88:91]\n\t" "ds_read_b64_tr_b16 a[158:159], %[vr5] offset:17952\n\t"
    "v_mfma_f32_16x16x32_bf16 v[92:95], a[136:139], a[108:111], v[92:95]\n\t" "v_mfma_f32_16x16x32_bf16 v[92:95], a[140:143], a[124:127], v[92:95]\n\t" "s_waitcnt lgkmcnt(4)\n\t"
    "v_mfma_f32_16x16x32_bf16 v[96:99], a[144:147], a[96:99], v[96:99]\n\t" "ds_read_b64_tr_b16 a[128:129], %[vr7] offset:0\n\t" "v_mfma_f32_16x16x32_bf16 v[96:99], a[148:151], a[112:115], v[96:99]\n\t"
    "ds_read_b64_tr_b16 a[130:131], %[vr7] offset:3584\n\t" "s_add_u32 m0, %[sl], 16384\n\t" "v_mfma_f32_16x16x32_bf16 v[100:103], a[144:147], a[100:103], v[100:103]\n\t"
    "global_load_lds_dwordx4 %[vb2], s[78:79]\n\t" "v_mfma_f32_16x16x32_bf16 v[100:103], a[148:151], a[116:119], v[100:103]\n\t"
    "v_mfma_f32_16x16x32_bf16 v[104:107], a[144:147], a[104:107], v[104:107]\n\t" "ds_read_b64_tr_b16 a[132:133], %[vr7] offset:14336\n\t"
    "v_mfma_f32_16x16x32_bf16 v[104:107], a[148:151], a[120:123], v[104:107]\n\t" "ds_read_b64_tr_b16 a[134:135], %[vr7] offset:17920\n\t"
    "v_mfma_f32_16x16x32_bf16 v[108:111], a[144:147], a[108:111], v[108:111]\n\t" "global_load_dwordx4 a[44:47], %[va], s[76:77] offset:3072 sc1\n\t"
    "v_mfma_f32_16x16x32_bf16 v[108:111], a[148:151], a[124:127], v[108:111]\n\t" "s_add_u32 s76, s76, 16384\n\t" "s_addc_u32 s77, s77, 0\n\t" "s_waitcnt lgkmcnt(4)\n\t"
    "v_mfma_f32_16x16x32_bf16 v[112:115], a[152:155], a[96:99], v[112:115]\n\t" "ds_read_b64_tr_b16 a[136:137], %[vr1] offset:7168\n\t"
    "v_mfma_f32_16x16x32_bf16 v[112:115], a[156:159], a[112:115], v[112:115]\n\t" "ds_read_b64_tr_b16 a[138:139], %[vr1] offset:10752\n\t"
    "v_mfma_f32_16x16x32_bf16 v[116:119], a[152:155], a[100:103], v[116:119]\n\t" "v_mfma_f32_16x16x32_bf16 v[116:119], a[156:159], a[116:119], v[116:119]\n\t"
    "v_mfma_f32_16x16x32_bf16 v[120:123], a[152:155], a[104:107], v[120:123]\n\t" "ds_read_b64_tr_b16 a[140:141], %[vr1] offset:21504\n\t"
    "v_mfma_f32_16x16x32_bf16 v[120:123], a[156:159], a[120:123], v[120:123]\n\t" "ds_read_b64_tr_b16 a[142:143], %[vr1] offset:25088\n\t" "s_add_u32 m0, %[sl], 17408\n\t"
    "v_mfma_f32_16x16x32_bf16 v[124:127], a[152:155], a[108:111], v[124:127]\n\t" "s_mov_b64 exec, s[82:83]\n\t" "global_load_lds_dwordx4 %[vb3], s[78:79]\n\t" "s_mov_b64 exec, -1\n\t"
    "v_mfma_f32_16x16x32_bf16 v[124:127], a[156:159], a[124:127], v[124:127]\n\t" "s_add_u32 s78, s78, 229376\n\t" "s_addc_u32 s79, s79, 0\n\t" "s_waitcnt lgkmcnt(4)\n\t"
    "v_mfma_f32_16x16x32_bf16 v[128:131], a[128:131], a[96:99], v[128:131]\n\t" "ds_read_b64_tr_b16 a[144:145], %[vr1] offset:7200\n\t"
    "v_mfma_f32_16x16x32_bf16 v[128:131], a[132:135], a[112:115], v[128:131]\n\t" "ds_read_b64_tr_b16 a[146:147], %[vr1] offset:10784\n\t"
    "v_mfma_f32_16x16x32_bf16 v[132:135], a[128:131], a[100:103], v[132:135]\n\t" "v_mfma_f32_16x16x32_bf16 v[132:135], a[132:135], a[116:119], v[132:135]\n\t"
    "v_mfma_f32_16x16x32_bf16 v[136:139], a[128:131], a[104:107], v[136:139]\n\t" "ds_read_b64_tr_b16 a[148:149], %[vr1] offset:21536\n\t"
    "v_mfma_f32_16x16x32_bf16 v[136:139], a[132:135], a[120:123], v[136:139]\n\t" "ds_read_b64_tr_b16 a[150:151], %[vr1] offset:25120\n\t"
    "v_mfma_f32_16x16x32_bf16 v[140:143], a[128:131], a[108:111], v[140:143]\n\t" "v_mfma_f32_16x16x32_bf16 v[140:143], a[132:135], a[124:127], v[140:143]\n\t" "s_waitcnt lgkmcnt(4)\n\t"
    "v_mfma_f32_16x16x32_bf16 v[144:147], a[136:139], a[96:99], v[144:147]\n\t" "ds_read_b64_tr_b16 a[152:153], %[vr3] offset:7168\n\t"
    "v_mfma_f32_16x16x32_bf16 v[144:147], a[140:143], a[112:115], v[144:147]\n\t" "ds_read_b64_tr_b16 a[154:155], %[vr3] offset:10752\n\t"
    "v_mfma_f32_16x16x32_bf16 v[148:151], a[136:139], a[100:103], v[148:151]\n\t" "global_load_dwordx4 a[48:51], %[va], s[76:77] offset:0 sc1\n\t"
    "v_mfma_f32_16x16x32_bf16 v[148:151], a[140:143], a[116:119], v[148:151]\n\t" "v_mfma_f32_16x16x32_bf16 v[152:155], a[136:139], a[104:107], v[152:155]\n\t"
    "ds_read_b64_tr_b16 a[156:157], %[vr3] offset:21504\n\t" "v_mfma_f32_16x16x32_bf16 v[152:155], a[140:143], a[120:123], v[152:155]\n\t" "ds_read_b64_tr_b16 a[158:159], %[vr3] offset:25088\n\t"
    "s_add_u32 m0, %[sl], 28672\n\t" "v_mfma_f32_16x16x32_bf16 v[156:159], a[136:139], a[108:111], v[156:159]\n\t" "global_load_lds_dwordx4 %[vb0], s[78:79]\n\t"
    "v_mfma_f32_16x16x32_bf16 v[156:159], a[140:143], a[124:127], v[156:159]\n\t" "s_waitcnt lgkmcnt(4)\n\t" "v_mfma_f32_16x16x32_bf16 v[160:163], a[144:147], a[96:99], v[160:163]\n\t"
    "ds_read_b64_tr_b16 a[128:129], %[vr3] offset:7200\n\t" "v_mfma_f32_16x16x32_bf16 v[160:163], a[148:151], a[112:115], v[160:163]\n\t" "ds_read_b64_tr_b16 a[130:131], %[vr3] offset:10784\n\t"
    "v_mfma_f32_16x16x32_bf16 v[164:167], a[144:147], a[100:103], v[164:167]\n\t" "v_mfma_f32_16x16x32_bf16 v[164:167], a[148:151], a[116:119], v[164:167]\n\t"
    "v_mfma_f32_16x16x32_bf16 v[168:171], a[144:147], a[104:107], v[168:171]\n\t" "ds_read_b64_tr_b16 a[132:133], %[vr3] offset:21536\n\t"
    "v_mfma_f32_16x16x32_bf16 v[168:171], a[148:151], a[120:123], v[168:171]\n\t" "ds_read_b64_tr_b16 a[134:135], %[vr3] offset:25120\n\t"
    "v_mfma_f32_16x16x32_bf16 v[172:175], a[144:147], a[108:111], v[172:175]\n\t" "global_load_dwordx4 a[52:55], %[va], s[76:77] offset:1024 sc1\n\t"
    "v_mfma_f32_16x16x32_bf16 v[172:175], a[148:151], a[124:127], v[172:175]\n\t" "s_waitcnt lgkmcnt(4)\n\t" "v_mfma_f32_16x16x32_bf16 v[176:179], a[152:155], a[96:99], v[176:179]\n\t"
    "ds_read_b64_tr_b16 a[136:137], %[vr5] offset:7168\n\t" "v_mfma_f32_16x16x32_bf16 v[176:179], a[156:159], a[112:115], v[176:179]\n\t" "ds_read_b64_tr_b16 a[138:139], %[vr5] offset:10752\n\t"
    "s_add_u32 m0, %[sl], 29696\n\t" "v_mfma_f32_16x16x32_bf16 v[180:183], a[152:155], a[100:103], v[180:183]\n\t" "global_load_lds_dwordx4 %[vb1], s[78:79]\n\t"
    "v_mfma_f32_16x16x32_bf16 v[180:183], a[156:159], a[116:119], v[180:183]\n\t" "v_mfma_f32_16x16x32_bf16 v[184:187], a[152:155], a[104:107], v[184:187]\n\t"
    "ds_read_b64_tr_b16 a[140:141], %[vr5] offset:21504\n\t" "v_mfma_f32_16x16x32_bf16 v[184:187], a[156:159], a[120:123], v[184:187]\n\t" "ds_read_b64_tr_b16 a[142:143], %[vr5] offset:25088\n\t"
    "v_mfma_f32_16x16x32_bf16 v[188:191], a[152:155], a[108:111], v[188:191]\n\t" "v_mfma_f32_16x16x32_bf16 v[188:191], a[156:159], a[124:127], v[188:191]\n\t" "s_waitcnt lgkmcnt(4)\n\t"
    "v_mfma_f32_16x16x32_bf16 v[192:195], a[128:131], a[96:99], v[192:195]\n\t" "ds_read_b64_tr_b16 a[144:145], %[vr5] offset:7200\n\t"
    "v_mfma_f32_16x16x32_bf16 v[192:195], a[132:135], a[112:115], v[192:195]\n\t" "ds_read_b64_tr_b16 a[146:147], %[vr5] offset:10784\n\t"
    "v_mfma_f32_16x16x32_bf16 v[196:199], a[128:131], a[100:103], v[196:199]\n\t" "global_load_dwordx4 a[56:59], %[va], s[76:77] offset:2048 sc1\n\t"
    "v_mfma_f32_16x16x32_bf16 v[196:199], a[132:135], a[116:119], v[196:199]\n\t" "v_mfma_f32_16x16x32_bf16 v[200:203], a[128:131], a[104:107], v[200:203]\n\t"
    "ds_read_b64_tr_b16 a[148:149], %[vr5] offset:21536\n\t" "v_mfma_f32_16x16x32_bf16 v[200:203], a[132:135], a[120:123], v[200:203]\n\t" "ds_read_b64_tr_b16 a[150:151], %[vr5] offset:25120\n\t"
    "v_mfma_f32_16x16x32_bf16 v[204:207], a[128:131], a[108:111], v[204:207]\n\t" "v_mfma_f32_16x16x32_bf16 v[204:207], a[132:135], a[124:127], v[204:207]\n\t" "s_waitcnt lgkmcnt(4)\n\t"
    "v_mfma_f32_16x16x32_bf16 v[208:211], a[136:139], a[96:99], v[208:211]\n\t" "ds_read_b64_tr_b16 a[152:153], %[vr7] offset:7168\n\t"
    "v_mfma_f32_16x16x32_bf16 v[208:211], a[140:143], a[112:115], v[208:211]\n\t" "ds_read_b64_tr_b16 a[154:155], %[vr7] offset:10752\n\t" "s_add_u32 m0, %[sl], 30720\n\t"
    "v_mfma_f32_16x16x32_bf16 v[212:215], a[136:139], a[100:103], v[212:215]\n\t" "global_load_lds_dwordx4 %[vb2], s[78:79]\n\t"
    "v_mfma_f32_16x16x32_bf16 v[212:215], a[140:143], a[116:119], v[212:215]\n\t" "v_mfma_f32_16x16x32_bf16 v[216:219], a[136:139], a[104:107], v[216:219]\n\t"
    "ds_read_b64_tr_b16 a[156:157], %[vr7] offset:21504\n\t" "v_mfma_f32_16x16x32_bf16 v[216:219], a[140:143], a[120:123], v[216:219]\n\t" "ds_read_b64_tr_b16 a[158:159], %[vr7] offset:25088\n\t"
    "v_mfma_f32_16x16x32_bf16 v[220:223], a[136:139], a[108:111], v[220:223]\n\t" "global_load_dwordx4 a[60:63], %[va], s[76:77] offset:3072 sc1\n\t"
    "v_mfma_f32_16x16x32_bf16 v[220:223], a[140:143], a[124:127], v[220:223]\n\t" "s_add_u32 s76, s76, 16384\n\t" "s_addc_u32 s77, s77, 0\n\t" "s_waitcnt lgkmcnt(4)\n\t"
    "v_mfma_f32_16x16x32_bf16 v[224:227], a[144:147], a[96:99], v[224:227]\n\t" "s_waitcnt vmcnt(24)\n\t" "s_barrier\n\t" "ds_read_b64_tr_b16 a[128:129], %[vr1] offset:28672\n\t"
    "v_mfma_f32_16x16x32_bf16 v[224:227], a[148:151], a[112:115], v[224:227]\n\t" "ds_read_b64_tr_b16 a[130:131], %[vr1] offset:32256\n\t"
    "v_mfma_f32_16x16x32_bf16 v[228:231], a[144:147], a[100:103], v[228:231]\n\t" "v_mfma_f32_16x16x32_bf16 v[228:231], a[148:151], a[116:119], v[228:231]\n\t"
    "v_mfma_f32_16x16x32_bf16 v[232:235], a[144:147], a[104:107], v[232:235]\n\t" "ds_read_b64_tr_b16 a[132:133], %[vr1] offset:43008\n\t"
    "v_mfma_f32_16x16x32_bf16 v[232:235], a[148:151], a[120:123], v[232:235]\n\t" "ds_read_b64_tr_b16 a[134:135], %[vr1] offset:46592\n\t" "s_add_u32 m0, %[sl], 31744\n\t"
    "v_mfma_f32_16x16x32_bf16 v[236:239], a[144:147], a[108:111], v[236:239]\n\t" "s_mov_b64 exec, s[82:83]\n\t" "global_load_lds_dwordx4 %[vb3], s[78:79]\n\t" "s_mov_b64 exec, -1\n\t"
    "v_mfma_f32_16x16x32_bf16 v[236:239], a[148:151], a[124:127], v[236:239]\n\t" "s_add_u32 s78, s78, 229376\n\t" "s_addc_u32 s79, s79, 0\n\t" "s_waitcnt lgkmcnt(4)\n\t"
    "v_mfma_f32_16x16x32_bf16 v[240:243], a[152:155], a[96:99], v[240:243]\n\t" "ds_read_b64_tr_b16 a[136:137], %[vr1] offset:28704\n\t"
    "v_mfma_f32_16x16x32_bf16 v[240:243], a[156:159], a[112:115], v[240:243]\n\t" "ds_read_b64_tr_b16 a[138:139], %[vr1] offset:32288\n\t"
    "v_mfma_f32_16x16x32_bf16 v[244:247], a[152:155], a[100:103], v[244:247]\n\t" "v_mfma_f32_16x16x32_bf16 v[244:247], a[156:159], a[116:119], v[244:247]\n\t"
    "v_mfma_f32_16x16x32_bf16 v[248:251], a[152:155], a[104:107], v[248:251]\n\t" "ds_read_b64_tr_b16 a[140:141], %[vr1] offset:43040\n\t"
    "v_mfma_f32_16x16x32_bf16 v[248:251], a[156:159], a[120:123], v[248:251]\n\t" "ds_read_b64_tr_b16 a[142:143], %[vr1] offset:46624\n\t"
    "v_mfma_f32_16x16x32_bf16 v[252:255], a[152:155], a[108:111], v[252:255]\n\t" "v_mfma_f32_16x16x32_bf16 v[252:255], a[156:159], a[124:127], v[252:255]\n\t" ".Ld1_%=:\n\t" "s_cmp_eq_u32 s80, 0\n\t"
    "s_cselect_b32 s86, %[wa], 16384\n\t" "s_ashr_i32 s87, s86, 31\n\t" "s_cmp_eq_u32 s80, 0\n\t" "s_cselect_b32 s88, %[wb], 229376\n\t" "s_ashr_i32 s89, s88, 31\n\t" "s_cmp_eq_u32 s80, %[e0]\n\t"
    "s_cbranch_scc1 .LE0_%=\n\t" "s_waitcnt lgkmcnt(4) vmcnt(17)\n\t" "v_mfma_f32_16x16x32_bf16 v[32:35], a[128:131], a[0:3], v[32:35]\n\t" "s_barrier\n\t"
    "ds_read_b64_tr_b16 a[144:145], %[vr3] offset:28672\n\t" "v_mfma_f32_16x16x32_bf16 v[32:35], a[132:135], a[16:19], v[32:35]\n\t" "ds_read_b64_tr_b16 a[146:147], %[vr3] offset:32256\n\t"
    "v_mfma_f32_16x16x32_bf16 v[36:39], a[128:131], a[4:7], v[36:39]\n\t" "global_load_dwordx4 a[64:67], %[va], s[76:77] offset:0 sc1\n\t"
    "v_mfma_f32_16x16x32_bf16 v[36:39], a[132:135], a[20:23], v[36:39]\n\t" "v_mfma_f32_16x16x32_bf16 v[40:43], a[128:131], a[8:11], v[40:43]\n\t" "ds_read_b64_tr_b16 a[148:149], %[vr3] offset:43008\n\t"
    "v_mfma_f32_16x16x32_bf16 v[40:43], a[132:135], a[24:27], v[40:43]\n\t" "ds_read_b64_tr_b16 a[150:151], %[vr3] offset:46592\n\t" "s_add_u32 m0, %[sl], 43008\n\t"
    "v_mfma_f32_16x16x32_bf16 v[44:47], a[128:131], a[12:15], v[44:47]\n\t" "global_load_lds_dwordx4 %[vb0], s[78:79]\n\t" "v_mfma_f32_16x16x32_bf16 v[44:47], a[132:135], a[28:31], v[44:47]\n\t"
    "s_waitcnt lgkmcnt(4)\n\t" "v_mfma_f32_16x16x32_bf16 v[48:51], a[136:139], a[0:3], v[48:51]\n\t" "ds_read_b64_tr_b16 a[152:153], %[vr3] offset:28704\n\t"
    "v_mfma_f32_16x16x32_bf16 v[48:51], a[140:143], a[16:19], v[48:51]\n\t" "ds_read_b64_tr_b16 a[154:155], %[vr3] offset:32288\n\t" "v_mfma_f32_16x16x32_bf16 v[52:55], a[136:139], a[4:7], v[52:55]\n\t"
    "v_mfma_f32_16x16x32_bf16 v[52:55], a[140:143], a[20:23], v[52:55]\n\t" "v_mfma_f32_16x16x32_bf16 v[56:59], a[136:139], a[8:11], v[56:59]\n\t" "ds_read_b64_tr_b16 a[156:157], %[vr3] offset:43040\n\t"
    "v_mfma_f32_16x16x32_bf16 v[56:59], a[140:143], a[24:27], v[56:59]\n\t" "ds_read_b64_tr_b16 a[158:159], %[vr3] offset:46624\n\t" "v_mfma_f32_16x16x32_bf16 v[60:63], a[136:139], a[12:15], v[60:63]\n\t"
    "global_load_dwordx4 a[68:71], %[va], s[76:77] offset:1024 sc1\n\t" "v_mfma_f32_16x16x32_bf16 v[60:63], a[140:143], a[28:31], v[60:63]\n\t" "s_waitcnt lgkmcnt(4)\n\t"
    "v_mfma_f32_16x16x32_bf16 v[64:67], a[144:147], a[0:3], v[64:67]\n\t" "ds_read_b64_tr_b16 a[128:129], %[vr5] offset:28672\n\t" "v_mfma_f32_16x16x32_bf16 v[64:67], a[148:151], a[16:19], v[64:67]\n\t"
    "ds_read_b64_tr_b16 a[130:131], %[vr5] offset:32256\n\t" "s_add_u32 m0, %[sl], 44032\n\t" "v_mfma_f32_16x16x32_bf16 v[68:71], a[144:147], a[4:7], v[68:71]\n\t"
    "global_load_lds_dwordx4 %[vb1], s[78:79]\n\t" "v_mfma_f32_16x16x32_bf16 v[68:71], a[148:151], a[20:23], v[68:71]\n\t" "v_mfma_f32_16x16x32_bf16 v[72:75], a[144:147], a[8:11], v[72:75]\n\t"
    "ds_read_b64_tr_b16 a[132:133], %[vr5] offset:43008\n\t" "v_mfma_f32_16x16x32_bf16 v[72:75], a[148:151], a[24:27], v[72:75]\n\t" "ds_read_b64_tr_b16 a[134:135], %[vr5] offset:46592\n\t"
    "v_mfma_f32_16x16x32_bf16 v[76:79], a[144:147], a[12:15], v[76:79]\n\t" "v_mfma_f32_16x16x32_bf16 v[76:79], a[148:151], a[28:31], v[76:79]\n\t" "s_waitcnt lgkmcnt(4)\n\t"
    "v_mfma_f32_16x16x32_bf16 v[80:83], a[152:155], a[0:3], v[80:83]\n\t" "ds_read_b64_tr_b16 a[136:137], %[vr5] offset:28704\n\t" "v_mfma_f32_16x16x32_bf16 v[80:83], a[156:159], a[16:19], v[80:83]\n\t"
    "ds_read_b64_tr_b16 a[138:139], %[vr5] offset:32288\n\t" "v_mfma_f32_16x16x32_bf16 v[84:87], a[152:155], a[4:7], v[84:87]\n\t" "global_load_dwordx4 a[72:75], %[va], s[76:77] offset:2048 sc1\n\t"
    "v_mfma_f32_16x16x32_bf16 v[84:87], a[156:159], a[20:23], v[84:87]\n\t" "v_mfma_f32_16x16x32_bf16 v[88:91], a[152:155], a[8:11], v[88:91]\n\t" "ds_read_b64_tr_b16 a[140:141], %[vr5] offset:43040\n\t"
    "v_mfma_f32_16x16x32_bf16 v[88:91], a[156:159], a[24:27], v[88:91]\n\t" "ds_read_b64_tr_b16 a[142:143], %[vr5] offset:46624\n\t" "v_mfma_f32_16x16x32_bf16 v[92:95], a[152:155], a[12:15], v[92:95]\n\t"
    "v_mfma_f32_16x16x32_bf16 v[92:95], a[156:159], a[28:31], v[92:95]\n\t" "s_waitcnt lgkmcnt(4)\n\t" "v_mfma_f32_16x16x32_bf16 v[96:99], a[128:131], a[0:3], v[96:99]\n\t"
    "ds_read_b64_tr_b16 a[144:145], %[vr7] offset:28672\n\t" "v_mfma_f32_16x16x32_bf16 v[96:99], a[132:135], a[16:19], v[96:99]\n\t" "ds_read_b64_tr_b16 a[146:147], %[vr7] offset:32256\n\t"
    "s_add_u32 m0, %[sl], 45056\n\t" "v_mfma_f32_16x16x32_bf16 v[100:103], a[128:131], a[4:7], v[100:103]\n\t" "global_load_lds_dwordx4 %[vb2], s[78:79]\n\t"
    "v_mfma_f32_16x16x32_bf16 v[100:103], a[132:135], a[20:23], v[100:103]\n\t" "v_mfma_f32_16x16x32_bf16 v[104:107], a[128:131], a[8:11], v[104:107]\n\t"
    "ds_read_b64_tr_b16 a[148:149], %[vr7] offset:43008\n\t" "v_mfma_f32_16x16x32_bf16 v[104:107], a[132:135], a[24:27], v[104:107]\n\t" "ds_read_b64_tr_b16 a[150:151], %[vr7] offset:46592\n\t"
    "v_mfma_f32_16x16x32_bf16 v[108:111], a[128:131], a[12:15], v[108:111]\n\t" "global_load_dwordx4 a[76:79], %[va], s[76:77] offset:3072 sc1\n\t"
    "v_mfma_f32_16x16x32_bf16 v[108:111], a[132:135], a[28:31], v[108:111]\n\t" "s_add_u32 s76, s76, 16384\n\t" "s_addc_u32 s77, s77, 0\n\t" "s_waitcnt lgkmcnt(4)\n\t"
    "v_mfma_f32_16x16x32_bf16 v[112:115], a[136:139], a[0:3], v[112:115]\n\t" "ds_read_b64_tr_b16 a[152:153], %[vr1] offset:35840\n\t"
    "v_mfma_f32_16x16x32_bf16 v[112:115], a[140:143], a[16:19], v[112:115]\n\t" "ds_read_b64_tr_b16 a[154:155], %[vr1] offset:39424\n\t"
    "v_mfma_f32_16x16x32_bf16 v[116:119], a[136:139], a[4:7], v[116:119]\n\t" "v_mfma_f32_16x16x32_bf16 v[116:119], a[140:143], a[20:23], v[116:119]\n\t"
    "v_mfma_f32_16x16x32_bf16 v[120:123], a[136:139], a[8:11], v[120:123]\n\t" "ds_read_b64_tr_b16 a[156:157], %[vr1] offset:50176\n\t"
    "v_mfma_f32_16x16x32_bf16 v[120:123], a[140:143], a[24:27], v[120:123]\n\t" "ds_read_b64_tr_b16 a[158:159], %[vr1] offset:53760\n\t" "s_add_u32 m0, %[sl], 46080\n\t"
    "v_mfma_f32_16x16x32_bf16 v[124:127], a[136:139], a[12:15], v[124:127]\n\t" "s_mov_b64 exec, s[82:83]\n\t" "global_load_lds_dwordx4 %[vb3], s[78:79]\n\t" "s_mov_b64 exec, -1\n\t"
    "v_mfma_f32_16x16x32_bf16 v[124:127], a[140:143], a[28:31], v[124:127]\n\t" "s_add_u32 s78, s78, 229376\n\t" "s_addc_u32 s79, s79, 0\n\t" "s_waitcnt lgkmcnt(4)\n\t"
    "v_mfma_f32_16x16x32_bf16 v[128:131], a[144:147], a[0:3], v[128:131]\n\t" "ds_read_b64_tr_b16 a[128:129], %[vr1] offset:35872\n\t"
    "v_mfma_f32_16x16x32_bf16 v[128:131], a[148:151], a[16:19], v[128:131]\n\t" "ds_read_b64_tr_b16 a[130:131], %[vr1] offset:39456\n\t"
    "v_mfma_f32_16x16x32_bf16 v[132:135], a[144:147], a[4:7], v[132:135]\n\t" "global_load_dword v29, %[va], %[cd]\n\t" "v_mfma_f32_16x16x32_bf16 v[132:135], a[148:151], a[20:23], v[132:135]\n\t"
    "v_mfma_f32_16x16x32_bf16 v[136:139], a[144:147], a[8:11], v[136:139]\n\t" "ds_read_b64_tr_b16 a[132:133], %[vr1] offset:50208\n\t"
    "v_mfma_f32_16x16x32_bf16 v[136:139], a[148:151], a[24:27], v[136:139]\n\t" "ds_read_b64_tr_b16 a[134:135], %[vr1] offset:53792\n\t"
    "v_mfma_f32_16x16x32_bf16 v[140:143], a[144:147], a[12:15], v[140:143]\n\t" "v_mfma_f32_16x16x32_bf16 v[140:143], a[148:151], a[28:31], v[140:143]\n\t" "s_waitcnt lgkmcnt(4)\n\t"
    "v_mfma_f32_16x16x32_bf16 v[144:147], a[152:155], a[0:3], v[144:147]\n\t" "ds_read_b64_tr_b16 a[136:137], %[vr3] offset:35840\n\t"
    "v_mfma_f32_16x16x32_bf16 v[144:147], a[156:159], a[16:19], v[144:147]\n\t" "ds_read_b64_tr_b16 a[138:139], %[vr3] offset:39424\n\t"
    "v_mfma_f32_16x16x32_bf16 v[148:151], a[152:155], a[4:7], v[148:151]\n\t" "global_load_dwordx4 a[80:83], %[va], s[76:77] offset:0 sc1\n\t"
    "v_mfma_f32_16x16x32_bf16 v[148:151], a[156:159], a[20:23], v[148:151]\n\t" "v_mfma_f32_16x16x32_bf16 v[152:155], a[152:155], a[8:11], v[152:155]\n\t"
    "ds_read_b64_tr_b16 a[140:141], %[vr3] offset:50176\n\t" "v_mfma_f32_16x16x32_bf16 v[152:155], a[156:159], a[24:27], v[152:155]\n\t" "ds_read_b64_tr_b16 a[142:143], %[vr3] offset:53760\n\t"
    "s_add_u32 m0, %[sl], 57344\n\t" "v_mfma_f32_16x16x32_bf16 v[156:159], a[152:155], a[12:15], v[156:159]\n\t" "global_load_lds_dwordx4 %[vb0], s[78:79]\n\t"
    "v_mfma_f32_16x16x32_bf16 v[156:159], a[156:159], a[28:31], v[156:159]\n\t" "s_waitcnt lgkmcnt(4)\n\t" "v_mfma_f32_16x16x32_bf16 v[160:163], a[128:131], a[0:3], v[160:163]\n\t"
    "ds_read_b64_tr_b16 a[144:145], %[vr3] offset:35872\n\t" "v_mfma_f32_16x16x32_bf16 v[160:163], a[132:135], a[16:19], v[160:163]\n\t" "ds_read_b64_tr_b16 a[146:147], %[vr3] offset:39456\n\t"
    "v_mfma_f32_16x16x32_bf16 v[164:167], a[128:131], a[4:7], v[164:167]\n\t" "v_mfma_f32_16x16x32_bf16 v[164:167], a[132:135], a[20:23], v[164:167]\n\t"
    "v_mfma_f32_16x16x32_bf16 v[168:171], a[128:131], a[8:11], v[168:171]\n\t" "ds_read_b64_tr_b16 a[148:149], %[vr3] offset:50208\n\t"
    "v_mfma_f32_16x16x32_bf16 v[168:171], a[132:135], a[24:27], v[168:171]\n\t" "ds_read_b64_tr_b16 a[150:151], %[vr3] offset:53792\n\t"
    "v_mfma_f32_16x16x32_bf16 v[172:175], a[128:131], a[12:15], v[172:175]\n\t" "global_load_dwordx4 a[84:87], %[va], s[76:77] offset:1024 sc1\n\t"
    "v_mfma_f32_16x16x32_bf16 v[172:175], a[132:135], a[28:31], v[172:175]\n\t" "s_waitcnt lgkmcnt(4)\n\t" "v_mfma_f32_16x16x32_bf16 v[176:179], a[136:139], a[0:3], v[176:179]\n\t"
    "ds_read_b64_tr_b16 a[152:153], %[vr5] offset:35840\n\t" "v_mfma_f32_16x16x32_bf16 v[176:179], a[140:143], a[16:19], v[176:179]\n\t" "ds_read_b64_tr_b16 a[154:155], %[vr5] offset:39424\n\t"
    "s_add_u32 m0, %[sl], 58368\n\t" "v_mfma_f32_16x16x32_bf16 v[180:183], a[136:139], a[4:7], v[180:183]\n\t" "global_load_lds_dwordx4 %[vb1], s[78:79]\n\t"
    "v_mfma_f32_16x16x32_bf16 v[180:183], a[140:143], a[20:23], v[180:183]\n\t" "v_mfma_f32_16x16x32_bf16 v[184:187], a[136:139], a[8:11], v[184:187]\n\t"
    "ds_read_b64_tr_b16 a[156:157], %[vr5] offset:50176\n\t" "v_mfma_f32_16x16x32_bf16 v[184:187], a[140:143], a[24:27], v[184:187]\n\t" "ds_read_b64_tr_b16 a[158:159], %[vr5] offset:53760\n\t"
    "v_mfma_f32_16x16x32_bf16 v[188:191], a[136:139], a[12:15], v[188:191]\n\t" "v_mfma_f32_16x16x32_bf16 v[188:191], a[140:143], a[28:31], v[188:191]\n\t" "s_waitcnt lgkmcnt(4)\n\t"
    "v_mfma_f32_16x16x32_bf16 v[192:195], a[144:147], a[0:3], v[192:195]\n\t" "ds_read_b64_tr_b16 a[128:129], %[vr5] offset:35872\n\t"
    "v_mfma_f32_16x16x32_bf16 v[192:195], a[148:151], a[16:19], v[192:195]\n\t" "ds_read_b64_tr_b16 a[130:131], %[vr5] offset:39456\n\t"
    "v_mfma_f32_16x16x32_bf16 v[196:199], a[144:147], a[4:7], v[196:199]\n\t" "global_load_dwordx4 a[88:91], %[va], s[76:77] offset:2048 sc1\n\t"
    "v_mfma_f32_16x16x32_bf16 v[196:199], a[148:151], a[20:23], v[196:199]\n\t" "v_mfma_f32_16x16x32_bf16 v[200:203], a[144:147], a[8:11], v[200:203]\n\t"
    "ds_read_b64_tr_b16 a[132:133], %[vr5] offset:50208\n\t" "v_mfma_f32_16x16x32_bf16 v[200:203], a[148:151], a[24:27], v[200:203]\n\t" "ds_read_b64_tr_b16 a[134:135], %[vr5] offset:53792\n\t"
    "v_mfma_f32_16x16x32_bf16 v[204:207], a[144:147], a[12:15], v[204:207]\n\t" "v_mfma_f32_16x16x32_bf16 v[204:207], a[148:151], a[28:31], v[204:207]\n\t" "s_waitcnt lgkmcnt(4)\n\t"
    "v_mfma_f32_16x16x32_bf16 v[208:211], a[152:155], a[0:3], v[208:211]\n\t" "ds_read_b64_tr_b16 a[136:137], %[vr7] offset:35840\n\t"
    "v_mfma_f32_16x16x32_bf16 v[208:211], a[156:159], a[16:19], v[208:211]\n\t" "ds_read_b64_tr_b16 a[138:139], %[vr7] offset:39424\n\t" "s_add_u32 m0, %[sl], 59392\n\t"
    "v_mfma_f32_16x16x32_bf16 v[212:215], a[152:155], a[4:7], v[212:215]\n\t" "global_load_lds_dwordx4 %[vb2], s[78:79]\n\t" "v_mfma_f32_16x16x32_bf16 v[212:215], a[156:159], a[20:23], v[212:215]\n\t"
    "v_mfma_f32_16x16x32_bf16 v[216:219], a[152:155], a[8:11], v[216:219]\n\t" "ds_read_b64_tr_b16 a[140:141], %[vr7] offset:50176\n\t"
    "v_mfma_f32_16x16x32_bf16 v[216:219], a[156:159], a[24:27], v[216:219]\n\t" "ds_read_b64_tr_b16 a[142:143], %[vr7] offset:53760\n\t"
    "v_mfma_f32_16x16x32_bf16 v[220:223], a[152:155], a[12:15], v[220:223]\n\t" "global_load_dwordx4 a[92:95], %[va], s[76:77] offset:3072 sc1\n\t"
    "v_mfma_f32_16x16x32_bf16 v[220:223], a[156:159], a[28:31], v[220:223]\n\t" "s_add_u32 s76, s76, 16384\n\t" "s_addc_u32 s77, s77, 0\n\t" "s_waitcnt lgkmcnt(4)\n\t"
    "v_mfma_f32_16x16x32_bf16 v[224:227], a[128:131], a[0:3], v[224:227]\n\t" "s_waitcnt vmcnt(24)\n\t" "s_barrier\n\t" "ds_read_b64_tr_b16 a[144:145], %[vr0] offset:0\n\t"
    "v_mfma_f32_16x16x32_bf16 v[224:227], a[132:135], a[16:19], v[224:227]\n\t" "ds_read_b64_tr_b16 a[146:147], %[vr0] offset:3584\n\t"
    "v_mfma_f32_16x16x32_bf16 v[228:231], a[128:131], a[4:7], v[228:231]\n\t" "v_mfma_f32_16x16x32_bf16 v[228:231], a[132:135], a[20:23], v[228:231]\n\t"
    "v_mfma_f32_16x16x32_bf16 v[232:235], a[128:131], a[8:11], v[232:235]\n\t" "ds_read_b64_tr_b16 a[148:149], %[vr0] offset:14336\n\t"
    "v_mfma_f32_16x16x32_bf16 v[232:235], a[132:135], a[24:27], v[232:235]\n\t" "ds_read_b64_tr_b16 a[150:151], %[vr0] offset:17920\n\t" "s_add_u32 m0, %[sl], 60416\n\t"
    "v_mfma_f32_16x16x32_bf16 v[236:239], a[128:131], a[12:15], v[236:239]\n\t" "s_mov_b64 exec, s[82:83]\n\t" "global_load_lds_dwordx4 %[vb3], s[78:79]\n\t" "s_mov_b64 exec, -1\n\t"
    "v_mfma_f32_16x16x32_bf16 v[236:239], a[132:135], a[28:31], v[236:239]\n\t" "s_add_u32 s78, s78, 229376\n\t" "s_addc_u32 s79, s79, 0\n\t" "s_waitcnt lgkmcnt(4)\n\t"
    "v_mfma_f32_16x16x32_bf16 v[240:243], a[136:139], a[0:3], v[240:243]\n\t" "ds_read_b64_tr_b16 a[152:153], %[vr0] offset:32\n\t"
    "v_mfma_f32_16x16x32_bf16 v[240:243], a[140:143], a[16:19], v[240:243]\n\t" "ds_read_b64_tr_b16 a[154:155], %[vr0] offset:3616\n\t"
    "v_mfma_f32_16x16x32_bf16 v[244:247], a[136:139], a[4:7], v[244:247]\n\t" "v_mfma_f32_16x16x32_bf16 v[244:247], a[140:143], a[20:23], v[244:247]\n\t"
    "v_mfma_f32_16x16x32_bf16 v[248:251], a[136:139], a[8:11], v[248:251]\n\t" "ds_read_b64_tr_b16 a[156:157], %[vr0] offset:14368\n\t"
    "v_mfma_f32_16x16x32_bf16 v[248:251], a[140:143], a[24:27], v[248:251]\n\t" "ds_read_b64_tr_b16 a[158:159], %[vr0] offset:17952\n\t"
    "v_mfma_f32_16x16x32_bf16 v[252:255], a[136:139], a[12:15], v[252:255]\n\t" "v_mfma_f32_16x16x32_bf16 v[252:255], a[140:143], a[28:31], v[252:255]\n\t" "s_waitcnt lgkmcnt(4) vmcnt(18)\n\t"
    "v_mfma_f32_16x16x32_bf16 v[32:35], a[144:147], a[32:35], v[32:35]\n\t" "s_barrier\n\t" "ds_read_b64_tr_b16 a[128:129], %[vr2] offset:0\n\t"
    "v_mfma_f32_16x16x32_bf16 v[32:35], a[148:151], a[48:51], v[32:35]\n\t" "ds_read_b64_tr_b16 a[130:131], %[vr2] offset:3584\n\t" "v_mfma_f32_16x16x32_bf16 v[36:39], a[144:147], a[36:39], v[36:39]\n\t"
    "global_load_dwordx4 a[96:99], %[va], s[76:77] offset:0 sc1\n\t" "v_mfma_f32_16x16x32_bf16 v[36:39], a[148:151], a[52:55], v[36:39]\n\t"
    "v_mfma_f32_16x16x32_bf16 v[40:43], a[144:147], a[40:43], v[40:43]\n\t" "ds_read_b64_tr_b16 a[132:133], %[vr2] offset:14336\n\t" "v_mfma_f32_16x16x32_bf16 v[40:43], a[148:151], a[56:59], v[40:43]\n\t"
    "ds_read_b64_tr_b16 a[134:135], %[vr2] offset:17920\n\t" "s_add_u32 m0, %[sl], 71680\n\t" "v_mfma_f32_16x16x32_bf16 v[44:47], a[144:147], a[44:47], v[44:47]\n\t"
    "global_load_lds_dwordx4 %[vb0], s[78:79]\n\t" "v_mfma_f32_16x16x32_bf16 v[44:47], a[148:151], a[60:63], v[44:47]\n\t" "s_waitcnt lgkmcnt(4)\n\t"
    "v_mfma_f32_16x16x32_bf16 v[48:51], a[152:155], a[32:35], v[48:51]\n\t" "ds_read_b64_tr_b16 a[136:137], %[vr2] offset:32\n\t" "v_mfma_f32_16x16x32_bf16 v[48:51], a[156:159], a[48:51], v[48:51]\n\t"
    "ds_read_b64_tr_b16 a[138:139], %[vr2] offset:3616\n\t" "v_mfma_f32_16x16x32_bf16 v[52:55], a[152:155], a[36:39], v[52:55]\n\t" "v_mfma_f32_16x16x32_bf16 v[52:55], a[156:159], a[52:55], v[52:55]\n\t"
    "v_mfma_f32_16x16x32_bf16 v[56:59], a[152:155], a[40:43], v[56:59]\n\t" "ds_read_b64_tr_b16 a[140:141], %[vr2] offset:14368\n\t" "v_mfma_f32_16x16x32_bf16 v[56:59], a[156:159], a[56:59], v[56:59]\n\t"
    "ds_read_b64_tr_b16 a[142:143], %[vr2] offset:17952\n\t" "v_mfma_f32_16x16x32_bf16 v[60:63], a[152:155], a[44:47], v[60:63]\n\t" "global_load_dwordx4 a[100:103], %[va], s[76:77] offset:1024 sc1\n\t"
    "v_mfma_f32_16x16x32_bf16 v[60:63], a[156:159], a[60:63], v[60:63]\n\t" "s_waitcnt lgkmcnt(4)\n\t" "v_mfma_f32_16x16x32_bf16 v[64:67], a[128:131], a[32:35], v[64:67]\n\t"
    "ds_read_b64_tr_b16 a[144:145], %[vr4] offset:0\n\t" "v_mfma_f32_16x16x32_bf16 v[64:67], a[132:135], a[48:51], v[64:67]\n\t" "ds_read_b64_tr_b16 a[146:147], %[vr4] offset:3584\n\t"
    "s_add_u32 m0, %[sl], 72704\n\t" "v_mfma_f32_16x16x32_bf16 v[68:71], a[128:131], a[36:39], v[68:71]\n\t" "global_load_lds_dwordx4 %[vb1], s[78:79]\n\t"
    "v_mfma_f32_16x16x32_bf16 v[68:71], a[132:135], a[52:55], v[68:71]\n\t" "v_mfma_f32_16x16x32_bf16 v[72:75], a[128:131], a[40:43], v[72:75]\n\t" "ds_read_b64_tr_b16 a[148:149], %[vr4] offset:14336\n\t"
    "v_mfma_f32_16x16x32_bf16 v[72:75], a[132:135], a[56:59], v[72:75]\n\t" "ds_read_b64_tr_b16 a[150:151], %[vr4] offset:17920\n\t" "v_mfma_f32_16x16x32_bf16 v[76:79], a[128:131], a[44:47], v[76:79]\n\t"
    "v_mfma_f32_16x16x32_bf16 v[76:79], a[132:135], a[60:63], v[76:79]\n\t" "s_waitcnt lgkmcnt(4)\n\t" "v_mfma_f32_16x16x32_bf16 v[80:83], a[136:139], a[32:35], v[80:83]\n\t"
    "ds_read_b64_tr_b16 a[152:153], %[vr4] offset:32\n\t" "v_mfma_f32_16x16x32_bf16 v[80:83], a[140:143], a[48:51], v[80:83]\n\t" "ds_read_b64_tr_b16 a[154:155], %[vr4] offset:3616\n\t"
    "v_mfma_f32_16x16x32_bf16 v[84:87], a[136:139], a[36:39], v[84:87]\n\t" "global_load_dwordx4 a[104:107], %[va], s[76:77] offset:2048 sc1\n\t"
    "v_mfma_f32_16x16x32_bf16 v[84:87], a[140:143], a[52:55], v[84:87]\n\t" "v_mfma_f32_16x16x32_bf16 v[88:91], a[136:139], a[40:43], v[88:91]\n\t" "ds_read_b64_tr_b16 a[156:157], %[vr4] offset:14368\n\t"
    "v_mfma_f32_16x16x32_bf16 v[88:91], a[140:143], a[56:59], v[88:91]\n\t" "ds_read_b64_tr_b16 a[158:159], %[vr4] offset:17952\n\t" "v_mfma_f32_16x16x32_bf16 v[92:95], a[136:139], a[44:47], v[92:95]\n\t"
    "v_mfma_f32_16x16x32_bf16 v[92:95], a[140:143], a[60:63], v[92:95]\n\t" "s_waitcnt lgkmcnt(4)\n\t" "v_mfma_f32_16x16x32_bf16 v[96:99], a[144:147], a[32:35], v[96:99]\n\t"
    "ds_read_b64_tr_b16 a[128:129], %[vr6] offset:0\n\t" "v_mfma_f32_16x16x32_bf16 v[96:99], a[148:151], a[48:51], v[96:99]\n\t" "ds_read_b64_tr_b16 a[130:131], %[vr6] offset:3584\n\t"
    "s_add_u32 m0, %[sl], 73728\n\t" "v_mfma_f32_16x16x32_bf16 v[100:103], a[144:147], a[36:39], v[100:103]\n\t" "global_load_lds_dwordx4 %[vb2], s[78:79]\n\t"
    "v_mfma_f32_16x16x32_bf16 v[100:103], a[148:151], a[52:55], v[100:103]\n\t" "v_mfma_f32_16x16x32_bf16 v[104:107], a[144:147], a[40:43], v[104:107]\n\t"
    "ds_read_b64_tr_b16 a[132:133], %[vr6] offset:14336\n\t" "v_mfma_f32_16x16x32_bf16 v[104:107], a[148:151], a[56:59], v[104:107]\n\t" "ds_read_b64_tr_b16 a[134:135], %[vr6] offset:17920\n\t"
    "v_mfma_f32_16x16x32_bf16 v[108:111], a[144:147], a[44:47], v[108:111]\n\t" "global_load_dwordx4 a[108:111], %[va], s[76:77] offset:3072 sc1\n\t"
    "v_mfma_f32_16x16x32_bf16 v[108:111], a[148:151], a[60:63], v[108:111]\n\t" "s_add_u32 s76, s76, 16384\n\t" "s_addc_u32 s77, s77, 0\n\t" "s_waitcnt lgkmcnt(4)\n\t"
    "v_mfma_f32_16x16x32_bf16 v[112:115], a[152:155], a[32:35], v[112:115]\n\t" "ds_read_b64_tr_b16 a[136:137], %[vr0] offset:7168\n\t"
    "v_mfma_f32_16x16x32_bf16 v[112:115], a[156:159], a[48:51], v[112:115]\n\t" "ds_read_b64_tr_b16 a[138:139], %[vr0] offset:10752\n\t"
    "v_mfma_f32_16x16x32_bf16 v[116:119], a[152:155], a[36:39], v[116:119]\n\t" "v_mfma_f32_16x16x32_bf16 v[116:119], a[156:159], a[52:55], v[116:119]\n\t"
    "v_mfma_f32_16x16x32_bf16 v[120:123], a[152:155], a[40:43], v[120:123]\n\t" "ds_read_b64_tr_b16 a[140:141], %[vr0] offset:21504\n\t"
    "v_mfma_f32_16x16x32_bf16 v[120:123], a[156:159], a[56:59], v[120:123]\n\t" "ds_read_b64_tr_b16 a[142:143], %[vr0] offset:25088\n\t" "s_add_u32 m0, %[sl], 74752\n\t"
    "v_mfma_f32_16x16x32_bf16 v[124:127], a[152:155], a[44:47], v[124:127]\n\t" "s_mov_b64 exec, s[82:83]\n\t" "global_load_lds_dwordx4 %[vb3], s[78:79]\n\t" "s_mov_b64 exec, -1\n\t"
    "v_mfma_f32_16x16x32_bf16 v[124:127], a[156:159], a[60:63], v[124:127]\n\t" "s_add_u32 s78, s78, s88\n\t" "s_addc_u32 s79, s79, s89\n\t" "s_waitcnt lgkmcnt(4)\n\t"
    "v_mfma_f32_16x16x32_bf16 v[128:131], a[128:131], a[32:35], v[128:131]\n\t" "ds_read_b64_tr_b16 a[144:145], %[vr0] offset:7200\n\t"
    "v_mfma_f32_16x16x32_bf16 v[128:131], a[132:135], a[48:51], v[128:131]\n\t" "ds_read_b64_tr_b16 a[146:147], %[vr0] offset:10784\n\t"
    "v_mfma_f32_16x16x32_bf16 v[132:135], a[128:131], a[36:39], v[132:135]\n\t" "v_mfma_f32_16x16x32_bf16 v[132:135], a[132:135], a[52:55], v[132:135]\n\t"
    "v_mfma_f32_16x16x32_bf16 v[136:139], a[128:131], a[40:43], v[136:139]\n\t" "ds_read_b64_tr_b16 a[148:149], %[vr0] offset:21536\n\t"
    "v_mfma_f32_16x16x32_bf16 v[136:139], a[132:135], a[56:59], v[136:139]\n\t" "ds_read_b64_tr_b16 a[150:151], %[vr0] offset:25120\n\t"
    "v_mfma_f32_16x16x32_bf16 v[140:143], a[128:131], a[44:47], v[140:143]\n\t" "v_mfma_f32_16x16x32_bf16 v[140:143], a[132:135], a[60:63], v[140:143]\n\t" "s_waitcnt lgkmcnt(4)\n\t"
    "v_mfma_f32_16x16x32_bf16 v[144:147], a[136:139], a[32:35], v[144:147]\n\t" "ds_read_b64_tr_b16 a[152:153], %[vr2] offset:7168\n\t"
    "v_mfma_f32_16x16x32_bf16 v[144:147], a[140:143], a[48:51], v[144:147]\n\t" "ds_read_b64_tr_b16 a[154:155], %[vr2] offset:10752\n\t"
    "v_mfma_f32_16x16x32_bf16 v[148:151], a[136:139], a[36:39], v[148:151]\n\t" "global_load_dwordx4 a[112:115], %[va], s[76:77] offset:0 sc1\n\t"
    "v_mfma_f32_16x16x32_bf16 v[148:151], a[140:143], a[52:55], v[148:151]\n\t" "v_mfma_f32_16x16x32_bf16 v[152:155], a[136:139], a[40:43], v[152:155]\n\t"
    "ds_read_b64_tr_b16 a[156:157], %[vr2] offset:21504\n\t" "v_mfma_f32_16x16x32_bf16 v[152:155], a[140:143], a[56:59], v[152:155]\n\t" "ds_read_b64_tr_b16 a[158:159], %[vr2] offset:25088\n\t"
    "s_add_u32 m0, %[sl], 86016\n\t" "v_mfma_f32_16x16x32_bf16 v[156:159], a[136:139], a[44:47], v[156:159]\n\t" "global_load_lds_dwordx4 %[vb0], s[78:79]\n\t"
    "v_mfma_f32_16x16x32_bf16 v[156:159], a[140:143], a[60:63], v[156:159]\n\t" "s_waitcnt lgkmcnt(4)\n\t" "v_mfma_f32_16x16x32_bf16 v[160:163], a[144:147], a[32:35], v[160:163]\n\t"
    "ds_read_b64_tr_b16 a[128:129], %[vr2] offset:7200\n\t" "v_mfma_f32_16x16x32_bf16 v[160:163], a[148:151], a[48:51], v[160:163]\n\t" "ds_read_b64_tr_b16 a[130:131], %[vr2] offset:10784\n\t"
    "v_mfma_f32_16x16x32_bf16 v[164:167], a[144:147], a[36:39], v[164:167]\n\t" "v_mfma_f32_16x16x32_bf16 v[164:167], a[148:151], a[52:55], v[164:167]\n\t"
    "v_mfma_f32_16x16x32_bf16 v[168:171], a[144:147], a[40:43], v[168:171]\n\t" "ds_read_b64_tr_b16 a[132:133], %[vr2] offset:21536\n\t"
    "v_mfma_f32_16x16x32_bf16 v[168:171], a[148:151], a[56:59], v[168:171]\n\t" "ds_read_b64_tr_b16 a[134:135], %[vr2] offset:25120\n\t"
    "v_mfma_f32_16x16x32_bf16 v[172:175], a[144:147], a[44:47], v[172:175]\n\t" "global_load_dwordx4 a[116:119], %[va], s[76:77] offset:1024 sc1\n\t"
    "v_mfma_f32_16x16x32_bf16 v[172:175], a[148:151], a[60:63], v[172:175]\n\t" "s_waitcnt lgkmcnt(4)\n\t" "v_mfma_f32_16x16x32_bf16 v[176:179], a[152:155], a[32:35], v[176:179]\n\t"
    "ds_read_b64_tr_b16 a[136:137], %[vr4] offset:7168\n\t" "v_mfma_f32_16x16x32_bf16 v[176:179], a[156:159], a[48:51], v[176:179]\n\t" "ds_read_b64_tr_b16 a[138:139], %[vr4] offset:10752\n\t"
    "s_add_u32 m0, %[sl], 87040\n\t" "v_mfma_f32_16x16x32_bf16 v[180:183], a[152:155], a[36:39], v[180:183]\n\t" "global_load_lds_dwordx4 %[vb1], s[78:79]\n\t"
    "v_mfma_f32_16x16x32_bf16 v[180:183], a[156:159], a[52:55], v[180:183]\n\t" "v_mfma_f32_16x16x32_bf16 v[184:187], a[152:155], a[40:43], v[184:187]\n\t"
    "ds_read_b64_tr_b16 a[140:141], %[vr4] offset:21504\n\t" "v_mfma_f32_16x16x32_bf16 v[184:187], a[156:159], a[56:59], v[184:187]\n\t" "ds_read_b64_tr_b16 a[142:143], %[vr4] offset:25088\n\t"
    "v_mfma_f32_16x16x32_bf16 v[188:191], a[152:155], a[44:47], v[188:191]\n\t" "v_mfma_f32_16x16x32_bf16 v[188:191], a[156:159], a[60:63], v[188:191]\n\t" "s_waitcnt lgkmcnt(4)\n\t"
    "v_mfma_f32_16x16x32_bf16 v[192:195], a[128:131], a[32:35], v[192:195]\n\t" "ds_read_b64_tr_b16 a[144:145], %[vr4] offset:7200\n\t"
    "v_mfma_f32_16x16x32_bf16 v[192:195], a[132:135], a[48:51], v[192:195]\n\t" "ds_read_b64_tr_b16 a[146:147], %[vr4] offset:10784\n\t"
    "v_mfma_f32_16x16x32_bf16 v[196:199], a[128:131], a[36:39], v[196:199]\n\t" "global_load_dwordx4 a[120:123], %[va], s[76:77] offset:2048 sc1\n\t"
    "v_mfma_f32_16x16x32_bf16 v[196:199], a[132:135], a[52:55], v[196:199]\n\t" "v_mfma_f32_16x16x32_bf16 v[200:203], a[128:131], a[40:43], v[200:203]\n\t"
    "ds_read_b64_tr_b16 a[148:149], %[vr4] offset:21536\n\t" "v_mfma_f32_16x16x32_bf16 v[200:203], a[132:135], a[56:59], v[200:203]\n\t" "ds_read_b64_tr_b16 a[150:151], %[vr4] offset:25120\n\t"
    "v_mfma_f32_16x16x32_bf16 v[204:207], a[128:131], a[44:47], v[204:207]\n\t" "v_mfma_f32_16x16x32_bf16 v[204:207], a[132:135], a[60:63], v[204:207]\n\t" "s_waitcnt lgkmcnt(4)\n\t"
    "v_mfma_f32_16x16x32_bf16 v[208:211], a[136:139], a[32:35], v[208:211]\n\t" "ds_read_b64_tr_b16 a[152:153], %[vr6] offset:7168\n\t"
    "v_mfma_f32_16x16x32_bf16 v[208:211], a[140:143], a[48:51], v[208:211]\n\t" "ds_read_b64_tr_b16 a[154:155], %[vr6] offset:10752\n\t" "s_add_u32 m0, %[sl], 88064\n\t"
    "v_mfma_f32_16x16x32_bf16 v[212:215], a[136:139], a[36:39], v[212:215]\n\t" "global_load_lds_dwordx4 %[vb2], s[78:79]\n\t" "v_mfma_f32_16x16x32_bf16 v[212:215], a[140:143], a[52:55], v[212:215]\n\t"
    "v_mfma_f32_16x16x32_bf16 v[216:219], a[136:139], a[40:43], v[216:219]\n\t" "ds_read_b64_tr_b16 a[156:157], %[vr6] offset:21504\n\t"
    "v_mfma_f32_16x16x32_bf16 v[216:219], a[140:143], a[56:59], v[216:219]\n\t" "ds_read_b64_tr_b16 a[158:159], %[vr6] offset:25088\n\t"
    "v_mfma_f32_16x16x32_bf16 v[220:223], a[136:139], a[44:47], v[220:223]\n\t" "global_load_dwordx4 a[124:127], %[va], s[76:77] offset:3072 sc1\n\t"
    "v_mfma_f32_16x16x32_bf16 v[220:223], a[140:143], a[60:63], v[220:223]\n\t" "s_add_u32 s76, s76, s86\n\t" "s_addc_u32 s77, s77, s87\n\t" "s_waitcnt lgkmcnt(4)\n\t"
    "v_mfma_f32_16x16x32_bf16 v[224:227], a[144:147], a[32:35], v[224:227]\n\t" "s_waitcnt vmcnt(24)\n\t" "s_barrier\n\t" "ds_read_b64_tr_b16 a[128:129], %[vr0] offset:28672\n\t"
    "v_mfma_f32_16x16x32_bf16 v[224:227], a[148:151], a[48:51], v[224:227]\n\t" "ds_read_b64_tr_b16 a[130:131], %[vr0] offset:32256\n\t"
    "v_mfma_f32_16x16x32_bf16 v[228:231], a[144:147], a[36:39], v[228:231]\n\t" "v_mfma_f32_16x16x32_bf16 v[228:231], a[148:151], a[52:55], v[228:231]\n\t"
    "v_mfma_f32_16x16x32_bf16 v[232:235], a[144:147], a[40:43], v[232:235]\n\t" "ds_read_b64_tr_b16 a[132:133], %[vr0] offset:43008\n\t"
    "v_mfma_f32_16x16x32_bf16 v[232:235], a[148:151], a[56:59], v[232:235]\n\t" "ds_read_b64_tr_b16 a[134:135], %[vr0] offset:46592\n\t" "s_add_u32 m0, %[sl], 89088\n\t"
    "v_mfma_f32_16x16x32_bf16 v[236:239], a[144:147], a[44:47], v[236:239]\n\t" "s_mov_b64 exec, s[82:83]\n\t" "global_load_lds_dwordx4 %[vb3], s[78:79]\n\t" "s_mov_b64 exec, -1\n\t"
    "v_mfma_f32_16x16x32_bf16 v[236:239], a[148:151], a[60:63], v[236:239]\n\t" "s_add_u32 s78, s78, 229376\n\t" "s_addc_u32 s79, s79, 0\n\t" "s_waitcnt lgkmcnt(4)\n\t"
    "v_mfma_f32_16x16x32_bf16 v[240:243], a[152:155], a[32:35], v[240:243]\n\t" "ds_read_b64_tr_b16 a[136:137], %[vr0] offset:28704\n\t"
    "v_mfma_f32_16x16x32_bf16 v[240:243], a[156:159], a[48:51], v[240:243]\n\t" "ds_read_b64_tr_b16 a[138:139], %[vr0] offset:32288\n\t"
    "v_mfma_f32_16x16x32_bf16 v[244:247], a[152:155], a[36:39], v[244:247]\n\t" "v_mfma_f32_16x16x32_bf16 v[244:247], a[156:159], a[52:55], v[244:247]\n\t"
    "v_mfma_f32_16x16x32_bf16 v[248:251], a[152:155], a[40:43], v[248:251]\n\t" "ds_read_b64_tr_b16 a[140:141], %[vr0] offset:43040\n\t"
    "v_mfma_f32_16x16x32_bf16 v[248:251], a[156:159], a[56:59], v[248:251]\n\t" "ds_read_b64_tr_b16 a[142:143], %[vr0] offset:46624\n\t"
    "v_mfma_f32_16x16x32_bf16 v[252:255], a[152:155], a[44:47], v[252:255]\n\t" "v_mfma_f32_16x16x32_bf16 v[252:255], a[156:159], a[60:63], v[252:255]\n\t" ".Ld0_%=:\n\t" "s_add_u32 s80, s80, 1\n\t"
    "s_cmp_lt_u32 s80, 2\n\t" "s_cbranch_scc1 .Lloop_%=\n\t" "s_branch .Ltail_%=\n\t" ".LE0_%=:\n\t" "s_waitcnt lgkmcnt(4) vmcnt(17)\n\t"
    "v_mfma_f32_16x16x32_bf16 v[32:35], a[128:131], a[0:3], v[32:35]\n\t" "s_barrier\n\t" "ds_read_b64_tr_b16 a[144:145], %[vr3] offset:28672\n\t"
    "v_mfma_f32_16x16x32_bf16 v[32:35], a[132:135], a[16:19], v[32:35]\n\t" "ds_read_b64_tr_b16 a[146:147], %[vr3] offset:32256\n\t" "v_mfma_f32_16x16x32_bf16 v[36:39], a[128:131], a[4:7], v[36:39]\n\t"
    "global_load_dwordx4 a[64:67], %[va], s[76:77] offset:0 sc1\n\t" "v_mfma_f32_16x16x32_bf16 v[36:39], a[132:135], a[20:23], v[36:39]\n\t"
    "v_mfma_f32_16x16x32_bf16 v[40:43], a[128:131], a[8:11], v[40:43]\n\t" "ds_read_b64_tr_b16 a[148:149], %[vr3] offset:43008\n\t" "v_mfma_f32_16x16x32_bf16 v[40:43], a[132:135], a[24:27], v[40:43]\n\t"
    "ds_read_b64_tr_b16 a[150:151], %[vr3] offset:46592\n\t" "s_add_u32 m0, %[sl], 43008\n\t" "v_mfma_f32_16x16x32_bf16 v[44:47], a[128:131], a[12:15], v[44:47]\n\t"
    "global_load_lds_dwordx4 %[vb0], s[78:79]\n\t" "v_mfma_f32_16x16x32_bf16 v[44:47], a[132:135], a[28:31], v[44:47]\n\t" "s_waitcnt lgkmcnt(4)\n\t"
    "v_mfma_f32_16x16x32_bf16 v[48:51], a[136:139], a[0:3], v[48:51]\n\t" "ds_read_b64_tr_b16 a[152:153], %[vr3] offset:28704\n\t" "v_mfma_f32_16x16x32_bf16 v[48:51], a[140:143], a[16:19], v[48:51]\n\t"
    "ds_read_b64_tr_b16 a[154:155], %[vr3] offset:32288\n\t" "v_mfma_f32_16x16x32_bf16 v[52:55], a[136:139], a[4:7], v[52:55]\n\t" "v_mfma_f32_16x16x32_bf16 v[52:55], a[140:143], a[20:23], v[52:55]\n\t"
    "v_mfma_f32_16x16x32_bf16 v[56:59], a[136:139], a[8:11], v[56:59]\n\t" "ds_read_b64_tr_b16 a[156:157], %[vr3] offset:43040\n\t" "v_mfma_f32_16x16x32_bf16 v[56:59], a[140:143], a[24:27], v[56:59]\n\t"
    "ds_read_b64_tr_b16 a[158:159], %[vr3] offset:46624\n\t" "v_mfma_f32_16x16x32_bf16 v[60:63], a[136:139], a[12:15], v[60:63]\n\t" "global_load_dwordx4 a[68:71], %[va], s[76:77] offset:1024 sc1\n\t"
    "v_mfma_f32_16x16x32_bf16 v[60:63], a[140:143], a[28:31], v[60:63]\n\t" "s_waitcnt lgkmcnt(4)\n\t" "v_mfma_f32_16x16x32_bf16 v[64:67], a[144:147], a[0:3], v[64:67]\n\t"
    "ds_read_b64_tr_b16 a[128:129], %[vr5] offset:28672\n\t" "v_mfma_f32_16x16x32_bf16 v[64:67], a[148:151], a[16:19], v[64:67]\n\t" "ds_read_b64_tr_b16 a[130:131], %[vr5] offset:32256\n\t"
    "s_add_u32 m0, %[sl], 44032\n\t" "v_mfma_f32_16x16x32_bf16 v[68:71], a[144:147], a[4:7], v[68:71]\n\t" "global_load_lds_dwordx4 %[vb1], s[78:79]\n\t"
    "v_mfma_f32_16x16x32_bf16 v[68:71], a[148:151], a[20:23], v[68:71]\n\t" "v_mfma_f32_16x16x32_bf16 v[72:75], a[144:147], a[8:11], v[72:75]\n\t" "ds_read_b64_tr_b16 a[132:133], %[vr5] offset:43008\n\t"
    "v_mfma_f32_16x16x32_bf16 v[72:75], a[148:151], a[24:27], v[72:75]\n\t" "ds_read_b64_tr_b16 a[134:135], %[vr5] offset:46592\n\t" "v_mfma_f32_16x16x32_bf16 v[76:79], a[144:147], a[12:15], v[76:79]\n\t"
    "v_mfma_f32_16x16x32_bf16 v[76:79], a[148:151], a[28:31], v[76:79]\n\t" "s_waitcnt lgkmcnt(4)\n\t" "v_mfma_f32_16x16x32_bf16 v[80:83], a[152:155], a[0:3], v[80:83]\n\t"
    "ds_read_b64_tr_b16 a[136:137], %[vr5] offset:28704\n\t" "v_mfma_f32_16x16x32_bf16 v[80:83], a[156:159], a[16:19], v[80:83]\n\t" "ds_read_b64_tr_b16 a[138:139], %[vr5] offset:32288\n\t"
    "v_mfma_f32_16x16x32_bf16 v[84:87], a[152:155], a[4:7], v[84:87]\n\t" "global_load_dwordx4 a[72:75], %[va], s[76:77] offset:2048 sc1\n\t"
    "v_mfma_f32_16x16x32_bf16 v[84:87], a[156:159], a[20:23], v[84:87]\n\t" "v_mfma_f32_16x16x32_bf16 v[88:91], a[152:155], a[8:11], v[88:91]\n\t" "ds_read_b64_tr_b16 a[140:141], %[vr5] offset:43040\n\t"
    "v_mfma_f32_16x16x32_bf16 v[88:91], a[156:159], a[24:27], v[88:91]\n\t" "ds_read_b64_tr_b16 a[142:143], %[vr5] offset:46624\n\t" "v_mfma_f32_16x16x32_bf16 v[92:95], a[152:155], a[12:15], v[92:95]\n\t"
    "v_mfma_f32_16x16x32_bf16 v[92:95], a[156:159], a[28:31], v[92:95]\n\t" "s_waitcnt lgkmcnt(4)\n\t" "v_mfma_f32_16x16x32_bf16 v[96:99], a[128:131], a[0:3], v[96:99]\n\t"
    "ds_read_b64_tr_b16 a[144:145], %[vr7] offset:28672\n\t" "v_mfma_f32_16x16x32_bf16 v[96:99], a[132:135], a[16:19], v[96:99]\n\t" "ds_read_b64_tr_b16 a[146:147], %[vr7] offset:32256\n\t"
    "v_mfma_f32_16x16x32_bf16 a[160:163], a[128:131], a[184:187], 0\n\t" "s_add_u32 m0, %[sl], 45056\n\t" "v_mfma_f32_16x16x32_bf16 v[100:103], a[128:131], a[4:7], v[100:103]\n\t"
    "global_load_lds_dwordx4 %[vb2], s[78:79]\n\t" "v_mfma_f32_16x16x32_bf16 v[100:103], a[132:135], a[20:23], v[100:103]\n\t" "v_mfma_f32_16x16x32_bf16 v[104:107], a[128:131], a[8:11], v[104:107]\n\t"
    "ds_read_b64_tr_b16 a[148:149], %[vr7] offset:43008\n\t" "v_mfma_f32_16x16x32_bf16 v[104:107], a[132:135], a[24:27], v[104:107]\n\t" "ds_read_b64_tr_b16 a[150:151], %[vr7] offset:46592\n\t"
    "v_mfma_f32_16x16x32_bf16 a[160:163], a[132:135], a[188:191], a[160:163]\n\t" "v_mfma_f32_16x16x32_bf16 v[108:111], a[128:131], a[12:15], v[108:111]\n\t"
    "global_load_dwordx4 a[76:79], %[va], s[76:77] offset:3072 sc1\n\t" "v_mfma_f32_16x16x32_bf16 v[108:111], a[132:135], a[28:31], v[108:111]\n\t" "s_add_u32 s76, s76, 16384\n\t"
    "s_addc_u32 s77, s77, 0\n\t" "s_waitcnt lgkmcnt(4)\n\t" "v_mfma_f32_16x16x32_bf16 v[112:115], a[136:139], a[0:3], v[112:115]\n\t" "ds_read_b64_tr_b16 a[152:153], %[vr1] offset:35840\n\t"
    "v_mfma_f32_16x16x32_bf16 v[112:115], a[140:143], a[16:19], v[112:115]\n\t" "ds_read_b64_tr_b16 a[154:155], %[vr1] offset:39424\n\t"
    "v_mfma_f32_16x16x32_bf16 a[164:167], a[136:139], a[184:187], 0\n\t" "v_mfma_f32_16x16x32_bf16 v[116:119], a[136:139], a[4:7], v[116:119]\n\t"
    "v_mfma_f32_16x16x32_bf16 v[116:119], a[140:143], a[20:23], v[116:119]\n\t" "v_mfma_f32_16x16x32_bf16 v[120:123], a[136:139], a[8:11], v[120:123]\n\t"
    "ds_read_b64_tr_b16 a[156:157], %[vr1] offset:50176\n\t" "v_mfma_f32_16x16x32_bf16 v[120:123], a[140:143], a[24:27], v[120:123]\n\t" "ds_read_b64_tr_b16 a[158:159], %[vr1] offset:53760\n\t"
    "v_mfma_f32_16x16x32_bf16 a[164:167], a[140:143], a[188:191], a[164:167]\n\t" "s_add_u32 m0, %[sl], 46080\n\t" "v_mfma_f32_16x16x32_bf16 v[124:127], a[136:139], a[12:15], v[124:127]\n\t"
    "s_mov_b64 exec, s[82:83]\n\t" "global_load_lds_dwordx4 %[vb3], s[78:79]\n\t" "s_mov_b64 exec, -1\n\t" "global_load_dword v29, %[va], %[cd]\n\t"
    "v_mfma_f32_16x16x32_bf16 v[124:127], a[140:143], a[28:31], v[124:127]\n\t" "s_add_u32 s78, s78, 229376\n\t" "s_addc_u32 s79, s79, 0\n\t" "s_waitcnt lgkmcnt(4)\n\t"
    "v_mfma_f32_16x16x32_bf16 v[128:131], a[144:147], a[0:3], v[128:131]\n\t" "ds_read_b64_tr_b16 a[128:129], %[vr1] offset:35872\n\t"
    "v_mfma_f32_16x16x32_bf16 v[128:131], a[148:151], a[16:19], v[128:131]\n\t" "ds_read_b64_tr_b16 a[130:131], %[vr1] offset:39456\n\t"
    "v_mfma_f32_16x16x32_bf16 a[168:171], a[144:147], a[184:187], 0\n\t" "v_mfma_f32_16x16x32_bf16 v[132:135], a[144:147], a[4:7], v[132:135]\n\t"
    "v_mfma_f32_16x16x32_bf16 v[132:135], a[148:151], a[20:23], v[132:135]\n\t" "v_mfma_f32_16x16x32_bf16 v[136:139], a[144:147], a[8:11], v[136:139]\n\t"
    "ds_read_b64_tr_b16 a[132:133], %[vr1] offset:50208\n\t" "v_mfma_f32_16x16x32_bf16 v[136:139], a[148:151], a[24:27], v[136:139]\n\t" "ds_read_b64_tr_b16 a[134:135], %[vr1] offset:53792\n\t"
    "v_mfma_f32_16x16x32_bf16 a[168:171], a[148:151], a[188:191], a[168:171]\n\t" "v_mfma_f32_16x16x32_bf16 v[140:143], a[144:147], a[12:15], v[140:143]\n\t"
    "v_mfma_f32_16x16x32_bf16 v[140:143], a[148:151], a[28:31], v[140:143]\n\t" "s_waitcnt lgkmcnt(4)\n\t" "v_mfma_f32_16x16x32_bf16 v[144:147], a[152:155], a[0:3], v[144:147]\n\t"
    "ds_read_b64_tr_b16 a[136:137], %[vr3] offset:35840\n\t" "v_mfma_f32_16x16x32_bf16 v[144:147], a[156:159], a[16:19], v[144:147]\n\t" "ds_read_b64_tr_b16 a[138:139], %[vr3] offset:39424\n\t"
    "v_mfma_f32_16x16x32_bf16 v[148:151], a[152:155], a[4:7], v[148:151]\n\t" "global_load_dwordx4 a[80:83], %[va], s[76:77] offset:0 sc1\n\t"
    "v_mfma_f32_16x16x32_bf16 v[148:151], a[156:159], a[20:23], v[148:151]\n\t" "v_mfma_f32_16x16x32_bf16 v[152:155], a[152:155], a[8:11], v[152:155]\n\t"
    "ds_read_b64_tr_b16 a[140:141], %[vr3] offset:50176\n\t" "v_mfma_f32_16x16x32_bf16 v[152:155], a[156:159], a[24:27], v[152:155]\n\t" "ds_read_b64_tr_b16 a[142:143], %[vr3] offset:53760\n\t"
    "s_add_u32 m0, %[sl], 57344\n\t" "v_mfma_f32_16x16x32_bf16 v[156:159], a[152:155], a[12:15], v[156:159]\n\t" "global_load_lds_dwordx4 %[vb0], s[78:79]\n\t"
    "v_mfma_f32_16x16x32_bf16 v[156:159], a[156:159], a[28:31], v[156:159]\n\t" "s_waitcnt lgkmcnt(4)\n\t" "v_mfma_f32_16x16x32_bf16 v[160:163], a[128:131], a[0:3], v[160:163]\n\t"
    "ds_read_b64_tr_b16 a[144:145], %[vr3] offset:35872\n\t" "v_mfma_f32_16x16x32_bf16 v[160:163], a[132:135], a[16:19], v[160:163]\n\t" "ds_read_b64_tr_b16 a[146:147], %[vr3] offset:39456\n\t"
    "v_mfma_f32_16x16x32_bf16 v[164:167], a[128:131], a[4:7], v[164:167]\n\t" "v_mfma_f32_16x16x32_bf16 v[164:167], a[132:135], a[20:23], v[164:167]\n\t"
    "v_mfma_f32_16x16x32_bf16 v[168:171], a[128:131], a[8:11], v[168:171]\n\t" "ds_read_b64_tr_b16 a[148:149], %[vr3] offset:50208\n\t"
    "v_mfma_f32_16x16x32_bf16 v[168:171], a[132:135], a[24:27], v[168:171]\n\t" "ds_read_b64_tr_b16 a[150:151], %[vr3] offset:53792\n\t"
    "v_mfma_f32_16x16x32_bf16 v[172:175], a[128:131], a[12:15], v[172:175]\n\t" "global_load_dwordx4 a[84:87], %[va], s[76:77] offset:1024 sc1\n\t"
    "v_mfma_f32_16x16x32_bf16 v[172:175], a[132:135], a[28:31], v[172:175]\n\t" "s_waitcnt lgkmcnt(4)\n\t" "v_mfma_f32_16x16x32_bf16 v[176:179], a[136:139], a[0:3], v[176:179]\n\t"
    "ds_read_b64_tr_b16 a[152:153], %[vr5] offset:35840\n\t" "v_mfma_f32_16x16x32_bf16 v[176:179], a[140:143], a[16:19], v[176:179]\n\t" "ds_read_b64_tr_b16 a[154:155], %[vr5] offset:39424\n\t"
    "s_add_u32 m0, %[sl], 58368\n\t" "v_mfma_f32_16x16x32_bf16 v[180:183], a[136:139], a[4:7], v[180:183]\n\t" "global_load_lds_dwordx4 %[vb1], s[78:79]\n\t"
    "v_mfma_f32_16x16x32_bf16 v[180:183], a[140:143], a[20:23], v[180:183]\n\t" "v_mfma_f32_16x16x32_bf16 v[184:187], a[136:139], a[8:11], v[184:187]\n\t"
    "ds_read_b64_tr_b16 a[156:157], %[vr5] offset:50176\n\t" "v_mfma_f32_16x16x32_bf16 v[184:187], a[140:143], a[24:27], v[184:187]\n\t" "ds_read_b64_tr_b16 a[158:159], %[vr5] offset:53760\n\t"
    "v_mfma_f32_16x16x32_bf16 v[188:191], a[136:139], a[12:15], v[188:191]\n\t" "v_mfma_f32_16x16x32_bf16 v[188:191], a[140:143], a[28:31], v[188:191]\n\t" "s_waitcnt lgkmcnt(4)\n\t"
    "v_mfma_f32_16x16x32_bf16 v[192:195], a[144:147], a[0:3], v[192:195]\n\t" "ds_read_b64_tr_b16 a[128:129], %[vr5] offset:35872\n\t"
    "v_mfma_f32_16x16x32_bf16 v[192:195], a[148:151], a[16:19], v[192:195]\n\t" "ds_read_b64_tr_b16 a[130:131], %[vr5] offset:39456\n\t"
    "v_mfma_f32_16x16x32_bf16 v[196:199], a[144:147], a[4:7], v[196:199]\n\t" "global_load_dwordx4 a[88:91], %[va], s[76:77] offset:2048 sc1\n\t"
    "v_mfma_f32_16x16x32_bf16 v[196:199], a[148:151], a[20:23], v[196:199]\n\t" "v_mfma_f32_16x16x32_bf16 v[200:203], a[144:147], a[8:11], v[200:203]\n\t"
    "ds_read_b64_tr_b16 a[132:133], %[vr5] offset:50208\n\t" "v_mfma_f32_16x16x32_bf16 v[200:203], a[148:151], a[24:27], v[200:203]\n\t" "ds_read_b64_tr_b16 a[134:135], %[vr5] offset:53792\n\t"
    "v_mfma_f32_16x16x32_bf16 v[204:207], a[144:147], a[12:15], v[204:207]\n\t" "v_mfma_f32_16x16x32_bf16 v[204:207], a[148:151], a[28:31], v[204:207]\n\t" "s_waitcnt lgkmcnt(4)\n\t"
    "v_mfma_f32_16x16x32_bf16 v[208:211], a[152:155], a[0:3], v[208:211]\n\t" "ds_read_b64_tr_b16 a[136:137], %[vr7] offset:35840\n\t"
    "v_mfma_f32_16x16x32_bf16 v[208:211], a[156:159], a[16:19], v[208:211]\n\t" "ds_read_b64_tr_b16 a[138:139], %[vr7] offset:39424\n\t"
    "v_mfma_f32_16x16x32_bf16 a[172:175], a[152:155], a[184:187], 0\n\t" "s_add_u32 m0, %[sl], 59392\n\t" "v_mfma_f32_16x16x32_bf16 v[212:215], a[152:155], a[4:7], v[212:215]\n\t"
    "global_load_lds_dwordx4 %[vb2], s[78:79]\n\t" "v_mfma_f32_16x16x32_bf16 v[212:215], a[156:159], a[20:23], v[212:215]\n\t" "v_mfma_f32_16x16x32_bf16 v[216:219], a[152:155], a[8:11], v[216:219]\n\t"
    "ds_read_b64_tr_b16 a[140:141], %[vr7] offset:50176\n\t" "v_mfma_f32_16x16x32_bf16 v[216:219], a[156:159], a[24:27], v[216:219]\n\t" "ds_read_b64_tr_b16 a[142:143], %[vr7] offset:53760\n\t"
    "v_mfma_f32_16x16x32_bf16 a[172:175], a[156:159], a[188:191], a[172:175]\n\t" "v_mfma_f32_16x16x32_bf16 v[220:223], a[152:155], a[12:15], v[220:223]\n\t"
    "global_load_dwordx4 a[92:95], %[va], s[76:77] offset:3072 sc1\n\t" "v_mfma_f32_16x16x32_bf16 v[220:223], a[156:159], a[28:31], v[220:223]\n\t" "s_add_u32 s76, s76, 16384\n\t"
    "s_addc_u32 s77, s77, 0\n\t" "s_waitcnt lgkmcnt(4)\n\t" "v_mfma_f32_16x16x32_bf16 v[224:227], a[128:131], a[0:3], v[224:227]\n\t" "s_waitcnt vmcnt(24)\n\t" "s_barrier\n\t"
    "ds_read_b64_tr_b16 a[144:145], %[vr0] offset:0\n\t" "v_mfma_f32_16x16x32_bf16 v[224:227], a[132:135], a[16:19], v[224:227]\n\t" "ds_read_b64_tr_b16 a[146:147], %[vr0] offset:3584\n\t"
    "v_mfma_f32_16x16x32_bf16 a[176:179], a[128:131], a[184:187], 0\n\t" "v_mfma_f32_16x16x32_bf16 v[228:231], a[128:131], a[4:7], v[228:231]\n\t"
    "v_mfma_f32_16x16x32_bf16 v[228:231], a[132:135], a[20:23], v[228:231]\n\t" "v_mfma_f32_16x16x32_bf16 v[232:235], a[128:131], a[8:11], v[232:235]\n\t"
    "ds_read_b64_tr_b16 a[148:149], %[vr0] offset:14336\n\t" "v_mfma_f32_16x16x32_bf16 v[232:235], a[132:135], a[24:27], v[232:235]\n\t" "ds_read_b64_tr_b16 a[150:151], %[vr0] offset:17920\n\t"
    "v_mfma_f32_16x16x32_bf16 a[176:179], a[132:135], a[188:191], a[176:179]\n\t" "s_add_u32 m0, %[sl], 60416\n\t" "v_mfma_f32_16x16x32_bf16 v[236:239], a[128:131], a[12:15], v[236:239]\n\t"
    "s_mov_b64 exec, s[82:83]\n\t" "global_load_lds_dwordx4 %[vb3], s[78:79]\n\t" "s_mov_b64 exec, -1\n\t" "v_mfma_f32_16x16x32_bf16 v[236:239], a[132:135], a[28:31], v[236:239]\n\t"
    "s_add_u32 s78, s78, 229376\n\t" "s_addc_u32 s79, s79, 0\n\t" "s_waitcnt lgkmcnt(4)\n\t" "v_mfma_f32_16x16x32_bf16 v[240:243], a[136:139], a[0:3], v[240:243]\n\t"
    "ds_read_b64_tr_b16 a[152:153], %[vr0] offset:32\n\t" "v_mfma_f32_16x16x32_bf16 v[240:243], a[140:143], a[16:19], v[240:243]\n\t" "ds_read_b64_tr_b16 a[154:155], %[vr0] offset:3616\n\t"
    "v_mfma_f32_16x16x32_bf16 a[180:183], a[136:139], a[184:187], 0\n\t" "v_mfma_f32_16x16x32_bf16 v[244:247], a[136:139], a[4:7], v[244:247]\n\t"
    "v_mfma_f32_16x16x32_bf16 v[244:247], a[140:143], a[20:23], v[244:247]\n\t" "v_mfma_f32_16x16x32_bf16 v[248:251], a[136:139], a[8:11], v[248:251]\n\t"
    "ds_read_b64_tr_b16 a[156:157], %[vr0] offset:14368\n\t" "v_mfma_f32_16x16x32_bf16 v[248:251], a[140:143], a[24:27], v[248:251]\n\t" "ds_read_b64_tr_b16 a[158:159], %[vr0] offset:17952\n\t"
    "v_mfma_f32_16x16x32_bf16 a[180:183], a[140:143], a[188:191], a[180:183]\n\t" "v_mfma_f32_16x16x32_bf16 v[252:255], a[136:139], a[12:15], v[252:255]\n\t"
    "v_mfma_f32_16x16x32_bf16 v[252:255], a[140:143], a[28:31], v[252:255]\n\t" "s_waitcnt lgkmcnt(4) vmcnt(18)\n\t" "v_mfma_f32_16x16x32_bf16 v[32:35], a[144:147], a[32:35], v[32:35]\n\t" "s_barrier\n\t"
    "ds_read_b64_tr_b16 a[128:129], %[vr2] offset:0\n\t" "v_mfma_f32_16x16x32_bf16 v[32:35], a[148:151], a[48:51], v[32:35]\n\t" "ds_read_b64_tr_b16 a[130:131], %[vr2] offset:3584\n\t"
    "v_mfma_f32_16x16x32_bf16 v[36:39], a[144:147], a[36:39], v[36:39]\n\t" "global_load_dwordx4 a[96:99], %[va], s[76:77] offset:0 sc1\n\t"
    "v_mfma_f32_16x16x32_bf16 v[36:39], a[148:151], a[52:55], v[36:39]\n\t" "v_mfma_f32_16x16x32_bf16 v[40:43], a[144:147], a[40:43], v[40:43]\n\t" "ds_read_b64_tr_b16 a[132:133], %[vr2] offset:14336\n\t"
    "v_mfma_f32_16x16x32_bf16 v[40:43], a[148:151], a[56:59], v[40:43]\n\t" "ds_read_b64_tr_b16 a[134:135], %[vr2] offset:17920\n\t" "s_add_u32 m0, %[sl], 71680\n\t"
    "v_mfma_f32_16x16x32_bf16 v[44:47], a[144:147], a[44:47], v[44:47]\n\t" "global_load_lds_dwordx4 %[vb0], s[78:79]\n\t" "v_mfma_f32_16x16x32_bf16 v[44:47], a[148:151], a[60:63], v[44:47]\n\t"
    "s_waitcnt lgkmcnt(4)\n\t" "v_mfma_f32_16x16x32_bf16 v[48:51], a[152:155], a[32:35], v[48:51]\n\t" "ds_read_b64_tr_b16 a[136:137], %[vr2] offset:32\n\t"
    "v_mfma_f32_16x16x32_bf16 v[48:51], a[156:159], a[48:51], v[48:51]\n\t" "ds_read_b64_tr_b16 a[138:139], %[vr2] offset:3616\n\t" "v_mfma_f32_16x16x32_bf16 v[52:55], a[152:155], a[36:39], v[52:55]\n\t"
    "v_mfma_f32_16x16x32_bf16 v[52:55], a[156:159], a[52:55], v[52:55]\n\t" "v_mfma_f32_16x16x32_bf16 v[56:59], a[152:155], a[40:43], v[56:59]\n\t" "ds_read_b64_tr_b16 a[140:141], %[vr2] offset:14368\n\t"
    "v_mfma_f32_16x16x32_bf16 v[56:59], a[156:159], a[56:59], v[56:59]\n\t" "ds_read_b64_tr_b16 a[142:143], %[vr2] offset:17952\n\t" "v_mfma_f32_16x16x32_bf16 v[60:63], a[152:155], a[44:47], v[60:63]\n\t"
    "global_load_dwordx4 a[100:103], %[va], s[76:77] offset:1024 sc1\n\t" "v_mfma_f32_16x16x32_bf16 v[60:63], a[156:159], a[60:63], v[60:63]\n\t" "s_waitcnt lgkmcnt(4)\n\t"
    "v_mfma_f32_16x16x32_bf16 v[64:67], a[128:131], a[32:35], v[64:67]\n\t" "ds_read_b64_tr_b16 a[144:145], %[vr4] offset:0\n\t" "v_mfma_f32_16x16x32_bf16 v[64:67], a[132:135], a[48:51], v[64:67]\n\t"
    "ds_read_b64_tr_b16 a[146:147], %[vr4] offset:3584\n\t" "s_add_u32 m0, %[sl], 72704\n\t" "v_mfma_f32_16x16x32_bf16 v[68:71], a[128:131], a[36:39], v[68:71]\n\t"
    "global_load_lds_dwordx4 %[vb1], s[78:79]\n\t" "v_mfma_f32_16x16x32_bf16 v[68:71], a[132:135], a[52:55], v[68:71]\n\t" "v_mfma_f32_16x16x32_bf16 v[72:75], a[128:131], a[40:43], v[72:75]\n\t"
    "ds_read_b64_tr_b16 a[148:149], %[vr4] offset:14336\n\t" "v_mfma_f32_16x16x32_bf16 v[72:75], a[132:135], a[56:59], v[72:75]\n\t" "ds_read_b64_tr_b16 a[150:151], %[vr4] offset:17920\n\t"
    "v_mfma_f32_16x16x32_bf16 v[76:79], a[128:131], a[44:47], v[76:79]\n\t" "v_mfma_f32_16x16x32_bf16 v[76:79], a[132:135], a[60:63], v[76:79]\n\t" "s_waitcnt lgkmcnt(4)\n\t"
    "v_mfma_f32_16x16x32_bf16 v[80:83], a[136:139], a[32:35], v[80:83]\n\t" "ds_read_b64_tr_b16 a[152:153], %[vr4] offset:32\n\t" "v_mfma_f32_16x16x32_bf16 v[80:83], a[140:143], a[48:51], v[80:83]\n\t"
    "ds_read_b64_tr_b16 a[154:155], %[vr4] offset:3616\n\t" "v_mfma_f32_16x16x32_bf16 v[84:87], a[136:139], a[36:39], v[84:87]\n\t" "global_load_dwordx4 a[104:107], %[va], s[76:77] offset:2048 sc1\n\t"
    "v_mfma_f32_16x16x32_bf16 v[84:87], a[140:143], a[52:55], v[84:87]\n\t" "v_mfma_f32_16x16x32_bf16 v[88:91], a[136:139], a[40:43], v[88:91]\n\t" "ds_read_b64_tr_b16 a[156:157], %[vr4] offset:14368\n\t"
    "v_mfma_f32_16x16x32_bf16 v[88:91], a[140:143], a[56:59], v[88:91]\n\t" "ds_read_b64_tr_b16 a[158:159], %[vr4] offset:17952\n\t" "v_mfma_f32_16x16x32_bf16 v[92:95], a[136:139], a[44:47], v[92:95]\n\t"
    "v_mfma_f32_16x16x32_bf16 v[92:95], a[140:143], a[60:63], v[92:95]\n\t" "s_waitcnt lgkmcnt(4)\n\t" "v_mfma_f32_16x16x32_bf16 v[96:99], a[144:147], a[32:35], v[96:99]\n\t"
    "ds_read_b64_tr_b16 a[128:129], %[vr6] offset:0\n\t" "v_mfma_f32_16x16x32_bf16 v[96:99], a[148:151], a[48:51], v[96:99]\n\t" "ds_read_b64_tr_b16 a[130:131], %[vr6] offset:3584\n\t"
    "v_mfma_f32_16x16x32_bf16 a[160:163], a[144:147], a[192:195], a[160:163]\n\t" "s_add_u32 m0, %[sl], 73728\n\t" "v_mfma_f32_16x16x32_bf16 v[100:103], a[144:147], a[36:39], v[100:103]\n\t"
    "global_load_lds_dwordx4 %[vb2], s[78:79]\n\t" "v_mfma_f32_16x16x32_bf16 v[100:103], a[148:151], a[52:55], v[100:103]\n\t" "v_mfma_f32_16x16x32_bf16 v[104:107], a[144:147], a[40:43], v[104:107]\n\t"
    "ds_read_b64_tr_b16 a[132:133], %[vr6] offset:14336\n\t" "v_mfma_f32_16x16x32_bf16 v[104:107], a[148:151], a[56:59], v[104:107]\n\t" "ds_read_b64_tr_b16 a[134:135], %[vr6] offset:17920\n\t"
    "v_mfma_f32_16x16x32_bf16 a[160:163], a[148:151], a[196:199], a[160:163]\n\t" "v_mfma_f32_16x16x32_bf16 v[108:111], a[144:147], a[44:47], v[108:111]\n\t"
    "global_load_dwordx4 a[108:111], %[va], s[76:77] offset:3072 sc1\n\t" "v_mfma_f32_16x16x32_bf16 v[108:111], a[148:151], a[60:63], v[108:111]\n\t" "s_add_u32 s76, s76, 16384\n\t"
    "s_addc_u32 s77, s77, 0\n\t" "s_waitcnt lgkmcnt(4)\n\t" "v_mfma_f32_16x16x32_bf16 v[112:115], a[152:155], a[32:35], v[112:115]\n\t" "ds_read_b64_tr_b16 a[136:137], %[vr0] offset:7168\n\t"
    "v_mfma_f32_16x16x32_bf16 v[112:115], a[156:159], a[48:51], v[112:115]\n\t" "ds_read_b64_tr_b16 a[138:139], %[vr0] offset:10752\n\t"
    "v_mfma_f32_16x16x32_bf16 a[164:167], a[152:155], a[192:195], a[164:167]\n\t" "v_mfma_f32_16x16x32_bf16 v[116:119], a[152:155], a[36:39], v[116:119]\n\t"
    "v_mfma_f32_16x16x32_bf16 v[116:119], a[156:159], a[52:55], v[116:119]\n\t" "v_mfma_f32_16x16x32_bf16 v[120:123], a[152:155], a[40:43], v[120:123]\n\t"
    "ds_read_b64_tr_b16 a[140:141], %[vr0] offset:21504\n\t" "v_mfma_f32_16x16x32_bf16 v[120:123], a[156:159], a[56:59], v[120:123]\n\t" "ds_read_b64_tr_b16 a[142:143], %[vr0] offset:25088\n\t"
    "v_mfma_f32_16x16x32_bf16 a[164:167], a[156:159], a[196:199], a[164:167]\n\t" "s_add_u32 m0, %[sl], 74752\n\t" "v_mfma_f32_16x16x32_bf16 v[124:127], a[152:155], a[44:47], v[124:127]\n\t"
    "s_mov_b64 exec, s[82:83]\n\t" "global_load_lds_dwordx4 %[vb3], s[78:79]\n\t" "s_mov_b64 exec, -1\n\t" "v_mfma_f32_16x16x32_bf16 v[124:127], a[156:159], a[60:63], v[124:127]\n\t"
    "s_add_u32 s78, s78, s88\n\t" "s_addc_u32 s79, s79, s89\n\t" "s_waitcnt lgkmcnt(4)\n\t" "v_mfma_f32_16x16x32_bf16 v[128:131], a[128:131], a[32:35], v[128:131]\n\t"
    "ds_read_b64_tr_b16 a[144:145], %[vr0] offset:7200\n\t" "v_mfma_f32_16x16x32_bf16 v[128:131], a[132:135], a[48:51], v[128:131]\n\t" "ds_read_b64_tr_b16 a[146:147], %[vr0] offset:10784\n\t"
    "v_mfma_f32_16x16x32_bf16 a[168:171], a[128:131], a[192:195], a[168:171]\n\t" "v_mfma_f32_16x16x32_bf16 v[132:135], a[128:131], a[36:39], v[132:135]\n\t"
    "v_mfma_f32_16x16x32_bf16 v[132:135], a[132:135], a[52:55], v[132:135]\n\t" "v_mfma_f32_16x16x32_bf16 v[136:139], a[128:131], a[40:43], v[136:139]\n\t"
    "ds_read_b64_tr_b16 a[148:149], %[vr0] offset:21536\n\t" "v_mfma_f32_16x16x32_bf16 v[136:139], a[132:135], a[56:59], v[136:139]\n\t" "ds_read_b64_tr_b16 a[150:151], %[vr0] offset:25120\n\t"
    "v_mfma_f32_16x16x32_bf16 a[168:171], a[132:135], a[196:199], a[168:171]\n\t" "v_mfma_f32_16x16x32_bf16 v[140:143], a[128:131], a[44:47], v[140:143]\n\t"
    "v_mfma_f32_16x16x32_bf16 v[140:143], a[132:135], a[60:63], v[140:143]\n\t" "s_waitcnt lgkmcnt(4)\n\t" "v_mfma_f32_16x16x32_bf16 v[144:147], a[136:139], a[32:35], v[144:147]\n\t"
    "ds_read_b64_tr_b16 a[152:153], %[vr2] offset:7168\n\t" "v_mfma_f32_16x16x32_bf16 v[144:147], a[140:143], a[48:51], v[144:147]\n\t" "ds_read_b64_tr_b16 a[154:155], %[vr2] offset:10752\n\t"
    "v_mfma_f32_16x16x32_bf16 v[148:151], a[136:139], a[36:39], v[148:151]\n\t" "global_load_dwordx4 a[112:115], %[va], s[76:77] offset:0 sc1\n\t"
    "v_mfma_f32_16x16x32_bf16 v[148:151], a[140:143], a[52:55], v[148:151]\n\t" "v_mfma_f32_16x16x32_bf16 v[152:155], a[136:139], a[40:43], v[152:155]\n\t"
    "ds_read_b64_tr_b16 a[156:157], %[vr2] offset:21504\n\t" "v_mfma_f32_16x16x32_bf16 v[152:155], a[140:143], a[56:59], v[152:155]\n\t" "ds_read_b64_tr_b16 a[158:159], %[vr2] offset:25088\n\t"
    "s_add_u32 m0, %[sl], 86016\n\t" "v_mfma_f32_16x16x32_bf16 v[156:159], a[136:139], a[44:47], v[156:159]\n\t" "global_load_lds_dwordx4 %[vb0], s[78:79]\n\t"
    "v_mfma_f32_16x16x32_bf16 v[156:159], a[140:143], a[60:63], v[156:159]\n\t" "s_waitcnt lgkmcnt(4)\n\t" "v_mfma_f32_16x16x32_bf16 v[160:163], a[144:147], a[32:35], v[160:163]\n\t"
    "ds_read_b64_tr_b16 a[128:129], %[vr2] offset:7200\n\t" "v_mfma_f32_16x16x32_bf16 v[160:163], a[148:151], a[48:51], v[160:163]\n\t" "ds_read_b64_tr_b16 a[130:131], %[vr2] offset:10784\n\t"
    "v_mfma_f32_16x16x32_bf16 v[164:167], a[144:147], a[36:39], v[164:167]\n\t" "v_mfma_f32_16x16x32_bf16 v[164:167], a[148:151], a[52:55], v[164:167]\n\t"
    "v_mfma_f32_16x16x32_bf16 v[168:171], a[144:147], a[40:43], v[168:171]\n\t" "ds_read_b64_tr_b16 a[132:133], %[vr2] offset:21536\n\t"
    "v_mfma_f32_16x16x32_bf16 v[168:171], a[148:151], a[56:59], v[168:171]\n\t" "ds_read_b64_tr_b16 a[134:135], %[vr2] offset:25120\n\t"
    "v_mfma_f32_16x16x32_bf16 v[172:175], a[144:147], a[44:47], v[172:175]\n\t" "global_load_dwordx4 a[116:119], %[va], s[76:77] offset:1024 sc1\n\t"
    "v_mfma_f32_16x16x32_bf16 v[172:175], a[148:151], a[60:63], v[172:175]\n\t" "s_waitcnt lgkmcnt(4)\n\t" "v_mfma_f32_16x16x32_bf16 v[176:179], a[152:155], a[32:35], v[176:179]\n\t"
    "ds_read_b64_tr_b16 a[136:137], %[vr4] offset:7168\n\t" "v_mfma_f32_16x16x32_bf16 v[176:179], a[156:159], a[48:51], v[176:179]\n\t" "ds_read_b64_tr_b16 a[138:139], %[vr4] offset:10752\n\t"
    "s_add_u32 m0, %[sl], 87040\n\t" "v_mfma_f32_16x16x32_bf16 v[180:183], a[152:155], a[36:39], v[180:183]\n\t" "global_load_lds_dwordx4 %[vb1], s[78:79]\n\t"
    "v_mfma_f32_16x16x32_bf16 v[180:183], a[156:159], a[52:55], v[180:183]\n\t" "v_mfma_f32_16x16x32_bf16 v[184:187], a[152:155], a[40:43], v[184:187]\n\t"
    "ds_read_b64_tr_b16 a[140:141], %[vr4] offset:21504\n\t" "v_mfma_f32_16x16x32_bf16 v[184:187], a[156:159], a[56:59], v[184:187]\n\t" "ds_read_b64_tr_b16 a[142:143], %[vr4] offset:25088\n\t"
    "v_mfma_f32_16x16x32_bf16 v[188:191], a[152:155], a[44:47], v[188:191]\n\t" "v_mfma_f32_16x16x32_bf16 v[188:191], a[156:159], a[60:63], v[188:191]\n\t" "s_waitcnt lgkmcnt(4)\n\t"
    "v_mfma_f32_16x16x32_bf16 v[192:195], a[128:131], a[32:35], v[192:195]\n\t" "ds_read_b64_tr_b16 a[144:145], %[vr4] offset:7200\n\t"
    "v_mfma_f32_16x16x32_bf16 v[192:195], a[132:135], a[48:51], v[192:195]\n\t" "ds_read_b64_tr_b16 a[146:147], %[vr4] offset:10784\n\t"
    "v_mfma_f32_16x16x32_bf16 v[196:199], a[128:131], a[36:39], v[196:199]\n\t" "global_load_dwordx4 a[120:123], %[va], s[76:77] offset:2048 sc1\n\t"
    "v_mfma_f32_16x16x32_bf16 v[196:199], a[132:135], a[52:55], v[196:199]\n\t" "v_mfma_f32_16x16x32_bf16 v[200:203], a[128:131], a[40:43], v[200:203]\n\t"
    "ds_read_b64_tr_b16 a[148:149], %[vr4] offset:21536\n\t" "v_mfma_f32_16x16x32_bf16 v[200:203], a[132:135], a[56:59], v[200:203]\n\t" "ds_read_b64_tr_b16 a[150:151], %[vr4] offset:25120\n\t"
    "v_mfma_f32_16x16x32_bf16 v[204:207], a[128:131], a[44:47], v[204:207]\n\t" "v_mfma_f32_16x16x32_bf16 v[204:207], a[132:135], a[60:63], v[204:207]\n\t" "s_waitcnt lgkmcnt(4)\n\t"
    "v_mfma_f32_16x16x32_bf16 v[208:211], a[136:139], a[32:35], v[208:211]\n\t" "ds_read_b64_tr_b16 a[152:153], %[vr6] offset:7168\n\t"
    "v_mfma_f32_16x16x32_bf16 v[208:211], a[140:143], a[48:51], v[208:211]\n\t" "ds_read_b64_tr_b16 a[154:155], %[vr6] offset:10752\n\t"
    "v_mfma_f32_16x16x32_bf16 a[172:175], a[136:139], a[192:195], a[172:175]\n\t" "s_add_u32 m0, %[sl], 88064\n\t" "v_mfma_f32_16x16x32_bf16 v[212:215], a[136:139], a[36:39], v[212:215]\n\t"
    "global_load_lds_dwordx4 %[vb2], s[78:79]\n\t" "v_mfma_f32_16x16x32_bf16 v[212:215], a[140:143], a[52:55], v[212:215]\n\t" "v_mfma_f32_16x16x32_bf16 v[216:219], a[136:139], a[40:43], v[216:219]\n\t"
    "ds_read_b64_tr_b16 a[156:157], %[vr6] offset:21504\n\t" "v_mfma_f32_16x16x32_bf16 v[216:219], a[140:143], a[56:59], v[216:219]\n\t" "ds_read_b64_tr_b16 a[158:159], %[vr6] offset:25088\n\t"
    "v_mfma_f32_16x16x32_bf16 a[172:175], a[140:143], a[196:199], a[172:175]\n\t" "v_mfma_f32_16x16x32_bf16 v[220:223], a[136:139], a[44:47], v[220:223]\n\t"
    "global_load_dwordx4 a[124:127], %[va], s[76:77] offset:3072 sc1\n\t" "v_mfma_f32_16x16x32_bf16 v[220:223], a[140:143], a[60:63], v[220:223]\n\t" "s_add_u32 s76, s76, s86\n\t"
    "s_addc_u32 s77, s77, s87\n\t" "s_waitcnt lgkmcnt(4)\n\t" "v_mfma_f32_16x16x32_bf16 v[224:227], a[144:147], a[32:35], v[224:227]\n\t" "s_waitcnt vmcnt(24)\n\t" "s_barrier\n\t"
    "ds_read_b64_tr_b16 a[128:129], %[vr0] offset:28672\n\t" "v_mfma_f32_16x16x32_bf16 v[224:227], a[148:151], a[48:51], v[224:227]\n\t" "ds_read_b64_tr_b16 a[130:131], %[vr0] offset:32256\n\t"
    "v_mfma_f32_16x16x32_bf16 a[176:179], a[144:147], a[192:195], a[176:179]\n\t" "v_mfma_f32_16x16x32_bf16 v[228:231], a[144:147], a[36:39], v[228:231]\n\t"
    "v_mfma_f32_16x16x32_bf16 v[228:231], a[148:151], a[52:55], v[228:231]\n\t" "v_mfma_f32_16x16x32_bf16 v[232:235], a[144:147], a[40:43], v[232:235]\n\t"
    "ds_read_b64_tr_b16 a[132:133], %[vr0] offset:43008\n\t" "v_mfma_f32_16x16x32_bf16 v[232:235], a[148:151], a[56:59], v[232:235]\n\t" "ds_read_b64_tr_b16 a[134:135], %[vr0] offset:46592\n\t"
    "v_mfma_f32_16x16x32_bf16 a[176:179], a[148:151], a[196:199], a[176:179]\n\t" "s_add_u32 m0, %[sl], 89088\n\t" "v_mfma_f32_16x16x32_bf16 v[236:239], a[144:147], a[44:47], v[236:239]\n\t"
    "s_mov_b64 exec, s[82:83]\n\t" "global_load_lds_dwordx4 %[vb3], s[78:79]\n\t" "s_mov_b64 exec, -1\n\t" "v_mfma_f32_16x16x32_bf16 v[236:239], a[148:151], a[60:63], v[236:239]\n\t"
    "s_add_u32 s78, s78, 229376\n\t" "s_addc_u32 s79, s79, 0\n\t" "s_waitcnt lgkmcnt(4)\n\t" "v_mfma_f32_16x16x32_bf16 v[240:243], a[152:155], a[32:35], v[240:243]\n\t"
    "ds_read_b64_tr_b16 a[136:137], %[vr0] offset:28704\n\t" "v_mfma_f32_16x16x32_bf16 v[240:243], a[156:159], a[48:51], v[240:243]\n\t" "ds_read_b64_tr_b16 a[138:139], %[vr0] offset:32288\n\t"
    "v_mfma_f32_16x16x32_bf16 a[180:183], a[152:155], a[192:195], a[180:183]\n\t" "v_mfma_f32_16x16x32_bf16 v[244:247], a[152:155], a[36:39], v[244:247]\n\t"
    "v_mfma_f32_16x16x32_bf16 v[244:247], a[156:159], a[52:55], v[244:247]\n\t" "v_mfma_f32_16x16x32_bf16 v[248:251], a[152:155], a[40:43], v[248:251]\n\t"
    "ds_read_b64_tr_b16 a[140:141], %[vr0] offset:43040\n\t" "v_mfma_f32_16x16x32_bf16 v[248:251], a[156:159], a[56:59], v[248:251]\n\t" "ds_read_b64_tr_b16 a[142:143], %[vr0] offset:46624\n\t"
    "v_mfma_f32_16x16x32_bf16 a[180:183], a[156:159], a[196:199], a[180:183]\n\t" "v_mfma_f32_16x16x32_bf16 v[252:255], a[152:155], a[44:47], v[252:255]\n\t"
    "v_mfma_f32_16x16x32_bf16 v[252:255], a[156:159], a[60:63], v[252:255]\n\t" "v_accvgpr_read_b32 v31, %[cp]\n\t" "s_nop 7\n\t" "s_mov_b64 exec, %[m01]\n\t"
    "global_store_dwordx4 v31, a[160:163], %[su0] offset:0 sc1\n\t" "global_store_dwordx4 v31, a[164:167], %[su0] offset:2304 sc1\n\t" "global_store_dwordx4 v31, a[172:175], %[su0] offset:1152 sc1\n\t"
    "global_store_dwordx4 v31, a[176:179], %[su0] offset:3456 sc1\n\t" "s_mov_b64 exec, %[m6]\n\t" "global_store_dwordx4 v31, a[168:171], %[su6] offset:0 sc1\n\t"
    "global_store_dwordx4 v31, a[180:183], %[su6] offset:1152 sc1\n\t" "s_mov_b64 exec, -1\n\t" "s_branch .Ld0_%=\n\t" ".LE1_%=:\n\t" "s_waitcnt lgkmcnt(4) vmcnt(17)\n\t"
    "v_mfma_f32_16x16x32_bf16 v[32:35], a[128:131], a[64:67], v[32:35]\n\t" "s_barrier\n\t" "ds_read_b64_tr_b16 a[144:145], %[vr2] offset:28672\n\t"
    "v_mfma_f32_16x16x32_bf16 v[32:35], a[132:135], a[80:83], v[32:35]\n\t" "ds_read_b64_tr_b16 a[146:147], %[vr2] offset:32256\n\t" "v_mfma_f32_16x16x32_bf16 v[36:39], a[128:131], a[68:71], v[36:39]\n\t"
    "global_load_dwordx4 a[0:3], %[va], s[76:77] offset:0 sc1\n\t" "v_mfma_f32_16x16x32_bf16 v[36:39], a[132:135], a[84:87], v[36:39]\n\t"
    "v_mfma_f32_16x16x32_bf16 v[40:43], a[128:131], a[72:75], v[40:43]\n\t" "ds_read_b64_tr_b16 a[148:149], %[vr2] offset:43008\n\t" "v_mfma_f32_16x16x32_bf16 v[40:43], a[132:135], a[88:91], v[40:43]\n\t"
    "ds_read_b64_tr_b16 a[150:151], %[vr2] offset:46592\n\t" "s_add_u32 m0, %[sl], 100352\n\t" "v_mfma_f32_16x16x32_bf16 v[44:47], a[128:131], a[76:79], v[44:47]\n\t"
    "global_load_lds_dwordx4 %[vb0], s[78:79]\n\t" "v_mfma_f32_16x16x32_bf16 v[44:47], a[132:135], a[92:95], v[44:47]\n\t" "s_waitcnt lgkmcnt(4)\n\t"
    "v_mfma_f32_16x16x32_bf16 v[48:51], a[136:139], a[64:67], v[48:51]\n\t" "ds_read_b64_tr_b16 a[152:153], %[vr2] offset:28704\n\t" "v_mfma_f32_16x16x32_bf16 v[48:51], a[140:143], a[80:83], v[48:51]\n\t"
    "ds_read_b64_tr_b16 a[154:155], %[vr2] offset:32288\n\t" "v_mfma_f32_16x16x32_bf16 v[52:55], a[136:139], a[68:71], v[52:55]\n\t" "v_mfma_f32_16x16x32_bf16 v[52:55], a[140:143], a[84:87], v[52:55]\n\t"
    "v_mfma_f32_16x16x32_bf16 v[56:59], a[136:139], a[72:75], v[56:59]\n\t" "ds_read_b64_tr_b16 a[156:157], %[vr2] offset:43040\n\t" "v_mfma_f32_16x16x32_bf16 v[56:59], a[140:143], a[88:91], v[56:59]\n\t"
    "ds_read_b64_tr_b16 a[158:159], %[vr2] offset:46624\n\t" "v_mfma_f32_16x16x32_bf16 v[60:63], a[136:139], a[76:79], v[60:63]\n\t" "global_load_dwordx4 a[4:7], %[va], s[76:77] offset:1024 sc1\n\t"
    "v_mfma_f32_16x16x32_bf16 v[60:63], a[140:143], a[92:95], v[60:63]\n\t" "s_waitcnt lgkmcnt(4)\n\t" "v_mfma_f32_16x16x32_bf16 v[64:67], a[144:147], a[64:67], v[64:67]\n\t"
    "ds_read_b64_tr_b16 a[128:129], %[vr4] offset:28672\n\t" "v_mfma_f32_16x16x32_bf16 v[64:67], a[148:151], a[80:83], v[64:67]\n\t" "ds_read_b64_tr_b16 a[130:131], %[vr4] offset:32256\n\t"
    "s_add_u32 m0, %[sl], 101376\n\t" "v_mfma_f32_16x16x32_bf16 v[68:71], a[144:147], a[68:71], v[68:71]\n\t" "global_load_lds_dwordx4 %[vb1], s[78:79]\n\t"
    "v_mfma_f32_16x16x32_bf16 v[68:71], a[148:151], a[84:87], v[68:71]\n\t" "v_mfma_f32_16x16x32_bf16 v[72:75], a[144:147], a[72:75], v[72:75]\n\t" "ds_read_b64_tr_b16 a[132:133], %[vr4] offset:43008\n\t"
    "v_mfma_f32_16x16x32_bf16 v[72:75], a[148:151], a[88:91], v[72:75]\n\t" "ds_read_b64_tr_b16 a[134:135], %[vr4] offset:46592\n\t" "v_mfma_f32_16x16x32_bf16 v[76:79], a[144:147], a[76:79], v[76:79]\n\t"
    "v_mfma_f32_16x16x32_bf16 v[76:79], a[148:151], a[92:95], v[76:79]\n\t" "s_waitcnt lgkmcnt(4)\n\t" "v_mfma_f32_16x16x32_bf16 v[80:83], a[152:155], a[64:67], v[80:83]\n\t"
    "ds_read_b64_tr_b16 a[136:137], %[vr4] offset:28704\n\t" "v_mfma_f32_16x16x32_bf16 v[80:83], a[156:159], a[80:83], v[80:83]\n\t" "ds_read_b64_tr_b16 a[138:139], %[vr4] offset:32288\n\t"
    "v_mfma_f32_16x16x32_bf16 v[84:87], a[152:155], a[68:71], v[84:87]\n\t" "global_load_dwordx4 a[8:11], %[va], s[76:77] offset:2048 sc1\n\t"
    "v_mfma_f32_16x16x32_bf16 v[84:87], a[156:159], a[84:87], v[84:87]\n\t" "v_mfma_f32_16x16x32_bf16 v[88:91], a[152:155], a[72:75], v[88:91]\n\t" "ds_read_b64_tr_b16 a[140:141], %[vr4] offset:43040\n\t"
    "v_mfma_f32_16x16x32_bf16 v[88:91], a[156:159], a[88:91], v[88:91]\n\t" "ds_read_b64_tr_b16 a[142:143], %[vr4] offset:46624\n\t" "v_mfma_f32_16x16x32_bf16 v[92:95], a[152:155], a[76:79], v[92:95]\n\t"
    "v_mfma_f32_16x16x32_bf16 v[92:95], a[156:159], a[92:95], v[92:95]\n\t" "s_waitcnt lgkmcnt(4)\n\t" "v_mfma_f32_16x16x32_bf16 v[96:99], a[128:131], a[64:67], v[96:99]\n\t"
    "ds_read_b64_tr_b16 a[144:145], %[vr6] offset:28672\n\t" "v_mfma_f32_16x16x32_bf16 v[96:99], a[132:135], a[80:83], v[96:99]\n\t" "ds_read_b64_tr_b16 a[146:147], %[vr6] offset:32256\n\t"
    "v_mfma_f32_16x16x32_bf16 a[160:163], a[128:131], a[184:187], 0\n\t" "s_add_u32 m0, %[sl], 102400\n\t" "v_mfma_f32_16x16x32_bf16 v[100:103], a[128:131], a[68:71], v[100:103]\n\t"
    "global_load_lds_dwordx4 %[vb2], s[78:79]\n\t" "v_mfma_f32_16x16x32_bf16 v[100:103], a[132:135], a[84:87], v[100:103]\n\t" "v_mfma_f32_16x16x32_bf16 v[104:107], a[128:131], a[72:75], v[104:107]\n\t"
    "ds_read_b64_tr_b16 a[148:149], %[vr6] offset:43008\n\t" "v_mfma_f32_16x16x32_bf16 v[104:107], a[132:135], a[88:91], v[104:107]\n\t" "ds_read_b64_tr_b16 a[150:151], %[vr6] offset:46592\n\t"
    "v_mfma_f32_16x16x32_bf16 a[160:163], a[132:135], a[188:191], a[160:163]\n\t" "v_mfma_f32_16x16x32_bf16 v[108:111], a[128:131], a[76:79], v[108:111]\n\t"
    "global_load_dwordx4 a[12:15], %[va], s[76:77] offset:3072 sc1\n\t" "v_mfma_f32_16x16x32_bf16 v[108:111], a[132:135], a[92:95], v[108:111]\n\t" "s_add_u32 s76, s76, 16384\n\t"
    "s_addc_u32 s77, s77, 0\n\t" "s_waitcnt lgkmcnt(4)\n\t" "v_mfma_f32_16x16x32_bf16 v[112:115], a[136:139], a[64:67], v[112:115]\n\t" "ds_read_b64_tr_b16 a[152:153], %[vr0] offset:35840\n\t"
    "v_mfma_f32_16x16x32_bf16 v[112:115], a[140:143], a[80:83], v[112:115]\n\t" "ds_read_b64_tr_b16 a[154:155], %[vr0] offset:39424\n\t"
    "v_mfma_f32_16x16x32_bf16 a[164:167], a[136:139], a[184:187], 0\n\t" "v_mfma_f32_16x16x32_bf16 v[116:119], a[136:139], a[68:71], v[116:119]\n\t"
    "v_mfma_f32_16x16x32_bf16 v[116:119], a[140:143], a[84:87], v[116:119]\n\t" "v_mfma_f32_16x16x32_bf16 v[120:123], a[136:139], a[72:75], v[120:123]\n\t"
    "ds_read_b64_tr_b16 a[156:157], %[vr0] offset:50176\n\t" "v_mfma_f32_16x16x32_bf16 v[120:123], a[140:143], a[88:91], v[120:123]\n\t" "ds_read_b64_tr_b16 a[158:159], %[vr0] offset:53760\n\t"
    "v_mfma_f32_16x16x32_bf16 a[164:167], a[140:143], a[188:191], a[164:167]\n\t" "s_add_u32 m0, %[sl], 103424\n\t" "v_mfma_f32_16x16x32_bf16 v[124:127], a[136:139], a[76:79], v[124:127]\n\t"
    "s_mov_b64 exec, s[82:83]\n\t" "global_load_lds_dwordx4 %[vb3], s[78:79]\n\t" "s_mov_b64 exec, -1\n\t" "global_load_dword v29, %[va], %[cd]\n\t"
    "v_mfma_f32_16x16x32_bf16 v[124:127], a[140:143], a[92:95], v[124:127]\n\t" "s_add_u32 s78, s78, 229376\n\t" "s_addc_u32 s79, s79, 0\n\t" "s_waitcnt lgkmcnt(4)\n\t"
    "v_mfma_f32_16x16x32_bf16 v[128:131], a[144:147], a[64:67], v[128:131]\n\t" "ds_read_b64_tr_b16 a[128:129], %[vr0] offset:35872\n\t"
    "v_mfma_f32_16x16x32_bf16 v[128:131], a[148:151], a[80:83], v[128:131]\n\t" "ds_read_b64_tr_b16 a[130:131], %[vr0] offset:39456\n\t"
    "v_mfma_f32_16x16x32_bf16 a[168:171], a[144:147], a[184:187], 0\n\t" "v_mfma_f32_16x16x32_bf16 v[132:135], a[144:147], a[68:71], v[132:135]\n\t"
    "v_mfma_f32_16x16x32_bf16 v[132:135], a[148:151], a[84:87], v[132:135]\n\t" "v_mfma_f32_16x16x32_bf16 v[136:139], a[144:147], a[72:75], v[136:139]\n\t"
    "ds_read_b64_tr_b16 a[132:133], %[vr0] offset:50208\n\t" "v_mfma_f32_16x16x32_bf16 v[136:139], a[148:151], a[88:91], v[136:139]\n\t" "ds_read_b64_tr_b16 a[134:135], %[vr0] offset:53792\n\t"
    "v_mfma_f32_16x16x32_bf16 a[168:171], a[148:151], a[188:191], a[168:171]\n\t" "v_mfma_f32_16x16x32_bf16 v[140:143], a[144:147], a[76:79], v[140:143]\n\t"
    "v_mfma_f32_16x16x32_bf16 v[140:143], a[148:151], a[92:95], v[140:143]\n\t" "s_waitcnt lgkmcnt(4)\n\t" "v_mfma_f32_16x16x32_bf16 v[144:147], a[152:155], a[64:67], v[144:147]\n\t"
    "ds_read_b64_tr_b16 a[136:137], %[vr2] offset:35840\n\t" "v_mfma_f32_16x16x32_bf16 v[144:147], a[156:159], a[80:83], v[144:147]\n\t" "ds_read_b64_tr_b16 a[138:139], %[vr2] offset:39424\n\t"
    "v_mfma_f32_16x16x32_bf16 v[148:151], a[152:155], a[68:71], v[148:151]\n\t" "global_load_dwordx4 a[16:19], %[va], s[76:77] offset:0 sc1\n\t"
    "v_mfma_f32_16x16x32_bf16 v[148:151], a[156:159], a[84:87], v[148:151]\n\t" "v_mfma_f32_16x16x32_bf16 v[152:155], a[152:155], a[72:75], v[152:155]\n\t"
    "ds_read_b64_tr_b16 a[140:141], %[vr2] offset:50176\n\t" "v_mfma_f32_16x16x32_bf16 v[152:155], a[156:159], a[88:91], v[152:155]\n\t" "ds_read_b64_tr_b16 a[142:143], %[vr2] offset:53760\n\t"
    "s_add_u32 m0, %[sl], 0\n\t" "v_mfma_f32_16x16x32_bf16 v[156:159], a[152:155], a[76:79], v[156:159]\n\t" "global_load_lds_dwordx4 %[vb0], s[78:79]\n\t"
    "v_mfma_f32_16x16x32_bf16 v[156:159], a[156:159], a[92:95], v[156:159]\n\t" "s_waitcnt lgkmcnt(4)\n\t" "v_mfma_f32_16x16x32_bf16 v[160:163], a[128:131], a[64:67], v[160:163]\n\t"
    "ds_read_b64_tr_b16 a[144:145], %[vr2] offset:35872\n\t" "v_mfma_f32_16x16x32_bf16 v[160:163], a[132:135], a[80:83], v[160:163]\n\t" "ds_read_b64_tr_b16 a[146:147], %[vr2] offset:39456\n\t"
    "v_mfma_f32_16x16x32_bf16 v[164:167], a[128:131], a[68:71], v[164:167]\n\t" "v_mfma_f32_16x16x32_bf16 v[164:167], a[132:135], a[84:87], v[164:167]\n\t"
    "v_mfma_f32_16x16x32_bf16 v[168:171], a[128:131], a[72:75], v[168:171]\n\t" "ds_read_b64_tr_b16 a[148:149], %[vr2] offset:50208\n\t"
    "v_mfma_f32_16x16x32_bf16 v[168:171], a[132:135], a[88:91], v[168:171]\n\t" "ds_read_b64_tr_b16 a[150:151], %[vr2] offset:53792\n\t"
    "v_mfma_f32_16x16x32_bf16 v[172:175], a[128:131], a[76:79], v[172:175]\n\t" "global_load_dwordx4 a[20:23], %[va], s[76:77] offset:1024 sc1\n\t"
    "v_mfma_f32_16x16x32_bf16 v[172:175], a[132:135], a[92:95], v[172:175]\n\t" "s_waitcnt lgkmcnt(4)\n\t" "v_mfma_f32_16x16x32_bf16 v[176:179], a[136:139], a[64:67], v[176:179]\n\t"
    "ds_read_b64_tr_b16 a[152:153], %[vr4] offset:35840\n\t" "v_mfma_f32_16x16x32_bf16 v[176:179], a[140:143], a[80:83], v[176:179]\n\t" "ds_read_b64_tr_b16 a[154:155], %[vr4] offset:39424\n\t"
    "s_add_u32 m0, %[sl], 1024\n\t" "v_mfma_f32_16x16x32_bf16 v[180:183], a[136:139], a[68:71], v[180:183]\n\t" "global_load_lds_dwordx4 %[vb1], s[78:79]\n\t"
    "v_mfma_f32_16x16x32_bf16 v[180:183], a[140:143], a[84:87], v[180:183]\n\t" "v_mfma_f32_16x16x32_bf16 v[184:187], a[136:139], a[72:75], v[184:187]\n\t"
    "ds_read_b64_tr_b16 a[156:157], %[vr4] offset:50176\n\t" "v_mfma_f32_16x16x32_bf16 v[184:187], a[140:143], a[88:91], v[184:187]\n\t" "ds_read_b64_tr_b16 a[158:159], %[vr4] offset:53760\n\t"
    "v_mfma_f32_16x16x32_bf16 v[188:191], a[136:139], a[76:79], v[188:191]\n\t" "v_mfma_f32_16x16x32_bf16 v[188:191], a[140:143], a[92:95], v[188:191]\n\t" "s_waitcnt lgkmcnt(4)\n\t"
    "v_mfma_f32_16x16x32_bf16 v[192:195], a[144:147], a[64:67], v[192:195]\n\t" "ds_read_b64_tr_b16 a[128:129], %[vr4] offset:35872\n\t"
    "v_mfma_f32_16x16x32_bf16 v[192:195], a[148:151], a[80:83], v[192:195]\n\t" "ds_read_b64_tr_b16 a[130:131], %[vr4] offset:39456\n\t"
    "v_mfma_f32_16x16x32_bf16 v[196:199], a[144:147], a[68:71], v[196:199]\n\t" "global_load_dwordx4 a[24:27], %[va], s[76:77] offset:2048 sc1\n\t"
    "v_mfma_f32_16x16x32_bf16 v[196:199], a[148:151], a[84:87], v[196:199]\n\t" "v_mfma_f32_16x16x32_bf16 v[200:203], a[144:147], a[72:75], v[200:203]\n\t"
    "ds_read_b64_tr_b16 a[132:133], %[vr4] offset:50208\n\t" "v_mfma_f32_16x16x32_bf16 v[200:203], a[148:151], a[88:91], v[200:203]\n\t" "ds_read_b64_tr_b16 a[134:135], %[vr4] offset:53792\n\t"
    "v_mfma_f32_16x16x32_bf16 v[204:207], a[144:147], a[76:79], v[204:207]\n\t" "v_mfma_f32_16x16x32_bf16 v[204:207], a[148:151], a[92:95], v[204:207]\n\t" "s_waitcnt lgkmcnt(4)\n\t"
    "v_mfma_f32_16x16x32_bf16 v[208:211], a[152:155], a[64:67], v[208:211]\n\t" "ds_read_b64_tr_b16 a[136:137], %[vr6] offset:35840\n\t"
    "v_mfma_f32_16x16x32_bf16 v[208:211], a[156:159], a[80:83], v[208:211]\n\t" "ds_read_b64_tr_b16 a[138:139], %[vr6] offset:39424\n\t"
    "v_mfma_f32_16x16x32_bf16 a[172:175], a[152:155], a[184:187], 0\n\t" "s_add_u32 m0, %[sl], 2048\n\t" "v_mfma_f32_16x16x32_bf16 v[212:215], a[152:155], a[68:71], v[212:215]\n\t"
    "global_load_lds_dwordx4 %[vb2], s[78:79]\n\t" "v_mfma_f32_16x16x32_bf16 v[212:215], a[156:159], a[84:87], v[212:215]\n\t" "v_mfma_f32_16x16x32_bf16 v[216:219], a[152:155], a[72:75], v[216:219]\n\t"
    "ds_read_b64_tr_b16 a[140:141], %[vr6] offset:50176\n\t" "v_mfma_f32_16x16x32_bf16 v[216:219], a[156:159], a[88:91], v[216:219]\n\t" "ds_read_b64_tr_b16 a[142:143], %[vr6] offset:53760\n\t"
    "v_mfma_f32_16x16x32_bf16 a[172:175], a[156:159], a[188:191], a[172:175]\n\t" "v_mfma_f32_16x16x32_bf16 v[220:223], a[152:155], a[76:79], v[220:223]\n\t"
    "global_load_dwordx4 a[28:31], %[va], s[76:77] offset:3072 sc1\n\t" "v_mfma_f32_16x16x32_bf16 v[220:223], a[156:159], a[92:95], v[220:223]\n\t" "s_add_u32 s76, s76, 16384\n\t"
    "s_addc_u32 s77, s77, 0\n\t" "s_waitcnt lgkmcnt(4)\n\t" "v_mfma_f32_16x16x32_bf16 v[224:227], a[128:131], a[64:67], v[224:227]\n\t" "s_waitcnt vmcnt(24)\n\t" "s_barrier\n\t"
    "ds_read_b64_tr_b16 a[144:145], %[vr1] offset:0\n\t" "v_mfma_f32_16x16x32_bf16 v[224:227], a[132:135], a[80:83], v[224:227]\n\t" "ds_read_b64_tr_b16 a[146:147], %[vr1] offset:3584\n\t"
    "v_mfma_f32_16x16x32_bf16 a[176:179], a[128:131], a[184:187], 0\n\t" "v_mfma_f32_16x16x32_bf16 v[228:231], a[128:131], a[68:71], v[228:231]\n\t"
    "v_mfma_f32_16x16x32_bf16 v[228:231], a[132:135], a[84:87], v[228:231]\n\t" "v_mfma_f32_16x16x32_bf16 v[232:235], a[128:131], a[72:75], v[232:235]\n\t"
    "ds_read_b64_tr_b16 a[148:149], %[vr1] offset:14336\n\t" "v_mfma_f32_16x16x32_bf16 v[232:235], a[132:135], a[88:91], v[232:235]\n\t" "ds_read_b64_tr_b16 a[150:151], %[vr1] offset:17920\n\t"
    "v_mfma_f32_16x16x32_bf16 a[176:179], a[132:135], a[188:191], a[176:179]\n\t" "s_add_u32 m0, %[sl], 3072\n\t" "v_mfma_f32_16x16x32_bf16 v[236:239], a[128:131], a[76:79], v[236:239]\n\t"
    "s_mov_b64 exec, s[82:83]\n\t" "global_load_lds_dwordx4 %[vb3], s[78:79]\n\t" "s_mov_b64 exec, -1\n\t" "v_mfma_f32_16x16x32_bf16 v[236:239], a[132:135], a[92:95], v[236:239]\n\t"
    "s_add_u32 s78, s78, 229376\n\t" "s_addc_u32 s79, s79, 0\n\t" "s_waitcnt lgkmcnt(4)\n\t" "v_mfma_f32_16x16x32_bf16 v[240:243], a[136:139], a[64:67], v[240:243]\n\t"
    "ds_read_b64_tr_b16 a[152:153], %[vr1] offset:32\n\t" "v_mfma_f32_16x16x32_bf16 v[240:243], a[140:143], a[80:83], v[240:243]\n\t" "ds_read_b64_tr_b16 a[154:155], %[vr1] offset:3616\n\t"
    "v_mfma_f32_16x16x32_bf16 a[180:183], a[136:139], a[184:187], 0\n\t" "v_mfma_f32_16x16x32_bf16 v[244:247], a[136:139], a[68:71], v[244:247]\n\t"
    "v_mfma_f32_16x16x32_bf16 v[244:247], a[140:143], a[84:87], v[244:247]\n\t" "v_mfma_f32_16x16x32_bf16 v[248:251], a[136:139], a[72:75], v[248:251]\n\t"
    "ds_read_b64_tr_b16 a[156:157], %[vr1] offset:14368\n\t" "v_mfma_f32_16x16x32_bf16 v[248:251], a[140:143], a[88:91], v[248:251]\n\t" "ds_read_b64_tr_b16 a[158:159], %[vr1] offset:17952\n\t"
    "v_mfma_f32_16x16x32_bf16 a[180:183], a[140:143], a[188:191], a[180:183]\n\t" "v_mfma_f32_16x16x32_bf16 v[252:255], a[136:139], a[76:79], v[252:255]\n\t"
    "v_mfma_f32_16x16x32_bf16 v[252:255], a[140:143], a[92:95], v[252:255]\n\t" "s_waitcnt lgkmcnt(4) vmcnt(18)\n\t" "v_mfma_f32_16x16x32_bf16 v[32:35], a[144:147], a[96:99], v[32:35]\n\t" "s_barrier\n\t"
    "ds_read_b64_tr_b16 a[128:129], %[vr3] offset:0\n\t" "v_mfma_f32_16x16x32_bf16 v[32:35], a[148:151], a[112:115], v[32:35]\n\t" "ds_read_b64_tr_b16 a[130:131], %[vr3] offset:3584\n\t"
    "v_mfma_f32_16x16x32_bf16 v[36:39], a[144:147], a[100:103], v[36:39]\n\t" "global_load_dwordx4 a[32:35], %[va], s[76:77] offset:0 sc1\n\t"
    "v_mfma_f32_16x16x32_bf16 v[36:39], a[148:151], a[116:119], v[36:39]\n\t" "v_mfma_f32_16x16x32_bf16 v[40:43], a[144:147], a[104:107], v[40:43]\n\t"
    "ds_read_b64_tr_b16 a[132:133], %[vr3] offset:14336\n\t" "v_mfma_f32_16x16x32_bf16 v[40:43], a[148:151], a[120:123], v[40:43]\n\t" "ds_read_b64_tr_b16 a[134:135], %[vr3] offset:17920\n\t"
    "s_add_u32 m0, %[sl], 14336\n\t" "v_mfma_f32_16x16x32_bf16 v[44:47], a[144:147], a[108:111], v[44:47]\n\t" "global_load_lds_dwordx4 %[vb0], s[78:79]\n\t"
    "v_mfma_f32_16x16x32_bf16 v[44:47], a[148:151], a[124:127], v[44:47]\n\t" "s_waitcnt lgkmcnt(4)\n\t" "v_mfma_f32_16x16x32_bf16 v[48:51], a[152:155], a[96:99], v[48:51]\n\t"
    "ds_read_b64_tr_b16 a[136:137], %[vr3] offset:32\n\t" "v_mfma_f32_16x16x32_bf16 v[48:51], a[156:159], a[112:115], v[48:51]\n\t" "ds_read_b64_tr_b16 a[138:139], %[vr3] offset:3616\n\t"
    "v_mfma_f32_16x16x32_bf16 v[52:55], a[152:155], a[100:103], v[52:55]\n\t" "v_mfma_f32_16x16x32_bf16 v[52:55], a[156:159], a[116:119], v[52:55]\n\t"
    "v_mfma_f32_16x16x32_bf16 v[56:59], a[152:155], a[104:107], v[56:59]\n\t" "ds_read_b64_tr_b16 a[140:141], %[vr3] offset:14368\n\t"
    "v_mfma_f32_16x16x32_bf16 v[56:59], a[156:159], a[120:123], v[56:59]\n\t" "ds_read_b64_tr_b16 a[142:143], %[vr3] offset:17952\n\t"
    "v_mfma_f32_16x16x32_bf16 v[60:63], a[152:155], a[108:111], v[60:63]\n\t" "global_load_dwordx4 a[36:39], %[va], s[76:77] offset:1024 sc1\n\t"
    "v_mfma_f32_16x16x32_bf16 v[60:63], a[156:159], a[124:127], v[60:63]\n\t" "s_waitcnt lgkmcnt(4)\n\t" "v_mfma_f32_16x16x32_bf16 v[64:67], a[128:131], a[96:99], v[64:67]\n\t"
    "ds_read_b64_tr_b16 a[144:145], %[vr5] offset:0\n\t" "v_mfma_f32_16x16x32_bf16 v[64:67], a[132:135], a[112:115], v[64:67]\n\t" "ds_read_b64_tr_b16 a[146:147], %[vr5] offset:3584\n\t"
    "s_add_u32 m0, %[sl], 15360\n\t" "v_mfma_f32_16x16x32_bf16 v[68:71], a[128:131], a[100:103], v[68:71]\n\t" "global_load_lds_dwordx4 %[vb1], s[78:79]\n\t"
    "v_mfma_f32_16x16x32_bf16 v[68:71], a[132:135], a[116:119], v[68:71]\n\t" "v_mfma_f32_16x16x32_bf16 v[72:75], a[128:131], a[104:107], v[72:75]\n\t"
    "ds_read_b64_tr_b16 a[148:149], %[vr5] offset:14336\n\t" "v_mfma_f32_16x16x32_bf16 v[72:75], a[132:135], a[120:123], v[72:75]\n\t" "ds_read_b64_tr_b16 a[150:151], %[vr5] offset:17920\n\t"
    "v_mfma_f32_16x16x32_bf16 v[76:79], a[128:131], a[108:111], v[76:79]\n\t" "v_mfma_f32_16x16x32_bf16 v[76:79], a[132:135], a[124:127], v[76:79]\n\t" "s_waitcnt lgkmcnt(4)\n\t"
    "v_mfma_f32_16x16x32_bf16 v[80:83], a[136:139], a[96:99], v[80:83]\n\t" "ds_read_b64_tr_b16 a[152:153], %[vr5] offset:32\n\t" "v_mfma_f32_16x16x32_bf16 v[80:83], a[140:143], a[112:115], v[80:83]\n\t"
    "ds_read_b64_tr_b16 a[154:155], %[vr5] offset:3616\n\t" "v_mfma_f32_16x16x32_bf16 v[84:87], a[136:139], a[100:103], v[84:87]\n\t" "global_load_dwordx4 a[40:43], %[va], s[76:77] offset:2048 sc1\n\t"
    "v_mfma_f32_16x16x32_bf16 v[84:87], a[140:143], a[116:119], v[84:87]\n\t" "v_mfma_f32_16x16x32_bf16 v[88:91], a[136:139], a[104:107], v[88:91]\n\t"
    "ds_read_b64_tr_b16 a[156:157], %[vr5] offset:14368\n\t" "v_mfma_f32_16x16x32_bf16 v[88:91], a[140:143], a[120:123], v[88:91]\n\t" "ds_read_b64_tr_b16 a[158:159], %[vr5] offset:17952\n\t"
    "v_mfma_f32_16x16x32_bf16 v[92:95], a[136:139], a[108:111], v[92:95]\n\t" "v_mfma_f32_16x16x32_bf16 v[92:95], a[140:143], a[124:127], v[92:95]\n\t" "s_waitcnt lgkmcnt(4)\n\t"
    "v_mfma_f32_16x16x32_bf16 v[96:99], a[144:147], a[96:99], v[96:99]\n\t" "ds_read_b64_tr_b16 a[128:129], %[vr7] offset:0\n\t" "v_mfma_f32_16x16x32_bf16 v[96:99], a[148:151], a[112:115], v[96:99]\n\t"
    "ds_read_b64_tr_b16 a[130:131], %[vr7] offset:3584\n\t" "v_mfma_f32_16x16x32_bf16 a[160:163], a[144:147], a[192:195], a[160:163]\n\t" "s_add_u32 m0, %[sl], 16384\n\t"
    "v_mfma_f32_16x16x32_bf16 v[100:103], a[144:147], a[100:103], v[100:103]\n\t" "global_load_lds_dwordx4 %[vb2], s[78:79]\n\t"
    "v_mfma_f32_16x16x32_bf16 v[100:103], a[148:151], a[116:119], v[100:103]\n\t" "v_mfma_f32_16x16x32_bf16 v[104:107], a[144:147], a[104:107], v[104:107]\n\t"
    "ds_read_b64_tr_b16 a[132:133], %[vr7] offset:14336\n\t" "v_mfma_f32_16x16x32_bf16 v[104:107], a[148:151], a[120:123], v[104:107]\n\t" "ds_read_b64_tr_b16 a[134:135], %[vr7] offset:17920\n\t"
    "v_mfma_f32_16x16x32_bf16 a[160:163], a[148:151], a[196:199], a[160:163]\n\t" "v_mfma_f32_16x16x32_bf16 v[108:111], a[144:147], a[108:111], v[108:111]\n\t"
    "global_load_dwordx4 a[44:47], %[va], s[76:77] offset:3072 sc1\n\t" "v_mfma_f32_16x16x32_bf16 v[108:111], a[148:151], a[124:127], v[108:111]\n\t" "s_add_u32 s76, s76, 16384\n\t"
    "s_addc_u32 s77, s77, 0\n\t" "s_waitcnt lgkmcnt(4)\n\t" "v_mfma_f32_16x16x32_bf16 v[112:115], a[152:155], a[96:99], v[112:115]\n\t" "ds_read_b64_tr_b16 a[136:137], %[vr1] offset:7168\n\t"
    "v_mfma_f32_16x16x32_bf16 v[112:115], a[156:159], a[112:115], v[112:115]\n\t" "ds_read_b64_tr_b16 a[138:139], %[vr1] offset:10752\n\t"
    "v_mfma_f32_16x16x32_bf16 a[164:167], a[152:155], a[192:195], a[164:167]\n\t" "v_mfma_f32_16x16x32_bf16 v[116:119], a[152:155], a[100:103], v[116:119]\n\t"
    "v_mfma_f32_16x16x32_bf16 v[116:119], a[156:159], a[116:119], v[116:119]\n\t" "v_mfma_f32_16x16x32_bf16 v[120:123], a[152:155], a[104:107], v[120:123]\n\t"
    "ds_read_b64_tr_b16 a[140:141], %[vr1] offset:21504\n\t" "v_mfma_f32_16x16x32_bf16 v[120:123], a[156:159], a[120:123], v[120:123]\n\t" "ds_read_b64_tr_b16 a[142:143], %[vr1] offset:25088\n\t"
    "v_mfma_f32_16x16x32_bf16 a[164:167], a[156:159], a[196:199], a[164:167]\n\t" "s_add_u32 m0, %[sl], 17408\n\t" "v_mfma_f32_16x16x32_bf16 v[124:127], a[152:155], a[108:111], v[124:127]\n\t"
    "s_mov_b64 exec, s[82:83]\n\t" "global_load_lds_dwordx4 %[vb3], s[78:79]\n\t" "s_mov_b64 exec, -1\n\t" "v_mfma_f32_16x16x32_bf16 v[124:127], a[156:159], a[124:127], v[124:127]\n\t"
    "s_add_u32 s78, s78, 229376\n\t" "s_addc_u32 s79, s79, 0\n\t" "s_waitcnt lgkmcnt(4)\n\t" "v_mfma_f32_16x16x32_bf16 v[128:131], a[128:131], a[96:99], v[128:131]\n\t"
    "ds_read_b64_tr_b16 a[144:145], %[vr1] offset:7200\n\t" "v_mfma_f32_16x16x32_bf16 v[128:131], a[132:135], a[112:115], v[128:131]\n\t" "ds_read_b64_tr_b16 a[146:147], %[vr1] offset:10784\n\t"
    "v_mfma_f32_16x16x32_bf16 a[168:171], a[128:131], a[192:195], a[168:171]\n\t" "v_mfma_f32_16x16x32_bf16 v[132:135], a[128:131], a[100:103], v[132:135]\n\t"
    "v_mfma_f32_16x16x32_bf16 v[132:135], a[132:135], a[116:119], v[132:135]\n\t" "v_mfma_f32_16x16x32_bf16 v[136:139], a[128:131], a[104:107], v[136:139]\n\t"
    "ds_read_b64_tr_b16 a[148:149], %[vr1] offset:21536\n\t" "v_mfma_f32_16x16x32_bf16 v[136:139], a[132:135], a[120:123], v[136:139]\n\t" "ds_read_b64_tr_b16 a[150:151], %[vr1] offset:25120\n\t"
    "v_mfma_f32_16x16x32_bf16 a[168:171], a[132:135], a[196:199], a[168:171]\n\t" "v_mfma_f32_16x16x32_bf16 v[140:143], a[128:131], a[108:111], v[140:143]\n\t"
    "v_mfma_f32_16x16x32_bf16 v[140:143], a[132:135], a[124:127], v[140:143]\n\t" "s_waitcnt lgkmcnt(4)\n\t" "v_mfma_f32_16x16x32_bf16 v[144:147], a[136:139], a[96:99], v[144:147]\n\t"
    "ds_read_b64_tr_b16 a[152:153], %[vr3] offset:7168\n\t" "v_mfma_f32_16x16x32_bf16 v[144:147], a[140:143], a[112:115], v[144:147]\n\t" "ds_read_b64_tr_b16 a[154:155], %[vr3] offset:10752\n\t"
    "v_mfma_f32_16x16x32_bf16 v[148:151], a[136:139], a[100:103], v[148:151]\n\t" "global_load_dwordx4 a[48:51], %[va], s[76:77] offset:0 sc1\n\t"
    "v_mfma_f32_16x16x32_bf16 v[148:151], a[140:143], a[116:119], v[148:151]\n\t" "v_mfma_f32_16x16x32_bf16 v[152:155], a[136:139], a[104:107], v[152:155]\n\t"
    "ds_read_b64_tr_b16 a[156:157], %[vr3] offset:21504\n\t" "v_mfma_f32_16x16x32_bf16 v[152:155], a[140:143], a[120:123], v[152:155]\n\t" "ds_read_b64_tr_b16 a[158:159], %[vr3] offset:25088\n\t"
    "s_add_u32 m0, %[sl], 28672\n\t" "v_mfma_f32_16x16x32_bf16 v[156:159], a[136:139], a[108:111], v[156:159]\n\t" "global_load_lds_dwordx4 %[vb0], s[78:79]\n\t"
    "v_mfma_f32_16x16x32_bf16 v[156:159], a[140:143], a[124:127], v[156:159]\n\t" "s_waitcnt lgkmcnt(4)\n\t" "v_mfma_f32_16x16x32_bf16 v[160:163], a[144:147], a[96:99], v[160:163]\n\t"
    "ds_read_b64_tr_b16 a[128:129], %[vr3] offset:7200\n\t" "v_mfma_f32_16x16x32_bf16 v[160:163], a[148:151], a[112:115], v[160:163]\n\t" "ds_read_b64_tr_b16 a[130:131], %[vr3] offset:10784\n\t"
    "v_mfma_f32_16x16x32_bf16 v[164:167], a[144:147], a[100:103], v[164:167]\n\t" "v_mfma_f32_16x16x32_bf16 v[164:167], a[148:151], a[116:119], v[164:167]\n\t"
    "v_mfma_f32_16x16x32_bf16 v[168:171], a[144:147], a[104:107], v[168:171]\n\t" "ds_read_b64_tr_b16 a[132:133], %[vr3] offset:21536\n\t"
    "v_mfma_f32_16x16x32_bf16 v[168:171], a[148:151], a[120:123], v[168:171]\n\t" "ds_read_b64_tr_b16 a[134:135], %[vr3] offset:25120\n\t"
    "v_mfma_f32_16x16x32_bf16 v[172:175], a[144:147], a[108:111], v[172:175]\n\t" "global_load_dwordx4 a[52:55], %[va], s[76:77] offset:1024 sc1\n\t"
    "v_mfma_f32_16x16x32_bf16 v[172:175], a[148:151], a[124:127], v[172:175]\n\t" "s_waitcnt lgkmcnt(4)\n\t" "v_mfma_f32_16x16x32_bf16 v[176:179], a[152:155], a[96:99], v[176:179]\n\t"
    "ds_read_b64_tr_b16 a[136:137], %[vr5] offset:7168\n\t" "v_mfma_f32_16x16x32_bf16 v[176:179], a[156:159], a[112:115], v[176:179]\n\t" "ds_read_b64_tr_b16 a[138:139], %[vr5] offset:10752\n\t"
    "s_add_u32 m0, %[sl], 29696\n\t" "v_mfma_f32_16x16x32_bf16 v[180:183], a[152:155], a[100:103], v[180:183]\n\t" "global_load_lds_dwordx4 %[vb1], s[78:79]\n\t"
    "v_mfma_f32_16x16x32_bf16 v[180:183], a[156:159], a[116:119], v[180:183]\n\t" "v_mfma_f32_16x16x32_bf16 v[184:187], a[152:155], a[104:107], v[184:187]\n\t"
    "ds_read_b64_tr_b16 a[140:141], %[vr5] offset:21504\n\t" "v_mfma_f32_16x16x32_bf16 v[184:187], a[156:159], a[120:123], v[184:187]\n\t" "ds_read_b64_tr_b16 a[142:143], %[vr5] offset:25088\n\t"
    "v_mfma_f32_16x16x32_bf16 v[188:191], a[152:155], a[108:111], v[188:191]\n\t" "v_mfma_f32_16x16x32_bf16 v[188:191], a[156:159], a[124:127], v[188:191]\n\t" "s_waitcnt lgkmcnt(4)\n\t"
    "v_mfma_f32_16x16x32_bf16 v[192:195], a[128:131], a[96:99], v[192:195]\n\t" "ds_read_b64_tr_b16 a[144:145], %[vr5] offset:7200\n\t"
    "v_mfma_f32_16x16x32_bf16 v[192:195], a[132:135], a[112:115], v[192:195]\n\t" "ds_read_b64_tr_b16 a[146:147], %[vr5] offset:10784\n\t"
    "v_mfma_f32_16x16x32_bf16 v[196:199], a[128:131], a[100:103], v[196:199]\n\t" "global_load_dwordx4 a[56:59], %[va], s[76:77] offset:2048 sc1\n\t"
    "v_mfma_f32_16x16x32_bf16 v[196:199], a[132:135], a[116:119], v[196:199]\n\t" "v_mfma_f32_16x16x32_bf16 v[200:203], a[128:131], a[104:107], v[200:203]\n\t"
    "ds_read_b64_tr_b16 a[148:149], %[vr5] offset:21536\n\t" "v_mfma_f32_16x16x32_bf16 v[200:203], a[132:135], a[120:123], v[200:203]\n\t" "ds_read_b64_tr_b16 a[150:151], %[vr5] offset:25120\n\t"
    "v_mfma_f32_16x16x32_bf16 v[204:207], a[128:131], a[108:111], v[204:207]\n\t" "v_mfma_f32_16x16x32_bf16 v[204:207], a[132:135], a[124:127], v[204:207]\n\t" "s_waitcnt lgkmcnt(4)\n\t"
    "v_mfma_f32_16x16x32_bf16 v[208:211], a[136:139], a[96:99], v[208:211]\n\t" "ds_read_b64_tr_b16 a[152:153], %[vr7] offset:7168\n\t"
    "v_mfma_f32_16x16x32_bf16 v[208:211], a[140:143], a[112:115], v[208:211]\n\t" "ds_read_b64_tr_b16 a[154:155], %[vr7] offset:10752\n\t"
    "v_mfma_f32_16x16x32_bf16 a[172:175], a[136:139], a[192:195], a[172:175]\n\t" "s_add_u32 m0, %[sl], 30720\n\t" "v_mfma_f32_16x16x32_bf16 v[212:215], a[136:139], a[100:103], v[212:215]\n\t"
    "global_load_lds_dwordx4 %[vb2], s[78:79]\n\t" "v_mfma_f32_16x16x32_bf16 v[212:215], a[140:143], a[116:119], v[212:215]\n\t"
    "v_mfma_f32_16x16x32_bf16 v[216:219], a[136:139], a[104:107], v[216:219]\n\t" "ds_read_b64_tr_b16 a[156:157], %[vr7] offset:21504\n\t"
    "v_mfma_f32_16x16x32_bf16 v[216:219], a[140:143], a[120:123], v[216:219]\n\t" "ds_read_b64_tr_b16 a[158:159], %[vr7] offset:25088\n\t"
    "v_mfma_f32_16x16x32_bf16 a[172:175], a[140:143], a[196:199], a[172:175]\n\t" "v_mfma_f32_16x16x32_bf16 v[220:223], a[136:139], a[108:111], v[220:223]\n\t"
    "global_load_dwordx4 a[60:63], %[va], s[76:77] offset:3072 sc1\n\t" "v_mfma_f32_16x16x32_bf16 v[220:223], a[140:143], a[124:127], v[220:223]\n\t" "s_add_u32 s76, s76, 16384\n\t"
    "s_addc_u32 s77, s77, 0\n\t" "s_waitcnt lgkmcnt(4)\n\t" "v_mfma_f32_16x16x32_bf16 v[224:227], a[144:147], a[96:99], v[224:227]\n\t" "s_waitcnt vmcnt(24)\n\t" "s_barrier\n\t"
    "ds_read_b64_tr_b16 a[128:129], %[vr1] offset:28672\n\t" "v_mfma_f32_16x16x32_bf16 v[224:227], a[148:151], a[112:115], v[224:227]\n\t" "ds_read_b64_tr_b16 a[130:131], %[vr1] offset:32256\n\t"
    "v_mfma_f32_16x16x32_bf16 a[176:179], a[144:147], a[192:195], a[176:179]\n\t" "v_mfma_f32_16x16x32_bf16 v[228:231], a[144:147], a[100:103], v[228:231]\n\t"
    "v_mfma_f32_16x16x32_bf16 v[228:231], a[148:151], a[116:119], v[228:231]\n\t" "v_mfma_f32_16x16x32_bf16 v[232:235], a[144:147], a[104:107], v[232:235]\n\t"
    "ds_read_b64_tr_b16 a[132:133], %[vr1] offset:43008\n\t" "v_mfma_f32_16x16x32_bf16 v[232:235], a[148:151], a[120:123], v[232:235]\n\t" "ds_read_b64_tr_b16 a[134:135], %[vr1] offset:46592\n\t"
    "v_mfma_f32_16x16x32_bf16 a[176:179], a[148:151], a[196:199], a[176:179]\n\t" "s_add_u32 m0, %[sl], 31744\n\t" "v_mfma_f32_16x16x32_bf16 v[236:239], a[144:147], a[108:111], v[236:239]\n\t"
    "s_mov_b64 exec, s[82:83]\n\t" "global_load_lds_dwordx4 %[vb3], s[78:79]\n\t" "s_mov_b64 exec, -1\n\t" "v_mfma_f32_16x16x32_bf16 v[236:239], a[148:151], a[124:127], v[236:239]\n\t"
    "s_add_u32 s78, s78, 229376\n\t" "s_addc_u32 s79, s79, 0\n\t" "s_waitcnt lgkmcnt(4)\n\t" "v_mfma_f32_16x16x32_bf16 v[240:243], a[152:155], a[96:99], v[240:243]\n\t"
    "ds_read_b64_tr_b16 a[136:137], %[vr1] offset:28704\n\t" "v_mfma_f32_16x16x32_bf16 v[240:243], a[156:159], a[112:115], v[240:243]\n\t" "ds_read_b64_tr_b16 a[138:139], %[vr1] offset:32288\n\t"
    "v_mfma_f32_16x16x32_bf16 a[180:183], a[152:155], a[192:195], a[180:183]\n\t" "v_mfma_f32_16x16x32_bf16 v[244:247], a[152:155], a[100:103], v[244:247]\n\t"
    "v_mfma_f32_16x16x32_bf16 v[244:247], a[156:159], a[116:119], v[244:247]\n\t" "v_mfma_f32_16x16x32_bf16 v[248:251], a[152:155], a[104:107], v[248:251]\n\t"
    "ds_read_b64_tr_b16 a[140:141], %[vr1] offset:43040\n\t" "v_mfma_f32_16x16x32_bf16 v[248:251], a[156:159], a[120:123], v[248:251]\n\t" "ds_read_b64_tr_b16 a[142:143], %[vr1] offset:46624\n\t"
    "v_mfma_f32_16x16x32_bf16 a[180:183], a[156:159], a[196:199], a[180:183]\n\t" "v_mfma_f32_16x16x32_bf16 v[252:255], a[152:155], a[108:111], v[252:255]\n\t"
    "v_mfma_f32_16x16x32_bf16 v[252:255], a[156:159], a[124:127], v[252:255]\n\t" "v_accvgpr_read_b32 v31, %[cp]\n\t" "s_nop 7\n\t" "s_mov_b64 exec, %[m01]\n\t"
    "global_store_dwordx4 v31, a[160:163], %[su0] offset:0 sc1\n\t" "global_store_dwordx4 v31, a[164:167], %[su0] offset:2304 sc1\n\t" "global_store_dwordx4 v31, a[172:175], %[su0] offset:1152 sc1\n\t"
    "global_store_dwordx4 v31, a[176:179], %[su0] offset:3456 sc1\n\t" "s_mov_b64 exec, %[m6]\n\t" "global_store_dwordx4 v31, a[168:171], %[su6] offset:0 sc1\n\t"
    "global_store_dwordx4 v31, a[180:183], %[su6] offset:1152 sc1\n\t" "s_mov_b64 exec, -1\n\t" "s_branch .Ld1_%=\n\t" ".Lph_%=:\n\t" "s_sleep 1\n\t"
    "global_load_dwordx2 v[24:25], v28, %[sw] offset:128 sc1\n\t" "s_waitcnt vmcnt(0)\n\t" "v_cmp_ne_u64_e64 vcc, %[tag], v[24:25]\n\t" "s_nop 4\n\t" "s_cbranch_vccz .Lrh_%=\n\t"
    "s_sub_u32 s81, s81, 1\n\t" "s_cbranch_scc0 .Lph_%=\n\t" "s_branch .Lrh_%=\n\t" ".Lslow0_%=:\n\t" "s_sleep 2\n\t" "v_accvgpr_read_b32 v31, %[cr0]\n\t" "s_mov_b64 s[96:97], 0\n\t"
    "v_subrev_u32 v31, %[rb], v31\n\t" "global_load_dword v24, v31, %[su] offset:0 sc1\n\t" "global_load_dword v25, v31, %[su] offset:16 sc1\n\t" "global_load_dword v26, v31, %[su] offset:32 sc1\n\t"
    "global_load_dword v27, v31, %[su] offset:48 sc1\n\t" "global_load_dword v28, v31, %[su] offset:64 sc1\n\t" "global_load_dword v29, v31, %[su] offset:80 sc1\n\t"
    "global_load_dword v30, v31, %[su] offset:96 sc1\n\t" "global_load_dword v31, v31, %[su] offset:112 sc1\n\t" "s_waitcnt vmcnt(0)\n\t" "v_cmp_eq_u32 vcc, -1, v24\n\t" "s_nop 1\n\t"
    "s_or_b64 s[96:97], s[96:97], vcc\n\t" "v_cmp_eq_u32 vcc, -1, v25\n\t" "s_nop 1\n\t" "s_or_b64 s[96:97], s[96:97], vcc\n\t" "v_cmp_eq_u32 vcc, -1, v26\n\t" "s_nop 1\n\t"
    "s_or_b64 s[96:97], s[96:97], vcc\n\t" "v_cmp_eq_u32 vcc, -1, v27\n\t" "s_nop 1\n\t" "s_or_b64 s[96:97], s[96:97], vcc\n\t" "v_cmp_eq_u32 vcc, -1, v28\n\t" "s_nop 1\n\t"
    "s_or_b64 s[96:97], s[96:97], vcc\n\t" "v_cmp_eq_u32 vcc, -1, v29\n\t" "s_nop 1\n\t" "s_or_b64 s[96:97], s[96:97], vcc\n\t" "v_cmp_eq_u32 vcc, -1, v30\n\t" "s_nop 1\n\t"
    "s_or_b64 s[96:97], s[96:97], vcc\n\t" "v_cmp_eq_u32 vcc, -1, v31\n\t" "s_nop 1\n\t" "s_or_b64 s[96:97], s[96:97], vcc\n\t" "v_add_f32 v24, v24, v25\n\t" "v_add_f32 v26, v26, v27\n\t"
    "v_add_f32 v28, v28, v29\n\t" "v_add_f32 v30, v30, v31\n\t" "v_add_f32 v24, v24, v26\n\t" "v_add_f32 v28, v28, v30\n\t" "v_add_f32_e64 v24, v24, v28\n\t" "s_cbranch_scc0 .Lgo0_%=\n\t"
    "s_sub_u32 s94, s94, 1\n\t" "s_cbranch_scc0 .Lslow0_%=\n\t" "s_branch .Lgo0_%=\n\t" ".Lslow1_%=:\n\t" "s_sleep 2\n\t" "v_accvgpr_read_b32 v31, %[cr1]\n\t" "s_mov_b64 s[96:97], 0\n\t"
    "v_subrev_u32 v31, %[rb], v31\n\t" "global_load_dword v24, v31, %[su] offset:0 sc1\n\t" "global_load_dword v25, v31, %[su] offset:16 sc1\n\t" "global_load_dword v26, v31, %[su] offset:32 sc1\n\t"
    "global_load_dword v27, v31, %[su] offset:48 sc1\n\t" "global_load_dword v28, v31, %[su] offset:64 sc1\n\t" "global_load_dword v29, v31, %[su] offset:80 sc1\n\t"
    "global_load_dword v30, v31, %[su] offset:96 sc1\n\t" "global_load_dword v31, v31, %[su] offset:112 sc1\n\t" "s_waitcnt vmcnt(0)\n\t" "v_cmp_eq_u32 vcc, -1, v24\n\t" "s_nop 1\n\t"
    "s_or_b64 s[96:97], s[96:97], vcc\n\t" "v_cmp_eq_u32 vcc, -1, v25\n\t" "s_nop 1\n\t" "s_or_b64 s[96:97], s[96:97], vcc\n\t" "v_cmp_eq_u32 vcc, -1, v26\n\t" "s_nop 1\n\t"
    "s_or_b64 s[96:97], s[96:97], vcc\n\t" "v_cmp_eq_u32 vcc, -1, v27\n\t" "s_nop 1\n\t" "s_or_b64 s[96:97], s[96:97], vcc\n\t" "v_cmp_eq_u32 vcc, -1, v28\n\t" "s_nop 1\n\t"
    "s_or_b64 s[96:97], s[96:97], vcc\n\t" "v_cmp_eq_u32 vcc, -1, v29\n\t" "s_nop 1\n\t" "s_or_b64 s[96:97], s[96:97], vcc\n\t" "v_cmp_eq_u32 vcc, -1, v30\n\t" "s_nop 1\n\t"
    "s_or_b64 s[96:97], s[96:97], vcc\n\t" "v_cmp_eq_u32 vcc, -1, v31\n\t" "s_nop 1\n\t" "s_or_b64 s[96:97], s[96:97], vcc\n\t" "v_add_f32 v24, v24, v25\n\t" "v_add_f32 v26, v26, v27\n\t"
    "v_add_f32 v28, v28, v29\n\t" "v_add_f32 v30, v30, v31\n\t" "v_add_f32 v24, v24, v26\n\t" "v_add_f32 v28, v28, v30\n\t" "v_add_f32_e64 v24, v24, v28\n\t" "s_cbranch_scc0 .Lgo1_%=\n\t"
    "s_sub_u32 s94, s94, 1\n\t" "s_cbranch_scc0 .Lslow1_%=\n\t" "s_branch .Lgo1_%=\n\t" ".Ltail_%=:\n\t" "s_cmp_eq_u32 s80, 1\n\t" "s_cselect_b32 s86, %[wa], 16384\n\t" "s_ashr_i32 s87, s86, 31\n\t"
    "s_cmp_eq_u32 s80, 1\n\t" "s_cselect_b32 s88, %[wb], 229376\n\t" "s_ashr_i32 s89, s88, 31\n\t" "s_waitcnt lgkmcnt(4) vmcnt(17)\n\t"
    "v_mfma_f32_16x16x32_bf16 v[32:35], a[128:131], a[64:67], v[32:35]\n\t" "s_barrier\n\t" "ds_read_b64_tr_b16 a[144:145], %[vr2] offset:28672\n\t" "v_accvgpr_write_b32 a200, 0\n\t"
    "v_mfma_f32_16x16x32_bf16 v[32:35], a[132:135], a[80:83], v[32:35]\n\t" "ds_read_b64_tr_b16 a[146:147], %[vr2] offset:32256\n\t" "v_accvgpr_write_b32 a201, 0\n\t"
    "v_mfma_f32_16x16x32_bf16 v[36:39], a[128:131], a[68:71], v[36:39]\n\t" "global_load_dwordx4 a[0:3], %[va], s[76:77] offset:0 sc1\n\t" "v_accvgpr_write_b32 a202, 0\n\t"
    "v_mfma_f32_16x16x32_bf16 v[36:39], a[132:135], a[84:87], v[36:39]\n\t" "v_accvgpr_write_b32 a203, 0\n\t" "v_mfma_f32_16x16x32_bf16 v[40:43], a[128:131], a[72:75], v[40:43]\n\t"
    "ds_read_b64_tr_b16 a[148:149], %[vr2] offset:43008\n\t" "v_accvgpr_write_b32 a204, 0\n\t" "v_mfma_f32_16x16x32_bf16 v[40:43], a[132:135], a[88:91], v[40:43]\n\t"
    "ds_read_b64_tr_b16 a[150:151], %[vr2] offset:46592\n\t" "v_accvgpr_write_b32 a205, 0\n\t" "s_add_u32 m0, %[sl], 100352\n\t" "v_mfma_f32_16x16x32_bf16 v[44:47], a[128:131], a[76:79], v[44:47]\n\t"
    "global_load_lds_dwordx4 %[vb0], s[78:79]\n\t" "v_accvgpr_write_b32 a206, 0\n\t" "v_mfma_f32_16x16x32_bf16 v[44:47], a[132:135], a[92:95], v[44:47]\n\t" "v_accvgpr_write_b32 a207, 0\n\t"
    "s_waitcnt lgkmcnt(4)\n\t" "v_mfma_f32_16x16x32_bf16 v[48:51], a[136:139], a[64:67], v[48:51]\n\t" "ds_read_b64_tr_b16 a[152:153], %[vr2] offset:28704\n\t" "v_accvgpr_write_b32 a208, 0\n\t"
    "v_mfma_f32_16x16x32_bf16 v[48:51], a[140:143], a[80:83], v[48:51]\n\t" "ds_read_b64_tr_b16 a[154:155], %[vr2] offset:32288\n\t" "v_accvgpr_write_b32 a209, 0\n\t"
    "v_mfma_f32_16x16x32_bf16 v[52:55], a[136:139], a[68:71], v[52:55]\n\t" "global_load_dwordx4 a[4:7], %[va], s[76:77] offset:1024 sc1\n\t" "v_accvgpr_write_b32 a210, 0\n\t"
    "v_mfma_f32_16x16x32_bf16 v[52:55], a[140:143], a[84:87], v[52:55]\n\t" "v_accvgpr_write_b32 a211, 0\n\t" "v_mfma_f32_16x16x32_bf16 v[56:59], a[136:139], a[72:75], v[56:59]\n\t"
    "ds_read_b64_tr_b16 a[156:157], %[vr2] offset:43040\n\t" "v_accvgpr_write_b32 a212, 0\n\t" "v_mfma_f32_16x16x32_bf16 v[56:59], a[140:143], a[88:91], v[56:59]\n\t"
    "ds_read_b64_tr_b16 a[158:159], %[vr2] offset:46624\n\t" "v_accvgpr_write_b32 a213, 0\n\t" "s_add_u32 m0, %[sl], 101376\n\t" "v_mfma_f32_16x16x32_bf16 v[60:63], a[136:139], a[76:79], v[60:63]\n\t"
    "global_load_lds_dwordx4 %[vb1], s[78:79]\n\t" "v_accvgpr_write_b32 a214, 0\n\t" "v_mfma_f32_16x16x32_bf16 v[60:63], a[140:143], a[92:95], v[60:63]\n\t" "v_accvgpr_write_b32 a215, 0\n\t"
    "s_waitcnt lgkmcnt(4)\n\t" "v_mfma_f32_16x16x32_bf16 v[64:67], a[144:147], a[64:67], v[64:67]\n\t" "ds_read_b64_tr_b16 a[128:129], %[vr4] offset:28672\n\t" "v_accvgpr_read_b32 v24, a216\n\t"
    "v_mfma_f32_16x16x32_bf16 v[64:67], a[148:151], a[80:83], v[64:67]\n\t" "ds_read_b64_tr_b16 a[130:131], %[vr4] offset:32256\n\t" "v_accvgpr_read_b32 v25, a217\n\t"
    "v_mfma_f32_16x16x32_bf16 v[68:71], a[144:147], a[68:71], v[68:71]\n\t" "v_accvgpr_read_b32 v28, a218\n\t" "v_mfma_f32_16x16x32_bf16 v[68:71], a[148:151], a[84:87], v[68:71]\n\t"
    "v_accvgpr_read_b32 v29, a219\n\t" "v_mfma_f32_16x16x32_bf16 v[72:75], a[144:147], a[72:75], v[72:75]\n\t" "ds_read_b64_tr_b16 a[132:133], %[vr4] offset:43008\n\t" "v_accvgpr_read_b32 v26, a224\n\t"
    "v_mfma_f32_16x16x32_bf16 v[72:75], a[148:151], a[88:91], v[72:75]\n\t" "ds_read_b64_tr_b16 a[134:135], %[vr4] offset:46592\n\t" "v_accvgpr_read_b32 v27, a225\n\t"
    "v_mfma_f32_16x16x32_bf16 v[76:79], a[144:147], a[76:79], v[76:79]\n\t" "global_load_dwordx4 a[8:11], %[va], s[76:77] offset:2048 sc1\n\t" "v_accvgpr_read_b32 v30, a226\n\t"
    "v_mfma_f32_16x16x32_bf16 v[76:79], a[148:151], a[92:95], v[76:79]\n\t" "v_accvgpr_read_b32 v31, a227\n\t" "s_waitcnt lgkmcnt(4)\n\t"
    "v_mfma_f32_16x16x32_bf16 v[80:83], a[152:155], a[64:67], v[80:83]\n\t" "ds_read_b64_tr_b16 a[136:137], %[vr4] offset:28704\n\t" "v_add_f32_e64 v24, v24, v26\n\t"
    "v_mfma_f32_16x16x32_bf16 v[80:83], a[156:159], a[80:83], v[80:83]\n\t" "ds_read_b64_tr_b16 a[138:139], %[vr4] offset:32288\n\t" "v_add_f32_e64 v25, v25, v27\n\t" "s_add_u32 m0, %[sl], 102400\n\t"
    "v_mfma_f32_16x16x32_bf16 v[84:87], a[152:155], a[68:71], v[84:87]\n\t" "global_load_lds_dwordx4 %[vb2], s[78:79]\n\t" "v_add_f32_e64 v28, v28, v30\n\t"
    "v_mfma_f32_16x16x32_bf16 v[84:87], a[156:159], a[84:87], v[84:87]\n\t" "v_add_f32_e64 v29, v29, v31\n\t" "v_mfma_f32_16x16x32_bf16 v[88:91], a[152:155], a[72:75], v[88:91]\n\t"
    "ds_read_b64_tr_b16 a[140:141], %[vr4] offset:43040\n\t" "v_accvgpr_read_b32 v26, a232\n\t" "v_mfma_f32_16x16x32_bf16 v[88:91], a[156:159], a[88:91], v[88:91]\n\t"
    "ds_read_b64_tr_b16 a[142:143], %[vr4] offset:46624\n\t" "v_accvgpr_read_b32 v27, a233\n\t" "v_mfma_f32_16x16x32_bf16 v[92:95], a[152:155], a[76:79], v[92:95]\n\t"
    "global_load_dwordx4 a[12:15], %[va], s[76:77] offset:3072 sc1\n\t" "v_accvgpr_read_b32 v30, a234\n\t" "v_mfma_f32_16x16x32_bf16 v[92:95], a[156:159], a[92:95], v[92:95]\n\t"
    "s_add_u32 s76, s76, 16384\n\t" "s_addc_u32 s77, s77, 0\n\t" "v_accvgpr_read_b32 v31, a235\n\t" "s_waitcnt lgkmcnt(4)\n\t" "v_mfma_f32_16x16x32_bf16 v[96:99], a[128:131], a[64:67], v[96:99]\n\t"
    "ds_read_b64_tr_b16 a[144:145], %[vr6] offset:28672\n\t" "v_add_f32_e64 v24, v24, v26\n\t" "v_mfma_f32_16x16x32_bf16 v[96:99], a[132:135], a[80:83], v[96:99]\n\t"
    "ds_read_b64_tr_b16 a[146:147], %[vr6] offset:32256\n\t" "v_add_f32_e64 v25, v25, v27\n\t" "v_mfma_f32_16x16x32_bf16 v[100:103], a[128:131], a[68:71], v[100:103]\n\t" "v_add_f32_e64 v28, v28, v30\n\t"
    "v_mfma_f32_16x16x32_bf16 v[100:103], a[132:135], a[84:87], v[100:103]\n\t" "v_add_f32_e64 v29, v29, v31\n\t" "v_mfma_f32_16x16x32_bf16 v[104:107], a[128:131], a[72:75], v[104:107]\n\t"
    "ds_read_b64_tr_b16 a[148:149], %[vr6] offset:43008\n\t" "v_accvgpr_read_b32 v26, a240\n\t" "v_mfma_f32_16x16x32_bf16 v[104:107], a[132:135], a[88:91], v[104:107]\n\t"
    "ds_read_b64_tr_b16 a[150:151], %[vr6] offset:46592\n\t" "v_accvgpr_read_b32 v27, a241\n\t" "s_add_u32 m0, %[sl], 103424\n\t"
    "v_mfma_f32_16x16x32_bf16 v[108:111], a[128:131], a[76:79], v[108:111]\n\t" "s_mov_b64 exec, s[82:83]\n\t" "global_load_lds_dwordx4 %[vb3], s[78:79]\n\t" "s_mov_b64 exec, -1\n\t"
    "v_accvgpr_read_b32 v30, a242\n\t" "v_mfma_f32_16x16x32_bf16 v[108:111], a[132:135], a[92:95], v[108:111]\n\t" "s_add_u32 s78, s78, 229376\n\t" "s_addc_u32 s79, s79, 0\n\t"
    "v_accvgpr_read_b32 v31, a243\n\t" "s_waitcnt lgkmcnt(4)\n\t" "v_mfma_f32_16x16x32_bf16 v[112:115], a[136:139], a[64:67], v[112:115]\n\t" "ds_read_b64_tr_b16 a[152:153], %[vr0] offset:35840\n\t"
    "v_add_f32_e64 v24, v24, v26\n\t" "v_mfma_f32_16x16x32_bf16 v[112:115], a[140:143], a[80:83], v[112:115]\n\t" "ds_read_b64_tr_b16 a[154:155], %[vr0] offset:39424\n\t" "v_add_f32_e64 v25, v25, v27\n\t"
    "v_mfma_f32_16x16x32_bf16 v[116:119], a[136:139], a[68:71], v[116:119]\n\t" "global_load_dwordx4 a[16:19], %[va], s[76:77] offset:0 sc1\n\t" "v_add_f32_e64 v28, v28, v30\n\t"
    "v_mfma_f32_16x16x32_bf16 v[116:119], a[140:143], a[84:87], v[116:119]\n\t" "v_add_f32_e64 v29, v29, v31\n\t" "v_mfma_f32_16x16x32_bf16 v[120:123], a[136:139], a[72:75], v[120:123]\n\t"
    "ds_read_b64_tr_b16 a[156:157], %[vr0] offset:50176\n\t" "v_mov_b32_e64 v26, v24\n\t" "v_mfma_f32_16x16x32_bf16 v[120:123], a[140:143], a[88:91], v[120:123]\n\t"
    "ds_read_b64_tr_b16 a[158:159], %[vr0] offset:53760\n\t" "v_mov_b32 v27, v25\n\t" "s_add_u32 m0, %[sl], 0\n\t" "v_mfma_f32_16x16x32_bf16 v[124:127], a[136:139], a[76:79], v[124:127]\n\t"
    "global_load_lds_dwordx4 %[vb0], s[78:79]\n\t" "v_mov_b32_e64 v30, v28\n\t" "v_mfma_f32_16x16x32_bf16 v[124:127], a[140:143], a[92:95], v[124:127]\n\t" "v_mov_b32 v31, v29\n\t"
    "s_waitcnt lgkmcnt(4)\n\t" "v_mfma_f32_16x16x32_bf16 v[128:131], a[144:147], a[64:67], v[128:131]\n\t" "ds_read_b64_tr_b16 a[128:129], %[vr0] offset:35872\n\t" "s_nop 0\n\t"
    "v_mfma_f32_16x16x32_bf16 v[128:131], a[148:151], a[80:83], v[128:131]\n\t" "ds_read_b64_tr_b16 a[130:131], %[vr0] offset:39456\n\t" "s_nop 0\n\t"
    "v_mfma_f32_16x16x32_bf16 v[132:135], a[144:147], a[68:71], v[132:135]\n\t" "v_permlane16_swap_b32 v24, v26\n\t" "v_mfma_f32_16x16x32_bf16 v[132:135], a[148:151], a[84:87], v[132:135]\n\t"
    "v_permlane16_swap_b32 v25, v27\n\t" "v_mfma_f32_16x16x32_bf16 v[136:139], a[144:147], a[72:75], v[136:139]\n\t" "ds_read_b64_tr_b16 a[132:133], %[vr0] offset:50208\n\t"
    "v_permlane16_swap_b32 v28, v30\n\t" "v_mfma_f32_16x16x32_bf16 v[136:139], a[148:151], a[88:91], v[136:139]\n\t" "ds_read_b64_tr_b16 a[134:135], %[vr0] offset:53792\n\t"
    "v_permlane16_swap_b32 v29, v31\n\t" "v_mfma_f32_16x16x32_bf16 v[140:143], a[144:147], a[76:79], v[140:143]\n\t" "v_add_f32_e64 v24, v24, v26\n\t"
    "v_mfma_f32_16x16x32_bf16 v[140:143], a[148:151], a[92:95], v[140:143]\n\t" "v_add_f32 v25, v25, v27\n\t" "s_waitcnt lgkmcnt(4)\n\t"
    "v_mfma_f32_16x16x32_bf16 v[144:147], a[152:155], a[64:67], v[144:147]\n\t" "ds_read_b64_tr_b16 a[136:137], %[vr2] offset:35840\n\t" "v_add_f32_e64 v28, v28, v30\n\t"
    "v_mfma_f32_16x16x32_bf16 v[144:147], a[156:159], a[80:83], v[144:147]\n\t" "ds_read_b64_tr_b16 a[138:139], %[vr2] offset:39424\n\t" "v_add_f32_e64 v29, v29, v31\n\t"
    "v_mfma_f32_16x16x32_bf16 v[148:151], a[152:155], a[68:71], v[148:151]\n\t" "global_load_dwordx4 a[20:23], %[va], s[76:77] offset:1024 sc1\n\t" "v_mov_b32_e64 v26, v24\n\t"
    "v_mfma_f32_16x16x32_bf16 v[148:151], a[156:159], a[84:87], v[148:151]\n\t" "v_mov_b32_e64 v27, v25\n\t" "v_mfma_f32_16x16x32_bf16 v[152:155], a[152:155], a[72:75], v[152:155]\n\t"
    "ds_read_b64_tr_b16 a[140:141], %[vr2] offset:50176\n\t" "v_mov_b32_e64 v30, v28\n\t" "v_mfma_f32_16x16x32_bf16 v[152:155], a[156:159], a[88:91], v[152:155]\n\t"
    "ds_read_b64_tr_b16 a[142:143], %[vr2] offset:53760\n\t" "v_mov_b32_e64 v31, v29\n\t" "s_add_u32 m0, %[sl], 1024\n\t" "v_mfma_f32_16x16x32_bf16 v[156:159], a[152:155], a[76:79], v[156:159]\n\t"
    "global_load_lds_dwordx4 %[vb1], s[78:79]\n\t" "s_nop 0\n\t" "v_mfma_f32_16x16x32_bf16 v[156:159], a[156:159], a[92:95], v[156:159]\n\t" "s_nop 0\n\t" "s_waitcnt lgkmcnt(4)\n\t"
    "v_mfma_f32_16x16x32_bf16 v[160:163], a[128:131], a[64:67], v[160:163]\n\t" "ds_read_b64_tr_b16 a[144:145], %[vr2] offset:35872\n\t" "v_permlane32_swap_b32 v24, v26\n\t"
    "v_mfma_f32_16x16x32_bf16 v[160:163], a[132:135], a[80:83], v[160:163]\n\t" "ds_read_b64_tr_b16 a[146:147], %[vr2] offset:39456\n\t" "v_permlane32_swap_b32 v25, v27\n\t"
    "v_mfma_f32_16x16x32_bf16 v[164:167], a[128:131], a[68:71], v[164:167]\n\t" "global_load_dwordx4 a[24:27], %[va], s[76:77] offset:2048 sc1\n\t" "v_permlane32_swap_b32 v28, v30\n\t"
    "v_mfma_f32_16x16x32_bf16 v[164:167], a[132:135], a[84:87], v[164:167]\n\t" "v_permlane32_swap_b32 v29, v31\n\t" "v_mfma_f32_16x16x32_bf16 v[168:171], a[128:131], a[72:75], v[168:171]\n\t"
    "ds_read_b64_tr_b16 a[148:149], %[vr2] offset:50208\n\t" "v_add_f32 v24, v24, v26\n\t" "v_mfma_f32_16x16x32_bf16 v[168:171], a[132:135], a[88:91], v[168:171]\n\t"
    "ds_read_b64_tr_b16 a[150:151], %[vr2] offset:53792\n\t" "v_add_f32_e64 v25, v25, v27\n\t" "v_mfma_f32_16x16x32_bf16 v[172:175], a[128:131], a[76:79], v[172:175]\n\t"
    "global_load_dwordx4 a[28:31], %[va], s[76:77] offset:3072 sc1\n\t" "v_add_f32_e64 v28, v28, v30\n\t" "v_mfma_f32_16x16x32_bf16 v[172:175], a[132:135], a[92:95], v[172:175]\n\t"
    "s_add_u32 s76, s76, 16384\n\t" "s_addc_u32 s77, s77, 0\n\t" "v_add_f32_e64 v29, v29, v31\n\t" "s_waitcnt lgkmcnt(4)\n\t" "v_mfma_f32_16x16x32_bf16 v[176:179], a[136:139], a[64:67], v[176:179]\n\t"
    "ds_read_b64_tr_b16 a[152:153], %[vr4] offset:35840\n\t" "v_mul_f32 v24, 0xba800000, v24\n\t" "v_mfma_f32_16x16x32_bf16 v[176:179], a[140:143], a[80:83], v[176:179]\n\t"
    "ds_read_b64_tr_b16 a[154:155], %[vr4] offset:39424\n\t" "v_mul_f32 v25, 0x3a800000, v25\n\t" "v_mfma_f32_16x16x32_bf16 v[180:183], a[136:139], a[68:71], v[180:183]\n\t"
    "v_mul_f32 v28, 0xba800000, v28\n\t" "v_mfma_f32_16x16x32_bf16 v[180:183], a[140:143], a[84:87], v[180:183]\n\t" "v_mul_f32 v29, 0x3a800000, v29\n\t"
    "v_mfma_f32_16x16x32_bf16 v[184:187], a[136:139], a[72:75], v[184:187]\n\t" "ds_read_b64_tr_b16 a[156:157], %[vr4] offset:50176\n\t" "v_fma_f32 v25, -v24, v24, v25\n\t"
    "v_mfma_f32_16x16x32_bf16 v[184:187], a[140:143], a[88:91], v[184:187]\n\t" "ds_read_b64_tr_b16 a[158:159], %[vr4] offset:53760\n\t" "v_and_b32 v26, 0xffff0000, v24\n\t"
    "s_add_u32 m0, %[sl], 2048\n\t" "v_mfma_f32_16x16x32_bf16 v[188:191], a[136:139], a[76:79], v[188:191]\n\t" "global_load_lds_dwordx4 %[vb2], s[78:79]\n\t" "v_fma_f32 v29, -v28, v28, v29\n\t"
    "v_mfma_f32_16x16x32_bf16 v[188:191], a[140:143], a[92:95], v[188:191]\n\t" "v_and_b32 v30, 0xffff0000, v28\n\t" "s_waitcnt lgkmcnt(4)\n\t"
    "v_mfma_f32_16x16x32_bf16 v[192:195], a[144:147], a[64:67], v[192:195]\n\t" "ds_read_b64_tr_b16 a[128:129], %[vr4] offset:35872\n\t" "v_max_f32 v25, 0, v25\n\t"
    "v_mfma_f32_16x16x32_bf16 v[192:195], a[148:151], a[80:83], v[192:195]\n\t" "ds_read_b64_tr_b16 a[130:131], %[vr4] offset:39456\n\t" "v_sub_f32_e64 v24, v24, v26\n\t"
    "v_mfma_f32_16x16x32_bf16 v[196:199], a[144:147], a[68:71], v[196:199]\n\t" "global_load_dwordx4 a[32:35], %[va], s[76:77] offset:0 sc1\n\t" "v_max_f32_e64 v29, 0, v29\n\t"
    "v_mfma_f32_16x16x32_bf16 v[196:199], a[148:151], a[84:87], v[196:199]\n\t" "v_sub_f32_e64 v28, v28, v30\n\t" "v_mfma_f32_16x16x32_bf16 v[200:203], a[144:147], a[72:75], v[200:203]\n\t"
    "ds_read_b64_tr_b16 a[132:133], %[vr4] offset:50208\n\t" "v_add_f32 v25, 0x3727c5ac, v25\n\t" "v_mfma_f32_16x16x32_bf16 v[200:203], a[148:151], a[88:91], v[200:203]\n\t"
    "ds_read_b64_tr_b16 a[134:135], %[vr4] offset:53792\n\t" "v_cvt_pk_bf16_f32 v26, v26, v26\n\t" "s_add_u32 m0, %[sl], 3072\n\t"
    "v_mfma_f32_16x16x32_bf16 v[204:207], a[144:147], a[76:79], v[204:207]\n\t" "s_mov_b64 exec, s[82:83]\n\t" "global_load_lds_dwordx4 %[vb3], s[78:79]\n\t" "s_mov_b64 exec, -1\n\t"
    "v_add_f32 v29, 0x3727c5ac, v29\n\t" "v_mfma_f32_16x16x32_bf16 v[204:207], a[148:151], a[92:95], v[204:207]\n\t" "s_add_u32 s78, s78, 229376\n\t" "s_addc_u32 s79, s79, 0\n\t"
    "v_cvt_pk_bf16_f32 v30, v30, v30\n\t" "s_waitcnt lgkmcnt(4)\n\t" "v_mfma_f32_16x16x32_bf16 v[208:211], a[152:155], a[64:67], v[208:211]\n\t" "ds_read_b64_tr_b16 a[136:137], %[vr6] offset:35840\n\t"
    "v_rsq_f32_e64 v27, v25\n\t" "v_mfma_f32_16x16x32_bf16 v[208:211], a[156:159], a[80:83], v[208:211]\n\t" "ds_read_b64_tr_b16 a[138:139], %[vr6] offset:39424\n\t" "v_cvt_pk_bf16_f32 v24, v24, v24\n\t"
    "v_mfma_f32_16x16x32_bf16 v[212:215], a[152:155], a[68:71], v[212:215]\n\t" "v_rsq_f32_e64 v31, v29\n\t" "v_mfma_f32_16x16x32_bf16 v[212:215], a[156:159], a[84:87], v[212:215]\n\t"
    "v_cvt_pk_bf16_f32 v28, v28, v28\n\t" "v_mfma_f32_16x16x32_bf16 v[216:219], a[152:155], a[72:75], v[216:219]\n\t" "ds_read_b64_tr_b16 a[140:141], %[vr6] offset:50176\n\t"
    "v_mul_f32_e64 v20, v25, v27\n\t" "v_mfma_f32_16x16x32_bf16 v[216:219], a[156:159], a[88:91], v[216:219]\n\t" "ds_read_b64_tr_b16 a[142:143], %[vr6] offset:53760\n\t"
    "v_mul_f32 v16, 0xbfb8aa3b, v27\n\t" "v_mfma_f32_16x16x32_bf16 v[220:223], a[152:155], a[76:79], v[220:223]\n\t" "global_load_dwordx4 a[36:39], %[va], s[76:77] offset:1024 sc1\n\t"
    "v_mul_f32_e64 v21, v29, v31\n\t" "v_mfma_f32_16x16x32_bf16 v[220:223], a[156:159], a[92:95], v[220:223]\n\t" "v_mul_f32 v17, 0xbfb8aa3b, v31\n\t" "s_waitcnt lgkmcnt(4)\n\t"
    "v_mfma_f32_16x16x32_bf16 v[224:227], a[128:131], a[64:67], v[224:227]\n\t" "s_waitcnt vmcnt(26)\n\t" "s_barrier\n\t" "ds_read_b64_tr_b16 a[144:145], %[vr1] offset:0\n\t"
    "v_and_b32 v27, 0xffff0000, v20\n\t" "v_mfma_f32_16x16x32_bf16 v[224:227], a[132:135], a[80:83], v[224:227]\n\t" "ds_read_b64_tr_b16 a[146:147], %[vr1] offset:3584\n\t" "s_nop 0\n\t"
    "v_mfma_f32_16x16x32_bf16 v[228:231], a[128:131], a[68:71], v[228:231]\n\t" "global_load_dwordx4 a[40:43], %[va], s[76:77] offset:2048 sc1\n\t" "v_and_b32 v31, 0xffff0000, v21\n\t"
    "v_mfma_f32_16x16x32_bf16 v[228:231], a[132:135], a[84:87], v[228:231]\n\t" "v_sub_f32_e64 v25, v20, v27\n\t" "v_mfma_f32_16x16x32_bf16 v[232:235], a[128:131], a[72:75], v[232:235]\n\t"
    "ds_read_b64_tr_b16 a[148:149], %[vr1] offset:14336\n\t" "v_cvt_pk_bf16_f32 v27, v27, v27\n\t" "v_mfma_f32_16x16x32_bf16 v[232:235], a[132:135], a[88:91], v[232:235]\n\t"
    "ds_read_b64_tr_b16 a[150:151], %[vr1] offset:17920\n\t" "v_sub_f32_e64 v29, v21, v31\n\t" "s_add_u32 m0, %[sl], 14336\n\t" "v_mfma_f32_16x16x32_bf16 v[236:239], a[128:131], a[76:79], v[236:239]\n\t"
    "global_load_lds_dwordx4 %[vb0], s[78:79]\n\t" "v_cvt_pk_bf16_f32 v31, v31, v31\n\t" "v_mfma_f32_16x16x32_bf16 v[236:239], a[132:135], a[92:95], v[236:239]\n\t" "v_cvt_pk_bf16_f32 v25, v25, v25\n\t"
    "s_waitcnt lgkmcnt(4)\n\t" "v_mfma_f32_16x16x32_bf16 v[240:243], a[136:139], a[64:67], v[240:243]\n\t" "ds_read_b64_tr_b16 a[152:153], %[vr1] offset:32\n\t" "s_nop 0\n\t"
    "v_mfma_f32_16x16x32_bf16 v[240:243], a[140:143], a[80:83], v[240:243]\n\t" "ds_read_b64_tr_b16 a[154:155], %[vr1] offset:3616\n\t" "v_cvt_pk_bf16_f32 v29, v29, v29\n\t"
    "v_mfma_f32_16x16x32_bf16 v[244:247], a[136:139], a[68:71], v[244:247]\n\t" "s_mov_b64 exec, 0xffff\n\t" "v_accvgpr_write_b32 a200, v26\n\t" "v_accvgpr_write_b32 a201, v24\n\t"
    "v_accvgpr_write_b32 a202, v27\n\t" "v_accvgpr_write_b32 a203, v25\n\t" "v_accvgpr_write_b32 a204, v30\n\t" "v_accvgpr_write_b32 a205, v28\n\t" "v_accvgpr_write_b32 a206, v31\n\t"
    "v_accvgpr_write_b32 a207, v29\n\t" "s_mov_b64 exec, -1\n\t" "v_mfma_f32_16x16x32_bf16 v[244:247], a[140:143], a[84:87], v[244:247]\n\t" "v_accvgpr_read_b32 v24, a220\n\t"
    "v_mfma_f32_16x16x32_bf16 v[248:251], a[136:139], a[72:75], v[248:251]\n\t" "ds_read_b64_tr_b16 a[156:157], %[vr1] offset:14368\n\t" "v_accvgpr_read_b32 v25, a221\n\t"
    "v_mfma_f32_16x16x32_bf16 v[248:251], a[140:143], a[88:91], v[248:251]\n\t" "ds_read_b64_tr_b16 a[158:159], %[vr1] offset:17952\n\t" "v_accvgpr_read_b32 v28, a222\n\t"
    "v_mfma_f32_16x16x32_bf16 v[252:255], a[136:139], a[76:79], v[252:255]\n\t" "v_accvgpr_read_b32 v29, a223\n\t" "v_mfma_f32_16x16x32_bf16 v[252:255], a[140:143], a[92:95], v[252:255]\n\t"
    "v_accvgpr_read_b32 v26, a228\n\t" "s_waitcnt lgkmcnt(4) vmcnt(21)\n\t" "v_mfma_f32_16x16x32_bf16 v[32:35], a[144:147], a[96:99], v[32:35]\n\t" "s_barrier\n\t"
    "ds_read_b64_tr_b16 a[128:129], %[vr3] offset:0\n\t" "v_accvgpr_read_b32 v27, a229\n\t" "v_mfma_f32_16x16x32_bf16 v[32:35], a[148:151], a[112:115], v[32:35]\n\t"
    "ds_read_b64_tr_b16 a[130:131], %[vr3] offset:3584\n\t" "v_accvgpr_read_b32 v30, a230\n\t" "v_mfma_f32_16x16x32_bf16 v[36:39], a[144:147], a[100:103], v[36:39]\n\t"
    "global_load_dwordx4 a[44:47], %[va], s[76:77] offset:3072 sc1\n\t" "s_mov_b32 m0, %[srl]\n\t" "s_nop 0\n\t" "global_load_lds_dwordx4 %[va], %[sr] offset:0 sc1\n\t" "v_accvgpr_read_b32 v31, a231\n\t"
    "v_mfma_f32_16x16x32_bf16 v[36:39], a[148:151], a[116:119], v[36:39]\n\t" "s_add_u32 s76, s76, 16384\n\t" "s_addc_u32 s77, s77, 0\n\t" "v_add_f32_e64 v24, v24, v26\n\t"
    "v_mfma_f32_16x16x32_bf16 v[40:43], a[144:147], a[104:107], v[40:43]\n\t" "ds_read_b64_tr_b16 a[132:133], %[vr3] offset:14336\n\t" "v_add_f32_e64 v25, v25, v27\n\t"
    "v_mfma_f32_16x16x32_bf16 v[40:43], a[148:151], a[120:123], v[40:43]\n\t" "ds_read_b64_tr_b16 a[134:135], %[vr3] offset:17920\n\t" "v_add_f32_e64 v28, v28, v30\n\t" "s_add_u32 m0, %[sl], 15360\n\t"
    "v_mfma_f32_16x16x32_bf16 v[44:47], a[144:147], a[108:111], v[44:47]\n\t" "global_load_lds_dwordx4 %[vb1], s[78:79]\n\t" "s_mov_b32 m0, %[srl]\n\t" "s_nop 0\n\t"
    "global_load_lds_dwordx4 %[va], %[sr] offset:1024 sc1\n\t" "v_add_f32_e64 v29, v29, v31\n\t" "v_mfma_f32_16x16x32_bf16 v[44:47], a[148:151], a[124:127], v[44:47]\n\t"
    "v_accvgpr_read_b32 v26, a236\n\t" "s_waitcnt lgkmcnt(4)\n\t" "v_mfma_f32_16x16x32_bf16 v[48:51], a[152:155], a[96:99], v[48:51]\n\t" "ds_read_b64_tr_b16 a[136:137], %[vr3] offset:32\n\t"
    "v_accvgpr_read_b32 v27, a237\n\t" "v_mfma_f32_16x16x32_bf16 v[48:51], a[156:159], a[112:115], v[48:51]\n\t" "ds_read_b64_tr_b16 a[138:139], %[vr3] offset:3616\n\t" "v_accvgpr_read_b32 v30, a238\n\t"
    "v_mfma_f32_16x16x32_bf16 v[52:55], a[152:155], a[100:103], v[52:55]\n\t" "global_load_dwordx4 a[48:51], %[va], s[76:77] offset:0 sc1\n\t" "s_mov_b32 m0, %[srl]\n\t" "s_nop 0\n\t"
    "global_load_lds_dwordx4 %[va], %[sr] offset:2048 sc1\n\t" "v_accvgpr_read_b32 v31, a239\n\t" "v_mfma_f32_16x16x32_bf16 v[52:55], a[156:159], a[116:119], v[52:55]\n\t" "v_add_f32 v24, v24, v26\n\t"
    "v_mfma_f32_16x16x32_bf16 v[56:59], a[152:155], a[104:107], v[56:59]\n\t" "ds_read_b64_tr_b16 a[140:141], %[vr3] offset:14368\n\t" "v_add_f32_e64 v25, v25, v27\n\t"
    "v_mfma_f32_16x16x32_bf16 v[56:59], a[156:159], a[120:123], v[56:59]\n\t" "ds_read_b64_tr_b16 a[142:143], %[vr3] offset:17952\n\t" "v_add_f32_e64 v28, v28, v30\n\t" "s_add_u32 m0, %[sl], 16384\n\t"
    "v_mfma_f32_16x16x32_bf16 v[60:63], a[152:155], a[108:111], v[60:63]\n\t" "global_load_lds_dwordx4 %[vb2], s[78:79]\n\t" "s_mov_b32 m0, %[srl]\n\t" "s_nop 0\n\t"
    "global_load_lds_dwordx4 %[va], %[sr] offset:3072 sc1\n\t" "v_add_f32_e64 v29, v29, v31\n\t" "v_mfma_f32_16x16x32_bf16 v[60:63], a[156:159], a[124:127], v[60:63]\n\t"
    "v_accvgpr_read_b32 v26, a244\n\t" "s_waitcnt lgkmcnt(4)\n\t" "v_mfma_f32_16x16x32_bf16 v[64:67], a[128:131], a[96:99], v[64:67]\n\t" "ds_read_b64_tr_b16 a[144:145], %[vr5] offset:0\n\t"
    "v_accvgpr_read_b32 v27, a245\n\t" "v_mfma_f32_16x16x32_bf16 v[64:67], a[132:135], a[112:115], v[64:67]\n\t" "ds_read_b64_tr_b16 a[146:147], %[vr5] offset:3584\n\t" "v_accvgpr_read_b32 v30, a246\n\t"
    "v_mfma_f32_16x16x32_bf16 v[68:71], a[128:131], a[100:103], v[68:71]\n\t" "v_accvgpr_read_b32 v31, a247\n\t" "v_mfma_f32_16x16x32_bf16 v[68:71], a[132:135], a[116:119], v[68:71]\n\t"
    "v_add_f32 v24, v24, v26\n\t" "v_mfma_f32_16x16x32_bf16 v[72:75], a[128:131], a[104:107], v[72:75]\n\t" "ds_read_b64_tr_b16 a[148:149], %[vr5] offset:14336\n\t" "v_add_f32_e64 v25, v25, v27\n\t"
    "v_mfma_f32_16x16x32_bf16 v[72:75], a[132:135], a[120:123], v[72:75]\n\t" "ds_read_b64_tr_b16 a[150:151], %[vr5] offset:17920\n\t" "v_add_f32_e64 v28, v28, v30\n\t"
    "v_mfma_f32_16x16x32_bf16 v[76:79], a[128:131], a[108:111], v[76:79]\n\t" "global_load_dwordx4 a[52:55], %[va], s[76:77] offset:1024 sc1\n\t" "v_add_f32_e64 v29, v29, v31\n\t"
    "v_mfma_f32_16x16x32_bf16 v[76:79], a[132:135], a[124:127], v[76:79]\n\t" "v_mov_b32 v26, v24\n\t" "s_waitcnt lgkmcnt(4)\n\t" "v_mfma_f32_16x16x32_bf16 v[80:83], a[136:139], a[96:99], v[80:83]\n\t"
    "ds_read_b64_tr_b16 a[152:153], %[vr5] offset:32\n\t" "v_mov_b32_e64 v27, v25\n\t" "v_mfma_f32_16x16x32_bf16 v[80:83], a[140:143], a[112:115], v[80:83]\n\t"
    "ds_read_b64_tr_b16 a[154:155], %[vr5] offset:3616\n\t" "v_mov_b32_e64 v30, v28\n\t" "s_add_u32 m0, %[sl], 17408\n\t" "v_mfma_f32_16x16x32_bf16 v[84:87], a[136:139], a[100:103], v[84:87]\n\t"
    "s_mov_b64 exec, s[82:83]\n\t" "global_load_lds_dwordx4 %[vb3], s[78:79]\n\t" "s_mov_b64 exec, -1\n\t" "v_mov_b32_e64 v31, v29\n\t"
    "v_mfma_f32_16x16x32_bf16 v[84:87], a[140:143], a[116:119], v[84:87]\n\t" "s_add_u32 s78, s78, 229376\n\t" "s_addc_u32 s79, s79, 0\n\t" "s_nop 0\n\t"
    "v_mfma_f32_16x16x32_bf16 v[88:91], a[136:139], a[104:107], v[88:91]\n\t" "ds_read_b64_tr_b16 a[156:157], %[vr5] offset:14368\n\t" "s_nop 0\n\t"
    "v_mfma_f32_16x16x32_bf16 v[88:91], a[140:143], a[120:123], v[88:91]\n\t" "ds_read_b64_tr_b16 a[158:159], %[vr5] offset:17952\n\t" "v_permlane16_swap_b32 v24, v26\n\t"
    "v_mfma_f32_16x16x32_bf16 v[92:95], a[136:139], a[108:111], v[92:95]\n\t" "global_load_dwordx4 a[56:59], %[va], s[76:77] offset:2048 sc1\n\t" "v_permlane16_swap_b32 v25, v27\n\t"
    "v_mfma_f32_16x16x32_bf16 v[92:95], a[140:143], a[124:127], v[92:95]\n\t" "v_permlane16_swap_b32 v28, v30\n\t" "s_waitcnt lgkmcnt(4)\n\t"
    "v_mfma_f32_16x16x32_bf16 v[96:99], a[144:147], a[96:99], v[96:99]\n\t" "ds_read_b64_tr_b16 a[128:129], %[vr7] offset:0\n\t" "v_permlane16_swap_b32 v29, v31\n\t"
    "v_mfma_f32_16x16x32_bf16 v[96:99], a[148:151], a[112:115], v[96:99]\n\t" "ds_read_b64_tr_b16 a[130:131], %[vr7] offset:3584\n\t" "v_add_f32_e64 v24, v24, v26\n\t"
    "v_mfma_f32_16x16x32_bf16 v[100:103], a[144:147], a[100:103], v[100:103]\n\t" "v_add_f32_e64 v25, v25, v27\n\t" "v_mfma_f32_16x16x32_bf16 v[100:103], a[148:151], a[116:119], v[100:103]\n\t"
    "v_add_f32_e64 v28, v28, v30\n\t" "v_mfma_f32_16x16x32_bf16 v[104:107], a[144:147], a[104:107], v[104:107]\n\t" "ds_read_b64_tr_b16 a[132:133], %[vr7] offset:14336\n\t"
    "v_add_f32_e64 v29, v29, v31\n\t" "v_mfma_f32_16x16x32_bf16 v[104:107], a[148:151], a[120:123], v[104:107]\n\t" "ds_read_b64_tr_b16 a[134:135], %[vr7] offset:17920\n\t" "v_mov_b32_e64 v26, v24\n\t"
    "s_add_u32 m0, %[sl], 28672\n\t" "v_mfma_f32_16x16x32_bf16 v[108:111], a[144:147], a[108:111], v[108:111]\n\t" "global_load_lds_dwordx4 %[vb0], s[78:79]\n\t" "v_mov_b32_e64 v27, v25\n\t"
    "v_mfma_f32_16x16x32_bf16 v[108:111], a[148:151], a[124:127], v[108:111]\n\t" "v_mov_b32 v30, v28\n\t" "s_waitcnt lgkmcnt(4)\n\t"
    "v_mfma_f32_16x16x32_bf16 v[112:115], a[152:155], a[96:99], v[112:115]\n\t" "ds_read_b64_tr_b16 a[136:137], %[vr1] offset:7168\n\t" "v_mov_b32_e64 v31, v29\n\t"
    "v_mfma_f32_16x16x32_bf16 v[112:115], a[156:159], a[112:115], v[112:115]\n\t" "ds_read_b64_tr_b16 a[138:139], %[vr1] offset:10752\n\t" "s_nop 0\n\t"
    "v_mfma_f32_16x16x32_bf16 v[116:119], a[152:155], a[100:103], v[116:119]\n\t" "global_load_dwordx4 a[60:63], %[va], s[76:77] offset:3072 sc1\n\t" "s_nop 0\n\t"
    "v_mfma_f32_16x16x32_bf16 v[116:119], a[156:159], a[116:119], v[116:119]\n\t" "s_add_u32 s76, s76, 16384\n\t" "s_addc_u32 s77, s77, 0\n\t" "v_permlane32_swap_b32 v24, v26\n\t"
    "v_mfma_f32_16x16x32_bf16 v[120:123], a[152:155], a[104:107], v[120:123]\n\t" "ds_read_b64_tr_b16 a[140:141], %[vr1] offset:21504\n\t" "v_permlane32_swap_b32 v25, v27\n\t"
    "v_mfma_f32_16x16x32_bf16 v[120:123], a[156:159], a[120:123], v[120:123]\n\t" "ds_read_b64_tr_b16 a[142:143], %[vr1] offset:25088\n\t" "v_permlane32_swap_b32 v28, v30\n\t"
    "s_add_u32 m0, %[sl], 29696\n\t" "v_mfma_f32_16x16x32_bf16 v[124:127], a[152:155], a[108:111], v[124:127]\n\t" "global_load_lds_dwordx4 %[vb1], s[78:79]\n\t" "v_permlane32_swap_b32 v29, v31\n\t"
    "v_mfma_f32_16x16x32_bf16 v[124:127], a[156:159], a[124:127], v[124:127]\n\t" "v_add_f32_e64 v24, v24, v26\n\t" "s_waitcnt lgkmcnt(4)\n\t"
    "v_mfma_f32_16x16x32_bf16 v[128:131], a[128:131], a[96:99], v[128:131]\n\t" "ds_read_b64_tr_b16 a[144:145], %[vr1] offset:7200\n\t" "v_add_f32_e64 v25, v25, v27\n\t"
    "v_mfma_f32_16x16x32_bf16 v[128:131], a[132:135], a[112:115], v[128:131]\n\t" "ds_read_b64_tr_b16 a[146:147], %[vr1] offset:10784\n\t" "v_add_f32_e64 v28, v28, v30\n\t"
    "v_mfma_f32_16x16x32_bf16 v[132:135], a[128:131], a[100:103], v[132:135]\n\t" "v_add_f32_e64 v29, v29, v31\n\t" "v_mfma_f32_16x16x32_bf16 v[132:135], a[132:135], a[116:119], v[132:135]\n\t"
    "v_mul_f32 v24, 0xba800000, v24\n\t" "v_mfma_f32_16x16x32_bf16 v[136:139], a[128:131], a[104:107], v[136:139]\n\t" "ds_read_b64_tr_b16 a[148:149], %[vr1] offset:21536\n\t"
    "v_mul_f32 v25, 0x3a800000, v25\n\t" "v_mfma_f32_16x16x32_bf16 v[136:139], a[132:135], a[120:123], v[136:139]\n\t" "ds_read_b64_tr_b16 a[150:151], %[vr1] offset:25120\n\t"
    "v_mul_f32 v28, 0xba800000, v28\n\t" "v_mfma_f32_16x16x32_bf16 v[140:143], a[128:131], a[108:111], v[140:143]\n\t" "v_mul_f32 v29, 0x3a800000, v29\n\t"
    "v_mfma_f32_16x16x32_bf16 v[140:143], a[132:135], a[124:127], v[140:143]\n\t" "v_fma_f32 v25, -v24, v24, v25\n\t" "s_waitcnt lgkmcnt(4)\n\t"
    "v_mfma_f32_16x16x32_bf16 v[144:147], a[136:139], a[96:99], v[144:147]\n\t" "ds_read_b64_tr_b16 a[152:153], %[vr3] offset:7168\n\t" "v_and_b32 v26, 0xffff0000, v24\n\t"
    "v_mfma_f32_16x16x32_bf16 v[144:147], a[140:143], a[112:115], v[144:147]\n\t" "ds_read_b64_tr_b16 a[154:155], %[vr3] offset:10752\n\t" "v_fma_f32 v29, -v28, v28, v29\n\t"
    "v_mfma_f32_16x16x32_bf16 v[148:151], a[136:139], a[100:103], v[148:151]\n\t" "global_load_dwordx4 a[64:67], %[va], s[76:77] offset:0 sc1\n\t" "v_and_b32 v30, 0xffff0000, v28\n\t"
    "v_mfma_f32_16x16x32_bf16 v[148:151], a[140:143], a[116:119], v[148:151]\n\t" "v_max_f32 v25, 0, v25\n\t" "v_mfma_f32_16x16x32_bf16 v[152:155], a[136:139], a[104:107], v[152:155]\n\t"
    "ds_read_b64_tr_b16 a[156:157], %[vr3] offset:21504\n\t" "v_sub_f32_e64 v24, v24, v26\n\t" "v_mfma_f32_16x16x32_bf16 v[152:155], a[140:143], a[120:123], v[152:155]\n\t"
    "ds_read_b64_tr_b16 a[158:159], %[vr3] offset:25088\n\t" "v_max_f32_e64 v29, 0, v29\n\t" "s_add_u32 m0, %[sl], 30720\n\t" "v_mfma_f32_16x16x32_bf16 v[156:159], a[136:139], a[108:111], v[156:159]\n\t"
    "global_load_lds_dwordx4 %[vb2], s[78:79]\n\t" "v_sub_f32_e64 v28, v28, v30\n\t" "v_mfma_f32_16x16x32_bf16 v[156:159], a[140:143], a[124:127], v[156:159]\n\t" "v_add_f32 v25, 0x3727c5ac, v25\n\t"
    "s_waitcnt lgkmcnt(4)\n\t" "v_mfma_f32_16x16x32_bf16 v[160:163], a[144:147], a[96:99], v[160:163]\n\t" "ds_read_b64_tr_b16 a[128:129], %[vr3] offset:7200\n\t" "v_cvt_pk_bf16_f32 v26, v26, v26\n\t"
    "v_mfma_f32_16x16x32_bf16 v[160:163], a[148:151], a[112:115], v[160:163]\n\t" "ds_read_b64_tr_b16 a[130:131], %[vr3] offset:10784\n\t" "v_add_f32 v29, 0x3727c5ac, v29\n\t"
    "v_mfma_f32_16x16x32_bf16 v[164:167], a[144:147], a[100:103], v[164:167]\n\t" "global_load_dwordx4 a[68:71], %[va], s[76:77] offset:1024 sc1\n\t" "v_cvt_pk_bf16_f32 v30, v30, v30\n\t"
    "v_mfma_f32_16x16x32_bf16 v[164:167], a[148:151], a[116:119], v[164:167]\n\t" "v_rsq_f32 v27, v25\n\t" "v_mfma_f32_16x16x32_bf16 v[168:171], a[144:147], a[104:107], v[168:171]\n\t"
    "ds_read_b64_tr_b16 a[132:133], %[vr3] offset:21536\n\t" "v_cvt_pk_bf16_f32 v24, v24, v24\n\t" "v_mfma_f32_16x16x32_bf16 v[168:171], a[148:151], a[120:123], v[168:171]\n\t"
    "ds_read_b64_tr_b16 a[134:135], %[vr3] offset:25120\n\t" "v_rsq_f32_e64 v31, v29\n\t" "s_add_u32 m0, %[sl], 31744\n\t" "v_mfma_f32_16x16x32_bf16 v[172:175], a[144:147], a[108:111], v[172:175]\n\t"
    "s_mov_b64 exec, s[82:83]\n\t" "global_load_lds_dwordx4 %[vb3], s[78:79]\n\t" "s_mov_b64 exec, -1\n\t" "v_cvt_pk_bf16_f32 v28, v28, v28\n\t"
    "v_mfma_f32_16x16x32_bf16 v[172:175], a[148:151], a[124:127], v[172:175]\n\t" "s_add_u32 s78, s78, 229376\n\t" "s_addc_u32 s79, s79, 0\n\t" "v_mul_f32_e64 v22, v25, v27\n\t" "s_waitcnt lgkmcnt(4)\n\t"
    "v_mfma_f32_16x16x32_bf16 v[176:179], a[152:155], a[96:99], v[176:179]\n\t" "ds_read_b64_tr_b16 a[136:137], %[vr5] offset:7168\n\t" "v_mul_f32 v18, 0xbfb8aa3b, v27\n\t"
    "v_mfma_f32_16x16x32_bf16 v[176:179], a[156:159], a[112:115], v[176:179]\n\t" "ds_read_b64_tr_b16 a[138:139], %[vr5] offset:10752\n\t" "v_mul_f32_e64 v23, v29, v31\n\t"
    "v_mfma_f32_16x16x32_bf16 v[180:183], a[152:155], a[100:103], v[180:183]\n\t" "v_mul_f32 v19, 0xbfb8aa3b, v31\n\t" "v_mfma_f32_16x16x32_bf16 v[180:183], a[156:159], a[116:119], v[180:183]\n\t"
    "v_and_b32 v27, 0xffff0000, v22\n\t" "v_mfma_f32_16x16x32_bf16 v[184:187], a[152:155], a[104:107], v[184:187]\n\t" "ds_read_b64_tr_b16 a[140:141], %[vr5] offset:21504\n\t" "s_nop 0\n\t"
    "v_mfma_f32_16x16x32_bf16 v[184:187], a[156:159], a[120:123], v[184:187]\n\t" "ds_read_b64_tr_b16 a[142:143], %[vr5] offset:25088\n\t" "v_and_b32 v31, 0xffff0000, v23\n\t"
    "v_mfma_f32_16x16x32_bf16 v[188:191], a[152:155], a[108:111], v[188:191]\n\t" "global_load_dwordx4 a[72:75], %[va], s[76:77] offset:2048 sc1\n\t" "v_sub_f32 v25, v22, v27\n\t"
    "v_mfma_f32_16x16x32_bf16 v[188:191], a[156:159], a[124:127], v[188:191]\n\t" "v_cvt_pk_bf16_f32 v27, v27, v27\n\t" "s_waitcnt lgkmcnt(4)\n\t"
    "v_mfma_f32_16x16x32_bf16 v[192:195], a[128:131], a[96:99], v[192:195]\n\t" "ds_read_b64_tr_b16 a[144:145], %[vr5] offset:7200\n\t" "v_sub_f32 v29, v23, v31\n\t"
    "v_mfma_f32_16x16x32_bf16 v[192:195], a[132:135], a[112:115], v[192:195]\n\t" "ds_read_b64_tr_b16 a[146:147], %[vr5] offset:10784\n\t" "v_cvt_pk_bf16_f32 v31, v31, v31\n\t"
    "s_add_u32 m0, %[sl], 43008\n\t" "v_mfma_f32_16x16x32_bf16 v[196:199], a[128:131], a[100:103], v[196:199]\n\t" "global_load_lds_dwordx4 %[vb0], s[78:79]\n\t" "v_cvt_pk_bf16_f32 v25, v25, v25\n\t"
    "v_mfma_f32_16x16x32_bf16 v[196:199], a[132:135], a[116:119], v[196:199]\n\t" "s_nop 0\n\t" "v_mfma_f32_16x16x32_bf16 v[200:203], a[128:131], a[104:107], v[200:203]\n\t"
    "ds_read_b64_tr_b16 a[148:149], %[vr5] offset:21536\n\t" "v_cvt_pk_bf16_f32 v29, v29, v29\n\t" "v_mfma_f32_16x16x32_bf16 v[200:203], a[132:135], a[120:123], v[200:203]\n\t"
    "ds_read_b64_tr_b16 a[150:151], %[vr5] offset:25120\n\t" "s_mov_b64 exec, 0xffff\n\t" "v_accvgpr_write_b32 a208, v26\n\t" "v_accvgpr_write_b32 a209, v24\n\t" "v_accvgpr_write_b32 a210, v27\n\t"
    "v_accvgpr_write_b32 a211, v25\n\t" "v_accvgpr_write_b32 a212, v30\n\t" "v_accvgpr_write_b32 a213, v28\n\t" "v_accvgpr_write_b32 a214, v31\n\t" "v_accvgpr_write_b32 a215, v29\n\t"
    "s_mov_b64 exec, -1\n\t" "v_mfma_f32_16x16x32_bf16 v[204:207], a[128:131], a[108:111], v[204:207]\n\t" "global_load_dwordx4 a[76:79], %[va], s[76:77] offset:3072 sc1\n\t"
    "v_mfma_f32_16x16x32_bf16 v[204:207], a[132:135], a[124:127], v[204:207]\n\t" "s_add_u32 s76, s76, 16384\n\t" "s_addc_u32 s77, s77, 0\n\t" "s_waitcnt lgkmcnt(4)\n\t"
    "v_mfma_f32_16x16x32_bf16 v[208:211], a[136:139], a[96:99], v[208:211]\n\t" "ds_read_b64_tr_b16 a[152:153], %[vr7] offset:7168\n\t"
    "v_mfma_f32_16x16x32_bf16 v[208:211], a[140:143], a[112:115], v[208:211]\n\t" "ds_read_b64_tr_b16 a[154:155], %[vr7] offset:10752\n\t"
    "v_mfma_f32_16x16x32_bf16 v[212:215], a[136:139], a[100:103], v[212:215]\n\t" "v_mfma_f32_16x16x32_bf16 v[212:215], a[140:143], a[116:119], v[212:215]\n\t"
    "v_mfma_f32_16x16x32_bf16 v[216:219], a[136:139], a[104:107], v[216:219]\n\t" "ds_read_b64_tr_b16 a[156:157], %[vr7] offset:21504\n\t"
    "v_mfma_f32_16x16x32_bf16 v[216:219], a[140:143], a[120:123], v[216:219]\n\t" "ds_read_b64_tr_b16 a[158:159], %[vr7] offset:25088\n\t" "s_add_u32 m0, %[sl], 44032\n\t"
    "v_mfma_f32_16x16x32_bf16 v[220:223], a[136:139], a[108:111], v[220:223]\n\t" "global_load_lds_dwordx4 %[vb1], s[78:79]\n\t"
    "v_mfma_f32_16x16x32_bf16 v[220:223], a[140:143], a[124:127], v[220:223]\n\t" "s_waitcnt lgkmcnt(4)\n\t" "v_mfma_f32_16x16x32_bf16 v[224:227], a[144:147], a[96:99], v[224:227]\n\t"
    "s_waitcnt vmcnt(14)\n\t" "s_barrier\n\t" "ds_read_b64_tr_b16 a[128:129], %[vr1] offset:28672\n\t" "v_mfma_f32_16x16x32_bf16 v[224:227], a[148:151], a[112:115], v[224:227]\n\t"
    "ds_read_b64_tr_b16 a[130:131], %[vr1] offset:32256\n\t" "v_mfma_f32_16x16x32_bf16 v[228:231], a[144:147], a[100:103], v[228:231]\n\t" "global_load_dwordx4 a[80:83], %[va], s[76:77] offset:0 sc1\n\t"
    "v_mfma_f32_16x16x32_bf16 v[228:231], a[148:151], a[116:119], v[228:231]\n\t" "v_mfma_f32_16x16x32_bf16 v[232:235], a[144:147], a[104:107], v[232:235]\n\t"
    "ds_read_b64_tr_b16 a[132:133], %[vr1] offset:43008\n\t" "v_mfma_f32_16x16x32_bf16 v[232:235], a[148:151], a[120:123], v[232:235]\n\t" "ds_read_b64_tr_b16 a[134:135], %[vr1] offset:46592\n\t"
    "s_add_u32 m0, %[sl], 45056\n\t" "v_mfma_f32_16x16x32_bf16 v[236:239], a[144:147], a[108:111], v[236:239]\n\t" "global_load_lds_dwordx4 %[vb2], s[78:79]\n\t"
    "v_mfma_f32_16x16x32_bf16 v[236:239], a[148:151], a[124:127], v[236:239]\n\t" "s_waitcnt lgkmcnt(4)\n\t" "v_mfma_f32_16x16x32_bf16 v[240:243], a[152:155], a[96:99], v[240:243]\n\t"
    "ds_read_b64_tr_b16 a[136:137], %[vr1] offset:28704\n\t" "v_mfma_f32_16x16x32_bf16 v[240:243], a[156:159], a[112:115], v[240:243]\n\t" "ds_read_b64_tr_b16 a[138:139], %[vr1] offset:32288\n\t"
    "v_mfma_f32_16x16x32_bf16 v[244:247], a[152:155], a[100:103], v[244:247]\n\t" "global_load_dword a231, %[va], %[cd]\n\t" "v_mfma_f32_16x16x32_bf16 v[244:247], a[156:159], a[116:119], v[244:247]\n\t"
    "v_mfma_f32_16x16x32_bf16 v[248:251], a[152:155], a[104:107], v[248:251]\n\t" "ds_read_b64_tr_b16 a[140:141], %[vr1] offset:43040\n\t"
    "v_mfma_f32_16x16x32_bf16 v[248:251], a[156:159], a[120:123], v[248:251]\n\t" "ds_read_b64_tr_b16 a[142:143], %[vr1] offset:46624\n\t"
    "v_mfma_f32_16x16x32_bf16 v[252:255], a[152:155], a[108:111], v[252:255]\n\t" "v_mfma_f32_16x16x32_bf16 v[252:255], a[156:159], a[124:127], v[252:255]\n\t" "s_waitcnt lgkmcnt(4)\n\t"
    "v_mfma_f32_16x16x32_bf16 v[32:35], a[128:131], a[0:3], v[32:35]\n\t" "s_barrier\n\t" "ds_read_b64_tr_b16 a[144:145], %[vr3] offset:28672\n\t" "v_add_u32_e64 v31, %[sld], %[va]\n\t"
    "v_mfma_f32_16x16x32_bf16 v[32:35], a[132:135], a[16:19], v[32:35]\n\t" "ds_read_b64_tr_b16 a[146:147], %[vr3] offset:32256\n\t" "v_mfma_f32_16x16x32_bf16 v[36:39], a[128:131], a[4:7], v[36:39]\n\t"
    "global_load_dwordx4 a[84:87], %[va], s[76:77] offset:1024 sc1\n\t" "ds_read_b128 a[232:235], v31 offset:24576\n\t" "v_mfma_f32_16x16x32_bf16 v[36:39], a[132:135], a[20:23], v[36:39]\n\t"
    "ds_read_b128 a[236:239], v31 offset:28672\n\t" "ds_read_b128 a[240:243], v31 offset:32768\n\t" "v_mfma_f32_16x16x32_bf16 v[40:43], a[128:131], a[8:11], v[40:43]\n\t"
    "ds_read_b64_tr_b16 a[148:149], %[vr3] offset:43008\n\t" "ds_read_b128 a[244:247], v31 offset:36864\n\t" "v_mfma_f32_16x16x32_bf16 v[40:43], a[132:135], a[24:27], v[40:43]\n\t"
    "ds_read_b64_tr_b16 a[150:151], %[vr3] offset:46592\n\t" "v_accvgpr_read_b32 v31, %[cr0]\n\t" "s_add_u32 m0, %[sl], 46080\n\t" "v_mfma_f32_16x16x32_bf16 v[44:47], a[128:131], a[12:15], v[44:47]\n\t"
    "s_mov_b64 exec, s[82:83]\n\t" "global_load_lds_dwordx4 %[vb3], s[78:79]\n\t" "s_mov_b64 exec, -1\n\t" "ds_read2_b32 v[24:25], v31 offset0:0 offset1:4\n\t"
    "v_mfma_f32_16x16x32_bf16 v[44:47], a[132:135], a[28:31], v[44:47]\n\t" "s_add_u32 s78, s78, 229376\n\t" "s_addc_u32 s79, s79, 0\n\t" "ds_read2_b32 v[26:27], v31 offset0:8 offset1:12\n\t"
    "ds_read2_b32 v[28:29], v31 offset0:16 offset1:20\n\t" "s_waitcnt lgkmcnt(11)\n\t" "v_mfma_f32_16x16x32_bf16 v[48:51], a[136:139], a[0:3], v[48:51]\n\t"
    "ds_read_b64_tr_b16 a[152:153], %[vr3] offset:28704\n\t" "ds_read2_b32 v[30:31], v31 offset0:24 offset1:28\n\t" "v_mfma_f32_16x16x32_bf16 v[48:51], a[140:143], a[16:19], v[48:51]\n\t"
    "ds_read_b64_tr_b16 a[154:155], %[vr3] offset:32288\n\t" "v_mfma_f32_16x16x32_bf16 v[52:55], a[136:139], a[4:7], v[52:55]\n\t" "global_load_dwordx4 a[88:91], %[va], s[76:77] offset:2048 sc1\n\t"
    "v_mfma_f32_16x16x32_bf16 v[52:55], a[140:143], a[20:23], v[52:55]\n\t" "v_mfma_f32_16x16x32_bf16 v[56:59], a[136:139], a[8:11], v[56:59]\n\t" "ds_read_b64_tr_b16 a[156:157], %[vr3] offset:43040\n\t"
    "v_mfma_f32_16x16x32_bf16 v[56:59], a[140:143], a[24:27], v[56:59]\n\t" "ds_read_b64_tr_b16 a[158:159], %[vr3] offset:46624\n\t" "v_mfma_f32_16x16x32_bf16 v[60:63], a[136:139], a[12:15], v[60:63]\n\t"
    "global_load_dwordx4 a[92:95], %[va], s[76:77] offset:3072 sc1\n\t" "v_mfma_f32_16x16x32_bf16 v[60:63], a[140:143], a[28:31], v[60:63]\n\t" "s_add_u32 s76, s76, 16384\n\t" "s_addc_u32 s77, s77, 0\n\t"
    "s_waitcnt lgkmcnt(3)\n\t" "v_add_f32_e64 v24, v24, v25\n\t" "v_mfma_f32_16x16x32_bf16 v[64:67], a[144:147], a[0:3], v[64:67]\n\t" "ds_read_b64_tr_b16 a[128:129], %[vr5] offset:28672\n\t"
    "v_add_f32_e64 v26, v26, v27\n\t" "v_mfma_f32_16x16x32_bf16 v[64:67], a[148:151], a[16:19], v[64:67]\n\t" "ds_read_b64_tr_b16 a[130:131], %[vr5] offset:32256\n\t" "v_add_f32 v28, v28, v29\n\t"
    "v_add_f32 v30, v30, v31\n\t" "v_mfma_f32_16x16x32_bf16 v[68:71], a[144:147], a[4:7], v[68:71]\n\t" "v_add_f32_e64 v24, v24, v26\n\t"
    "v_mfma_f32_16x16x32_bf16 v[68:71], a[148:151], a[20:23], v[68:71]\n\t" "v_add_f32_e64 v28, v28, v30\n\t" "v_mfma_f32_16x16x32_bf16 v[72:75], a[144:147], a[8:11], v[72:75]\n\t"
    "ds_read_b64_tr_b16 a[132:133], %[vr5] offset:43008\n\t" "v_add_f32_e64 v24, v24, v28\n\t" "v_mfma_f32_16x16x32_bf16 v[72:75], a[148:151], a[24:27], v[72:75]\n\t"
    "ds_read_b64_tr_b16 a[134:135], %[vr5] offset:46592\n\t" "s_add_u32 m0, %[sl], 57344\n\t" "v_mfma_f32_16x16x32_bf16 v[76:79], a[144:147], a[12:15], v[76:79]\n\t"
    "global_load_lds_dwordx4 %[vb0], s[78:79]\n\t" "v_cmp_u_f32_e64 vcc, v24, v24\n\t" "v_mfma_f32_16x16x32_bf16 v[76:79], a[148:151], a[28:31], v[76:79]\n\t" "s_waitcnt lgkmcnt(4)\n\t"
    "v_mfma_f32_16x16x32_bf16 v[80:83], a[152:155], a[0:3], v[80:83]\n\t" "ds_read_b64_tr_b16 a[136:137], %[vr5] offset:28704\n\t" "v_mfma_f32_16x16x32_bf16 v[80:83], a[156:159], a[16:19], v[80:83]\n\t"
    "ds_read_b64_tr_b16 a[138:139], %[vr5] offset:32288\n\t" "v_mfma_f32_16x16x32_bf16 v[84:87], a[152:155], a[4:7], v[84:87]\n\t" "global_load_dwordx4 a[96:99], %[va], s[76:77] offset:0 sc1\n\t"
    "v_mfma_f32_16x16x32_bf16 v[84:87], a[156:159], a[20:23], v[84:87]\n\t" "s_nop 0\n\t" "s_cbranch_vccnz .Lslow0_%=\n\t" ".Lgo0_%=:\n\t" "v_accvgpr_read_b32 v27, %[cw]\n\t"
    "v_mfma_f32_16x16x32_bf16 v[88:91], a[152:155], a[8:11], v[88:91]\n\t" "ds_read_b64_tr_b16 a[140:141], %[vr5] offset:43040\n\t" "v_and_b32 v25, 0xffff0000, v24\n\t"
    "v_mfma_f32_16x16x32_bf16 v[88:91], a[156:159], a[24:27], v[88:91]\n\t" "ds_read_b64_tr_b16 a[142:143], %[vr5] offset:46624\n\t" "s_add_u32 m0, %[sl], 58368\n\t"
    "v_mfma_f32_16x16x32_bf16 v[92:95], a[152:155], a[12:15], v[92:95]\n\t" "global_load_lds_dwordx4 %[vb1], s[78:79]\n\t" "v_add_u32 v27, 0x0, v27\n\t"
    "v_mfma_f32_16x16x32_bf16 v[92:95], a[156:159], a[28:31], v[92:95]\n\t" "v_sub_f32 v24, v24, v25\n\t" "s_waitcnt lgkmcnt(4)\n\t" "v_mfma_f32_16x16x32_bf16 v[96:99], a[128:131], a[0:3], v[96:99]\n\t"
    "ds_read_b64_tr_b16 a[144:145], %[vr7] offset:28672\n\t" "v_cvt_pk_bf16_f32 v24, v25, v24\n\t" "v_mfma_f32_16x16x32_bf16 v[96:99], a[132:135], a[16:19], v[96:99]\n\t"
    "ds_read_b64_tr_b16 a[146:147], %[vr7] offset:32256\n\t" "v_mfma_f32_16x16x32_bf16 v[100:103], a[128:131], a[4:7], v[100:103]\n\t" "ds_write2_b32 v27, v24, v24 offset0:0 offset1:1\n\t"
    "v_mfma_f32_16x16x32_bf16 v[100:103], a[132:135], a[20:23], v[100:103]\n\t" "v_accvgpr_read_b32 v31, %[cr1]\n\t" "v_mfma_f32_16x16x32_bf16 v[104:107], a[128:131], a[8:11], v[104:107]\n\t"
    "ds_read_b64_tr_b16 a[148:149], %[vr7] offset:43008\n\t" "ds_read2_b32 v[24:25], v31 offset0:0 offset1:4\n\t" "v_mfma_f32_16x16x32_bf16 v[104:107], a[132:135], a[24:27], v[104:107]\n\t"
    "ds_read_b64_tr_b16 a[150:151], %[vr7] offset:46592\n\t" "ds_read2_b32 v[26:27], v31 offset0:8 offset1:12\n\t" "ds_read2_b32 v[28:29], v31 offset0:16 offset1:20\n\t"
    "v_mfma_f32_16x16x32_bf16 v[108:111], a[128:131], a[12:15], v[108:111]\n\t" "global_load_dwordx4 a[100:103], %[va], s[76:77] offset:1024 sc1\n\t" "ds_read2_b32 v[30:31], v31 offset0:24 offset1:28\n\t"
    "v_mfma_f32_16x16x32_bf16 v[108:111], a[132:135], a[28:31], v[108:111]\n\t" "s_waitcnt lgkmcnt(9)\n\t" "v_mfma_f32_16x16x32_bf16 v[112:115], a[136:139], a[0:3], v[112:115]\n\t"
    "ds_read_b64_tr_b16 a[152:153], %[vr1] offset:35840\n\t" "v_mfma_f32_16x16x32_bf16 v[112:115], a[140:143], a[16:19], v[112:115]\n\t" "ds_read_b64_tr_b16 a[154:155], %[vr1] offset:39424\n\t"
    "v_mfma_f32_16x16x32_bf16 v[116:119], a[136:139], a[4:7], v[116:119]\n\t" "global_load_dwordx4 a[104:107], %[va], s[76:77] offset:2048 sc1\n\t"
    "v_mfma_f32_16x16x32_bf16 v[116:119], a[140:143], a[20:23], v[116:119]\n\t" "v_mfma_f32_16x16x32_bf16 v[120:123], a[136:139], a[8:11], v[120:123]\n\t"
    "ds_read_b64_tr_b16 a[156:157], %[vr1] offset:50176\n\t" "v_mfma_f32_16x16x32_bf16 v[120:123], a[140:143], a[24:27], v[120:123]\n\t" "ds_read_b64_tr_b16 a[158:159], %[vr1] offset:53760\n\t"
    "s_waitcnt lgkmcnt(4)\n\t" "v_add_f32_e64 v24, v24, v25\n\t" "s_add_u32 m0, %[sl], 59392\n\t" "v_mfma_f32_16x16x32_bf16 v[124:127], a[136:139], a[12:15], v[124:127]\n\t"
    "global_load_lds_dwordx4 %[vb2], s[78:79]\n\t" "v_add_f32_e64 v26, v26, v27\n\t" "v_mfma_f32_16x16x32_bf16 v[124:127], a[140:143], a[28:31], v[124:127]\n\t" "v_add_f32 v28, v28, v29\n\t"
    "v_add_f32 v30, v30, v31\n\t" "v_mfma_f32_16x16x32_bf16 v[128:131], a[144:147], a[0:3], v[128:131]\n\t" "ds_read_b64_tr_b16 a[128:129], %[vr1] offset:35872\n\t" "v_add_f32_e64 v24, v24, v26\n\t"
    "v_mfma_f32_16x16x32_bf16 v[128:131], a[148:151], a[16:19], v[128:131]\n\t" "ds_read_b64_tr_b16 a[130:131], %[vr1] offset:39456\n\t" "v_add_f32_e64 v28, v28, v30\n\t"
    "v_mfma_f32_16x16x32_bf16 v[132:135], a[144:147], a[4:7], v[132:135]\n\t" "v_add_f32_e64 v24, v24, v28\n\t" "v_mfma_f32_16x16x32_bf16 v[132:135], a[148:151], a[20:23], v[132:135]\n\t"
    "v_mfma_f32_16x16x32_bf16 v[136:139], a[144:147], a[8:11], v[136:139]\n\t" "ds_read_b64_tr_b16 a[132:133], %[vr1] offset:50208\n\t" "v_cmp_u_f32_e64 vcc, v24, v24\n\t"
    "v_mfma_f32_16x16x32_bf16 v[136:139], a[148:151], a[24:27], v[136:139]\n\t" "ds_read_b64_tr_b16 a[134:135], %[vr1] offset:53792\n\t"
    "v_mfma_f32_16x16x32_bf16 v[140:143], a[144:147], a[12:15], v[140:143]\n\t" "v_mfma_f32_16x16x32_bf16 v[140:143], a[148:151], a[28:31], v[140:143]\n\t" "s_waitcnt lgkmcnt(4)\n\t"
    "v_mfma_f32_16x16x32_bf16 v[144:147], a[152:155], a[0:3], v[144:147]\n\t" "ds_read_b64_tr_b16 a[136:137], %[vr3] offset:35840\n\t"
    "v_mfma_f32_16x16x32_bf16 v[144:147], a[156:159], a[16:19], v[144:147]\n\t" "ds_read_b64_tr_b16 a[138:139], %[vr3] offset:39424\n\t" "s_nop 0\n\t" "s_cbranch_vccnz .Lslow1_%=\n\t" ".Lgo1_%=:\n\t"
    "v_accvgpr_read_b32 v27, %[cw]\n\t" "v_mfma_f32_16x16x32_bf16 v[148:151], a[152:155], a[4:7], v[148:151]\n\t" "global_load_dwordx4 a[108:111], %[va], s[76:77] offset:3072 sc1\n\t"
    "v_and_b32 v25, 0xffff0000, v24\n\t" "v_mfma_f32_16x16x32_bf16 v[148:151], a[156:159], a[20:23], v[148:151]\n\t" "s_add_u32 s76, s76, 16384\n\t" "s_addc_u32 s77, s77, 0\n\t"
    "v_mfma_f32_16x16x32_bf16 v[152:155], a[152:155], a[8:11], v[152:155]\n\t" "ds_read_b64_tr_b16 a[140:141], %[vr3] offset:50176\n\t" "v_add_u32 v27, 0x800, v27\n\t"
    "v_mfma_f32_16x16x32_bf16 v[152:155], a[156:159], a[24:27], v[152:155]\n\t" "ds_read_b64_tr_b16 a[142:143], %[vr3] offset:53760\n\t" "v_sub_f32_e64 v24, v24, v25\n\t" "s_add_u32 m0, %[sl], 60416\n\t"
    "v_mfma_f32_16x16x32_bf16 v[156:159], a[152:155], a[12:15], v[156:159]\n\t" "s_mov_b64 exec, s[82:83]\n\t" "global_load_lds_dwordx4 %[vb3], s[78:79]\n\t" "s_mov_b64 exec, -1\n\t"
    "v_cvt_pk_bf16_f32 v24, v25, v24\n\t" "v_mfma_f32_16x16x32_bf16 v[156:159], a[156:159], a[28:31], v[156:159]\n\t" "s_add_u32 s78, s78, 229376\n\t" "s_addc_u32 s79, s79, 0\n\t"
    "s_waitcnt lgkmcnt(4)\n\t" "v_mfma_f32_16x16x32_bf16 v[160:163], a[128:131], a[0:3], v[160:163]\n\t" "ds_read_b64_tr_b16 a[144:145], %[vr3] offset:35872\n\t" "s_cmp_lg_u64 %[m01], 0\n\t"
    "s_cselect_b64 exec, -1, 0\n\t" "ds_write2_b32 v27, v24, v24 offset0:0 offset1:1\n\t" "s_mov_b64 exec, -1\n\t" "v_mfma_f32_16x16x32_bf16 v[160:163], a[132:135], a[16:19], v[160:163]\n\t"
    "ds_read_b64_tr_b16 a[146:147], %[vr3] offset:39456\n\t" "v_mfma_f32_16x16x32_bf16 v[164:167], a[128:131], a[4:7], v[164:167]\n\t" "global_load_dwordx4 a[112:115], %[va], s[76:77] offset:0 sc1\n\t"
    "v_mfma_f32_16x16x32_bf16 v[164:167], a[132:135], a[20:23], v[164:167]\n\t" "v_mfma_f32_16x16x32_bf16 v[168:171], a[128:131], a[8:11], v[168:171]\n\t"
    "ds_read_b64_tr_b16 a[148:149], %[vr3] offset:50208\n\t" "v_mfma_f32_16x16x32_bf16 v[168:171], a[132:135], a[24:27], v[168:171]\n\t" "ds_read_b64_tr_b16 a[150:151], %[vr3] offset:53792\n\t"
    "s_add_u32 m0, %[sl], 71680\n\t" "v_mfma_f32_16x16x32_bf16 v[172:175], a[128:131], a[12:15], v[172:175]\n\t" "global_load_lds_dwordx4 %[vb0], s[78:79]\n\t"
    "v_mfma_f32_16x16x32_bf16 v[172:175], a[132:135], a[28:31], v[172:175]\n\t" "s_waitcnt lgkmcnt(5)\n\t" "v_mfma_f32_16x16x32_bf16 v[176:179], a[136:139], a[0:3], v[176:179]\n\t"
    "ds_read_b64_tr_b16 a[152:153], %[vr5] offset:35840\n\t" "v_mfma_f32_16x16x32_bf16 v[176:179], a[140:143], a[16:19], v[176:179]\n\t" "ds_read_b64_tr_b16 a[154:155], %[vr5] offset:39424\n\t"
    "v_mfma_f32_16x16x32_bf16 v[180:183], a[136:139], a[4:7], v[180:183]\n\t" "v_mfma_f32_16x16x32_bf16 v[180:183], a[140:143], a[20:23], v[180:183]\n\t"
    "v_mfma_f32_16x16x32_bf16 v[184:187], a[136:139], a[8:11], v[184:187]\n\t" "ds_read_b64_tr_b16 a[156:157], %[vr5] offset:50176\n\t"
    "v_mfma_f32_16x16x32_bf16 v[184:187], a[140:143], a[24:27], v[184:187]\n\t" "ds_read_b64_tr_b16 a[158:159], %[vr5] offset:53760\n\t"
    "v_mfma_f32_16x16x32_bf16 v[188:191], a[136:139], a[12:15], v[188:191]\n\t" "global_load_dwordx4 a[116:119], %[va], s[76:77] offset:1024 sc1\n\t"
    "v_mfma_f32_16x16x32_bf16 v[188:191], a[140:143], a[28:31], v[188:191]\n\t" "s_waitcnt lgkmcnt(4)\n\t" "v_mfma_f32_16x16x32_bf16 v[192:195], a[144:147], a[0:3], v[192:195]\n\t"
    "ds_read_b64_tr_b16 a[128:129], %[vr5] offset:35872\n\t" "v_mfma_f32_16x16x32_bf16 v[192:195], a[148:151], a[16:19], v[192:195]\n\t" "ds_read_b64_tr_b16 a[130:131], %[vr5] offset:39456\n\t"
    "s_add_u32 m0, %[sl], 72704\n\t" "v_mfma_f32_16x16x32_bf16 v[196:199], a[144:147], a[4:7], v[196:199]\n\t" "global_load_lds_dwordx4 %[vb1], s[78:79]\n\t"
    "v_mfma_f32_16x16x32_bf16 v[196:199], a[148:151], a[20:23], v[196:199]\n\t" "v_mfma_f32_16x16x32_bf16 v[200:203], a[144:147], a[8:11], v[200:203]\n\t"
    "ds_read_b64_tr_b16 a[132:133], %[vr5] offset:50208\n\t" "v_mfma_f32_16x16x32_bf16 v[200:203], a[148:151], a[24:27], v[200:203]\n\t" "ds_read_b64_tr_b16 a[134:135], %[vr5] offset:53792\n\t"
    "v_mfma_f32_16x16x32_bf16 v[204:207], a[144:147], a[12:15], v[204:207]\n\t" "global_load_dwordx4 a[120:123], %[va], s[76:77] offset:2048 sc1\n\t"
    "v_mfma_f32_16x16x32_bf16 v[204:207], a[148:151], a[28:31], v[204:207]\n\t" "s_waitcnt lgkmcnt(4)\n\t" "v_mfma_f32_16x16x32_bf16 v[208:211], a[152:155], a[0:3], v[208:211]\n\t"
    "ds_read_b64_tr_b16 a[136:137], %[vr7] offset:35840\n\t" "v_mfma_f32_16x16x32_bf16 v[208:211], a[156:159], a[16:19], v[208:211]\n\t" "ds_read_b64_tr_b16 a[138:139], %[vr7] offset:39424\n\t"
    "v_mfma_f32_16x16x32_bf16 v[212:215], a[152:155], a[4:7], v[212:215]\n\t" "v_mfma_f32_16x16x32_bf16 v[212:215], a[156:159], a[20:23], v[212:215]\n\t"
    "v_mfma_f32_16x16x32_bf16 v[216:219], a[152:155], a[8:11], v[216:219]\n\t" "ds_read_b64_tr_b16 a[140:141], %[vr7] offset:50176\n\t"
    "v_mfma_f32_16x16x32_bf16 v[216:219], a[156:159], a[24:27], v[216:219]\n\t" "ds_read_b64_tr_b16 a[142:143], %[vr7] offset:53760\n\t" "s_add_u32 m0, %[sl], 73728\n\t"
    "v_mfma_f32_16x16x32_bf16 v[220:223], a[152:155], a[12:15], v[220:223]\n\t" "global_load_lds_dwordx4 %[vb2], s[78:79]\n\t" "v_mfma_f32_16x16x32_bf16 v[220:223], a[156:159], a[28:31], v[220:223]\n\t"
    "s_waitcnt lgkmcnt(4)\n\t" "v_mfma_f32_16x16x32_bf16 v[224:227], a[128:131], a[0:3], v[224:227]\n\t" "s_waitcnt vmcnt(14)\n\t" "s_barrier\n\t" "v_accvgpr_read_b32 v31, %[ct]\n\t"
    "s_mov_b64 exec, 0xffff\n\t" "v_add_u32_e64 v31, %[gt0], v31\n\t" "s_mov_b64 exec, -1\n\t" "ds_read_b128 a[144:147], v31 offset:0\n\t"
    "v_mfma_f32_16x16x32_bf16 v[224:227], a[132:135], a[16:19], v[224:227]\n\t" "v_mfma_f32_16x16x32_bf16 v[228:231], a[128:131], a[4:7], v[228:231]\n\t"
    "global_load_dwordx4 a[124:127], %[va], s[76:77] offset:3072 sc1\n\t" "v_mfma_f32_16x16x32_bf16 v[228:231], a[132:135], a[20:23], v[228:231]\n\t" "s_add_u32 s76, s76, s86\n\t"
    "s_addc_u32 s77, s77, s87\n\t" "v_mfma_f32_16x16x32_bf16 v[232:235], a[128:131], a[8:11], v[232:235]\n\t" "ds_read_b128 a[148:151], v31 offset:256\n\t"
    "v_mfma_f32_16x16x32_bf16 v[232:235], a[132:135], a[24:27], v[232:235]\n\t" "s_add_u32 m0, %[sl], 74752\n\t" "v_mfma_f32_16x16x32_bf16 v[236:239], a[128:131], a[12:15], v[236:239]\n\t"
    "s_mov_b64 exec, s[82:83]\n\t" "global_load_lds_dwordx4 %[vb3], s[78:79]\n\t" "s_mov_b64 exec, -1\n\t" "v_mfma_f32_16x16x32_bf16 v[236:239], a[132:135], a[28:31], v[236:239]\n\t"
    "s_add_u32 s78, s78, s88\n\t" "s_addc_u32 s79, s79, s89\n\t" "s_waitcnt lgkmcnt(2)\n\t" "v_mfma_f32_16x16x32_bf16 v[240:243], a[136:139], a[0:3], v[240:243]\n\t"
    "ds_read_b64_tr_b16 a[152:153], %[vr0] offset:0\n\t" "v_mfma_f32_16x16x32_bf16 v[240:243], a[140:143], a[16:19], v[240:243]\n\t" "ds_read_b64_tr_b16 a[154:155], %[vr0] offset:3584\n\t"
    "v_mfma_f32_16x16x32_bf16 v[244:247], a[136:139], a[4:7], v[244:247]\n\t" "v_mfma_f32_16x16x32_bf16 v[244:247], a[140:143], a[20:23], v[244:247]\n\t"
    "v_mfma_f32_16x16x32_bf16 v[248:251], a[136:139], a[8:11], v[248:251]\n\t" "ds_read_b64_tr_b16 a[156:157], %[vr0] offset:14336\n\t"
    "v_mfma_f32_16x16x32_bf16 v[248:251], a[140:143], a[24:27], v[248:251]\n\t" "ds_read_b64_tr_b16 a[158:159], %[vr0] offset:17920\n\t"
    "v_mfma_f32_16x16x32_bf16 v[252:255], a[136:139], a[12:15], v[252:255]\n\t" "v_mfma_f32_16x16x32_bf16 v[252:255], a[140:143], a[28:31], v[252:255]\n\t" "s_waitcnt lgkmcnt(4)\n\t"
    "v_mfma_f32_16x16x32_bf16 v[32:35], a[144:147], a[200:203], v[32:35]\n\t" "ds_read_b64_tr_b16 a[128:129], %[vr0] offset:7168\n\t"
    "v_mfma_f32_16x16x32_bf16 v[36:39], a[144:147], a[204:207], v[36:39]\n\t" "ds_read_b64_tr_b16 a[130:131], %[vr0] offset:10752\n\t"
    "v_mfma_f32_16x16x32_bf16 v[40:43], a[144:147], a[208:211], v[40:43]\n\t" "v_mfma_f32_16x16x32_bf16 v[44:47], a[144:147], a[212:215], v[44:47]\n\t"
    "v_mfma_f32_16x16x32_bf16 v[144:147], a[148:151], a[200:203], v[144:147]\n\t" "ds_read_b64_tr_b16 a[132:133], %[vr0] offset:21504\n\t"
    "v_mfma_f32_16x16x32_bf16 v[148:151], a[148:151], a[204:207], v[148:151]\n\t" "ds_read_b64_tr_b16 a[134:135], %[vr0] offset:25088\n\t"
    "v_mfma_f32_16x16x32_bf16 v[152:155], a[148:151], a[208:211], v[152:155]\n\t" "v_mfma_f32_16x16x32_bf16 v[156:159], a[148:151], a[212:215], v[156:159]\n\t" "s_waitcnt lgkmcnt(4)\n\t"
    "v_mfma_f32_16x16x32_bf16 v[32:35], a[152:155], a[32:35], v[32:35]\n\t" "ds_read_b64_tr_b16 a[136:137], %[vr0] offset:28672\n\t" "v_mfma_f32_16x16x32_bf16 v[32:35], a[156:159], a[48:51], v[32:35]\n\t"
    "ds_read_b64_tr_b16 a[138:139], %[vr0] offset:32256\n\t" "v_mfma_f32_16x16x32_bf16 v[36:39], a[152:155], a[36:39], v[36:39]\n\t" "v_mfma_f32_16x16x32_bf16 v[36:39], a[156:159], a[52:55], v[36:39]\n\t"
    "v_mfma_f32_16x16x32_bf16 v[40:43], a[152:155], a[40:43], v[40:43]\n\t" "ds_read_b64_tr_b16 a[140:141], %[vr0] offset:43008\n\t" "v_mfma_f32_16x16x32_bf16 v[40:43], a[156:159], a[56:59], v[40:43]\n\t"
    "ds_read_b64_tr_b16 a[142:143], %[vr0] offset:46592\n\t" "v_mfma_f32_16x16x32_bf16 v[44:47], a[152:155], a[44:47], v[44:47]\n\t" "v_mfma_f32_16x16x32_bf16 v[44:47], a[156:159], a[60:63], v[44:47]\n\t"
    "s_waitcnt lgkmcnt(4)\n\t" "v_mfma_f32_16x16x32_bf16 v[144:147], a[128:131], a[32:35], v[144:147]\n\t" "ds_read_b64_tr_b16 a[144:145], %[vr0] offset:35840\n\t"
    "v_mfma_f32_16x16x32_bf16 v[144:147], a[132:135], a[48:51], v[144:147]\n\t" "ds_read_b64_tr_b16 a[146:147], %[vr0] offset:39424\n\t"
    "v_mfma_f32_16x16x32_bf16 v[148:151], a[128:131], a[36:39], v[148:151]\n\t" "v_mfma_f32_16x16x32_bf16 v[148:151], a[132:135], a[52:55], v[148:151]\n\t"
    "v_mfma_f32_16x16x32_bf16 v[152:155], a[128:131], a[40:43], v[152:155]\n\t" "ds_read_b64_tr_b16 a[148:149], %[vr0] offset:50176\n\t"
    "v_mfma_f32_16x16x32_bf16 v[152:155], a[132:135], a[56:59], v[152:155]\n\t" "ds_read_b64_tr_b16 a[150:151], %[vr0] offset:53760\n\t"
    "v_mfma_f32_16x16x32_bf16 v[156:159], a[128:131], a[44:47], v[156:159]\n\t" "v_mfma_f32_16x16x32_bf16 v[156:159], a[132:135], a[60:63], v[156:159]\n\t" "s_waitcnt lgkmcnt(4)\n\t"
    "v_mfma_f32_16x16x32_bf16 v[32:35], a[136:139], a[64:67], v[32:35]\n\t" "s_waitcnt vmcnt(0)\n\t" "s_barrier\n\t" "v_lshlrev_b32 v30, 3, %[va]\n\t" "s_mov_b32 exec_hi, 0xffffff\n\t"
    "global_load_dword a230, v30, %[tp0]\n\t" "s_mov_b64 exec, -1\n\t" "ds_read_b64_tr_b16 a[152:153], %[vr1] offset:0\n\t" "v_mfma_f32_16x16x32_bf16 v[32:35], a[140:143], a[80:83], v[32:35]\n\t"
    "ds_read_b64_tr_b16 a[154:155], %[vr1] offset:3584\n\t" "v_mfma_f32_16x16x32_bf16 v[36:39], a[136:139], a[68:71], v[36:39]\n\t" "v_mfma_f32_16x16x32_bf16 v[36:39], a[140:143], a[84:87], v[36:39]\n\t"
    "v_mfma_f32_16x16x32_bf16 v[40:43], a[136:139], a[72:75], v[40:43]\n\t" "ds_read_b64_tr_b16 a[156:157], %[vr1] offset:14336\n\t" "v_mfma_f32_16x16x32_bf16 v[40:43], a[140:143], a[88:91], v[40:43]\n\t"
    "ds_read_b64_tr_b16 a[158:159], %[vr1] offset:17920\n\t" "v_mfma_f32_16x16x32_bf16 v[44:47], a[136:139], a[76:79], v[44:47]\n\t" "v_mfma_f32_16x16x32_bf16 v[44:47], a[140:143], a[92:95], v[44:47]\n\t"
    "s_waitcnt lgkmcnt(4)\n\t" "v_mfma_f32_16x16x32_bf16 v[144:147], a[144:147], a[64:67], v[144:147]\n\t" "ds_read_b64_tr_b16 a[128:129], %[vr1] offset:7168\n\t"
    "v_mfma_f32_16x16x32_bf16 v[144:147], a[148:151], a[80:83], v[144:147]\n\t" "ds_read_b64_tr_b16 a[130:131], %[vr1] offset:10752\n\t"
    "v_mfma_f32_16x16x32_bf16 v[148:151], a[144:147], a[68:71], v[148:151]\n\t" "v_mfma_f32_16x16x32_bf16 v[148:151], a[148:151], a[84:87], v[148:151]\n\t"
    "v_mfma_f32_16x16x32_bf16 v[152:155], a[144:147], a[72:75], v[152:155]\n\t" "ds_read_b64_tr_b16 a[132:133], %[vr1] offset:21504\n\t"
    "v_mfma_f32_16x16x32_bf16 v[152:155], a[148:151], a[88:91], v[152:155]\n\t" "ds_read_b64_tr_b16 a[134:135], %[vr1] offset:25088\n\t"
    "v_mfma_f32_16x16x32_bf16 v[156:159], a[144:147], a[76:79], v[156:159]\n\t" "v_mfma_f32_16x16x32_bf16 v[156:159], a[148:151], a[92:95], v[156:159]\n\t" "s_waitcnt lgkmcnt(4)\n\t"
    "v_mfma_f32_16x16x32_bf16 v[32:35], a[152:155], a[96:99], v[32:35]\n\t" "ds_read_b128 a[136:139], v31 offset:512\n\t" "v_mfma_f32_16x16x32_bf16 v[32:35], a[156:159], a[112:115], v[32:35]\n\t"
    "v_mfma_f32_16x16x32_bf16 v[36:39], a[152:155], a[100:103], v[36:39]\n\t" "v_mfma_f32_16x16x32_bf16 v[36:39], a[156:159], a[116:119], v[36:39]\n\t"
    "v_mfma_f32_16x16x32_bf16 v[40:43], a[152:155], a[104:107], v[40:43]\n\t" "ds_read_b128 a[140:143], v31 offset:768\n\t" "v_mfma_f32_16x16x32_bf16 v[40:43], a[156:159], a[120:123], v[40:43]\n\t"
    "v_mfma_f32_16x16x32_bf16 v[44:47], a[152:155], a[108:111], v[44:47]\n\t" "v_mfma_f32_16x16x32_bf16 v[44:47], a[156:159], a[124:127], v[44:47]\n\t" "s_waitcnt lgkmcnt(2)\n\t"
    "v_mfma_f32_16x16x32_bf16 v[144:147], a[128:131], a[96:99], v[144:147]\n\t" "ds_read_b64_tr_b16 a[144:145], %[vr0] offset:32\n\t"
    "v_mfma_f32_16x16x32_bf16 v[144:147], a[132:135], a[112:115], v[144:147]\n\t" "ds_read_b64_tr_b16 a[146:147], %[vr0] offset:3616\n\t"
    "v_mfma_f32_16x16x32_bf16 v[148:151], a[128:131], a[100:103], v[148:151]\n\t" "v_mfma_f32_16x16x32_bf16 v[148:151], a[132:135], a[116:119], v[148:151]\n\t"
    "v_mfma_f32_16x16x32_bf16 v[152:155], a[128:131], a[104:107], v[152:155]\n\t" "ds_read_b64_tr_b16 a[148:149], %[vr0] offset:14368\n\t"
    "v_mfma_f32_16x16x32_bf16 v[152:155], a[132:135], a[120:123], v[152:155]\n\t" "ds_read_b64_tr_b16 a[150:151], %[vr0] offset:17952\n\t"
    "v_mfma_f32_16x16x32_bf16 v[156:159], a[128:131], a[108:111], v[156:159]\n\t" "v_mfma_f32_16x16x32_bf16 v[156:159], a[132:135], a[124:127], v[156:159]\n\t" "s_waitcnt lgkmcnt(4)\n\t"
    "v_mfma_f32_16x16x32_bf16 v[48:51], a[136:139], a[200:203], v[48:51]\n\t" "ds_read_b64_tr_b16 a[152:153], %[vr0] offset:7200\n\t" "v_mul_f32 v32, v32, v16\n\t" "v_mul_f32_e64 v33, v33, v16\n\t"
    "v_mfma_f32_16x16x32_bf16 v[52:55], a[136:139], a[204:207], v[52:55]\n\t" "ds_read_b64_tr_b16 a[154:155], %[vr0] offset:10784\n\t" "v_mul_f32 v34, v34, v16\n\t" "v_mul_f32 v35, v35, v16\n\t"
    "v_mfma_f32_16x16x32_bf16 v[56:59], a[136:139], a[208:211], v[56:59]\n\t" "v_exp_f32_e64 v32, v32\n\t" "v_mfma_f32_16x16x32_bf16 v[60:63], a[136:139], a[212:215], v[60:63]\n\t"
    "v_exp_f32_e64 v33, v33\n\t" "v_mfma_f32_16x16x32_bf16 v[160:163], a[140:143], a[200:203], v[160:163]\n\t" "ds_read_b64_tr_b16 a[156:157], %[vr0] offset:21536\n\t" "v_exp_f32_e64 v34, v34\n\t"
    "v_mfma_f32_16x16x32_bf16 v[164:167], a[140:143], a[204:207], v[164:167]\n\t" "ds_read_b64_tr_b16 a[158:159], %[vr0] offset:25120\n\t" "v_exp_f32_e64 v35, v35\n\t"
    "v_mfma_f32_16x16x32_bf16 v[168:171], a[140:143], a[208:211], v[168:171]\n\t" "v_fma_f32 v32, v32, v20, v20\n\t" "v_fma_f32 v33, v33, v20, v20\n\t"
    "v_mfma_f32_16x16x32_bf16 v[172:175], a[140:143], a[212:215], v[172:175]\n\t" "v_fma_f32 v34, v34, v20, v20\n\t" "v_fma_f32 v35, v35, v20, v20\n\t" "s_waitcnt lgkmcnt(4)\n\t"
    "v_mfma_f32_16x16x32_bf16 v[48:51], a[144:147], a[32:35], v[48:51]\n\t" "ds_read_b64_tr_b16 a[128:129], %[vr0] offset:28704\n\t" "v_rcp_f32 v32, v32\n\t"
    "v_mfma_f32_16x16x32_bf16 v[48:51], a[148:151], a[48:51], v[48:51]\n\t" "ds_read_b64_tr_b16 a[130:131], %[vr0] offset:32288\n\t" "v_rcp_f32_e64 v33, v33\n\t"
    "v_mfma_f32_16x16x32_bf16 v[52:55], a[144:147], a[36:39], v[52:55]\n\t" "v_rcp_f32_e64 v34, v34\n\t" "v_mfma_f32_16x16x32_bf16 v[52:55], a[148:151], a[52:55], v[52:55]\n\t"
    "v_rcp_f32_e64 v35, v35\n\t" "v_mfma_f32_16x16x32_bf16 v[56:59], a[144:147], a[40:43], v[56:59]\n\t" "ds_read_b64_tr_b16 a[132:133], %[vr0] offset:43040\n\t" "v_mul_f32 v144, v144, v32\n\t"
    "v_mul_f32 v145, v145, v33\n\t" "v_mfma_f32_16x16x32_bf16 v[56:59], a[148:151], a[56:59], v[56:59]\n\t" "ds_read_b64_tr_b16 a[134:135], %[vr0] offset:46624\n\t" "v_mul_f32 v146, v146, v34\n\t"
    "v_mul_f32 v147, v147, v35\n\t" "v_mfma_f32_16x16x32_bf16 v[60:63], a[144:147], a[44:47], v[60:63]\n\t" "v_cvt_pk_bf16_f32 v32, v144, v145\n\t" "v_cvt_pk_bf16_f32 v33, v146, v147\n\t"
    "v_mfma_f32_16x16x32_bf16 v[60:63], a[148:151], a[60:63], v[60:63]\n\t" "v_mul_f32 v36, v36, v17\n\t" "v_mul_f32_e64 v37, v37, v17\n\t" "s_waitcnt lgkmcnt(4)\n\t"
    "v_mfma_f32_16x16x32_bf16 v[160:163], a[152:155], a[32:35], v[160:163]\n\t" "ds_read_b64_tr_b16 a[136:137], %[vr0] offset:35872\n\t" "v_mul_f32 v38, v38, v17\n\t" "v_mul_f32 v39, v39, v17\n\t"
    "v_mfma_f32_16x16x32_bf16 v[160:163], a[156:159], a[48:51], v[160:163]\n\t" "ds_read_b64_tr_b16 a[138:139], %[vr0] offset:39456\n\t" "v_exp_f32_e64 v36, v36\n\t"
    "v_mfma_f32_16x16x32_bf16 v[164:167], a[152:155], a[36:39], v[164:167]\n\t" "v_exp_f32_e64 v37, v37\n\t" "v_mfma_f32_16x16x32_bf16 v[164:167], a[156:159], a[52:55], v[164:167]\n\t"
    "v_exp_f32_e64 v38, v38\n\t" "v_mfma_f32_16x16x32_bf16 v[168:171], a[152:155], a[40:43], v[168:171]\n\t" "ds_read_b64_tr_b16 a[140:141], %[vr0] offset:50208\n\t" "v_exp_f32_e64 v39, v39\n\t"
    "v_mfma_f32_16x16x32_bf16 v[168:171], a[156:159], a[56:59], v[168:171]\n\t" "ds_read_b64_tr_b16 a[142:143], %[vr0] offset:53792\n\t" "v_fma_f32 v36, v36, v21, v21\n\t"
    "v_fma_f32 v37, v37, v21, v21\n\t" "v_mfma_f32_16x16x32_bf16 v[172:175], a[152:155], a[44:47], v[172:175]\n\t" "v_fma_f32 v38, v38, v21, v21\n\t" "v_fma_f32 v39, v39, v21, v21\n\t"
    "v_mfma_f32_16x16x32_bf16 v[172:175], a[156:159], a[60:63], v[172:175]\n\t" "v_rcp_f32 v36, v36\n\t" "s_waitcnt lgkmcnt(4)\n\t" "v_mfma_f32_16x16x32_bf16 v[48:51], a[128:131], a[64:67], v[48:51]\n\t"
    "ds_read_b64_tr_b16 a[144:145], %[vr1] offset:32\n\t" "v_rcp_f32_e64 v37, v37\n\t" "v_mfma_f32_16x16x32_bf16 v[48:51], a[132:135], a[80:83], v[48:51]\n\t"
    "ds_read_b64_tr_b16 a[146:147], %[vr1] offset:3616\n\t" "v_rcp_f32_e64 v38, v38\n\t" "v_mfma_f32_16x16x32_bf16 v[52:55], a[128:131], a[68:71], v[52:55]\n\t" "v_rcp_f32_e64 v39, v39\n\t"
    "v_mfma_f32_16x16x32_bf16 v[52:55], a[132:135], a[84:87], v[52:55]\n\t" "v_mul_f32 v148, v148, v36\n\t" "v_mul_f32 v149, v149, v37\n\t"
    "v_mfma_f32_16x16x32_bf16 v[56:59], a[128:131], a[72:75], v[56:59]\n\t" "ds_read_b64_tr_b16 a[148:149], %[vr1] offset:14368\n\t" "v_mul_f32 v150, v150, v38\n\t" "v_mul_f32 v151, v151, v39\n\t"
    "v_mfma_f32_16x16x32_bf16 v[56:59], a[132:135], a[88:91], v[56:59]\n\t" "ds_read_b64_tr_b16 a[150:151], %[vr1] offset:17952\n\t" "v_cvt_pk_bf16_f32 v36, v148, v149\n\t"
    "v_cvt_pk_bf16_f32 v37, v150, v151\n\t" "v_mfma_f32_16x16x32_bf16 v[60:63], a[128:131], a[76:79], v[60:63]\n\t" "v_mul_f32 v40, v40, v18\n\t" "v_mul_f32 v41, v41, v18\n\t"
    "v_mfma_f32_16x16x32_bf16 v[60:63], a[132:135], a[92:95], v[60:63]\n\t" "v_mul_f32 v42, v42, v18\n\t" "v_mul_f32_e64 v43, v43, v18\n\t" "s_waitcnt lgkmcnt(4)\n\t"
    "v_mfma_f32_16x16x32_bf16 v[160:163], a[136:139], a[64:67], v[160:163]\n\t" "ds_read_b64_tr_b16 a[152:153], %[vr1] offset:7200\n\t" "v_exp_f32_e64 v40, v40\n\t"
    "v_mfma_f32_16x16x32_bf16 v[160:163], a[140:143], a[80:83], v[160:163]\n\t" "ds_read_b64_tr_b16 a[154:155], %[vr1] offset:10784\n\t" "v_exp_f32_e64 v41, v41\n\t"
    "v_mfma_f32_16x16x32_bf16 v[164:167], a[136:139], a[68:71], v[164:167]\n\t" "v_exp_f32_e64 v42, v42\n\t" "v_mfma_f32_16x16x32_bf16 v[164:167], a[140:143], a[84:87], v[164:167]\n\t"
    "v_exp_f32_e64 v43, v43\n\t" "v_mfma_f32_16x16x32_bf16 v[168:171], a[136:139], a[72:75], v[168:171]\n\t" "ds_read_b64_tr_b16 a[156:157], %[vr1] offset:21536\n\t" "v_fma_f32 v40, v40, v22, v22\n\t"
    "v_fma_f32 v41, v41, v22, v22\n\t" "v_mfma_f32_16x16x32_bf16 v[168:171], a[140:143], a[88:91], v[168:171]\n\t" "ds_read_b64_tr_b16 a[158:159], %[vr1] offset:25120\n\t"
    "v_fma_f32 v42, v42, v22, v22\n\t" "v_fma_f32 v43, v43, v22, v22\n\t" "v_mfma_f32_16x16x32_bf16 v[172:175], a[136:139], a[76:79], v[172:175]\n\t" "v_rcp_f32_e64 v40, v40\n\t"
    "v_mfma_f32_16x16x32_bf16 v[172:175], a[140:143], a[92:95], v[172:175]\n\t" "v_rcp_f32 v41, v41\n\t" "s_waitcnt lgkmcnt(4)\n\t" "v_mfma_f32_16x16x32_bf16 v[48:51], a[144:147], a[96:99], v[48:51]\n\t"
    "v_accvgpr_read_b32 v31, %[ct]\n\t" "s_mov_b64 exec, 0xffff\n\t" "v_add_u32 v31, %[gt1], v31\n\t" "s_mov_b64 exec, -1\n\t" "ds_read_b128 a[128:131], v31 offset:0\n\t" "v_rcp_f32_e64 v42, v42\n\t"
    "v_mfma_f32_16x16x32_bf16 v[48:51], a[148:151], a[112:115], v[48:51]\n\t" "v_rcp_f32_e64 v43, v43\n\t" "v_mfma_f32_16x16x32_bf16 v[52:55], a[144:147], a[100:103], v[52:55]\n\t"
    "v_mul_f32 v152, v152, v40\n\t" "v_mul_f32 v153, v153, v41\n\t" "v_mfma_f32_16x16x32_bf16 v[52:55], a[148:151], a[116:119], v[52:55]\n\t" "v_mul_f32 v154, v154, v42\n\t"
    "v_mul_f32 v155, v155, v43\n\t" "v_mfma_f32_16x16x32_bf16 v[56:59], a[144:147], a[104:107], v[56:59]\n\t" "ds_read_b128 a[132:135], v31 offset:256\n\t" "v_cvt_pk_bf16_f32 v40, v152, v153\n\t"
    "v_cvt_pk_bf16_f32 v41, v154, v155\n\t" "v_mfma_f32_16x16x32_bf16 v[56:59], a[148:151], a[120:123], v[56:59]\n\t" "v_mul_f32 v44, v44, v19\n\t" "v_mul_f32 v45, v45, v19\n\t"
    "v_mfma_f32_16x16x32_bf16 v[60:63], a[144:147], a[108:111], v[60:63]\n\t" "v_mul_f32 v46, v46, v19\n\t" "v_mul_f32 v47, v47, v19\n\t"
    "v_mfma_f32_16x16x32_bf16 v[60:63], a[148:151], a[124:127], v[60:63]\n\t" "v_exp_f32 v44, v44\n\t" "s_waitcnt lgkmcnt(2)\n\t"
    "v_mfma_f32_16x16x32_bf16 v[160:163], a[152:155], a[96:99], v[160:163]\n\t" "ds_read_b64_tr_b16 a[136:137], %[vr2] offset:0\n\t" "v_exp_f32_e64 v45, v45\n\t"
    "v_mfma_f32_16x16x32_bf16 v[160:163], a[156:159], a[112:115], v[160:163]\n\t" "ds_read_b64_tr_b16 a[138:139], %[vr2] offset:3584\n\t" "v_exp_f32_e64 v46, v46\n\t"
    "v_mfma_f32_16x16x32_bf16 v[164:167], a[152:155], a[100:103], v[164:167]\n\t" "global_load_dword a231, %[va], %[cd]\n\t" "v_exp_f32_e64 v47, v47\n\t"
    "v_mfma_f32_16x16x32_bf16 v[164:167], a[156:159], a[116:119], v[164:167]\n\t" "v_fma_f32 v44, v44, v23, v23\n\t" "v_fma_f32 v45, v45, v23, v23\n\t"
    "v_mfma_f32_16x16x32_bf16 v[168:171], a[152:155], a[104:107], v[168:171]\n\t" "ds_read_b64_tr_b16 a[140:141], %[vr2] offset:14336\n\t" "v_fma_f32 v46, v46, v23, v23\n\t"
    "v_fma_f32 v47, v47, v23, v23\n\t" "v_mfma_f32_16x16x32_bf16 v[168:171], a[156:159], a[120:123], v[168:171]\n\t" "ds_read_b64_tr_b16 a[142:143], %[vr2] offset:17920\n\t" "v_rcp_f32_e64 v44, v44\n\t"
    "v_mfma_f32_16x16x32_bf16 v[172:175], a[152:155], a[108:111], v[172:175]\n\t" "v_rcp_f32_e64 v45, v45\n\t" "v_mfma_f32_16x16x32_bf16 v[172:175], a[156:159], a[124:127], v[172:175]\n\t"
    "v_rcp_f32 v46, v46\n\t" "s_waitcnt lgkmcnt(4)\n\t" "v_mfma_f32_16x16x32_bf16 v[64:67], a[128:131], a[200:203], v[64:67]\n\t" "ds_read_b64_tr_b16 a[144:145], %[vr2] offset:7168\n\t"
    "v_rcp_f32_e64 v47, v47\n\t" "v_mfma_f32_16x16x32_bf16 v[68:71], a[128:131], a[204:207], v[68:71]\n\t" "ds_read_b64_tr_b16 a[146:147], %[vr2] offset:10752\n\t" "v_mul_f32 v156, v156, v44\n\t"
    "v_mul_f32 v157, v157, v45\n\t" "v_mfma_f32_16x16x32_bf16 v[72:75], a[128:131], a[208:211], v[72:75]\n\t" "v_mul_f32 v158, v158, v46\n\t" "v_mul_f32 v159, v159, v47\n\t"
    "v_mfma_f32_16x16x32_bf16 v[76:79], a[128:131], a[212:215], v[76:79]\n\t" "v_cvt_pk_bf16_f32 v44, v156, v157\n\t" "v_cvt_pk_bf16_f32 v45, v158, v159\n\t"
    "v_mfma_f32_16x16x32_bf16 v[176:179], a[132:135], a[200:203], v[176:179]\n\t" "ds_read_b64_tr_b16 a[148:149], %[vr2] offset:21504\n\t" "s_add_u32 s90, %[shl], %[go0]\n\t"
    "s_addc_u32 s91, %[shh], 0\n\t" "s_add_u32 s92, s90, 229376\n\t" "s_addc_u32 s93, s91, 0\n\t" "v_mfma_f32_16x16x32_bf16 v[180:183], a[132:135], a[204:207], v[180:183]\n\t"
    "ds_read_b64_tr_b16 a[150:151], %[vr2] offset:25088\n\t" "v_mul_f32 v48, v48, v16\n\t" "v_mul_f32_e64 v49, v49, v16\n\t" "v_mfma_f32_16x16x32_bf16 v[184:187], a[132:135], a[208:211], v[184:187]\n\t"
    "v_accvgpr_read_b32 v29, %[vbd]\n\t" "s_nop 0\n\t" "s_add_u32 m0, %[sld], 24576\n\t" "s_nop 0\n\t" "global_load_lds_dwordx4 v29, %[sbw] offset:0\n\t" "v_mul_f32 v50, v50, v16\n\t"
    "v_mul_f32 v51, v51, v16\n\t" "v_mfma_f32_16x16x32_bf16 v[188:191], a[132:135], a[212:215], v[188:191]\n\t" "v_exp_f32 v48, v48\n\t" "s_waitcnt lgkmcnt(4)\n\t"
    "v_mfma_f32_16x16x32_bf16 v[64:67], a[136:139], a[32:35], v[64:67]\n\t" "ds_read_b64_tr_b16 a[152:153], %[vr2] offset:28672\n\t" "v_exp_f32_e64 v49, v49\n\t"
    "v_mfma_f32_16x16x32_bf16 v[64:67], a[140:143], a[48:51], v[64:67]\n\t" "ds_read_b64_tr_b16 a[154:155], %[vr2] offset:32256\n\t" "v_exp_f32_e64 v50, v50\n\t"
    "v_mfma_f32_16x16x32_bf16 v[68:71], a[136:139], a[36:39], v[68:71]\n\t" "v_exp_f32_e64 v51, v51\n\t" "v_mfma_f32_16x16x32_bf16 v[68:71], a[140:143], a[52:55], v[68:71]\n\t"
    "v_fma_f32 v48, v48, v20, v20\n\t" "v_fma_f32 v49, v49, v20, v20\n\t" "v_mfma_f32_16x16x32_bf16 v[72:75], a[136:139], a[40:43], v[72:75]\n\t" "ds_read_b64_tr_b16 a[156:157], %[vr2] offset:43008\n\t"
    "v_fma_f32 v50, v50, v20, v20\n\t" "v_fma_f32 v51, v51, v20, v20\n\t" "v_mfma_f32_16x16x32_bf16 v[72:75], a[140:143], a[56:59], v[72:75]\n\t" "ds_read_b64_tr_b16 a[158:159], %[vr2] offset:46592\n\t"
    "v_rcp_f32_e64 v48, v48\n\t" "v_mfma_f32_16x16x32_bf16 v[76:79], a[136:139], a[44:47], v[76:79]\n\t" "s_add_u32 m0, %[sld], 28544\n\t" "s_nop 0\n\t"
    "global_load_lds_dwordx4 v29, %[sbw] offset:128\n\t" "v_add_u32 v29, 0x40000, v29\n\t" "v_rcp_f32 v49, v49\n\t" "v_mfma_f32_16x16x32_bf16 v[76:79], a[140:143], a[60:63], v[76:79]\n\t"
    "v_rcp_f32 v50, v50\n\t" "s_waitcnt lgkmcnt(4)\n\t" "v_mfma_f32_16x16x32_bf16 v[176:179], a[144:147], a[32:35], v[176:179]\n\t" "ds_read_b64_tr_b16 a[128:129], %[vr2] offset:35840\n\t"
    "v_rcp_f32_e64 v51, v51\n\t" "v_mfma_f32_16x16x32_bf16 v[176:179], a[148:151], a[48:51], v[176:179]\n\t" "ds_read_b64_tr_b16 a[130:131], %[vr2] offset:39424\n\t" "v_mul_f32 v160, v160, v48\n\t"
    "v_mul_f32 v161, v161, v49\n\t" "v_mfma_f32_16x16x32_bf16 v[180:183], a[144:147], a[36:39], v[180:183]\n\t" "v_mul_f32 v162, v162, v50\n\t" "v_mul_f32 v163, v163, v51\n\t"
    "v_mfma_f32_16x16x32_bf16 v[180:183], a[148:151], a[52:55], v[180:183]\n\t" "v_cvt_pk_bf16_f32 v34, v160, v161\n\t" "v_cvt_pk_bf16_f32 v35, v162, v163\n\t"
    "v_mfma_f32_16x16x32_bf16 v[184:187], a[144:147], a[40:43], v[184:187]\n\t" "ds_read_b64_tr_b16 a[132:133], %[vr2] offset:50176\n\t" "global_store_dwordx4 %[va], v[32:35], s[90:91] offset:0 sc1\n\t"
    "v_mul_f32_e64 v52, v52, v17\n\t" "v_mfma_f32_16x16x32_bf16 v[184:187], a[148:151], a[56:59], v[184:187]\n\t" "ds_read_b64_tr_b16 a[134:135], %[vr2] offset:53760\n\t" "v_mul_f32 v53, v53, v17\n\t"
    "v_mul_f32 v54, v54, v17\n\t" "v_mfma_f32_16x16x32_bf16 v[188:191], a[144:147], a[44:47], v[188:191]\n\t" "s_add_u32 m0, %[sld], 32768\n\t" "s_nop 0\n\t"
    "global_load_lds_dwordx4 v29, %[sbw] offset:0\n\t" "v_mul_f32 v55, v55, v17\n\t" "v_exp_f32_e64 v52, v52\n\t" "v_mfma_f32_16x16x32_bf16 v[188:191], a[148:151], a[60:63], v[188:191]\n\t"
    "v_exp_f32 v53, v53\n\t" "s_waitcnt lgkmcnt(4)\n\t" "v_mfma_f32_16x16x32_bf16 v[64:67], a[152:155], a[64:67], v[64:67]\n\t" "ds_read_b64_tr_b16 a[136:137], %[vr3] offset:0\n\t"
    "v_exp_f32_e64 v54, v54\n\t" "v_mfma_f32_16x16x32_bf16 v[64:67], a[156:159], a[80:83], v[64:67]\n\t" "ds_read_b64_tr_b16 a[138:139], %[vr3] offset:3584\n\t" "v_exp_f32_e64 v55, v55\n\t"
    "v_mfma_f32_16x16x32_bf16 v[68:71], a[152:155], a[68:71], v[68:71]\n\t" "v_fma_f32 v52, v52, v21, v21\n\t" "v_fma_f32 v53, v53, v21, v21\n\t"
    "v_mfma_f32_16x16x32_bf16 v[68:71], a[156:159], a[84:87], v[68:71]\n\t" "v_fma_f32 v54, v54, v21, v21\n\t" "v_fma_f32 v55, v55, v21, v21\n\t"
    "v_mfma_f32_16x16x32_bf16 v[72:75], a[152:155], a[72:75], v[72:75]\n\t" "ds_read_b64_tr_b16 a[140:141], %[vr3] offset:14336\n\t" "v_rcp_f32_e64 v52, v52\n\t"
    "v_mfma_f32_16x16x32_bf16 v[72:75], a[156:159], a[88:91], v[72:75]\n\t" "ds_read_b64_tr_b16 a[142:143], %[vr3] offset:17920\n\t" "v_rcp_f32_e64 v53, v53\n\t"
    "v_mfma_f32_16x16x32_bf16 v[76:79], a[152:155], a[76:79], v[76:79]\n\t" "s_add_u32 m0, %[sld], 36736\n\t" "s_nop 0\n\t" "global_load_lds_dwordx4 v29, %[sbw] offset:128\n\t"
    "v_add_u32 v29, 0x30000, v29\n\t" "v_rcp_f32 v54, v54\n\t" "v_mfma_f32_16x16x32_bf16 v[76:79], a[156:159], a[92:95], v[76:79]\n\t" "v_rcp_f32 v55, v55\n\t" "s_waitcnt lgkmcnt(4)\n\t"
    "v_mfma_f32_16x16x32_bf16 v[176:179], a[128:131], a[64:67], v[176:179]\n\t" "ds_read_b64_tr_b16 a[144:145], %[vr3] offset:7168\n\t" "v_mul_f32 v164, v164, v52\n\t" "v_mul_f32 v165, v165, v53\n\t"
    "v_mfma_f32_16x16x32_bf16 v[176:179], a[132:135], a[80:83], v[176:179]\n\t" "ds_read_b64_tr_b16 a[146:147], %[vr3] offset:10752\n\t" "v_mul_f32 v166, v166, v54\n\t" "v_mul_f32 v167, v167, v55\n\t"
    "v_mfma_f32_16x16x32_bf16 v[180:183], a[128:131], a[68:71], v[180:183]\n\t" "v_cvt_pk_bf16_f32 v38, v164, v165\n\t" "v_cvt_pk_bf16_f32 v39, v166, v167\n\t"
    "v_mfma_f32_16x16x32_bf16 v[180:183], a[132:135], a[84:87], v[180:183]\n\t" "global_store_dwordx4 %[va], v[36:39], s[90:91] offset:1024 sc1\n\t" "v_mul_f32_e64 v56, v56, v18\n\t"
    "v_mfma_f32_16x16x32_bf16 v[184:187], a[128:131], a[72:75], v[184:187]\n\t" "ds_read_b64_tr_b16 a[148:149], %[vr3] offset:21504\n\t" "v_mul_f32 v57, v57, v18\n\t" "v_mul_f32 v58, v58, v18\n\t"
    "v_mfma_f32_16x16x32_bf16 v[184:187], a[132:135], a[88:91], v[184:187]\n\t" "ds_read_b64_tr_b16 a[150:151], %[vr3] offset:25088\n\t" "v_mul_f32 v59, v59, v18\n\t" "v_exp_f32 v56, v56\n\t"
    "v_mfma_f32_16x16x32_bf16 v[188:191], a[128:131], a[76:79], v[188:191]\n\t" "s_add_u32 m0, %[sld], 40960\n\t" "s_nop 0\n\t" "global_load_lds_dwordx4 v29, %[sbw] offset:0\n\t" "v_exp_f32 v57, v57\n\t"
    "v_mfma_f32_16x16x32_bf16 v[188:191], a[132:135], a[92:95], v[188:191]\n\t" "v_exp_f32 v58, v58\n\t" "s_waitcnt lgkmcnt(4)\n\t" "v_mfma_f32_16x16x32_bf16 v[64:67], a[136:139], a[96:99], v[64:67]\n\t"
    "ds_read_b128 a[152:155], v31 offset:512\n\t" "v_exp_f32_e64 v59, v59\n\t" "v_mfma_f32_16x16x32_bf16 v[64:67], a[140:143], a[112:115], v[64:67]\n\t" "v_fma_f32 v56, v56, v22, v22\n\t"
    "v_fma_f32 v57, v57, v22, v22\n\t" "v_mfma_f32_16x16x32_bf16 v[68:71], a[136:139], a[100:103], v[68:71]\n\t" "v_fma_f32 v58, v58, v22, v22\n\t" "v_fma_f32 v59, v59, v22, v22\n\t"
    "v_mfma_f32_16x16x32_bf16 v[68:71], a[140:143], a[116:119], v[68:71]\n\t" "v_rcp_f32_e64 v56, v56\n\t" "v_mfma_f32_16x16x32_bf16 v[72:75], a[136:139], a[104:107], v[72:75]\n\t"
    "ds_read_b128 a[156:159], v31 offset:768\n\t" "v_rcp_f32_e64 v57, v57\n\t" "v_mfma_f32_16x16x32_bf16 v[72:75], a[140:143], a[120:123], v[72:75]\n\t" "v_rcp_f32_e64 v58, v58\n\t"
    "v_mfma_f32_16x16x32_bf16 v[76:79], a[136:139], a[108:111], v[76:79]\n\t" "s_add_u32 m0, %[sld], 44928\n\t" "s_nop 0\n\t" "global_load_lds_dwordx4 v29, %[sbw] offset:128\n\t"
    "v_add_u32 v29, 0x40000, v29\n\t" "v_rcp_f32 v59, v59\n\t" "v_mfma_f32_16x16x32_bf16 v[76:79], a[140:143], a[124:127], v[76:79]\n\t" "v_mul_f32 v168, v168, v56\n\t" "v_mul_f32_e64 v169, v169, v57\n\t"
    "s_waitcnt lgkmcnt(2)\n\t" "v_mfma_f32_16x16x32_bf16 v[176:179], a[144:147], a[96:99], v[176:179]\n\t" "ds_read_b64_tr_b16 a[128:129], %[vr2] offset:32\n\t" "v_mul_f32 v170, v170, v58\n\t"
    "v_mul_f32 v171, v171, v59\n\t" "v_mfma_f32_16x16x32_bf16 v[176:179], a[148:151], a[112:115], v[176:179]\n\t" "ds_read_b64_tr_b16 a[130:131], %[vr2] offset:3616\n\t"
    "v_cvt_pk_bf16_f32 v42, v168, v169\n\t" "v_cvt_pk_bf16_f32 v43, v170, v171\n\t" "v_mfma_f32_16x16x32_bf16 v[180:183], a[144:147], a[100:103], v[180:183]\n\t"
    "global_store_dwordx4 %[va], v[40:43], s[92:93] offset:0 sc1\n\t" "v_mul_f32_e64 v60, v60, v19\n\t" "v_mfma_f32_16x16x32_bf16 v[180:183], a[148:151], a[116:119], v[180:183]\n\t"
    "v_mul_f32 v61, v61, v19\n\t" "v_mul_f32 v62, v62, v19\n\t" "v_mfma_f32_16x16x32_bf16 v[184:187], a[144:147], a[104:107], v[184:187]\n\t" "ds_read_b64_tr_b16 a[132:133], %[vr2] offset:14368\n\t"
    "v_mul_f32 v63, v63, v19\n\t" "v_exp_f32 v60, v60\n\t" "v_mfma_f32_16x16x32_bf16 v[184:187], a[148:151], a[120:123], v[184:187]\n\t" "ds_read_b64_tr_b16 a[134:135], %[vr2] offset:17952\n\t"
    "v_exp_f32_e64 v61, v61\n\t" "v_mfma_f32_16x16x32_bf16 v[188:191], a[144:147], a[108:111], v[188:191]\n\t" "s_add_u32 m0, %[sld], 139264\n\t" "s_nop 0\n\t"
    "global_load_lds_dwordx4 v29, %[sbw] offset:0\n\t" "v_exp_f32 v62, v62\n\t" "v_mfma_f32_16x16x32_bf16 v[188:191], a[148:151], a[124:127], v[188:191]\n\t" "v_exp_f32 v63, v63\n\t"
    "s_waitcnt lgkmcnt(4)\n\t" "v_mfma_f32_16x16x32_bf16 v[80:83], a[152:155], a[200:203], v[80:83]\n\t" "ds_read_b64_tr_b16 a[136:137], %[vr2] offset:7200\n\t" "v_fma_f32 v60, v60, v23, v23\n\t"
    "v_fma_f32 v61, v61, v23, v23\n\t" "v_mfma_f32_16x16x32_bf16 v[84:87], a[152:155], a[204:207], v[84:87]\n\t" "ds_read_b64_tr_b16 a[138:139], %[vr2] offset:10784\n\t" "v_fma_f32 v62, v62, v23, v23\n\t"
    "v_fma_f32 v63, v63, v23, v23\n\t" "v_mfma_f32_16x16x32_bf16 v[88:91], a[152:155], a[208:211], v[88:91]\n\t" "v_rcp_f32_e64 v60, v60\n\t"
    "v_mfma_f32_16x16x32_bf16 v[92:95], a[152:155], a[212:215], v[92:95]\n\t" "v_rcp_f32_e64 v61, v61\n\t" "v_mfma_f32_16x16x32_bf16 v[192:195], a[156:159], a[200:203], v[192:195]\n\t"
    "ds_read_b64_tr_b16 a[140:141], %[vr2] offset:21536\n\t" "v_rcp_f32_e64 v62, v62\n\t" "v_mfma_f32_16x16x32_bf16 v[196:199], a[156:159], a[204:207], v[196:199]\n\t"
    "ds_read_b64_tr_b16 a[142:143], %[vr2] offset:25120\n\t" "v_rcp_f32_e64 v63, v63\n\t" "v_mfma_f32_16x16x32_bf16 v[200:203], a[156:159], a[208:211], v[200:203]\n\t" "s_add_u32 m0, %[sld], 143232\n\t"
    "s_nop 0\n\t" "global_load_lds_dwordx4 v29, %[sbw] offset:128\n\t" "v_add_u32 v29, 0x30000, v29\n\t" "v_mul_f32 v172, v172, v60\n\t" "v_mul_f32_e64 v173, v173, v61\n\t"
    "v_mfma_f32_16x16x32_bf16 v[204:207], a[156:159], a[212:215], v[204:207]\n\t" "v_mul_f32 v174, v174, v62\n\t" "v_mul_f32_e64 v175, v175, v63\n\t" "s_waitcnt lgkmcnt(4)\n\t"
    "v_mfma_f32_16x16x32_bf16 v[80:83], a[128:131], a[32:35], v[80:83]\n\t" "ds_read_b64_tr_b16 a[144:145], %[vr2] offset:28704\n\t" "v_cvt_pk_bf16_f32 v46, v172, v173\n\t"
    "v_cvt_pk_bf16_f32 v47, v174, v175\n\t" "v_mfma_f32_16x16x32_bf16 v[80:83], a[132:135], a[48:51], v[80:83]\n\t" "ds_read_b64_tr_b16 a[146:147], %[vr2] offset:32288\n\t"
    "global_store_dwordx4 %[va], v[44:47], s[92:93] offset:1024 sc1\n\t" "v_mul_f32_e64 v64, v64, v16\n\t" "v_mfma_f32_16x16x32_bf16 v[84:87], a[128:131], a[36:39], v[84:87]\n\t"
    "v_mul_f32 v65, v65, v16\n\t" "v_mul_f32 v66, v66, v16\n\t" "v_mfma_f32_16x16x32_bf16 v[84:87], a[132:135], a[52:55], v[84:87]\n\t" "v_mul_f32 v67, v67, v16\n\t" "v_exp_f32 v64, v64\n\t"
    "v_mfma_f32_16x16x32_bf16 v[88:91], a[128:131], a[40:43], v[88:91]\n\t" "ds_read_b64_tr_b16 a[148:149], %[vr2] offset:43040\n\t" "v_exp_f32_e64 v65, v65\n\t"
    "v_mfma_f32_16x16x32_bf16 v[88:91], a[132:135], a[56:59], v[88:91]\n\t" "ds_read_b64_tr_b16 a[150:151], %[vr2] offset:46624\n\t" "v_exp_f32_e64 v66, v66\n\t"
    "v_mfma_f32_16x16x32_bf16 v[92:95], a[128:131], a[44:47], v[92:95]\n\t" "s_add_u32 m0, %[sld], 147456\n\t" "s_nop 0\n\t" "global_load_lds_dwordx4 v29, %[sbw] offset:0\n\t" "v_exp_f32 v67, v67\n\t"
    "v_mfma_f32_16x16x32_bf16 v[92:95], a[132:135], a[60:63], v[92:95]\n\t" "v_fma_f32 v64, v64, v20, v20\n\t" "v_fma_f32 v65, v65, v20, v20\n\t" "s_waitcnt lgkmcnt(4)\n\t"
    "v_mfma_f32_16x16x32_bf16 v[192:195], a[136:139], a[32:35], v[192:195]\n\t" "ds_read_b64_tr_b16 a[152:153], %[vr2] offset:35872\n\t" "v_fma_f32 v66, v66, v20, v20\n\t"
    "v_fma_f32 v67, v67, v20, v20\n\t" "v_mfma_f32_16x16x32_bf16 v[192:195], a[140:143], a[48:51], v[192:195]\n\t" "ds_read_b64_tr_b16 a[154:155], %[vr2] offset:39456\n\t" "v_rcp_f32 v64, v64\n\t"
    "v_mfma_f32_16x16x32_bf16 v[196:199], a[136:139], a[36:39], v[196:199]\n\t" "v_rcp_f32_e64 v65, v65\n\t" "v_mfma_f32_16x16x32_bf16 v[196:199], a[140:143], a[52:55], v[196:199]\n\t"
    "v_rcp_f32_e64 v66, v66\n\t" "v_mfma_f32_16x16x32_bf16 v[200:203], a[136:139], a[40:43], v[200:203]\n\t" "ds_read_b64_tr_b16 a[156:157], %[vr2] offset:50208\n\t" "v_rcp_f32_e64 v67, v67\n\t"
    "v_mfma_f32_16x16x32_bf16 v[200:203], a[140:143], a[56:59], v[200:203]\n\t" "ds_read_b64_tr_b16 a[158:159], %[vr2] offset:53792\n\t" "v_mul_f32 v176, v176, v64\n\t" "v_mul_f32 v177, v177, v65\n\t"
    "v_mfma_f32_16x16x32_bf16 v[204:207], a[136:139], a[44:47], v[204:207]\n\t" "s_add_u32 m0, %[sld], 151424\n\t" "s_nop 0\n\t" "global_load_lds_dwordx4 v29, %[sbw] offset:128\n\t"
    "v_add_u32 v29, 0x40000, v29\n\t" "v_mul_f32 v178, v178, v66\n\t" "v_mul_f32_e64 v179, v179, v67\n\t" "v_mfma_f32_16x16x32_bf16 v[204:207], a[140:143], a[60:63], v[204:207]\n\t"
    "v_cvt_pk_bf16_f32 v64, v176, v177\n\t" "v_cvt_pk_bf16_f32 v65, v178, v179\n\t" "s_waitcnt lgkmcnt(4)\n\t" "v_mfma_f32_16x16x32_bf16 v[80:83], a[144:147], a[64:67], v[80:83]\n\t"
    "ds_read_b64_tr_b16 a[128:129], %[vr3] offset:32\n\t" "v_mul_f32 v68, v68, v17\n\t" "v_mul_f32_e64 v69, v69, v17\n\t" "v_mfma_f32_16x16x32_bf16 v[80:83], a[148:151], a[80:83], v[80:83]\n\t"
    "ds_read_b64_tr_b16 a[130:131], %[vr3] offset:3616\n\t" "v_mul_f32 v70, v70, v17\n\t" "v_mul_f32 v71, v71, v17\n\t" "v_mfma_f32_16x16x32_bf16 v[84:87], a[144:147], a[68:71], v[84:87]\n\t"
    "v_exp_f32_e64 v68, v68\n\t" "v_mfma_f32_16x16x32_bf16 v[84:87], a[148:151], a[84:87], v[84:87]\n\t" "v_exp_f32_e64 v69, v69\n\t"
    "v_mfma_f32_16x16x32_bf16 v[88:91], a[144:147], a[72:75], v[88:91]\n\t" "ds_read_b64_tr_b16 a[132:133], %[vr3] offset:14368\n\t" "v_exp_f32_e64 v70, v70\n\t"
    "v_mfma_f32_16x16x32_bf16 v[88:91], a[148:151], a[88:91], v[88:91]\n\t" "ds_read_b64_tr_b16 a[134:135], %[vr3] offset:17952\n\t" "v_exp_f32_e64 v71, v71\n\t"
    "v_mfma_f32_16x16x32_bf16 v[92:95], a[144:147], a[76:79], v[92:95]\n\t" "s_add_u32 m0, %[sld], 155648\n\t" "s_nop 0\n\t" "global_load_lds_dwordx4 v29, %[sbw] offset:0\n\t"
    "v_fma_f32 v68, v68, v21, v21\n\t" "v_fma_f32 v69, v69, v21, v21\n\t" "v_mfma_f32_16x16x32_bf16 v[92:95], a[148:151], a[92:95], v[92:95]\n\t" "v_fma_f32 v70, v70, v21, v21\n\t"
    "v_fma_f32 v71, v71, v21, v21\n\t" "s_waitcnt lgkmcnt(4)\n\t" "v_mfma_f32_16x16x32_bf16 v[192:195], a[152:155], a[64:67], v[192:195]\n\t" "ds_read_b64_tr_b16 a[136:137], %[vr3] offset:7200\n\t"
    "v_rcp_f32_e64 v68, v68\n\t" "v_mfma_f32_16x16x32_bf16 v[192:195], a[156:159], a[80:83], v[192:195]\n\t" "ds_read_b64_tr_b16 a[138:139], %[vr3] offset:10784\n\t" "v_rcp_f32_e64 v69, v69\n\t"
    "v_mfma_f32_16x16x32_bf16 v[196:199], a[152:155], a[68:71], v[196:199]\n\t" "v_rcp_f32_e64 v70, v70\n\t" "v_mfma_f32_16x16x32_bf16 v[196:199], a[156:159], a[84:87], v[196:199]\n\t"
    "v_rcp_f32_e64 v71, v71\n\t" "v_mfma_f32_16x16x32_bf16 v[200:203], a[152:155], a[72:75], v[200:203]\n\t" "ds_read_b64_tr_b16 a[140:141], %[vr3] offset:21536\n\t" "v_mul_f32 v180, v180, v68\n\t"
    "v_mul_f32 v181, v181, v69\n\t" "v_mfma_f32_16x16x32_bf16 v[200:203], a[156:159], a[88:91], v[200:203]\n\t" "ds_read_b64_tr_b16 a[142:143], %[vr3] offset:25120\n\t" "v_mul_f32 v182, v182, v70\n\t"
    "v_mul_f32 v183, v183, v71\n\t" "v_mfma_f32_16x16x32_bf16 v[204:207], a[152:155], a[76:79], v[204:207]\n\t" "s_add_u32 m0, %[sld], 159616\n\t" "s_nop 0\n\t"
    "global_load_lds_dwordx4 v29, %[sbw] offset:128\n\t" "v_add_u32 v29, 0x30000, v29\n\t" "v_cvt_pk_bf16_f32 v68, v180, v181\n\t" "v_cvt_pk_bf16_f32 v69, v182, v183\n\t"
    "v_mfma_f32_16x16x32_bf16 v[204:207], a[156:159], a[92:95], v[204:207]\n\t" "v_mul_f32 v72, v72, v18\n\t" "v_mul_f32 v73, v73, v18\n\t" "s_waitcnt lgkmcnt(4)\n\t"
    "v_mfma_f32_16x16x32_bf16 v[80:83], a[128:131], a[96:99], v[80:83]\n\t" "v_accvgpr_read_b32 v31, %[ct]\n\t" "s_mov_b64 exec, 0xffff\n\t" "v_add_u32 v31, %[gt2], v31\n\t" "s_mov_b64 exec, -1\n\t"
    "ds_read_b128 a[144:147], v31 offset:0\n\t" "v_mul_f32 v74, v74, v18\n\t" "v_mul_f32 v75, v75, v18\n\t" "v_mfma_f32_16x16x32_bf16 v[80:83], a[132:135], a[112:115], v[80:83]\n\t"
    "v_exp_f32_e64 v72, v72\n\t" "v_mfma_f32_16x16x32_bf16 v[84:87], a[128:131], a[100:103], v[84:87]\n\t" "v_exp_f32_e64 v73, v73\n\t"
    "v_mfma_f32_16x16x32_bf16 v[84:87], a[132:135], a[116:119], v[84:87]\n\t" "v_exp_f32_e64 v74, v74\n\t" "v_mfma_f32_16x16x32_bf16 v[88:91], a[128:131], a[104:107], v[88:91]\n\t"
    "ds_read_b128 a[148:151], v31 offset:256\n\t" "v_exp_f32_e64 v75, v75\n\t" "v_mfma_f32_16x16x32_bf16 v[88:91], a[132:135], a[120:123], v[88:91]\n\t" "v_fma_f32 v72, v72, v22, v22\n\t"
    "v_fma_f32 v73, v73, v22, v22\n\t" "v_mfma_f32_16x16x32_bf16 v[92:95], a[128:131], a[108:111], v[92:95]\n\t" "s_add_u32 m0, %[sld], 0\n\t" "s_nop 0\n\t"
    "global_load_lds_dwordx4 v29, %[sbw] offset:0\n\t" "v_fma_f32 v74, v74, v22, v22\n\t" "v_fma_f32 v75, v75, v22, v22\n\t" "v_mfma_f32_16x16x32_bf16 v[92:95], a[132:135], a[124:127], v[92:95]\n\t"
    "v_rcp_f32 v72, v72\n\t" "s_waitcnt lgkmcnt(2)\n\t" "v_mfma_f32_16x16x32_bf16 v[192:195], a[136:139], a[96:99], v[192:195]\n\t" "ds_read_b64_tr_b16 a[152:153], %[vr4] offset:0\n\t"
    "v_rcp_f32_e64 v73, v73\n\t" "v_mfma_f32_16x16x32_bf16 v[192:195], a[140:143], a[112:115], v[192:195]\n\t" "ds_read_b64_tr_b16 a[154:155], %[vr4] offset:3584\n\t" "v_rcp_f32_e64 v74, v74\n\t"
    "v_mfma_f32_16x16x32_bf16 v[196:199], a[136:139], a[100:103], v[196:199]\n\t" "v_rcp_f32_e64 v75, v75\n\t" "v_mfma_f32_16x16x32_bf16 v[196:199], a[140:143], a[116:119], v[196:199]\n\t"
    "v_mul_f32 v184, v184, v72\n\t" "v_mul_f32 v185, v185, v73\n\t" "v_mfma_f32_16x16x32_bf16 v[200:203], a[136:139], a[104:107], v[200:203]\n\t" "ds_read_b64_tr_b16 a[156:157], %[vr4] offset:14336\n\t"
    "v_mul_f32 v186, v186, v74\n\t" "v_mul_f32 v187, v187, v75\n\t" "v_mfma_f32_16x16x32_bf16 v[200:203], a[140:143], a[120:123], v[200:203]\n\t" "ds_read_b64_tr_b16 a[158:159], %[vr4] offset:17920\n\t"
    "v_cvt_pk_bf16_f32 v72, v184, v185\n\t" "v_cvt_pk_bf16_f32 v73, v186, v187\n\t" "v_mfma_f32_16x16x32_bf16 v[204:207], a[136:139], a[108:111], v[204:207]\n\t" "s_add_u32 m0, %[sld], 3968\n\t"
    "s_nop 0\n\t" "global_load_lds_dwordx4 v29, %[sbw] offset:128\n\t" "v_add_u32 v29, 0x40000, v29\n\t" "v_mul_f32 v76, v76, v19\n\t" "v_mul_f32_e64 v77, v77, v19\n\t"
    "v_mfma_f32_16x16x32_bf16 v[204:207], a[140:143], a[124:127], v[204:207]\n\t" "v_mul_f32 v78, v78, v19\n\t" "v_mul_f32_e64 v79, v79, v19\n\t" "s_waitcnt lgkmcnt(4)\n\t"
    "v_mfma_f32_16x16x32_bf16 v[96:99], a[144:147], a[200:203], v[96:99]\n\t" "ds_read_b64_tr_b16 a[128:129], %[vr4] offset:7168\n\t" "v_exp_f32_e64 v76, v76\n\t"
    "v_mfma_f32_16x16x32_bf16 v[100:103], a[144:147], a[204:207], v[100:103]\n\t" "ds_read_b64_tr_b16 a[130:131], %[vr4] offset:10752\n\t" "v_exp_f32_e64 v77, v77\n\t"
    "v_mfma_f32_16x16x32_bf16 v[104:107], a[144:147], a[208:211], v[104:107]\n\t" "s_waitcnt vmcnt(6)\n\t" "s_barrier\n\t" "v_mov_b32 v28, 0\n\t" "v_mov_b64 v[26:27], %[tag]\n\t"
    "s_cmp_eq_u32 %[wid], 0\n\t" "s_cselect_b64 exec, 1, 0\n\t" "global_store_dwordx2 v28, v[26:27], %[sc] offset:128 sc1\n\t" "s_mov_b64 exec, -1\n\t" "v_exp_f32 v78, v78\n\t"
    "v_mfma_f32_16x16x32_bf16 v[108:111], a[144:147], a[212:215], v[108:111]\n\t" "v_exp_f32_e64 v79, v79\n\t" "v_mfma_f32_16x16x32_bf16 v[208:211], a[148:151], a[200:203], v[208:211]\n\t"
    "ds_read_b64_tr_b16 a[132:133], %[vr4] offset:21504\n\t" "v_fma_f32 v76, v76, v23, v23\n\t" "v_fma_f32 v77, v77, v23, v23\n\t"
    "v_mfma_f32_16x16x32_bf16 v[212:215], a[148:151], a[204:207], v[212:215]\n\t" "ds_read_b64_tr_b16 a[134:135], %[vr4] offset:25088\n\t" "v_fma_f32 v78, v78, v23, v23\n\t"
    "v_fma_f32 v79, v79, v23, v23\n\t" "v_mfma_f32_16x16x32_bf16 v[216:219], a[148:151], a[208:211], v[216:219]\n\t" "s_add_u32 m0, %[sld], 8192\n\t" "s_nop 0\n\t"
    "global_load_lds_dwordx4 v29, %[sbw] offset:0\n\t" "v_rcp_f32 v76, v76\n\t" "v_mfma_f32_16x16x32_bf16 v[220:223], a[148:151], a[212:215], v[220:223]\n\t" "v_rcp_f32 v77, v77\n\t"
    "s_waitcnt lgkmcnt(4)\n\t" "v_mfma_f32_16x16x32_bf16 v[96:99], a[152:155], a[32:35], v[96:99]\n\t" "ds_read_b64_tr_b16 a[136:137], %[vr4] offset:28672\n\t" "v_rcp_f32_e64 v78, v78\n\t"
    "v_mfma_f32_16x16x32_bf16 v[96:99], a[156:159], a[48:51], v[96:99]\n\t" "ds_read_b64_tr_b16 a[138:139], %[vr4] offset:32256\n\t" "v_rcp_f32_e64 v79, v79\n\t"
    "v_mfma_f32_16x16x32_bf16 v[100:103], a[152:155], a[36:39], v[100:103]\n\t" "v_mul_f32 v188, v188, v76\n\t" "v_mul_f32 v189, v189, v77\n\t"
    "v_mfma_f32_16x16x32_bf16 v[100:103], a[156:159], a[52:55], v[100:103]\n\t" "v_mul_f32 v190, v190, v78\n\t" "v_mul_f32 v191, v191, v79\n\t"
    "v_mfma_f32_16x16x32_bf16 v[104:107], a[152:155], a[40:43], v[104:107]\n\t" "ds_read_b64_tr_b16 a[140:141], %[vr4] offset:43008\n\t" "v_cvt_pk_bf16_f32 v76, v188, v189\n\t"
    "v_cvt_pk_bf16_f32 v77, v190, v191\n\t" "v_mfma_f32_16x16x32_bf16 v[104:107], a[156:159], a[56:59], v[104:107]\n\t" "ds_read_b64_tr_b16 a[142:143], %[vr4] offset:46592\n\t"
    "s_add_u32 s90, %[shl], %[go1]\n\t" "s_addc_u32 s91, %[shh], 0\n\t" "s_add_u32 s92, s90, 229376\n\t" "s_addc_u32 s93, s91, 0\n\t"
    "v_mfma_f32_16x16x32_bf16 v[108:111], a[152:155], a[44:47], v[108:111]\n\t" "s_add_u32 m0, %[sld], 12160\n\t" "s_nop 0\n\t" "global_load_lds_dwordx4 v29, %[sbw] offset:128\n\t"
    "v_add_u32 v29, 0x30000, v29\n\t" "v_mul_f32 v80, v80, v16\n\t" "v_mul_f32 v81, v81, v16\n\t" "v_mfma_f32_16x16x32_bf16 v[108:111], a[156:159], a[60:63], v[108:111]\n\t" "v_mul_f32 v82, v82, v16\n\t"
    "v_mul_f32_e64 v83, v83, v16\n\t" "s_waitcnt lgkmcnt(4)\n\t" "v_mfma_f32_16x16x32_bf16 v[208:211], a[128:131], a[32:35], v[208:211]\n\t" "ds_read_b64_tr_b16 a[144:145], %[vr4] offset:35840\n\t"
    "v_exp_f32_e64 v80, v80\n\t" "v_mfma_f32_16x16x32_bf16 v[208:211], a[132:135], a[48:51], v[208:211]\n\t" "ds_read_b64_tr_b16 a[146:147], %[vr4] offset:39424\n\t" "v_exp_f32_e64 v81, v81\n\t"
    "v_mfma_f32_16x16x32_bf16 v[212:215], a[128:131], a[36:39], v[212:215]\n\t" "v_exp_f32_e64 v82, v82\n\t" "v_mfma_f32_16x16x32_bf16 v[212:215], a[132:135], a[52:55], v[212:215]\n\t"
    "v_exp_f32_e64 v83, v83\n\t" "v_mfma_f32_16x16x32_bf16 v[216:219], a[128:131], a[40:43], v[216:219]\n\t" "ds_read_b64_tr_b16 a[148:149], %[vr4] offset:50176\n\t" "v_fma_f32 v80, v80, v20, v20\n\t"
    "v_fma_f32 v81, v81, v20, v20\n\t" "v_mfma_f32_16x16x32_bf16 v[216:219], a[132:135], a[56:59], v[216:219]\n\t" "ds_read_b64_tr_b16 a[150:151], %[vr4] offset:53760\n\t"
    "v_fma_f32 v82, v82, v20, v20\n\t" "v_fma_f32 v83, v83, v20, v20\n\t" "v_mfma_f32_16x16x32_bf16 v[220:223], a[128:131], a[44:47], v[220:223]\n\t" "v_rcp_f32_e64 v80, v80\n\t"
    "v_mfma_f32_16x16x32_bf16 v[220:223], a[132:135], a[60:63], v[220:223]\n\t" "v_rcp_f32 v81, v81\n\t" "s_waitcnt lgkmcnt(4)\n\t" "v_mfma_f32_16x16x32_bf16 v[96:99], a[136:139], a[64:67], v[96:99]\n\t"
    "ds_read_b64_tr_b16 a[152:153], %[vr5] offset:0\n\t" "v_rcp_f32_e64 v82, v82\n\t" "v_mfma_f32_16x16x32_bf16 v[96:99], a[140:143], a[80:83], v[96:99]\n\t"
    "ds_read_b64_tr_b16 a[154:155], %[vr5] offset:3584\n\t" "v_rcp_f32_e64 v83, v83\n\t" "v_mfma_f32_16x16x32_bf16 v[100:103], a[136:139], a[68:71], v[100:103]\n\t" "v_mul_f32 v192, v192, v80\n\t"
    "v_mul_f32 v193, v193, v81\n\t" "v_mfma_f32_16x16x32_bf16 v[100:103], a[140:143], a[84:87], v[100:103]\n\t" "v_mul_f32 v194, v194, v82\n\t" "v_mul_f32 v195, v195, v83\n\t"
    "v_mfma_f32_16x16x32_bf16 v[104:107], a[136:139], a[72:75], v[104:107]\n\t" "ds_read_b64_tr_b16 a[156:157], %[vr5] offset:14336\n\t" "v_cvt_pk_bf16_f32 v66, v192, v193\n\t"
    "v_cvt_pk_bf16_f32 v67, v194, v195\n\t" "v_mfma_f32_16x16x32_bf16 v[104:107], a[140:143], a[88:91], v[104:107]\n\t" "ds_read_b64_tr_b16 a[158:159], %[vr5] offset:17920\n\t"
    "global_store_dwordx4 %[va], v[64:67], s[90:91] offset:0 sc1\n\t" "v_mul_f32_e64 v84, v84, v17\n\t" "v_mfma_f32_16x16x32_bf16 v[108:111], a[136:139], a[76:79], v[108:111]\n\t"
    "v_mul_f32 v85, v85, v17\n\t" "v_mul_f32 v86, v86, v17\n\t" "v_mfma_f32_16x16x32_bf16 v[108:111], a[140:143], a[92:95], v[108:111]\n\t" "v_mul_f32 v87, v87, v17\n\t" "v_exp_f32_e64 v84, v84\n\t"
    "s_waitcnt lgkmcnt(4)\n\t" "v_mfma_f32_16x16x32_bf16 v[208:211], a[144:147], a[64:67], v[208:211]\n\t" "ds_read_b64_tr_b16 a[128:129], %[vr5] offset:7168\n\t" "v_exp_f32_e64 v85, v85\n\t"
    "v_mfma_f32_16x16x32_bf16 v[208:211], a[148:151], a[80:83], v[208:211]\n\t" "ds_read_b64_tr_b16 a[130:131], %[vr5] offset:10752\n\t" "v_exp_f32_e64 v86, v86\n\t"
    "v_mfma_f32_16x16x32_bf16 v[212:215], a[144:147], a[68:71], v[212:215]\n\t" "v_exp_f32_e64 v87, v87\n\t" "v_mfma_f32_16x16x32_bf16 v[212:215], a[148:151], a[84:87], v[212:215]\n\t"
    "v_fma_f32 v84, v84, v21, v21\n\t" "v_fma_f32 v85, v85, v21, v21\n\t" "v_mfma_f32_16x16x32_bf16 v[216:219], a[144:147], a[72:75], v[216:219]\n\t"
    "ds_read_b64_tr_b16 a[132:133], %[vr5] offset:21504\n\t" "v_fma_f32 v86, v86, v21, v21\n\t" "v_fma_f32 v87, v87, v21, v21\n\t"
    "v_mfma_f32_16x16x32_bf16 v[216:219], a[148:151], a[88:91], v[216:219]\n\t" "ds_read_b64_tr_b16 a[134:135], %[vr5] offset:25088\n\t" "v_rcp_f32_e64 v84, v84\n\t"
    "v_mfma_f32_16x16x32_bf16 v[220:223], a[144:147], a[76:79], v[220:223]\n\t" "v_rcp_f32_e64 v85, v85\n\t" "v_mfma_f32_16x16x32_bf16 v[220:223], a[148:151], a[92:95], v[220:223]\n\t"
    "v_rcp_f32 v86, v86\n\t" "s_waitcnt lgkmcnt(4)\n\t" "v_mfma_f32_16x16x32_bf16 v[96:99], a[152:155], a[96:99], v[96:99]\n\t" "ds_read_b128 a[136:139], v31 offset:512\n\t" "v_rcp_f32_e64 v87, v87\n\t"
    "v_mfma_f32_16x16x32_bf16 v[96:99], a[156:159], a[112:115], v[96:99]\n\t" "v_mul_f32 v196, v196, v84\n\t" "v_mul_f32 v197, v197, v85\n\t"
    "v_mfma_f32_16x16x32_bf16 v[100:103], a[152:155], a[100:103], v[100:103]\n\t" "v_mul_f32 v198, v198, v86\n\t" "v_mul_f32 v199, v199, v87\n\t"
    "v_mfma_f32_16x16x32_bf16 v[100:103], a[156:159], a[116:119], v[100:103]\n\t" "v_cvt_pk_bf16_f32 v70, v196, v197\n\t" "v_cvt_pk_bf16_f32 v71, v198, v199\n\t"
    "v_mfma_f32_16x16x32_bf16 v[104:107], a[152:155], a[104:107], v[104:107]\n\t" "ds_read_b128 a[140:143], v31 offset:768\n\t" "global_store_dwordx4 %[va], v[68:71], s[90:91] offset:1024 sc1\n\t"
    "v_mul_f32_e64 v88, v88, v18\n\t" "v_mfma_f32_16x16x32_bf16 v[104:107], a[156:159], a[120:123], v[104:107]\n\t" "v_mul_f32 v89, v89, v18\n\t" "v_mul_f32 v90, v90, v18\n\t"
    "v_mfma_f32_16x16x32_bf16 v[108:111], a[152:155], a[108:111], v[108:111]\n\t" "v_mul_f32 v91, v91, v18\n\t" "v_exp_f32 v88, v88\n\t"
    "v_mfma_f32_16x16x32_bf16 v[108:111], a[156:159], a[124:127], v[108:111]\n\t" "v_exp_f32 v89, v89\n\t" "s_waitcnt lgkmcnt(2)\n\t"
    "v_mfma_f32_16x16x32_bf16 v[208:211], a[128:131], a[96:99], v[208:211]\n\t" "ds_read_b64_tr_b16 a[144:145], %[vr4] offset:32\n\t" "v_exp_f32_e64 v90, v90\n\t"
    "v_mfma_f32_16x16x32_bf16 v[208:211], a[132:135], a[112:115], v[208:211]\n\t" "ds_read_b64_tr_b16 a[146:147], %[vr4] offset:3616\n\t" "v_exp_f32_e64 v91, v91\n\t"
    "v_mfma_f32_16x16x32_bf16 v[212:215], a[128:131], a[100:103], v[212:215]\n\t" "v_fma_f32 v88, v88, v22, v22\n\t" "v_fma_f32 v89, v89, v22, v22\n\t"
    "v_mfma_f32_16x16x32_bf16 v[212:215], a[132:135], a[116:119], v[212:215]\n\t" "v_fma_f32 v90, v90, v22, v22\n\t" "v_fma_f32 v91, v91, v22, v22\n\t"
    "v_mfma_f32_16x16x32_bf16 v[216:219], a[128:131], a[104:107], v[216:219]\n\t" "ds_read_b64_tr_b16 a[148:149], %[vr4] offset:14368\n\t" "v_rcp_f32_e64 v88, v88\n\t"
    "v_mfma_f32_16x16x32_bf16 v[216:219], a[132:135], a[120:123], v[216:219]\n\t" "ds_read_b64_tr_b16 a[150:151], %[vr4] offset:17952\n\t" "v_rcp_f32_e64 v89, v89\n\t"
    "v_mfma_f32_16x16x32_bf16 v[220:223], a[128:131], a[108:111], v[220:223]\n\t" "v_rcp_f32_e64 v90, v90\n\t" "v_mfma_f32_16x16x32_bf16 v[220:223], a[132:135], a[124:127], v[220:223]\n\t"
    "v_rcp_f32 v91, v91\n\t" "s_waitcnt lgkmcnt(4)\n\t" "v_mfma_f32_16x16x32_bf16 v[112:115], a[136:139], a[200:203], v[112:115]\n\t" "ds_read_b64_tr_b16 a[152:153], %[vr4] offset:7200\n\t"
    "v_mul_f32 v200, v200, v88\n\t" "v_mul_f32 v201, v201, v89\n\t" "v_mfma_f32_16x16x32_bf16 v[116:119], a[136:139], a[204:207], v[116:119]\n\t" "ds_read_b64_tr_b16 a[154:155], %[vr4] offset:10784\n\t"
    "v_mul_f32 v202, v202, v90\n\t" "v_mul_f32 v203, v203, v91\n\t" "v_mfma_f32_16x16x32_bf16 v[120:123], a[136:139], a[208:211], v[120:123]\n\t" "v_cvt_pk_bf16_f32 v74, v200, v201\n\t"
    "v_cvt_pk_bf16_f32 v75, v202, v203\n\t" "v_mfma_f32_16x16x32_bf16 v[124:127], a[136:139], a[212:215], v[124:127]\n\t" "global_store_dwordx4 %[va], v[72:75], s[92:93] offset:0 sc1\n\t"
    "v_mul_f32_e64 v92, v92, v19\n\t" "v_mfma_f32_16x16x32_bf16 v[224:227], a[140:143], a[200:203], v[224:227]\n\t" "ds_read_b64_tr_b16 a[156:157], %[vr4] offset:21536\n\t" "v_mul_f32 v93, v93, v19\n\t"
    "v_mul_f32 v94, v94, v19\n\t" "v_mfma_f32_16x16x32_bf16 v[228:231], a[140:143], a[204:207], v[228:231]\n\t" "ds_read_b64_tr_b16 a[158:159], %[vr4] offset:25120\n\t" "v_mul_f32 v95, v95, v19\n\t"
    "v_exp_f32 v92, v92\n\t" "v_mfma_f32_16x16x32_bf16 v[232:235], a[140:143], a[208:211], v[232:235]\n\t" "v_exp_f32_e64 v93, v93\n\t"
    "v_mfma_f32_16x16x32_bf16 v[236:239], a[140:143], a[212:215], v[236:239]\n\t" "v_exp_f32 v94, v94\n\t" "s_waitcnt lgkmcnt(4)\n\t"
    "v_mfma_f32_16x16x32_bf16 v[112:115], a[144:147], a[32:35], v[112:115]\n\t" "ds_read_b64_tr_b16 a[128:129], %[vr4] offset:28704\n\t" "v_exp_f32_e64 v95, v95\n\t"
    "v_mfma_f32_16x16x32_bf16 v[112:115], a[148:151], a[48:51], v[112:115]\n\t" "ds_read_b64_tr_b16 a[130:131], %[vr4] offset:32288\n\t" "v_fma_f32 v92, v92, v23, v23\n\t"
    "v_fma_f32 v93, v93, v23, v23\n\t" "v_mfma_f32_16x16x32_bf16 v[116:119], a[144:147], a[36:39], v[116:119]\n\t" "v_fma_f32 v94, v94, v23, v23\n\t" "v_fma_f32 v95, v95, v23, v23\n\t"
    "v_mfma_f32_16x16x32_bf16 v[116:119], a[148:151], a[52:55], v[116:119]\n\t" "v_rcp_f32_e64 v92, v92\n\t" "v_mfma_f32_16x16x32_bf16 v[120:123], a[144:147], a[40:43], v[120:123]\n\t"
    "ds_read_b64_tr_b16 a[132:133], %[vr4] offset:43040\n\t" "v_rcp_f32_e64 v93, v93\n\t" "v_mfma_f32_16x16x32_bf16 v[120:123], a[148:151], a[56:59], v[120:123]\n\t"
    "ds_read_b64_tr_b16 a[134:135], %[vr4] offset:46624\n\t" "v_rcp_f32_e64 v94, v94\n\t" "v_mfma_f32_16x16x32_bf16 v[124:127], a[144:147], a[44:47], v[124:127]\n\t" "v_rcp_f32_e64 v95, v95\n\t"
    "v_mfma_f32_16x16x32_bf16 v[124:127], a[148:151], a[60:63], v[124:127]\n\t" "v_mul_f32 v204, v204, v92\n\t" "v_mul_f32_e64 v205, v205, v93\n\t" "s_waitcnt lgkmcnt(4)\n\t"
    "v_mfma_f32_16x16x32_bf16 v[224:227], a[152:155], a[32:35], v[224:227]\n\t" "ds_read_b64_tr_b16 a[136:137], %[vr4] offset:35872\n\t" "v_mul_f32 v206, v206, v94\n\t" "v_mul_f32 v207, v207, v95\n\t"
    "v_mfma_f32_16x16x32_bf16 v[224:227], a[156:159], a[48:51], v[224:227]\n\t" "ds_read_b64_tr_b16 a[138:139], %[vr4] offset:39456\n\t" "v_cvt_pk_bf16_f32 v78, v204, v205\n\t"
    "v_cvt_pk_bf16_f32 v79, v206, v207\n\t" "v_mfma_f32_16x16x32_bf16 v[228:231], a[152:155], a[36:39], v[228:231]\n\t" "global_store_dwordx4 %[va], v[76:79], s[92:93] offset:1024 sc1\n\t"
    "v_mul_f32_e64 v96, v96, v16\n\t" "v_mfma_f32_16x16x32_bf16 v[228:231], a[156:159], a[52:55], v[228:231]\n\t" "v_mul_f32 v97, v97, v16\n\t" "v_mul_f32 v98, v98, v16\n\t"
    "v_mfma_f32_16x16x32_bf16 v[232:235], a[152:155], a[40:43], v[232:235]\n\t" "ds_read_b64_tr_b16 a[140:141], %[vr4] offset:50208\n\t" "v_mul_f32 v99, v99, v16\n\t" "v_exp_f32 v96, v96\n\t"
    "v_mfma_f32_16x16x32_bf16 v[232:235], a[156:159], a[56:59], v[232:235]\n\t" "ds_read_b64_tr_b16 a[142:143], %[vr4] offset:53792\n\t" "v_exp_f32_e64 v97, v97\n\t"
    "v_mfma_f32_16x16x32_bf16 v[236:239], a[152:155], a[44:47], v[236:239]\n\t" "v_and_b32 v28, 0xf0, %[va]\n\t" "s_mov_b64 s[76:77], %[sd]\n\t" "v_lshlrev_b32 v28, 4, v28\n\t"
    "s_mov_b32 s81, 0x400000\n\t" "global_load_dwordx2 v[24:25], v28, %[sw] offset:128 sc1\n\t" "v_exp_f32_e64 v98, v98\n\t" "v_mfma_f32_16x16x32_bf16 v[236:239], a[156:159], a[60:63], v[236:239]\n\t"
    "v_exp_f32 v99, v99\n\t" "s_waitcnt lgkmcnt(4)\n\t" "v_mfma_f32_16x16x32_bf16 v[112:115], a[128:131], a[64:67], v[112:115]\n\t" "ds_read_b64_tr_b16 a[144:145], %[vr5] offset:32\n\t"
    "v_fma_f32 v96, v96, v20, v20\n\t" "v_fma_f32 v97, v97, v20, v20\n\t" "v_mfma_f32_16x16x32_bf16 v[112:115], a[132:135], a[80:83], v[112:115]\n\t"
    "ds_read_b64_tr_b16 a[146:147], %[vr5] offset:3616\n\t" "v_fma_f32 v98, v98, v20, v20\n\t" "v_fma_f32 v99, v99, v20, v20\n\t"
    "v_mfma_f32_16x16x32_bf16 v[116:119], a[128:131], a[68:71], v[116:119]\n\t" "v_rcp_f32_e64 v96, v96\n\t" "v_mfma_f32_16x16x32_bf16 v[116:119], a[132:135], a[84:87], v[116:119]\n\t"
    "v_rcp_f32_e64 v97, v97\n\t" "v_mfma_f32_16x16x32_bf16 v[120:123], a[128:131], a[72:75], v[120:123]\n\t" "ds_read_b64_tr_b16 a[148:149], %[vr5] offset:14368\n\t" "v_rcp_f32_e64 v98, v98\n\t"
    "v_mfma_f32_16x16x32_bf16 v[120:123], a[132:135], a[88:91], v[120:123]\n\t" "ds_read_b64_tr_b16 a[150:151], %[vr5] offset:17952\n\t" "v_rcp_f32_e64 v99, v99\n\t"
    "v_mfma_f32_16x16x32_bf16 v[124:127], a[128:131], a[76:79], v[124:127]\n\t" "v_mul_f32 v208, v208, v96\n\t" "v_mul_f32 v209, v209, v97\n\t"
    "v_mfma_f32_16x16x32_bf16 v[124:127], a[132:135], a[92:95], v[124:127]\n\t" "v_mul_f32 v210, v210, v98\n\t" "v_mul_f32_e64 v211, v211, v99\n\t" "s_waitcnt lgkmcnt(4)\n\t"
    "v_mfma_f32_16x16x32_bf16 v[224:227], a[136:139], a[64:67], v[224:227]\n\t" "ds_read_b64_tr_b16 a[152:153], %[vr5] offset:7200\n\t" "v_cvt_pk_bf16_f32 v96, v208, v209\n\t"
    "v_cvt_pk_bf16_f32 v97, v210, v211\n\t" "v_mfma_f32_16x16x32_bf16 v[224:227], a[140:143], a[80:83], v[224:227]\n\t" "ds_read_b64_tr_b16 a[154:155], %[vr5] offset:10784\n\t"
    "v_mul_f32 v100, v100, v17\n\t" "v_mul_f32 v101, v101, v17\n\t" "v_mfma_f32_16x16x32_bf16 v[228:231], a[136:139], a[68:71], v[228:231]\n\t" "v_mul_f32 v102, v102, v17\n\t"
    "v_mul_f32 v103, v103, v17\n\t" "v_mfma_f32_16x16x32_bf16 v[228:231], a[140:143], a[84:87], v[228:231]\n\t" "v_exp_f32_e64 v100, v100\n\t"
    "v_mfma_f32_16x16x32_bf16 v[232:235], a[136:139], a[72:75], v[232:235]\n\t" "ds_read_b64_tr_b16 a[156:157], %[vr5] offset:21536\n\t" "v_exp_f32_e64 v101, v101\n\t"
    "v_mfma_f32_16x16x32_bf16 v[232:235], a[140:143], a[88:91], v[232:235]\n\t" "ds_read_b64_tr_b16 a[158:159], %[vr5] offset:25120\n\t" "v_exp_f32_e64 v102, v102\n\t"
    "v_mfma_f32_16x16x32_bf16 v[236:239], a[136:139], a[76:79], v[236:239]\n\t" "v_exp_f32_e64 v103, v103\n\t" "v_mfma_f32_16x16x32_bf16 v[236:239], a[140:143], a[92:95], v[236:239]\n\t"
    "v_fma_f32 v100, v100, v21, v21\n\t" "v_fma_f32 v101, v101, v21, v21\n\t" "s_waitcnt lgkmcnt(4)\n\t" "v_mfma_f32_16x16x32_bf16 v[112:115], a[144:147], a[96:99], v[112:115]\n\t"
    "v_accvgpr_read_b32 v31, %[ct]\n\t" "s_mov_b64 exec, 0xffff\n\t" "v_add_u32_e64 v31, %[gt3], v31\n\t" "s_mov_b64 exec, -1\n\t" "ds_read_b128 a[128:131], v31 offset:0\n\t"
    "v_fma_f32 v102, v102, v21, v21\n\t" "v_fma_f32 v103, v103, v21, v21\n\t" "v_mfma_f32_16x16x32_bf16 v[112:115], a[148:151], a[112:115], v[112:115]\n\t" "v_rcp_f32_e64 v100, v100\n\t"
    "v_mfma_f32_16x16x32_bf16 v[116:119], a[144:147], a[100:103], v[116:119]\n\t" "v_rcp_f32_e64 v101, v101\n\t" "v_mfma_f32_16x16x32_bf16 v[116:119], a[148:151], a[116:119], v[116:119]\n\t"
    "v_rcp_f32_e64 v102, v102\n\t" "v_mfma_f32_16x16x32_bf16 v[120:123], a[144:147], a[104:107], v[120:123]\n\t" "ds_read_b128 a[132:135], v31 offset:256\n\t" "s_waitcnt vmcnt(0)\n\t"
    "v_cmp_ne_u64 vcc, %[tag], v[24:25]\n\t" "v_rcp_f32_e64 v103, v103\n\t" "v_mfma_f32_16x16x32_bf16 v[120:123], a[148:151], a[120:123], v[120:123]\n\t" "v_mul_f32 v212, v212, v100\n\t"
    "v_mul_f32 v213, v213, v101\n\t" "v_mfma_f32_16x16x32_bf16 v[124:127], a[144:147], a[108:111], v[124:127]\n\t" "v_mul_f32 v214, v214, v102\n\t" "v_mul_f32 v215, v215, v103\n\t"
    "v_mfma_f32_16x16x32_bf16 v[124:127], a[148:151], a[124:127], v[124:127]\n\t" "s_nop 1\n\t" "s_cbranch_vccnz .Lph_%=\n\t" ".Lrh_%=:\n\t" "v_cvt_pk_bf16_f32 v100, v212, v213\n\t"
    "v_cvt_pk_bf16_f32 v101, v214, v215\n\t" "s_waitcnt lgkmcnt(2)\n\t" "v_mfma_f32_16x16x32_bf16 v[224:227], a[152:155], a[96:99], v[224:227]\n\t" "ds_read_b64_tr_b16 a[136:137], %[vr6] offset:0\n\t"
    "v_mul_f32 v104, v104, v18\n\t" "v_mul_f32_e64 v105, v105, v18\n\t" "v_mfma_f32_16x16x32_bf16 v[224:227], a[156:159], a[112:115], v[224:227]\n\t"
    "ds_read_b64_tr_b16 a[138:139], %[vr6] offset:3584\n\t" "v_mul_f32 v106, v106, v18\n\t" "v_mul_f32 v107, v107, v18\n\t" "v_mfma_f32_16x16x32_bf16 v[228:231], a[152:155], a[100:103], v[228:231]\n\t"
    "global_load_dword a231, %[va], %[cd]\n\t" "v_exp_f32_e64 v104, v104\n\t" "v_mfma_f32_16x16x32_bf16 v[228:231], a[156:159], a[116:119], v[228:231]\n\t"
    "global_load_dwordx4 a[0:3], %[va], s[76:77] offset:-4096 sc1\n\t" "v_exp_f32_e64 v105, v105\n\t" "v_mfma_f32_16x16x32_bf16 v[232:235], a[152:155], a[104:107], v[232:235]\n\t"
    "ds_read_b64_tr_b16 a[140:141], %[vr6] offset:14336\n\t" "v_exp_f32_e64 v106, v106\n\t" "v_mfma_f32_16x16x32_bf16 v[232:235], a[156:159], a[120:123], v[232:235]\n\t"
    "ds_read_b64_tr_b16 a[142:143], %[vr6] offset:17920\n\t" "v_exp_f32_e64 v107, v107\n\t" "v_mfma_f32_16x16x32_bf16 v[236:239], a[152:155], a[108:111], v[236:239]\n\t"
    "v_fma_f32 v104, v104, v22, v22\n\t" "v_fma_f32 v105, v105, v22, v22\n\t" "v_mfma_f32_16x16x32_bf16 v[236:239], a[156:159], a[124:127], v[236:239]\n\t"
    "global_load_dwordx4 a[4:7], %[va], s[76:77] offset:-3072 sc1\n\t" "v_fma_f32 v106, v106, v22, v22\n\t" "v_fma_f32 v107, v107, v22, v22\n\t" "s_waitcnt lgkmcnt(4)\n\t"
    "v_mfma_f32_16x16x32_bf16 v[128:131], a[128:131], a[200:203], v[128:131]\n\t" "ds_read_b64_tr_b16 a[144:145], %[vr6] offset:7168\n\t" "v_rcp_f32 v104, v104\n\t"
    "v_mfma_f32_16x16x32_bf16 v[132:135], a[128:131], a[204:207], v[132:135]\n\t" "ds_read_b64_tr_b16 a[146:147], %[vr6] offset:10752\n\t" "v_rcp_f32_e64 v105, v105\n\t"
    "v_mfma_f32_16x16x32_bf16 v[136:139], a[128:131], a[208:211], v[136:139]\n\t" "v_rcp_f32_e64 v106, v106\n\t" "v_mfma_f32_16x16x32_bf16 v[140:143], a[128:131], a[212:215], v[140:143]\n\t"
    "global_load_dwordx4 a[8:11], %[va], s[76:77] offset:-2048 sc1\n\t" "v_rcp_f32_e64 v107, v107\n\t" "v_mfma_f32_16x16x32_bf16 v[240:243], a[132:135], a[200:203], v[240:243]\n\t"
    "ds_read_b64_tr_b16 a[148:149], %[vr6] offset:21504\n\t" "v_mul_f32 v216, v216, v104\n\t" "v_mul_f32 v217, v217, v105\n\t" "v_mfma_f32_16x16x32_bf16 v[244:247], a[132:135], a[204:207], v[244:247]\n\t"
    "ds_read_b64_tr_b16 a[150:151], %[vr6] offset:25088\n\t" "v_mul_f32 v218, v218, v106\n\t" "v_mul_f32 v219, v219, v107\n\t" "v_mfma_f32_16x16x32_bf16 v[248:251], a[132:135], a[208:211], v[248:251]\n\t"
    "v_cvt_pk_bf16_f32 v104, v216, v217\n\t" "v_cvt_pk_bf16_f32 v105, v218, v219\n\t" "v_mfma_f32_16x16x32_bf16 v[252:255], a[132:135], a[212:215], v[252:255]\n\t"
    "global_load_dwordx4 a[12:15], %[va], s[76:77] offset:-1024 sc1\n\t" "v_mul_f32 v108, v108, v19\n\t" "v_mul_f32_e64 v109, v109, v19\n\t" "s_waitcnt lgkmcnt(4)\n\t"
    "v_mfma_f32_16x16x32_bf16 v[128:131], a[136:139], a[32:35], v[128:131]\n\t" "ds_read_b64_tr_b16 a[152:153], %[vr6] offset:28672\n\t" "v_mul_f32 v110, v110, v19\n\t" "v_mul_f32 v111, v111, v19\n\t"
    "v_mfma_f32_16x16x32_bf16 v[128:131], a[140:143], a[48:51], v[128:131]\n\t" "ds_read_b64_tr_b16 a[154:155], %[vr6] offset:32256\n\t" "v_exp_f32_e64 v108, v108\n\t"
    "v_mfma_f32_16x16x32_bf16 v[132:135], a[136:139], a[36:39], v[132:135]\n\t" "v_exp_f32_e64 v109, v109\n\t" "v_mfma_f32_16x16x32_bf16 v[132:135], a[140:143], a[52:55], v[132:135]\n\t"
    "global_load_dwordx4 a[16:19], %[va], s[76:77] offset:0 sc1\n\t" "v_exp_f32_e64 v110, v110\n\t" "v_mfma_f32_16x16x32_bf16 v[136:139], a[136:139], a[40:43], v[136:139]\n\t"
    "ds_read_b64_tr_b16 a[156:157], %[vr6] offset:43008\n\t" "v_exp_f32_e64 v111, v111\n\t" "v_mfma_f32_16x16x32_bf16 v[136:139], a[140:143], a[56:59], v[136:139]\n\t"
    "ds_read_b64_tr_b16 a[158:159], %[vr6] offset:46592\n\t" "v_fma_f32 v108, v108, v23, v23\n\t" "v_fma_f32 v109, v109, v23, v23\n\t"
    "v_mfma_f32_16x16x32_bf16 v[140:143], a[136:139], a[44:47], v[140:143]\n\t" "v_fma_f32 v110, v110, v23, v23\n\t" "v_fma_f32 v111, v111, v23, v23\n\t"
    "v_mfma_f32_16x16x32_bf16 v[140:143], a[140:143], a[60:63], v[140:143]\n\t" "global_load_dwordx4 a[20:23], %[va], s[76:77] offset:1024 sc1\n\t" "v_rcp_f32 v108, v108\n\t" "s_waitcnt lgkmcnt(4)\n\t"
    "v_mfma_f32_16x16x32_bf16 v[240:243], a[144:147], a[32:35], v[240:243]\n\t" "ds_read_b64_tr_b16 a[128:129], %[vr6] offset:35840\n\t" "v_rcp_f32_e64 v109, v109\n\t"
    "v_mfma_f32_16x16x32_bf16 v[240:243], a[148:151], a[48:51], v[240:243]\n\t" "ds_read_b64_tr_b16 a[130:131], %[vr6] offset:39424\n\t" "v_rcp_f32_e64 v110, v110\n\t"
    "v_mfma_f32_16x16x32_bf16 v[244:247], a[144:147], a[36:39], v[244:247]\n\t" "v_rcp_f32_e64 v111, v111\n\t" "v_mfma_f32_16x16x32_bf16 v[244:247], a[148:151], a[52:55], v[244:247]\n\t"
    "global_load_dwordx4 a[24:27], %[va], s[76:77] offset:2048 sc1\n\t" "v_mul_f32 v220, v220, v108\n\t" "v_mul_f32 v221, v221, v109\n\t"
    "v_mfma_f32_16x16x32_bf16 v[248:251], a[144:147], a[40:43], v[248:251]\n\t" "ds_read_b64_tr_b16 a[132:133], %[vr6] offset:50176\n\t" "v_mul_f32 v222, v222, v110\n\t" "v_mul_f32 v223, v223, v111\n\t"
    "v_mfma_f32_16x16x32_bf16 v[248:251], a[148:151], a[56:59], v[248:251]\n\t" "ds_read_b64_tr_b16 a[134:135], %[vr6] offset:53760\n\t" "v_cvt_pk_bf16_f32 v108, v220, v221\n\t"
    "v_cvt_pk_bf16_f32 v109, v222, v223\n\t" "v_mfma_f32_16x16x32_bf16 v[252:255], a[144:147], a[44:47], v[252:255]\n\t" "s_add_u32 s90, %[shl], %[go2]\n\t" "s_addc_u32 s91, %[shh], 0\n\t"
    "s_add_u32 s92, s90, 229376\n\t" "s_addc_u32 s93, s91, 0\n\t" "v_mfma_f32_16x16x32_bf16 v[252:255], a[148:151], a[60:63], v[252:255]\n\t"
    "global_load_dwordx4 a[28:31], %[va], s[76:77] offset:3072 sc1\n\t" "s_add_u32 s76, s76, 8192\n\t" "s_addc_u32 s77, s77, 0\n\t" "v_mul_f32 v112, v112, v16\n\t" "v_mul_f32_e64 v113, v113, v16\n\t"
    "s_waitcnt lgkmcnt(4)\n\t" "v_mfma_f32_16x16x32_bf16 v[128:131], a[152:155], a[64:67], v[128:131]\n\t" "ds_read_b64_tr_b16 a[136:137], %[vr7] offset:0\n\t" "v_mul_f32 v114, v114, v16\n\t"
    "v_mul_f32 v115, v115, v16\n\t" "v_mfma_f32_16x16x32_bf16 v[128:131], a[156:159], a[80:83], v[128:131]\n\t" "ds_read_b64_tr_b16 a[138:139], %[vr7] offset:3584\n\t" "v_exp_f32_e64 v112, v112\n\t"
    "v_mfma_f32_16x16x32_bf16 v[132:135], a[152:155], a[68:71], v[132:135]\n\t" "v_exp_f32_e64 v113, v113\n\t" "v_mfma_f32_16x16x32_bf16 v[132:135], a[156:159], a[84:87], v[132:135]\n\t"
    "global_load_dwordx4 v[144:147], %[va], s[76:77] offset:-4096 sc1\n\t" "v_exp_f32_e64 v114, v114\n\t" "v_mfma_f32_16x16x32_bf16 v[136:139], a[152:155], a[72:75], v[136:139]\n\t"
    "ds_read_b64_tr_b16 a[140:141], %[vr7] offset:14336\n\t" "v_exp_f32_e64 v115, v115\n\t" "v_mfma_f32_16x16x32_bf16 v[136:139], a[156:159], a[88:91], v[136:139]\n\t"
    "ds_read_b64_tr_b16 a[142:143], %[vr7] offset:17920\n\t" "v_fma_f32 v112, v112, v20, v20\n\t" "v_fma_f32 v113, v113, v20, v20\n\t"
    "v_mfma_f32_16x16x32_bf16 v[140:143], a[152:155], a[76:79], v[140:143]\n\t" "v_fma_f32 v114, v114, v20, v20\n\t" "v_fma_f32 v115, v115, v20, v20\n\t"
    "v_mfma_f32_16x16x32_bf16 v[140:143], a[156:159], a[92:95], v[140:143]\n\t" "global_load_dwordx4 v[148:151], %[va], s[76:77] offset:-3072 sc1\n\t" "v_rcp_f32 v112, v112\n\t" "s_waitcnt lgkmcnt(4)\n\t"
    "v_mfma_f32_16x16x32_bf16 v[240:243], a[128:131], a[64:67], v[240:243]\n\t" "ds_read_b64_tr_b16 a[144:145], %[vr7] offset:7168\n\t" "v_rcp_f32_e64 v113, v113\n\t"
    "v_mfma_f32_16x16x32_bf16 v[240:243], a[132:135], a[80:83], v[240:243]\n\t" "ds_read_b64_tr_b16 a[146:147], %[vr7] offset:10752\n\t" "v_rcp_f32_e64 v114, v114\n\t"
    "v_mfma_f32_16x16x32_bf16 v[244:247], a[128:131], a[68:71], v[244:247]\n\t" "v_rcp_f32_e64 v115, v115\n\t" "v_mfma_f32_16x16x32_bf16 v[244:247], a[132:135], a[84:87], v[244:247]\n\t"
    "global_load_dwordx4 v[152:155], %[va], s[76:77] offset:-2048 sc1\n\t" "v_mul_f32 v224, v224, v112\n\t" "v_mul_f32 v225, v225, v113\n\t"
    "v_mfma_f32_16x16x32_bf16 v[248:251], a[128:131], a[72:75], v[248:251]\n\t" "ds_read_b64_tr_b16 a[148:149], %[vr7] offset:21504\n\t" "v_mul_f32 v226, v226, v114\n\t" "v_mul_f32 v227, v227, v115\n\t"
    "v_mfma_f32_16x16x32_bf16 v[248:251], a[132:135], a[88:91], v[248:251]\n\t" "ds_read_b64_tr_b16 a[150:151], %[vr7] offset:25088\n\t" "v_cvt_pk_bf16_f32 v98, v224, v225\n\t"
    "v_cvt_pk_bf16_f32 v99, v226, v227\n\t" "v_mfma_f32_16x16x32_bf16 v[252:255], a[128:131], a[76:79], v[252:255]\n\t" "global_store_dwordx4 %[va], v[96:99], s[90:91] offset:0 sc1\n\t"
    "v_mul_f32_e64 v116, v116, v17\n\t" "v_mfma_f32_16x16x32_bf16 v[252:255], a[132:135], a[92:95], v[252:255]\n\t" "global_load_dwordx4 v[156:159], %[va], s[76:77] offset:-1024 sc1\n\t"
    "v_mul_f32 v117, v117, v17\n\t" "v_mul_f32_e64 v118, v118, v17\n\t" "s_waitcnt lgkmcnt(4)\n\t" "v_mfma_f32_16x16x32_bf16 v[128:131], a[136:139], a[96:99], v[128:131]\n\t"
    "v_mul_f32 v119, v119, v17\n\t" "v_exp_f32 v116, v116\n\t" "v_mfma_f32_16x16x32_bf16 v[128:131], a[140:143], a[112:115], v[128:131]\n\t" "v_exp_f32_e64 v117, v117\n\t"
    "v_mfma_f32_16x16x32_bf16 v[132:135], a[136:139], a[100:103], v[132:135]\n\t" "v_exp_f32_e64 v118, v118\n\t" "v_mfma_f32_16x16x32_bf16 v[132:135], a[140:143], a[116:119], v[132:135]\n\t"
    "global_load_dwordx4 v[160:163], %[va], s[76:77] offset:0 sc1\n\t" "v_exp_f32_e64 v119, v119\n\t" "v_mfma_f32_16x16x32_bf16 v[136:139], a[136:139], a[104:107], v[136:139]\n\t"
    "v_fma_f32 v116, v116, v21, v21\n\t" "v_fma_f32 v117, v117, v21, v21\n\t" "v_mfma_f32_16x16x32_bf16 v[136:139], a[140:143], a[120:123], v[136:139]\n\t" "v_fma_f32 v118, v118, v21, v21\n\t"
    "v_fma_f32 v119, v119, v21, v21\n\t" "v_mfma_f32_16x16x32_bf16 v[140:143], a[136:139], a[108:111], v[140:143]\n\t" "v_rcp_f32_e64 v116, v116\n\t"
    "v_mfma_f32_16x16x32_bf16 v[140:143], a[140:143], a[124:127], v[140:143]\n\t" "global_load_dwordx4 v[48:51], %[va], s[76:77] offset:1024 sc1\n\t" "v_rcp_f32 v117, v117\n\t" "s_waitcnt lgkmcnt(0)\n\t"
    "v_mfma_f32_16x16x32_bf16 v[240:243], a[144:147], a[96:99], v[240:243]\n\t" "v_rcp_f32_e64 v118, v118\n\t" "v_mfma_f32_16x16x32_bf16 v[240:243], a[148:151], a[112:115], v[240:243]\n\t"
    "v_rcp_f32_e64 v119, v119\n\t" "v_mfma_f32_16x16x32_bf16 v[244:247], a[144:147], a[100:103], v[244:247]\n\t" "v_mul_f32 v228, v228, v116\n\t" "v_mul_f32 v229, v229, v117\n\t"
    "v_mfma_f32_16x16x32_bf16 v[244:247], a[148:151], a[116:119], v[244:247]\n\t" "global_load_dwordx4 v[32:35], %[va], s[76:77] offset:2048 sc1\n\t" "v_mul_f32 v230, v230, v118\n\t"
    "v_mul_f32 v231, v231, v119\n\t" "v_mfma_f32_16x16x32_bf16 v[248:251], a[144:147], a[104:107], v[248:251]\n\t" "v_cvt_pk_bf16_f32 v102, v228, v229\n\t" "v_cvt_pk_bf16_f32 v103, v230, v231\n\t"
    "v_mfma_f32_16x16x32_bf16 v[248:251], a[148:151], a[120:123], v[248:251]\n\t" "global_store_dwordx4 %[va], v[100:103], s[90:91] offset:1024 sc1\n\t" "v_mul_f32_e64 v120, v120, v18\n\t"
    "v_mfma_f32_16x16x32_bf16 v[252:255], a[144:147], a[108:111], v[252:255]\n\t" "v_mul_f32 v121, v121, v18\n\t" "v_mul_f32 v122, v122, v18\n\t"
    "v_mfma_f32_16x16x32_bf16 v[252:255], a[148:151], a[124:127], v[252:255]\n\t" "global_load_dwordx4 v[164:167], %[va], s[76:77] offset:3072 sc1\n\t" "s_add_u32 s76, s76, 8192\n\t"
    "s_addc_u32 s77, s77, 0\n\t" "v_mul_f32 v123, v123, v18\n\t" "v_exp_f32 v120, v120\n\t" "v_exp_f32 v121, v121\n\t" "v_exp_f32 v122, v122\n\t" "v_exp_f32 v123, v123\n\t"
    "v_fma_f32 v120, v120, v22, v22\n\t" "v_fma_f32 v121, v121, v22, v22\n\t" "v_fma_f32 v122, v122, v22, v22\n\t" "v_fma_f32 v123, v123, v22, v22\n\t" "v_rcp_f32 v120, v120\n\t"
    "v_rcp_f32 v121, v121\n\t" "v_rcp_f32 v122, v122\n\t" "v_rcp_f32 v123, v123\n\t" "v_mul_f32 v232, v232, v120\n\t" "v_mul_f32 v233, v233, v121\n\t" "v_mul_f32 v234, v234, v122\n\t"
    "v_mul_f32 v235, v235, v123\n\t" "v_cvt_pk_bf16_f32 v106, v232, v233\n\t" "v_cvt_pk_bf16_f32 v107, v234, v235\n\t" "global_store_dwordx4 %[va], v[104:107], s[92:93] offset:0 sc1\n\t"
    "v_mul_f32 v124, v124, v19\n\t" "v_mul_f32 v125, v125, v19\n\t" "v_mul_f32 v126, v126, v19\n\t" "v_mul_f32 v127, v127, v19\n\t" "v_exp_f32 v124, v124\n\t" "v_exp_f32 v125, v125\n\t"
    "v_exp_f32 v126, v126\n\t" "v_exp_f32 v127, v127\n\t" "v_fma_f32 v124, v124, v23, v23\n\t" "v_fma_f32 v125, v125, v23, v23\n\t" "v_fma_f32 v126, v126, v23, v23\n\t"
    "v_fma_f32 v127, v127, v23, v23\n\t" "v_rcp_f32 v124, v124\n\t" "v_rcp_f32 v125, v125\n\t" "v_rcp_f32 v126, v126\n\t" "v_rcp_f32 v127, v127\n\t" "v_mul_f32 v236, v236, v124\n\t"
    "v_mul_f32 v237, v237, v125\n\t" "v_mul_f32 v238, v238, v126\n\t" "v_mul_f32 v239, v239, v127\n\t" "v_cvt_pk_bf16_f32 v110, v236, v237\n\t" "v_cvt_pk_bf16_f32 v111, v238, v239\n\t"
    "global_store_dwordx4 %[va], v[108:111], s[92:93] offset:1024 sc1\n\t" "s_nop 7\n\t" "s_add_u32 s90, %[shl], %[go3]\n\t" "s_addc_u32 s91, %[shh], 0\n\t" "s_add_u32 s92, s90, 229376\n\t"
    "s_addc_u32 s93, s91, 0\n\t" "v_pk_mul_f32 v[128:129], v[128:129], v[16:17] op_sel:[0,0] op_sel_hi:[1,0]\n\t" "v_pk_mul_f32 v[130:131], v[130:131], v[16:17] op_sel:[0,0] op_sel_hi:[1,0]\n\t"
    "v_exp_f32 v128, v128\n\t" "v_exp_f32 v129, v129\n\t" "v_exp_f32 v130, v130\n\t" "v_exp_f32 v131, v131\n\t"
    "v_pk_fma_f32 v[128:129], v[128:129], v[20:21], v[20:21] op_sel:[0,0,0] op_sel_hi:[1,0,0]\n\t" "v_pk_fma_f32 v[130:131], v[130:131], v[20:21], v[20:21] op_sel:[0,0,0] op_sel_hi:[1,0,0]\n\t"
    "v_rcp_f32 v128, v128\n\t" "v_rcp_f32 v129, v129\n\t" "v_rcp_f32 v130, v130\n\t" "v_rcp_f32 v131, v131\n\t" "v_pk_mul_f32 v[240:241], v[240:241], v[128:129]\n\t"
    "v_pk_mul_f32 v[242:243], v[242:243], v[130:131]\n\t" "v_cvt_pk_bf16_f32 v128, v240, v241\n\t" "v_cvt_pk_bf16_f32 v129, v242, v243\n\t" "v_lshrrev_b32_e64 v31, 1, %[va]\n\t"
    "global_store_dwordx2 v31, v[128:129], s[90:91] offset:0 sc1\n\t" "v_pk_mul_f32 v[132:133], v[132:133], v[16:17] op_sel:[0,1] op_sel_hi:[1,1]\n\t"
    "v_pk_mul_f32 v[134:135], v[134:135], v[16:17] op_sel:[0,1] op_sel_hi:[1,1]\n\t" "v_exp_f32 v132, v132\n\t" "v_exp_f32 v133, v133\n\t" "v_exp_f32 v134, v134\n\t" "v_exp_f32 v135, v135\n\t"
    "v_pk_fma_f32 v[132:133], v[132:133], v[20:21], v[20:21] op_sel:[0,1,1] op_sel_hi:[1,1,1]\n\t" "v_pk_fma_f32 v[134:135], v[134:135], v[20:21], v[20:21] op_sel:[0,1,1] op_sel_hi:[1,1,1]\n\t"
    "v_rcp_f32 v132, v132\n\t" "v_rcp_f32 v133, v133\n\t" "v_rcp_f32 v134, v134\n\t" "v_rcp_f32 v135, v135\n\t" "v_pk_mul_f32 v[244:245], v[244:245], v[132:133]\n\t"
    "v_pk_mul_f32 v[246:247], v[246:247], v[134:135]\n\t" "v_cvt_pk_bf16_f32 v132, v244, v245\n\t" "v_cvt_pk_bf16_f32 v133, v246, v247\n\t"
    "global_store_dwordx2 v31, v[132:133], s[90:91] offset:512 sc1\n\t" "v_pk_mul_f32 v[136:137], v[136:137], v[18:19] op_sel:[0,0] op_sel_hi:[1,0]\n\t"
    "v_pk_mul_f32 v[138:139], v[138:139], v[18:19] op_sel:[0,0] op_sel_hi:[1,0]\n\t" "v_exp_f32 v136, v136\n\t" "v_exp_f32 v137, v137\n\t" "v_exp_f32 v138, v138\n\t" "v_exp_f32 v139, v139\n\t"
    "v_pk_fma_f32 v[136:137], v[136:137], v[22:23], v[22:23] op_sel:[0,0,0] op_sel_hi:[1,0,0]\n\t" "v_pk_fma_f32 v[138:139], v[138:139], v[22:23], v[22:23] op_sel:[0,0,0] op_sel_hi:[1,0,0]\n\t"
    "v_rcp_f32 v136, v136\n\t" "v_rcp_f32 v137, v137\n\t" "v_rcp_f32 v138, v138\n\t" "v_rcp_f32 v139, v139\n\t" "v_pk_mul_f32 v[248:249], v[248:249], v[136:137]\n\t"
    "v_pk_mul_f32 v[250:251], v[250:251], v[138:139]\n\t" "v_cvt_pk_bf16_f32 v136, v248, v249\n\t" "v_cvt_pk_bf16_f32 v137, v250, v251\n\t"
    "global_store_dwordx2 v31, v[136:137], s[92:93] offset:0 sc1\n\t" "v_pk_mul_f32 v[140:141], v[140:141], v[18:19] op_sel:[0,1] op_sel_hi:[1,1]\n\t"
    "v_pk_mul_f32 v[142:143], v[142:143], v[18:19] op_sel:[0,1] op_sel_hi:[1,1]\n\t" "v_exp_f32 v140, v140\n\t" "v_exp_f32 v141, v141\n\t" "v_exp_f32 v142, v142\n\t" "v_exp_f32 v143, v143\n\t"
    "v_pk_fma_f32 v[140:141], v[140:141], v[22:23], v[22:23] op_sel:[0,1,1] op_sel_hi:[1,1,1]\n\t" "v_pk_fma_f32 v[142:143], v[142:143], v[22:23], v[22:23] op_sel:[0,1,1] op_sel_hi:[1,1,1]\n\t"
    "v_rcp_f32 v140, v140\n\t" "v_rcp_f32 v141, v141\n\t" "v_rcp_f32 v142, v142\n\t" "v_rcp_f32 v143, v143\n\t" "v_pk_mul_f32 v[252:253], v[252:253], v[140:141]\n\t"
    "v_pk_mul_f32 v[254:255], v[254:255], v[142:143]\n\t" "v_cvt_pk_bf16_f32 v140, v252, v253\n\t" "v_cvt_pk_bf16_f32 v141, v254, v255\n\t"
    "global_store_dwordx2 v31, v[140:141], s[92:93] offset:512 sc1\n\t" "s_barrier\n\t"
    : "=&{v[24:25]}"(fl), "=&{v[144:147]}"(stg[0]), "=&{v[148:151]}"(stg[1]), "=&{v[152:155]}"(stg[2]), "=&{v[156:159]}"(stg[3]), "=&{v[160:163]}"(stg[4]), "=&{v[48:51]}"(stg[5]), "=&{v[32:35]}"(stg[6]), "=&{v[164:167]}"(stg[7])
    : [va] "{v0}"(va), [ve] "{a248}"(ve), [vb0] "{v1}"(vb[0]), [vb1] "{v2}"(vb[1]), [vb2] "{v3}"(vb[2]), [vb3] "{v4}"(vb[3]), [vr0] "{v5}"(vr[0]), [vr1] "{v6}"(vr[1]), [vr2] "{v7}"(vr[2]), [vr3] "{v8}"(vr[3]), [vr4] "{v9}"(vr[4]), [vr5] "{v10}"(vr[5]), [vr6] "{v11}"(vr[6]), [vr7] "{v12}"(vr[7]), [gt0] "s"(gt[0]), [go0] "s"(go[0]), [gt1] "s"(gt[1]), [go1] "s"(go[1]), [gt2] "s"(gt[2]), [go2] "s"(go[2]), [gt3] "s"(gt[3]), [go3] "s"(go[3]), [ct] "{a249}"(ct), [cr0] "{a250}"(cr0), [cr1] "{a251}"(cr1), [cw] "{a252}"(cw), [cp] "{a253}"(vp), [vs] "{a254}"(vs), [vf] "{v13}"(vf), [sq] "s"(sq), [wid] "s"(wid), [tag] "s"(tag), [cd] "s"(cd), [sa] "s"(sa), [sb] "s"(sb + 917504), [se] "s"(se), [ss] "s"(ss), [sl] "s"(sl), [su0] "s"(su0), [su6] "s"(su6), [sr] "s"(sr), [su] "s"(su), [srl] "s"(srl), [rb] "s"(ldsb + RAW), [shl] "s"(shl), [shh] "s"(shh), [wa] "s"(wa), [wb] "s"(wb), [e0] "s"(e0), [e1] "s"(e1), [m01] "s"(m01), [m6] "s"(m6), [tp0] "s"(tp0), [vbd] "{a255}"(vbd), [sbw] "s"(sbd), [sw] "s"(sw), [sd] "s"(sd), [sld] "s"(sld), [sl0] "s"(ldsb), [sc] "s"(scnt)
    : "v16", "v17", "v18", "v19", "v20", "v21", "v22", "v23", "v26", "v27", "v28", "v29", "v30", "v31", "v36", "v37", "v38", "v39", "v40", "v41", "v42", "v43", "v44", "v45", "v46", "v47", "v52", "v53", "v54", "v55", "v56", "v57", "v58", "v59", "v60", "v61", "v62", "v63", "v64", "v65", "v66", "v67", "v68", "v69", "v70", "v71", "v72", "v73", "v74", "v75", "v76", "v77", "v78", "v79", "v80", "v81", "v82", "v83", "v84", "v85", "v86", "v87", "v88", "v89", "v90", "v91", "v92", "v93", "v94", "v95", "v96", "v97", "v98", "v99", "v100", "v101", "v102", "v103", "v104", "v105", "v106", "v107", "v108", "v109", "v110", "v111", "v112", "v113", "v114", "v115", "v116", "v117", "v118", "v119", "v120", "v121", "v122", "v123", "v124", "v125", "v126", "v127", "v128", "v129", "v130", "v131", "v132", "v133", "v134", "v135", "v136", "v137", "v138", "v139", "v140", "v141", "v142", "v143", "v168", "v169", "v170", "v171", "v172", "v173", "v174", "v175", "v176", "v177", "v178", "v179", "v180", "v181", "v182", "v183", "v184", "v185", "v186", "v187", "v188", "v189", "v190", "v191", "v192", "v193", "v194", "v195", "v196", "v197", "v198", "v199", "v200", "v201", "v202", "v203", "v204", "v205", "v206", "v207", "v208", "v209", "v210", "v211", "v212", "v213", "v214", "v215", "v216", "v217", "v218", "v219", "v220", "v221", "v222", "v223", "v224", "v225", "v226", "v227", "v228", "v229", "v230", "v231", "v232", "v233", "v234", "v235", "v236", "v237", "v238", "v239", "v240", "v241", "v242", "v243", "v244", "v245", "v246", "v247", "v248", "v249", "v250", "v251", "v252", "v253", "v254", "v255", "vcc", "a0", "a1", "a2", "a3", "a4", "a5", "a6", "a7", "a8", "a9", "a10", "a11", "a12", "a13", "a14", "a15", "a16", "a17", "a18", "a19", "a20", "a21", "a22", "a23", "a24", "a25", "a26", "a27", "a28", "a29", "a30", "a31", "a32", "a33", "a34", "a35", "a36", "a37", "a38", "a39", "a40", "a41", "a42", "a43", "a44", "a45", "a46", "a47", "a48", "a49", "a50", "a51", "a52", "a53", "a54", "a55", "a56", "a57", "a58", "a59", "a60", "a61", "a62", "a63", "a64", "a65", "a66", "a67", "a68", "a69", "a70", "a71", "a72", "a73", "a74", "a75", "a76", "a77", "a78", "a79", "a80", "a81", "a82", "a83", "a84", "a85", "a86", "a87", "a88", "a89", "a90", "a91", "a92", "a93", "a94", "a95", "a96", "a97", "a98", "a99", "a100", "a101", "a102", "a103", "a104", "a105", "a106", "a107", "a108", "a109", "a110", "a111", "a112", "a113", "a114", "a115", "a116", "a117", "a118", "a119", "a120", "a121", "a122", "a123", "a124", "a125", "a126", "a127", "a128", "a129", "a130", "a131", "a132", "a133", "a134", "a135", "a136", "a137", "a138", "a139", "a140", "a141", "a142", "a143", "a144", "a145", "a146", "a147", "a148", "a149", "a150", "a151", "a152", "a153", "a154", "a155", "a156", "a157", "a158", "a159", "a160", "a161", "a162", "a163", "a164", "a165", "a166", "a167", "a168", "a169", "a170", "a171", "a172", "a173", "a174", "a175", "a176", "a177", "a178", "a179", "a180", "a181", "a182", "a183", "a184", "a185", "a186", "a187", "a188", "a189", "a190", "a191", "a192", "a193", "a194", "a195", "a196", "a197", "a198", "a199", "a200", "a201", "a202", "a203", "a204", "a205", "a206", "a207", "a208", "a209", "a210", "a211", "a212", "a213", "a214", "a215", "a216", "a217", "a218", "a219", "a220", "a221", "a222", "a223", "a224", "a225", "a226", "a227", "a228", "a229", "a230", "a231", "a232", "a233", "a234", "a235", "a236", "a237", "a238", "a239", "a240", "a241", "a242", "a243", "a244", "a245", "a246", "a247", "s76", "s77", "s78", "s79", "s80", "s81", "s82", "s83", "s84", "s85", "s86", "s87", "s88", "s89", "s90", "s91", "s92", "s93", "s94", "s95", "s96", "s97", "s98", "s99", "scc", "memory");
  } } }
  {
  asm volatile("" : "+s"(bx), "+s"(wid), "+s"(ws)); asm volatile("" : "+s"(tag));
  int lane = __builtin_amdgcn_mbcnt_hi(~0u, __builtin_amdgcn_mbcnt_lo(~0u, 0u));
  unsigned long long fl; f32x4 ac[16]; unsigned vb0, vb1, vr[24], sl0, sl1; const unsigned vx = lane * 16; const char *sa, *sb;
  { const int xcd = bx & 7, loc = bx >> 3, kh = xcd & 1, rb = (xcd >> 1) * 4 + (loc & 3), k0 = KD;
    sl0 = ldsb + wid * 1024 + (kh ^ 1) * 4096; sl1 = sl0 ^ 4096; vb0 = f_vb(wid, lane) + (kh ^ 1) * 128 + (k0 + 28 + EAR * (rb & 1)) * 65536; vb1 = vb0 ^ 128;
#pragma unroll
    for (int j = 0; j < 12; ++j) { vr[j] = ldsb + f_vr(lane, j & 3) + (kh ^ 1) * 4096 + (j >> 2) * 65536; vr[12 + j] = vr[j] ^ 4096; }
    sa = ws + (unsigned)(W_HH + ((rb * 4 + wid) * 112 + kh * 56 + k0) * 2048); sb = (const char*)pfw + (unsigned)(kh * 1792 * 2048 + ROT * 256);
    asm volatile(".p2align 3\n\t" "s_mov_b64 s[40:41], %[sa]\n\t" "s_add_u32 s40, s40, 16384\n\t" "s_addc_u32 s41, s41, 0\n\t" "ds_read_b64_tr_b16 a[176:177], %[vr0] offset:24576\n\t"
    "ds_read_b64_tr_b16 a[178:179], %[vr0] offset:26624\n\t" "ds_read_b64_tr_b16 a[180:181], %[vr0] offset:32768\n\t" "ds_read_b64_tr_b16 a[182:183], %[vr0] offset:34816\n\t"
    "ds_read_b64_tr_b16 a[184:185], %[vr1] offset:24576\n\t" "ds_read_b64_tr_b16 a[186:187], %[vr1] offset:26624\n\t" "ds_read_b64_tr_b16 a[188:189], %[vr1] offset:32768\n\t"
    "ds_read_b64_tr_b16 a[190:191], %[vr1] offset:34816\n\t" "ds_read_b64_tr_b16 a[192:193], %[vr2] offset:24576\n\t" "ds_read_b64_tr_b16 a[194:195], %[vr2] offset:26624\n\t"
    "ds_read_b64_tr_b16 a[196:197], %[vr2] offset:32768\n\t" "ds_read_b64_tr_b16 a[198:199], %[vr2] offset:34816\n\t" "s_waitcnt vmcnt(22)\n\t" "s_waitcnt lgkmcnt(4) vmcnt(20)\n\t"
    "v_mfma_f32_16x16x32_bf16 v[224:227], a[176:179], a[0:3], 0\n\t" "ds_read_b64_tr_b16 a[200:201], %[vr3] offset:24576\n\t" "s_add_u32 m0, %[sl0], 49152\n\t"
    "v_mfma_f32_16x16x32_bf16 v[224:227], a[180:183], a[8:11], v[224:227]\n\t" "ds_read_b64_tr_b16 a[202:203], %[vr3] offset:26624\n\t" "global_load_lds_dwordx4 %[vb0], %[sb]\n\t"
    "v_mfma_f32_16x16x32_bf16 v[240:243], a[176:179], a[4:7], 0\n\t" "ds_read_b64_tr_b16 a[204:205], %[vr3] offset:32768\n\t" "v_add_u32 %[vb0], 0x40000, %[vb0]\n\t"
    "v_mfma_f32_16x16x32_bf16 v[240:243], a[180:183], a[12:15], v[240:243]\n\t" "ds_read_b64_tr_b16 a[206:207], %[vr3] offset:34816\n\t" "s_add_u32 m0, %[sl1], 49152\n\t"
    "v_mfma_f32_16x16x32_bf16 v[228:231], a[184:187], a[0:3], 0\n\t" "ds_read_b64_tr_b16 a[176:177], %[vr12] offset:24576\n\t" "v_mfma_f32_16x16x32_bf16 v[228:231], a[188:191], a[8:11], v[228:231]\n\t"
    "ds_read_b64_tr_b16 a[178:179], %[vr12] offset:26624\n\t" "global_load_lds_dwordx4 %[vb1], %[sb]\n\t" "v_mfma_f32_16x16x32_bf16 v[244:247], a[184:187], a[4:7], 0\n\t"
    "ds_read_b64_tr_b16 a[180:181], %[vr12] offset:32768\n\t" "v_add_u32 %[vb1], 0x40000, %[vb1]\n\t" "v_mfma_f32_16x16x32_bf16 v[244:247], a[188:191], a[12:15], v[244:247]\n\t"
    "ds_read_b64_tr_b16 a[182:183], %[vr12] offset:34816\n\t" "s_waitcnt lgkmcnt(4)\n\t" "v_mfma_f32_16x16x32_bf16 v[232:235], a[192:195], a[0:3], 0\n\t"
    "ds_read_b64_tr_b16 a[184:185], %[vr13] offset:24576\n\t" "s_add_u32 m0, %[sl0], 57344\n\t" "v_mfma_f32_16x16x32_bf16 v[232:235], a[196:199], a[8:11], v[232:235]\n\t"
    "ds_read_b64_tr_b16 a[186:187], %[vr13] offset:26624\n\t" "global_load_lds_dwordx4 %[vb0], %[sb]\n\t" "v_mfma_f32_16x16x32_bf16 v[248:251], a[192:195], a[4:7], 0\n\t"
    "ds_read_b64_tr_b16 a[188:189], %[vr13] offset:32768\n\t" "v_add_u32 %[vb0], 0x30000, %[vb0]\n\t" "v_mfma_f32_16x16x32_bf16 v[248:251], a[196:199], a[12:15], v[248:251]\n\t"
    "ds_read_b64_tr_b16 a[190:191], %[vr13] offset:34816\n\t" "s_add_u32 m0, %[sl1], 57344\n\t" "v_mfma_f32_16x16x32_bf16 v[236:239], a[200:203], a[0:3], 0\n\t"
    "ds_read_b64_tr_b16 a[192:193], %[vr14] offset:24576\n\t" "v_mfma_f32_16x16x32_bf16 v[236:239], a[204:207], a[8:11], v[236:239]\n\t" "ds_read_b64_tr_b16 a[194:195], %[vr14] offset:26624\n\t"
    "global_load_lds_dwordx4 %[vb1], %[sb]\n\t" "v_mfma_f32_16x16x32_bf16 v[252:255], a[200:203], a[4:7], 0\n\t" "ds_read_b64_tr_b16 a[196:197], %[vr14] offset:32768\n\t"
    "v_add_u32 %[vb1], 0x30000, %[vb1]\n\t" "v_mfma_f32_16x16x32_bf16 v[252:255], a[204:207], a[12:15], v[252:255]\n\t" "ds_read_b64_tr_b16 a[198:199], %[vr14] offset:34816\n\t" "s_waitcnt lgkmcnt(4)\n\t"
    "v_mfma_f32_16x16x32_bf16 v[96:99], a[176:179], a[0:3], 0\n\t" "ds_read_b64_tr_b16 a[200:201], %[vr15] offset:24576\n\t" "v_mfma_f32_16x16x32_bf16 v[96:99], a[180:183], a[8:11], v[96:99]\n\t"
    "ds_read_b64_tr_b16 a[202:203], %[vr15] offset:26624\n\t" "global_load_dwordx4 a[64:67], %[vx], s[40:41] offset:-4096 sc1\n\t" "v_mfma_f32_16x16x32_bf16 v[100:103], a[176:179], a[4:7], 0\n\t"
    "ds_read_b64_tr_b16 a[204:205], %[vr15] offset:32768\n\t" "v_mfma_f32_16x16x32_bf16 v[100:103], a[180:183], a[12:15], v[100:103]\n\t" "ds_read_b64_tr_b16 a[206:207], %[vr15] offset:34816\n\t"
    "v_mfma_f32_16x16x32_bf16 v[104:107], a[184:187], a[0:3], 0\n\t" "s_barrier\n\t" "ds_read_b64_tr_b16 a[176:177], %[vr0] offset:40960\n\t"
    "v_mfma_f32_16x16x32_bf16 v[104:107], a[188:191], a[8:11], v[104:107]\n\t" "ds_read_b64_tr_b16 a[178:179], %[vr0] offset:43008\n\t" "global_load_dwordx4 a[68:71], %[vx], s[40:41] offset:-3072 sc1\n\t"
    "v_mfma_f32_16x16x32_bf16 v[108:111], a[184:187], a[4:7], 0\n\t" "ds_read_b64_tr_b16 a[180:181], %[vr8] offset:8192\n\t" "v_mfma_f32_16x16x32_bf16 v[108:111], a[188:191], a[12:15], v[108:111]\n\t"
    "ds_read_b64_tr_b16 a[182:183], %[vr8] offset:10240\n\t" "s_waitcnt lgkmcnt(4)\n\t" "v_mfma_f32_16x16x32_bf16 v[112:115], a[192:195], a[0:3], 0\n\t"
    "ds_read_b64_tr_b16 a[184:185], %[vr1] offset:40960\n\t" "s_add_u32 m0, %[sl0], 65536\n\t" "v_mfma_f32_16x16x32_bf16 v[112:115], a[196:199], a[8:11], v[112:115]\n\t"
    "ds_read_b64_tr_b16 a[186:187], %[vr1] offset:43008\n\t" "global_load_lds_dwordx4 %[vb0], %[sb]\n\t" "v_mfma_f32_16x16x32_bf16 v[116:119], a[192:195], a[4:7], 0\n\t"
    "ds_read_b64_tr_b16 a[188:189], %[vr9] offset:8192\n\t" "v_add_u32 %[vb0], 0x40000, %[vb0]\n\t" "v_mfma_f32_16x16x32_bf16 v[116:119], a[196:199], a[12:15], v[116:119]\n\t"
    "ds_read_b64_tr_b16 a[190:191], %[vr9] offset:10240\n\t" "s_add_u32 m0, %[sl1], 65536\n\t" "v_mfma_f32_16x16x32_bf16 v[120:123], a[200:203], a[0:3], 0\n\t"
    "ds_read_b64_tr_b16 a[192:193], %[vr2] offset:40960\n\t" "v_mfma_f32_16x16x32_bf16 v[120:123], a[204:207], a[8:11], v[120:123]\n\t" "ds_read_b64_tr_b16 a[194:195], %[vr2] offset:43008\n\t"
    "global_load_lds_dwordx4 %[vb1], %[sb]\n\t" "v_mfma_f32_16x16x32_bf16 v[124:127], a[200:203], a[4:7], 0\n\t" "ds_read_b64_tr_b16 a[196:197], %[vr10] offset:8192\n\t"
    "v_add_u32 %[vb1], 0x40000, %[vb1]\n\t" "v_mfma_f32_16x16x32_bf16 v[124:127], a[204:207], a[12:15], v[124:127]\n\t" "ds_read_b64_tr_b16 a[198:199], %[vr10] offset:10240\n\t"
    "global_load_dwordx4 a[72:75], %[vx], s[40:41] offset:-2048 sc1\n\t"
    : [vb0] "+v"(vb0), [vb1] "+v"(vb1), "+{v[144:147]}"(stg[0]), "+{v[148:151]}"(stg[1]), "+{v[152:155]}"(stg[2]), "+{v[156:159]}"(stg[3]), "+{v[160:163]}"(stg[4]), "+{v[48:51]}"(stg[5]), "+{v[32:35]}"(stg[6]), "+{v[164:167]}"(stg[7]), "=&{v[96:99]}"(ac[0]), "=&{v[100:103]}"(ac[1]), "=&{v[104:107]}"(ac[2]), "=&{v[108:111]}"(ac[3]), "=&{v[112:115]}"(ac[4]), "=&{v[116:119]}"(ac[5]), "=&{v[120:123]}"(ac[6]), "=&{v[124:127]}"(ac[7]), "=&{v[224:227]}"(ac[8]), "=&{v[228:231]}"(ac[9]), "=&{v[232:235]}"(ac[10]), "=&{v[236:239]}"(ac[11]), "=&{v[240:243]}"(ac[12]), "=&{v[244:247]}"(ac[13]), "=&{v[248:251]}"(ac[14]), "=&{v[252:255]}"(ac[15])
    : [vr0] "v"(vr[0]), [vr1] "v"(vr[1]), [vr2] "v"(vr[2]), [vr3] "v"(vr[3]), [vr4] "v"(vr[4]), [vr5] "v"(vr[5]), [vr6] "v"(vr[6]), [vr7] "v"(vr[7]), [vr8] "v"(vr[8]), [vr9] "v"(vr[9]), [vr10] "v"(vr[10]), [vr11] "v"(vr[11]), [vr12] "v"(vr[12]), [vr13] "v"(vr[13]), [vr14] "v"(vr[14]), [vr15] "v"(vr[15]), [vr16] "v"(vr[16]), [vr17] "v"(vr[17]), [vr18] "v"(vr[18]), [vr19] "v"(vr[19]), [vr20] "v"(vr[20]), [vr21] "v"(vr[21]), [vr22] "v"(vr[22]), [vr23] "v"(vr[23]), [vx] "v"(vx), [sa] "s"(sa + 4096), [sb] "s"(sb), [sl0] "s"(sl0), [sl1] "s"(sl1), [cd] "s"(cd)
    : "a0", "a1", "a2", "a3", "a4", "a5", "a6", "a7", "a8", "a9", "a10", "a11", "a12", "a13", "a14", "a15", "a16", "a17", "a18", "a19", "a20", "a21", "a22", "a23", "a24", "a25", "a26", "a27", "a28", "a29", "a30", "a31", "a32", "a33", "a34", "a35", "a36", "a37", "a38", "a39", "a40", "a41", "a42", "a43", "a44", "a45", "a46", "a47", "a48", "a49", "a50", "a51", "a52", "a53", "a54", "a55", "a56", "a57", "a58", "a59", "a60", "a61", "a62", "a63", "a64", "a65", "a66", "a67", "a68", "a69", "a70", "a71", "a72", "a73", "a74", "a75", "a76", "a77", "a78", "a79", "a80", "a81", "a82", "a83", "a84", "a85", "a86", "a87", "a88", "a89", "a90", "a91", "a92", "a93", "a94", "a95", "a96", "a97", "a98", "a99", "a100", "a101", "a102", "a103", "a104", "a105", "a106", "a107", "a108", "a109", "a110", "a111", "a112", "a113", "a114", "a115", "a116", "a117", "a118", "a119", "a120", "a121", "a122", "a123", "a124", "a125", "a126", "a127", "a128", "a129", "a130", "a131", "a132", "a133", "a134", "a135", "a136", "a137", "a138", "a139", "a140", "a141", "a142", "a143", "a144", "a145", "a146", "a147", "a148", "a149", "a150", "a151", "a152", "a153", "a154", "a155", "a156", "a157", "a158", "a159", "a160", "a161", "a162", "a163", "a164", "a165", "a166", "a167", "a168", "a169", "a170", "a171", "a172", "a173", "a174", "a175", "a176", "a177", "a178", "a179", "a180", "a181", "a182", "a183", "a184", "a185", "a186", "a187", "a188", "a189", "a190", "a191", "a192", "a193", "a194", "a195", "a196", "a197", "a198", "a199", "a200", "a201", "a202", "a203", "a204", "a205", "a206", "a207", "a208", "a209", "a210", "a211", "a212", "a213", "a214", "a215", "a216", "a217", "a218", "a219", "a220", "a221", "a222", "a223", "a224", "a225", "a226", "a227", "a228", "a229", "a230", "a231", "a232", "a233", "a234", "a235", "a236", "a237", "a238", "a239", "a240", "a241", "a242", "a243", "a244", "a245", "a246", "a247", "a248", "a249", "a250", "a251", "a252", "a253", "a254", "a255", "s40", "s41", "s42", "s43", "s44", "s45", "scc", "memory");
  }
  asm volatile("" : "+s"(bx), "+s"(wid), "+s"(ws)); asm volatile("" : "+v"(lane));
  const int xcd = bx & 7, loc = bx >> 3, kh = xcd & 1, rb = (xcd >> 1) * 4 + (loc & 3), cb = ROT, fr = lane & 15, fq = lane >> 4, wr = 56 - KD;
  const unsigned vo = lane * 8, vg = fq * 16, vf = fr * FS, vc = lane * 4 + 4096, ww0 = ldsb + wid * 1024 + vx, ww1 = ww0 + 65536, ww2 = ww0 + 131072;
  const unsigned one = 0x3f80u << (lane & 1) * 16, tp = ldsb + 114688 + wid * 4096; const u32x2 id = {(lane & 2) ? 0u : one, (lane & 2) ? one : 0u};
  const unsigned tw0 = tp + fr * 128 + (((fq >> 1) ^ (fr & 7)) << 4) + (fq & 1) * 8, tw1 = tw0 ^ 32, tw2 = tw0 ^ 64, tw3 = tw0 ^ 96, tr = tp + (lane >> 3) * 128 + (((lane & 7) ^ (lane >> 3)) << 4), vs = (lane >> 3) * 2048 + (lane & 7) * 16;
  const char* sw = ws + (unsigned)(W_TAGS + HF * 8 + FA((rb >> 1) * 32 + kh * 16)); const char* sg = ws + (unsigned)(W_GB + bx * 256);
  const char* sm = ws + (unsigned)(W_XCH + (bx * 4 + wid) * 4352); const char* st = ws + (unsigned)(W_XCH + ((bx ^ 1) * 4 + wid) * 4352); const u16* so = out + (unsigned)((rb * 128 + wid * 32) * D_ + cb * 128 + kh * 64);
  const char* sc = ws + (unsigned)(W_TAGS + HF * 8 + FA((kh * 4 + (loc & 3)) * 32 + (xcd >> 1) * 8 + (loc >> 2))); const int pw = (1 + (rb & 1)) * 65536, qw = (2 - (rb & 1)) * 65536, jw = (-52 - 2 * (rb & 1)) * 65536;
  asm volatile(".p2align 3\n\t" "s_mov_b64 s[40:41], %[sa]\n\t" "s_mov_b32 s44, 0x400000\n\t" "v_mov_b64 v[192:193], %[tag]\n\t" "v_mov_b32 v194, 0\n\t" "s_waitcnt lgkmcnt(4) vmcnt(25)\n\t"
    "v_mfma_f32_16x16x32_bf16 v[224:227], a[176:179], a[16:19], v[224:227]\n\t" "ds_read_b64_tr_b16 a[200:201], %[vr3] offset:40960\n\t"
    "v_mfma_f32_16x16x32_bf16 v[224:227], a[180:183], a[24:27], v[224:227]\n\t" "ds_read_b64_tr_b16 a[202:203], %[vr3] offset:43008\n\t" "global_load_dword a255, %[vx], %[cd]\n\t"
    "v_mfma_f32_16x16x32_bf16 v[240:243], a[176:179], a[20:23], v[240:243]\n\t" "ds_read_b64_tr_b16 a[204:205], %[vr11] offset:8192\n\t"
    "v_mfma_f32_16x16x32_bf16 v[240:243], a[180:183], a[28:31], v[240:243]\n\t" "ds_read_b64_tr_b16 a[206:207], %[vr11] offset:10240\n\t"
    "global_load_dwordx4 a[76:79], %[vx], s[40:41] offset:-1024 sc1\n\t" "v_mfma_f32_16x16x32_bf16 v[228:231], a[184:187], a[16:19], v[228:231]\n\t"
    "ds_read_b64_tr_b16 a[176:177], %[vr12] offset:40960\n\t" "v_mfma_f32_16x16x32_bf16 v[228:231], a[188:191], a[24:27], v[228:231]\n\t" "ds_read_b64_tr_b16 a[178:179], %[vr12] offset:43008\n\t"
    "s_add_u32 m0, %[sl0], 73728\n\t" "v_mfma_f32_16x16x32_bf16 v[244:247], a[184:187], a[20:23], v[244:247]\n\t" "ds_read_b64_tr_b16 a[180:181], %[vr20] offset:8192\n\t"
    "v_mfma_f32_16x16x32_bf16 v[244:247], a[188:191], a[28:31], v[244:247]\n\t" "ds_read_b64_tr_b16 a[182:183], %[vr20] offset:10240\n\t" "s_waitcnt lgkmcnt(4)\n\t"
    "v_mfma_f32_16x16x32_bf16 v[232:235], a[192:195], a[16:19], v[232:235]\n\t" "ds_read_b64_tr_b16 a[184:185], %[vr13] offset:40960\n\t" "global_load_lds_dwordx4 %[vb0], %[sb]\n\t"
    "v_mfma_f32_16x16x32_bf16 v[232:235], a[196:199], a[24:27], v[232:235]\n\t" "ds_read_b64_tr_b16 a[186:187], %[vr13] offset:43008\n\t" "v_add_u32 %[vb0], 0x30000, %[vb0]\n\t"
    "v_mfma_f32_16x16x32_bf16 v[248:251], a[192:195], a[20:23], v[248:251]\n\t" "ds_read_b64_tr_b16 a[188:189], %[vr21] offset:8192\n\t" "s_add_u32 m0, %[sl1], 73728\n\t"
    "v_mfma_f32_16x16x32_bf16 v[248:251], a[196:199], a[28:31], v[248:251]\n\t" "ds_read_b64_tr_b16 a[190:191], %[vr21] offset:10240\n\t" "global_load_lds_dwordx4 %[vb1], %[sb]\n\t"
    "v_mfma_f32_16x16x32_bf16 v[236:239], a[200:203], a[16:19], v[236:239]\n\t" "ds_read_b64_tr_b16 a[192:193], %[vr14] offset:40960\n\t" "v_add_u32 %[vb1], 0x30000, %[vb1]\n\t"
    "v_mfma_f32_16x16x32_bf16 v[236:239], a[204:207], a[24:27], v[236:239]\n\t" "ds_read_b64_tr_b16 a[194:195], %[vr14] offset:43008\n\t"
    "v_mfma_f32_16x16x32_bf16 v[252:255], a[200:203], a[20:23], v[252:255]\n\t" "ds_read_b64_tr_b16 a[196:197], %[vr22] offset:8192\n\t"
    "v_mfma_f32_16x16x32_bf16 v[252:255], a[204:207], a[28:31], v[252:255]\n\t" "ds_read_b64_tr_b16 a[198:199], %[vr22] offset:10240\n\t" "s_waitcnt lgkmcnt(4)\n\t"
    "v_mfma_f32_16x16x32_bf16 v[96:99], a[176:179], a[16:19], v[96:99]\n\t" "ds_read_b64_tr_b16 a[200:201], %[vr15] offset:40960\n\t" "global_load_dwordx4 a[0:3], %[vx], s[40:41] offset:0 sc1\n\t"
    "v_mfma_f32_16x16x32_bf16 v[96:99], a[180:183], a[24:27], v[96:99]\n\t" "ds_read_b64_tr_b16 a[202:203], %[vr15] offset:43008\n\t"
    "v_mfma_f32_16x16x32_bf16 v[100:103], a[176:179], a[20:23], v[100:103]\n\t" "ds_read_b64_tr_b16 a[204:205], %[vr23] offset:8192\n\t"
    "v_mfma_f32_16x16x32_bf16 v[100:103], a[180:183], a[28:31], v[100:103]\n\t" "ds_read_b64_tr_b16 a[206:207], %[vr23] offset:10240\n\t" "global_load_dwordx4 a[4:7], %[vx], s[40:41] offset:1024 sc1\n\t"
    "v_mfma_f32_16x16x32_bf16 v[104:107], a[184:187], a[16:19], v[104:107]\n\t" "s_waitcnt vmcnt(15)\n\t" "s_barrier\n\t" "s_cmp_eq_u32 %[wid], 0\n\t" "s_cselect_b64 exec, 1, 0\n\t"
    "global_store_dwordx2 v194, v[192:193], %[sc] sc1\n\t" "s_mov_b64 exec, -1\n\t" "ds_read_b64_tr_b16 a[176:177], %[vr8] offset:16384\n\t"
    "v_mfma_f32_16x16x32_bf16 v[104:107], a[188:191], a[24:27], v[104:107]\n\t" "ds_read_b64_tr_b16 a[178:179], %[vr8] offset:18432\n\t" "s_add_u32 m0, %[sl0], 81920\n\t"
    "v_mfma_f32_16x16x32_bf16 v[108:111], a[184:187], a[20:23], v[108:111]\n\t" "ds_read_b64_tr_b16 a[180:181], %[vr8] offset:24576\n\t"
    "v_mfma_f32_16x16x32_bf16 v[108:111], a[188:191], a[28:31], v[108:111]\n\t" "ds_read_b64_tr_b16 a[182:183], %[vr8] offset:26624\n\t" "s_waitcnt lgkmcnt(4)\n\t"
    "v_mfma_f32_16x16x32_bf16 v[112:115], a[192:195], a[16:19], v[112:115]\n\t" "ds_read_b64_tr_b16 a[184:185], %[vr9] offset:16384\n\t" "global_load_lds_dwordx4 %[vb0], %[sb]\n\t"
    "v_mfma_f32_16x16x32_bf16 v[112:115], a[196:199], a[24:27], v[112:115]\n\t" "ds_read_b64_tr_b16 a[186:187], %[vr9] offset:18432\n\t" "v_add_u32 %[vb0], 0x40000, %[vb0]\n\t"
    "v_mfma_f32_16x16x32_bf16 v[116:119], a[192:195], a[20:23], v[116:119]\n\t" "ds_read_b64_tr_b16 a[188:189], %[vr9] offset:24576\n\t" "s_add_u32 m0, %[sl1], 81920\n\t"
    "v_mfma_f32_16x16x32_bf16 v[116:119], a[196:199], a[28:31], v[116:119]\n\t" "ds_read_b64_tr_b16 a[190:191], %[vr9] offset:26624\n\t" "global_load_lds_dwordx4 %[vb1], %[sb]\n\t"
    "v_mfma_f32_16x16x32_bf16 v[120:123], a[200:203], a[16:19], v[120:123]\n\t" "ds_read_b64_tr_b16 a[192:193], %[vr10] offset:16384\n\t" "v_add_u32 %[vb1], 0x40000, %[vb1]\n\t"
    "v_mfma_f32_16x16x32_bf16 v[120:123], a[204:207], a[24:27], v[120:123]\n\t" "ds_read_b64_tr_b16 a[194:195], %[vr10] offset:18432\n\t"
    "v_mfma_f32_16x16x32_bf16 v[124:127], a[200:203], a[20:23], v[124:127]\n\t" "ds_read_b64_tr_b16 a[196:197], %[vr10] offset:24576\n\t"
    "v_mfma_f32_16x16x32_bf16 v[124:127], a[204:207], a[28:31], v[124:127]\n\t" "ds_read_b64_tr_b16 a[198:199], %[vr10] offset:26624\n\t" "s_waitcnt lgkmcnt(4)\n\t"
    "v_mfma_f32_16x16x32_bf16 v[224:227], a[176:179], v[144:147], v[224:227]\n\t" "ds_read_b64_tr_b16 a[200:201], %[vr11] offset:16384\n\t"
    "global_load_dwordx4 a[8:11], %[vx], s[40:41] offset:2048 sc1\n\t" "v_mfma_f32_16x16x32_bf16 v[224:227], a[180:183], v[152:155], v[224:227]\n\t"
    "ds_read_b64_tr_b16 a[202:203], %[vr11] offset:18432\n\t" "v_mfma_f32_16x16x32_bf16 v[240:243], a[176:179], v[148:151], v[240:243]\n\t" "ds_read_b64_tr_b16 a[204:205], %[vr11] offset:24576\n\t"
    "v_mfma_f32_16x16x32_bf16 v[240:243], a[180:183], v[156:159], v[240:243]\n\t" "ds_read_b64_tr_b16 a[206:207], %[vr11] offset:26624\n\t"
    "global_load_dwordx4 a[12:15], %[vx], s[40:41] offset:3072 sc1\n\t" "v_mfma_f32_16x16x32_bf16 v[228:231], a[184:187], v[144:147], v[228:231]\n\t"
    "ds_read_b64_tr_b16 a[176:177], %[vr20] offset:16384\n\t" "v_mfma_f32_16x16x32_bf16 v[228:231], a[188:191], v[152:155], v[228:231]\n\t" "ds_read_b64_tr_b16 a[178:179], %[vr20] offset:18432\n\t"
    "s_add_u32 m0, %[sl0], 90112\n\t" "v_mfma_f32_16x16x32_bf16 v[244:247], a[184:187], v[148:151], v[244:247]\n\t" "ds_read_b64_tr_b16 a[180:181], %[vr20] offset:24576\n\t"
    "v_mfma_f32_16x16x32_bf16 v[244:247], a[188:191], v[156:159], v[244:247]\n\t" "ds_read_b64_tr_b16 a[182:183], %[vr20] offset:26624\n\t" "s_waitcnt lgkmcnt(4)\n\t"
    "v_mfma_f32_16x16x32_bf16 v[232:235], a[192:195], v[144:147], v[232:235]\n\t" "ds_read_b64_tr_b16 a[184:185], %[vr21] offset:16384\n\t" "global_load_lds_dwordx4 %[vb0], %[sb]\n\t"
    "v_mfma_f32_16x16x32_bf16 v[232:235], a[196:199], v[152:155], v[232:235]\n\t" "ds_read_b64_tr_b16 a[186:187], %[vr21] offset:18432\n\t" "v_add_u32 %[vb0], 0x30000, %[vb0]\n\t"
    "v_mfma_f32_16x16x32_bf16 v[248:251], a[192:195], v[148:151], v[248:251]\n\t" "ds_read_b64_tr_b16 a[188:189], %[vr21] offset:24576\n\t" "s_add_u32 m0, %[sl1], 90112\n\t"
    "v_mfma_f32_16x16x32_bf16 v[248:251], a[196:199], v[156:159], v[248:251]\n\t" "ds_read_b64_tr_b16 a[190:191], %[vr21] offset:26624\n\t" "global_load_lds_dwordx4 %[vb1], %[sb]\n\t"
    "v_mfma_f32_16x16x32_bf16 v[236:239], a[200:203], v[144:147], v[236:239]\n\t" "ds_read_b64_tr_b16 a[192:193], %[vr22] offset:16384\n\t" "v_add_u32 %[vb1], 0x30000, %[vb1]\n\t"
    "v_mfma_f32_16x16x32_bf16 v[236:239], a[204:207], v[152:155], v[236:239]\n\t" "ds_read_b64_tr_b16 a[194:195], %[vr22] offset:18432\n\t" "s_add_u32 s40, s40, 8192\n\t" "s_addc_u32 s41, s41, 0\n\t"
    "v_mfma_f32_16x16x32_bf16 v[252:255], a[200:203], v[148:151], v[252:255]\n\t" "ds_read_b64_tr_b16 a[196:197], %[vr22] offset:24576\n\t"
    "v_mfma_f32_16x16x32_bf16 v[252:255], a[204:207], v[156:159], v[252:255]\n\t" "ds_read_b64_tr_b16 a[198:199], %[vr22] offset:26624\n\t" "s_waitcnt lgkmcnt(4)\n\t"
    "v_mfma_f32_16x16x32_bf16 v[96:99], a[176:179], v[144:147], v[96:99]\n\t" "ds_read_b64_tr_b16 a[200:201], %[vr23] offset:16384\n\t" "global_load_dwordx4 a[16:19], %[vx], s[40:41] offset:-4096 sc1\n\t"
    "v_mfma_f32_16x16x32_bf16 v[96:99], a[180:183], v[152:155], v[96:99]\n\t" "ds_read_b64_tr_b16 a[202:203], %[vr23] offset:18432\n\t"
    "v_mfma_f32_16x16x32_bf16 v[100:103], a[176:179], v[148:151], v[100:103]\n\t" "ds_read_b64_tr_b16 a[204:205], %[vr23] offset:24576\n\t"
    "v_mfma_f32_16x16x32_bf16 v[100:103], a[180:183], v[156:159], v[100:103]\n\t" "ds_read_b64_tr_b16 a[206:207], %[vr23] offset:26624\n\t"
    "global_load_dwordx4 a[20:23], %[vx], s[40:41] offset:-3072 sc1\n\t" "v_mfma_f32_16x16x32_bf16 v[104:107], a[184:187], v[144:147], v[104:107]\n\t" "s_barrier\n\t"
    "ds_read_b64_tr_b16 a[176:177], %[vr0] offset:0\n\t" "v_mfma_f32_16x16x32_bf16 v[104:107], a[188:191], v[152:155], v[104:107]\n\t" "ds_read_b64_tr_b16 a[178:179], %[vr0] offset:2048\n\t"
    "s_add_u32 m0, %[sl0], 98304\n\t" "v_mfma_f32_16x16x32_bf16 v[108:111], a[184:187], v[148:151], v[108:111]\n\t" "ds_read_b64_tr_b16 a[180:181], %[vr0] offset:8192\n\t"
    "v_mfma_f32_16x16x32_bf16 v[108:111], a[188:191], v[156:159], v[108:111]\n\t" "ds_read_b64_tr_b16 a[182:183], %[vr0] offset:10240\n\t" "s_waitcnt lgkmcnt(4)\n\t"
    "v_mfma_f32_16x16x32_bf16 v[112:115], a[192:195], v[144:147], v[112:115]\n\t" "ds_read_b64_tr_b16 a[184:185], %[vr1] offset:0\n\t" "global_load_lds_dwordx4 %[vb0], %[sb]\n\t"
    "v_mfma_f32_16x16x32_bf16 v[112:115], a[196:199], v[152:155], v[112:115]\n\t" "ds_read_b64_tr_b16 a[186:187], %[vr1] offset:2048\n\t" "v_add_u32 %[vb0], 0x40000, %[vb0]\n\t"
    "v_mfma_f32_16x16x32_bf16 v[116:119], a[192:195], v[148:151], v[116:119]\n\t" "ds_read_b64_tr_b16 a[188:189], %[vr1] offset:8192\n\t" "s_add_u32 m0, %[sl1], 98304\n\t"
    "v_mfma_f32_16x16x32_bf16 v[116:119], a[196:199], v[156:159], v[116:119]\n\t" "ds_read_b64_tr_b16 a[190:191], %[vr1] offset:10240\n\t" "global_load_lds_dwordx4 %[vb1], %[sb]\n\t"
    "v_mfma_f32_16x16x32_bf16 v[120:123], a[200:203], v[144:147], v[120:123]\n\t" "ds_read_b64_tr_b16 a[192:193], %[vr2] offset:0\n\t" "v_add_u32 %[vb1], 0x40000, %[vb1]\n\t"
    "v_mfma_f32_16x16x32_bf16 v[120:123], a[204:207], v[152:155], v[120:123]\n\t" "ds_read_b64_tr_b16 a[194:195], %[vr2] offset:2048\n\t"
    "v_mfma_f32_16x16x32_bf16 v[124:127], a[200:203], v[148:151], v[124:127]\n\t" "ds_read_b64_tr_b16 a[196:197], %[vr2] offset:8192\n\t"
    "v_mfma_f32_16x16x32_bf16 v[124:127], a[204:207], v[156:159], v[124:127]\n\t" "ds_read_b64_tr_b16 a[198:199], %[vr2] offset:10240\n\t" "s_waitcnt lgkmcnt(4)\n\t"
    "v_mfma_f32_16x16x32_bf16 v[224:227], a[176:179], v[160:163], v[224:227]\n\t" "ds_read_b64_tr_b16 a[200:201], %[vr3] offset:0\n\t" "global_load_dwordx4 a[24:27], %[vx], s[40:41] offset:-2048 sc1\n\t"
    "v_mfma_f32_16x16x32_bf16 v[224:227], a[180:183], v[32:35], v[224:227]\n\t" "ds_read_b64_tr_b16 a[202:203], %[vr3] offset:2048\n\t"
    "v_mfma_f32_16x16x32_bf16 v[240:243], a[176:179], v[48:51], v[240:243]\n\t" "ds_read_b64_tr_b16 a[204:205], %[vr3] offset:8192\n\t"
    "v_mfma_f32_16x16x32_bf16 v[240:243], a[180:183], v[164:167], v[240:243]\n\t" "ds_read_b64_tr_b16 a[206:207], %[vr3] offset:10240\n\t"
    "global_load_dwordx4 a[28:31], %[vx], s[40:41] offset:-1024 sc1\n\t" "v_mfma_f32_16x16x32_bf16 v[228:231], a[184:187], v[160:163], v[228:231]\n\t" "ds_read_b64_tr_b16 a[176:177], %[vr12] offset:0\n\t"
    "v_mfma_f32_16x16x32_bf16 v[228:231], a[188:191], v[32:35], v[228:231]\n\t" "ds_read_b64_tr_b16 a[178:179], %[vr12] offset:2048\n\t" "s_add_u32 m0, %[sl0], 106496\n\t"
    "v_mfma_f32_16x16x32_bf16 v[244:247], a[184:187], v[48:51], v[244:247]\n\t" "ds_read_b64_tr_b16 a[180:181], %[vr12] offset:8192\n\t"
    "v_mfma_f32_16x16x32_bf16 v[244:247], a[188:191], v[164:167], v[244:247]\n\t" "ds_read_b64_tr_b16 a[182:183], %[vr12] offset:10240\n\t" "s_waitcnt lgkmcnt(4)\n\t"
    "v_mfma_f32_16x16x32_bf16 v[232:235], a[192:195], v[160:163], v[232:235]\n\t" "ds_read_b64_tr_b16 a[184:185], %[vr13] offset:0\n\t" "global_load_lds_dwordx4 %[vb0], %[sb]\n\t"
    "v_mfma_f32_16x16x32_bf16 v[232:235], a[196:199], v[32:35], v[232:235]\n\t" "ds_read_b64_tr_b16 a[186:187], %[vr13] offset:2048\n\t" "v_add_u32_e64 %[vb0], %[jw], %[vb0]\n\t"
    "v_mfma_f32_16x16x32_bf16 v[248:251], a[192:195], v[48:51], v[248:251]\n\t" "ds_read_b64_tr_b16 a[188:189], %[vr13] offset:8192\n\t" "s_add_u32 m0, %[sl1], 106496\n\t"
    "v_mfma_f32_16x16x32_bf16 v[248:251], a[196:199], v[164:167], v[248:251]\n\t" "ds_read_b64_tr_b16 a[190:191], %[vr13] offset:10240\n\t" "global_load_lds_dwordx4 %[vb1], %[sb]\n\t"
    "v_mfma_f32_16x16x32_bf16 v[236:239], a[200:203], v[160:163], v[236:239]\n\t" "ds_read_b64_tr_b16 a[192:193], %[vr14] offset:0\n\t" "v_add_u32_e64 %[vb1], %[jw], %[vb1]\n\t"
    "v_mfma_f32_16x16x32_bf16 v[236:239], a[204:207], v[32:35], v[236:239]\n\t" "ds_read_b64_tr_b16 a[194:195], %[vr14] offset:2048\n\t"
    "v_mfma_f32_16x16x32_bf16 v[252:255], a[200:203], v[48:51], v[252:255]\n\t" "ds_read_b64_tr_b16 a[196:197], %[vr14] offset:8192\n\t"
    "v_mfma_f32_16x16x32_bf16 v[252:255], a[204:207], v[164:167], v[252:255]\n\t" "ds_read_b64_tr_b16 a[198:199], %[vr14] offset:10240\n\t" "s_waitcnt lgkmcnt(4)\n\t"
    "v_mfma_f32_16x16x32_bf16 v[96:99], a[176:179], v[160:163], v[96:99]\n\t" "ds_read_b64_tr_b16 a[200:201], %[vr15] offset:0\n\t" "global_load_dwordx4 a[32:35], %[vx], s[40:41] offset:0 sc1\n\t"
    "v_mfma_f32_16x16x32_bf16 v[96:99], a[180:183], v[32:35], v[96:99]\n\t" "ds_read_b64_tr_b16 a[202:203], %[vr15] offset:2048\n\t"
    "v_mfma_f32_16x16x32_bf16 v[100:103], a[176:179], v[48:51], v[100:103]\n\t" "ds_read_b64_tr_b16 a[204:205], %[vr15] offset:8192\n\t"
    "v_mfma_f32_16x16x32_bf16 v[100:103], a[180:183], v[164:167], v[100:103]\n\t" "ds_read_b64_tr_b16 a[206:207], %[vr15] offset:10240\n\t"
    "global_load_dwordx4 a[36:39], %[vx], s[40:41] offset:1024 sc1\n\t" "v_mfma_f32_16x16x32_bf16 v[104:107], a[184:187], v[160:163], v[104:107]\n\t" "s_waitcnt vmcnt(27)\n\t" "s_barrier\n\t"
    "ds_read_b64_tr_b16 a[176:177], %[vr0] offset:49152\n\t" "v_mfma_f32_16x16x32_bf16 v[104:107], a[188:191], v[32:35], v[104:107]\n\t" "ds_read_b64_tr_b16 a[178:179], %[vr0] offset:51200\n\t"
    "s_add_u32 m0, %[sl0], 114688\n\t" "v_mfma_f32_16x16x32_bf16 v[108:111], a[184:187], v[48:51], v[108:111]\n\t" "ds_read_b64_tr_b16 a[180:181], %[vr0] offset:57344\n\t"
    "v_mfma_f32_16x16x32_bf16 v[108:111], a[188:191], v[164:167], v[108:111]\n\t" "ds_read_b64_tr_b16 a[182:183], %[vr0] offset:59392\n\t" "s_waitcnt lgkmcnt(4)\n\t"
    "v_mfma_f32_16x16x32_bf16 v[112:115], a[192:195], v[160:163], v[112:115]\n\t" "ds_read_b64_tr_b16 a[184:185], %[vr1] offset:49152\n\t" "global_load_lds_dwordx4 %[vb0], %[sb]\n\t"
    "v_mfma_f32_16x16x32_bf16 v[112:115], a[196:199], v[32:35], v[112:115]\n\t" "ds_read_b64_tr_b16 a[186:187], %[vr1] offset:51200\n\t" "v_add_u32_e64 %[vb0], %[pw], %[vb0]\n\t"
    "v_mfma_f32_16x16x32_bf16 v[116:119], a[192:195], v[48:51], v[116:119]\n\t" "ds_read_b64_tr_b16 a[188:189], %[vr1] offset:57344\n\t" "s_add_u32 m0, %[sl1], 114688\n\t"
    "v_mfma_f32_16x16x32_bf16 v[116:119], a[196:199], v[164:167], v[116:119]\n\t" "ds_read_b64_tr_b16 a[190:191], %[vr1] offset:59392\n\t" "global_load_lds_dwordx4 %[vb1], %[sb]\n\t"
    "v_mfma_f32_16x16x32_bf16 v[120:123], a[200:203], v[160:163], v[120:123]\n\t" "ds_read_b64_tr_b16 a[192:193], %[vr2] offset:49152\n\t" "v_add_u32_e64 %[vb1], %[pw], %[vb1]\n\t"
    "v_mfma_f32_16x16x32_bf16 v[120:123], a[204:207], v[32:35], v[120:123]\n\t" "ds_read_b64_tr_b16 a[194:195], %[vr2] offset:51200\n\t"
    "v_mfma_f32_16x16x32_bf16 v[124:127], a[200:203], v[48:51], v[124:127]\n\t" "ds_read_b64_tr_b16 a[196:197], %[vr2] offset:57344\n\t"
    "v_mfma_f32_16x16x32_bf16 v[124:127], a[204:207], v[164:167], v[124:127]\n\t" "ds_read_b64_tr_b16 a[198:199], %[vr2] offset:59392\n\t" "s_waitcnt lgkmcnt(4) vmcnt(22)\n\t"
    "v_mfma_f32_16x16x32_bf16 v[224:227], a[176:179], a[64:67], v[224:227]\n\t" "ds_read_b64_tr_b16 a[200:201], %[vr3] offset:49152\n\t" "global_load_dwordx4 a[40:43], %[vx], s[40:41] offset:2048 sc1\n\t"
    "v_mfma_f32_16x16x32_bf16 v[224:227], a[180:183], a[72:75], v[224:227]\n\t" "ds_read_b64_tr_b16 a[202:203], %[vr3] offset:51200\n\t"
    "v_mfma_f32_16x16x32_bf16 v[240:243], a[176:179], a[68:71], v[240:243]\n\t" "ds_read_b64_tr_b16 a[204:205], %[vr3] offset:57344\n\t"
    "v_mfma_f32_16x16x32_bf16 v[240:243], a[180:183], a[76:79], v[240:243]\n\t" "ds_read_b64_tr_b16 a[206:207], %[vr3] offset:59392\n\t" "global_load_dwordx4 a[44:47], %[vx], s[40:41] offset:3072 sc1\n\t"
    "v_mfma_f32_16x16x32_bf16 v[228:231], a[184:187], a[64:67], v[228:231]\n\t" "ds_read_b64_tr_b16 a[176:177], %[vr12] offset:49152\n\t"
    "v_mfma_f32_16x16x32_bf16 v[228:231], a[188:191], a[72:75], v[228:231]\n\t" "ds_read_b64_tr_b16 a[178:179], %[vr12] offset:51200\n\t" "s_add_u32 m0, %[sl0], 122880\n\t"
    "v_mfma_f32_16x16x32_bf16 v[244:247], a[184:187], a[68:71], v[244:247]\n\t" "ds_read_b64_tr_b16 a[180:181], %[vr12] offset:57344\n\t"
    "v_mfma_f32_16x16x32_bf16 v[244:247], a[188:191], a[76:79], v[244:247]\n\t" "ds_read_b64_tr_b16 a[182:183], %[vr12] offset:59392\n\t" "s_waitcnt lgkmcnt(4)\n\t"
    "v_mfma_f32_16x16x32_bf16 v[232:235], a[192:195], a[64:67], v[232:235]\n\t" "ds_read_b64_tr_b16 a[184:185], %[vr13] offset:49152\n\t" "global_load_lds_dwordx4 %[vb0], %[sb]\n\t"
    "v_mfma_f32_16x16x32_bf16 v[232:235], a[196:199], a[72:75], v[232:235]\n\t" "ds_read_b64_tr_b16 a[186:187], %[vr13] offset:51200\n\t" "v_add_u32 %[vb0], 0x10000, %[vb0]\n\t"
    "v_mfma_f32_16x16x32_bf16 v[248:251], a[192:195], a[68:71], v[248:251]\n\t" "ds_read_b64_tr_b16 a[188:189], %[vr13] offset:57344\n\t" "s_add_u32 m0, %[sl1], 122880\n\t"
    "v_mfma_f32_16x16x32_bf16 v[248:251], a[196:199], a[76:79], v[248:251]\n\t" "ds_read_b64_tr_b16 a[190:191], %[vr13] offset:59392\n\t" "global_load_lds_dwordx4 %[vb1], %[sb]\n\t"
    "v_mfma_f32_16x16x32_bf16 v[236:239], a[200:203], a[64:67], v[236:239]\n\t" "ds_read_b64_tr_b16 a[192:193], %[vr14] offset:49152\n\t" "v_add_u32 %[vb1], 0x10000, %[vb1]\n\t"
    "v_mfma_f32_16x16x32_bf16 v[236:239], a[204:207], a[72:75], v[236:239]\n\t" "ds_read_b64_tr_b16 a[194:195], %[vr14] offset:51200\n\t" "s_add_u32 m0, %[sl0], 131072\n\t"
    "v_mfma_f32_16x16x32_bf16 v[252:255], a[200:203], a[68:71], v[252:255]\n\t" "ds_read_b64_tr_b16 a[196:197], %[vr14] offset:57344\n\t"
    "v_mfma_f32_16x16x32_bf16 v[252:255], a[204:207], a[76:79], v[252:255]\n\t" "ds_read_b64_tr_b16 a[198:199], %[vr14] offset:59392\n\t" "s_waitcnt lgkmcnt(4)\n\t"
    "v_mfma_f32_16x16x32_bf16 v[96:99], a[176:179], a[64:67], v[96:99]\n\t" "ds_read_b64_tr_b16 a[200:201], %[vr15] offset:49152\n\t" "global_load_lds_dwordx4 %[vb0], %[sb]\n\t"
    "v_mfma_f32_16x16x32_bf16 v[96:99], a[180:183], a[72:75], v[96:99]\n\t" "ds_read_b64_tr_b16 a[202:203], %[vr15] offset:51200\n\t" "v_add_u32 %[vb0], %[qw], %[vb0]\n\t"
    "v_mfma_f32_16x16x32_bf16 v[100:103], a[176:179], a[68:71], v[100:103]\n\t" "ds_read_b64_tr_b16 a[204:205], %[vr15] offset:57344\n\t" "s_add_u32 m0, %[sl1], 131072\n\t"
    "v_mfma_f32_16x16x32_bf16 v[100:103], a[180:183], a[76:79], v[100:103]\n\t" "ds_read_b64_tr_b16 a[206:207], %[vr15] offset:59392\n\t" "global_load_lds_dwordx4 %[vb1], %[sb]\n\t"
    "v_mfma_f32_16x16x32_bf16 v[104:107], a[184:187], a[64:67], v[104:107]\n\t" "s_waitcnt vmcnt(26)\n\t" "s_barrier\n\t" "ds_read_b64_tr_b16 a[176:177], %[vr4] offset:0\n\t"
    "v_mfma_f32_16x16x32_bf16 v[104:107], a[188:191], a[72:75], v[104:107]\n\t" "ds_read_b64_tr_b16 a[178:179], %[vr4] offset:2048\n\t" "v_add_u32_e64 %[vb1], %[qw], %[vb1]\n\t"
    "v_mfma_f32_16x16x32_bf16 v[108:111], a[184:187], a[68:71], v[108:111]\n\t" "ds_read_b64_tr_b16 a[180:181], %[vr4] offset:8192\n\t" "s_add_u32 m0, %[sl0], 16384\n\t"
    "v_mfma_f32_16x16x32_bf16 v[108:111], a[188:191], a[76:79], v[108:111]\n\t" "ds_read_b64_tr_b16 a[182:183], %[vr4] offset:10240\n\t" "s_waitcnt lgkmcnt(4)\n\t"
    "v_mfma_f32_16x16x32_bf16 v[112:115], a[192:195], a[64:67], v[112:115]\n\t" "ds_read_b64_tr_b16 a[184:185], %[vr5] offset:0\n\t" "global_load_lds_dwordx4 %[vb0], %[sb]\n\t"
    "v_mfma_f32_16x16x32_bf16 v[112:115], a[196:199], a[72:75], v[112:115]\n\t" "ds_read_b64_tr_b16 a[186:187], %[vr5] offset:2048\n\t" "v_add_u32 %[vb0], %[pw], %[vb0]\n\t"
    "v_mfma_f32_16x16x32_bf16 v[116:119], a[192:195], a[68:71], v[116:119]\n\t" "ds_read_b64_tr_b16 a[188:189], %[vr5] offset:8192\n\t" "s_add_u32 m0, %[sl1], 16384\n\t"
    "v_mfma_f32_16x16x32_bf16 v[116:119], a[196:199], a[76:79], v[116:119]\n\t" "ds_read_b64_tr_b16 a[190:191], %[vr5] offset:10240\n\t" "global_load_lds_dwordx4 %[vb1], %[sb]\n\t"
    "v_mfma_f32_16x16x32_bf16 v[120:123], a[200:203], a[64:67], v[120:123]\n\t" "ds_read_b64_tr_b16 a[192:193], %[vr6] offset:0\n\t" "v_add_u32_e64 %[vb1], %[pw], %[vb1]\n\t"
    "v_mfma_f32_16x16x32_bf16 v[120:123], a[204:207], a[72:75], v[120:123]\n\t" "ds_read_b64_tr_b16 a[194:195], %[vr6] offset:2048\n\t" "s_add_u32 m0, %[sl0], 24576\n\t"
    "v_mfma_f32_16x16x32_bf16 v[124:127], a[200:203], a[68:71], v[124:127]\n\t" "ds_read_b64_tr_b16 a[196:197], %[vr6] offset:8192\n\t"
    "v_mfma_f32_16x16x32_bf16 v[124:127], a[204:207], a[76:79], v[124:127]\n\t" "ds_read_b64_tr_b16 a[198:199], %[vr6] offset:10240\n\t" "s_waitcnt lgkmcnt(4) vmcnt(22)\n\t"
    "v_mfma_f32_16x16x32_bf16 v[224:227], a[176:179], a[0:3], v[224:227]\n\t" "ds_read_b64_tr_b16 a[200:201], %[vr7] offset:0\n\t" "global_load_lds_dwordx4 %[vb0], %[sb]\n\t"
    "v_mfma_f32_16x16x32_bf16 v[224:227], a[180:183], a[8:11], v[224:227]\n\t" "ds_read_b64_tr_b16 a[202:203], %[vr7] offset:2048\n\t" "v_add_u32 %[vb0], %[qw], %[vb0]\n\t"
    "v_mfma_f32_16x16x32_bf16 v[240:243], a[176:179], a[4:7], v[240:243]\n\t" "ds_read_b64_tr_b16 a[204:205], %[vr7] offset:8192\n\t" "s_add_u32 m0, %[sl1], 24576\n\t"
    "global_load_dwordx2 %[fl], %[vf], %[sw] sc1\n\t" "v_mfma_f32_16x16x32_bf16 v[240:243], a[180:183], a[12:15], v[240:243]\n\t" "ds_read_b64_tr_b16 a[206:207], %[vr7] offset:10240\n\t"
    "global_load_lds_dwordx4 %[vb1], %[sb]\n\t" "v_mfma_f32_16x16x32_bf16 v[228:231], a[184:187], a[0:3], v[228:231]\n\t" "ds_read_b64_tr_b16 a[176:177], %[vr16] offset:0\n\t"
    "v_add_u32_e64 %[vb1], %[qw], %[vb1]\n\t" "v_mfma_f32_16x16x32_bf16 v[228:231], a[188:191], a[8:11], v[228:231]\n\t" "ds_read_b64_tr_b16 a[178:179], %[vr16] offset:2048\n\t"
    "s_add_u32 m0, %[sl0], 32768\n\t" "v_mfma_f32_16x16x32_bf16 v[244:247], a[184:187], a[4:7], v[244:247]\n\t" "ds_read_b64_tr_b16 a[180:181], %[vr16] offset:8192\n\t"
    "v_mfma_f32_16x16x32_bf16 v[244:247], a[188:191], a[12:15], v[244:247]\n\t" "ds_read_b64_tr_b16 a[182:183], %[vr16] offset:10240\n\t" "s_waitcnt lgkmcnt(4)\n\t"
    "v_mfma_f32_16x16x32_bf16 v[232:235], a[192:195], a[0:3], v[232:235]\n\t" "ds_read_b64_tr_b16 a[184:185], %[vr17] offset:0\n\t" "global_load_lds_dwordx4 %[vb0], %[sb]\n\t"
    "v_mfma_f32_16x16x32_bf16 v[232:235], a[196:199], a[8:11], v[232:235]\n\t" "ds_read_b64_tr_b16 a[186:187], %[vr17] offset:2048\n\t" "v_add_u32 %[vb0], %[pw], %[vb0]\n\t"
    "v_mfma_f32_16x16x32_bf16 v[248:251], a[192:195], a[4:7], v[248:251]\n\t" "ds_read_b64_tr_b16 a[188:189], %[vr17] offset:8192\n\t" "s_add_u32 m0, %[sl1], 32768\n\t"
    "v_mfma_f32_16x16x32_bf16 v[248:251], a[196:199], a[12:15], v[248:251]\n\t" "ds_read_b64_tr_b16 a[190:191], %[vr17] offset:10240\n\t" "global_load_lds_dwordx4 %[vb1], %[sb]\n\t"
    "v_mfma_f32_16x16x32_bf16 v[236:239], a[200:203], a[0:3], v[236:239]\n\t" "ds_read_b64_tr_b16 a[192:193], %[vr18] offset:0\n\t" "v_add_u32_e64 %[vb1], %[pw], %[vb1]\n\t"
    "v_mfma_f32_16x16x32_bf16 v[236:239], a[204:207], a[8:11], v[236:239]\n\t" "ds_read_b64_tr_b16 a[194:195], %[vr18] offset:2048\n\t" "s_add_u32 m0, %[sl0], 40960\n\t"
    "v_mfma_f32_16x16x32_bf16 v[252:255], a[200:203], a[4:7], v[252:255]\n\t" "ds_read_b64_tr_b16 a[196:197], %[vr18] offset:8192\n\t"
    "v_mfma_f32_16x16x32_bf16 v[252:255], a[204:207], a[12:15], v[252:255]\n\t" "ds_read_b64_tr_b16 a[198:199], %[vr18] offset:10240\n\t" "s_waitcnt lgkmcnt(4)\n\t"
    "v_mfma_f32_16x16x32_bf16 v[96:99], a[176:179], a[0:3], v[96:99]\n\t" "ds_read_b64_tr_b16 a[200:201], %[vr19] offset:0\n\t" "global_load_lds_dwordx4 %[vb0], %[sb]\n\t"
    "v_mfma_f32_16x16x32_bf16 v[96:99], a[180:183], a[8:11], v[96:99]\n\t" "ds_read_b64_tr_b16 a[202:203], %[vr19] offset:2048\n\t" "v_add_u32 %[vb0], 0x10000, %[vb0]\n\t"
    "v_mfma_f32_16x16x32_bf16 v[100:103], a[176:179], a[4:7], v[100:103]\n\t" "ds_read_b64_tr_b16 a[204:205], %[vr19] offset:8192\n\t" "s_add_u32 m0, %[sl1], 40960\n\t"
    "v_mfma_f32_16x16x32_bf16 v[100:103], a[180:183], a[12:15], v[100:103]\n\t" "ds_read_b64_tr_b16 a[206:207], %[vr19] offset:10240\n\t" "global_load_lds_dwordx4 %[vb1], %[sb]\n\t"
    "v_mfma_f32_16x16x32_bf16 v[104:107], a[184:187], a[0:3], v[104:107]\n\t" "s_waitcnt vmcnt(27)\n\t" "s_barrier\n\t" "ds_read_b64_tr_b16 a[176:177], %[vr4] offset:16384\n\t"
    "v_mfma_f32_16x16x32_bf16 v[104:107], a[188:191], a[8:11], v[104:107]\n\t" "ds_read_b64_tr_b16 a[178:179], %[vr4] offset:18432\n\t" "v_add_u32 %[vb1], 0x10000, %[vb1]\n\t"
    "v_mfma_f32_16x16x32_bf16 v[108:111], a[184:187], a[4:7], v[108:111]\n\t" "ds_read_b64_tr_b16 a[180:181], %[vr4] offset:24576\n\t" "s_add_u32 m0, %[sl0], 139264\n\t"
    "v_mfma_f32_16x16x32_bf16 v[108:111], a[188:191], a[12:15], v[108:111]\n\t" "ds_read_b64_tr_b16 a[182:183], %[vr4] offset:26624\n\t" "s_waitcnt lgkmcnt(4)\n\t"
    "v_mfma_f32_16x16x32_bf16 v[112:115], a[192:195], a[0:3], v[112:115]\n\t" "ds_read_b64_tr_b16 a[184:185], %[vr5] offset:16384\n\t" "global_load_lds_dwordx4 %[vb0], %[sb]\n\t"
    "v_mfma_f32_16x16x32_bf16 v[112:115], a[196:199], a[8:11], v[112:115]\n\t" "ds_read_b64_tr_b16 a[186:187], %[vr5] offset:18432\n\t" "v_add_u32_e64 %[vb0], %[qw], %[vb0]\n\t"
    "v_mfma_f32_16x16x32_bf16 v[116:119], a[192:195], a[4:7], v[116:119]\n\t" "ds_read_b64_tr_b16 a[188:189], %[vr5] offset:24576\n\t" "s_add_u32 m0, %[sl1], 139264\n\t"
    "v_mfma_f32_16x16x32_bf16 v[116:119], a[196:199], a[12:15], v[116:119]\n\t" "ds_read_b64_tr_b16 a[190:191], %[vr5] offset:26624\n\t" "global_load_lds_dwordx4 %[vb1], %[sb]\n\t"
    "v_mfma_f32_16x16x32_bf16 v[120:123], a[200:203], a[0:3], v[120:123]\n\t" "ds_read_b64_tr_b16 a[192:193], %[vr6] offset:16384\n\t" "v_add_u32_e64 %[vb1], %[qw], %[vb1]\n\t"
    "v_mfma_f32_16x16x32_bf16 v[120:123], a[204:207], a[8:11], v[120:123]\n\t" "ds_read_b64_tr_b16 a[194:195], %[vr6] offset:18432\n\t" "s_add_u32 m0, %[sl0], 147456\n\t"
    "v_mfma_f32_16x16x32_bf16 v[124:127], a[200:203], a[4:7], v[124:127]\n\t" "ds_read_b64_tr_b16 a[196:197], %[vr6] offset:24576\n\t"
    "v_mfma_f32_16x16x32_bf16 v[124:127], a[204:207], a[12:15], v[124:127]\n\t" "ds_read_b64_tr_b16 a[198:199], %[vr6] offset:26624\n\t" "s_waitcnt lgkmcnt(4) vmcnt(23)\n\t"
    "v_mfma_f32_16x16x32_bf16 v[224:227], a[176:179], a[16:19], v[224:227]\n\t" "ds_read_b64_tr_b16 a[200:201], %[vr7] offset:16384\n\t" "global_load_lds_dwordx4 %[vb0], %[sb]\n\t"
    "v_mfma_f32_16x16x32_bf16 v[224:227], a[180:183], a[24:27], v[224:227]\n\t" "ds_read_b64_tr_b16 a[202:203], %[vr7] offset:18432\n\t" "v_add_u32 %[vb0], %[pw], %[vb0]\n\t"
    "v_mfma_f32_16x16x32_bf16 v[240:243], a[176:179], a[20:23], v[240:243]\n\t" "ds_read_b64_tr_b16 a[204:205], %[vr7] offset:24576\n\t" "s_add_u32 m0, %[sl1], 147456\n\t" "s_waitcnt vmcnt(8)\n\t"
    "v_cmp_ne_u64 vcc, %[tag], %[fl]\n\t" "v_mfma_f32_16x16x32_bf16 v[240:243], a[180:183], a[28:31], v[240:243]\n\t" "ds_read_b64_tr_b16 a[206:207], %[vr7] offset:26624\n\t"
    "global_load_lds_dwordx4 %[vb1], %[sb]\n\t" "v_mfma_f32_16x16x32_bf16 v[228:231], a[184:187], a[16:19], v[228:231]\n\t" "ds_read_b64_tr_b16 a[176:177], %[vr16] offset:16384\n\t"
    "v_add_u32_e64 %[vb1], %[pw], %[vb1]\n\t" "v_mfma_f32_16x16x32_bf16 v[228:231], a[188:191], a[24:27], v[228:231]\n\t" "ds_read_b64_tr_b16 a[178:179], %[vr16] offset:18432\n\t"
    "s_add_u32 m0, %[sl0], 155648\n\t" "s_cbranch_vccnz .Lw2_%=\n\t" ".Lg2_%=:\n\t" "v_mfma_f32_16x16x32_bf16 v[244:247], a[184:187], a[20:23], v[244:247]\n\t"
    "ds_read_b64_tr_b16 a[180:181], %[vr16] offset:24576\n\t" "v_mfma_f32_16x16x32_bf16 v[244:247], a[188:191], a[28:31], v[244:247]\n\t" "ds_read_b64_tr_b16 a[182:183], %[vr16] offset:26624\n\t"
    "s_waitcnt lgkmcnt(4)\n\t" "v_mfma_f32_16x16x32_bf16 v[232:235], a[192:195], a[16:19], v[232:235]\n\t" "ds_read_b64_tr_b16 a[184:185], %[vr17] offset:16384\n\t"
    "global_load_lds_dwordx4 %[vb0], %[sb]\n\t" "v_mfma_f32_16x16x32_bf16 v[232:235], a[196:199], a[24:27], v[232:235]\n\t" "ds_read_b64_tr_b16 a[186:187], %[vr17] offset:18432\n\t"
    "v_add_u32_e64 %[vb0], %[qw], %[vb0]\n\t" "v_mfma_f32_16x16x32_bf16 v[248:251], a[192:195], a[20:23], v[248:251]\n\t" "ds_read_b64_tr_b16 a[188:189], %[vr17] offset:24576\n\t"
    "s_add_u32 s40, s40, 8192\n\t" "s_addc_u32 s41, s41, 0\n\t" "v_mfma_f32_16x16x32_bf16 v[248:251], a[196:199], a[28:31], v[248:251]\n\t" "ds_read_b64_tr_b16 a[190:191], %[vr17] offset:26624\n\t"
    "global_load_dwordx4 a[48:51], %[vx], s[40:41] offset:-4096 sc1\n\t" "v_mfma_f32_16x16x32_bf16 v[236:239], a[200:203], a[16:19], v[236:239]\n\t"
    "ds_read_b64_tr_b16 a[192:193], %[vr18] offset:16384\n\t" "v_mfma_f32_16x16x32_bf16 v[236:239], a[204:207], a[24:27], v[236:239]\n\t" "ds_read_b64_tr_b16 a[194:195], %[vr18] offset:18432\n\t"
    "v_mfma_f32_16x16x32_bf16 v[252:255], a[200:203], a[20:23], v[252:255]\n\t" "ds_read_b64_tr_b16 a[196:197], %[vr18] offset:24576\n\t"
    "v_mfma_f32_16x16x32_bf16 v[252:255], a[204:207], a[28:31], v[252:255]\n\t" "ds_read_b64_tr_b16 a[198:199], %[vr18] offset:26624\n\t" "s_waitcnt lgkmcnt(4)\n\t"
    "v_mfma_f32_16x16x32_bf16 v[96:99], a[176:179], a[16:19], v[96:99]\n\t" "ds_read_b64_tr_b16 a[200:201], %[vr19] offset:16384\n\t" "global_load_dwordx4 a[52:55], %[vx], s[40:41] offset:-3072 sc1\n\t"
    "v_mfma_f32_16x16x32_bf16 v[96:99], a[180:183], a[24:27], v[96:99]\n\t" "ds_read_b64_tr_b16 a[202:203], %[vr19] offset:18432\n\t"
    "v_mfma_f32_16x16x32_bf16 v[100:103], a[176:179], a[20:23], v[100:103]\n\t" "ds_read_b64_tr_b16 a[204:205], %[vr19] offset:24576\n\t"
    "v_mfma_f32_16x16x32_bf16 v[100:103], a[180:183], a[28:31], v[100:103]\n\t" "ds_read_b64_tr_b16 a[206:207], %[vr19] offset:26624\n\t"
    "global_load_dwordx4 a[56:59], %[vx], s[40:41] offset:-2048 sc1\n\t" "v_mfma_f32_16x16x32_bf16 v[104:107], a[184:187], a[16:19], v[104:107]\n\t" "s_barrier\n\t"
    "ds_read_b64_tr_b16 a[176:177], %[vr4] offset:32768\n\t" "v_mfma_f32_16x16x32_bf16 v[104:107], a[188:191], a[24:27], v[104:107]\n\t" "ds_read_b64_tr_b16 a[178:179], %[vr4] offset:34816\n\t"
    "v_mfma_f32_16x16x32_bf16 v[108:111], a[184:187], a[20:23], v[108:111]\n\t" "ds_read_b64_tr_b16 a[180:181], %[vr4] offset:40960\n\t"
    "v_mfma_f32_16x16x32_bf16 v[108:111], a[188:191], a[28:31], v[108:111]\n\t" "ds_read_b64_tr_b16 a[182:183], %[vr4] offset:43008\n\t" "s_waitcnt lgkmcnt(4)\n\t"
    "v_mfma_f32_16x16x32_bf16 v[112:115], a[192:195], a[16:19], v[112:115]\n\t" "ds_read_b64_tr_b16 a[184:185], %[vr5] offset:32768\n\t"
    "global_load_dwordx4 a[60:63], %[vx], s[40:41] offset:-1024 sc1\n\t" "v_mfma_f32_16x16x32_bf16 v[112:115], a[196:199], a[24:27], v[112:115]\n\t"
    "ds_read_b64_tr_b16 a[186:187], %[vr5] offset:34816\n\t" "v_mfma_f32_16x16x32_bf16 v[116:119], a[192:195], a[20:23], v[116:119]\n\t" "ds_read_b64_tr_b16 a[188:189], %[vr5] offset:40960\n\t"
    "v_mfma_f32_16x16x32_bf16 v[116:119], a[196:199], a[28:31], v[116:119]\n\t" "ds_read_b64_tr_b16 a[190:191], %[vr5] offset:43008\n\t" "global_load_dwordx2 a[64:65], %[vo], s[40:41] offset:0 sc1\n\t"
    "v_mfma_f32_16x16x32_bf16 v[120:123], a[200:203], a[16:19], v[120:123]\n\t" "ds_read_b64_tr_b16 a[192:193], %[vr6] offset:32768\n\t"
    "v_mfma_f32_16x16x32_bf16 v[120:123], a[204:207], a[24:27], v[120:123]\n\t" "ds_read_b64_tr_b16 a[194:195], %[vr6] offset:34816\n\t" "global_load_dwordx2 a[66:67], %[vo], s[40:41] offset:1024 sc1\n\t"
    "v_mfma_f32_16x16x32_bf16 v[124:127], a[200:203], a[20:23], v[124:127]\n\t" "ds_read_b64_tr_b16 a[196:197], %[vr6] offset:40960\n\t"
    "v_mfma_f32_16x16x32_bf16 v[124:127], a[204:207], a[28:31], v[124:127]\n\t" "ds_read_b64_tr_b16 a[198:199], %[vr6] offset:43008\n\t" "s_waitcnt lgkmcnt(4)\n\t"
    "v_mfma_f32_16x16x32_bf16 v[224:227], a[176:179], a[32:35], v[224:227]\n\t" "ds_read_b64_tr_b16 a[200:201], %[vr7] offset:32768\n\t" "global_load_dwordx2 a[68:69], %[vo], s[40:41] offset:512 sc1\n\t"
    "v_mfma_f32_16x16x32_bf16 v[224:227], a[180:183], a[40:43], v[224:227]\n\t" "ds_read_b64_tr_b16 a[202:203], %[vr7] offset:34816\n\t" "global_load_dwordx2 a[70:71], %[vo], s[40:41] offset:1536 sc1\n\t"
    "v_mfma_f32_16x16x32_bf16 v[240:243], a[176:179], a[36:39], v[240:243]\n\t" "ds_read_b64_tr_b16 a[204:205], %[vr7] offset:40960\n\t"
    "v_mfma_f32_16x16x32_bf16 v[240:243], a[180:183], a[44:47], v[240:243]\n\t" "ds_read_b64_tr_b16 a[206:207], %[vr7] offset:43008\n\t" "global_load_dword a255, %[vx], %[cd]\n\t"
    "v_mfma_f32_16x16x32_bf16 v[228:231], a[184:187], a[32:35], v[228:231]\n\t" "ds_read_b64_tr_b16 a[176:177], %[vr16] offset:32768\n\t"
    "v_mfma_f32_16x16x32_bf16 v[228:231], a[188:191], a[40:43], v[228:231]\n\t" "ds_read_b64_tr_b16 a[178:179], %[vr16] offset:34816\n\t"
    "global_load_dwordx4 a[72:75], %[vx], s[40:41] offset:2048 sc1\n\t" "v_mfma_f32_16x16x32_bf16 v[244:247], a[184:187], a[36:39], v[244:247]\n\t"
    "ds_read_b64_tr_b16 a[180:181], %[vr16] offset:40960\n\t" "v_mfma_f32_16x16x32_bf16 v[244:247], a[188:191], a[44:47], v[244:247]\n\t" "ds_read_b64_tr_b16 a[182:183], %[vr16] offset:43008\n\t"
    "s_waitcnt lgkmcnt(4)\n\t" "v_mfma_f32_16x16x32_bf16 v[232:235], a[192:195], a[32:35], v[232:235]\n\t" "ds_read_b64_tr_b16 a[184:185], %[vr17] offset:32768\n\t"
    "v_mfma_f32_16x16x32_bf16 v[232:235], a[196:199], a[40:43], v[232:235]\n\t" "ds_read_b64_tr_b16 a[186:187], %[vr17] offset:34816\n\t"
    "global_load_dwordx4 a[76:79], %[vx], s[40:41] offset:3072 sc1\n\t" "v_mfma_f32_16x16x32_bf16 v[248:251], a[192:195], a[36:39], v[248:251]\n\t"
    "ds_read_b64_tr_b16 a[188:189], %[vr17] offset:40960\n\t" "v_mfma_f32_16x16x32_bf16 v[248:251], a[196:199], a[44:47], v[248:251]\n\t" "ds_read_b64_tr_b16 a[190:191], %[vr17] offset:43008\n\t"
    "s_add_u32 s40, s40, 8192\n\t" "s_addc_u32 s41, s41, 0\n\t" "v_mfma_f32_16x16x32_bf16 v[236:239], a[200:203], a[32:35], v[236:239]\n\t" "ds_read_b64_tr_b16 a[192:193], %[vr18] offset:32768\n\t"
    "v_mfma_f32_16x16x32_bf16 v[236:239], a[204:207], a[40:43], v[236:239]\n\t" "ds_read_b64_tr_b16 a[194:195], %[vr18] offset:34816\n\t" "global_load_dwordx4 a[0:3], %[vx], s[40:41] offset:-4096 sc1\n\t"
    "v_mfma_f32_16x16x32_bf16 v[252:255], a[200:203], a[36:39], v[252:255]\n\t" "ds_read_b64_tr_b16 a[196:197], %[vr18] offset:40960\n\t"
    "v_mfma_f32_16x16x32_bf16 v[252:255], a[204:207], a[44:47], v[252:255]\n\t" "ds_read_b64_tr_b16 a[198:199], %[vr18] offset:43008\n\t" "s_waitcnt lgkmcnt(4)\n\t"
    "v_mfma_f32_16x16x32_bf16 v[96:99], a[176:179], a[32:35], v[96:99]\n\t" "ds_read_b64_tr_b16 a[200:201], %[vr19] offset:32768\n\t"
    "v_mfma_f32_16x16x32_bf16 v[96:99], a[180:183], a[40:43], v[96:99]\n\t" "ds_read_b64_tr_b16 a[202:203], %[vr19] offset:34816\n\t" "global_load_dwordx4 a[4:7], %[vx], s[40:41] offset:-3072 sc1\n\t"
    "v_mfma_f32_16x16x32_bf16 v[100:103], a[176:179], a[36:39], v[100:103]\n\t" "ds_read_b64_tr_b16 a[204:205], %[vr19] offset:40960\n\t"
    "v_mfma_f32_16x16x32_bf16 v[100:103], a[180:183], a[44:47], v[100:103]\n\t" "ds_read_b64_tr_b16 a[206:207], %[vr19] offset:43008\n\t"
    "v_mfma_f32_16x16x32_bf16 v[104:107], a[184:187], a[32:35], v[104:107]\n\t" "s_barrier\n\t" "ds_read_b64_tr_b16 a[176:177], %[vr4] offset:49152\n\t"
    "v_mfma_f32_16x16x32_bf16 v[104:107], a[188:191], a[40:43], v[104:107]\n\t" "ds_read_b64_tr_b16 a[178:179], %[vr4] offset:51200\n\t" "global_load_dwordx4 a[8:11], %[vx], s[40:41] offset:-2048 sc1\n\t"
    "v_mfma_f32_16x16x32_bf16 v[108:111], a[184:187], a[36:39], v[108:111]\n\t" "ds_read_b64_tr_b16 a[180:181], %[vr4] offset:57344\n\t"
    "v_mfma_f32_16x16x32_bf16 v[108:111], a[188:191], a[44:47], v[108:111]\n\t" "ds_read_b64_tr_b16 a[182:183], %[vr4] offset:59392\n\t" "s_waitcnt lgkmcnt(4)\n\t"
    "v_mfma_f32_16x16x32_bf16 v[112:115], a[192:195], a[32:35], v[112:115]\n\t" "ds_read_b64_tr_b16 a[184:185], %[vr5] offset:49152\n\t"
    "v_mfma_f32_16x16x32_bf16 v[112:115], a[196:199], a[40:43], v[112:115]\n\t" "ds_read_b64_tr_b16 a[186:187], %[vr5] offset:51200\n\t"
    "global_load_dwordx4 a[12:15], %[vx], s[40:41] offset:-1024 sc1\n\t" "v_mfma_f32_16x16x32_bf16 v[116:119], a[192:195], a[36:39], v[116:119]\n\t"
    "ds_read_b64_tr_b16 a[188:189], %[vr5] offset:57344\n\t" "v_mfma_f32_16x16x32_bf16 v[116:119], a[196:199], a[44:47], v[116:119]\n\t" "ds_read_b64_tr_b16 a[190:191], %[vr5] offset:59392\n\t"
    "v_mfma_f32_16x16x32_bf16 v[120:123], a[200:203], a[32:35], v[120:123]\n\t" "ds_read_b64_tr_b16 a[192:193], %[vr6] offset:49152\n\t"
    "v_mfma_f32_16x16x32_bf16 v[120:123], a[204:207], a[40:43], v[120:123]\n\t" "ds_read_b64_tr_b16 a[194:195], %[vr6] offset:51200\n\t" "global_load_dwordx4 a[16:19], %[vx], s[40:41] offset:0 sc1\n\t"
    "v_mfma_f32_16x16x32_bf16 v[124:127], a[200:203], a[36:39], v[124:127]\n\t" "ds_read_b64_tr_b16 a[196:197], %[vr6] offset:57344\n\t"
    "v_mfma_f32_16x16x32_bf16 v[124:127], a[204:207], a[44:47], v[124:127]\n\t" "ds_read_b64_tr_b16 a[198:199], %[vr6] offset:59392\n\t" "s_waitcnt lgkmcnt(4) vmcnt(12)\n\t"
    "v_mfma_f32_16x16x32_bf16 v[224:227], a[176:179], a[48:51], v[224:227]\n\t" "ds_read_b64_tr_b16 a[200:201], %[vr7] offset:49152\n\t"
    "v_mfma_f32_16x16x32_bf16 v[224:227], a[180:183], a[56:59], v[224:227]\n\t" "ds_read_b64_tr_b16 a[202:203], %[vr7] offset:51200\n\t" "global_load_dwordx4 a[20:23], %[vx], s[40:41] offset:1024 sc1\n\t"
    "v_mfma_f32_16x16x32_bf16 v[240:243], a[176:179], a[52:55], v[240:243]\n\t" "ds_read_b64_tr_b16 a[204:205], %[vr7] offset:57344\n\t"
    "v_mfma_f32_16x16x32_bf16 v[240:243], a[180:183], a[60:63], v[240:243]\n\t" "ds_read_b64_tr_b16 a[206:207], %[vr7] offset:59392\n\t"
    "v_mfma_f32_16x16x32_bf16 v[228:231], a[184:187], a[48:51], v[228:231]\n\t" "ds_read_b64_tr_b16 a[176:177], %[vr16] offset:49152\n\t"
    "v_mfma_f32_16x16x32_bf16 v[228:231], a[188:191], a[56:59], v[228:231]\n\t" "ds_read_b64_tr_b16 a[178:179], %[vr16] offset:51200\n\t"
    "global_load_dwordx2 a[24:25], %[vo], s[40:41] offset:2048 sc1\n\t" "v_mfma_f32_16x16x32_bf16 v[244:247], a[184:187], a[52:55], v[244:247]\n\t"
    "ds_read_b64_tr_b16 a[180:181], %[vr16] offset:57344\n\t" "v_mfma_f32_16x16x32_bf16 v[244:247], a[188:191], a[60:63], v[244:247]\n\t" "ds_read_b64_tr_b16 a[182:183], %[vr16] offset:59392\n\t"
    "s_waitcnt lgkmcnt(4)\n\t" "v_mfma_f32_16x16x32_bf16 v[232:235], a[192:195], a[48:51], v[232:235]\n\t" "ds_read_b64_tr_b16 a[184:185], %[vr17] offset:49152\n\t"
    "global_load_dwordx2 a[26:27], %[vo], s[40:41] offset:3072 sc1\n\t" "v_mfma_f32_16x16x32_bf16 v[232:235], a[196:199], a[56:59], v[232:235]\n\t"
    "ds_read_b64_tr_b16 a[186:187], %[vr17] offset:51200\n\t" "global_load_dwordx2 a[28:29], %[vo], s[40:41] offset:2560 sc1\n\t"
    "v_mfma_f32_16x16x32_bf16 v[248:251], a[192:195], a[52:55], v[248:251]\n\t" "ds_read_b64_tr_b16 a[188:189], %[vr17] offset:57344\n\t"
    "v_mfma_f32_16x16x32_bf16 v[248:251], a[196:199], a[60:63], v[248:251]\n\t" "ds_read_b64_tr_b16 a[190:191], %[vr17] offset:59392\n\t"
    "global_load_dwordx2 a[30:31], %[vo], s[40:41] offset:3584 sc1\n\t" "v_mfma_f32_16x16x32_bf16 v[236:239], a[200:203], a[48:51], v[236:239]\n\t"
    "ds_read_b64_tr_b16 a[192:193], %[vr18] offset:49152\n\t" "s_add_u32 m0, %[sl1], 155648\n\t" "v_mfma_f32_16x16x32_bf16 v[236:239], a[204:207], a[56:59], v[236:239]\n\t"
    "ds_read_b64_tr_b16 a[194:195], %[vr18] offset:51200\n\t" "global_load_lds_dwordx4 %[vb1], %[sb]\n\t" "v_mfma_f32_16x16x32_bf16 v[252:255], a[200:203], a[52:55], v[252:255]\n\t"
    "ds_read_b64_tr_b16 a[196:197], %[vr18] offset:57344\n\t" "v_add_u32_e64 %[vb1], %[qw], %[vb1]\n\t" "v_mfma_f32_16x16x32_bf16 v[252:255], a[204:207], a[60:63], v[252:255]\n\t"
    "ds_read_b64_tr_b16 a[198:199], %[vr18] offset:59392\n\t" "s_waitcnt lgkmcnt(4)\n\t" "v_mfma_f32_16x16x32_bf16 v[96:99], a[176:179], a[48:51], v[96:99]\n\t"
    "ds_read_b64_tr_b16 a[200:201], %[vr19] offset:49152\n\t" "s_add_u32 s40, s40, 8192\n\t" "s_addc_u32 s41, s41, 0\n\t" "v_mfma_f32_16x16x32_bf16 v[96:99], a[180:183], a[56:59], v[96:99]\n\t"
    "ds_read_b64_tr_b16 a[202:203], %[vr19] offset:51200\n\t" "global_load_dwordx4 a[32:35], %[vx], s[40:41] offset:-4096 sc1\n\t"
    "v_mfma_f32_16x16x32_bf16 v[100:103], a[176:179], a[52:55], v[100:103]\n\t" "ds_read_b64_tr_b16 a[204:205], %[vr19] offset:57344\n\t"
    "v_mfma_f32_16x16x32_bf16 v[100:103], a[180:183], a[60:63], v[100:103]\n\t" "ds_read_b64_tr_b16 a[206:207], %[vr19] offset:59392\n\t"
    "v_mfma_f32_16x16x32_bf16 v[104:107], a[184:187], a[48:51], v[104:107]\n\t" "s_barrier\n\t" "ds_read_b64_tr_b16 a[176:177], %[vr8] offset:0\n\t"
    "v_mfma_f32_16x16x32_bf16 v[104:107], a[188:191], a[56:59], v[104:107]\n\t" "ds_read_b64_tr_b16 a[178:179], %[vr8] offset:2048\n\t" "global_load_dwordx4 a[36:39], %[vx], s[40:41] offset:-3072 sc1\n\t"
    "v_mfma_f32_16x16x32_bf16 v[108:111], a[184:187], a[52:55], v[108:111]\n\t" "ds_read_b64_tr_b16 a[180:181], %[vr0] offset:16384\n\t"
    "v_mfma_f32_16x16x32_bf16 v[108:111], a[188:191], a[60:63], v[108:111]\n\t" "ds_read_b64_tr_b16 a[182:183], %[vr0] offset:18432\n\t" "s_waitcnt lgkmcnt(4)\n\t"
    "v_mfma_f32_16x16x32_bf16 v[112:115], a[192:195], a[48:51], v[112:115]\n\t" "ds_read_b64_tr_b16 a[184:185], %[vr9] offset:0\n\t" "s_add_u32 m0, %[sl0], 0\n\t"
    "v_mfma_f32_16x16x32_bf16 v[112:115], a[196:199], a[56:59], v[112:115]\n\t" "ds_read_b64_tr_b16 a[186:187], %[vr9] offset:2048\n\t" "global_load_lds_dwordx4 %[vb0], %[sb]\n\t"
    "v_mfma_f32_16x16x32_bf16 v[116:119], a[192:195], a[52:55], v[116:119]\n\t" "ds_read_b64_tr_b16 a[188:189], %[vr1] offset:16384\n\t" "v_add_u32 %[vb0], %[pw], %[vb0]\n\t"
    "v_mfma_f32_16x16x32_bf16 v[116:119], a[196:199], a[60:63], v[116:119]\n\t" "ds_read_b64_tr_b16 a[190:191], %[vr1] offset:18432\n\t" "s_add_u32 m0, %[sl1], 0\n\t"
    "v_mfma_f32_16x16x32_bf16 v[120:123], a[200:203], a[48:51], v[120:123]\n\t" "ds_read_b64_tr_b16 a[192:193], %[vr10] offset:0\n\t"
    "v_mfma_f32_16x16x32_bf16 v[120:123], a[204:207], a[56:59], v[120:123]\n\t" "ds_read_b64_tr_b16 a[194:195], %[vr10] offset:2048\n\t" "global_load_lds_dwordx4 %[vb1], %[sb]\n\t"
    "v_mfma_f32_16x16x32_bf16 v[124:127], a[200:203], a[52:55], v[124:127]\n\t" "ds_read_b64_tr_b16 a[196:197], %[vr2] offset:16384\n\t" "v_add_u32 %[vb1], %[pw], %[vb1]\n\t"
    "v_mfma_f32_16x16x32_bf16 v[124:127], a[204:207], a[60:63], v[124:127]\n\t" "ds_read_b64_tr_b16 a[198:199], %[vr2] offset:18432\n\t" "s_waitcnt lgkmcnt(4) vmcnt(15)\n\t"
    "v_mfma_f32_16x16x32_bf16 v[224:227], a[176:179], a[64:67], v[224:227]\n\t" "ds_read_b64_tr_b16 a[200:201], %[vr11] offset:0\n\t"
    "v_mfma_f32_16x16x32_bf16 v[224:227], a[180:183], a[72:75], v[224:227]\n\t" "ds_read_b64_tr_b16 a[202:203], %[vr11] offset:2048\n\t"
    "global_load_dwordx4 a[40:43], %[vx], s[40:41] offset:-2048 sc1\n\t" "v_mfma_f32_16x16x32_bf16 v[240:243], a[176:179], a[68:71], v[240:243]\n\t"
    "ds_read_b64_tr_b16 a[204:205], %[vr3] offset:16384\n\t" "v_mfma_f32_16x16x32_bf16 v[240:243], a[180:183], a[76:79], v[240:243]\n\t" "ds_read_b64_tr_b16 a[206:207], %[vr3] offset:18432\n\t"
    "v_mfma_f32_16x16x32_bf16 v[228:231], a[184:187], a[64:67], v[228:231]\n\t" "ds_read_b64_tr_b16 a[176:177], %[vr20] offset:0\n\t"
    "v_mfma_f32_16x16x32_bf16 v[228:231], a[188:191], a[72:75], v[228:231]\n\t" "ds_read_b64_tr_b16 a[178:179], %[vr20] offset:2048\n\t"
    "global_load_dwordx4 a[44:47], %[vx], s[40:41] offset:-1024 sc1\n\t" "v_mfma_f32_16x16x32_bf16 v[244:247], a[184:187], a[68:71], v[244:247]\n\t"
    "ds_read_b64_tr_b16 a[180:181], %[vr12] offset:16384\n\t" "v_mfma_f32_16x16x32_bf16 v[244:247], a[188:191], a[76:79], v[244:247]\n\t" "ds_read_b64_tr_b16 a[182:183], %[vr12] offset:18432\n\t"
    "s_waitcnt lgkmcnt(4)\n\t" "v_mfma_f32_16x16x32_bf16 v[232:235], a[192:195], a[64:67], v[232:235]\n\t" "ds_read_b64_tr_b16 a[184:185], %[vr21] offset:0\n\t" "s_add_u32 m0, %[sl0], 8192\n\t"
    "v_mfma_f32_16x16x32_bf16 v[232:235], a[196:199], a[72:75], v[232:235]\n\t" "ds_read_b64_tr_b16 a[186:187], %[vr21] offset:2048\n\t" "global_load_lds_dwordx4 %[vb0], %[sb]\n\t"
    "v_mfma_f32_16x16x32_bf16 v[248:251], a[192:195], a[68:71], v[248:251]\n\t" "ds_read_b64_tr_b16 a[188:189], %[vr13] offset:16384\n\t" "v_add_u32 %[vb0], 0x10000, %[vb0]\n\t"
    "v_mfma_f32_16x16x32_bf16 v[248:251], a[196:199], a[76:79], v[248:251]\n\t" "ds_read_b64_tr_b16 a[190:191], %[vr13] offset:18432\n\t" "s_add_u32 m0, %[sl1], 8192\n\t"
    "v_mfma_f32_16x16x32_bf16 v[236:239], a[200:203], a[64:67], v[236:239]\n\t" "ds_read_b64_tr_b16 a[192:193], %[vr22] offset:0\n\t"
    "v_mfma_f32_16x16x32_bf16 v[236:239], a[204:207], a[72:75], v[236:239]\n\t" "ds_read_b64_tr_b16 a[194:195], %[vr22] offset:2048\n\t" "global_load_lds_dwordx4 %[vb1], %[sb]\n\t"
    "v_mfma_f32_16x16x32_bf16 v[252:255], a[200:203], a[68:71], v[252:255]\n\t" "ds_read_b64_tr_b16 a[196:197], %[vr14] offset:16384\n\t" "v_add_u32 %[vb1], 0x10000, %[vb1]\n\t"
    "v_mfma_f32_16x16x32_bf16 v[252:255], a[204:207], a[76:79], v[252:255]\n\t" "ds_read_b64_tr_b16 a[198:199], %[vr14] offset:18432\n\t" "s_waitcnt lgkmcnt(4)\n\t"
    "v_mfma_f32_16x16x32_bf16 v[96:99], a[176:179], a[64:67], v[96:99]\n\t" "ds_read_b64_tr_b16 a[200:201], %[vr23] offset:0\n\t" "v_mfma_f32_16x16x32_bf16 v[96:99], a[180:183], a[72:75], v[96:99]\n\t"
    "ds_read_b64_tr_b16 a[202:203], %[vr23] offset:2048\n\t" "global_load_dwordx4 a[48:51], %[vx], s[40:41] offset:0 sc1\n\t" "v_mfma_f32_16x16x32_bf16 v[100:103], a[176:179], a[68:71], v[100:103]\n\t"
    "ds_read_b64_tr_b16 a[204:205], %[vr15] offset:16384\n\t" "v_mfma_f32_16x16x32_bf16 v[100:103], a[180:183], a[76:79], v[100:103]\n\t" "ds_read_b64_tr_b16 a[206:207], %[vr15] offset:18432\n\t"
    "v_mfma_f32_16x16x32_bf16 v[104:107], a[184:187], a[64:67], v[104:107]\n\t" "s_barrier\n\t" "ds_read_b64_tr_b16 a[176:177], %[vr0] offset:24576\n\t"
    "v_mfma_f32_16x16x32_bf16 v[104:107], a[188:191], a[72:75], v[104:107]\n\t" "ds_read_b64_tr_b16 a[178:179], %[vr0] offset:26624\n\t" "global_load_dwordx4 a[52:55], %[vx], s[40:41] offset:1024 sc1\n\t"
    "v_mfma_f32_16x16x32_bf16 v[108:111], a[184:187], a[68:71], v[108:111]\n\t" "ds_read_b64_tr_b16 a[180:181], %[vr0] offset:32768\n\t"
    "v_mfma_f32_16x16x32_bf16 v[108:111], a[188:191], a[76:79], v[108:111]\n\t" "ds_read_b64_tr_b16 a[182:183], %[vr0] offset:34816\n\t" "s_waitcnt lgkmcnt(4)\n\t"
    "v_mfma_f32_16x16x32_bf16 v[112:115], a[192:195], a[64:67], v[112:115]\n\t" "ds_read_b64_tr_b16 a[184:185], %[vr1] offset:24576\n\t" "s_add_u32 m0, %[sl0], 49152\n\t"
    "v_mfma_f32_16x16x32_bf16 v[112:115], a[196:199], a[72:75], v[112:115]\n\t" "ds_read_b64_tr_b16 a[186:187], %[vr1] offset:26624\n\t" "global_load_lds_dwordx4 %[vb0], %[sb]\n\t"
    "v_mfma_f32_16x16x32_bf16 v[116:119], a[192:195], a[68:71], v[116:119]\n\t" "ds_read_b64_tr_b16 a[188:189], %[vr1] offset:32768\n\t" "v_add_u32 %[vb0], %[qw], %[vb0]\n\t"
    "v_mfma_f32_16x16x32_bf16 v[116:119], a[196:199], a[76:79], v[116:119]\n\t" "ds_read_b64_tr_b16 a[190:191], %[vr1] offset:34816\n\t" "s_add_u32 m0, %[sl1], 49152\n\t"
    "v_mfma_f32_16x16x32_bf16 v[120:123], a[200:203], a[64:67], v[120:123]\n\t" "ds_read_b64_tr_b16 a[192:193], %[vr2] offset:24576\n\t"
    "v_mfma_f32_16x16x32_bf16 v[120:123], a[204:207], a[72:75], v[120:123]\n\t" "ds_read_b64_tr_b16 a[194:195], %[vr2] offset:26624\n\t" "global_load_lds_dwordx4 %[vb1], %[sb]\n\t"
    "v_mfma_f32_16x16x32_bf16 v[124:127], a[200:203], a[68:71], v[124:127]\n\t" "ds_read_b64_tr_b16 a[196:197], %[vr2] offset:32768\n\t" "v_add_u32_e64 %[vb1], %[qw], %[vb1]\n\t"
    "v_mfma_f32_16x16x32_bf16 v[124:127], a[204:207], a[76:79], v[124:127]\n\t" "ds_read_b64_tr_b16 a[198:199], %[vr2] offset:34816\n\t" "s_waitcnt lgkmcnt(4) vmcnt(19)\n\t"
    "v_mfma_f32_16x16x32_bf16 v[224:227], a[176:179], a[0:3], v[224:227]\n\t" "ds_read_b64_tr_b16 a[200:201], %[vr3] offset:24576\n\t"
    "v_mfma_f32_16x16x32_bf16 v[224:227], a[180:183], a[8:11], v[224:227]\n\t" "ds_read_b64_tr_b16 a[202:203], %[vr3] offset:26624\n\t" "global_load_dwordx4 a[56:59], %[vx], s[40:41] offset:2048 sc1\n\t"
    "v_mfma_f32_16x16x32_bf16 v[240:243], a[176:179], a[4:7], v[240:243]\n\t" "ds_read_b64_tr_b16 a[204:205], %[vr3] offset:32768\n\t"
    "v_mfma_f32_16x16x32_bf16 v[240:243], a[180:183], a[12:15], v[240:243]\n\t" "ds_read_b64_tr_b16 a[206:207], %[vr3] offset:34816\n\t"
    "v_mfma_f32_16x16x32_bf16 v[228:231], a[184:187], a[0:3], v[228:231]\n\t" "ds_read_b64_tr_b16 a[176:177], %[vr12] offset:24576\n\t"
    "v_mfma_f32_16x16x32_bf16 v[228:231], a[188:191], a[8:11], v[228:231]\n\t" "ds_read_b64_tr_b16 a[178:179], %[vr12] offset:26624\n\t" "global_load_dwordx4 a[60:63], %[vx], s[40:41] offset:3072 sc1\n\t"
    "v_mfma_f32_16x16x32_bf16 v[244:247], a[184:187], a[4:7], v[244:247]\n\t" "ds_read_b64_tr_b16 a[180:181], %[vr12] offset:32768\n\t"
    "v_mfma_f32_16x16x32_bf16 v[244:247], a[188:191], a[12:15], v[244:247]\n\t" "ds_read_b64_tr_b16 a[182:183], %[vr12] offset:34816\n\t" "s_waitcnt lgkmcnt(4)\n\t"
    "v_mfma_f32_16x16x32_bf16 v[232:235], a[192:195], a[0:3], v[232:235]\n\t" "ds_read_b64_tr_b16 a[184:185], %[vr13] offset:24576\n\t" "s_add_u32 m0, %[sl0], 57344\n\t"
    "v_mfma_f32_16x16x32_bf16 v[232:235], a[196:199], a[8:11], v[232:235]\n\t" "ds_read_b64_tr_b16 a[186:187], %[vr13] offset:26624\n\t" "global_load_lds_dwordx4 %[vb0], %[sb]\n\t"
    "v_mfma_f32_16x16x32_bf16 v[248:251], a[192:195], a[4:7], v[248:251]\n\t" "ds_read_b64_tr_b16 a[188:189], %[vr13] offset:32768\n\t" "v_add_u32_e64 %[vb0], %[pw], %[vb0]\n\t"
    "v_mfma_f32_16x16x32_bf16 v[248:251], a[196:199], a[12:15], v[248:251]\n\t" "ds_read_b64_tr_b16 a[190:191], %[vr13] offset:34816\n\t" "s_add_u32 m0, %[sl1], 57344\n\t"
    "v_mfma_f32_16x16x32_bf16 v[236:239], a[200:203], a[0:3], v[236:239]\n\t" "ds_read_b64_tr_b16 a[192:193], %[vr14] offset:24576\n\t"
    "v_mfma_f32_16x16x32_bf16 v[236:239], a[204:207], a[8:11], v[236:239]\n\t" "ds_read_b64_tr_b16 a[194:195], %[vr14] offset:26624\n\t" "global_load_lds_dwordx4 %[vb1], %[sb]\n\t"
    "v_mfma_f32_16x16x32_bf16 v[252:255], a[200:203], a[4:7], v[252:255]\n\t" "ds_read_b64_tr_b16 a[196:197], %[vr14] offset:32768\n\t" "v_add_u32_e64 %[vb1], %[pw], %[vb1]\n\t"
    "v_mfma_f32_16x16x32_bf16 v[252:255], a[204:207], a[12:15], v[252:255]\n\t" "ds_read_b64_tr_b16 a[198:199], %[vr14] offset:34816\n\t" "s_waitcnt lgkmcnt(4)\n\t"
    "v_mfma_f32_16x16x32_bf16 v[96:99], a[176:179], a[0:3], v[96:99]\n\t" "ds_read_b64_tr_b16 a[200:201], %[vr15] offset:24576\n\t" "s_add_u32 s40, s40, 8192\n\t" "s_addc_u32 s41, s41, 0\n\t"
    "v_mfma_f32_16x16x32_bf16 v[96:99], a[180:183], a[8:11], v[96:99]\n\t" "ds_read_b64_tr_b16 a[202:203], %[vr15] offset:26624\n\t" "global_load_dwordx2 a[64:65], %[vo], s[40:41] offset:-4096 sc1\n\t"
    "v_mfma_f32_16x16x32_bf16 v[100:103], a[176:179], a[4:7], v[100:103]\n\t" "ds_read_b64_tr_b16 a[204:205], %[vr15] offset:32768\n\t"
    "v_mfma_f32_16x16x32_bf16 v[100:103], a[180:183], a[12:15], v[100:103]\n\t" "ds_read_b64_tr_b16 a[206:207], %[vr15] offset:34816\n\t"
    "global_load_dwordx2 a[66:67], %[vo], s[40:41] offset:-3072 sc1\n\t" "v_mfma_f32_16x16x32_bf16 v[104:107], a[184:187], a[0:3], v[104:107]\n\t" "s_barrier\n\t"
    "ds_read_b64_tr_b16 a[176:177], %[vr0] offset:40960\n\t" "v_mfma_f32_16x16x32_bf16 v[104:107], a[188:191], a[8:11], v[104:107]\n\t" "ds_read_b64_tr_b16 a[178:179], %[vr0] offset:43008\n\t"
    "global_load_dwordx2 a[68:69], %[vo], s[40:41] offset:-3584 sc1\n\t" "v_mfma_f32_16x16x32_bf16 v[108:111], a[184:187], a[4:7], v[108:111]\n\t" "ds_read_b64_tr_b16 a[180:181], %[vr8] offset:8192\n\t"
    "v_mfma_f32_16x16x32_bf16 v[108:111], a[188:191], a[12:15], v[108:111]\n\t" "ds_read_b64_tr_b16 a[182:183], %[vr8] offset:10240\n\t" "s_waitcnt lgkmcnt(4)\n\t"
    "v_mfma_f32_16x16x32_bf16 v[112:115], a[192:195], a[0:3], v[112:115]\n\t" "ds_read_b64_tr_b16 a[184:185], %[vr1] offset:40960\n\t" "global_load_dwordx2 a[70:71], %[vo], s[40:41] offset:-2560 sc1\n\t"
    "v_mfma_f32_16x16x32_bf16 v[112:115], a[196:199], a[8:11], v[112:115]\n\t" "ds_read_b64_tr_b16 a[186:187], %[vr1] offset:43008\n\t" "s_add_u32 m0, %[sl0], 65536\n\t"
    "v_mfma_f32_16x16x32_bf16 v[116:119], a[192:195], a[4:7], v[116:119]\n\t" "ds_read_b64_tr_b16 a[188:189], %[vr9] offset:8192\n\t" "global_load_lds_dwordx4 %[vb0], %[sb]\n\t"
    "v_mfma_f32_16x16x32_bf16 v[116:119], a[196:199], a[12:15], v[116:119]\n\t" "ds_read_b64_tr_b16 a[190:191], %[vr9] offset:10240\n\t" "v_add_u32_e64 %[vb0], %[qw], %[vb0]\n\t"
    "v_mfma_f32_16x16x32_bf16 v[120:123], a[200:203], a[0:3], v[120:123]\n\t" "ds_read_b64_tr_b16 a[192:193], %[vr2] offset:40960\n\t" "s_add_u32 m0, %[sl1], 65536\n\t"
    "v_mfma_f32_16x16x32_bf16 v[120:123], a[204:207], a[8:11], v[120:123]\n\t" "ds_read_b64_tr_b16 a[194:195], %[vr2] offset:43008\n\t" "global_load_lds_dwordx4 %[vb1], %[sb]\n\t"
    "v_mfma_f32_16x16x32_bf16 v[124:127], a[200:203], a[4:7], v[124:127]\n\t" "ds_read_b64_tr_b16 a[196:197], %[vr10] offset:8192\n\t" "v_add_u32_e64 %[vb1], %[qw], %[vb1]\n\t"
    "v_mfma_f32_16x16x32_bf16 v[124:127], a[204:207], a[12:15], v[124:127]\n\t" "ds_read_b64_tr_b16 a[198:199], %[vr10] offset:10240\n\t" "s_waitcnt lgkmcnt(4) vmcnt(23)\n\t"
    "v_mfma_f32_16x16x32_bf16 v[224:227], a[176:179], a[16:19], v[224:227]\n\t" "ds_read_b64_tr_b16 a[200:201], %[vr3] offset:40960\n\t"
    "v_mfma_f32_16x16x32_bf16 v[224:227], a[180:183], a[24:27], v[224:227]\n\t" "ds_read_b64_tr_b16 a[202:203], %[vr3] offset:43008\n\t"
    "global_load_dwordx4 a[72:75], %[vx], s[40:41] offset:-2048 sc1\n\t" "v_mfma_f32_16x16x32_bf16 v[240:243], a[176:179], a[20:23], v[240:243]\n\t"
    "ds_read_b64_tr_b16 a[204:205], %[vr11] offset:8192\n\t" "v_mfma_f32_16x16x32_bf16 v[240:243], a[180:183], a[28:31], v[240:243]\n\t" "ds_read_b64_tr_b16 a[206:207], %[vr11] offset:10240\n\t"
    "v_mfma_f32_16x16x32_bf16 v[228:231], a[184:187], a[16:19], v[228:231]\n\t" "ds_read_b64_tr_b16 a[176:177], %[vr12] offset:40960\n\t"
    "v_mfma_f32_16x16x32_bf16 v[228:231], a[188:191], a[24:27], v[228:231]\n\t" "ds_read_b64_tr_b16 a[178:179], %[vr12] offset:43008\n\t"
    "global_load_dwordx4 a[76:79], %[vx], s[40:41] offset:-1024 sc1\n\t" "v_mfma_f32_16x16x32_bf16 v[244:247], a[184:187], a[20:23], v[244:247]\n\t"
    "ds_read_b64_tr_b16 a[180:181], %[vr20] offset:8192\n\t" "v_mfma_f32_16x16x32_bf16 v[244:247], a[188:191], a[28:31], v[244:247]\n\t" "ds_read_b64_tr_b16 a[182:183], %[vr20] offset:10240\n\t"
    "s_waitcnt lgkmcnt(4)\n\t" "v_mfma_f32_16x16x32_bf16 v[232:235], a[192:195], a[16:19], v[232:235]\n\t" "ds_read_b64_tr_b16 a[184:185], %[vr13] offset:40960\n\t" "s_add_u32 m0, %[sl0], 73728\n\t"
    "v_mfma_f32_16x16x32_bf16 v[232:235], a[196:199], a[24:27], v[232:235]\n\t" "ds_read_b64_tr_b16 a[186:187], %[vr13] offset:43008\n\t" "global_load_lds_dwordx4 %[vb0], %[sb]\n\t"
    "v_mfma_f32_16x16x32_bf16 v[248:251], a[192:195], a[20:23], v[248:251]\n\t" "ds_read_b64_tr_b16 a[188:189], %[vr21] offset:8192\n\t" "v_add_u32_e64 %[vb0], %[pw], %[vb0]\n\t"
    "v_mfma_f32_16x16x32_bf16 v[248:251], a[196:199], a[28:31], v[248:251]\n\t" "ds_read_b64_tr_b16 a[190:191], %[vr21] offset:10240\n\t" "s_add_u32 m0, %[sl1], 73728\n\t"
    "v_mfma_f32_16x16x32_bf16 v[236:239], a[200:203], a[16:19], v[236:239]\n\t" "ds_read_b64_tr_b16 a[192:193], %[vr14] offset:40960\n\t"
    "v_mfma_f32_16x16x32_bf16 v[236:239], a[204:207], a[24:27], v[236:239]\n\t" "ds_read_b64_tr_b16 a[194:195], %[vr14] offset:43008\n\t" "global_load_lds_dwordx4 %[vb1], %[sb]\n\t"
    "v_mfma_f32_16x16x32_bf16 v[252:255], a[200:203], a[20:23], v[252:255]\n\t" "ds_read_b64_tr_b16 a[196:197], %[vr22] offset:8192\n\t" "v_add_u32_e64 %[vb1], %[pw], %[vb1]\n\t"
    "v_mfma_f32_16x16x32_bf16 v[252:255], a[204:207], a[28:31], v[252:255]\n\t" "ds_read_b64_tr_b16 a[198:199], %[vr22] offset:10240\n\t" "s_waitcnt lgkmcnt(4)\n\t"
    "v_mfma_f32_16x16x32_bf16 v[96:99], a[176:179], a[16:19], v[96:99]\n\t" "ds_read_b64_tr_b16 a[200:201], %[vr15] offset:40960\n\t"
    "v_mfma_f32_16x16x32_bf16 v[96:99], a[180:183], a[24:27], v[96:99]\n\t" "ds_read_b64_tr_b16 a[202:203], %[vr15] offset:43008\n\t" "global_load_dwordx4 a[0:3], %[vx], s[40:41] offset:0 sc1\n\t"
    "v_mfma_f32_16x16x32_bf16 v[100:103], a[176:179], a[20:23], v[100:103]\n\t" "ds_read_b64_tr_b16 a[204:205], %[vr23] offset:8192\n\t"
    "v_mfma_f32_16x16x32_bf16 v[100:103], a[180:183], a[28:31], v[100:103]\n\t" "ds_read_b64_tr_b16 a[206:207], %[vr23] offset:10240\n\t"
    "v_mfma_f32_16x16x32_bf16 v[104:107], a[184:187], a[16:19], v[104:107]\n\t" "s_waitcnt vmcnt(27)\n\t" "s_barrier\n\t" "ds_read_b64_tr_b16 a[176:177], %[vr8] offset:16384\n\t"
    "v_mfma_f32_16x16x32_bf16 v[104:107], a[188:191], a[24:27], v[104:107]\n\t" "ds_read_b64_tr_b16 a[178:179], %[vr8] offset:18432\n\t" "global_load_dwordx4 a[4:7], %[vx], s[40:41] offset:1024 sc1\n\t"
    "v_mfma_f32_16x16x32_bf16 v[108:111], a[184:187], a[20:23], v[108:111]\n\t" "ds_read_b64_tr_b16 a[180:181], %[vr8] offset:24576\n\t"
    "v_mfma_f32_16x16x32_bf16 v[108:111], a[188:191], a[28:31], v[108:111]\n\t" "ds_read_b64_tr_b16 a[182:183], %[vr8] offset:26624\n\t" "s_waitcnt lgkmcnt(4)\n\t"
    "v_mfma_f32_16x16x32_bf16 v[112:115], a[192:195], a[16:19], v[112:115]\n\t" "ds_read_b64_tr_b16 a[184:185], %[vr9] offset:16384\n\t" "s_add_u32 m0, %[sl0], 81920\n\t"
    "v_mfma_f32_16x16x32_bf16 v[112:115], a[196:199], a[24:27], v[112:115]\n\t" "ds_read_b64_tr_b16 a[186:187], %[vr9] offset:18432\n\t" "global_load_lds_dwordx4 %[vb0], %[sb]\n\t"
    "v_mfma_f32_16x16x32_bf16 v[116:119], a[192:195], a[20:23], v[116:119]\n\t" "ds_read_b64_tr_b16 a[188:189], %[vr9] offset:24576\n\t" "v_add_u32 %[vb0], 0x10000, %[vb0]\n\t"
    "v_mfma_f32_16x16x32_bf16 v[116:119], a[196:199], a[28:31], v[116:119]\n\t" "ds_read_b64_tr_b16 a[190:191], %[vr9] offset:26624\n\t" "s_add_u32 m0, %[sl1], 81920\n\t"
    "v_mfma_f32_16x16x32_bf16 v[120:123], a[200:203], a[16:19], v[120:123]\n\t" "ds_read_b64_tr_b16 a[192:193], %[vr10] offset:16384\n\t"
    "v_mfma_f32_16x16x32_bf16 v[120:123], a[204:207], a[24:27], v[120:123]\n\t" "ds_read_b64_tr_b16 a[194:195], %[vr10] offset:18432\n\t" "global_load_lds_dwordx4 %[vb1], %[sb]\n\t"
    "v_mfma_f32_16x16x32_bf16 v[124:127], a[200:203], a[20:23], v[124:127]\n\t" "ds_read_b64_tr_b16 a[196:197], %[vr10] offset:24576\n\t" "v_add_u32 %[vb1], 0x10000, %[vb1]\n\t"
    "v_mfma_f32_16x16x32_bf16 v[124:127], a[204:207], a[28:31], v[124:127]\n\t" "ds_read_b64_tr_b16 a[198:199], %[vr10] offset:26624\n\t" "s_waitcnt lgkmcnt(4) vmcnt(24)\n\t"
    "v_mfma_f32_16x16x32_bf16 v[224:227], a[176:179], a[32:35], v[224:227]\n\t" "ds_read_b64_tr_b16 a[200:201], %[vr11] offset:16384\n\t"
    "v_mfma_f32_16x16x32_bf16 v[224:227], a[180:183], a[40:43], v[224:227]\n\t" "ds_read_b64_tr_b16 a[202:203], %[vr11] offset:18432\n\t" "global_load_dwordx4 a[8:11], %[vx], s[40:41] offset:2048 sc1\n\t"
    "v_mfma_f32_16x16x32_bf16 v[240:243], a[176:179], a[36:39], v[240:243]\n\t" "ds_read_b64_tr_b16 a[204:205], %[vr11] offset:24576\n\t"
    "v_mfma_f32_16x16x32_bf16 v[240:243], a[180:183], a[44:47], v[240:243]\n\t" "ds_read_b64_tr_b16 a[206:207], %[vr11] offset:26624\n\t"
    "v_mfma_f32_16x16x32_bf16 v[228:231], a[184:187], a[32:35], v[228:231]\n\t" "ds_read_b64_tr_b16 a[176:177], %[vr20] offset:16384\n\t"
    "v_mfma_f32_16x16x32_bf16 v[228:231], a[188:191], a[40:43], v[228:231]\n\t" "ds_read_b64_tr_b16 a[178:179], %[vr20] offset:18432\n\t"
    "global_load_dwordx4 a[12:15], %[vx], s[40:41] offset:3072 sc1\n\t" "v_mfma_f32_16x16x32_bf16 v[244:247], a[184:187], a[36:39], v[244:247]\n\t"
    "ds_read_b64_tr_b16 a[180:181], %[vr20] offset:24576\n\t" "v_mfma_f32_16x16x32_bf16 v[244:247], a[188:191], a[44:47], v[244:247]\n\t" "ds_read_b64_tr_b16 a[182:183], %[vr20] offset:26624\n\t"
    "s_waitcnt lgkmcnt(4)\n\t" "v_mfma_f32_16x16x32_bf16 v[232:235], a[192:195], a[32:35], v[232:235]\n\t" "ds_read_b64_tr_b16 a[184:185], %[vr21] offset:16384\n\t" "s_add_u32 m0, %[sl0], 90112\n\t"
    "v_mfma_f32_16x16x32_bf16 v[232:235], a[196:199], a[40:43], v[232:235]\n\t" "ds_read_b64_tr_b16 a[186:187], %[vr21] offset:18432\n\t" "global_load_lds_dwordx4 %[vb0], %[sb]\n\t"
    "v_mfma_f32_16x16x32_bf16 v[248:251], a[192:195], a[36:39], v[248:251]\n\t" "ds_read_b64_tr_b16 a[188:189], %[vr21] offset:24576\n\t" "v_add_u32_e64 %[vb0], %[qw], %[vb0]\n\t"
    "v_mfma_f32_16x16x32_bf16 v[248:251], a[196:199], a[44:47], v[248:251]\n\t" "ds_read_b64_tr_b16 a[190:191], %[vr21] offset:26624\n\t" "s_add_u32 m0, %[sl1], 90112\n\t"
    "v_mfma_f32_16x16x32_bf16 v[236:239], a[200:203], a[32:35], v[236:239]\n\t" "ds_read_b64_tr_b16 a[192:193], %[vr22] offset:16384\n\t"
    "v_mfma_f32_16x16x32_bf16 v[236:239], a[204:207], a[40:43], v[236:239]\n\t" "ds_read_b64_tr_b16 a[194:195], %[vr22] offset:18432\n\t" "global_load_lds_dwordx4 %[vb1], %[sb]\n\t"
    "v_mfma_f32_16x16x32_bf16 v[252:255], a[200:203], a[36:39], v[252:255]\n\t" "ds_read_b64_tr_b16 a[196:197], %[vr22] offset:24576\n\t" "v_add_u32_e64 %[vb1], %[qw], %[vb1]\n\t"
    "v_mfma_f32_16x16x32_bf16 v[252:255], a[204:207], a[44:47], v[252:255]\n\t" "ds_read_b64_tr_b16 a[198:199], %[vr22] offset:26624\n\t" "s_waitcnt lgkmcnt(4)\n\t"
    "v_mfma_f32_16x16x32_bf16 v[96:99], a[176:179], a[32:35], v[96:99]\n\t" "ds_read_b64_tr_b16 a[200:201], %[vr23] offset:16384\n\t" "s_add_u32 s40, s40, 8192\n\t" "s_addc_u32 s41, s41, 0\n\t"
    "v_mfma_f32_16x16x32_bf16 v[96:99], a[180:183], a[40:43], v[96:99]\n\t" "ds_read_b64_tr_b16 a[202:203], %[vr23] offset:18432\n\t" "global_load_dwordx4 a[16:19], %[vx], s[40:41] offset:-4096 sc1\n\t"
    "v_mfma_f32_16x16x32_bf16 v[100:103], a[176:179], a[36:39], v[100:103]\n\t" "ds_read_b64_tr_b16 a[204:205], %[vr23] offset:24576\n\t"
    "v_mfma_f32_16x16x32_bf16 v[100:103], a[180:183], a[44:47], v[100:103]\n\t" "ds_read_b64_tr_b16 a[206:207], %[vr23] offset:26624\n\t"
    "v_mfma_f32_16x16x32_bf16 v[104:107], a[184:187], a[32:35], v[104:107]\n\t" "s_waitcnt vmcnt(27)\n\t" "s_barrier\n\t" "ds_read_b64_tr_b16 a[176:177], %[vr0] offset:0\n\t"
    "v_mfma_f32_16x16x32_bf16 v[104:107], a[188:191], a[40:43], v[104:107]\n\t" "ds_read_b64_tr_b16 a[178:179], %[vr0] offset:2048\n\t" "global_load_dwordx4 a[20:23], %[vx], s[40:41] offset:-3072 sc1\n\t"
    "v_mfma_f32_16x16x32_bf16 v[108:111], a[184:187], a[36:39], v[108:111]\n\t" "ds_read_b64_tr_b16 a[180:181], %[vr0] offset:8192\n\t"
    "v_mfma_f32_16x16x32_bf16 v[108:111], a[188:191], a[44:47], v[108:111]\n\t" "ds_read_b64_tr_b16 a[182:183], %[vr0] offset:10240\n\t" "s_waitcnt lgkmcnt(4)\n\t"
    "v_mfma_f32_16x16x32_bf16 v[112:115], a[192:195], a[32:35], v[112:115]\n\t" "ds_read_b64_tr_b16 a[184:185], %[vr1] offset:0\n\t" "s_add_u32 m0, %[sl0], 98304\n\t"
    "v_mfma_f32_16x16x32_bf16 v[112:115], a[196:199], a[40:43], v[112:115]\n\t" "ds_read_b64_tr_b16 a[186:187], %[vr1] offset:2048\n\t" "global_load_lds_dwordx4 %[vb0], %[sb]\n\t"
    "v_mfma_f32_16x16x32_bf16 v[116:119], a[192:195], a[36:39], v[116:119]\n\t" "ds_read_b64_tr_b16 a[188:189], %[vr1] offset:8192\n\t" "v_add_u32 %[vb0], %[pw], %[vb0]\n\t"
    "v_mfma_f32_16x16x32_bf16 v[116:119], a[196:199], a[44:47], v[116:119]\n\t" "ds_read_b64_tr_b16 a[190:191], %[vr1] offset:10240\n\t" "s_add_u32 m0, %[sl1], 98304\n\t"
    "v_mfma_f32_16x16x32_bf16 v[120:123], a[200:203], a[32:35], v[120:123]\n\t" "ds_read_b64_tr_b16 a[192:193], %[vr2] offset:0\n\t"
    "v_mfma_f32_16x16x32_bf16 v[120:123], a[204:207], a[40:43], v[120:123]\n\t" "ds_read_b64_tr_b16 a[194:195], %[vr2] offset:2048\n\t" "global_load_lds_dwordx4 %[vb1], %[sb]\n\t"
    "v_mfma_f32_16x16x32_bf16 v[124:127], a[200:203], a[36:39], v[124:127]\n\t" "ds_read_b64_tr_b16 a[196:197], %[vr2] offset:8192\n\t" "v_add_u32_e64 %[vb1], %[pw], %[vb1]\n\t"
    "v_mfma_f32_16x16x32_bf16 v[124:127], a[204:207], a[44:47], v[124:127]\n\t" "ds_read_b64_tr_b16 a[198:199], %[vr2] offset:10240\n\t" "s_waitcnt lgkmcnt(4) vmcnt(24)\n\t"
    "v_mfma_f32_16x16x32_bf16 v[224:227], a[176:179], a[48:51], v[224:227]\n\t" "ds_read_b64_tr_b16 a[200:201], %[vr3] offset:0\n\t"
    "v_mfma_f32_16x16x32_bf16 v[224:227], a[180:183], a[56:59], v[224:227]\n\t" "ds_read_b64_tr_b16 a[202:203], %[vr3] offset:2048\n\t" "global_load_dwordx2 a[24:25], %[vo], s[40:41] offset:-2048 sc1\n\t"
    "v_mfma_f32_16x16x32_bf16 v[240:243], a[176:179], a[52:55], v[240:243]\n\t" "ds_read_b64_tr_b16 a[204:205], %[vr3] offset:8192\n\t"
    "v_mfma_f32_16x16x32_bf16 v[240:243], a[180:183], a[60:63], v[240:243]\n\t" "ds_read_b64_tr_b16 a[206:207], %[vr3] offset:10240\n\t" "global_load_dwordx4 v[176:179], %[vg], %[sg] offset:0 sc1\n\t"
    "v_mfma_f32_16x16x32_bf16 v[228:231], a[184:187], a[48:51], v[228:231]\n\t" "ds_read_b64_tr_b16 a[176:177], %[vr12] offset:0\n\t"
    "v_mfma_f32_16x16x32_bf16 v[228:231], a[188:191], a[56:59], v[228:231]\n\t" "ds_read_b64_tr_b16 a[178:179], %[vr12] offset:2048\n\t"
    "global_load_dwordx2 a[26:27], %[vo], s[40:41] offset:-1024 sc1\n\t" "v_mfma_f32_16x16x32_bf16 v[244:247], a[184:187], a[52:55], v[244:247]\n\t"
    "ds_read_b64_tr_b16 a[180:181], %[vr12] offset:8192\n\t" "v_mfma_f32_16x16x32_bf16 v[244:247], a[188:191], a[60:63], v[244:247]\n\t" "ds_read_b64_tr_b16 a[182:183], %[vr12] offset:10240\n\t"
    "s_waitcnt lgkmcnt(4)\n\t" "v_mfma_f32_16x16x32_bf16 v[232:235], a[192:195], a[48:51], v[232:235]\n\t" "ds_read_b64_tr_b16 a[184:185], %[vr13] offset:0\n\t"
    "global_load_dwordx2 a[28:29], %[vo], s[40:41] offset:-1536 sc1\n\t" "v_mfma_f32_16x16x32_bf16 v[232:235], a[196:199], a[56:59], v[232:235]\n\t"
    "ds_read_b64_tr_b16 a[186:187], %[vr13] offset:2048\n\t" "global_load_dwordx2 a[30:31], %[vo], s[40:41] offset:-512 sc1\n\t" "v_mfma_f32_16x16x32_bf16 v[248:251], a[192:195], a[52:55], v[248:251]\n\t"
    "ds_read_b64_tr_b16 a[188:189], %[vr13] offset:8192\n\t" "s_add_u32 m0, %[sl0], 106496\n\t" "v_mfma_f32_16x16x32_bf16 v[248:251], a[196:199], a[60:63], v[248:251]\n\t"
    "ds_read_b64_tr_b16 a[190:191], %[vr13] offset:10240\n\t" "global_load_lds_dwordx4 %[vb0], %[sb]\n\t" "v_mfma_f32_16x16x32_bf16 v[236:239], a[200:203], a[48:51], v[236:239]\n\t"
    "ds_read_b64_tr_b16 a[192:193], %[vr14] offset:0\n\t" "v_add_u32_e64 %[vb0], %[qw], %[vb0]\n\t" "v_mfma_f32_16x16x32_bf16 v[236:239], a[204:207], a[56:59], v[236:239]\n\t"
    "ds_read_b64_tr_b16 a[194:195], %[vr14] offset:2048\n\t" "s_add_u32 m0, %[sl1], 106496\n\t" "v_mfma_f32_16x16x32_bf16 v[252:255], a[200:203], a[52:55], v[252:255]\n\t"
    "ds_read_b64_tr_b16 a[196:197], %[vr14] offset:8192\n\t" "v_mfma_f32_16x16x32_bf16 v[252:255], a[204:207], a[60:63], v[252:255]\n\t" "ds_read_b64_tr_b16 a[198:199], %[vr14] offset:10240\n\t"
    "s_waitcnt lgkmcnt(4)\n\t" "v_mfma_f32_16x16x32_bf16 v[96:99], a[176:179], a[48:51], v[96:99]\n\t" "ds_read_b64_tr_b16 a[200:201], %[vr15] offset:0\n\t" "global_load_lds_dwordx4 %[vb1], %[sb]\n\t"
    "v_mfma_f32_16x16x32_bf16 v[96:99], a[180:183], a[56:59], v[96:99]\n\t" "ds_read_b64_tr_b16 a[202:203], %[vr15] offset:2048\n\t" "v_add_u32 %[vb1], %[qw], %[vb1]\n\t"
    "v_mfma_f32_16x16x32_bf16 v[100:103], a[176:179], a[52:55], v[100:103]\n\t" "ds_read_b64_tr_b16 a[204:205], %[vr15] offset:8192\n\t"
    "v_mfma_f32_16x16x32_bf16 v[100:103], a[180:183], a[60:63], v[100:103]\n\t" "ds_read_b64_tr_b16 a[206:207], %[vr15] offset:10240\n\t" "global_load_dwordx4 v[180:183], %[vg], %[sg] offset:64 sc1\n\t"
    "v_mfma_f32_16x16x32_bf16 v[104:107], a[184:187], a[48:51], v[104:107]\n\t" "s_waitcnt vmcnt(30)\n\t" "s_barrier\n\t" "ds_read_b64_tr_b16 a[176:177], %[vr0] offset:49152\n\t"
    "v_mfma_f32_16x16x32_bf16 v[104:107], a[188:191], a[56:59], v[104:107]\n\t" "ds_read_b64_tr_b16 a[178:179], %[vr0] offset:51200\n\t" "global_load_dwordx4 a[32:35], %[vx], s[40:41] offset:0 sc1\n\t"
    "v_mfma_f32_16x16x32_bf16 v[108:111], a[184:187], a[52:55], v[108:111]\n\t" "ds_read_b64_tr_b16 a[180:181], %[vr0] offset:57344\n\t"
    "v_mfma_f32_16x16x32_bf16 v[108:111], a[188:191], a[60:63], v[108:111]\n\t" "ds_read_b64_tr_b16 a[182:183], %[vr0] offset:59392\n\t" "s_waitcnt lgkmcnt(4)\n\t"
    "v_mfma_f32_16x16x32_bf16 v[112:115], a[192:195], a[48:51], v[112:115]\n\t" "ds_read_b64_tr_b16 a[184:185], %[vr1] offset:49152\n\t"
    "v_mfma_f32_16x16x32_bf16 v[112:115], a[196:199], a[56:59], v[112:115]\n\t" "ds_read_b64_tr_b16 a[186:187], %[vr1] offset:51200\n\t" "global_load_dwordx4 a[36:39], %[vx], s[40:41] offset:1024 sc1\n\t"
    "v_mfma_f32_16x16x32_bf16 v[116:119], a[192:195], a[52:55], v[116:119]\n\t" "ds_read_b64_tr_b16 a[188:189], %[vr1] offset:57344\n\t"
    "v_mfma_f32_16x16x32_bf16 v[116:119], a[196:199], a[60:63], v[116:119]\n\t" "ds_read_b64_tr_b16 a[190:191], %[vr1] offset:59392\n\t" "s_add_u32 m0, %[sl0], 114688\n\t"
    "v_mfma_f32_16x16x32_bf16 v[120:123], a[200:203], a[48:51], v[120:123]\n\t" "ds_read_b64_tr_b16 a[192:193], %[vr2] offset:49152\n\t"
    "v_mfma_f32_16x16x32_bf16 v[120:123], a[204:207], a[56:59], v[120:123]\n\t" "ds_read_b64_tr_b16 a[194:195], %[vr2] offset:51200\n\t" "global_load_lds_dwordx4 %[vb0], %[sb]\n\t"
    "v_mfma_f32_16x16x32_bf16 v[124:127], a[200:203], a[52:55], v[124:127]\n\t" "ds_read_b64_tr_b16 a[196:197], %[vr2] offset:57344\n\t" "v_add_u32 %[vb0], %[pw], %[vb0]\n\t"
    "v_mfma_f32_16x16x32_bf16 v[124:127], a[204:207], a[60:63], v[124:127]\n\t" "ds_read_b64_tr_b16 a[198:199], %[vr2] offset:59392\n\t" "s_waitcnt lgkmcnt(4) vmcnt(25)\n\t"
    "v_mfma_f32_16x16x32_bf16 v[224:227], a[176:179], a[64:67], v[224:227]\n\t" "ds_read_b64_tr_b16 a[200:201], %[vr3] offset:49152\n\t" "s_add_u32 m0, %[sl1], 114688\n\t"
    "v_mfma_f32_16x16x32_bf16 v[224:227], a[180:183], a[72:75], v[224:227]\n\t" "ds_read_b64_tr_b16 a[202:203], %[vr3] offset:51200\n\t" "global_load_lds_dwordx4 %[vb1], %[sb]\n\t"
    "v_mfma_f32_16x16x32_bf16 v[240:243], a[176:179], a[68:71], v[240:243]\n\t" "ds_read_b64_tr_b16 a[204:205], %[vr3] offset:57344\n\t" "v_add_u32 %[vb1], %[pw], %[vb1]\n\t"
    "v_mfma_f32_16x16x32_bf16 v[240:243], a[180:183], a[76:79], v[240:243]\n\t" "ds_read_b64_tr_b16 a[206:207], %[vr3] offset:59392\n\t"
    "v_mfma_f32_16x16x32_bf16 v[228:231], a[184:187], a[64:67], v[228:231]\n\t" "ds_read_b64_tr_b16 a[176:177], %[vr12] offset:49152\n\t"
    "v_mfma_f32_16x16x32_bf16 v[228:231], a[188:191], a[72:75], v[228:231]\n\t" "ds_read_b64_tr_b16 a[178:179], %[vr12] offset:51200\n\t" "global_load_dwordx4 v[184:187], %[vg], %[sg] offset:128 sc1\n\t"
    "v_mfma_f32_16x16x32_bf16 v[244:247], a[184:187], a[68:71], v[244:247]\n\t" "ds_read_b64_tr_b16 a[180:181], %[vr12] offset:57344\n\t"
    "v_mfma_f32_16x16x32_bf16 v[244:247], a[188:191], a[76:79], v[244:247]\n\t" "ds_read_b64_tr_b16 a[182:183], %[vr12] offset:59392\n\t" "s_waitcnt lgkmcnt(4)\n\t"
    "v_mfma_f32_16x16x32_bf16 v[232:235], a[192:195], a[64:67], v[232:235]\n\t" "ds_read_b64_tr_b16 a[184:185], %[vr13] offset:49152\n\t" "global_load_dword a255, %[vx], %[cd]\n\t"
    "v_mfma_f32_16x16x32_bf16 v[232:235], a[196:199], a[72:75], v[232:235]\n\t" "ds_read_b64_tr_b16 a[186:187], %[vr13] offset:51200\n\t"
    "global_load_dwordx4 a[40:43], %[vx], s[40:41] offset:2048 sc1\n\t" "v_mfma_f32_16x16x32_bf16 v[248:251], a[192:195], a[68:71], v[248:251]\n\t"
    "ds_read_b64_tr_b16 a[188:189], %[vr13] offset:57344\n\t" "v_mfma_f32_16x16x32_bf16 v[248:251], a[196:199], a[76:79], v[248:251]\n\t" "ds_read_b64_tr_b16 a[190:191], %[vr13] offset:59392\n\t"
    "v_mfma_f32_16x16x32_bf16 v[236:239], a[200:203], a[64:67], v[236:239]\n\t" "ds_read_b64_tr_b16 a[192:193], %[vr14] offset:49152\n\t"
    "v_mfma_f32_16x16x32_bf16 v[236:239], a[204:207], a[72:75], v[236:239]\n\t" "ds_read_b64_tr_b16 a[194:195], %[vr14] offset:51200\n\t"
    "global_load_dwordx4 a[44:47], %[vx], s[40:41] offset:3072 sc1\n\t" "v_mfma_f32_16x16x32_bf16 v[252:255], a[200:203], a[68:71], v[252:255]\n\t"
    "ds_read_b64_tr_b16 a[196:197], %[vr14] offset:57344\n\t" "v_mfma_f32_16x16x32_bf16 v[252:255], a[204:207], a[76:79], v[252:255]\n\t" "ds_read_b64_tr_b16 a[198:199], %[vr14] offset:59392\n\t"
    "s_waitcnt lgkmcnt(4)\n\t" "v_mfma_f32_16x16x32_bf16 v[96:99], a[176:179], a[64:67], v[96:99]\n\t" "ds_read_b64_tr_b16 a[200:201], %[vr15] offset:49152\n\t" "s_add_u32 m0, %[sl0], 122880\n\t"
    "v_mfma_f32_16x16x32_bf16 v[96:99], a[180:183], a[72:75], v[96:99]\n\t" "ds_read_b64_tr_b16 a[202:203], %[vr15] offset:51200\n\t" "global_load_lds_dwordx4 %[vb0], %[sb]\n\t"
    "v_mfma_f32_16x16x32_bf16 v[100:103], a[176:179], a[68:71], v[100:103]\n\t" "ds_read_b64_tr_b16 a[204:205], %[vr15] offset:57344\n\t" "v_add_u32 %[vb0], 0x10000, %[vb0]\n\t"
    "v_mfma_f32_16x16x32_bf16 v[100:103], a[180:183], a[76:79], v[100:103]\n\t" "ds_read_b64_tr_b16 a[206:207], %[vr15] offset:59392\n\t"
    "v_mfma_f32_16x16x32_bf16 v[104:107], a[184:187], a[64:67], v[104:107]\n\t" "s_waitcnt vmcnt(29)\n\t" "s_barrier\n\t" "ds_read_b64_tr_b16 a[176:177], %[vr4] offset:0\n\t"
    "v_mfma_f32_16x16x32_bf16 v[104:107], a[188:191], a[72:75], v[104:107]\n\t" "ds_read_b64_tr_b16 a[178:179], %[vr4] offset:2048\n\t" "global_load_dwordx4 v[188:191], %[vg], %[sg] offset:192 sc1\n\t"
    "v_mfma_f32_16x16x32_bf16 v[108:111], a[184:187], a[68:71], v[108:111]\n\t" "ds_read_b64_tr_b16 a[180:181], %[vr4] offset:8192\n\t" "s_add_u32 m0, %[sl1], 122880\n\t"
    "v_mfma_f32_16x16x32_bf16 v[108:111], a[188:191], a[76:79], v[108:111]\n\t" "ds_read_b64_tr_b16 a[182:183], %[vr4] offset:10240\n\t" "s_waitcnt lgkmcnt(4)\n\t"
    "v_mfma_f32_16x16x32_bf16 v[112:115], a[192:195], a[64:67], v[112:115]\n\t" "ds_read_b64_tr_b16 a[184:185], %[vr5] offset:0\n\t" "global_load_lds_dwordx4 %[vb1], %[sb]\n\t"
    "v_mfma_f32_16x16x32_bf16 v[112:115], a[196:199], a[72:75], v[112:115]\n\t" "ds_read_b64_tr_b16 a[186:187], %[vr5] offset:2048\n\t" "v_add_u32 %[vb1], 0x10000, %[vb1]\n\t"
    "v_mfma_f32_16x16x32_bf16 v[116:119], a[192:195], a[68:71], v[116:119]\n\t" "ds_read_b64_tr_b16 a[188:189], %[vr5] offset:8192\n\t" "s_add_u32 s40, s40, 8192\n\t" "s_addc_u32 s41, s41, 0\n\t"
    "v_mfma_f32_16x16x32_bf16 v[116:119], a[196:199], a[76:79], v[116:119]\n\t" "ds_read_b64_tr_b16 a[190:191], %[vr5] offset:10240\n\t"
    "global_load_dwordx4 a[48:51], %[vx], s[40:41] offset:-4096 sc1\n\t" "v_mfma_f32_16x16x32_bf16 v[120:123], a[200:203], a[64:67], v[120:123]\n\t" "ds_read_b64_tr_b16 a[192:193], %[vr6] offset:0\n\t"
    "v_mfma_f32_16x16x32_bf16 v[120:123], a[204:207], a[72:75], v[120:123]\n\t" "ds_read_b64_tr_b16 a[194:195], %[vr6] offset:2048\n\t"
    "v_mfma_f32_16x16x32_bf16 v[124:127], a[200:203], a[68:71], v[124:127]\n\t" "ds_read_b64_tr_b16 a[196:197], %[vr6] offset:8192\n\t"
    "v_mfma_f32_16x16x32_bf16 v[124:127], a[204:207], a[76:79], v[124:127]\n\t" "ds_read_b64_tr_b16 a[198:199], %[vr6] offset:10240\n\t" "s_waitcnt lgkmcnt(4) vmcnt(26)\n\t"
    "v_mfma_f32_16x16x32_bf16 v[224:227], a[176:179], a[0:3], v[224:227]\n\t" "ds_read_b64_tr_b16 a[200:201], %[vr7] offset:0\n\t" "global_load_dwordx4 a[52:55], %[vx], s[40:41] offset:-3072 sc1\n\t"
    "v_mfma_f32_16x16x32_bf16 v[224:227], a[180:183], a[8:11], v[224:227]\n\t" "ds_read_b64_tr_b16 a[202:203], %[vr7] offset:2048\n\t" "s_add_u32 m0, %[sl0], 131072\n\t"
    "v_mfma_f32_4x4x4_16b_bf16 v[144:147], %[id], a[232:233], 0\n\t" "v_mfma_f32_4x4x4_16b_bf16 v[152:155], %[id], a[234:235], 0\n\t" "v_mfma_f32_4x4x4_16b_bf16 v[160:163], %[id], a[236:237], 0\n\t"
    "v_mfma_f32_4x4x4_16b_bf16 v[168:171], %[id], a[238:239], 0\n\t" "v_mfma_f32_4x4x4_16b_bf16 v[148:151], %[id], a[240:241], 0\n\t" "v_mfma_f32_4x4x4_16b_bf16 v[156:159], %[id], a[242:243], 0\n\t"
    "v_mfma_f32_4x4x4_16b_bf16 v[164:167], %[id], a[244:245], 0\n\t" "v_mfma_f32_4x4x4_16b_bf16 v[172:175], %[id], a[246:247], 0\n\t"
    "v_mfma_f32_16x16x32_bf16 v[240:243], a[176:179], a[4:7], v[240:243]\n\t" "ds_read_b64_tr_b16 a[204:205], %[vr7] offset:8192\n\t"
    "v_mfma_f32_16x16x32_bf16 v[240:243], a[180:183], a[12:15], v[240:243]\n\t" "ds_read_b64_tr_b16 a[206:207], %[vr7] offset:10240\n\t" "global_load_lds_dwordx4 %[vb0], %[sb]\n\t"
    "v_mfma_f32_16x16x32_bf16 v[228:231], a[184:187], a[0:3], v[228:231]\n\t" "ds_read_b64_tr_b16 a[176:177], %[vr16] offset:0\n\t" "v_add_u32 %[vb0], %[qw], %[vb0]\n\t"
    "v_mfma_f32_16x16x32_bf16 v[228:231], a[188:191], a[8:11], v[228:231]\n\t" "ds_read_b64_tr_b16 a[178:179], %[vr16] offset:2048\n\t" "s_add_u32 m0, %[sl1], 131072\n\t"
    "v_mfma_f32_16x16x32_bf16 v[244:247], a[184:187], a[4:7], v[244:247]\n\t" "ds_read_b64_tr_b16 a[180:181], %[vr16] offset:8192\n\t"
    "v_mfma_f32_16x16x32_bf16 v[244:247], a[188:191], a[12:15], v[244:247]\n\t" "ds_read_b64_tr_b16 a[182:183], %[vr16] offset:10240\n\t" "s_waitcnt lgkmcnt(4)\n\t"
    "v_mfma_f32_16x16x32_bf16 v[232:235], a[192:195], a[0:3], v[232:235]\n\t" "ds_read_b64_tr_b16 a[184:185], %[vr17] offset:0\n\t" "global_load_lds_dwordx4 %[vb1], %[sb]\n\t"
    "v_mfma_f32_16x16x32_bf16 v[232:235], a[196:199], a[8:11], v[232:235]\n\t" "ds_read_b64_tr_b16 a[186:187], %[vr17] offset:2048\n\t" "v_add_u32 %[vb1], %[qw], %[vb1]\n\t"
    "v_mfma_f32_16x16x32_bf16 v[248:251], a[192:195], a[4:7], v[248:251]\n\t" "ds_read_b64_tr_b16 a[188:189], %[vr17] offset:8192\n\t"
    "v_mfma_f32_16x16x32_bf16 v[248:251], a[196:199], a[12:15], v[248:251]\n\t" "ds_read_b64_tr_b16 a[190:191], %[vr17] offset:10240\n\t"
    "global_load_dwordx4 a[56:59], %[vx], s[40:41] offset:-2048 sc1\n\t" "v_mfma_f32_16x16x32_bf16 v[236:239], a[200:203], a[0:3], v[236:239]\n\t" "ds_read_b64_tr_b16 a[192:193], %[vr18] offset:0\n\t"
    "v_mfma_f32_16x16x32_bf16 v[236:239], a[204:207], a[8:11], v[236:239]\n\t" "ds_read_b64_tr_b16 a[194:195], %[vr18] offset:2048\n\t"
    "v_mfma_f32_16x16x32_bf16 v[252:255], a[200:203], a[4:7], v[252:255]\n\t" "ds_read_b64_tr_b16 a[196:197], %[vr18] offset:8192\n\t"
    "v_mfma_f32_16x16x32_bf16 v[252:255], a[204:207], a[12:15], v[252:255]\n\t" "ds_read_b64_tr_b16 a[198:199], %[vr18] offset:10240\n\t" "s_waitcnt lgkmcnt(4)\n\t"
    "v_mfma_f32_16x16x32_bf16 v[96:99], a[176:179], a[0:3], v[96:99]\n\t" "ds_read_b64_tr_b16 a[200:201], %[vr19] offset:0\n\t" "global_load_dwordx4 a[60:63], %[vx], s[40:41] offset:-1024 sc1\n\t"
    "v_mfma_f32_16x16x32_bf16 v[96:99], a[180:183], a[8:11], v[96:99]\n\t" "ds_read_b64_tr_b16 a[202:203], %[vr19] offset:2048\n\t" "s_add_u32 m0, %[sl0], 16384\n\t"
    "v_mfma_f32_16x16x32_bf16 v[100:103], a[176:179], a[4:7], v[100:103]\n\t" "ds_read_b64_tr_b16 a[204:205], %[vr19] offset:8192\n\t"
    "v_mfma_f32_16x16x32_bf16 v[100:103], a[180:183], a[12:15], v[100:103]\n\t" "ds_read_b64_tr_b16 a[206:207], %[vr19] offset:10240\n\t" "global_load_lds_dwordx4 %[vb0], %[sb]\n\t"
    "v_mfma_f32_16x16x32_bf16 v[104:107], a[184:187], a[0:3], v[104:107]\n\t" "s_waitcnt vmcnt(30)\n\t" "s_barrier\n\t" "ds_read_b64_tr_b16 a[176:177], %[vr4] offset:16384\n\t"
    "v_mfma_f32_16x16x32_bf16 v[104:107], a[188:191], a[8:11], v[104:107]\n\t" "ds_read_b64_tr_b16 a[178:179], %[vr4] offset:18432\n\t" "v_add_u32 %[vb0], %[pw], %[vb0]\n\t"
    "v_mfma_f32_16x16x32_bf16 v[108:111], a[184:187], a[4:7], v[108:111]\n\t" "ds_read_b64_tr_b16 a[180:181], %[vr4] offset:24576\n\t" "s_add_u32 m0, %[sl1], 16384\n\t"
    "v_mfma_f32_16x16x32_bf16 v[108:111], a[188:191], a[12:15], v[108:111]\n\t" "ds_read_b64_tr_b16 a[182:183], %[vr4] offset:26624\n\t" "s_waitcnt lgkmcnt(4)\n\t"
    "v_mfma_f32_16x16x32_bf16 v[112:115], a[192:195], a[0:3], v[112:115]\n\t" "ds_read_b64_tr_b16 a[184:185], %[vr5] offset:16384\n\t" "global_load_lds_dwordx4 %[vb1], %[sb]\n\t"
    "v_mfma_f32_16x16x32_bf16 v[112:115], a[196:199], a[8:11], v[112:115]\n\t" "ds_read_b64_tr_b16 a[186:187], %[vr5] offset:18432\n\t" "v_add_u32 %[vb1], %[pw], %[vb1]\n\t"
    "v_mfma_f32_16x16x32_bf16 v[116:119], a[192:195], a[4:7], v[116:119]\n\t" "ds_read_b64_tr_b16 a[188:189], %[vr5] offset:24576\n\t"
    "v_mfma_f32_16x16x32_bf16 v[116:119], a[196:199], a[12:15], v[116:119]\n\t" "ds_read_b64_tr_b16 a[190:191], %[vr5] offset:26624\n\t" "global_load_dwordx2 a[64:65], %[vo], s[40:41] offset:0 sc1\n\t"
    "v_mfma_f32_16x16x32_bf16 v[120:123], a[200:203], a[0:3], v[120:123]\n\t" "ds_read_b64_tr_b16 a[192:193], %[vr6] offset:16384\n\t"
    "v_mfma_f32_16x16x32_bf16 v[120:123], a[204:207], a[8:11], v[120:123]\n\t" "ds_read_b64_tr_b16 a[194:195], %[vr6] offset:18432\n\t" "global_load_dwordx2 a[66:67], %[vo], s[40:41] offset:1024 sc1\n\t"
    "v_mfma_f32_16x16x32_bf16 v[124:127], a[200:203], a[4:7], v[124:127]\n\t" "ds_read_b64_tr_b16 a[196:197], %[vr6] offset:24576\n\t"
    "v_mfma_f32_16x16x32_bf16 v[124:127], a[204:207], a[12:15], v[124:127]\n\t" "ds_read_b64_tr_b16 a[198:199], %[vr6] offset:26624\n\t" "s_waitcnt lgkmcnt(4) vmcnt(24)\n\t"
    "v_mfma_f32_16x16x32_bf16 v[224:227], a[176:179], a[16:19], v[224:227]\n\t" "ds_read_b64_tr_b16 a[200:201], %[vr7] offset:16384\n\t" "global_load_dwordx2 a[68:69], %[vo], s[40:41] offset:512 sc1\n\t"
    "v_mfma_f32_16x16x32_bf16 v[224:227], a[180:183], a[24:27], v[224:227]\n\t" "ds_read_b64_tr_b16 a[202:203], %[vr7] offset:18432\n\t" "global_load_dwordx2 a[70:71], %[vo], s[40:41] offset:1536 sc1\n\t"
    "v_mfma_f32_16x16x32_bf16 v[240:243], a[176:179], a[20:23], v[240:243]\n\t" "ds_read_b64_tr_b16 a[204:205], %[vr7] offset:24576\n\t" "s_add_u32 m0, %[sl0], 24576\n\t"
    "v_mfma_f32_16x16x32_bf16 v[240:243], a[180:183], a[28:31], v[240:243]\n\t" "ds_read_b64_tr_b16 a[206:207], %[vr7] offset:26624\n\t" "global_load_lds_dwordx4 %[vb0], %[sb]\n\t"
    "v_mfma_f32_16x16x32_bf16 v[228:231], a[184:187], a[16:19], v[228:231]\n\t" "ds_read_b64_tr_b16 a[176:177], %[vr16] offset:16384\n\t" "v_add_u32 %[vb0], %[qw], %[vb0]\n\t"
    "v_mfma_f32_16x16x32_bf16 v[228:231], a[188:191], a[24:27], v[228:231]\n\t" "ds_read_b64_tr_b16 a[178:179], %[vr16] offset:18432\n\t" "s_add_u32 m0, %[sl1], 24576\n\t"
    "v_mfma_f32_16x16x32_bf16 v[244:247], a[184:187], a[20:23], v[244:247]\n\t" "ds_read_b64_tr_b16 a[180:181], %[vr16] offset:24576\n\t"
    "v_mfma_f32_16x16x32_bf16 v[244:247], a[188:191], a[28:31], v[244:247]\n\t" "ds_read_b64_tr_b16 a[182:183], %[vr16] offset:26624\n\t" "s_waitcnt lgkmcnt(4)\n\t"
    "v_mfma_f32_16x16x32_bf16 v[232:235], a[192:195], a[16:19], v[232:235]\n\t" "ds_read_b64_tr_b16 a[184:185], %[vr17] offset:16384\n\t" "global_load_lds_dwordx4 %[vb1], %[sb]\n\t"
    "v_mfma_f32_16x16x32_bf16 v[232:235], a[196:199], a[24:27], v[232:235]\n\t" "ds_read_b64_tr_b16 a[186:187], %[vr17] offset:18432\n\t" "v_add_u32 %[vb1], %[qw], %[vb1]\n\t"
    "v_mfma_f32_16x16x32_bf16 v[248:251], a[192:195], a[20:23], v[248:251]\n\t" "ds_read_b64_tr_b16 a[188:189], %[vr17] offset:24576\n\t"
    "v_mfma_f32_16x16x32_bf16 v[248:251], a[196:199], a[28:31], v[248:251]\n\t" "ds_read_b64_tr_b16 a[190:191], %[vr17] offset:26624\n\t"
    "global_load_dwordx4 a[72:75], %[vx], s[40:41] offset:2048 sc1\n\t" "v_mfma_f32_16x16x32_bf16 v[236:239], a[200:203], a[16:19], v[236:239]\n\t"
    "ds_read_b64_tr_b16 a[192:193], %[vr18] offset:16384\n\t" "v_mfma_f32_16x16x32_bf16 v[236:239], a[204:207], a[24:27], v[236:239]\n\t" "ds_read_b64_tr_b16 a[194:195], %[vr18] offset:18432\n\t"
    "v_mfma_f32_16x16x32_bf16 v[252:255], a[200:203], a[20:23], v[252:255]\n\t" "ds_read_b64_tr_b16 a[196:197], %[vr18] offset:24576\n\t"
    "v_mfma_f32_16x16x32_bf16 v[252:255], a[204:207], a[28:31], v[252:255]\n\t" "ds_read_b64_tr_b16 a[198:199], %[vr18] offset:26624\n\t" "s_waitcnt lgkmcnt(4)\n\t"
    "v_mfma_f32_16x16x32_bf16 v[96:99], a[176:179], a[16:19], v[96:99]\n\t" "ds_read_b64_tr_b16 a[200:201], %[vr19] offset:16384\n\t" "global_load_dwordx4 a[76:79], %[vx], s[40:41] offset:3072 sc1\n\t"
    "v_mfma_f32_16x16x32_bf16 v[96:99], a[180:183], a[24:27], v[96:99]\n\t" "ds_read_b64_tr_b16 a[202:203], %[vr19] offset:18432\n\t" "s_add_u32 m0, %[sl0], 32768\n\t"
    "v_mfma_f32_16x16x32_bf16 v[100:103], a[176:179], a[20:23], v[100:103]\n\t" "ds_read_b64_tr_b16 a[204:205], %[vr19] offset:24576\n\t"
    "v_mfma_f32_16x16x32_bf16 v[100:103], a[180:183], a[28:31], v[100:103]\n\t" "ds_read_b64_tr_b16 a[206:207], %[vr19] offset:26624\n\t" "global_load_lds_dwordx4 %[vb0], %[sb]\n\t"
    "v_mfma_f32_16x16x32_bf16 v[104:107], a[184:187], a[16:19], v[104:107]\n\t" "s_waitcnt vmcnt(29)\n\t" "s_barrier\n\t" "ds_read_b64_tr_b16 a[176:177], %[vr4] offset:32768\n\t"
    "v_mfma_f32_16x16x32_bf16 v[104:107], a[188:191], a[24:27], v[104:107]\n\t" "ds_read_b64_tr_b16 a[178:179], %[vr4] offset:34816\n\t" "v_add_u32 %[vb0], %[pw], %[vb0]\n\t"
    "v_mfma_f32_16x16x32_bf16 v[108:111], a[184:187], a[20:23], v[108:111]\n\t" "ds_read_b64_tr_b16 a[180:181], %[vr4] offset:40960\n\t" "s_add_u32 m0, %[sl1], 32768\n\t"
    "v_mfma_f32_16x16x32_bf16 v[108:111], a[188:191], a[28:31], v[108:111]\n\t" "ds_read_b64_tr_b16 a[182:183], %[vr4] offset:43008\n\t" "s_waitcnt lgkmcnt(4)\n\t"
    "v_mfma_f32_16x16x32_bf16 v[112:115], a[192:195], a[16:19], v[112:115]\n\t" "ds_read_b64_tr_b16 a[184:185], %[vr5] offset:32768\n\t" "global_load_lds_dwordx4 %[vb1], %[sb]\n\t"
    "v_mfma_f32_16x16x32_bf16 v[112:115], a[196:199], a[24:27], v[112:115]\n\t" "ds_read_b64_tr_b16 a[186:187], %[vr5] offset:34816\n\t" "v_add_u32 %[vb1], %[pw], %[vb1]\n\t"
    "v_mfma_f32_16x16x32_bf16 v[116:119], a[192:195], a[20:23], v[116:119]\n\t" "ds_read_b64_tr_b16 a[188:189], %[vr5] offset:40960\n\t" "s_add_u32 s40, s40, 8192\n\t" "s_addc_u32 s41, s41, 0\n\t"
    "v_mfma_f32_16x16x32_bf16 v[116:119], a[196:199], a[28:31], v[116:119]\n\t" "ds_read_b64_tr_b16 a[190:191], %[vr5] offset:43008\n\t" "global_load_dwordx4 a[0:3], %[vx], s[40:41] offset:-4096 sc1\n\t"
    "v_mfma_f32_16x16x32_bf16 v[120:123], a[200:203], a[16:19], v[120:123]\n\t" "ds_read_b64_tr_b16 a[192:193], %[vr6] offset:32768\n\t"
    "v_mfma_f32_16x16x32_bf16 v[120:123], a[204:207], a[24:27], v[120:123]\n\t" "ds_read_b64_tr_b16 a[194:195], %[vr6] offset:34816\n\t"
    "v_mfma_f32_16x16x32_bf16 v[124:127], a[200:203], a[20:23], v[124:127]\n\t" "ds_read_b64_tr_b16 a[196:197], %[vr6] offset:40960\n\t"
    "v_mfma_f32_16x16x32_bf16 v[124:127], a[204:207], a[28:31], v[124:127]\n\t" "ds_read_b64_tr_b16 a[198:199], %[vr6] offset:43008\n\t" "s_waitcnt lgkmcnt(4) vmcnt(22)\n\t"
    "v_mfma_f32_16x16x32_bf16 v[224:227], a[176:179], a[32:35], v[224:227]\n\t" "ds_read_b64_tr_b16 a[200:201], %[vr7] offset:32768\n\t" "global_load_dwordx4 a[4:7], %[vx], s[40:41] offset:-3072 sc1\n\t"
    "v_mfma_f32_16x16x32_bf16 v[224:227], a[180:183], a[40:43], v[224:227]\n\t" "ds_read_b64_tr_b16 a[202:203], %[vr7] offset:34816\n\t" "s_add_u32 m0, %[sl0], 40960\n\t"
    "v_mfma_f32_16x16x32_bf16 v[240:243], a[176:179], a[36:39], v[240:243]\n\t" "ds_read_b64_tr_b16 a[204:205], %[vr7] offset:40960\n\t"
    "v_mfma_f32_16x16x32_bf16 v[240:243], a[180:183], a[44:47], v[240:243]\n\t" "ds_read_b64_tr_b16 a[206:207], %[vr7] offset:43008\n\t" "global_load_lds_dwordx4 %[vb0], %[sb]\n\t"
    "v_mfma_f32_16x16x32_bf16 v[228:231], a[184:187], a[32:35], v[228:231]\n\t" "ds_read_b64_tr_b16 a[176:177], %[vr16] offset:32768\n\t" "v_add_u32 %[vb0], 0x10000, %[vb0]\n\t"
    "v_mfma_f32_16x16x32_bf16 v[228:231], a[188:191], a[40:43], v[228:231]\n\t" "ds_read_b64_tr_b16 a[178:179], %[vr16] offset:34816\n\t" "s_add_u32 m0, %[sl1], 40960\n\t"
    "v_mfma_f32_16x16x32_bf16 v[244:247], a[184:187], a[36:39], v[244:247]\n\t" "ds_read_b64_tr_b16 a[180:181], %[vr16] offset:40960\n\t"
    "v_mfma_f32_16x16x32_bf16 v[244:247], a[188:191], a[44:47], v[244:247]\n\t" "ds_read_b64_tr_b16 a[182:183], %[vr16] offset:43008\n\t" "s_waitcnt lgkmcnt(4)\n\t"
    "v_mfma_f32_16x16x32_bf16 v[232:235], a[192:195], a[32:35], v[232:235]\n\t" "ds_read_b64_tr_b16 a[184:185], %[vr17] offset:32768\n\t" "global_load_lds_dwordx4 %[vb1], %[sb]\n\t"
    "v_mfma_f32_16x16x32_bf16 v[232:235], a[196:199], a[40:43], v[232:235]\n\t" "ds_read_b64_tr_b16 a[186:187], %[vr17] offset:34816\n\t" "v_add_u32 %[vb1], 0x10000, %[vb1]\n\t"
    "v_mfma_f32_16x16x32_bf16 v[248:251], a[192:195], a[36:39], v[248:251]\n\t" "ds_read_b64_tr_b16 a[188:189], %[vr17] offset:40960\n\t"
    "v_mfma_f32_16x16x32_bf16 v[248:251], a[196:199], a[44:47], v[248:251]\n\t" "ds_read_b64_tr_b16 a[190:191], %[vr17] offset:43008\n\t"
    "global_load_dwordx4 a[8:11], %[vx], s[40:41] offset:-2048 sc1\n\t" "v_mfma_f32_16x16x32_bf16 v[236:239], a[200:203], a[32:35], v[236:239]\n\t"
    "ds_read_b64_tr_b16 a[192:193], %[vr18] offset:32768\n\t" "v_mfma_f32_16x16x32_bf16 v[236:239], a[204:207], a[40:43], v[236:239]\n\t" "ds_read_b64_tr_b16 a[194:195], %[vr18] offset:34816\n\t"
    "v_mfma_f32_16x16x32_bf16 v[252:255], a[200:203], a[36:39], v[252:255]\n\t" "ds_read_b64_tr_b16 a[196:197], %[vr18] offset:40960\n\t"
    "v_mfma_f32_16x16x32_bf16 v[252:255], a[204:207], a[44:47], v[252:255]\n\t" "ds_read_b64_tr_b16 a[198:199], %[vr18] offset:43008\n\t" "s_waitcnt lgkmcnt(4)\n\t"
    "v_mfma_f32_16x16x32_bf16 v[96:99], a[176:179], a[32:35], v[96:99]\n\t" "ds_read_b64_tr_b16 a[200:201], %[vr19] offset:32768\n\t" "global_load_dwordx4 a[12:15], %[vx], s[40:41] offset:-1024 sc1\n\t"
    "v_mfma_f32_16x16x32_bf16 v[96:99], a[180:183], a[40:43], v[96:99]\n\t" "ds_read_b64_tr_b16 a[202:203], %[vr19] offset:34816\n\t" "s_add_u32 m0, %[sl0], 139264\n\t"
    "v_mfma_f32_16x16x32_bf16 v[100:103], a[176:179], a[36:39], v[100:103]\n\t" "ds_read_b64_tr_b16 a[204:205], %[vr19] offset:40960\n\t"
    "v_mfma_f32_16x16x32_bf16 v[100:103], a[180:183], a[44:47], v[100:103]\n\t" "ds_read_b64_tr_b16 a[206:207], %[vr19] offset:43008\n\t" "global_load_lds_dwordx4 %[vb0], %[sb]\n\t"
    "v_mfma_f32_16x16x32_bf16 v[104:107], a[184:187], a[32:35], v[104:107]\n\t" "s_waitcnt vmcnt(25)\n\t" "s_barrier\n\t" "ds_read_b64_tr_b16 a[176:177], %[vr4] offset:49152\n\t"
    "v_mfma_f32_16x16x32_bf16 v[104:107], a[188:191], a[40:43], v[104:107]\n\t" "ds_read_b64_tr_b16 a[178:179], %[vr4] offset:51200\n\t" "v_add_u32_e64 %[vb0], %[qw], %[vb0]\n\t"
    "v_mfma_f32_16x16x32_bf16 v[108:111], a[184:187], a[36:39], v[108:111]\n\t" "ds_read_b64_tr_b16 a[180:181], %[vr4] offset:57344\n\t" "s_add_u32 m0, %[sl1], 139264\n\t"
    "v_mfma_f32_16x16x32_bf16 v[108:111], a[188:191], a[44:47], v[108:111]\n\t" "ds_read_b64_tr_b16 a[182:183], %[vr4] offset:59392\n\t" "s_waitcnt lgkmcnt(4)\n\t"
    "v_mfma_f32_16x16x32_bf16 v[112:115], a[192:195], a[32:35], v[112:115]\n\t" "ds_read_b64_tr_b16 a[184:185], %[vr5] offset:49152\n\t" "global_load_lds_dwordx4 %[vb1], %[sb]\n\t"
    "v_mfma_f32_16x16x32_bf16 v[112:115], a[196:199], a[40:43], v[112:115]\n\t" "ds_read_b64_tr_b16 a[186:187], %[vr5] offset:51200\n\t" "v_add_u32 %[vb1], %[qw], %[vb1]\n\t"
    "v_mfma_f32_16x16x32_bf16 v[116:119], a[192:195], a[36:39], v[116:119]\n\t" "ds_read_b64_tr_b16 a[188:189], %[vr5] offset:57344\n\t"
    "v_mfma_f32_16x16x32_bf16 v[116:119], a[196:199], a[44:47], v[116:119]\n\t" "ds_read_b64_tr_b16 a[190:191], %[vr5] offset:59392\n\t" "global_load_dwordx4 a[16:19], %[vx], s[40:41] offset:0 sc1\n\t"
    "v_mfma_f32_16x16x32_bf16 v[120:123], a[200:203], a[32:35], v[120:123]\n\t" "ds_read_b64_tr_b16 a[192:193], %[vr6] offset:49152\n\t"
    "v_mfma_f32_16x16x32_bf16 v[120:123], a[204:207], a[40:43], v[120:123]\n\t" "ds_read_b64_tr_b16 a[194:195], %[vr6] offset:51200\n\t"
    "v_mfma_f32_16x16x32_bf16 v[124:127], a[200:203], a[36:39], v[124:127]\n\t" "ds_read_b64_tr_b16 a[196:197], %[vr6] offset:57344\n\t"
    "v_mfma_f32_16x16x32_bf16 v[124:127], a[204:207], a[44:47], v[124:127]\n\t" "ds_read_b64_tr_b16 a[198:199], %[vr6] offset:59392\n\t" "s_waitcnt lgkmcnt(4) vmcnt(21)\n\t"
    "v_mfma_f32_16x16x32_bf16 v[224:227], a[176:179], a[48:51], v[224:227]\n\t" "ds_read_b64_tr_b16 a[200:201], %[vr7] offset:49152\n\t" "global_load_dwordx4 a[20:23], %[vx], s[40:41] offset:1024 sc1\n\t"
    "v_mfma_f32_16x16x32_bf16 v[224:227], a[180:183], a[56:59], v[224:227]\n\t" "ds_read_b64_tr_b16 a[202:203], %[vr7] offset:51200\n\t" "s_add_u32 m0, %[sl0], 147456\n\t"
    "v_mfma_f32_16x16x32_bf16 v[240:243], a[176:179], a[52:55], v[240:243]\n\t" "ds_read_b64_tr_b16 a[204:205], %[vr7] offset:57344\n\t"
    "v_mfma_f32_16x16x32_bf16 v[240:243], a[180:183], a[60:63], v[240:243]\n\t" "ds_read_b64_tr_b16 a[206:207], %[vr7] offset:59392\n\t" "global_load_lds_dwordx4 %[vb0], %[sb]\n\t"
    "v_mfma_f32_16x16x32_bf16 v[228:231], a[184:187], a[48:51], v[228:231]\n\t" "ds_read_b64_tr_b16 a[176:177], %[vr16] offset:49152\n\t" "v_add_u32 %[vb0], %[pw], %[vb0]\n\t"
    "v_mfma_f32_16x16x32_bf16 v[228:231], a[188:191], a[56:59], v[228:231]\n\t" "ds_read_b64_tr_b16 a[178:179], %[vr16] offset:51200\n\t"
    "v_mfma_f32_16x16x32_bf16 v[244:247], a[184:187], a[52:55], v[244:247]\n\t" "ds_read_b64_tr_b16 a[180:181], %[vr16] offset:57344\n\t"
    "v_mfma_f32_16x16x32_bf16 v[244:247], a[188:191], a[60:63], v[244:247]\n\t" "ds_read_b64_tr_b16 a[182:183], %[vr16] offset:59392\n\t" "s_waitcnt lgkmcnt(4)\n\t"
    "v_mfma_f32_16x16x32_bf16 v[232:235], a[192:195], a[48:51], v[232:235]\n\t" "ds_read_b64_tr_b16 a[184:185], %[vr17] offset:49152\n\t"
    "global_load_dwordx2 a[24:25], %[vo], s[40:41] offset:2048 sc1\n\t" "v_mfma_f32_16x16x32_bf16 v[232:235], a[196:199], a[56:59], v[232:235]\n\t"
    "ds_read_b64_tr_b16 a[186:187], %[vr17] offset:51200\n\t" "global_load_dwordx2 a[26:27], %[vo], s[40:41] offset:3072 sc1\n\t"
    "v_mfma_f32_16x16x32_bf16 v[248:251], a[192:195], a[52:55], v[248:251]\n\t" "ds_read_b64_tr_b16 a[188:189], %[vr17] offset:57344\n\t"
    "v_mfma_f32_16x16x32_bf16 v[248:251], a[196:199], a[60:63], v[248:251]\n\t" "ds_read_b64_tr_b16 a[190:191], %[vr17] offset:59392\n\t"
    "global_load_dwordx2 a[28:29], %[vo], s[40:41] offset:2560 sc1\n\t" "v_mfma_f32_16x16x32_bf16 v[236:239], a[200:203], a[48:51], v[236:239]\n\t"
    "ds_read_b64_tr_b16 a[192:193], %[vr18] offset:49152\n\t" "v_mfma_f32_16x16x32_bf16 v[236:239], a[204:207], a[56:59], v[236:239]\n\t" "ds_read_b64_tr_b16 a[194:195], %[vr18] offset:51200\n\t"
    "global_load_dwordx2 a[30:31], %[vo], s[40:41] offset:3584 sc1\n\t" "v_mfma_f32_16x16x32_bf16 v[252:255], a[200:203], a[52:55], v[252:255]\n\t"
    "ds_read_b64_tr_b16 a[196:197], %[vr18] offset:57344\n\t" "s_add_u32 m0, %[sl0], 155648\n\t" "v_mfma_f32_16x16x32_bf16 v[252:255], a[204:207], a[60:63], v[252:255]\n\t"
    "ds_read_b64_tr_b16 a[198:199], %[vr18] offset:59392\n\t" "s_waitcnt lgkmcnt(4)\n\t" "v_mfma_f32_16x16x32_bf16 v[96:99], a[176:179], a[48:51], v[96:99]\n\t"
    "ds_read_b64_tr_b16 a[200:201], %[vr19] offset:49152\n\t" "global_load_lds_dwordx4 %[vb0], %[sb]\n\t" "v_mfma_f32_16x16x32_bf16 v[96:99], a[180:183], a[56:59], v[96:99]\n\t"
    "ds_read_b64_tr_b16 a[202:203], %[vr19] offset:51200\n\t" "v_add_u32_e64 %[vb0], %[qw], %[vb0]\n\t" "v_mfma_f32_16x16x32_bf16 v[100:103], a[176:179], a[52:55], v[100:103]\n\t"
    "ds_read_b64_tr_b16 a[204:205], %[vr19] offset:57344\n\t" "s_add_u32 s40, s40, 8192\n\t" "s_addc_u32 s41, s41, 0\n\t" "v_mfma_f32_16x16x32_bf16 v[100:103], a[180:183], a[60:63], v[100:103]\n\t"
    "ds_read_b64_tr_b16 a[206:207], %[vr19] offset:59392\n\t" "global_load_dwordx4 a[80:83], %[vx], s[40:41] offset:-4096 sc1\n\t"
    "v_mfma_f32_16x16x32_bf16 v[104:107], a[184:187], a[48:51], v[104:107]\n\t" "s_waitcnt vmcnt(27)\n\t" "s_barrier\n\t" "ds_read_b64_tr_b16 a[176:177], %[vr8] offset:0\n\t"
    "v_mfma_f32_16x16x32_bf16 v[104:107], a[188:191], a[56:59], v[104:107]\n\t" "ds_read_b64_tr_b16 a[178:179], %[vr8] offset:2048\n\t"
    "v_mfma_f32_16x16x32_bf16 v[108:111], a[184:187], a[52:55], v[108:111]\n\t" "ds_read_b64_tr_b16 a[180:181], %[vr0] offset:16384\n\t"
    "v_mfma_f32_16x16x32_bf16 v[108:111], a[188:191], a[60:63], v[108:111]\n\t" "ds_read_b64_tr_b16 a[182:183], %[vr0] offset:18432\n\t" "s_waitcnt lgkmcnt(4)\n\t"
    "v_mfma_f32_16x16x32_bf16 v[112:115], a[192:195], a[48:51], v[112:115]\n\t" "ds_read_b64_tr_b16 a[184:185], %[vr9] offset:0\n\t" "global_load_dwordx4 a[84:87], %[vx], s[40:41] offset:-3072 sc1\n\t"
    "v_mfma_f32_16x16x32_bf16 v[112:115], a[196:199], a[56:59], v[112:115]\n\t" "ds_read_b64_tr_b16 a[186:187], %[vr9] offset:2048\n\t" "s_add_u32 m0, %[sl0], 0\n\t"
    "v_mfma_f32_16x16x32_bf16 v[116:119], a[192:195], a[52:55], v[116:119]\n\t" "ds_read_b64_tr_b16 a[188:189], %[vr1] offset:16384\n\t"
    "v_mfma_f32_16x16x32_bf16 v[116:119], a[196:199], a[60:63], v[116:119]\n\t" "ds_read_b64_tr_b16 a[190:191], %[vr1] offset:18432\n\t" "global_load_lds_dwordx4 %[vb0], %[sb]\n\t"
    "v_mfma_f32_16x16x32_bf16 v[120:123], a[200:203], a[48:51], v[120:123]\n\t" "ds_read_b64_tr_b16 a[192:193], %[vr10] offset:0\n\t" "v_add_u32 %[vb0], %[pw], %[vb0]\n\t"
    "v_mfma_f32_16x16x32_bf16 v[120:123], a[204:207], a[56:59], v[120:123]\n\t" "ds_read_b64_tr_b16 a[194:195], %[vr10] offset:2048\n\t"
    "v_mfma_f32_16x16x32_bf16 v[124:127], a[200:203], a[52:55], v[124:127]\n\t" "ds_read_b64_tr_b16 a[196:197], %[vr2] offset:16384\n\t"
    "v_mfma_f32_16x16x32_bf16 v[124:127], a[204:207], a[60:63], v[124:127]\n\t" "ds_read_b64_tr_b16 a[198:199], %[vr2] offset:18432\n\t" "s_waitcnt lgkmcnt(4) vmcnt(21)\n\t"
    "v_mfma_f32_16x16x32_bf16 v[224:227], a[176:179], a[64:67], v[224:227]\n\t" "ds_read_b64_tr_b16 a[200:201], %[vr11] offset:0\n\t" "global_load_dwordx4 a[88:91], %[vx], s[40:41] offset:-2048 sc1\n\t"
    "v_mfma_f32_16x16x32_bf16 v[224:227], a[180:183], a[72:75], v[224:227]\n\t" "ds_read_b64_tr_b16 a[202:203], %[vr11] offset:2048\n\t"
    "v_mfma_f32_16x16x32_bf16 v[240:243], a[176:179], a[68:71], v[240:243]\n\t" "ds_read_b64_tr_b16 a[204:205], %[vr3] offset:16384\n\t"
    "v_mfma_f32_16x16x32_bf16 v[240:243], a[180:183], a[76:79], v[240:243]\n\t" "ds_read_b64_tr_b16 a[206:207], %[vr3] offset:18432\n\t"
    "global_load_dwordx4 a[92:95], %[vx], s[40:41] offset:-1024 sc1\n\t" "v_mfma_f32_16x16x32_bf16 v[228:231], a[184:187], a[64:67], v[228:231]\n\t" "ds_read_b64_tr_b16 a[176:177], %[vr20] offset:0\n\t"
    "v_mfma_f32_16x16x32_bf16 v[228:231], a[188:191], a[72:75], v[228:231]\n\t" "ds_read_b64_tr_b16 a[178:179], %[vr20] offset:2048\n\t" "s_add_u32 m0, %[sl0], 8192\n\t"
    "v_mfma_f32_16x16x32_bf16 v[244:247], a[184:187], a[68:71], v[244:247]\n\t" "ds_read_b64_tr_b16 a[180:181], %[vr12] offset:16384\n\t"
    "v_mfma_f32_16x16x32_bf16 v[244:247], a[188:191], a[76:79], v[244:247]\n\t" "ds_read_b64_tr_b16 a[182:183], %[vr12] offset:18432\n\t" "s_waitcnt lgkmcnt(4)\n\t"
    "v_mfma_f32_16x16x32_bf16 v[232:235], a[192:195], a[64:67], v[232:235]\n\t" "ds_read_b64_tr_b16 a[184:185], %[vr21] offset:0\n\t" "global_load_lds_dwordx4 %[vb0], %[sb]\n\t"
    "v_mfma_f32_16x16x32_bf16 v[232:235], a[196:199], a[72:75], v[232:235]\n\t" "ds_read_b64_tr_b16 a[186:187], %[vr21] offset:2048\n\t" "v_add_u32 %[vb0], 0x10000, %[vb0]\n\t"
    "v_mfma_f32_16x16x32_bf16 v[248:251], a[192:195], a[68:71], v[248:251]\n\t" "ds_read_b64_tr_b16 a[188:189], %[vr13] offset:16384\n\t"
    "v_mfma_f32_16x16x32_bf16 v[248:251], a[196:199], a[76:79], v[248:251]\n\t" "ds_read_b64_tr_b16 a[190:191], %[vr13] offset:18432\n\t" "global_load_dwordx4 a[96:99], %[vx], s[40:41] offset:0 sc1\n\t"
    "v_mfma_f32_16x16x32_bf16 v[236:239], a[200:203], a[64:67], v[236:239]\n\t" "ds_read_b64_tr_b16 a[192:193], %[vr22] offset:0\n\t"
    "v_mfma_f32_16x16x32_bf16 v[236:239], a[204:207], a[72:75], v[236:239]\n\t" "ds_read_b64_tr_b16 a[194:195], %[vr22] offset:2048\n\t"
    "v_mfma_f32_16x16x32_bf16 v[252:255], a[200:203], a[68:71], v[252:255]\n\t" "ds_read_b64_tr_b16 a[196:197], %[vr14] offset:16384\n\t"
    "v_mfma_f32_16x16x32_bf16 v[252:255], a[204:207], a[76:79], v[252:255]\n\t" "ds_read_b64_tr_b16 a[198:199], %[vr14] offset:18432\n\t" "s_waitcnt lgkmcnt(4)\n\t"
    "v_mfma_f32_16x16x32_bf16 v[96:99], a[176:179], a[64:67], v[96:99]\n\t" "ds_read_b64_tr_b16 a[200:201], %[vr23] offset:0\n\t" "global_load_dwordx4 a[100:103], %[vx], s[40:41] offset:1024 sc1\n\t"
    "v_mfma_f32_16x16x32_bf16 v[96:99], a[180:183], a[72:75], v[96:99]\n\t" "ds_read_b64_tr_b16 a[202:203], %[vr23] offset:2048\n\t" "s_add_u32 m0, %[sl0], 49152\n\t"
    "v_mfma_f32_16x16x32_bf16 v[100:103], a[176:179], a[68:71], v[100:103]\n\t" "ds_read_b64_tr_b16 a[204:205], %[vr15] offset:16384\n\t"
    "v_mfma_f32_16x16x32_bf16 v[100:103], a[180:183], a[76:79], v[100:103]\n\t" "ds_read_b64_tr_b16 a[206:207], %[vr15] offset:18432\n\t" "global_load_lds_dwordx4 %[vb0], %[sb]\n\t"
    "v_mfma_f32_16x16x32_bf16 v[104:107], a[184:187], a[64:67], v[104:107]\n\t" "s_waitcnt vmcnt(25)\n\t" "s_barrier\n\t" "ds_read_b64_tr_b16 a[176:177], %[vr0] offset:24576\n\t"
    "v_mfma_f32_16x16x32_bf16 v[104:107], a[188:191], a[72:75], v[104:107]\n\t" "ds_read_b64_tr_b16 a[178:179], %[vr0] offset:26624\n\t" "v_add_u32 %[vb0], %[qw], %[vb0]\n\t"
    "v_mfma_f32_16x16x32_bf16 v[108:111], a[184:187], a[68:71], v[108:111]\n\t" "ds_read_b64_tr_b16 a[180:181], %[vr0] offset:32768\n\t"
    "v_mfma_f32_16x16x32_bf16 v[108:111], a[188:191], a[76:79], v[108:111]\n\t" "ds_read_b64_tr_b16 a[182:183], %[vr0] offset:34816\n\t" "s_waitcnt lgkmcnt(4)\n\t"
    "v_mfma_f32_16x16x32_bf16 v[112:115], a[192:195], a[64:67], v[112:115]\n\t" "ds_read_b64_tr_b16 a[184:185], %[vr1] offset:24576\n\t"
    "global_load_dwordx4 a[104:107], %[vx], s[40:41] offset:2048 sc1\n\t" "v_mfma_f32_16x16x32_bf16 v[112:115], a[196:199], a[72:75], v[112:115]\n\t"
    "ds_read_b64_tr_b16 a[186:187], %[vr1] offset:26624\n\t" "v_mfma_f32_16x16x32_bf16 v[116:119], a[192:195], a[68:71], v[116:119]\n\t" "ds_read_b64_tr_b16 a[188:189], %[vr1] offset:32768\n\t"
    "v_mfma_f32_16x16x32_bf16 v[116:119], a[196:199], a[76:79], v[116:119]\n\t" "ds_read_b64_tr_b16 a[190:191], %[vr1] offset:34816\n\t"
    "global_load_dwordx4 a[108:111], %[vx], s[40:41] offset:3072 sc1\n\t" "v_mfma_f32_16x16x32_bf16 v[120:123], a[200:203], a[64:67], v[120:123]\n\t"
    "ds_read_b64_tr_b16 a[192:193], %[vr2] offset:24576\n\t" "v_mfma_f32_16x16x32_bf16 v[120:123], a[204:207], a[72:75], v[120:123]\n\t" "ds_read_b64_tr_b16 a[194:195], %[vr2] offset:26624\n\t"
    "s_add_u32 m0, %[sl0], 57344\n\t" "v_mfma_f32_16x16x32_bf16 v[124:127], a[200:203], a[68:71], v[124:127]\n\t" "ds_read_b64_tr_b16 a[196:197], %[vr2] offset:32768\n\t"
    "v_mfma_f32_16x16x32_bf16 v[124:127], a[204:207], a[76:79], v[124:127]\n\t" "ds_read_b64_tr_b16 a[198:199], %[vr2] offset:34816\n\t" "s_waitcnt lgkmcnt(4) vmcnt(21)\n\t"
    "v_mfma_f32_16x16x32_bf16 v[224:227], a[176:179], a[0:3], v[224:227]\n\t" "ds_read_b64_tr_b16 a[200:201], %[vr3] offset:24576\n\t" "global_load_lds_dwordx4 %[vb0], %[sb]\n\t"
    "v_mfma_f32_16x16x32_bf16 v[224:227], a[180:183], a[8:11], v[224:227]\n\t" "ds_read_b64_tr_b16 a[202:203], %[vr3] offset:26624\n\t" "v_add_u32_e64 %[vb0], %[pw], %[vb0]\n\t"
    "v_mfma_f32_16x16x32_bf16 v[240:243], a[176:179], a[4:7], v[240:243]\n\t" "ds_read_b64_tr_b16 a[204:205], %[vr3] offset:32768\n\t" "s_add_u32 s40, s40, 8192\n\t" "s_addc_u32 s41, s41, 0\n\t"
    "v_mfma_f32_16x16x32_bf16 v[240:243], a[180:183], a[12:15], v[240:243]\n\t" "ds_read_b64_tr_b16 a[206:207], %[vr3] offset:34816\n\t"
    "global_load_dwordx2 a[112:113], %[vo], s[40:41] offset:-4096 sc1\n\t" "v_mfma_f32_16x16x32_bf16 v[228:231], a[184:187], a[0:3], v[228:231]\n\t"
    "ds_read_b64_tr_b16 a[176:177], %[vr12] offset:24576\n\t" "v_mfma_f32_16x16x32_bf16 v[228:231], a[188:191], a[8:11], v[228:231]\n\t" "ds_read_b64_tr_b16 a[178:179], %[vr12] offset:26624\n\t"
    "global_load_dwordx2 a[114:115], %[vo], s[40:41] offset:-3072 sc1\n\t" "v_mfma_f32_16x16x32_bf16 v[244:247], a[184:187], a[4:7], v[244:247]\n\t"
    "ds_read_b64_tr_b16 a[180:181], %[vr12] offset:32768\n\t" "v_mfma_f32_16x16x32_bf16 v[244:247], a[188:191], a[12:15], v[244:247]\n\t" "ds_read_b64_tr_b16 a[182:183], %[vr12] offset:34816\n\t"
    "s_waitcnt lgkmcnt(4)\n\t" "v_mfma_f32_16x16x32_bf16 v[232:235], a[192:195], a[0:3], v[232:235]\n\t" "ds_read_b64_tr_b16 a[184:185], %[vr13] offset:24576\n\t"
    "global_load_dwordx2 a[116:117], %[vo], s[40:41] offset:-3584 sc1\n\t" "v_mfma_f32_16x16x32_bf16 v[232:235], a[196:199], a[8:11], v[232:235]\n\t"
    "ds_read_b64_tr_b16 a[186:187], %[vr13] offset:26624\n\t" "global_load_dwordx2 a[118:119], %[vo], s[40:41] offset:-2560 sc1\n\t"
    "v_mfma_f32_16x16x32_bf16 v[248:251], a[192:195], a[4:7], v[248:251]\n\t" "ds_read_b64_tr_b16 a[188:189], %[vr13] offset:32768\n\t" "s_add_u32 m0, %[sl0], 65536\n\t"
    "v_mfma_f32_16x16x32_bf16 v[248:251], a[196:199], a[12:15], v[248:251]\n\t" "ds_read_b64_tr_b16 a[190:191], %[vr13] offset:34816\n\t" "global_load_lds_dwordx4 %[vb0], %[sb]\n\t"
    "v_mfma_f32_16x16x32_bf16 v[236:239], a[200:203], a[0:3], v[236:239]\n\t" "ds_read_b64_tr_b16 a[192:193], %[vr14] offset:24576\n\t" "v_add_u32_e64 %[vb0], %[qw], %[vb0]\n\t"
    "v_mfma_f32_16x16x32_bf16 v[236:239], a[204:207], a[8:11], v[236:239]\n\t" "ds_read_b64_tr_b16 a[194:195], %[vr14] offset:26624\n\t"
    "v_mfma_f32_16x16x32_bf16 v[252:255], a[200:203], a[4:7], v[252:255]\n\t" "ds_read_b64_tr_b16 a[196:197], %[vr14] offset:32768\n\t"
    "v_mfma_f32_16x16x32_bf16 v[252:255], a[204:207], a[12:15], v[252:255]\n\t" "ds_read_b64_tr_b16 a[198:199], %[vr14] offset:34816\n\t" "s_waitcnt lgkmcnt(4)\n\t"
    "v_mfma_f32_16x16x32_bf16 v[96:99], a[176:179], a[0:3], v[96:99]\n\t" "ds_read_b64_tr_b16 a[200:201], %[vr15] offset:24576\n\t" "global_load_dwordx4 a[120:123], %[vx], s[40:41] offset:-2048 sc1\n\t"
    "v_mfma_f32_16x16x32_bf16 v[96:99], a[180:183], a[8:11], v[96:99]\n\t" "ds_read_b64_tr_b16 a[202:203], %[vr15] offset:26624\n\t"
    "v_mfma_f32_16x16x32_bf16 v[100:103], a[176:179], a[4:7], v[100:103]\n\t" "ds_read_b64_tr_b16 a[204:205], %[vr15] offset:32768\n\t"
    "v_mfma_f32_16x16x32_bf16 v[100:103], a[180:183], a[12:15], v[100:103]\n\t" "ds_read_b64_tr_b16 a[206:207], %[vr15] offset:34816\n\t"
    "global_load_dwordx4 a[124:127], %[vx], s[40:41] offset:-1024 sc1\n\t" "v_mfma_f32_16x16x32_bf16 v[104:107], a[184:187], a[0:3], v[104:107]\n\t" "s_waitcnt vmcnt(27)\n\t" "s_barrier\n\t"
    "ds_read_b64_tr_b16 a[176:177], %[vr0] offset:40960\n\t" "v_mfma_f32_16x16x32_bf16 v[104:107], a[188:191], a[8:11], v[104:107]\n\t" "ds_read_b64_tr_b16 a[178:179], %[vr0] offset:43008\n\t"
    "s_add_u32 m0, %[sl0], 73728\n\t" "v_mfma_f32_16x16x32_bf16 v[108:111], a[184:187], a[4:7], v[108:111]\n\t" "ds_read_b64_tr_b16 a[180:181], %[vr8] offset:8192\n\t"
    "v_mfma_f32_16x16x32_bf16 v[108:111], a[188:191], a[12:15], v[108:111]\n\t" "ds_read_b64_tr_b16 a[182:183], %[vr8] offset:10240\n\t" "s_waitcnt lgkmcnt(4)\n\t"
    "v_mfma_f32_16x16x32_bf16 v[112:115], a[192:195], a[0:3], v[112:115]\n\t" "ds_read_b64_tr_b16 a[184:185], %[vr1] offset:40960\n\t" "global_load_lds_dwordx4 %[vb0], %[sb]\n\t"
    "v_mfma_f32_16x16x32_bf16 v[112:115], a[196:199], a[8:11], v[112:115]\n\t" "ds_read_b64_tr_b16 a[186:187], %[vr1] offset:43008\n\t" "v_add_u32_e64 %[vb0], %[pw], %[vb0]\n\t"
    "v_mfma_f32_16x16x32_bf16 v[116:119], a[192:195], a[4:7], v[116:119]\n\t" "ds_read_b64_tr_b16 a[188:189], %[vr9] offset:8192\n\t"
    "v_mfma_f32_16x16x32_bf16 v[116:119], a[196:199], a[12:15], v[116:119]\n\t" "ds_read_b64_tr_b16 a[190:191], %[vr9] offset:10240\n\t" "global_load_dwordx4 a[128:131], %[vx], s[40:41] offset:0 sc1\n\t"
    "v_mfma_f32_16x16x32_bf16 v[120:123], a[200:203], a[0:3], v[120:123]\n\t" "ds_read_b64_tr_b16 a[192:193], %[vr2] offset:40960\n\t"
    "v_mfma_f32_16x16x32_bf16 v[120:123], a[204:207], a[8:11], v[120:123]\n\t" "ds_read_b64_tr_b16 a[194:195], %[vr2] offset:43008\n\t"
    "v_mfma_f32_16x16x32_bf16 v[124:127], a[200:203], a[4:7], v[124:127]\n\t" "ds_read_b64_tr_b16 a[196:197], %[vr10] offset:8192\n\t"
    "v_mfma_f32_16x16x32_bf16 v[124:127], a[204:207], a[12:15], v[124:127]\n\t" "ds_read_b64_tr_b16 a[198:199], %[vr10] offset:10240\n\t" "s_waitcnt lgkmcnt(4) vmcnt(22)\n\t"
    "v_mfma_f32_16x16x32_bf16 v[224:227], a[176:179], a[16:19], v[224:227]\n\t" "ds_read_b64_tr_b16 a[200:201], %[vr3] offset:40960\n\t"
    "global_load_dwordx4 a[132:135], %[vx], s[40:41] offset:1024 sc1\n\t" "v_mfma_f32_16x16x32_bf16 v[224:227], a[180:183], a[24:27], v[224:227]\n\t"
    "ds_read_b64_tr_b16 a[202:203], %[vr3] offset:43008\n\t" "s_add_u32 m0, %[sl0], 81920\n\t" "v_mfma_f32_16x16x32_bf16 v[240:243], a[176:179], a[20:23], v[240:243]\n\t"
    "ds_read_b64_tr_b16 a[204:205], %[vr11] offset:8192\n\t" "v_mfma_f32_16x16x32_bf16 v[240:243], a[180:183], a[28:31], v[240:243]\n\t" "ds_read_b64_tr_b16 a[206:207], %[vr11] offset:10240\n\t"
    "global_load_lds_dwordx4 %[vb0], %[sb]\n\t" "v_mfma_f32_16x16x32_bf16 v[228:231], a[184:187], a[16:19], v[228:231]\n\t" "ds_read_b64_tr_b16 a[176:177], %[vr12] offset:40960\n\t"
    "v_add_u32 %[vb0], 0x10000, %[vb0]\n\t" "v_mfma_f32_16x16x32_bf16 v[228:231], a[188:191], a[24:27], v[228:231]\n\t" "ds_read_b64_tr_b16 a[178:179], %[vr12] offset:43008\n\t"
    "v_mfma_f32_16x16x32_bf16 v[244:247], a[184:187], a[20:23], v[244:247]\n\t" "ds_read_b64_tr_b16 a[180:181], %[vr20] offset:8192\n\t"
    "v_mfma_f32_16x16x32_bf16 v[244:247], a[188:191], a[28:31], v[244:247]\n\t" "ds_read_b64_tr_b16 a[182:183], %[vr20] offset:10240\n\t" "s_waitcnt lgkmcnt(4)\n\t"
    "v_mfma_f32_16x16x32_bf16 v[232:235], a[192:195], a[16:19], v[232:235]\n\t" "ds_read_b64_tr_b16 a[184:185], %[vr13] offset:40960\n\t"
    "global_load_dwordx4 a[136:139], %[vx], s[40:41] offset:2048 sc1\n\t" "v_mfma_f32_16x16x32_bf16 v[232:235], a[196:199], a[24:27], v[232:235]\n\t"
    "ds_read_b64_tr_b16 a[186:187], %[vr13] offset:43008\n\t" "v_mfma_f32_16x16x32_bf16 v[248:251], a[192:195], a[20:23], v[248:251]\n\t" "ds_read_b64_tr_b16 a[188:189], %[vr21] offset:8192\n\t"
    "v_mfma_f32_16x16x32_bf16 v[248:251], a[196:199], a[28:31], v[248:251]\n\t" "ds_read_b64_tr_b16 a[190:191], %[vr21] offset:10240\n\t"
    "global_load_dwordx4 a[140:143], %[vx], s[40:41] offset:3072 sc1\n\t" "v_mfma_f32_16x16x32_bf16 v[236:239], a[200:203], a[16:19], v[236:239]\n\t"
    "ds_read_b64_tr_b16 a[192:193], %[vr14] offset:40960\n\t" "v_mfma_f32_16x16x32_bf16 v[236:239], a[204:207], a[24:27], v[236:239]\n\t" "ds_read_b64_tr_b16 a[194:195], %[vr14] offset:43008\n\t"
    "s_add_u32 m0, %[sl0], 90112\n\t" "v_mfma_f32_16x16x32_bf16 v[252:255], a[200:203], a[20:23], v[252:255]\n\t" "ds_read_b64_tr_b16 a[196:197], %[vr22] offset:8192\n\t"
    "v_mfma_f32_16x16x32_bf16 v[252:255], a[204:207], a[28:31], v[252:255]\n\t" "ds_read_b64_tr_b16 a[198:199], %[vr22] offset:10240\n\t" "s_waitcnt lgkmcnt(4)\n\t"
    "v_mfma_f32_16x16x32_bf16 v[96:99], a[176:179], a[16:19], v[96:99]\n\t" "ds_read_b64_tr_b16 a[200:201], %[vr15] offset:40960\n\t" "global_load_lds_dwordx4 %[vb0], %[sb]\n\t"
    "v_mfma_f32_16x16x32_bf16 v[96:99], a[180:183], a[24:27], v[96:99]\n\t" "ds_read_b64_tr_b16 a[202:203], %[vr15] offset:43008\n\t" "v_add_u32 %[vb0], %[qw], %[vb0]\n\t"
    "v_mfma_f32_16x16x32_bf16 v[100:103], a[176:179], a[20:23], v[100:103]\n\t" "ds_read_b64_tr_b16 a[204:205], %[vr23] offset:8192\n\t" "s_add_u32 s40, s40, 8192\n\t" "s_addc_u32 s41, s41, 0\n\t"
    "v_mfma_f32_16x16x32_bf16 v[100:103], a[180:183], a[28:31], v[100:103]\n\t" "ds_read_b64_tr_b16 a[206:207], %[vr23] offset:10240\n\t"
    "global_load_dwordx4 a[144:147], %[vx], s[40:41] offset:-4096 sc1\n\t" "v_mfma_f32_16x16x32_bf16 v[104:107], a[184:187], a[16:19], v[104:107]\n\t" "s_waitcnt vmcnt(27)\n\t" "s_barrier\n\t"
    "ds_read_b64_tr_b16 a[176:177], %[vr8] offset:16384\n\t" "v_mfma_f32_16x16x32_bf16 v[104:107], a[188:191], a[24:27], v[104:107]\n\t" "ds_read_b64_tr_b16 a[178:179], %[vr8] offset:18432\n\t"
    "v_mfma_f32_16x16x32_bf16 v[108:111], a[184:187], a[20:23], v[108:111]\n\t" "ds_read_b64_tr_b16 a[180:181], %[vr8] offset:24576\n\t"
    "v_mfma_f32_16x16x32_bf16 v[108:111], a[188:191], a[28:31], v[108:111]\n\t" "ds_read_b64_tr_b16 a[182:183], %[vr8] offset:26624\n\t" "s_waitcnt lgkmcnt(4)\n\t"
    "v_mfma_f32_16x16x32_bf16 v[112:115], a[192:195], a[16:19], v[112:115]\n\t" "ds_read_b64_tr_b16 a[184:185], %[vr9] offset:16384\n\t"
    "global_load_dwordx4 a[148:151], %[vx], s[40:41] offset:-3072 sc1\n\t" "v_mfma_f32_16x16x32_bf16 v[112:115], a[196:199], a[24:27], v[112:115]\n\t"
    "ds_read_b64_tr_b16 a[186:187], %[vr9] offset:18432\n\t" "s_add_u32 m0, %[sl0], 98304\n\t" "v_mfma_f32_16x16x32_bf16 v[116:119], a[192:195], a[20:23], v[116:119]\n\t"
    "ds_read_b64_tr_b16 a[188:189], %[vr9] offset:24576\n\t" "v_mfma_f32_16x16x32_bf16 v[116:119], a[196:199], a[28:31], v[116:119]\n\t" "ds_read_b64_tr_b16 a[190:191], %[vr9] offset:26624\n\t"
    "global_load_lds_dwordx4 %[vb0], %[sb]\n\t" "v_mfma_f32_16x16x32_bf16 v[120:123], a[200:203], a[16:19], v[120:123]\n\t" "ds_read_b64_tr_b16 a[192:193], %[vr10] offset:16384\n\t"
    "v_add_u32_e64 %[vb0], %[pw], %[vb0]\n\t" "v_mfma_f32_16x16x32_bf16 v[120:123], a[204:207], a[24:27], v[120:123]\n\t" "ds_read_b64_tr_b16 a[194:195], %[vr10] offset:18432\n\t"
    "v_mfma_f32_16x16x32_bf16 v[124:127], a[200:203], a[20:23], v[124:127]\n\t" "ds_read_b64_tr_b16 a[196:197], %[vr10] offset:24576\n\t"
    "v_mfma_f32_16x16x32_bf16 v[124:127], a[204:207], a[28:31], v[124:127]\n\t" "ds_read_b64_tr_b16 a[198:199], %[vr10] offset:26624\n\t" "s_waitcnt lgkmcnt(4) vmcnt(24)\n\t"
    "v_mfma_f32_16x16x32_bf16 v[224:227], a[176:179], a[80:83], v[224:227]\n\t" "ds_read_b64_tr_b16 a[200:201], %[vr11] offset:16384\n\t"
    "global_load_dwordx2 a[152:153], %[vo], s[40:41] offset:-2048 sc1\n\t" "v_mfma_f32_16x16x32_bf16 v[224:227], a[180:183], a[88:91], v[224:227]\n\t"
    "ds_read_b64_tr_b16 a[202:203], %[vr11] offset:18432\n\t" "global_load_dwordx2 a[154:155], %[vo], s[40:41] offset:-1024 sc1\n\t"
    "v_mfma_f32_16x16x32_bf16 v[240:243], a[176:179], a[84:87], v[240:243]\n\t" "ds_read_b64_tr_b16 a[204:205], %[vr11] offset:24576\n\t"
    "global_load_dwordx2 a[156:157], %[vo], s[40:41] offset:-1536 sc1\n\t" "v_mfma_f32_16x16x32_bf16 v[240:243], a[180:183], a[92:95], v[240:243]\n\t"
    "ds_read_b64_tr_b16 a[206:207], %[vr11] offset:26624\n\t" "global_load_dwordx2 a[158:159], %[vo], s[40:41] offset:-512 sc1\n\t"
    "v_mfma_f32_16x16x32_bf16 v[228:231], a[184:187], a[80:83], v[228:231]\n\t" "s_waitcnt vmcnt(27)\n\t" "s_barrier\n\t" "ds_read_b64_tr_b16 a[176:177], %[vr0] offset:0\n\t"
    "v_mfma_f32_16x16x32_bf16 v[228:231], a[188:191], a[88:91], v[228:231]\n\t" "ds_read_b64_tr_b16 a[178:179], %[vr0] offset:2048\n\t" "s_add_u32 m0, %[sl0], 106496\n\t"
    "v_mfma_f32_16x16x32_bf16 v[244:247], a[184:187], a[84:87], v[244:247]\n\t" "ds_read_b64_tr_b16 a[180:181], %[vr0] offset:8192\n\t" "global_load_lds_dwordx4 %[vb0], %[sb]\n\t"
    "v_mfma_f32_16x16x32_bf16 v[244:247], a[188:191], a[92:95], v[244:247]\n\t" "ds_read_b64_tr_b16 a[182:183], %[vr0] offset:10240\n\t" "s_waitcnt lgkmcnt(4)\n\t"
    "v_mfma_f32_16x16x32_bf16 v[232:235], a[192:195], a[80:83], v[232:235]\n\t" "ds_read_b64_tr_b16 a[184:185], %[vr1] offset:0\n\t" "global_load_dwordx4 a[160:163], %[vx], s[40:41] offset:0 sc1\n\t"
    "v_mfma_f32_16x16x32_bf16 v[232:235], a[196:199], a[88:91], v[232:235]\n\t" "ds_read_b64_tr_b16 a[186:187], %[vr1] offset:2048\n\t"
    "v_mfma_f32_16x16x32_bf16 v[248:251], a[192:195], a[84:87], v[248:251]\n\t" "ds_read_b64_tr_b16 a[188:189], %[vr1] offset:8192\n\t"
    "global_load_dwordx4 a[164:167], %[vx], s[40:41] offset:1024 sc1\n\t" "v_mfma_f32_16x16x32_bf16 v[248:251], a[196:199], a[92:95], v[248:251]\n\t"
    "ds_read_b64_tr_b16 a[190:191], %[vr1] offset:10240\n\t" "s_add_u32 m0, %[sl1], 147456\n\t" "v_mfma_f32_16x16x32_bf16 v[236:239], a[200:203], a[80:83], v[236:239]\n\t"
    "ds_read_b64_tr_b16 a[192:193], %[vr2] offset:0\n\t" "v_mfma_f32_16x16x32_bf16 v[236:239], a[204:207], a[88:91], v[236:239]\n\t" "ds_read_b64_tr_b16 a[194:195], %[vr2] offset:2048\n\t"
    "global_load_lds_dwordx4 %[vb1], %[sb]\n\t" "v_mfma_f32_16x16x32_bf16 v[252:255], a[200:203], a[84:87], v[252:255]\n\t" "ds_read_b64_tr_b16 a[196:197], %[vr2] offset:8192\n\t"
    "v_add_u32_e64 %[vb1], %[pw], %[vb1]\n\t" "v_mfma_f32_16x16x32_bf16 v[252:255], a[204:207], a[92:95], v[252:255]\n\t" "ds_read_b64_tr_b16 a[198:199], %[vr2] offset:10240\n\t"
    "s_waitcnt lgkmcnt(4) vmcnt(26)\n\t" "v_mfma_f32_16x16x32_bf16 v[224:227], a[176:179], a[96:99], v[224:227]\n\t" "ds_read_b64_tr_b16 a[200:201], %[vr3] offset:0\n\t"
    "global_load_dwordx4 a[168:171], %[vx], s[40:41] offset:2048 sc1\n\t" "v_mfma_f32_16x16x32_bf16 v[224:227], a[180:183], a[104:107], v[224:227]\n\t"
    "ds_read_b64_tr_b16 a[202:203], %[vr3] offset:2048\n\t" "v_mfma_f32_16x16x32_bf16 v[240:243], a[176:179], a[100:103], v[240:243]\n\t" "ds_read_b64_tr_b16 a[204:205], %[vr3] offset:8192\n\t"
    "global_load_dwordx4 a[172:175], %[vx], s[40:41] offset:3072 sc1\n\t" "v_mfma_f32_16x16x32_bf16 v[240:243], a[180:183], a[108:111], v[240:243]\n\t"
    "ds_read_b64_tr_b16 a[206:207], %[vr3] offset:10240\n\t" "s_add_u32 m0, %[sl1], 155648\n\t" "v_mfma_f32_16x16x32_bf16 v[228:231], a[184:187], a[96:99], v[228:231]\n\t" "s_waitcnt vmcnt(27)\n\t"
    "s_barrier\n\t" "ds_read_b64_tr_b16 a[176:177], %[vr0] offset:49152\n\t" "v_mfma_f32_16x16x32_bf16 v[228:231], a[188:191], a[104:107], v[228:231]\n\t"
    "ds_read_b64_tr_b16 a[178:179], %[vr0] offset:51200\n\t" "global_load_lds_dwordx4 %[vb1], %[sb]\n\t" "v_mfma_f32_16x16x32_bf16 v[244:247], a[184:187], a[100:103], v[244:247]\n\t"
    "ds_read_b64_tr_b16 a[180:181], %[vr0] offset:57344\n\t" "v_add_u32 %[vb1], %[qw], %[vb1]\n\t" "v_mfma_f32_16x16x32_bf16 v[244:247], a[188:191], a[108:111], v[244:247]\n\t"
    "ds_read_b64_tr_b16 a[182:183], %[vr0] offset:59392\n\t" "s_waitcnt lgkmcnt(4)\n\t" "v_mfma_f32_16x16x32_bf16 v[232:235], a[192:195], a[96:99], v[232:235]\n\t"
    "ds_read_b64_tr_b16 a[184:185], %[vr1] offset:49152\n\t" "s_add_u32 m0, %[sl1], 0\n\t" "v_mfma_f32_16x16x32_bf16 v[232:235], a[196:199], a[104:107], v[232:235]\n\t"
    "ds_read_b64_tr_b16 a[186:187], %[vr1] offset:51200\n\t" "global_load_lds_dwordx4 %[vb1], %[sb]\n\t" "v_mfma_f32_16x16x32_bf16 v[248:251], a[192:195], a[100:103], v[248:251]\n\t"
    "ds_read_b64_tr_b16 a[188:189], %[vr1] offset:57344\n\t" "v_add_u32_e64 %[vb1], %[pw], %[vb1]\n\t" "v_mfma_f32_16x16x32_bf16 v[248:251], a[196:199], a[108:111], v[248:251]\n\t"
    "ds_read_b64_tr_b16 a[190:191], %[vr1] offset:59392\n\t" "s_add_u32 m0, %[sl1], 8192\n\t" "v_mfma_f32_16x16x32_bf16 v[236:239], a[200:203], a[96:99], v[236:239]\n\t"
    "ds_read_b64_tr_b16 a[192:193], %[vr2] offset:49152\n\t" "global_load_lds_dwordx4 %[vb1], %[sb]\n\t" "v_mfma_f32_16x16x32_bf16 v[236:239], a[204:207], a[104:107], v[236:239]\n\t"
    "ds_read_b64_tr_b16 a[194:195], %[vr2] offset:51200\n\t" "v_add_u32 %[vb1], 0x10000, %[vb1]\n\t" "v_mfma_f32_16x16x32_bf16 v[252:255], a[200:203], a[100:103], v[252:255]\n\t"
    "ds_read_b64_tr_b16 a[196:197], %[vr2] offset:57344\n\t" "s_add_u32 m0, %[sl1], 49152\n\t" "v_mfma_f32_16x16x32_bf16 v[252:255], a[204:207], a[108:111], v[252:255]\n\t"
    "ds_read_b64_tr_b16 a[198:199], %[vr2] offset:59392\n\t" "s_waitcnt lgkmcnt(4) vmcnt(23)\n\t" "v_mfma_f32_16x16x32_bf16 v[224:227], a[176:179], a[112:115], v[224:227]\n\t"
    "ds_read_b64_tr_b16 a[200:201], %[vr3] offset:49152\n\t" "global_load_lds_dwordx4 %[vb1], %[sb]\n\t" "v_mfma_f32_16x16x32_bf16 v[224:227], a[180:183], a[120:123], v[224:227]\n\t"
    "ds_read_b64_tr_b16 a[202:203], %[vr3] offset:51200\n\t" "v_add_u32 %[vb1], %[qw], %[vb1]\n\t" "v_mfma_f32_16x16x32_bf16 v[240:243], a[176:179], a[116:119], v[240:243]\n\t"
    "ds_read_b64_tr_b16 a[204:205], %[vr3] offset:57344\n\t" "s_add_u32 m0, %[sl1], 57344\n\t" "v_mfma_f32_16x16x32_bf16 v[240:243], a[180:183], a[124:127], v[240:243]\n\t"
    "ds_read_b64_tr_b16 a[206:207], %[vr3] offset:59392\n\t" "global_load_lds_dwordx4 %[vb1], %[sb]\n\t" "v_mfma_f32_16x16x32_bf16 v[228:231], a[184:187], a[112:115], v[228:231]\n\t"
    "s_waitcnt vmcnt(24)\n\t" "s_barrier\n\t" "ds_read_b64_tr_b16 a[176:177], %[vr4] offset:0\n\t" "v_mfma_f32_16x16x32_bf16 v[228:231], a[188:191], a[120:123], v[228:231]\n\t"
    "ds_read_b64_tr_b16 a[178:179], %[vr4] offset:2048\n\t" "v_add_u32_e64 %[vb1], %[pw], %[vb1]\n\t" "v_mfma_f32_16x16x32_bf16 v[244:247], a[184:187], a[116:119], v[244:247]\n\t"
    "ds_read_b64_tr_b16 a[180:181], %[vr4] offset:8192\n\t" "s_add_u32 m0, %[sl1], 65536\n\t" "v_mfma_f32_16x16x32_bf16 v[244:247], a[188:191], a[124:127], v[244:247]\n\t"
    "ds_read_b64_tr_b16 a[182:183], %[vr4] offset:10240\n\t" "s_waitcnt lgkmcnt(4)\n\t" "v_mfma_f32_16x16x32_bf16 v[232:235], a[192:195], a[112:115], v[232:235]\n\t"
    "ds_read_b64_tr_b16 a[184:185], %[vr5] offset:0\n\t" "global_load_lds_dwordx4 %[vb1], %[sb]\n\t" "v_mfma_f32_16x16x32_bf16 v[232:235], a[196:199], a[120:123], v[232:235]\n\t"
    "ds_read_b64_tr_b16 a[186:187], %[vr5] offset:2048\n\t" "v_add_u32 %[vb1], %[qw], %[vb1]\n\t" "v_mfma_f32_16x16x32_bf16 v[248:251], a[192:195], a[116:119], v[248:251]\n\t"
    "ds_read_b64_tr_b16 a[188:189], %[vr5] offset:8192\n\t" "s_add_u32 m0, %[sl1], 73728\n\t" "v_mfma_f32_16x16x32_bf16 v[248:251], a[196:199], a[124:127], v[248:251]\n\t"
    "ds_read_b64_tr_b16 a[190:191], %[vr5] offset:10240\n\t" "global_load_lds_dwordx4 %[vb1], %[sb]\n\t" "v_mfma_f32_16x16x32_bf16 v[236:239], a[200:203], a[112:115], v[236:239]\n\t"
    "ds_read_b64_tr_b16 a[192:193], %[vr6] offset:0\n\t" "v_add_u32_e64 %[vb1], %[pw], %[vb1]\n\t" "v_mfma_f32_16x16x32_bf16 v[236:239], a[204:207], a[120:123], v[236:239]\n\t"
    "ds_read_b64_tr_b16 a[194:195], %[vr6] offset:2048\n\t" "v_mfma_f32_16x16x32_bf16 v[252:255], a[200:203], a[116:119], v[252:255]\n\t" "ds_read_b64_tr_b16 a[196:197], %[vr6] offset:8192\n\t"
    "v_mfma_f32_16x16x32_bf16 v[252:255], a[204:207], a[124:127], v[252:255]\n\t" "ds_read_b64_tr_b16 a[198:199], %[vr6] offset:10240\n\t" "s_waitcnt lgkmcnt(4) vmcnt(21)\n\t"
    "v_mfma_f32_16x16x32_bf16 v[224:227], a[176:179], a[128:131], v[224:227]\n\t" "ds_read_b64_tr_b16 a[200:201], %[vr7] offset:0\n\t"
    "v_mfma_f32_16x16x32_bf16 v[224:227], a[180:183], a[136:139], v[224:227]\n\t" "ds_read_b64_tr_b16 a[202:203], %[vr7] offset:2048\n\t"
    "v_mfma_f32_16x16x32_bf16 v[240:243], a[176:179], a[132:135], v[240:243]\n\t" "ds_read_b64_tr_b16 a[204:205], %[vr7] offset:8192\n\t"
    "v_mfma_f32_16x16x32_bf16 v[240:243], a[180:183], a[140:143], v[240:243]\n\t" "ds_read_b64_tr_b16 a[206:207], %[vr7] offset:10240\n\t"
    "v_mfma_f32_16x16x32_bf16 v[228:231], a[184:187], a[128:131], v[228:231]\n\t" "s_waitcnt vmcnt(20)\n\t" "s_barrier\n\t" "ds_read_b64_tr_b16 a[176:177], %[vr4] offset:16384\n\t"
    "v_mfma_f32_16x16x32_bf16 v[228:231], a[188:191], a[136:139], v[228:231]\n\t" "ds_read_b64_tr_b16 a[178:179], %[vr4] offset:18432\n\t"
    "v_mfma_f32_16x16x32_bf16 v[244:247], a[184:187], a[132:135], v[244:247]\n\t" "ds_read_b64_tr_b16 a[180:181], %[vr4] offset:24576\n\t"
    "v_mfma_f32_16x16x32_bf16 v[244:247], a[188:191], a[140:143], v[244:247]\n\t" "ds_read_b64_tr_b16 a[182:183], %[vr4] offset:26624\n\t" "s_waitcnt lgkmcnt(4)\n\t"
    "v_mfma_f32_16x16x32_bf16 v[232:235], a[192:195], a[128:131], v[232:235]\n\t" "ds_read_b64_tr_b16 a[184:185], %[vr5] offset:16384\n\t"
    "v_mfma_f32_16x16x32_bf16 v[232:235], a[196:199], a[136:139], v[232:235]\n\t" "ds_read_b64_tr_b16 a[186:187], %[vr5] offset:18432\n\t"
    "v_mfma_f32_16x16x32_bf16 v[248:251], a[192:195], a[132:135], v[248:251]\n\t" "ds_read_b64_tr_b16 a[188:189], %[vr5] offset:24576\n\t"
    "v_mfma_f32_16x16x32_bf16 v[248:251], a[196:199], a[140:143], v[248:251]\n\t" "ds_read_b64_tr_b16 a[190:191], %[vr5] offset:26624\n\t"
    "v_mfma_f32_16x16x32_bf16 v[236:239], a[200:203], a[128:131], v[236:239]\n\t" "ds_read_b64_tr_b16 a[192:193], %[vr6] offset:16384\n\t"
    "v_mfma_f32_16x16x32_bf16 v[236:239], a[204:207], a[136:139], v[236:239]\n\t" "ds_read_b64_tr_b16 a[194:195], %[vr6] offset:18432\n\t"
    "v_mfma_f32_16x16x32_bf16 v[252:255], a[200:203], a[132:135], v[252:255]\n\t" "ds_read_b64_tr_b16 a[196:197], %[vr6] offset:24576\n\t"
    "v_mfma_f32_16x16x32_bf16 v[252:255], a[204:207], a[140:143], v[252:255]\n\t" "ds_read_b64_tr_b16 a[198:199], %[vr6] offset:26624\n\t" "s_waitcnt lgkmcnt(4) vmcnt(13)\n\t"
    "v_mfma_f32_16x16x32_bf16 v[224:227], a[176:179], a[144:147], v[224:227]\n\t" "ds_read_b64_tr_b16 a[200:201], %[vr7] offset:16384\n\t"
    "v_mfma_f32_16x16x32_bf16 v[224:227], a[180:183], a[152:155], v[224:227]\n\t" "ds_read_b64_tr_b16 a[202:203], %[vr7] offset:18432\n\t"
    "v_mfma_f32_16x16x32_bf16 v[240:243], a[176:179], a[148:151], v[240:243]\n\t" "ds_read_b64_tr_b16 a[204:205], %[vr7] offset:24576\n\t"
    "v_mfma_f32_16x16x32_bf16 v[240:243], a[180:183], a[156:159], v[240:243]\n\t" "ds_read_b64_tr_b16 a[206:207], %[vr7] offset:26624\n\t"
    "v_mfma_f32_16x16x32_bf16 v[228:231], a[184:187], a[144:147], v[228:231]\n\t" "s_waitcnt vmcnt(12)\n\t" "s_barrier\n\t" "ds_read_b64_tr_b16 a[176:177], %[vr4] offset:32768\n\t"
    "v_mfma_f32_16x16x32_bf16 v[228:231], a[188:191], a[152:155], v[228:231]\n\t" "ds_read_b64_tr_b16 a[178:179], %[vr4] offset:34816\n\t"
    "v_mfma_f32_16x16x32_bf16 v[244:247], a[184:187], a[148:151], v[244:247]\n\t" "ds_read_b64_tr_b16 a[180:181], %[vr4] offset:40960\n\t"
    "v_mfma_f32_16x16x32_bf16 v[244:247], a[188:191], a[156:159], v[244:247]\n\t" "ds_read_b64_tr_b16 a[182:183], %[vr4] offset:43008\n\t" "s_waitcnt lgkmcnt(4)\n\t"
    "v_mfma_f32_16x16x32_bf16 v[232:235], a[192:195], a[144:147], v[232:235]\n\t" "ds_read_b64_tr_b16 a[184:185], %[vr5] offset:32768\n\t"
    "v_mfma_f32_16x16x32_bf16 v[232:235], a[196:199], a[152:155], v[232:235]\n\t" "ds_read_b64_tr_b16 a[186:187], %[vr5] offset:34816\n\t"
    "v_mfma_f32_16x16x32_bf16 v[248:251], a[192:195], a[148:151], v[248:251]\n\t" "ds_read_b64_tr_b16 a[188:189], %[vr5] offset:40960\n\t"
    "v_mfma_f32_16x16x32_bf16 v[248:251], a[196:199], a[156:159], v[248:251]\n\t" "ds_read_b64_tr_b16 a[190:191], %[vr5] offset:43008\n\t"
    "v_mfma_f32_16x16x32_bf16 v[236:239], a[200:203], a[144:147], v[236:239]\n\t" "ds_read_b64_tr_b16 a[192:193], %[vr6] offset:32768\n\t"
    "v_mfma_f32_16x16x32_bf16 v[236:239], a[204:207], a[152:155], v[236:239]\n\t" "ds_read_b64_tr_b16 a[194:195], %[vr6] offset:34816\n\t"
    "v_mfma_f32_16x16x32_bf16 v[252:255], a[200:203], a[148:151], v[252:255]\n\t" "ds_read_b64_tr_b16 a[196:197], %[vr6] offset:40960\n\t"
    "v_mfma_f32_16x16x32_bf16 v[252:255], a[204:207], a[156:159], v[252:255]\n\t" "ds_read_b64_tr_b16 a[198:199], %[vr6] offset:43008\n\t" "s_waitcnt lgkmcnt(4) vmcnt(7)\n\t"
    "v_mfma_f32_16x16x32_bf16 v[224:227], a[176:179], a[160:163], v[224:227]\n\t" "ds_read_b64_tr_b16 a[200:201], %[vr7] offset:32768\n\t"
    "v_mfma_f32_16x16x32_bf16 v[224:227], a[180:183], a[168:171], v[224:227]\n\t" "ds_read_b64_tr_b16 a[202:203], %[vr7] offset:34816\n\t"
    "v_mfma_f32_16x16x32_bf16 v[240:243], a[176:179], a[164:167], v[240:243]\n\t" "ds_read_b64_tr_b16 a[204:205], %[vr7] offset:40960\n\t"
    "v_mfma_f32_16x16x32_bf16 v[240:243], a[180:183], a[172:175], v[240:243]\n\t" "ds_read_b64_tr_b16 a[206:207], %[vr7] offset:43008\n\t"
    "v_mfma_f32_16x16x32_bf16 v[228:231], a[184:187], a[160:163], v[228:231]\n\t" "s_waitcnt vmcnt(6)\n\t" "s_barrier\n\t" "ds_read_b64_tr_b16 a[176:177], %[vr20] offset:16384\n\t"
    "v_mfma_f32_16x16x32_bf16 v[228:231], a[188:191], a[168:171], v[228:231]\n\t" "ds_read_b64_tr_b16 a[178:179], %[vr20] offset:18432\n\t"
    "v_mfma_f32_16x16x32_bf16 v[244:247], a[184:187], a[164:167], v[244:247]\n\t" "ds_read_b64_tr_b16 a[180:181], %[vr20] offset:24576\n\t"
    "v_mfma_f32_16x16x32_bf16 v[244:247], a[188:191], a[172:175], v[244:247]\n\t" "ds_read_b64_tr_b16 a[182:183], %[vr20] offset:26624\n\t" "s_waitcnt lgkmcnt(4)\n\t"
    "v_mfma_f32_16x16x32_bf16 v[232:235], a[192:195], a[160:163], v[232:235]\n\t" "ds_read_b64_tr_b16 a[184:185], %[vr21] offset:16384\n\t"
    "v_mfma_f32_16x16x32_bf16 v[232:235], a[196:199], a[168:171], v[232:235]\n\t" "ds_read_b64_tr_b16 a[186:187], %[vr21] offset:18432\n\t"
    "v_mfma_f32_16x16x32_bf16 v[248:251], a[192:195], a[164:167], v[248:251]\n\t" "ds_read_b64_tr_b16 a[188:189], %[vr21] offset:24576\n\t"
    "v_mfma_f32_16x16x32_bf16 v[248:251], a[196:199], a[172:175], v[248:251]\n\t" "ds_read_b64_tr_b16 a[190:191], %[vr21] offset:26624\n\t"
    "v_mfma_f32_16x16x32_bf16 v[236:239], a[200:203], a[160:163], v[236:239]\n\t" "ds_read_b64_tr_b16 a[192:193], %[vr22] offset:16384\n\t"
    "v_mfma_f32_16x16x32_bf16 v[236:239], a[204:207], a[168:171], v[236:239]\n\t" "ds_read_b64_tr_b16 a[194:195], %[vr22] offset:18432\n\t"
    "v_mfma_f32_16x16x32_bf16 v[252:255], a[200:203], a[164:167], v[252:255]\n\t" "ds_read_b64_tr_b16 a[196:197], %[vr22] offset:24576\n\t"
    "v_mfma_f32_16x16x32_bf16 v[252:255], a[204:207], a[172:175], v[252:255]\n\t" "ds_read_b64_tr_b16 a[198:199], %[vr22] offset:26624\n\t" "s_waitcnt lgkmcnt(4)\n\t"
    "v_mfma_f32_16x16x32_bf16 v[96:99], a[176:179], a[80:83], v[96:99]\n\t" "ds_read_b64_tr_b16 a[200:201], %[vr23] offset:16384\n\t"
    "v_mfma_f32_16x16x32_bf16 v[96:99], a[180:183], a[88:91], v[96:99]\n\t" "ds_read_b64_tr_b16 a[202:203], %[vr23] offset:18432\n\t"
    "v_mfma_f32_16x16x32_bf16 v[100:103], a[176:179], a[84:87], v[100:103]\n\t" "ds_read_b64_tr_b16 a[204:205], %[vr23] offset:24576\n\t"
    "v_mfma_f32_16x16x32_bf16 v[100:103], a[180:183], a[92:95], v[100:103]\n\t" "ds_read_b64_tr_b16 a[206:207], %[vr23] offset:26624\n\t" "v_cvt_pk_bf16_f32 v224, v224, v225\n\t"
    "v_cvt_pk_bf16_f32 v225, v226, v227\n\t" "v_add3_u32 v207, %[t32], v224, v225\n\t" "v_mfma_f32_16x16x32_bf16 v[104:107], a[184:187], a[80:83], v[104:107]\n\t" "s_waitcnt vmcnt(4)\n\t" "s_barrier\n\t"
    "ds_read_b64_tr_b16 a[176:177], %[vr12] offset:0\n\t" "v_cvt_pk_bf16_f32 v226, v228, v229\n\t" "v_cvt_pk_bf16_f32 v227, v230, v231\n\t" "v_add3_u32 v207, v207, v226, v227\n\t"
    "v_mfma_f32_16x16x32_bf16 v[104:107], a[188:191], a[88:91], v[104:107]\n\t" "ds_read_b64_tr_b16 a[178:179], %[vr12] offset:2048\n\t" "global_store_dwordx4 %[vx], v[224:227], %[sm] offset:0 sc1\n\t"
    "v_cvt_pk_bf16_f32 v232, v232, v233\n\t" "v_cvt_pk_bf16_f32 v233, v234, v235\n\t" "v_add3_u32 v207, v207, v232, v233\n\t" "v_mfma_f32_16x16x32_bf16 v[108:111], a[184:187], a[84:87], v[108:111]\n\t"
    "ds_read_b64_tr_b16 a[180:181], %[vr12] offset:8192\n\t" "v_cvt_pk_bf16_f32 v234, v236, v237\n\t" "v_cvt_pk_bf16_f32 v235, v238, v239\n\t" "v_add3_u32 v207, v207, v234, v235\n\t"
    "v_mfma_f32_16x16x32_bf16 v[108:111], a[188:191], a[92:95], v[108:111]\n\t" "ds_read_b64_tr_b16 a[182:183], %[vr12] offset:10240\n\t"
    "global_store_dwordx4 %[vx], v[232:235], %[sm] offset:1024 sc1\n\t" "v_cvt_pk_bf16_f32 v240, v240, v241\n\t" "v_cvt_pk_bf16_f32 v241, v242, v243\n\t" "v_add3_u32 v207, v207, v240, v241\n\t"
    "s_waitcnt lgkmcnt(4)\n\t" "v_mfma_f32_16x16x32_bf16 v[112:115], a[192:195], a[80:83], v[112:115]\n\t" "ds_read_b64_tr_b16 a[184:185], %[vr13] offset:0\n\t" "v_cvt_pk_bf16_f32 v242, v244, v245\n\t"
    "v_cvt_pk_bf16_f32 v243, v246, v247\n\t" "v_add3_u32 v207, v207, v242, v243\n\t" "v_mfma_f32_16x16x32_bf16 v[112:115], a[196:199], a[88:91], v[112:115]\n\t"
    "ds_read_b64_tr_b16 a[186:187], %[vr13] offset:2048\n\t" "global_store_dwordx4 %[vx], v[240:243], %[sm] offset:2048 sc1\n\t" "v_cvt_pk_bf16_f32 v248, v248, v249\n\t"
    "v_cvt_pk_bf16_f32 v249, v250, v251\n\t" "v_add3_u32 v207, v207, v248, v249\n\t" "v_mfma_f32_16x16x32_bf16 v[116:119], a[192:195], a[84:87], v[116:119]\n\t"
    "ds_read_b64_tr_b16 a[188:189], %[vr13] offset:8192\n\t" "v_cvt_pk_bf16_f32 v250, v252, v253\n\t" "v_cvt_pk_bf16_f32 v251, v254, v255\n\t" "v_add3_u32 v207, v207, v250, v251\n\t"
    "v_mfma_f32_16x16x32_bf16 v[116:119], a[196:199], a[92:95], v[116:119]\n\t" "ds_read_b64_tr_b16 a[190:191], %[vr13] offset:10240\n\t"
    "global_store_dwordx4 %[vx], v[248:251], %[sm] offset:3072 sc1\n\t" "v_mfma_f32_16x16x32_bf16 v[120:123], a[200:203], a[80:83], v[120:123]\n\t" "ds_read_b64_tr_b16 a[192:193], %[vr14] offset:0\n\t"
    "global_store_dword %[vc], v207, %[sm] sc1\n\t" "v_mfma_f32_16x16x32_bf16 v[120:123], a[204:207], a[88:91], v[120:123]\n\t" "ds_read_b64_tr_b16 a[194:195], %[vr14] offset:2048\n\t"
    "s_add_u32 m0, %[sl1], 81920\n\t" "v_mfma_f32_16x16x32_bf16 v[124:127], a[200:203], a[84:87], v[124:127]\n\t" "ds_read_b64_tr_b16 a[196:197], %[vr14] offset:8192\n\t"
    "global_load_lds_dwordx4 %[vb1], %[sb]\n\t" "v_mfma_f32_16x16x32_bf16 v[124:127], a[204:207], a[92:95], v[124:127]\n\t" "ds_read_b64_tr_b16 a[198:199], %[vr14] offset:10240\n\t"
    "s_waitcnt lgkmcnt(4)\n\t" "v_mfma_f32_16x16x32_bf16 v[96:99], a[176:179], a[96:99], v[96:99]\n\t" "ds_read_b64_tr_b16 a[200:201], %[vr15] offset:0\n\t" "v_add_u32 %[vb1], 0x10000, %[vb1]\n\t"
    "v_mfma_f32_16x16x32_bf16 v[96:99], a[180:183], a[104:107], v[96:99]\n\t" "ds_read_b64_tr_b16 a[202:203], %[vr15] offset:2048\n\t" "s_add_u32 m0, %[sl1], 90112\n\t"
    "v_mfma_f32_16x16x32_bf16 v[100:103], a[176:179], a[100:103], v[100:103]\n\t" "ds_read_b64_tr_b16 a[204:205], %[vr15] offset:8192\n\t" "global_load_lds_dwordx4 %[vb1], %[sb]\n\t"
    "v_mfma_f32_16x16x32_bf16 v[100:103], a[180:183], a[108:111], v[100:103]\n\t" "ds_read_b64_tr_b16 a[206:207], %[vr15] offset:10240\n\t" "v_add_u32 %[vb1], %[qw], %[vb1]\n\t"
    "v_mfma_f32_16x16x32_bf16 v[104:107], a[184:187], a[96:99], v[104:107]\n\t" "s_waitcnt vmcnt(9)\n\t" "s_barrier\n\t" "ds_read_b64_tr_b16 a[176:177], %[vr12] offset:49152\n\t"
    "v_mfma_f32_16x16x32_bf16 v[104:107], a[188:191], a[104:107], v[104:107]\n\t" "ds_read_b64_tr_b16 a[178:179], %[vr12] offset:51200\n\t" "s_add_u32 m0, %[sl1], 98304\n\t"
    "v_mfma_f32_16x16x32_bf16 v[108:111], a[184:187], a[100:103], v[108:111]\n\t" "ds_read_b64_tr_b16 a[180:181], %[vr12] offset:57344\n\t" "global_load_lds_dwordx4 %[vb1], %[sb]\n\t"
    "v_mfma_f32_16x16x32_bf16 v[108:111], a[188:191], a[108:111], v[108:111]\n\t" "ds_read_b64_tr_b16 a[182:183], %[vr12] offset:59392\n\t" "s_waitcnt lgkmcnt(4)\n\t"
    "v_mfma_f32_16x16x32_bf16 v[112:115], a[192:195], a[96:99], v[112:115]\n\t" "ds_read_b64_tr_b16 a[184:185], %[vr13] offset:49152\n\t" "v_add_u32 %[vb1], %[pw], %[vb1]\n\t"
    "v_mfma_f32_16x16x32_bf16 v[112:115], a[196:199], a[104:107], v[112:115]\n\t" "ds_read_b64_tr_b16 a[186:187], %[vr13] offset:51200\n\t" "s_add_u32 m0, %[sl1], 106496\n\t"
    "v_mfma_f32_16x16x32_bf16 v[116:119], a[192:195], a[100:103], v[116:119]\n\t" "ds_read_b64_tr_b16 a[188:189], %[vr13] offset:57344\n\t" "global_load_lds_dwordx4 %[vb1], %[sb]\n\t"
    "v_mfma_f32_16x16x32_bf16 v[116:119], a[196:199], a[108:111], v[116:119]\n\t" "ds_read_b64_tr_b16 a[190:191], %[vr13] offset:59392\n\t"
    "v_mfma_f32_16x16x32_bf16 v[120:123], a[200:203], a[96:99], v[120:123]\n\t" "ds_read_b64_tr_b16 a[192:193], %[vr14] offset:49152\n\t"
    "v_mfma_f32_16x16x32_bf16 v[120:123], a[204:207], a[104:107], v[120:123]\n\t" "ds_read_b64_tr_b16 a[194:195], %[vr14] offset:51200\n\t"
    "v_mfma_f32_16x16x32_bf16 v[124:127], a[200:203], a[100:103], v[124:127]\n\t" "ds_read_b64_tr_b16 a[196:197], %[vr14] offset:57344\n\t"
    "v_mfma_f32_16x16x32_bf16 v[124:127], a[204:207], a[108:111], v[124:127]\n\t" "ds_read_b64_tr_b16 a[198:199], %[vr14] offset:59392\n\t" "s_waitcnt lgkmcnt(4)\n\t"
    "v_mfma_f32_16x16x32_bf16 v[96:99], a[176:179], a[112:115], v[96:99]\n\t" "ds_read_b64_tr_b16 a[200:201], %[vr15] offset:49152\n\t"
    "v_mfma_f32_16x16x32_bf16 v[96:99], a[180:183], a[120:123], v[96:99]\n\t" "ds_read_b64_tr_b16 a[202:203], %[vr15] offset:51200\n\t"
    "v_mfma_f32_16x16x32_bf16 v[100:103], a[176:179], a[116:119], v[100:103]\n\t" "ds_read_b64_tr_b16 a[204:205], %[vr15] offset:57344\n\t"
    "v_mfma_f32_16x16x32_bf16 v[100:103], a[180:183], a[124:127], v[100:103]\n\t" "ds_read_b64_tr_b16 a[206:207], %[vr15] offset:59392\n\t"
    "v_mfma_f32_16x16x32_bf16 v[104:107], a[184:187], a[112:115], v[104:107]\n\t" "s_waitcnt vmcnt(9)\n\t" "s_barrier\n\t" "ds_read_b64_tr_b16 a[176:177], %[vr16] offset:0\n\t"
    "v_mfma_f32_16x16x32_bf16 v[104:107], a[188:191], a[120:123], v[104:107]\n\t" "ds_read_b64_tr_b16 a[178:179], %[vr16] offset:2048\n\t"
    "v_mfma_f32_16x16x32_bf16 v[108:111], a[184:187], a[116:119], v[108:111]\n\t" "ds_read_b64_tr_b16 a[180:181], %[vr16] offset:8192\n\t"
    "v_mfma_f32_16x16x32_bf16 v[108:111], a[188:191], a[124:127], v[108:111]\n\t" "ds_read_b64_tr_b16 a[182:183], %[vr16] offset:10240\n\t" "s_waitcnt lgkmcnt(4)\n\t"
    "v_mfma_f32_16x16x32_bf16 v[112:115], a[192:195], a[112:115], v[112:115]\n\t" "ds_read_b64_tr_b16 a[184:185], %[vr17] offset:0\n\t"
    "v_mfma_f32_16x16x32_bf16 v[112:115], a[196:199], a[120:123], v[112:115]\n\t" "ds_read_b64_tr_b16 a[186:187], %[vr17] offset:2048\n\t"
    "v_mfma_f32_16x16x32_bf16 v[116:119], a[192:195], a[116:119], v[116:119]\n\t" "ds_read_b64_tr_b16 a[188:189], %[vr17] offset:8192\n\t"
    "v_mfma_f32_16x16x32_bf16 v[116:119], a[196:199], a[124:127], v[116:119]\n\t" "ds_read_b64_tr_b16 a[190:191], %[vr17] offset:10240\n\t"
    "v_mfma_f32_16x16x32_bf16 v[120:123], a[200:203], a[112:115], v[120:123]\n\t" "ds_read_b64_tr_b16 a[192:193], %[vr18] offset:0\n\t"
    "v_mfma_f32_16x16x32_bf16 v[120:123], a[204:207], a[120:123], v[120:123]\n\t" "ds_read_b64_tr_b16 a[194:195], %[vr18] offset:2048\n\t"
    "v_mfma_f32_16x16x32_bf16 v[124:127], a[200:203], a[116:119], v[124:127]\n\t" "ds_read_b64_tr_b16 a[196:197], %[vr18] offset:8192\n\t"
    "v_mfma_f32_16x16x32_bf16 v[124:127], a[204:207], a[124:127], v[124:127]\n\t" "ds_read_b64_tr_b16 a[198:199], %[vr18] offset:10240\n\t" "s_waitcnt lgkmcnt(4)\n\t"
    "v_mfma_f32_16x16x32_bf16 v[96:99], a[176:179], a[128:131], v[96:99]\n\t" "ds_read_b64_tr_b16 a[200:201], %[vr19] offset:0\n\t"
    "v_mfma_f32_16x16x32_bf16 v[96:99], a[180:183], a[136:139], v[96:99]\n\t" "ds_read_b64_tr_b16 a[202:203], %[vr19] offset:2048\n\t"
    "v_mfma_f32_16x16x32_bf16 v[100:103], a[176:179], a[132:135], v[100:103]\n\t" "ds_read_b64_tr_b16 a[204:205], %[vr19] offset:8192\n\t"
    "v_mfma_f32_16x16x32_bf16 v[100:103], a[180:183], a[140:143], v[100:103]\n\t" "ds_read_b64_tr_b16 a[206:207], %[vr19] offset:10240\n\t"
    "v_mfma_f32_16x16x32_bf16 v[104:107], a[184:187], a[128:131], v[104:107]\n\t" "s_waitcnt vmcnt(2)\n\t" "s_barrier\n\t" "ds_read_b64_tr_b16 a[176:177], %[vr16] offset:16384\n\t"
    "v_mfma_f32_16x16x32_bf16 v[104:107], a[188:191], a[136:139], v[104:107]\n\t" "ds_read_b64_tr_b16 a[178:179], %[vr16] offset:18432\n\t"
    "v_mfma_f32_16x16x32_bf16 v[108:111], a[184:187], a[132:135], v[108:111]\n\t" "ds_read_b64_tr_b16 a[180:181], %[vr16] offset:24576\n\t"
    "v_mfma_f32_16x16x32_bf16 v[108:111], a[188:191], a[140:143], v[108:111]\n\t" "ds_read_b64_tr_b16 a[182:183], %[vr16] offset:26624\n\t" "s_waitcnt lgkmcnt(4)\n\t"
    "v_mfma_f32_16x16x32_bf16 v[112:115], a[192:195], a[128:131], v[112:115]\n\t" "ds_read_b64_tr_b16 a[184:185], %[vr17] offset:16384\n\t"
    "v_mfma_f32_16x16x32_bf16 v[112:115], a[196:199], a[136:139], v[112:115]\n\t" "ds_read_b64_tr_b16 a[186:187], %[vr17] offset:18432\n\t"
    "v_mfma_f32_16x16x32_bf16 v[116:119], a[192:195], a[132:135], v[116:119]\n\t" "ds_read_b64_tr_b16 a[188:189], %[vr17] offset:24576\n\t"
    "v_mfma_f32_16x16x32_bf16 v[116:119], a[196:199], a[140:143], v[116:119]\n\t" "ds_read_b64_tr_b16 a[190:191], %[vr17] offset:26624\n\t"
    "v_mfma_f32_16x16x32_bf16 v[120:123], a[200:203], a[128:131], v[120:123]\n\t" "ds_read_b64_tr_b16 a[192:193], %[vr18] offset:16384\n\t"
    "v_mfma_f32_16x16x32_bf16 v[120:123], a[204:207], a[136:139], v[120:123]\n\t" "ds_read_b64_tr_b16 a[194:195], %[vr18] offset:18432\n\t"
    "v_mfma_f32_16x16x32_bf16 v[124:127], a[200:203], a[132:135], v[124:127]\n\t" "ds_read_b64_tr_b16 a[196:197], %[vr18] offset:24576\n\t"
    "v_mfma_f32_16x16x32_bf16 v[124:127], a[204:207], a[140:143], v[124:127]\n\t" "ds_read_b64_tr_b16 a[198:199], %[vr18] offset:26624\n\t" "s_waitcnt lgkmcnt(4)\n\t"
    "v_mfma_f32_16x16x32_bf16 v[96:99], a[176:179], a[144:147], v[96:99]\n\t" "ds_read_b64_tr_b16 a[200:201], %[vr19] offset:16384\n\t" "global_load_dwordx4 v[208:211], %[vx], %[st] offset:0 sc1\n\t"
    "v_mfma_f32_16x16x32_bf16 v[96:99], a[180:183], a[152:155], v[96:99]\n\t" "ds_read_b64_tr_b16 a[202:203], %[vr19] offset:18432\n\t"
    "v_mfma_f32_16x16x32_bf16 v[100:103], a[176:179], a[148:151], v[100:103]\n\t" "ds_read_b64_tr_b16 a[204:205], %[vr19] offset:24576\n\t"
    "global_load_dwordx4 v[212:215], %[vx], %[st] offset:1024 sc1\n\t" "v_mfma_f32_16x16x32_bf16 v[100:103], a[180:183], a[156:159], v[100:103]\n\t"
    "ds_read_b64_tr_b16 a[206:207], %[vr19] offset:26624\n\t" "v_mfma_f32_16x16x32_bf16 v[104:107], a[184:187], a[144:147], v[104:107]\n\t" "s_waitcnt vmcnt(2)\n\t" "s_barrier\n\t"
    "ds_read_b64_tr_b16 a[176:177], %[vr16] offset:32768\n\t" "global_load_dwordx4 v[216:219], %[vx], %[st] offset:2048 sc1\n\t"
    "v_mfma_f32_16x16x32_bf16 v[104:107], a[188:191], a[152:155], v[104:107]\n\t" "ds_read_b64_tr_b16 a[178:179], %[vr16] offset:34816\n\t"
    "v_mfma_f32_16x16x32_bf16 v[108:111], a[184:187], a[148:151], v[108:111]\n\t" "ds_read_b64_tr_b16 a[180:181], %[vr16] offset:40960\n\t"
    "global_load_dwordx4 v[220:223], %[vx], %[st] offset:3072 sc1\n\t" "v_mfma_f32_16x16x32_bf16 v[108:111], a[188:191], a[156:159], v[108:111]\n\t"
    "ds_read_b64_tr_b16 a[182:183], %[vr16] offset:43008\n\t" "global_load_dword v206, %[vc], %[st] sc1\n\t" "s_waitcnt lgkmcnt(4)\n\t"
    "v_mfma_f32_16x16x32_bf16 v[112:115], a[192:195], a[144:147], v[112:115]\n\t" "ds_read_b64_tr_b16 a[184:185], %[vr17] offset:32768\n\t"
    "v_mfma_f32_16x16x32_bf16 v[112:115], a[196:199], a[152:155], v[112:115]\n\t" "ds_read_b64_tr_b16 a[186:187], %[vr17] offset:34816\n\t"
    "v_mfma_f32_16x16x32_bf16 v[116:119], a[192:195], a[148:151], v[116:119]\n\t" "ds_read_b64_tr_b16 a[188:189], %[vr17] offset:40960\n\t"
    "v_mfma_f32_16x16x32_bf16 v[116:119], a[196:199], a[156:159], v[116:119]\n\t" "ds_read_b64_tr_b16 a[190:191], %[vr17] offset:43008\n\t"
    "v_mfma_f32_16x16x32_bf16 v[120:123], a[200:203], a[144:147], v[120:123]\n\t" "ds_read_b64_tr_b16 a[192:193], %[vr18] offset:32768\n\t"
    "v_mfma_f32_16x16x32_bf16 v[120:123], a[204:207], a[152:155], v[120:123]\n\t" "ds_read_b64_tr_b16 a[194:195], %[vr18] offset:34816\n\t"
    "v_mfma_f32_16x16x32_bf16 v[124:127], a[200:203], a[148:151], v[124:127]\n\t" "ds_read_b64_tr_b16 a[196:197], %[vr18] offset:40960\n\t"
    "v_mfma_f32_16x16x32_bf16 v[124:127], a[204:207], a[156:159], v[124:127]\n\t" "ds_read_b64_tr_b16 a[198:199], %[vr18] offset:43008\n\t" "s_waitcnt lgkmcnt(4)\n\t"
    "v_mfma_f32_16x16x32_bf16 v[96:99], a[176:179], a[160:163], v[96:99]\n\t" "ds_read_b64_tr_b16 a[200:201], %[vr19] offset:32768\n\t"
    "v_mfma_f32_16x16x32_bf16 v[96:99], a[180:183], a[168:171], v[96:99]\n\t" "ds_read_b64_tr_b16 a[202:203], %[vr19] offset:34816\n\t"
    "v_mfma_f32_16x16x32_bf16 v[100:103], a[176:179], a[164:167], v[100:103]\n\t" "ds_read_b64_tr_b16 a[204:205], %[vr19] offset:40960\n\t"
    "v_mfma_f32_16x16x32_bf16 v[100:103], a[180:183], a[172:175], v[100:103]\n\t" "ds_read_b64_tr_b16 a[206:207], %[vr19] offset:43008\n\t"
    "v_mfma_f32_16x16x32_bf16 v[104:107], a[184:187], a[160:163], v[104:107]\n\t" "v_mfma_f32_16x16x32_bf16 v[104:107], a[188:191], a[168:171], v[104:107]\n\t"
    "v_mfma_f32_16x16x32_bf16 v[108:111], a[184:187], a[164:167], v[108:111]\n\t" "v_mfma_f32_16x16x32_bf16 v[108:111], a[188:191], a[172:175], v[108:111]\n\t" "s_waitcnt lgkmcnt(0)\n\t"
    "v_mfma_f32_16x16x32_bf16 v[112:115], a[192:195], a[160:163], v[112:115]\n\t" "v_mfma_f32_16x16x32_bf16 v[112:115], a[196:199], a[168:171], v[112:115]\n\t"
    "v_mfma_f32_16x16x32_bf16 v[116:119], a[192:195], a[164:167], v[116:119]\n\t" "v_mfma_f32_16x16x32_bf16 v[116:119], a[196:199], a[172:175], v[116:119]\n\t"
    "v_mfma_f32_16x16x32_bf16 v[120:123], a[200:203], a[160:163], v[120:123]\n\t" "v_mfma_f32_16x16x32_bf16 v[120:123], a[204:207], a[168:171], v[120:123]\n\t"
    "v_mfma_f32_16x16x32_bf16 v[124:127], a[200:203], a[164:167], v[124:127]\n\t" "v_mfma_f32_16x16x32_bf16 v[124:127], a[204:207], a[172:175], v[124:127]\n\t" "s_mov_b32 s44, 0x400000\n\t"
    ".Lpoll_%=:\n\t" "s_waitcnt vmcnt(0)\n\t" "v_add3_u32 v224, %[t32], v208, v209\n\t" "v_add3_u32 v224, v224, v210, v211\n\t" "v_add3_u32 v224, v224, v212, v213\n\t"
    "v_add3_u32 v224, v224, v214, v215\n\t" "v_add3_u32 v224, v224, v216, v217\n\t" "v_add3_u32 v224, v224, v218, v219\n\t" "v_add3_u32 v224, v224, v220, v221\n\t" "v_add3_u32 v224, v224, v222, v223\n\t"
    "v_cmp_ne_u32 vcc, v206, v224\n\t" "s_nop 4\n\t" "s_cbranch_vccz .Lrdy_%=\n\t" "global_load_dwordx4 v[208:211], %[vx], %[st] offset:0 sc1\n\t"
    "global_load_dwordx4 v[212:215], %[vx], %[st] offset:1024 sc1\n\t" "global_load_dwordx4 v[216:219], %[vx], %[st] offset:2048 sc1\n\t" "global_load_dwordx4 v[220:223], %[vx], %[st] offset:3072 sc1\n\t"
    "global_load_dword v206, %[vc], %[st] sc1\n\t" "s_sub_u32 s44, s44, 1\n\t" "s_cbranch_scc0 .Lpoll_%=\n\t" "s_waitcnt vmcnt(0)\n\t" ".Lrdy_%=:\n\t"
    "v_mfma_f32_4x4x4_16b_bf16 v[96:99], %[id], v[208:209], v[96:99]\n\t" "v_mfma_f32_4x4x4_16b_bf16 v[104:107], %[id], v[210:211], v[104:107]\n\t"
    "v_mfma_f32_4x4x4_16b_bf16 v[112:115], %[id], v[212:213], v[112:115]\n\t" "v_mfma_f32_4x4x4_16b_bf16 v[120:123], %[id], v[214:215], v[120:123]\n\t"
    "v_mfma_f32_4x4x4_16b_bf16 v[100:103], %[id], v[216:217], v[100:103]\n\t" "v_mfma_f32_4x4x4_16b_bf16 v[108:111], %[id], v[218:219], v[108:111]\n\t"
    "v_mfma_f32_4x4x4_16b_bf16 v[116:119], %[id], v[220:221], v[116:119]\n\t" "v_mfma_f32_4x4x4_16b_bf16 v[124:127], %[id], v[222:223], v[124:127]\n\t" "s_mov_b64 s[40:41], %[so]\n\t"
    "v_pk_fma_f32 v[96:97], v[176:177], v[96:97], v[144:145]\n\t" "v_pk_fma_f32 v[98:99], v[178:179], v[98:99], v[146:147]\n\t" "v_cvt_pk_bf16_f32 v96, v96, v97\n\t" "v_cvt_pk_bf16_f32 v97, v98, v99\n\t"
    "ds_write_b64 %[tw0], v[96:97] offset:0\n\t" "v_pk_fma_f32 v[104:105], v[180:181], v[104:105], v[152:153]\n\t" "v_pk_fma_f32 v[106:107], v[182:183], v[106:107], v[154:155]\n\t"
    "v_cvt_pk_bf16_f32 v104, v104, v105\n\t" "v_cvt_pk_bf16_f32 v105, v106, v107\n\t" "ds_write_b64 %[tw1], v[104:105] offset:0\n\t" "v_pk_fma_f32 v[112:113], v[184:185], v[112:113], v[160:161]\n\t"
    "v_pk_fma_f32 v[114:115], v[186:187], v[114:115], v[162:163]\n\t" "v_cvt_pk_bf16_f32 v112, v112, v113\n\t" "v_cvt_pk_bf16_f32 v113, v114, v115\n\t" "ds_write_b64 %[tw2], v[112:113] offset:0\n\t"
    "v_pk_fma_f32 v[120:121], v[188:189], v[120:121], v[168:169]\n\t" "v_pk_fma_f32 v[122:123], v[190:191], v[122:123], v[170:171]\n\t" "v_cvt_pk_bf16_f32 v120, v120, v121\n\t"
    "v_cvt_pk_bf16_f32 v121, v122, v123\n\t" "ds_write_b64 %[tw3], v[120:121] offset:0\n\t" "ds_read_b128 v[224:227], %[tr] offset:0\n\t" "ds_read_b128 v[228:231], %[tr] offset:1024\n\t"
    "v_pk_fma_f32 v[100:101], v[176:177], v[100:101], v[148:149]\n\t" "v_pk_fma_f32 v[102:103], v[178:179], v[102:103], v[150:151]\n\t" "v_cvt_pk_bf16_f32 v100, v100, v101\n\t"
    "v_cvt_pk_bf16_f32 v101, v102, v103\n\t" "ds_write_b64 %[tw0], v[100:101] offset:2048\n\t" "v_pk_fma_f32 v[108:109], v[180:181], v[108:109], v[156:157]\n\t"
    "v_pk_fma_f32 v[110:111], v[182:183], v[110:111], v[158:159]\n\t" "v_cvt_pk_bf16_f32 v108, v108, v109\n\t" "v_cvt_pk_bf16_f32 v109, v110, v111\n\t" "ds_write_b64 %[tw1], v[108:109] offset:2048\n\t"
    "v_pk_fma_f32 v[116:117], v[184:185], v[116:117], v[164:165]\n\t" "v_pk_fma_f32 v[118:119], v[186:187], v[118:119], v[166:167]\n\t" "v_cvt_pk_bf16_f32 v116, v116, v117\n\t"
    "v_cvt_pk_bf16_f32 v117, v118, v119\n\t" "ds_write_b64 %[tw2], v[116:117] offset:2048\n\t" "v_pk_fma_f32 v[124:125], v[188:189], v[124:125], v[172:173]\n\t"
    "v_pk_fma_f32 v[126:127], v[190:191], v[126:127], v[174:175]\n\t" "v_cvt_pk_bf16_f32 v124, v124, v125\n\t" "v_cvt_pk_bf16_f32 v125, v126, v127\n\t" "ds_write_b64 %[tw3], v[124:125] offset:2048\n\t"
    "ds_read_b128 v[232:235], %[tr] offset:2048\n\t" "ds_read_b128 v[236:239], %[tr] offset:3072\n\t" "s_waitcnt lgkmcnt(7)\n\t" "global_store_dwordx4 %[vs], v[224:227], s[40:41]\n\t"
    "s_add_u32 s40, s40, 16384\n\t" "s_addc_u32 s41, s41, 0\n\t" "s_waitcnt lgkmcnt(6)\n\t" "global_store_dwordx4 %[vs], v[228:231], s[40:41]\n\t" "s_add_u32 s40, s40, 16384\n\t"
    "s_addc_u32 s41, s41, 0\n\t" "s_waitcnt lgkmcnt(1)\n\t" "global_store_dwordx4 %[vs], v[232:235], s[40:41]\n\t" "s_add_u32 s40, s40, 16384\n\t" "s_addc_u32 s41, s41, 0\n\t" "s_waitcnt lgkmcnt(0)\n\t"
    "global_store_dwordx4 %[vs], v[236:239], s[40:41]\n\t" "s_branch .Lend_%=\n\t" ".Lw2_%=:\n\t" "s_sleep 1\n\t" "global_load_dwordx2 %[fl], %[vf], %[sw] sc1\n\t" "s_waitcnt vmcnt(0)\n\t"
    "v_cmp_ne_u64 vcc, %[tag], %[fl]\n\t" "s_nop 4\n\t" "s_cbranch_vccz .Lg2_%=\n\t" "s_sub_u32 s44, s44, 1\n\t" "s_cbranch_scc0 .Lw2_%=\n\t" "s_branch .Lg2_%=\n\t" ".Lend_%=:\n\t"
    : [fl] "=&v"(fl), [vb0] "+v"(vb0), [vb1] "+v"(vb1), "+{v[144:147]}"(stg[0]), "+{v[148:151]}"(stg[1]), "+{v[152:155]}"(stg[2]), "+{v[156:159]}"(stg[3]), "+{v[160:163]}"(stg[4]), "+{v[48:51]}"(stg[5]), "+{v[32:35]}"(stg[6]), "+{v[164:167]}"(stg[7]), "+{v[96:99]}"(ac[0]), "+{v[100:103]}"(ac[1]), "+{v[104:107]}"(ac[2]), "+{v[108:111]}"(ac[3]), "+{v[112:115]}"(ac[4]), "+{v[116:119]}"(ac[5]), "+{v[120:123]}"(ac[6]), "+{v[124:127]}"(ac[7]), "+{v[224:227]}"(ac[8]), "+{v[228:231]}"(ac[9]), "+{v[232:235]}"(ac[10]), "+{v[236:239]}"(ac[11]), "+{v[240:243]}"(ac[12]), "+{v[244:247]}"(ac[13]), "+{v[248:251]}"(ac[14]), "+{v[252:255]}"(ac[15])
    : [vr0] "v"(vr[0]), [vr1] "v"(vr[1]), [vr2] "v"(vr[2]), [vr3] "v"(vr[3]), [vr4] "v"(vr[4]), [vr5] "v"(vr[5]), [vr6] "v"(vr[6]), [vr7] "v"(vr[7]), [vr8] "v"(vr[8]), [vr9] "v"(vr[9]), [vr10] "v"(vr[10]), [vr11] "v"(vr[11]), [vr12] "v"(vr[12]), [vr13] "v"(vr[13]), [vr14] "v"(vr[14]), [vr15] "v"(vr[15]), [vr16] "v"(vr[16]), [vr17] "v"(vr[17]), [vr18] "v"(vr[18]), [vr19] "v"(vr[19]), [vr20] "v"(vr[20]), [vr21] "v"(vr[21]), [vr22] "v"(vr[22]), [vr23] "v"(vr[23]), [vo] "v"(vo), [vx] "v"(vx), [vg] "v"(vg), [vf] "v"(vf), [vc] "v"(vc), [id] "v"(id), [tw0] "v"(tw0), [tw1] "v"(tw1), [tw2] "v"(tw2), [tw3] "v"(tw3), [tr] "v"(tr), [vs] "v"(vs), [t32] "s"((unsigned)tag), [sa] "s"(sa + 20480), [tag] "s"(tag), [wid] "s"(wid), [sb] "s"(sb), [sl0] "s"(sl0), [sl1] "s"(sl1), [sg] "s"(sg), [sm] "s"(sm), [st] "s"(st), [sw] "s"(sw), [cd] "s"(cd), [so] "s"(so), [sc] "s"(sc), [pw] "s"(pw), [qw] "s"(qw), [jw] "s"(jw)
    : "v168", "v169", "v170", "v171", "v172", "v173", "v174", "v175", "v176", "v177", "v178", "v179", "v180", "v181", "v182", "v183", "v184", "v185", "v186", "v187", "v188", "v189", "v190", "v191", "v192", "v193", "v194", "v195", "v196", "v197", "v198", "v199", "v200", "v201", "v202", "v203", "v204", "v205", "v206", "v207", "v208", "v209", "v210", "v211", "v212", "v213", "v214", "v215", "v216", "v217", "v218", "v219", "v220", "v221", "v222", "v223", "vcc", "a0", "a1", "a2", "a3", "a4", "a5", "a6", "a7", "a8", "a9", "a10", "a11", "a12", "a13", "a14", "a15", "a16", "a17", "a18", "a19", "a20", "a21", "a22", "a23", "a24", "a25", "a26", "a27", "a28", "a29", "a30", "a31", "a32", "a33", "a34", "a35", "a36", "a37", "a38", "a39", "a40", "a41", "a42", "a43", "a44", "a45", "a46", "a47", "a48", "a49", "a50", "a51", "a52", "a53", "a54", "a55", "a56", "a57", "a58", "a59", "a60", "a61", "a62", "a63", "a64", "a65", "a66", "a67", "a68", "a69", "a70", "a71", "a72", "a73", "a74", "a75", "a76", "a77", "a78", "a79", "a80", "a81", "a82", "a83", "a84", "a85", "a86", "a87", "a88", "a89", "a90", "a91", "a92", "a93", "a94", "a95", "a96", "a97", "a98", "a99", "a100", "a101", "a102", "a103", "a104", "a105", "a106", "a107", "a108", "a109", "a110", "a111", "a112", "a113", "a114", "a115", "a116", "a117", "a118", "a119", "a120", "a121", "a122", "a123", "a124", "a125", "a126", "a127", "a128", "a129", "a130", "a131", "a132", "a133", "a134", "a135", "a136", "a137", "a138", "a139", "a140", "a141", "a142", "a143", "a144", "a145", "a146", "a147", "a148", "a149", "a150", "a151", "a152", "a153", "a154", "a155", "a156", "a157", "a158", "a159", "a160", "a161", "a162", "a163", "a164", "a165", "a166", "a167", "a168", "a169", "a170", "a171", "a172", "a173", "a174", "a175", "a176", "a177", "a178", "a179", "a180", "a181", "a182", "a183", "a184", "a185", "a186", "a187", "a188", "a189", "a190", "a191", "a192", "a193", "a194", "a195", "a196", "a197", "a198", "a199", "a200", "a201", "a202", "a203", "a204", "a205", "a206", "a207", "a208", "a209", "a210", "a211", "a212", "a213", "a214", "a215", "a216", "a217", "a218", "a219", "a220", "a221", "a222", "a223", "a224", "a225", "a226", "a227", "a228", "a229", "a230", "a231", "a232", "a233", "a234", "a235", "a236", "a237", "a238", "a239", "a240", "a241", "a242", "a243", "a244", "a245", "a246", "a247", "a248", "a249", "a250", "a251", "a252", "a253", "a254", "a255", "s40", "s41", "s42", "s43", "s44", "s45", "scc", "memory");
  }
  asm volatile("k_down_end:");
}

extern "C" void kernel_launch(void* const* d_in, const int* in_sizes, int n_in, void* d_out, int out_size, void* d_ws, size_t ws_size, hipStream_t stream) {
  const u16* x = (const u16*)d_in[0]; const u16* wqkv = (const u16*)d_in[1]; const u16* wo = (const u16*)d_in[2];
  const u16* wg = (const u16*)d_in[4]; const u16* wu = (const u16*)d_in[5]; const u16* wd = (const u16*)d_in[6];
  const u16* c = (const u16*)d_in[7]; const u16* wmod = (const u16*)d_in[8];
  k_main<<<256, 256, 0, stream>>>(c, wmod, x, wqkv, wo, wg, wu, wd, (u16*)d_out, (char*)d_ws);
}
```
